```python
import torch

def _softmax(x, axis=-1):
    return torch.softmax(x, dim=axis)

def _gelu(x):
    return torch.nn.functional.gelu(x, approximate='tanh')

def _silu(x):
    return torch.nn.functional.silu(x)

def _sigmoid(x):
    return torch.sigmoid(x)

def _layernorm(x, axis=-1, eps=1e-05):
    m = x.mean(dim=axis, keepdim=True)
    v = x.var(dim=axis, keepdim=True, unbiased=False)
    return (x - m) / torch.sqrt(v + eps)

def _scatter_add(tgt, idx, src, axis=0):
    return tgt.clone().index_add_(axis, idx, src)

def _segment_sum(src, seg, n):
    out = torch.zeros((n,) + src.shape[1:], dtype=src.dtype)
    return out.index_add_(0, seg, src)

def _pad(x, pw):
    flat = tuple((v for pair in reversed(pw) for v in pair))
    return torch.nn.functional.pad(x, flat)
kernel_name = 'interleave_layers__sliding_local_dilated__mixer_gmlp__prompt'

def setup_inputs():

    _w = lambda a: (lambda t: t.to(torch.bfloat16) if t.is_floating_point() else t)(torch.as_tensor(a))

    def _A_setup():
        B, S, D = (1, 2048, 576)
        return {'x': _w((lambda *s: torch.randn(*s, dtype=torch.bfloat16) * 0.05)(B, S, D)), 'wqkv': _w((lambda *s: torch.randn(*s, dtype=torch.bfloat16) * 0.05)(D, 3 * D)), 'wo': _w((lambda *s: torch.randn(*s, dtype=torch.bfloat16) * 0.05)(D, D))}
    _A_in = _A_setup()

    def _B_setup():
        B, S, D, F = (1, 2048, 576, 4608)
        Fh = F // 2
        return {'x': _w((lambda *s: torch.randn(*s, dtype=torch.bfloat16) * 0.05)(B, S, D)), 'w_in': _w((lambda *s: torch.randn(*s, dtype=torch.bfloat16) * 0.2)(D, 2 * Fh)), 'w_sp': _w(0.01 * (lambda *s: torch.randn(*s, dtype=torch.bfloat16) * 0.2)(S, S)), 'b_sp': _w(1.0 + 0.01 * (lambda *s: torch.randn(*s, dtype=torch.bfloat16) * 0.05)(S)), 'w_out': _w((lambda *s: torch.randn(*s, dtype=torch.bfloat16) * 0.05)(Fh, D))}
    _B_in = _B_setup()
    return {'A_x': _A_in['x'], 'A_wqkv': _A_in['wqkv'], 'A_wo': _A_in['wo'], 'B_x': _B_in['x'], 'B_w_in': _B_in['w_in'], 'B_w_sp': _B_in['w_sp'], 'B_b_sp': _B_in['b_sp'], 'B_w_out': _B_in['w_out']}

def reference(A_x, A_wqkv, A_wo, B_x, B_w_in, B_w_sp, B_b_sp, B_w_out):

    def _A_ref(x, wqkv, wo):
        B, S, D = x.shape
        H = 32
        Dh = D // H
        W, dil = (8, 2)
        qkv = (lambda x, *d: x.permute(*d))((x @ wqkv).reshape(B, S, 3, H, Dh), 2, 0, 3, 1, 4)
        q, k, v = (qkv[0], qkv[1], qkv[2])
        scale = Dh ** (-0.5)
        pos = torch.arange(S)
        off = torch.arange(W) * dil
        idx = pos[:, None] - off[None, :]
        mask = idx >= 0
        idx_c = torch.where(mask, idx, 0).reshape(-1)
        kg = k[:, :, idx_c, :].reshape(B, H, S, W, Dh)
        vg = v[:, :, idx_c, :].reshape(B, H, S, W, Dh)
        logits = torch.einsum('bhsd,bhswd->bhsw', q, kg) * scale
        logits = torch.where(mask[None, None, :, :], logits, -1000000000.0)
        attn = _softmax(logits, axis=-1)
        o = torch.einsum('bhsw,bhswd->bhsd', attn, vg)
        return (lambda x, *d: x.permute(*d))(o, 0, 2, 1, 3).reshape(B, S, D) @ wo

    def _B_ref(x, w_in, w_sp, b_sp, w_out):
        B, S, D = x.shape
        F = w_in.shape[-1]
        Fh = F // 2
        z = _gelu(torch.einsum('bsd,df->bsf', x, w_in))
        z1 = z[..., :Fh].clone()
        z2 = _layernorm(z[..., Fh:], axis=-1)
        g = torch.einsum('ts,bsf->btf', w_sp, z2) + b_sp[None, :, None]
        return torch.einsum('bsf,fd->bsd', z1 * g, w_out)
    x = A_x
    x = x + _A_ref(_layernorm(x, axis=-1), A_wqkv, A_wo)
    x = x + _B_ref(_layernorm(x, axis=-1), B_w_in, B_w_sp, B_b_sp, B_w_out)
    x = x + _A_ref(_layernorm(x, axis=-1), A_wqkv, A_wo)
    x = x + _B_ref(_layernorm(x, axis=-1), B_w_in, B_w_sp, B_b_sp, B_w_out)
    return x
```

```cpp
#include <hip/hip_runtime.h>
#include <hip/hip_bf16.h>
#include <stdint.h>

#define DEVI __device__ __forceinline__

typedef unsigned short u16;
typedef __attribute__((ext_vector_type(8))) short s16x8;
typedef __attribute__((ext_vector_type(4))) short s16x4;
typedef __attribute__((ext_vector_type(4))) float f32x4;
typedef __attribute__((ext_vector_type(2))) float f32x2;
typedef __attribute__((ext_vector_type(2))) __bf16 bf16x2;
typedef __attribute__((ext_vector_type(2))) unsigned u32x2;
typedef __attribute__((ext_vector_type(4))) unsigned u32x4;

#define MK(I) ((void)0)
#define MX(S, I) ((void)0)
#define MF(I) ((void)0)
#define MF_ARG
#define MF_PASS
#define MK_ARG
#define MK_PASS

#define SPIN_LDS for (;;)
#define SPIN_MEM (1u << 22)

namespace {

constexpr int S_ = 2048, D_ = 576, F_ = 4608, FH_ = 2304;
constexpr int NQKV = 3 * D_;

constexpr size_t SZ_X = (size_t)S_ * D_ * 2;
constexpr size_t SZ_Z = (size_t)S_ * FH_ * 2;
constexpr size_t OFF_X1 = 0;
constexpr size_t OFF_O = OFF_X1 + 3 * SZ_X;
constexpr size_t OFF_Z2 = OFF_O + 2 * SZ_X;
constexpr size_t OFF_H = OFF_Z2 + 2 * SZ_Z;
constexpr size_t SZ_XS = (size_t)S_ * 16 * 8;
constexpr size_t OFF_XS = OFF_H + 2 * SZ_Z;
constexpr size_t OFF_CSQ = OFF_XS + 3 * SZ_XS;
constexpr size_t OFF_ZP = OFF_CSQ + 256 * 512;
constexpr size_t SZ_ZP = (size_t)16 * 16 * 1024;
constexpr size_t OFF_PQ = OFF_ZP + 2 * SZ_ZP;
constexpr size_t OFF_PO = OFF_PQ + (size_t)16 * 9 * 64 * 112 * 2;
constexpr size_t OFF_PI = OFF_PO + (size_t)D_ * D_ * 2;
constexpr size_t OFF_PW = OFF_PI + (size_t)32 * 9 * 64 * 144 * 2;
constexpr size_t OFF_CSP = OFF_PW + (size_t)FH_ * D_ * 2;
constexpr size_t OFF_CNT = OFF_CSP + (size_t)18 * F_ * 4;
constexpr int CN_X1 = 0, CN_H = 2, CN_X2 = 4, CN_O = 5, CN_Z = 7, CN_PK = 9, NCNT = 10;
constexpr size_t OFF_TAG = OFF_CNT + (size_t)NCNT * 256 * 128;
constexpr size_t OFF_HELLO = OFF_TAG + 128;
constexpr size_t OFF_POT = OFF_HELLO + 2048;
constexpr size_t WS_END = OFF_POT + 1024;

DEVI float bf_lo(unsigned u) { return __uint_as_float(u << 16); }
DEVI float bf_hi(unsigned u) { return __uint_as_float(u & 0xffff0000u); }
DEVI unsigned pack2(float a, float b) {
  f32x2 v = {a, b};
  bf16x2 r = __builtin_convertvector(v, bf16x2);
  return __builtin_bit_cast(unsigned, r);
}
DEVI float rbf(float a) {
  return bf_lo(pack2(a, 0.f));
}

DEVI float sum_g(float x) {
  const unsigned u = __float_as_uint(x);
  const auto p = __builtin_amdgcn_permlane32_swap(u, u, false, false);
  const unsigned v = __float_as_uint(__uint_as_float(p[0]) + __uint_as_float(p[1]));
  const auto q = __builtin_amdgcn_permlane16_swap(v, v, false, false);
  return __uint_as_float(q[0]) + __uint_as_float(q[1]);
}

DEVI unsigned vconst(unsigned c) {
  unsigned r;
  asm volatile("v_mov_b32 %0, %1" : "=v"(r) : "s"(c));
  return r;
}
DEVI const char* uni_ptr(const char* p) {
  unsigned long long v = (unsigned long long)p;
  unsigned lo = __builtin_amdgcn_readfirstlane((unsigned)v), hi = __builtin_amdgcn_readfirstlane((unsigned)(v >> 32));
  asm("s_nop 4" : "+s"(lo), "+s"(hi));
  return (const char*)(((unsigned long long)hi << 32) | lo);
}

DEVI void dma16(const char* sbase, unsigned voff, unsigned m0val) {
  asm volatile("s_mov_b32 m0, %2\n\ts_nop 0\n\tglobal_load_lds_dwordx4 %0, %1"
               :
               : "v"(voff), "s"(sbase), "s"(m0val)
               : "memory");
}

DEVI void ln_finish(float s, float q, float invn, float& mb, float& rinv) {
  float mean = s * invn;
  asm("" : "+v"(mean));
  float var = fmaxf(__builtin_fmaf(-mean, mean, q * invn), 0.f);
  mb = rbf(mean);
  float v = rbf(var);
  float t = rbf(v + 1e-5f);
  float sd = rbf(__builtin_amdgcn_sqrtf(t));
  rinv = __builtin_amdgcn_rcpf(sd);
}

DEVI unsigned pack2v(f32x2 v) { return pack2(v[0], v[1]); }

template <int BM_, int BN_>
struct GemmCfg {
  static constexpr int BM = BM_, BN = BN_, NS = 4;
  static constexpr int P = BN * 2;
  static constexpr int XB = BM * 128, WB = 64 * P, SB = XB + WB;
  static constexpr int LDS_BYTES = NS * SB;
};

DEVI int swap23(int r) { return (r & ~12) | ((r & 4) << 1) | ((r & 8) >> 1); }

DEVI unsigned lds_rd32(unsigned addr) {
  unsigned v;
  asm volatile("ds_read_b32 %0, %1\n\ts_waitcnt lgkmcnt(0)" : "=v"(v) : "v"(addr) : "memory");
  return v;
}
DEVI void lds_wr32(unsigned addr, unsigned v) { asm volatile("ds_write_b32 %0, %1" ::"v"(addr), "v"(v) : "memory"); }

template <class C, int MT, int NT, int D, int R, bool XCM, int WT, bool SQ = false>
struct GemmMath {
  static constexpr int WO = WT ? 1 : 2;
  static constexpr int NS = C::NS, SB = C::SB, P = C::P, NU = 2 * NT;
  static constexpr int XPU = MT <= 2 ? MT : MT - 2;
  static constexpr int XU = (MT + XPU - 1) / XPU;
  static_assert(NU % R == 0 && R > D && D <= NT && XU <= D, "shape");
  s16x4 wlo[R], whi[R];
  s16x8 wf[R];
  s16x8 xf[2][MT];
  unsigned xrd[2], wrd, xa[2], wa, so;
  f32x4 (&acc)[MT][NT];
  f32x4 accq[SQ ? MT : 1];
  DEVI GemmMath(f32x4 (&a)[MT][NT]) : acc(a) {}

  template <int PU>
  DEVI void wread_lo() {
    constexpr int off = (PU / NT) * 32 * P + (PU % NT) * (WT == 2 ? 1024 : 32);
    if constexpr (WT) asm volatile("ds_read_b128 %0, %1 offset:%2" : "=v"(wf[PU % R]) : "v"(wa), "n"(off));
    else asm volatile("ds_read_b64_tr_b16 %0, %1 offset:%2" : "=v"(wlo[PU % R]) : "v"(wa), "n"(off));
  }
  template <int PU>
  DEVI void wread_hi() {
    if constexpr (!WT)
      asm volatile("ds_read_b64_tr_b16 %0, %1 offset:%2" : "=v"(whi[PU % R]) : "v"(wa), "n"((PU / NT) * 32 * P + (PU % NT) * 32 + 8 * P));
  }
  template <int H, int M>
  DEVI void xread1() {
    asm volatile("ds_read_b128 %0, %1 offset:%2" : "=v"(xf[H][M]) : "v"(xa[H]), "n"(M * (XCM ? 256 : 2048)));
  }
  static constexpr int xops(int v) {
    const int k = (v + D) % NT;
    if (k >= XU) return 0;
    const int n = MT - k * XPU;
    return n < XPU ? n : XPU;
  }
  static constexpr int ops(int v, bool last) { return (last && v + D >= NU) ? 0 : WO + xops(v); }
  static constexpr int newer(int U, bool last) {
    int n = 0;
    int v0 = U - D;
    if (U % NT == 0) v0 += XU - 1;
    else n += v0 >= 0 ? xops(v0) : xops(v0 + NU);
    for (int v = v0 + 1; v < U; ++v) n += v >= 0 ? ops(v, last) : ops(v + NU, false);
    return n;
  }
  template <int U, int M_>
  DEVI void mf() {
    constexpr int M = ((U % NT) & 1) ? MT - 1 - M_ : M_;
    s16x8 w;
    if constexpr (WT) w = wf[U % R];
    else w = __builtin_shufflevector(wlo[U % R], whi[U % R], 0, 1, 2, 3, 4, 5, 6, 7);
    acc[M][U % NT] = __builtin_amdgcn_mfma_f32_16x16x32_bf16(w, xf[U / NT][M], acc[M][U % NT], 0, 0, 0);
    __builtin_amdgcn_sched_barrier(0);
  }
  DEVI void pin() {
#pragma unroll
    for (int m = 0; m < MT; ++m)
#pragma unroll
      for (int n = 0; n < NT; ++n) asm volatile("" : "+v"(acc[m][n]));
  }
  template <int U, int I>
  DEVI void dsop() {
    constexpr int PU = (U + D) % NU;
    if constexpr (I == 0) wread_lo<PU>();
    else if constexpr (I == 1) wread_hi<PU>();
    else if constexpr (I - 2 < xops(U)) {
      constexpr int k = (U + D) % NT;
      xread1<((U + D - k) % NU) / NT, k * XPU + I - 2>();
    }
  }
  template <int U, bool LAST, bool BAR, int M>
  DEVI void slot() {
    constexpr bool PF = !(LAST && U + D >= NU);
    mf<U, M>();
    if constexpr (M == 0 && PF && U + D == NU) {
      if constexpr (BAR) __builtin_amdgcn_s_barrier();
      so = (so == (unsigned)((NS - 1) * SB)) ? 0u : so + (unsigned)SB;
      wa = wrd + so;
      xa[0] = xrd[0] + so;
      xa[1] = xrd[1] + so;
      __builtin_amdgcn_sched_barrier(0);
    }
    if constexpr (PF) {
      dsop<U, M>();
      if constexpr (M == MT - 1) {
        if constexpr (MT <= 1) dsop<U, 1>();
        if constexpr (MT <= 2) {
          dsop<U, 2>();
          dsop<U, 3>();
        }
      }
      __builtin_amdgcn_sched_barrier(0);
    }
  }
  template <int U, bool LAST, bool BAR, int... M>
  DEVI void unit(std::integer_sequence<int, M...>) {
    static_assert(newer(U, LAST) <= 15, "lgkmcnt range");
    asm volatile("s_waitcnt lgkmcnt(%0)" ::"n"(newer(U, LAST)));
    __builtin_amdgcn_sched_barrier(0);
    (slot<U, LAST, BAR, M>(), ...);
    if constexpr (SQ && (U % NT) < MT) {
      constexpr int m = U % NT;
      accq[m] = __builtin_amdgcn_mfma_f32_16x16x32_bf16(xf[U / NT][m], xf[U / NT][m], accq[m], 0, 0, 0);
      __builtin_amdgcn_sched_barrier(0);
    }
    __builtin_amdgcn_sched_barrier(0);
  }
  template <bool LAST, bool BAR, int... U>
  DEVI void stage(std::integer_sequence<int, U...>) {
    (unit<U, LAST, BAR>(std::make_integer_sequence<int, MT>{}), ...);
  }
  template <int... U>
  DEVI void prow(std::integer_sequence<int, U...>) {
    ((wread_lo<U>(), wread_hi<U>()), ...);
  }
  template <int... M>
  DEVI void prox(std::integer_sequence<int, M...>) {
    (xread1<0, M>(), ...);
  }
  DEVI void setup(unsigned lds0, int mt0, int nt0, int lane, unsigned so0) {
    const int g = lane >> 4, r = lane & 15, q = (lane >> 2) & 3, p = lane & 3;
#pragma unroll
    for (int h = 0; h < 2; ++h)
      xrd[h] = XCM ? lds0 + (unsigned)((4 * h + g) * (C::BM * 16) + (mt0 * 16 + r) * 16)
                   : lds0 + (unsigned)((mt0 * 16 + r) * 128 + (((4 * h + g) ^ ((r >> 1) & 7)) * 16));
    wrd = WT == 2   ? lds0 + (unsigned)(C::XB + nt0 * 1024 + lane * 16)
          : WT == 1 ? lds0 + (unsigned)(C::XB + (g * 8 + (r & 7)) * P + (r >> 3) * 16 + nt0 * 32)
                    : lds0 + (unsigned)(C::XB + (16 * (g >> 1) + 4 * (g & 1) + q) * P + 8 * p + nt0 * 32);
    so = so0;
    xa[0] = xrd[0] + so;
    xa[1] = xrd[1] + so;
    wa = wrd + so;
  }
  DEVI void first() {
    prox(std::make_integer_sequence<int, MT>{});
    prow(std::make_integer_sequence<int, D>{});
    asm volatile("s_waitcnt lgkmcnt(0)");
  }
  template <int N, bool BAR = true>
  DEVI void mid() {
#pragma unroll 1
    for (int s = 0; s < N; ++s) stage<false, BAR>(std::make_integer_sequence<int, NU>{});
  }
  DEVI void last() {
    stage<true, true>(std::make_integer_sequence<int, NU>{});
    pin();
  }
  template <int KS>
  DEVI void run(unsigned lds0, int mt0, int nt0, int lane, unsigned so0) {
    setup(lds0, mt0, nt0, lane, so0);
    __builtin_amdgcn_s_barrier();
    first();
    mid<KS - 1>();
    last();
  }
};

DEVI const char* code_here() {
  unsigned long long pc;
  asm volatile("s_getpc_b64 %0" : "=s"(pc));
  return (const char*)pc;
}
DEVI int okmap(int p) {
  const int ch = p >> 3, e = p & 7, c = (ch - 8) & 3;
  return ch < 8 ? (2 * ch + (e >> 2)) * 36 + 16 + (e & 3) : ((ch - 8) >> 2) * 36 + c * 8 + (c >> 1) * 4 + e;
}

DEVI void pk_st16(void* p, u32x4 v) {
  asm volatile("global_store_dwordx4 %0, %1, off sc1\n\ts_nop 1" ::"v"(p), "v"(v) : "memory");
}

using K1C = GemmCfg<144, 112>;
constexpr int K1_TP = 240;
constexpr int K1_T_OFF = K1C::SB;
constexpr int K1_ST_OFF = 163840 - 128 - 1152;
static_assert(K1_T_OFF + 144 * K1_TP + 64 <= 3 * K1C::SB, "K1 LDS alias");
using K1R = GemmCfg<144, 144>;
constexpr int K1R_T_OFF = K1R::SB;
constexpr int K1R_CS_OFF = K1R_T_OFF + 144 * K1_TP;
constexpr int K1R_ST_OFF = K1R_CS_OFF + 512;
constexpr int K1R_DUMMY = K1R::LDS_BYTES;
constexpr int K1R_PO = K1R_DUMMY + 1024;
constexpr int K1R_CNT = 163840 - 128 + 28 * 4;
DEVI void msync(unsigned addr) {
  asm volatile("s_waitcnt lgkmcnt(0)" ::: "memory");
  asm volatile("s_mov_b64 exec, 1\n\tds_add_u32 %0, %1\n\ts_mov_b64 exec, -1" ::"v"(addr), "v"(vconst(1u)) : "memory");
  SPIN_LDS {
    if (__builtin_amdgcn_readfirstlane(lds_rd32(addr)) >= 8u) break;
  }
}
static_assert(K1R_PO + 13824 + 256 <= 163840 - 128 && K1R_ST_OFF + 144 * 8 <= 3 * K1R::SB, "K1R LDS");

DEVI void touch64(const char* sbase, unsigned voff, unsigned m0val) {
  asm volatile("s_mov_b32 m0, %2\n\ts_nop 0\n\tglobal_load_lds_dword %0, %1" : : "v"(voff), "s"(sbase), "s"(m0val) : "memory");
}

template <int MT, bool G1>
DEVI void k1_gemm_raw(char* smem, int hp, int mt0, int lane MF_ARG) {
  constexpr int NT = 4, nt0 = G1 ? 4 : 0;
  const unsigned lds0 = (unsigned)(size_t)smem;
  const int g = lane >> 4, r = lane & 15;
  unsigned ta[NT];
#pragma unroll
  for (int nt = 0; nt < NT; ++nt) {
    const int j = (nt0 + nt) * 16 + g * 4, seg = j / 40, cj = j - seg * 40 - (hp & 1) * 4;
    const int to = (seg < 3 && cj >= 0 && cj < 36) ? seg * 80 + cj * 2 + (cj >= 20 ? 8 : 0) : 40;
    ta[nt] = lds0 + (unsigned)(K1R_T_OFF + (mt0 * 16 + r) * K1_TP + to);
    asm volatile("" : "+v"(ta[nt]));
  }
  unsigned sa = lds0 + (unsigned)(K1R_ST_OFF + (mt0 * 16 + r) * 8), ca = lds0 + (unsigned)(K1R_CS_OFF + (nt0 * 16 + g * 4) * 4);
  asm volatile("" : "+v"(sa), "+v"(ca));
  f32x4 acc[MT][NT];
#pragma unroll
  for (int mt = 0; mt < MT; ++mt)
#pragma unroll
    for (int nt = 0; nt < NT; ++nt) acc[mt][nt] = f32x4{0.f, 0.f, 0.f, 0.f};
  {
    GemmMath<K1R, MT, 4, 3, 4, false, 0, G1> gm(acc);
    if constexpr (G1) {
#pragma unroll
      for (int mt = 0; mt < MT; ++mt) gm.accq[mt] = f32x4{0.f, 0.f, 0.f, 0.f};
    }
    MF(14);
    if constexpr (!G1) {
      if (mt0 < 7) asm volatile("s_waitcnt vmcnt(0)" ::: "memory");
    }
    MF(15);
    gm.template run<9>(lds0, mt0, nt0, lane, 0u);
    MF(2);
    if constexpr (G1) {
#pragma unroll
      for (int mt = 0; mt < MT; ++mt) {
        const f32x4 q = gm.accq[mt];
        const float d = (r & 2) ? ((r & 1) ? q[3] : q[2]) : ((r & 1) ? q[1] : q[0]);
        const float qq = sum_g(g == (r >> 2) ? d : 0.f);
        float mb, rinv;
        ln_finish(acc[mt][3][0], qq, 1.0f / (float)D_, mb, rinv);
        const f32x2 st = {-mb, rinv};
        if (g == 2) asm volatile("ds_write_b64 %0, %1 offset:%2" ::"v"(sa), "v"(st), "n"(mt * 128) : "memory");
      }
    }
  }
  if (mt0 == 0 && r == 0) {
#pragma unroll
    for (int nt = 0; nt < NT; ++nt) *reinterpret_cast<f32x4*>(smem + K1R_CS_OFF + ((nt0 + nt) * 16 + g * 4) * 4) = acc[0][nt];
  }
  msync(lds0 + (unsigned)K1R_CNT);
  MF(3);
  f32x2 st[MT];
  f32x4 cs[NT];
#pragma unroll
  for (int nt = 0; nt < NT; ++nt) asm volatile("ds_read_b128 %0, %1 offset:%2" : "=v"(cs[nt]) : "v"(ca), "n"(nt * 64) : "memory");
#pragma unroll
  for (int mt = 0; mt < MT; ++mt) asm volatile("ds_read_b64 %0, %1 offset:%2" : "=v"(st[mt]) : "v"(sa), "n"(mt * 128) : "memory");
#pragma unroll
  for (int i = 0; i < MT; ++i) asm volatile("s_waitcnt lgkmcnt(0)" : "+v"(st[i]));
#pragma unroll
  for (int i = 0; i < NT; ++i) asm volatile("" : "+v"(cs[i]));
#pragma unroll
  for (int mt = 0; mt < MT; ++mt) {
    f32x2 M = {st[mt][0], st[mt][0]}, R = {st[mt][1], st[mt][1]};
    asm volatile("" : "+v"(M), "+v"(R));
#pragma unroll
    for (int nt = 0; nt < NT; ++nt) {
      const f32x2 v0 = R * __builtin_elementwise_fma(M, f32x2{cs[nt][0], cs[nt][1]}, f32x2{acc[mt][nt][0], acc[mt][nt][1]});
      const f32x2 v1 = R * __builtin_elementwise_fma(M, f32x2{cs[nt][2], cs[nt][3]}, f32x2{acc[mt][nt][2], acc[mt][nt][3]});
      acc[mt][nt][0] = __uint_as_float(pack2v(v0));
      acc[mt][nt][1] = __uint_as_float(pack2v(v1));
      const f32x2 pk = {acc[mt][nt][0], acc[mt][nt][1]};
      asm volatile("ds_write_b64 %0, %1 offset:%2" ::"v"(ta[nt]), "v"(pk), "n"(mt * 16 * K1_TP) : "memory");
    }
  }
}

DEVI void dma16m(const char* sbase, unsigned voff, unsigned m0val, unsigned long long mask) {
  asm volatile("s_mov_b32 m0, %2\n\ts_mov_b64 exec, %3\n\tglobal_load_lds_dwordx4 %0, %1\n\ts_mov_b64 exec, -1"
               :
               : "v"(voff), "s"(sbase), "s"(m0val), "s"(mask)
               : "memory");
}
template <int N>
DEVI void wait_vm() {
  asm volatile("s_waitcnt vmcnt(%0)" ::"n"(N) : "memory");
}

constexpr int LDS_ALL = 163840;
constexpr int LW_OFF = LDS_ALL - 128;
constexpr int LW_GO = 0, LW_T = 2, LW_O = 4  , LW_X1 = 6, LW_H = 8, LW_X2 = 12, LW_PK = 13, LW_Z = 14, LW_PAIR = 16, LW_AD = 20,
              LW_L = 22, LW_S = 24, LW_ST = 29, LW_HELLO = 30, LW_Q = 31;
DEVI void announce(unsigned addr, unsigned n, char* ws, int kind, int who, bool act) {
  const char* cb = uni_ptr(ws + OFF_CNT + (size_t)kind * 256 * 128);
  const unsigned long long mask = __ballot(act);
  unsigned t, u;
  asm volatile(
      "s_waitcnt vmcnt(0)\n\t"
      "s_mov_b64 exec, 1\n\t"
      "ds_add_rtn_u32 %0, %2, %3\n\t"
      "s_waitcnt lgkmcnt(0)\n\t"
      "v_readfirstlane_b32 %1, %0\n\t"
      "s_cmp_eq_u32 %1, %4\n\t"
      "s_cselect_b64 exec, %5, 0\n\t"
      "global_atomic_add %6, %3, %7 sc1\n\t"
      "s_mov_b64 exec, -1\n\t"
      "s_nop 0"
      : "=&v"(t), "=&s"(u)
      : "v"(addr), "v"(vconst(1u)), "s"(n - 1u), "s"(mask), "v"((unsigned)(who & 255) * 128u), "s"(cb)
      : "memory", "scc");
}
DEVI void cnt_wait(const char* ws, int kind, int who, unsigned n) {
  const char* cp = ws + OFF_CNT + (size_t)(kind * 256 + who) * 128;
#pragma unroll 1
  for (unsigned spin = 0; spin < SPIN_MEM; ++spin) {
    unsigned f;
    asm volatile("global_load_dword %0, %1, off sc1\n\ts_waitcnt vmcnt(0)" : "=&v"(f) : "v"(cp) : "memory");
    if (__builtin_amdgcn_readfirstlane(f) == n - 1u) break;
    __builtin_amdgcn_s_sleep(1);
  }
}
DEVI void dma16_sc1(const char* sbase, unsigned voff, unsigned m0val) {
  asm volatile("s_mov_b32 m0, %2\n\ts_nop 0\n\tglobal_load_lds_dwordx4 %0, %1 sc1"
               :
               : "v"(voff), "s"(sbase), "s"(m0val)
               : "memory");
}
DEVI void bar() { __builtin_amdgcn_s_barrier(); }
DEVI void lds_inc(unsigned addr, unsigned by = 1u) {
  asm volatile("s_mov_b64 exec, 1\n\tds_add_u32 %0, %1\n\ts_mov_b64 exec, -1" ::"v"(addr), "v"(vconst(by)) : "memory");
}
DEVI void lds_spin(unsigned addr, unsigned n) {
  SPIN_LDS {
    if (__builtin_amdgcn_readfirstlane(lds_rd32(addr)) >= n) break;
    __builtin_amdgcn_s_sleep(1);
  }
}
template <int SL = 1>
DEVI void lds_spin4(unsigned addr, unsigned n) {
  SPIN_LDS {
    if (((__builtin_amdgcn_readfirstlane(lds_rd32(addr)) + (0x80u - n) * 0x01010101u) & 0x80808080u) == 0x80808080u) break;
    __builtin_amdgcn_s_sleep(SL);
  }
}
DEVI int lane_id() {
  int l;
  asm volatile("v_mbcnt_lo_u32_b32 %0, -1, 0\n\tv_mbcnt_hi_u32_b32 %0, -1, %0" : "=v"(l));
  __builtin_assume((unsigned)l < 64u);
  return l;
}

template <int I>
DEVI u32x4 tr8(const u32x4 (&v)[8]) {
  constexpr unsigned sel = (I & 1) ? 0x07060302u : 0x05040100u;
  return u32x4{__builtin_amdgcn_perm(v[1][I / 2], v[0][I / 2], sel), __builtin_amdgcn_perm(v[3][I / 2], v[2][I / 2], sel),
               __builtin_amdgcn_perm(v[5][I / 2], v[4][I / 2], sel), __builtin_amdgcn_perm(v[7][I / 2], v[6][I / 2], sel)};
}
template <int PITCH, bool SUM, int... I>
DEVI void tr8_store(const u32x4 (&v)[8], unsigned dst, float (&cs)[8], std::integer_sequence<int, I...>) {
  auto one = [&](auto I_) {
    const u32x4 o = tr8<I_>(v);
    const unsigned d_ = dst;
    asm volatile("ds_write_b128 %0, %1 offset:%2" ::"v"(d_), "v"(o), "n"(I_ * PITCH) : "memory");
    if constexpr (SUM) {
#pragma unroll
      for (int d = 0; d < 4; ++d) {
        const unsigned od = o[d];
        cs[I_] = __builtin_amdgcn_fdot2_f32_bf16(__builtin_bit_cast(bf16x2, od), __builtin_bit_cast(bf16x2, 0x3f803f80u), cs[I_], false);
      }
    }
  };
  (one(std::integral_constant<int, I>{}), ...);
}
template <int NB>
DEVI void lds_out(unsigned src, char* dst, int lane) {
  constexpr int N = (NB + 1023) / 1024;
  u32x4 t[N];
  const unsigned a = src + (unsigned)lane * 16u;
#pragma unroll
  for (int i = 0; i < N; ++i) asm volatile("ds_read_b128 %0, %1 offset:%2" : "=v"(t[i]) : "v"(a), "n"(i * 1024) : "memory");
  const char* d = uni_ptr(dst);
#pragma unroll
  for (int i = 0; i < N; ++i) {
    asm volatile("s_waitcnt lgkmcnt(%1)" : "+v"(t[i]) : "n"(N - 1 - i) : "memory");
    if (i < N - 1 || lane < (NB - (N - 1) * 1024) / 16)
      asm volatile("global_store_dwordx4 %0, %1, %2 sc1\n\ts_nop 1" ::"v"((unsigned)lane * 16u), "v"(t[i]), "s"(d + i * 1024) : "memory");
  }
}
template <int A, int B, class F>
DEVI void static_for(F f) {
  if constexpr (A < B) {
    f(std::integral_constant<int, A>{});
    static_for<A + 1, B>(f);
  }
}
constexpr int WSP_V = 3;
constexpr int RAW_S1 = 7;
constexpr int RAW_S0 = 4;
template <int E>
struct RawX {
  static constexpr int first(int s) {
    return s <= RAW_S0 ? 0 : (s > RAW_S1 ? E : (E * (s - RAW_S0) + RAW_S1 - RAW_S0) / (RAW_S1 + 1 - RAW_S0));
  }
  static constexpr int n(int s) { return first(s + 1) - first(s); }
};
struct RawLd {
  static constexpr int VPS = 10, SB = K1R::SB;
  unsigned xoff[5], woff[5], m0x;
  unsigned long long xact0, wact[5], tag;
  const char *xp, *wp;
  int qb;
  DEVI void q_hook(int st, char* ws, unsigned lds0, int hp) {
    if ((qb >> 1) == st) {
      const int lane = lane_id(), g = lane >> 4;
      const unsigned a = lds0 + (unsigned)((st & 3) * SB + K1R::XB + ((qb & 1) * 32 + 16 * (g >> 1) + 4 * (g & 1) + ((lane >> 2) & 3)) * 288 +
                                           (lane & 3) * 8 + (hp & 1) * 8);
      const unsigned a2 = a + ((lane & 3) >= 1 ? 8u : 0u), a4 = a + ((lane & 3) >= 2 ? 8u : 0u);
      u32x2 qf[7][2];
#pragma unroll
      for (int nt = 0; nt < 7; ++nt)
        asm volatile("ds_read_b64_tr_b16 %0, %2 offset:%3\n\tds_read_b64_tr_b16 %1, %2 offset:%4"
                     : "=&v"(qf[nt][0]), "=&v"(qf[nt][1])
                     : "v"(nt == 2 ? a2 : (nt == 4 ? a4 : a)), "n"(nt * 32 + (nt > 4 ? 16 : (nt > 2 ? 8 : 0))),
                       "n"(2304 + nt * 32 + (nt > 4 ? 16 : (nt > 2 ? 8 : 0)))
                     : "memory");
      const char* d = uni_ptr(ws + OFF_PQ + (size_t)(hp * 18 + qb) * 7168 + 3072);
#pragma unroll
      for (int nt = 0; nt < 7; ++nt) {
        asm volatile("s_waitcnt lgkmcnt(%2)" : "+v"(qf[nt][0]), "+v"(qf[nt][1]) : "n"(12 - 2 * nt) : "memory");
        const u32x4 v = {qf[nt][0][0], qf[nt][0][1], qf[nt][1][0], qf[nt][1][1]};
        asm volatile("global_store_dwordx4 %0, %1, %2 offset:%3 sc1\n\ts_nop 1" ::"v"((unsigned)lane * 16u), "v"(v), "s"(d), "n"(nt * 1024 - 3072) : "memory");
      }
      if (st == 8) lds_wr32(lds0 + (unsigned)(LW_OFF + LW_Q * 4), 1u);
    }
  }
  DEVI void stage_x(unsigned so) {
    dma16m(xp, xoff[0], m0x + so, xact0);
#pragma unroll
    for (int i = 1; i < 4; ++i) dma16(xp, xoff[i], m0x + so + (unsigned)(i * 1024));
    dma16m(xp, xoff[4], m0x + so + 4096u, 0xffffffffull);
    xp += 128;
  }
  DEVI void stage_w(unsigned so) {
#pragma unroll
    for (int i = 0; i < 5; ++i) dma16m(wp, woff[i], m0x + so + (unsigned)(K1R::XB + i * 1024), wact[i]);
    wp += 64 * NQKV * 2;
  }
  DEVI void stage(unsigned so) {
    stage_x(so);
    stage_w(so);
  }
  DEVI void front(const u16* x, const u16* wqkv, const u16* wo, char* ws, int bid, unsigned lds0, int wl, int lane, int rt, int hp,
                  int pj MF_ARG) {
    xp = uni_ptr((const char*)x);
    wp = uni_ptr((const char*)wqkv + ((hp * 72) & ~15));
    m0x = lds0 + (unsigned)(wl * 288 * 16);
    xact0 = wl ? ~0ull : ~0xffull;
    const int row0 = wl * 36 + (lane >> 3);
#pragma unroll
    for (int i = 0; i < 5; ++i) {
      const int row = row0 + i * 8;
      int gr = rt * 128 - 16 + row;
      if (gr < 0) gr = 0;
      xoff[i] = (unsigned)(gr * (D_ * 2)) + (unsigned)(((lane ^ (row >> 1)) & 7) * 16);
    }
    stage_x(0u);
    u32x2 tg;
    asm volatile("global_load_dwordx2 %0, %1, %2 sc1" : "=v"(tg) : "v"(0u), "s"(uni_ptr(ws + OFF_TAG)) : "memory");
    asm volatile("" : "+v"(lane));
#pragma unroll
    for (int i = 0; i < 5; ++i) {
      const unsigned c = (unsigned)(i * 64 + (lane & 63));
      const unsigned wr = (c * 3641u) >> 16, wc = c - wr * 18u, seg = (wc * 13u) >> 6;
      wact[i] = __ballot(wc < 15u) & (i < 4 ? ~0ull : 0xffffffffull);
      woff[i] = (unsigned)(wl * 16 + swap23((int)wr)) * (unsigned)(NQKV * 2) + seg * (unsigned)(D_ * 2 - 80) + wc * 16u;
    }
    stage_w(0u);
    stage((unsigned)SB);
    MF(10);
    asm volatile("s_waitcnt vmcnt(%1)" : "+v"(tg) : "n"(VPS) : "memory");
    tag = (((unsigned long long)(unsigned)__builtin_amdgcn_readfirstlane(tg[1]) << 32) | (unsigned)__builtin_amdgcn_readfirstlane(tg[0])) +
          0x9e3779b97f4a7c15ull;
    MF(1);
    __builtin_amdgcn_s_barrier();
    MF(15);
    if (wl == 0 && lane == 0)
      asm volatile("global_store_dwordx2 %0, %1, off sc1\n\ts_nop 1" ::"v"(ws + OFF_HELLO + bid * 8), "v"(tag) : "memory");
    stage((unsigned)(2 * SB));
    qb = wl == 3 ? rt : ((wl == 2 && rt < 2) ? 16 + rt : 99);
    q_hook(0, ws, lds0, hp);
    const int nb = pj;
    const bool po = pj >= 0 && wl < 2;
    u32x4 v[8];
    unsigned da = 0u;
    if (po) {
      const int l54 = lane < 54 ? lane : 53, gl = l54 / 9, c = l54 - gl * 9, kl = wl * 6 + gl, t = nb / 6, kg = (nb - t * 6) * 12 + kl;
      da = lds0 + (unsigned)(K1R_PO + (kl >> 2) * 4608 + (16 * (kl & 1) + ((kl >> 1) & 1)) * 144 + c * 16);
      const unsigned sa[2] = {(unsigned)(okmap(kg * 8) * D_ + t * 72 + c * 8) * 2u, (unsigned)(okmap(kg * 8 + 4) * D_ + t * 72 + c * 8) * 2u};
      const char* ps = uni_ptr((const char*)wo);
#pragma unroll
      for (int j = 0; j < 8; ++j)
        asm volatile("global_load_dwordx4 %0, %1, %2 offset:%3" : "=v"(v[j]) : "v"(sa[j / 4]), "s"(ps), "n"((j % 4) * D_ * 2) : "memory");
    } else {
#pragma unroll
      for (int j = 0; j < 8; ++j) asm volatile("" : "=v"(v[j]));
    }
    unsigned slot = (unsigned)(3 * SB);
#pragma unroll
    for (int S = 0; S < RAW_S0; ++S) {
      wait_vm<VPS>();
      __builtin_amdgcn_s_barrier();
      if (S == 0) MF(16);
      if (S == 1) MF(17);
      if (S == 2) MF(18);
      if (S == 3) MF(19);
      stage(slot);
      slot = (slot == (unsigned)(3 * SB)) ? 0u : slot + (unsigned)SB;
      q_hook(S + 1, ws, lds0, hp);
      if (po && S == 1) {
        float cs[8];
        asm volatile("" : "+v"(v[0]), "+v"(v[1]), "+v"(v[2]), "+v"(v[3]), "+v"(v[4]), "+v"(v[5]), "+v"(v[6]), "+v"(v[7]));
        tr8_store<288, false>(v, da, cs, std::make_integer_sequence<int, 8>{});
        asm volatile("s_waitcnt lgkmcnt(0)" ::: "memory");
      }
      if (po && S == 2) lds_out<6912>(lds0 + (unsigned)(K1R_PO + wl * 6912), ws + OFF_PO + (size_t)nb * 13824 + wl * 6912, lane);
    }
  }
};
template <bool JOB>
DEVI void k1_raw_loader(RawLd& ld, const u16* __restrict__ w_in, const u16* __restrict__ w_out, char* __restrict__ ws,
                        unsigned long long tag, int bid, unsigned lds0, int wl, int lane, int rt, int hp, int ct, int job, int pj MF_ARG) {
  constexpr int VPS = RawLd::VPS, SB = RawLd::SB, E = JOB ? 16 : 0;
  const bool isi = job < 144;
  const int kb = isi ? job >> 3 : job - 144;
  const int pitch = isi ? F_ * 2 : D_ * 2;
  const int l36 = lane < 36 ? lane : 35;
  unsigned sa = (unsigned)l36 * 16u, da;
  if (isi) da = lds0 + (unsigned)((l36 >= 18 ? 9216 - 288 : 0) + wl * 8 * 288 + l36 * 16);
  else da = lds0 + (unsigned)((l36 / 9) * (4608 - 144) + (16 * (wl & 1) + (wl >> 1)) * 144 + l36 * 16);
  asm volatile("" : "+v"(sa), "+v"(da));
  u32x4 v[2][8];
  const char* ps = uni_ptr((const char*)(isi ? w_in + (job & 7) * 576 : w_out) + (size_t)(kb * 32 + wl * 8) * pitch);
  auto extra = [&](auto J_) {
    constexpr int j = J_;
    const char* q = ps + (j / 2) * pitch;
    u32x4 t;
    asm volatile("global_load_dwordx4 %0, %1, %2 offset:%3" : "=v"(t) : "v"(sa), "s"(q), "n"((j % 2) * 576) : "memory");
    v[j % 2][j / 2] = t;
  };
  auto step = [&](auto S_) {
    constexpr int S = S_;
    wait_vm<RawX<E>::n(S - 2) + (S <= 6 ? VPS : 0) + RawX<E>::n(S - 1)>();
    __builtin_amdgcn_s_barrier();
    MF(16 + S);
    if constexpr (S == RAW_S0) {
      static_assert(RAW_S0 >= 4, "wo blocks");
      if (pj >= 0 && wl == 0 && lane == 0)
        asm volatile("global_store_dwordx2 %0, %1, off sc1\n\ts_nop 1" ::"v"(ws + OFF_POT + (pj / 6) * 128 + (pj % 6) * 8), "v"(tag) : "memory");
    }
    if constexpr (S <= 5) ld.stage((unsigned)(((S + 3) % 4) * SB));
    ld.q_hook(S + 1, ws, lds0, hp);
    static_for<RawX<E>::first(S), RawX<E>::first(S + 1)>(extra);
  };
  const char* hp_ = ws + OFF_HELLO + lane * 32;
  u32x4 a, b;
  if (wl == 1) asm volatile("global_load_dwordx4 %0, %2, off sc1\n\tglobal_load_dwordx4 %1, %2, off offset:16 sc1" : "=&v"(a), "=&v"(b) : "v"(hp_) : "memory");
  else asm volatile("" : "=v"(a), "=v"(b));
  static_for<RAW_S0, 8>(step);
  MF(2);
  if (wl == 1) {
    asm volatile("" : "+v"(a), "+v"(b));
    const unsigned t0 = (unsigned)tag, t1 = (unsigned)(tag >> 32);
#pragma unroll 1
    for (unsigned spin = 0; spin < SPIN_MEM; ++spin) {
      const unsigned bad = ((a[0] ^ t0) | (a[2] ^ t0) | (b[0] ^ t0) | (b[2] ^ t0)) | ((a[1] ^ t1) | (a[3] ^ t1) | (b[1] ^ t1) | (b[3] ^ t1));
      if (!__any(bad != 0u)) break;
      __builtin_amdgcn_s_sleep(4);
      asm volatile("global_load_dwordx4 %0, %2, off sc1\n\tglobal_load_dwordx4 %1, %2, off offset:16 sc1\n\ts_waitcnt vmcnt(0)"
                   : "=&v"(a), "=&v"(b)
                   : "v"(hp_)
                   : "memory");
    }
    lds_wr32(lds0 + (unsigned)(LW_OFF + LW_HELLO * 4), 1u);
    if (bid == 0 && lane == 0) asm volatile("global_store_dwordx2 %0, %1, off sc1\n\ts_nop 1" ::"v"(ws + OFF_TAG), "v"(tag) : "memory");
  }
  MF(11);
  if (rt < 2) lds_spin(lds0 + (unsigned)(LW_OFF + LW_Q * 4), 1u);
  lds_spin(lds0 + (unsigned)K1R_CNT, 8u);
  if constexpr (JOB) {
#pragma unroll
    for (int b = 0; b < 2; ++b)
      asm volatile("s_waitcnt vmcnt(0)"
                   : "+v"(v[b][0]), "+v"(v[b][1]), "+v"(v[b][2]), "+v"(v[b][3]), "+v"(v[b][4]), "+v"(v[b][5]), "+v"(v[b][6]), "+v"(v[b][7])
                   :
                   : "memory");
  }
  MF(12);
  const char* tp = ws + OFF_POT + ct * 128 + (lane < 6 ? lane : 5) * 8;
  u32x2 f;
  asm volatile("global_load_dwordx2 %0, %1, off sc1" : "=v"(f) : "v"(tp) : "memory");
  if constexpr (JOB) {
    static_assert(SB == 4 * 9216, "staging");
#pragma unroll
    for (int b = 0; b < 2; ++b) {
      float cs[8] = {0.f, 0.f, 0.f, 0.f, 0.f, 0.f, 0.f, 0.f};
      tr8_store<288, true>(v[b], da + (unsigned)(b * 18432), cs, std::make_integer_sequence<int, 8>{});
      const f32x4 c0 = {cs[0], cs[1], cs[2], cs[3]}, c1 = {cs[4], cs[5], cs[6], cs[7]};
      const unsigned ca = lds0 + (unsigned)(3 * SB + wl * 2304) + sa * 2u;
      asm volatile("ds_write_b128 %0, %1 offset:%2" ::"v"(ca), "v"(c0), "n"(b * 1152) : "memory");
      asm volatile("ds_write_b128 %0, %1 offset:%2" ::"v"(ca), "v"(c1), "n"(b * 1152 + 16) : "memory");
    }
    lds_inc(lds0 + (unsigned)(LW_OFF + LW_ST * 4));
    lds_spin(lds0 + (unsigned)(LW_OFF + LW_ST * 4), 4u);
    if (isi) {
      const int j = (job & 7) * 4 + wl, ti = j < 16 ? 2 * j + 1 : 2 * (j - 16);
      lds_out<9216>(lds0 + (unsigned)(wl * 9216), ws + OFF_PI + (size_t)(ti * 18 + kb) * 9216, lane);
      f32x4 c[4];
      const unsigned ca = lds0 + (unsigned)(3 * SB + wl * 576) + sa;
#pragma unroll
      for (int i = 0; i < 4; ++i) asm volatile("ds_read_b128 %0, %1 offset:%2" : "=v"(c[i]) : "v"(ca), "n"(i * 2304) : "memory");
      asm volatile("s_waitcnt lgkmcnt(0)" : "+v"(c[0]), "+v"(c[1]), "+v"(c[2]), "+v"(c[3]));
      const f32x4 t = (c[0] + c[1]) + (c[2] + c[3]);
      if (lane < 36) pk_st16(ws + OFF_CSP + ((size_t)kb * F_ + ti * 144) * 4 + sa, __builtin_bit_cast(u32x4, t));
    } else {
      lds_out<4608>(lds0 + (unsigned)(wl * 9216), ws + OFF_PW + (size_t)(wl * 144 + kb) * 4608, lane);
      lds_out<4608>(lds0 + (unsigned)(wl * 9216 + 4608), ws + OFF_PW + (size_t)(wl * 144 + 72 + kb) * 4608, lane);
    }
  }
  MF(8);
  announce(lds0 + (unsigned)(LW_OFF + LW_PK * 4), 4u, ws, CN_PK, isi ? 8 + (job & 3) * 4 + lane : 24 + lane, JOB && lane < (isi ? 4 : 8));
  MF(9);
  if (wl == 0) {
    asm volatile("" : "+v"(f));
#pragma unroll 1
    for (unsigned spin = 0; spin < SPIN_MEM; ++spin) {
      if (!__any(f[0] != (unsigned)tag || f[1] != (unsigned)(tag >> 32))) break;
      __builtin_amdgcn_s_sleep(2);
      asm volatile("global_load_dwordx2 %0, %1, off sc1\n\ts_waitcnt vmcnt(0)" : "=&v"(f) : "v"(tp) : "memory");
    }
  }
}

template <int MT, int NT>
DEVI void k1_gemm(char* smem, const float* __restrict__ csq, int hp, int mt0, int nt0,
                  int lane MK_ARG) {
  const unsigned lds0 = (unsigned)(size_t)smem;
  const int g = lane >> 4;
  f32x4 cs[NT];
  unsigned ta[NT];
  const char* csp = uni_ptr((const char*)(csq + (hp & 1) * 4));
#pragma unroll
  for (int nt = 0; nt < NT; ++nt) {
    int c = (nt0 + nt) * 16 + g * 4;
    if (c > 104) c = 104;
    const int part = c / 36, ci = c - part * 36;
    asm volatile("global_load_dwordx4 %0, %1, %2 sc1" : "=v"(cs[nt]) : "v"((unsigned)(part * 40 + ci) * 4u), "s"(csp));
    const int to = (nt0 + nt) * 16 + g * 4 > 104 ? 40 : part * 80 + ci * 2 + (ci >= 20 ? 8 : 0);
    ta[nt] = lds0 + (unsigned)(K1_T_OFF + (mt0 * 16 + (lane & 15)) * K1_TP + to);
    asm volatile("" : "+v"(ta[nt]));
  }
  unsigned sa = lds0 + (unsigned)(K1_ST_OFF + (mt0 * 16 + (lane & 15)) * 8);
  asm volatile("" : "+v"(sa));
  f32x4 acc[MT][NT];
#pragma unroll
  for (int mt = 0; mt < MT; ++mt)
#pragma unroll
    for (int nt = 0; nt < NT; ++nt) acc[mt][nt] = f32x4{0.f, 0.f, 0.f, 0.f};
  bar();
  MK(1);
  {
    GemmMath<K1C, MT, NT, (NT == 7 ? 5 : 3), (NT == 3 ? 6 : NT), true, 2> gm(acc);
    gm.template run<9>(lds0, mt0, nt0, lane, 0u);
  }
  asm volatile("s_waitcnt vmcnt(0)" ::: "memory");
#pragma unroll
  for (int nt = 0; nt < NT; ++nt) asm volatile("" : "+v"(cs[nt]));
  MK(2);
  f32x2 st[MT];
#pragma unroll
  for (int mt = 0; mt < MT; ++mt) asm volatile("ds_read_b64 %0, %1 offset:%2" : "=v"(st[mt]) : "v"(sa), "n"(mt * 128) : "memory");
#pragma unroll
  for (int mt = 0; mt < MT; ++mt) {
    if (mt == 0) {
#pragma unroll
      for (int i = 0; i < MT; ++i) asm volatile("s_waitcnt lgkmcnt(0)" : "+v"(st[i]));
    }
    f32x2 M = {st[mt][0], st[mt][0]}, R = {st[mt][1], st[mt][1]};
    asm volatile("" : "+v"(M), "+v"(R));
#pragma unroll
    for (int nt = 0; nt < NT; ++nt) {
      const f32x2 v0 = R * __builtin_elementwise_fma(M, f32x2{cs[nt][0], cs[nt][1]}, f32x2{acc[mt][nt][0], acc[mt][nt][1]});
      const f32x2 v1 = R * __builtin_elementwise_fma(M, f32x2{cs[nt][2], cs[nt][3]}, f32x2{acc[mt][nt][2], acc[mt][nt][3]});
      acc[mt][nt][0] = __uint_as_float(pack2v(v0));
      acc[mt][nt][1] = __uint_as_float(pack2v(v1));
      const f32x2 pk = {acc[mt][nt][0], acc[mt][nt][1]};
      asm volatile("ds_write_b64 %0, %1 offset:%2" ::"v"(ta[nt]), "v"(pk), "n"(mt * 16 * K1_TP) : "memory");
    }
  }
}

DEVI float vmax(float a, float b) {
  float r;
  asm("v_max_f32 %0, %1, %2" : "=v"(r) : "v"(a), "v"(b));
  return r;
}
DEVI float max_g(float x) {
  const unsigned u = __float_as_uint(x);
  const auto p = __builtin_amdgcn_permlane32_swap(u, u, false, false);
  const unsigned v = __float_as_uint(vmax(__uint_as_float(p[0]), __uint_as_float(p[1])));
  const auto q = __builtin_amdgcn_permlane16_swap(v, v, false, false);
  return vmax(__uint_as_float(q[0]), __uint_as_float(q[1]));
}
struct Attn {
  unsigned qa, ka, kb, va, vb, m0, m1, m2, pm[2], ok, oa;
  float fill[4];
  DEVI void setup(unsigned T, int r0, int hp, int wave, int lane) {
    const int g = lane >> 4, r = lane & 15, blk = wave >> 1, hd = wave & 1;
    const unsigned co = (unsigned)(g < 2 ? hd * 48 + g * 16 : 32);
    ka = T + (unsigned)((blk * 32 + 2 * r) * K1_TP) + co;
    kb = T + (unsigned)((blk * 32 + 2 * (r & 7)) * K1_TP) + co;
    const int vr = 4 * g + ((lane >> 2) & 3);
    va = T + (unsigned)((blk * 32 + 2 * vr) * K1_TP + (lane & 3) * 8);
    vb = T + (unsigned)((blk * 32 + 2 * (vr & 7)) * K1_TP + (lane & 3) * 8);
    m0 = (g < 2 || (g == 2 && hd == 0)) ? ~0u : 0u;
    m1 = (g < 2 || (g == 2 && hd == 1)) ? ~0u : 0u;
    m2 = g < 2 ? ~0u : 0u;
    int wmax = (r0 + blk * 32) / 2 + r;
    if (wmax > 7) wmax = 7;
    ok = 0u;
    pm[0] = pm[1] = 0u;
    const float cm = __uint_as_float(vconst(0xce6e6b28u)), ci = __uint_as_float(vconst(0xff800000u));
    const unsigned ch = vconst(0xffffu);
#pragma unroll
    for (int e = 0; e < 4; ++e) {
      const int w1 = r + 8 - 4 * g - e, w = w1 & 15;
      fill[e] = w <= 7 ? cm : ci;
      if (w <= wmax) ok |= 1u << e;
      if (w1 >= 8) pm[e >> 1] |= ch << (16 * (e & 1));
    }
    oa = (unsigned)((8 + hp * 4 + hd * 2 + (g >> 1)) * 1024 + ((blk & 1) * 32 + 2 * r + (g & 1)) * 16);
    asm volatile("" : "+v"(ka), "+v"(kb), "+v"(va), "+v"(vb), "+v"(m0), "+v"(m1), "+v"(m2), "+v"(pm[0]), "+v"(pm[1]), "+v"(ok), "+v"(oa));
    asm volatile("" : "+v"(fill[0]), "+v"(fill[1]), "+v"(fill[2]), "+v"(fill[3]));
  }
  DEVI void run(char* o, int r0, int hp, int wave, int lane) {
    const int hd = wave & 1;
    u32x4 q[2], k0[2], k1[2];
    u32x2 v[2][2][2];
#pragma unroll
    for (int p = 0; p < 2; ++p) {
      asm volatile("ds_read_b128 %0, %1 offset:%2" : "=v"(q[p]) : "v"(ka), "n"((16 + p) * K1_TP) : "memory");
      asm volatile("ds_read_b128 %0, %1 offset:%2" : "=v"(k0[p]) : "v"(ka), "n"(p * K1_TP + 80) : "memory");
      asm volatile("ds_read_b128 %0, %1 offset:%2" : "=v"(k1[p]) : "v"(kb), "n"((32 + p) * K1_TP + 80) : "memory");
    }
    const unsigned vh = (unsigned)hd * 48u;
#pragma unroll
    for (int p = 0; p < 2; ++p) {
      asm volatile("ds_read_b64_tr_b16 %0, %1 offset:%2" : "=v"(v[p][0][0]) : "v"(va + vh), "n"(p * K1_TP + 160) : "memory");
      asm volatile("ds_read_b64_tr_b16 %0, %1 offset:%2" : "=v"(v[p][0][1]) : "v"(vb + vh), "n"((32 + p) * K1_TP + 160) : "memory");
      asm volatile("ds_read_b64_tr_b16 %0, %1 offset:%2" : "=v"(v[p][1][0]) : "v"(va), "n"(p * K1_TP + 192) : "memory");
      asm volatile("ds_read_b64_tr_b16 %0, %1 offset:%2" : "=v"(v[p][1][1]) : "v"(vb), "n"((32 + p) * K1_TP + 192) : "memory");
    }
    const char* ob = uni_ptr(o + (size_t)((r0 >> 6) + (wave >> 2)) * (72 * 1024));
    const bool okb[4] = {(ok & 1u) != 0u, (ok & 2u) != 0u, (ok & 4u) != 0u, (ok & 8u) != 0u};
    const float scale = 0.23570226039551584f, l2e = 1.4426950408889634f;
    const f32x4 z = {0.f, 0.f, 0.f, 0.f};
    f32x4 sc[2][2];
#pragma unroll
    for (int p = 0; p < 2; ++p) {
      asm volatile("s_waitcnt lgkmcnt(%3)" : "+v"(q[p]), "+v"(k0[p]), "+v"(k1[p]) : "n"(11 - 3 * p));
      const s16x8 qf = __builtin_bit_cast(s16x8, q[p] & u32x4{m0, m1, m2, m2});
      sc[p][0] = __builtin_amdgcn_mfma_f32_16x16x32_bf16(__builtin_bit_cast(s16x8, k0[p] & u32x4{~0u, ~0u, m2, m2}), qf, z, 0, 0, 0);
      sc[p][1] = __builtin_amdgcn_mfma_f32_16x16x32_bf16(__builtin_bit_cast(s16x8, k1[p] & u32x4{~0u, ~0u, m2, m2}), qf, z, 0, 0, 0);
    }
    float lg[2][4], mx[2], sum[2];
#pragma unroll
    for (int p = 0; p < 2; ++p) {
#pragma unroll
      for (int h = 0; h < 2; ++h) {
        const unsigned a0 = pack2(sc[p][0][2 * h], sc[p][0][2 * h + 1]), a1 = pack2(sc[p][1][2 * h], sc[p][1][2 * h + 1]);
        const unsigned a = (a1 & pm[h]) | (a0 & ~pm[h]);
        const unsigned b = pack2(bf_lo(a) * scale, bf_hi(a) * scale);
        lg[p][2 * h] = okb[2 * h] ? bf_lo(b) : fill[2 * h];
        lg[p][2 * h + 1] = okb[2 * h + 1] ? bf_hi(b) : fill[2 * h + 1];
      }
      mx[p] = vmax(vmax(lg[p][0], lg[p][1]), vmax(lg[p][2], lg[p][3]));
    }
#pragma unroll
    for (int p = 0; p < 2; ++p) mx[p] = max_g(mx[p]);
#pragma unroll
    for (int p = 0; p < 2; ++p) {
#pragma unroll
      for (int e = 0; e < 4; ++e) lg[p][e] = __builtin_amdgcn_exp2f((lg[p][e] - mx[p]) * l2e);
      sum[p] = (lg[p][0] + lg[p][1]) + (lg[p][2] + lg[p][3]);
    }
#pragma unroll
    for (int p = 0; p < 2; ++p) sum[p] = sum_g(sum[p]);
    asm volatile("s_waitcnt lgkmcnt(0)"
                 : "+v"(v[0][0][0]), "+v"(v[0][0][1]), "+v"(v[0][1][0]), "+v"(v[0][1][1]), "+v"(v[1][0][0]), "+v"(v[1][0][1]),
                   "+v"(v[1][1][0]), "+v"(v[1][1][1]));
    u32x2 y[2], yl;
#pragma unroll
    for (int p = 0; p < 2; ++p) {
      const float inv = __builtin_amdgcn_rcpf(sum[p]);
      const unsigned p0 = pack2(lg[p][0] * inv, lg[p][1] * inv), p1 = pack2(lg[p][2] * inv, lg[p][3] * inv);
      const s16x8 pf = __builtin_bit_cast(s16x8, u32x4{p0 & ~pm[0], p1 & ~pm[1], p0 & pm[0], p1 & pm[1]});
      const u32x4 v0 = {v[p][0][0][0], v[p][0][0][1], v[p][0][1][0], v[p][0][1][1]};
      const u32x4 v1 = {v[p][1][0][0], v[p][1][0][1], v[p][1][1][0], v[p][1][1][1]};
      const f32x4 o0 = __builtin_amdgcn_mfma_f32_16x16x32_bf16(__builtin_bit_cast(s16x8, v0), pf, z, 0, 0, 0);
      const f32x4 o1 = __builtin_amdgcn_mfma_f32_16x16x32_bf16(__builtin_bit_cast(s16x8, v1), pf, z, 0, 0, 0);
      y[p] = u32x2{pack2(o0[0], o0[1]), pack2(o0[2], o0[3])};
      yl[p] = hd ? pack2(o1[2], o1[3]) : pack2(o1[0], o1[1]);
    }
    const auto s0 = __builtin_amdgcn_permlane16_swap(y[0][0], y[1][0], false, false);
    const auto s1 = __builtin_amdgcn_permlane16_swap(y[0][1], y[1][1], false, false);
    const u32x4 w = {s0[0], s1[0], s0[1], s1[1]};
    asm volatile("global_store_dwordx4 %0, %1, %2 sc1\n\ts_nop 1" ::"v"(oa), "v"(w), "s"(ob) : "memory");
    if (lane < 16)
      asm volatile("global_store_dwordx2 %0, %1, %2 sc1\n\ts_nop 1" ::"v"(((oa & 1023u) >> 2) + (unsigned)((hp * 2 + hd) * 256)), "v"(yl), "s"(ob) : "memory");
  }
};

#define NL_MAIN 679
DEVI void math_start(char* smem, const char* code0, const u16* x, const u16* wqkv, char* ws, int bid, int wave, int lane) {
  const unsigned lds0 = (unsigned)(size_t)smem;
  const int tid = wave * 64 + lane, idx = bid >> 3;
  if (wave < 3) {
    static_assert(2 * SZ_ZP == (size_t)256 * 2048 && NCNT <= 16, "mark split");
    const u32x4 mk = {0xffffffffu, 0xffffffffu, 0xffffffffu, 0xffffffffu};
    char* c = ws + OFF_CNT + (size_t)((lane >> 3) * 256 + bid) * 128 + (lane & 7) * 16;
    pk_st16(wave < 2 ? ws + OFF_ZP + (size_t)((bid * 2 + wave) * 64 + lane) * 16 : c, mk);
    if (wave == 2 && lane < (NCNT - 8) * 8) pk_st16(c + 8 * 256 * 128, mk);
  }
  if (wave == 3) {
    const int li = idx * 54 + (lane < 54 ? lane : 53);
    touch64(uni_ptr((const char*)wqkv), (unsigned)(li * 1152 + 512), lds0 + (unsigned)(K1R_DUMMY + 768));
  }
  if (wave == 4) {
    constexpr int PER = (2 * NL_MAIN + 31) / 32;
    static_assert(PER <= 64, "one load");
    int li = idx * PER + (lane < PER ? lane : PER - 1);
    if (li > 2 * NL_MAIN - 1) li = 2 * NL_MAIN - 1;
    touch64(code0, (unsigned)li * 64u, lds0 + (unsigned)(K1R_DUMMY + 768));
  }
  const u32x4 ones = {0x3f803f80u, 0x3f803f80u, 0x3f803f80u, 0x3f803f80u};
  if (tid < 256) *reinterpret_cast<u32x4*>(smem + (tid >> 6) * K1R::SB + K1R::XB + (tid & 63) * 288 + 240) = ones;
  else if (tid < 288) *reinterpret_cast<u32x4*>(smem + ((tid - 256) >> 3) * K1R::SB + (tid & 7) * 16) = ones;
  else if (tid < 296) *reinterpret_cast<u32x4*>(smem + LW_OFF + (tid - 288) * 16) = u32x4{0u, 0u, 0u, 0u};
  asm volatile("s_waitcnt lgkmcnt(0)" ::: "memory");
}

template <int NST, int NSW, int WOFF, int XW, int X0, int S0, unsigned BAR>
struct ResX {
  static constexpr int WS = 4 * 4608, D = 2;
  s16x8 xf[XW];
  s16x8 wf[D + 1][5];
  unsigned wrd, wrd2, wrd4, xo;
  const char* xp;
  f32x4 (&acc)[5];
  DEVI ResX(f32x4 (&a)[5]) : acc(a) {}
  static constexpr int xreq(int T) {
    int n = X0 + 2 * (T + 1);
    if (n > T + 1 + XW) n = T + 1 + XW;
    return n < NST ? n : NST;
  }
  template <int T>
  DEVI void xld() {
    asm volatile("global_load_dwordx4 %0, %1, %2 sc1" : "=v"(xf[T % XW]) : "v"(xo), "s"(xp + T * 8192));
  }
  template <int T, int I>
  DEVI void rd() {
    constexpr int sl = ((T / 2 + S0) % NSW) * WS, hi = (sl / (3 * WS)) * (3 * WS), off = sl - hi + (T & 1) * 9216;
    const unsigned a = (I < 2 ? wrd : (I < 4 ? wrd2 : wrd4)) + (unsigned)hi;
    asm volatile("ds_read_b128 %0, %1 offset:%2" : "=v"(wf[T % (D + 1)][I]) : "v"(a), "n"(off + (I < 4 ? (I & 1) * 32 : 0)));
  }
  template <int T, int J>
  DEVI void unit() {
    acc[J] = __builtin_amdgcn_mfma_f32_16x16x32_bf16(wf[T % (D + 1)][J], xf[T % XW], acc[J], 0, 0, 0);
    __builtin_amdgcn_sched_barrier(0);
    if constexpr (T + D < NST) {
      rd<T + D, J>();
      __builtin_amdgcn_sched_barrier(0);
    }
    if constexpr (J == 2 && xreq(T) - xreq(T - 1) == 2) {
      xld<xreq(T - 1)>();
      __builtin_amdgcn_sched_barrier(0);
    }
  }
  template <int T, int... J>
  DEVI void step(std::integer_sequence<int, J...>) {
    constexpr int last = T + D - 1 < NST - 1 ? T + D - 1 : NST - 1;
    asm volatile("s_waitcnt lgkmcnt(%0)" ::"n"(5 * (last - T)));
    if constexpr ((T & 1) && ((BAR >> (T / 2)) & 1u)) bar();
    asm volatile("s_waitcnt vmcnt(%1)" : "+v"(xf[T % XW]) : "n"(xreq(T - 1) - 1 - T));
    __builtin_amdgcn_sched_barrier(0);
    (unit<T, J>(), ...);
    if constexpr (xreq(T) > xreq(T - 1)) xld<xreq(T) - 1>();
    __builtin_amdgcn_sched_barrier(0);
  }
  template <int... T>
  DEVI void steps(std::integer_sequence<int, T...>) {
    (step<T>(std::make_integer_sequence<int, 5>{}), ...);
  }
  template <int... I>
  DEVI void pro(std::integer_sequence<int, I...>) {
    (rd<I / 5, I % 5>(), ...);
  }
  DEVI void prow() { pro(std::make_integer_sequence<int, 5 * D>{}); }
  template <int T0, int... T>
  DEVI void prox(std::integer_sequence<int, T...>) {
    (xld<T0 + T>(), ...);
  }
  DEVI void setup(unsigned lds0, const char* xbase, int wave, int lane) {
    const int g = lane >> 4, r = lane & 15, kq = wave >> 2, wm = wave & 3;
    xp = xbase;
    xo = (unsigned)((4 * kq + g) * 1024 + (wm * 16 + r) * 16);
    const unsigned a = lds0 + (unsigned)(WOFF + kq * 4608 + (2 * (r & 7) + 16 * (g & 1) + (g >> 1)) * 144 + (r >> 3) * 16);
    wrd = a + (unsigned)(kq * 64);
    wrd2 = a + (unsigned)((kq ^ 1) * 64);
    wrd4 = a + 128u;
#pragma unroll
    for (int i = 0; i < 5; ++i) acc[i] = f32x4{0.f, 0.f, 0.f, 0.f};
  }
};

constexpr int P5_XW = 10;
constexpr int P5_X0 = 4;
constexpr unsigned P5_BAR = 0x2aaau;
constexpr int P5_W = 15 * 1024;
constexpr int P2_W = K1_ST_OFF - 18 * 4608;
static_assert(P2_W >= K1_T_OFF + 144 * K1_TP + 64 && P2_W % 16 == 0 && 8 * 3072 <= P5_W + 2 * 18432 && P5_W + 8 * 18432 <= LW_OFF,
              "LDS placement");

DEVI void res_ldx(u32x2 (&xv)[3], const char* src, bool rm, int rb, int ct, int wave, int lane) {
  const int g = lane >> 4, g2 = g & 1, rl = (wave & 3) * 16 + (lane & 15), hi = wave >> 2;
  const char* xp = uni_ptr(rm ? src + ((size_t)rb * 64 * D_ + ct * 72 + hi * 32) * 2 : src + (size_t)(rb * 72 + ct * 9 + hi * 4) * 1024);
  const unsigned o = rm ? (unsigned)(rl * (D_ * 2) + g * 8) : (unsigned)((g >> 1) * 1024 + rl * 16 + g2 * 8);
  const unsigned o2 = rm ? (unsigned)(rl * (D_ * 2) + g2 * 8 + 64) : (unsigned)(4096 + rl * 16 + g2 * 8);
  asm volatile("global_load_dwordx2 %0, %1, %2 sc1" : "=v"(xv[0]) : "v"(o), "s"(xp));
  asm volatile("global_load_dwordx2 %0, %1, %2 sc1" : "=v"(xv[1]) : "v"(o + (rm ? 32u : 2048u)), "s"(xp));
  asm volatile("global_load_dwordx2 %0, %1, %2 sc1" : "=v"(xv[2]) : "v"(o2), "s"(xp));
}

DEVI void p2_gemm(f32x4 (&acc)[5], unsigned lds0, const char* ob, int layer, int wave, int lane MK_ARG) {
  ResX<9, 5, P2_W, 9, 9, 0, 0u> gm(acc);
  gm.setup(lds0, ob, wave, lane);
  const unsigned a = (gm.xo & ~1023u) | ((gm.xo & 1023u) >> 2);
  bar();
  MK(6);
  unsigned e[4];
#pragma unroll
  for (int d = 0; d < 4; ++d) asm volatile("global_load_dword %0, %1, %2 offset:%3 sc1" : "=v"(e[d]) : "v"(a), "s"(gm.xp), "n"(d * 256));
  gm.template prox<1>(std::make_integer_sequence<int, 8>{});
  lds_inc(lds0 + (unsigned)(LW_OFF + (LW_T + layer) * 4));
  gm.prow();
  asm volatile("s_waitcnt vmcnt(8)" : "+v"(e[0]), "+v"(e[1]), "+v"(e[2]), "+v"(e[3]));
  gm.xf[0] = __builtin_bit_cast(s16x8, u32x4{e[0], e[1], e[2], e[3]});
  __builtin_amdgcn_sched_barrier(0);
  gm.steps(std::make_integer_sequence<int, 9>{});
}

DEVI void res_fin(unsigned lds0, f32x4 (&acc)[5], u32x2 (&xv)[3], char* __restrict__ xim, char* __restrict__ xs, u16* __restrict__ out,
                  bool fin, int rb, int ct, int wave, int lane, unsigned nth, unsigned red MK_ARG) {
  const int g = lane >> 4, hi = wave >> 2;
  float xf[3][4];
#pragma unroll
  for (int nt = 0; nt < 3; ++nt) {
    asm volatile("" : "+v"(xv[nt]));
    xf[nt][0] = bf_lo(xv[nt][0]);
    xf[nt][1] = bf_hi(xv[nt][0]);
    xf[nt][2] = bf_lo(xv[nt][1]);
    xf[nt][3] = bf_hi(xv[nt][1]);
    asm volatile("" : "+v"(xf[nt][0]), "+v"(xf[nt][1]), "+v"(xf[nt][2]), "+v"(xf[nt][3]));
  }
  __builtin_amdgcn_sched_barrier(0);
  {
    const unsigned a4 = lds0 + red + (unsigned)((wave ^ 4) * 3072 + lane * 16);
    asm volatile("ds_write_b128 %0, %1" ::"v"(a4), "v"(acc[2]) : "memory");
    asm volatile("ds_write_b128 %0, %1 offset:1024" ::"v"(a4), "v"(acc[3]) : "memory");
    if (!hi) asm volatile("ds_write_b128 %0, %1 offset:2048" ::"v"(a4), "v"(acc[4]) : "memory");
    lds_inc(lds0 + (unsigned)(LW_OFF + (LW_PAIR + (wave & 3)) * 4));
  }
  const int rl = (wave & 3) * 16 + (lane & 15);
  const char* xp = uni_ptr(fin ? (const char*)(out + (size_t)rb * 64 * D_ + ct * 72 + hi * 32) : xim + (size_t)(rb * 72 + ct * 9 + hi * 4) * 1024);
  const char* sp = uni_ptr(xs + (size_t)(rb * 16 + ct * 2 + hi) * 512);
  unsigned xo = (unsigned)(((g & 1) * 2 + (g >> 1)) * (fin ? 16 : 1024) + rl * (fin ? D_ * 2 : 16));
  unsigned ra = lds0 + red + (unsigned)(wave * 3072 + lane * 16);
  asm volatile("" : "+v"(xo), "+v"(ra));
  __builtin_amdgcn_sched_barrier(0);
  SPIN_LDS {
    if (__builtin_amdgcn_readfirstlane(lds_rd32(lds0 + (unsigned)(LW_OFF + (LW_PAIR + (wave & 3)) * 4))) >= 2u * nth) break;
  }
  f32x4 pp[3];
#pragma unroll
  for (int nt = 0; nt < 3; ++nt) asm volatile("ds_read_b128 %0, %1 offset:%2" : "=v"(pp[nt]) : "v"(ra), "n"(nt * 1024) : "memory");
#pragma unroll
  for (int nt = 0; nt < 3; ++nt) {
    asm volatile("s_waitcnt lgkmcnt(%1)" : "+v"(pp[nt]) : "n"(2 - nt));
    const f32x4 a = acc[nt < 2 ? nt : 4] + pp[nt];
    const u32x2 ar = {pack2(a[0], a[1]), pack2(a[2], a[3])};
    xv[nt] = u32x2{pack2(xf[nt][0] + bf_lo(ar[0]), xf[nt][1] + bf_hi(ar[0])), pack2(xf[nt][2] + bf_lo(ar[1]), xf[nt][3] + bf_hi(ar[1]))};
  }
  const bool v2 = hi && g < 2;
  {
    const auto s0 = __builtin_amdgcn_permlane16_swap(xv[0][0], xv[1][0], false, false);
    const auto s1 = __builtin_amdgcn_permlane16_swap(xv[0][1], xv[1][1], false, false);
    const u32x4 v = {s0[0], s1[0], s0[1], s1[1]};
    const auto t0 = __builtin_amdgcn_permlane16_swap(xv[2][0], xv[2][0], false, false);
    const auto t1 = __builtin_amdgcn_permlane16_swap(xv[2][1], xv[2][1], false, false);
    const u32x4 w = {t0[0], t1[0], t0[1], t1[1]};
    if (fin) {
      asm volatile("global_store_dwordx4 %0, %1, %2\n\ts_nop 1" ::"v"(xo), "v"(v), "s"(xp) : "memory");
      if (hi && g == 0) asm volatile("global_store_dwordx4 %0, %1, %2 offset:64\n\ts_nop 1" ::"v"(xo), "v"(w), "s"(xp) : "memory");
      return;
    }
    asm volatile("global_store_dwordx4 %0, %1, %2 sc1\n\ts_nop 1" ::"v"(xo), "v"(v), "s"(xp) : "memory");
    if (hi && g == 0) asm volatile("global_store_dwordx4 %0, %1, %2 sc1\n\ts_nop 1" ::"v"(xo + 4096u), "v"(w), "s"(xp) : "memory");
  }
  float s = 0.f, q = 0.f;
#pragma unroll
  for (int i = 0; i < 6; ++i) {
    const unsigned y = (i < 4 || v2) ? xv[i / 2][i % 2] : 0u;
    s = __builtin_amdgcn_fdot2_f32_bf16(__builtin_bit_cast(bf16x2, y), __builtin_bit_cast(bf16x2, 0x3f803f80u), s, false);
    q = __builtin_amdgcn_fdot2_f32_bf16(__builtin_bit_cast(bf16x2, y), __builtin_bit_cast(bf16x2, y), q, false);
  }
  s = sum_g(s);
  q = sum_g(q);
  const f32x2 sq = {s, q};
  if (g == 0) asm volatile("global_store_dwordx2 %0, %1, %2 sc1\n\ts_nop 1" ::"v"((unsigned)(rl * 8)), "v"(sq), "s"(sp) : "memory");
}

using K3C = GemmCfg<128, 144>;
constexpr int K3_Z1_OFF = K3C::LDS_BYTES;
constexpr int K3_C2_OFF = K3_Z1_OFF + K3C::XB;
constexpr int K3_ZC_OFF = K3_C2_OFF + 2048;
constexpr int K3_DM_OFF = K3_ZC_OFF + 1024;
constexpr int K3_ST_OFF = K3_Z1_OFF + 4 * 5120;
constexpr int K3_C1_OFF = K3_ST_OFF + 2048;
constexpr int K3_BS_OFF = K3_C1_OFF + 576;
static_assert(K3_BS_OFF + 512 <= LW_OFF, "K3 LDS");
static_assert(P5_W + 19 * 1024 == K3C::SB && K3C::SB == 34 * 1024, "w_out ring");
constexpr int K3_D = 4;

DEVI void red16(const f32x4& a, const f32x4& b, const f32x4& c, const f32x4& d, float& s, float& q) {
  float t[4];
#pragma unroll
  for (int j = 0; j < 4; ++j) {
    float u = a[j] + b[j], v = c[j] + d[j];
    asm volatile("" : "+v"(u), "+v"(v));
    t[j] = u + v;
    asm volatile("" : "+v"(t[j]));
  }
#pragma unroll
  for (int j = 0; j < 2; ++j) {
    const auto p = __builtin_amdgcn_permlane16_swap(__float_as_uint(t[j]), __float_as_uint(t[2 + j]), false, false);
    const unsigned v = __float_as_uint(__uint_as_float(p[0]) + __uint_as_float(p[1]));
    const auto w = __builtin_amdgcn_permlane32_swap(v, v, false, false);
    t[j] = __uint_as_float(w[0]) + __uint_as_float(w[1]);
    asm volatile("" : "+v"(t[j]));
  }
  s = t[0];
  q = t[1];
}

constexpr int b_k(int s) { return s == 0 ? 0 : (s < 3 ? 6 + s : (s < 5 ? 2 + s : (s == 5 ? 4 : s - 5))); }
#define NOP_(n) "=s"(n)
template <bool B>
struct UMath {
  static constexpr int D = 4, R = 6, NV = 162, SB = K3C::SB, P = K3C::P;
  s16x8 wf[R], xf[2][2];
  s16x8 (&xc)[4][2][2];
  f32x4 (&acc)[2][9];
  unsigned xa, wa;
  unsigned fa, fw, fv;
  DEVI UMath(f32x4 (&a)[2][9], s16x8 (&x)[4][2][2]) : xc(x), acc(a) {}
  static constexpr int wslot(int s) { return B ? (s + 1) % 4 : s % 4; }
  static constexpr int xslot(int s) { return B ? b_k(s) % 5 : s % 5; }
  static constexpr bool res(int q) { return !B || b_k(q / 2) >= 4; }
#define BPS 9
  static constexpr bool chk(int s) { return s >= 3 && s < 7 && s != BPS; }
  static constexpr bool xop(int v) { return v + D < NV && (v + D) % 9 < 2 && res((v + D) / 9); }
  static constexpr int ops1(int v) { return (xop(v) || (B && ((v % 18 == 9 && chk(v / 18)) || (v % 18 == 0 && v > 0)))) ? 1 : 0; }
  static constexpr int newer(int V) {
    int v0 = (V % 9 == 8 ? V : V + 1) - D, n = 0;
    if (v0 >= 0 && !(V % 9 == 0 && res(V / 9))) n += ops1(v0);
    for (int v = v0 + 1; v < V; ++v)
      if (v >= 0) n += (v + D < NV ? 1 : 0) + ops1(v);
    return n;
  }
  template <int V>
  DEVI void wread() {
    asm volatile("ds_read_b128 %0, %1 offset:%2" : "=v"(wf[V % R]) : "v"(wa), "n"((wslot(V / 18) & 1) * SB + ((V % 18) / 9) * 32 * P + (V % 9) * 32));
  }
  template <int Q, int M>
  DEVI void xread() {
    asm volatile("ds_read_b128 %0, %1 offset:%2" : "=v"(xf[Q & 1][M]) : "v"(xa), "n"((xslot(Q / 2) & 1) * SB + (Q & 1) * 8192 + M * (B ? 256 : 64)));
  }
  template <int V, int M_>
  DEVI void mf() {
    constexpr int M = ((V % 9) & 1) ? 1 - M_ : M_, q = V / 9;
    if constexpr (!B) acc[M][V % 9] = __builtin_amdgcn_mfma_f32_16x16x32_bf16(xf[q & 1][M], wf[V % R], acc[M][V % 9], 0, 0, 0);
    else if constexpr (res(q)) acc[M][V % 9] = __builtin_amdgcn_mfma_f32_16x16x32_bf16(wf[V % R], xf[q & 1][M], acc[M][V % 9], 0, 0, 0);
    else acc[M][V % 9] = __builtin_amdgcn_mfma_f32_16x16x32_bf16(wf[V % R], xc[b_k(q / 2)][q & 1][M], acc[M][V % 9], 0, 0, 0);
    __builtin_amdgcn_sched_barrier(0);
  }
  template <int V>
  DEVI void unit() {
    constexpr int U = V % 18, s = V / 18;
    static_assert(newer(V) <= 15, "lgkmcnt range");
    if constexpr ((V % 9) % 2 == 0 && V > 0) asm volatile("s_waitcnt lgkmcnt(%0)" ::"n"(newer(V)));
    __builtin_amdgcn_sched_barrier(0);
    mf<V, 0>();
    if constexpr (V + D < NV) wread<V + D>();
    if constexpr (s < 8 && U == 13 && (wslot(s + 1) >> 1) != (wslot(s) >> 1))
      asm volatile("v_add_u32 %0, %1, %0" : "+v"(wa) : "n"(((wslot(s + 1) >> 1) - (wslot(s) >> 1)) * 2 * SB));
    __builtin_amdgcn_sched_barrier(0);
    mf<V, 1>();
    if constexpr (xop(V)) xread<(V + D) / 9, (V + D) % 9>();
    if constexpr (s < 8) {
      if constexpr (U == 11 && res(2 * s + 2) && (xslot(s + 1) >> 1) != (xslot(s) >> 1))
        asm volatile("v_add_u32 %0, %1, %0" : "+v"(xa) : "n"(((xslot(s + 1) >> 1) - (xslot(s) >> 1)) * 2 * SB));
      if constexpr (!B && U == 13) __builtin_amdgcn_s_barrier();
      if constexpr (B && U == 9 && chk(s)) asm volatile("ds_read_u8 %0, %1 offset:%2" : "+v"(fv) : "v"(fa), "n"(s == 6 ? 0 : 8));
#define SAFE_OUT(L)
      unsigned n = 1u << 18;
      if constexpr (B && U == 13 && chk(s)) {
        asm volatile(
            "v_cmp_gt_u32 vcc, %3, %0\n\ts_cbranch_vccnz .Lw%=\n\t.subsection 1\n.Lw%=:\n\tds_read_u8 %0, %2 offset:%4\n\ts_waitcnt lgkmcnt(0)\n\t"
            "v_cmp_gt_u32 vcc, %3, %0\n\t" SAFE_OUT(".Lr%=") "s_cbranch_vccnz .Lw%=\n\ts_branch .Lr%=\n\t.subsection 0\n.Lr%=:"
            : "+v"(fv), NOP_(n)
            : "v"(fa), "n"(s == 6 ? 4 : s + 2), "n"(s == 6 ? 0 : 8)
            : "vcc", "scc");
      }
      if constexpr (B && U == 13 && s == BPS) {
        asm volatile(
            ".La%=:\n\ts_sleep 1\n\tds_read_u8 %0, %2\n\ts_waitcnt lgkmcnt(0)\n\tv_cmp_gt_u32 vcc, 3, %0\n\t" SAFE_OUT(".Lr%=") "s_cbranch_vccnz .La%=\n"
            ".Lb%=:\n\tds_read_u8 %0, %2 offset:8\n\ts_waitcnt lgkmcnt(0)\n\tv_cmp_gt_u32 vcc, %3, %0\n\t" SAFE_OUT(".Lr%=") "s_cbranch_vccnz .Lb%=\n.Lr%=:"
            : "+v"(fv), NOP_(n)
            : "v"(fa), "n"(s + 2)
            : "vcc", "scc");
      }
    }
    if constexpr (B && U == 15) asm volatile("v_mov_b32 %0, %1" : "+v"(fv) : "n"(s + 1));
    if constexpr (B && ((U == 0 && s > 0) || V == NV - 1)) asm volatile("ds_write_b8 %0, %1" ::"v"(fw), "v"(fv));
    __builtin_amdgcn_sched_barrier(0);
  }
  template <int... V>
  DEVI void units(std::integer_sequence<int, V...>) {
    (unit<V>(), ...);
  }
  DEVI void first() {
    if constexpr (!B) {
      xread<0, 0>();
      xread<0, 1>();
    }
    wread<0>();
    wread<1>();
    wread<2>();
    wread<3>();
    asm volatile("s_waitcnt lgkmcnt(0)");
  }
  DEVI void run() {
    units(std::make_integer_sequence<int, NV>{});
#pragma unroll
    for (int m = 0; m < 2; ++m)
#pragma unroll
      for (int n = 0; n < 9; ++n) asm volatile("" : "+v"(acc[m][n]));
  }
};

#define ZSLEEP 6
DEVI void p3_g0(char* smem, char* __restrict__ ws, int layer, int mi, int ni, int wm, int lane MK_ARG) {
  constexpr int SB = K3C::SB;
  const unsigned lds0 = (unsigned)(size_t)smem;
  const int g = lane >> 4, r = lane & 15;
  const unsigned lw = lds0 + (unsigned)(LW_OFF + layer * 4), me = 1u << (8 * wm);
  f32x4 ac[2][9];
#pragma unroll
  for (int mt = 0; mt < 2; ++mt)
#pragma unroll
    for (int j = 0; j < 9; ++j) ac[mt][j] = f32x4{0.f, 0.f, 0.f, 0.f};
  unsigned ra = lds0 + (unsigned)(K3_ST_OFF + (wm * 32 + g * 8) * 4), ca = lds0 + (unsigned)(K3_C2_OFF + r * 8);
  asm volatile("" : "+v"(ra), "+v"(ca));
  {
    s16x8 xc[4][2][2];
    UMath<false> gm(ac, xc);
    gm.xa = lds0 + (unsigned)(g * 2048 + (wm * 32 + 8 * (r >> 2) + (r & 3)) * 16);
    gm.wa = lds0 + (unsigned)(K3C::XB + (g * 8 + (r & 7)) * K3C::P + (r >> 3) * 16);
    asm volatile("" : "+v"(gm.xa), "+v"(gm.wa));
    bar();
    gm.first();
    gm.run();
  }
  lds_inc(lw + LW_AD * 4, me);
  MK(11);
  f32x2 z[9][4];
  f32x2 sm[4], sq[4];
  {
    f32x4 rc[4][2];
    f32x2 cs[9];
#pragma unroll
    for (int i = 0; i < 8; ++i)
      asm volatile("ds_read_b128 %0, %1 offset:%2" : "=v"(rc[i / 2][i % 2]) : "v"(ra), "n"((i / 2) * 512 + (i % 2) * 16) : "memory");
#pragma unroll
    for (int j = 0; j < 9; ++j) asm volatile("ds_read_b64 %0, %1 offset:%2" : "=v"(cs[j]) : "v"(ca), "n"(j * 128) : "memory");
    asm volatile("s_waitcnt lgkmcnt(0)"
                 : "+v"(rc[0][0]), "+v"(rc[0][1]), "+v"(rc[1][0]), "+v"(rc[1][1]), "+v"(rc[2][0]), "+v"(rc[2][1]), "+v"(rc[3][0]), "+v"(rc[3][1]));
#pragma unroll
    for (int j = 0; j < 9; ++j) asm volatile("" : "+v"(cs[j]));
    auto RC = [&](int k, int i) { return f32x2{rc[k][i / 2][(i % 2) * 2], rc[k][i / 2][(i % 2) * 2 + 1]}; };
#pragma unroll
    for (int j = 0; j < 9; ++j) {
      f32x2 u[4], w[4];
#pragma unroll
      for (int i = 0; i < 4; ++i) u[i] = __builtin_elementwise_fma(RC(0, i), cs[j], f32x2{ac[i / 2][j][(i % 2) * 2], ac[i / 2][j][(i % 2) * 2 + 1]});
#pragma unroll
      for (int i = 0; i < 4; ++i) w[i] = u[i] * u[i];
#pragma unroll
      for (int i = 0; i < 4; ++i) w[i] = __builtin_elementwise_fma(w[i], RC(3, i), RC(2, i));
#pragma unroll
      for (int i = 0; i < 4; ++i) w[i] = u[i] * w[i];
      __builtin_amdgcn_sched_barrier(0);
#pragma unroll
      for (int i = 0; i < 4; ++i) w[i] = f32x2{__builtin_amdgcn_exp2f(w[i][0]), __builtin_amdgcn_exp2f(w[i][1])};
      __builtin_amdgcn_sched_barrier(0);
#pragma unroll
      for (int i = 0; i < 4; ++i) w[i] = __builtin_elementwise_fma(w[i], RC(1, i), RC(1, i));
      __builtin_amdgcn_sched_barrier(0);
#pragma unroll
      for (int i = 0; i < 4; ++i) w[i] = f32x2{__builtin_amdgcn_rcpf(w[i][0]), __builtin_amdgcn_rcpf(w[i][1])};
      __builtin_amdgcn_sched_barrier(0);
#pragma unroll
      for (int i = 0; i < 4; ++i) z[j][i] = u[i] * w[i];
#pragma unroll
      for (int i = 0; i < 4; ++i) {
        if (j == 1) sm[i] = z[0][i] + z[1][i];
        if (j > 1) sm[i] += z[j][i];
      }
#pragma unroll
      for (int i = 0; i < 4; ++i) sq[i] = j ? __builtin_elementwise_fma(z[j][i], z[j][i], sq[i]) : z[j][i] * z[j][i];
      asm volatile("" : "+v"(z[j][0]), "+v"(z[j][1]), "+v"(z[j][2]), "+v"(z[j][3]));
      __builtin_amdgcn_sched_barrier(0);
    }
  }
  f32x4 e;
  {
    float v[4][4], d[2][4], f[4];
#pragma unroll
    for (int i = 0; i < 4; ++i) {
      v[i][0] = sm[i][0];
      v[i][1] = sq[i][0];
      v[i][2] = sm[i][1];
      v[i][3] = sq[i][1];
    }
    asm volatile("s_nop 1" : "+v"(v[3][1]), "+v"(v[3][3]), "+v"(v[2][1]), "+v"(v[2][3]));
#pragma unroll
    for (int i = 0; i < 2; ++i)
#pragma unroll
      for (int c = 0; c < 4; ++c)
        asm volatile("v_add_f32_dpp %0, %1, %1 row_ror:8 row_mask:0xf bank_mask:0x3\n\tv_add_f32_dpp %0, %2, %2 row_ror:8 row_mask:0xf bank_mask:0xc"
                     : "=&v"(d[i][c])
                     : "v"(v[i][c]), "v"(v[i + 2][c]));
#pragma unroll
    for (int c = 0; c < 4; ++c)
      asm volatile("v_add_f32_dpp %0, %1, %1 row_shl:4 row_mask:0xf bank_mask:0x5\n\tv_add_f32_dpp %0, %2, %2 row_shr:4 row_mask:0xf bank_mask:0xa"
                   : "=&v"(f[c])
                   : "v"(d[0][c]), "v"(d[1][c]));
    asm volatile(
        "v_add_f32_dpp %0, %0, %0 quad_perm:[1,0,3,2] row_mask:0xf bank_mask:0xf\n\t"
        "v_add_f32_dpp %1, %1, %1 quad_perm:[1,0,3,2] row_mask:0xf bank_mask:0xf\n\t"
        "v_add_f32_dpp %2, %2, %2 quad_perm:[1,0,3,2] row_mask:0xf bank_mask:0xf\n\t"
        "v_add_f32_dpp %3, %3, %3 quad_perm:[1,0,3,2] row_mask:0xf bank_mask:0xf\n\t"
        "v_add_f32_dpp %0, %0, %0 quad_perm:[2,3,0,1] row_mask:0xf bank_mask:0xf\n\t"
        "v_add_f32_dpp %1, %1, %1 quad_perm:[2,3,0,1] row_mask:0xf bank_mask:0xf\n\t"
        "v_add_f32_dpp %2, %2, %2 quad_perm:[2,3,0,1] row_mask:0xf bank_mask:0xf\n\t"
        "v_add_f32_dpp %3, %3, %3 quad_perm:[2,3,0,1] row_mask:0xf bank_mask:0xf"
        : "+v"(f[0]), "+v"(f[1]), "+v"(f[2]), "+v"(f[3]));
    e = f32x4{f[0], __uint_as_float(__float_as_uint(f[1]) & 0x7fffffffu), f[2], __uint_as_float(__float_as_uint(f[3]) & 0x7fffffffu)};
  }
  const char* zp = uni_ptr(ws + OFF_ZP + (size_t)layer * SZ_ZP + (size_t)mi * 16384 + wm * 256);
  if ((lane & 3) == 0)
    asm volatile("global_store_dwordx4 %0, %1, %2 sc1\n\ts_nop 1" ::"v"((unsigned)(ni * 1024 + lane * 4)), "v"(e), "s"(zp) : "memory");
  lds_inc(lw + LW_AD * 4, me);
  __builtin_amdgcn_s_setprio(2);
  MK(12);
  const char* dst = uni_ptr(ws + OFF_Z2 + (size_t)layer * SZ_Z + (size_t)ni * S_ * 288 + (size_t)(mi * 4 + wm) * 9216 + 4096);
  const unsigned lane16 = (unsigned)lane * 16u;
  u32x4 wb[9];
  {
    static_assert(b_k(7) == 2 && b_k(8) == 3, "order");
    const char* wp = uni_ptr(ws + OFF_PI + (size_t)ni * (18 * K3C::WB) + 11 * K3C::WB + wm * 9216 + 4096);
#pragma unroll
    for (int j = 0; j < 8; ++j) asm volatile("global_load_dwordx4 %0, %1, %2 offset:%3 sc1" : "=v"(wb[j]) : "v"(lane16), "s"(wp), "n"(j * 1024 - 4096) : "memory");
    asm volatile("global_load_dwordx4 %0, %1, %2 sc1" : "=v"(wb[8]) : "v"(lane16 + 4096u), "s"(wp) : "memory");
  }
  u32x4 y[9];
  {
    const unsigned src = (unsigned)(g * 4096 + r * 16);
    unsigned zw = lds0 + (unsigned)(K3_ZC_OFF + (wm * 32 + 2 * r + (g & 1)) * 4), zr = lds0 + (unsigned)(K3_ZC_OFF + (wm * 32 + g * 8) * 4);
    asm volatile("" : "+v"(zw), "+v"(zr));
    f32x4 a, b, c, d;
    __builtin_amdgcn_s_sleep(ZSLEEP);
#pragma unroll 1
    for (unsigned spin = 0; spin < SPIN_MEM; ++spin) {
      asm volatile(
          "global_load_dwordx4 %0, %4, %5 sc1\n\t"
          "global_load_dwordx4 %1, %4, %5 offset:1024 sc1\n\t"
          "global_load_dwordx4 %2, %4, %5 offset:2048 sc1\n\t"
          "global_load_dwordx4 %3, %4, %5 offset:3072 sc1\n\t"
          "s_waitcnt vmcnt(0)"
          : "=&v"(a), "=&v"(b), "=&v"(c), "=&v"(d)
          : "v"(src), "s"(zp)
          : "memory");
      const unsigned bad = (__float_as_uint(a[1]) | __float_as_uint(a[3]) | __float_as_uint(b[1]) | __float_as_uint(b[3]) |
                            __float_as_uint(c[1]) | __float_as_uint(c[3]) | __float_as_uint(d[1]) | __float_as_uint(d[3])) >> 31;
      if (!__any(bad)) break;
    }
    MK(13);
    float s, q, zmb, zi;
    red16(a, b, c, d, s, q);
    ln_finish(s, q, 1.0f / (float)FH_, zmb, zi);
    f32x4 R[2], M[2];
    asm volatile(
        "ds_write2st64_b32 %4, %5, %6 offset1:2\n\t"
        "ds_read_b128 %0, %7\n\tds_read_b128 %2, %7 offset:512\n\tds_read_b128 %1, %7 offset:16\n\tds_read_b128 %3, %7 offset:528\n\t"
        "s_waitcnt lgkmcnt(0)"
        : "=&v"(R[0]), "=&v"(R[1]), "=&v"(M[0]), "=&v"(M[1])
        : "v"(zw), "v"(zi), "v"(-zmb * zi), "v"(zr)
        : "memory");
    MK(28);
#define NRM 0
    auto st = [&](int j) {
      if (j < 8) asm volatile("global_store_dwordx4 %0, %1, %2 offset:%3 sc1\n\ts_nop 1" ::"v"(lane16), "v"(y[j]), "s"(dst), "n"(j * 1024 - 4096) : "memory");
      else asm volatile("global_store_dwordx4 %0, %1, %2 sc1\n\ts_nop 1" ::"v"(lane16 + 4096u), "v"(y[j]), "s"(dst) : "memory");
    };
#pragma unroll
    for (int j = 0; j < 9; ++j) {
      if (NRM & 2) {
        f32x2 t[4];
#pragma unroll
        for (int k = 0; k < 4; ++k)
          t[k] = __builtin_elementwise_fma(z[j][k], f32x2{R[k / 2][(k % 2) * 2], R[k / 2][(k % 2) * 2 + 1]}, f32x2{M[k / 2][(k % 2) * 2], M[k / 2][(k % 2) * 2 + 1]});
        y[j] = u32x4{pack2v(t[0]), pack2v(t[1]), pack2v(t[2]), pack2v(t[3])};
      } else {
        float t[8];
#pragma unroll
        for (int k = 0; k < 8; ++k) {
          t[k] = __builtin_fmaf(z[j][k / 2][k % 2], R[k / 4][k % 4], M[k / 4][k % 4]);
          asm volatile("" : "+v"(t[k]));
        }
        y[j] = u32x4{pack2(t[0], t[1]), pack2(t[2], t[3]), pack2(t[4], t[5]), pack2(t[6], t[7])};
      }
      if (NRM & 1) asm volatile("" : "+v"(y[j]));
      else st(j);
      __builtin_amdgcn_sched_barrier(0);
    }
    MK(29);
    if (NRM & 1) {
#pragma unroll
      for (int j = 0; j < 9; ++j) st(j);
    }
  }
  lds_inc(lw + LW_AD * 4, me);
  MK(24);
  __builtin_amdgcn_s_setprio(0);
  lds_spin4(lw + LW_S * 4, 4u + (unsigned)(wm >> 1));
  {
    const unsigned fa = lds0 + (unsigned)((wm >> 1) * SB + K3C::XB + (wm & 1) * 9216) + lane16;
#pragma unroll
    for (int j = 0; j < 9; ++j) asm volatile("ds_write_b128 %0, %1 offset:%2" ::"v"(fa), "v"(wb[j]), "n"(j * 1024) : "memory");
    lds_inc(lw + LW_AD * 4, me);
  }
  announce(lw + LW_Z * 4, 4u, ws, CN_Z + layer, lane * 16 + ni, lane < 16);
  lds_inc(lw + LW_AD * 4, me);
  MK(27);
  f32x4 acc[2][9];
  {
    float bs[2];
    asm volatile("ds_read2_b32 %0, %1 offset1:16\n\ts_waitcnt lgkmcnt(0)"
                 : "=v"(*reinterpret_cast<f32x2*>(bs))
                 : "v"(lds0 + (unsigned)(K3_BS_OFF + (wm * 32 + r) * 4))
                 : "memory");
    lds_spin4(lw + LW_S * 4, 6u + (unsigned)(wm >> 1));
    const unsigned fa = lds0 + (unsigned)((2 + (wm >> 1)) * SB + K3C::XB + (wm & 1) * 9216) + lane16;
#pragma unroll
    for (int j = 0; j < 9; ++j) asm volatile("ds_write_b128 %0, %1 offset:%2" ::"v"(fa), "v"(y[j]), "n"(j * 1024) : "memory");
    lds_inc(lw + LW_AD * 4, me);
#pragma unroll
    for (int mt = 0; mt < 2; ++mt)
#pragma unroll
      for (int j = 0; j < 9; ++j) acc[mt][j] = f32x4{bs[mt], bs[mt], bs[mt], bs[mt]};
  }
  u32x4 z1[9];
  {
    GemmMath<K3C, 2, 9, K3_D, 6, false, 2> gm(acc);
    gm.setup(lds0, wm * 2, 0, lane, (unsigned)(2 * SB));
    MK(15);
    lds_spin4(lw + LW_AD * 4, 6u);
    lds_spin4(lw + LW_L * 4, 8u);
    MK(17);
    gm.first();
    gm.template mid<1, false>();
    gm.template mid<11>();
    const unsigned za = gm.wrd + (unsigned)(K3_Z1_OFF - K3C::XB + wm * 5120);
#pragma unroll
    for (int j = 0; j < 5; ++j) asm volatile("ds_read_b128 %0, %1 offset:%2" : "=v"(z1[j]) : "v"(za), "n"(j * 1024));
    gm.template mid<10>();
#pragma unroll
    for (int j = 5; j < 9; ++j) asm volatile("ds_read_b128 %0, %1 offset:%2" : "=v"(z1[j]) : "v"(za), "n"((j - 5) * 1024));
    gm.template mid<9>();
    gm.last();
  }
  MK(16);
  const char* hp = uni_ptr(ws + OFF_H + (size_t)layer * SZ_Z + (size_t)((mi * 2 + (wm >> 1)) * 288 + ni * 18) * 1024);
  const int l2 = lane_id();
  const unsigned ho = (unsigned)((l2 >> 5) * 1024 + ((wm & 1) * 32 + (l2 & 31)) * 16);
#pragma unroll
  for (int j = 0; j < 9; ++j) {
    asm volatile("" : "+v"(z1[j]));
    u32x2 h[2];
#pragma unroll
    for (int mt = 0; mt < 2; ++mt) {
      const f32x4 a = acc[mt][j];
      h[mt] = u32x2{pack2v(f32x2{bf_lo(z1[j][2 * mt]), bf_hi(z1[j][2 * mt])} * f32x2{a[0], a[1]}),
                    pack2v(f32x2{bf_lo(z1[j][2 * mt + 1]), bf_hi(z1[j][2 * mt + 1])} * f32x2{a[2], a[3]})};
    }
    const auto s0 = __builtin_amdgcn_permlane16_swap(h[0][0], h[1][0], false, false);
    const auto s1 = __builtin_amdgcn_permlane16_swap(h[0][1], h[1][1], false, false);
    const u32x4 v = {s0[0], s1[0], s0[1], s1[1]};
    asm volatile("global_store_dwordx4 %0, %1, %2 offset:%3 sc1\n\ts_nop 1" ::"v"(ho), "v"(v), "s"(hp + (j / 2) * 4096), "n"((j % 2) * 2048) : "memory");
    __builtin_amdgcn_sched_barrier(0);
  }
  MK(18);
  announce(lds0 + (unsigned)(LW_OFF + (LW_H + layer * 2 + (wm >> 1)) * 4), 2u, ws, CN_H + layer, (mi * 2 + (wm >> 1)) * 8 + l2, l2 < 8);
  MK(19);
}

DEVI void p3_g1(char* smem, char* __restrict__ ws, int layer, int mi, int wm, int lane MK_ARG) {
  const unsigned lds0 = (unsigned)(size_t)smem;
  const int g = lane >> 4, r = lane & 15;
  const unsigned lw = lds0 + (unsigned)(LW_OFF + layer * 4);
  const unsigned xrd = lds0 + (unsigned)(g * 2048 + (wm * 32 + r) * 16);
  s16x8 xc[4][2][2];
  {
    const char* xs = uni_ptr(ws + OFF_XS + (size_t)layer * 2 * SZ_XS + (size_t)(mi * 2 + (wm >> 1)) * 8192 + (wm & 1) * 256);
    f32x4 a, b, c, d;
    auto pick = [&](int t) {
      bar();
      asm volatile(
          "ds_read_b128 %0, %4\n\tds_read_b128 %1, %4 offset:256\n\tds_read_b128 %2, %4 offset:8192\n\tds_read_b128 %3, %4 offset:8448\n\t"
          "s_waitcnt lgkmcnt(0)"
          : "=&v"(xc[t][0][0]), "=&v"(xc[t][0][1]), "=&v"(xc[t][1][0]), "=&v"(xc[t][1][1])
          : "v"(xrd + (unsigned)(t * K3C::SB))
          : "memory");
    };
#define G1P 2
    __builtin_amdgcn_s_setprio(G1P);
    pick(0);
    pick(1);
    asm volatile(
        "global_load_dwordx4 %0, %4, %5 sc1\n\t"
        "global_load_dwordx4 %1, %4, %5 offset:512 sc1\n\t"
        "global_load_dwordx4 %2, %4, %5 offset:1024 sc1\n\t"
        "global_load_dwordx4 %3, %4, %5 offset:1536 sc1"
        : "=&v"(a), "=&v"(b), "=&v"(c), "=&v"(d)
        : "v"((unsigned)(g * 2048 + r * 16)), "s"(xs)
        : "memory");
    pick(2);
    pick(3);
    bar();
    asm volatile("s_waitcnt vmcnt(0)" : "+v"(a), "+v"(b), "+v"(c), "+v"(d) : : "memory");
    float s, q, mb, rinv;
    red16(a, b, c, d, s, q);
    bar();
    asm volatile("" : "+v"(s), "+v"(q));
    ln_finish(s, q, 1.0f / (float)D_, mb, rinv);
    asm volatile("" : "+v"(mb), "+v"(rinv));
    bar();
    asm volatile("" : "+v"(mb), "+v"(rinv));
    const float k0 = -2.0f * 0.7978845608028654f * 1.4426950408889634f, k1 = k0 * 0.044715f;
    float sd = __builtin_amdgcn_rcpf(rinv), c0 = k0 * rinv, c1 = k1 * rinv * rinv * rinv;
    asm volatile("" : "+v"(sd));
    asm volatile("" : "+v"(c0));
    asm volatile("" : "+v"(c1));
    const float e0 = lane < 32 ? -mb : c0, e1 = lane < 32 ? sd : c1;
    asm volatile("ds_write2st64_b32 %0, %1, %2 offset1:2" ::"v"(
                     lds0 + (unsigned)(K3_ST_OFF + (g >> 1) * 1024 + (wm * 32 + 2 * r + (g & 1)) * 4)),
                 "v"(e0), "v"(e1)
                 : "memory");
    bar();
    asm volatile("s_waitcnt lgkmcnt(0)" ::: "memory");
    bar();
    __builtin_amdgcn_s_setprio(0);
  }
  f32x4 ac[2][9];
#pragma unroll
  for (int mt = 0; mt < 2; ++mt)
#pragma unroll
    for (int j = 0; j < 9; ++j) ac[mt][j] = f32x4{0.f, 0.f, 0.f, 0.f};
  {
    UMath<true> gm(ac, xc);
    gm.xa = xrd;
    gm.wa = lds0 + (unsigned)(K3C::XB + (g * 8 + (r & 7)) * K3C::P + (r >> 3) * 16);
    gm.fa = lw + (unsigned)(LW_AD * 4 + (lane & 3));
    gm.fw = lw + (unsigned)(LW_S * 4 + wm);
    gm.fv = 0u;
    asm volatile("" : "+v"(gm.fa), "+v"(gm.fw), "+v"(gm.fv), "+v"(gm.xa), "+v"(gm.wa));
    lds_spin4<4>(lw + LW_L * 4, 4u);
    gm.first();
    lds_spin4(lw + LW_AD * 4, 2u);
#define BPR 1
    __builtin_amdgcn_s_setprio(BPR);
    gm.run();
    __builtin_amdgcn_s_setprio(0);
  }
  MK(14);
  f32x2 rc[4];
  f32x4 cs[9];
  const int l2 = lane_id();
  {
    const unsigned ra = lds0 + (unsigned)(K3_ST_OFF + (wm * 32 + (l2 & 15)) * 4), ca = lds0 + (unsigned)(K3_C1_OFF + (l2 & 48));
#pragma unroll
    for (int k = 0; k < 4; ++k) asm volatile("ds_read2_b32 %0, %1 offset1:16" : "=v"(rc[k]) : "v"(ra + (unsigned)(k * 512)) : "memory");
#pragma unroll
    for (int j = 0; j < 9; ++j) asm volatile("ds_read_b128 %0, %1 offset:%2" : "=v"(cs[j]) : "v"(ca), "n"(j * 64) : "memory");
    asm volatile("s_waitcnt lgkmcnt(0)" : "+v"(rc[0]), "+v"(rc[1]), "+v"(rc[2]), "+v"(rc[3]));
#pragma unroll
    for (int j = 0; j < 9; ++j) asm volatile("" : "+v"(cs[j]));
  }
  const unsigned za = lds0 + (unsigned)(K3_Z1_OFF + wm * 5120 + l2 * 16);
  u32x4 z1[9];
#pragma unroll
  for (int t = 0; t < 18; ++t) {
    const int j = t / 2, mt = t % 2;
    float u[4], w[4];
#pragma unroll
    for (int i = 0; i < 4; ++i) asm volatile("v_fma_f32 %0, %1, %2, %3" : "=v"(u[i]) : "v"(rc[0][mt]), "v"(cs[j][i]), "v"(ac[mt][j][i]));
#pragma unroll
    for (int i = 0; i < 4; ++i) asm volatile("v_mul_f32 %0, %1, %1" : "=v"(w[i]) : "v"(u[i]));
#pragma unroll
    for (int i = 0; i < 4; ++i) asm volatile("v_fma_f32 %0, %0, %1, %2" : "+v"(w[i]) : "v"(rc[3][mt]), "v"(rc[2][mt]));
#pragma unroll
    for (int i = 0; i < 4; ++i) asm volatile("v_mul_f32 %0, %0, %1" : "+v"(w[i]) : "v"(u[i]));
#pragma unroll
    for (int i = 0; i < 4; ++i) asm volatile("v_exp_f32 %0, %0" : "+v"(w[i]));
#pragma unroll
    for (int i = 0; i < 4; ++i) asm volatile("v_fma_f32 %0, %0, %1, %1" : "+v"(w[i]) : "v"(rc[1][mt]));
#pragma unroll
    for (int i = 0; i < 4; ++i) asm volatile("v_rcp_f32 %0, %0" : "+v"(w[i]));
#pragma unroll
    for (int i = 0; i < 4; ++i) asm volatile("v_mul_f32 %0, %0, %1" : "+v"(w[i]) : "v"(u[i]));
    z1[j][2 * mt] = pack2(w[0], w[1]);
    z1[j][2 * mt + 1] = pack2(w[2], w[3]);
    if (mt && j < 5) asm volatile("ds_write_b128 %0, %1 offset:%2" ::"v"(za), "v"(z1[j]), "n"(j * 1024) : "memory");
    if (t == 17) {
#pragma unroll
      for (int k = 5; k < 9; ++k) asm volatile("ds_write_b128 %0, %1 offset:%2" ::"v"(za), "v"(z1[k]), "n"((k - 5) * 1024) : "memory");
    }
    if (t == 9 || t == 17) asm volatile("s_waitcnt lgkmcnt(0)" ::: "memory");
    __builtin_amdgcn_sched_barrier(0);
    bar();
    __builtin_amdgcn_sched_barrier(0);
  }
  MK(15);
#pragma unroll 1
  for (int t = 20; t < 32; ++t) bar();
}

DEVI void p3_math(char* smem, char* __restrict__ ws, int layer, float cv, int mi, int ni, int wave, int tid MK_ARG) {
  if (tid < 144) *reinterpret_cast<f32x2*>(smem + K3_C2_OFF + tid * 8) = f32x2{cv, cv};
  else if (tid < 288 || tid >= 384) *reinterpret_cast<float*>(smem + (tid < 288 ? K3_C1_OFF - 576 : K3_BS_OFF - 1536) + tid * 4) = cv;
  bar();
  MK(10);
  asm volatile("s_waitcnt lgkmcnt(0)" ::: "memory");
  if (wave >> 2) p3_g1(smem, ws, layer, mi, wave & 3, lane_id() MK_PASS);
  else p3_g0(smem, ws, layer, mi, ni, wave, lane_id() MK_PASS);
  bar();
}

constexpr int W5A = 22, W5B = 26;
constexpr int NPF = 30;
struct Loader {
  u32x4 pf[NPF];
  unsigned lds0, lane16;
  int wl, lane;

  template <int R0, int N, int P0, int PE>
  DEVI void pf_load(const char* src) {
    const unsigned o = lane16 + (unsigned)(wl * 1024);
#pragma unroll
    for (int t = 0; t < N; ++t) {
      unsigned a = o + (unsigned)((P0 + 4 * t) * 1024);
      if (P0 + 4 * t + 3 >= PE && P0 + 4 * t + wl >= PE) a = lane16;
      asm volatile("global_load_dwordx4 %0, %1, %2 sc1" : "=v"(pf[R0 + t]) : "v"(a), "s"(src) : "memory");
    }
  }
  template <int R0, int N>
  DEVI void pf_pin() {
#pragma unroll
    for (int t = 0; t < N; ++t) asm volatile("" : "+v"(pf[R0 + t]));
  }
  template <int R0, int N>
  DEVI void pf_undef() {
#pragma unroll
    for (int t = 0; t < N; ++t) asm volatile("" : "=v"(pf[R0 + t]));
  }
  template <int PB, int A, int N>
  DEVI void pf_put(unsigned dst) {
    constexpr int T0 = (A - PB) / 4, T1 = (A + N - 1 - PB) / 4;
    const unsigned a = lds0 + dst + lane16 + (unsigned)((wl + PB + 4 * T0 - A + 4) * 1024) - 4096u;
#pragma unroll
    for (int t = T0; t <= T1; ++t) {
      const int p0 = PB + 4 * t;
      if ((p0 >= A || p0 + wl >= A) && (p0 + 3 < A + N || p0 + wl < A + N))
        asm volatile("ds_write_b128 %0, %1 offset:%2" ::"v"(a), "v"(pf[t]), "n"((t - T0) * 4096) : "memory");
    }
  }
  template <int V>
  DEVI void wbar() {
    asm volatile("s_waitcnt vmcnt(%0) lgkmcnt(0)" ::"n"(V) : "memory");
    bar();
  }

  DEVI void st_load(f32x2 (&pv)[8], const char* xs, int row) {
    const unsigned o = (unsigned)(((row >> 6) * 16 + (lane >> 5) * 8) * 512 + (row & 63) * 8);
#pragma unroll
    for (int i = 0; i < 8; ++i) asm volatile("global_load_dwordx2 %0, %1, %2 offset:%3 sc1" : "=v"(pv[i]) : "v"(o), "s"(xs), "n"(i * 512) : "memory");
  }
  DEVI void st_fin(f32x2 (&pv)[8], float& mb, float& rinv) {
    asm volatile("" : "+v"(pv[0]), "+v"(pv[1]), "+v"(pv[2]), "+v"(pv[3]), "+v"(pv[4]), "+v"(pv[5]), "+v"(pv[6]), "+v"(pv[7]));
    float a[2];
#pragma unroll
    for (int j = 0; j < 2; ++j) {
      a[j] = pv[0][j];
#pragma unroll
      for (int i = 1; i < 8; ++i) {
        a[j] += pv[i][j];
        asm volatile("" : "+v"(a[j]));
      }
    }
    const auto s = __builtin_amdgcn_permlane32_swap(__float_as_uint(a[0]), __float_as_uint(a[0]), false, false);
    const auto q = __builtin_amdgcn_permlane32_swap(__float_as_uint(a[1]), __float_as_uint(a[1]), false, false);
    ln_finish(__uint_as_float(s[0]) + __uint_as_float(s[1]), __uint_as_float(q[0]) + __uint_as_float(q[1]), 1.0f / (float)D_, mb, rinv);
  }

  DEVI void p1(char* smem, char* ws, int rt, int hp MK_ARG) {
    constexpr int SB = K1C::SB;
    static_assert(K1C::XB == 4 * 4608 && K1C::WB == 14 * 1024, "K1 shape");
    pf_put<0, 0, 14>((unsigned)K1C::XB);
    pf_put<0, 14, 14>((unsigned)(SB + K1C::XB));
    pf_put<0, 28, 14>((unsigned)(2 * SB + K1C::XB));
    pf_put<0, 42, 14>((unsigned)(3 * SB + K1C::XB));
    unsigned xo[5];
#pragma unroll
    for (int i = 0; i < 5; ++i) {
      const int c = wl * 288 + ((i < 4 || lane < 32) ? i * 64 + lane : 0);
      const int ch = c / 144;
      int gr = rt * 128 - 16 + (c - ch * 144);
      if (gr < 0) gr = 0;
      xo[i] = (unsigned)(((gr >> 6) * 72 + ch) * 1024 + (gr & 63) * 16);
      asm volatile("" : "+v"(xo[i]));
    }
    const char* xp = uni_ptr(ws + OFF_X1 + SZ_X);
    const char* wp = uni_ptr(ws + OFF_PQ + (size_t)hp * (9 * K1C::WB) + 4 * K1C::WB + wl * 3584);
    const char* xs = uni_ptr(ws + OFF_XS + SZ_XS);
    auto dma_x = [&](unsigned so) {
      const unsigned m = lds0 + so + (unsigned)(wl * 288 * 16);
#pragma unroll
      for (int i = 0; i < 4; ++i) dma16_sc1(xp, xo[i], m + (unsigned)(i * 1024));
      if (lane < 32) dma16_sc1(xp, xo[4], m + 4096u);
      xp += 8192;
    };
    auto stage = [&](unsigned so) {
      dma_x(so);
      const unsigned m = lds0 + so + (unsigned)(K1C::XB + wl * 3584);
#pragma unroll
      for (int i = 0; i < 3; ++i) dma16_sc1(wp + i * 1024, lane16, m + (unsigned)(i * 1024));
      if (lane < 32) dma16_sc1(wp + 3072, lane16, m + 3072u);
      wp += K1C::WB;
    };
    const int tr = wl * 32 + (lane & 31);
    int ra = rt * 128 - 16 + tr;
    if (ra < 0) ra = 0;
    if (wl == 0) cnt_wait(ws, CN_X2, rt * 16 + hp, rt ? 24u : 16u);
    MK(1);
    bar();
    dma_x(0u);
    dma_x((unsigned)SB);
    wbar<5>();
    MK(29);
    dma_x((unsigned)(2 * SB));
    dma_x((unsigned)(3 * SB));
    wbar<10>();
    f32x2 pv[8], pw[8];
    st_load(pv, xs, ra);
    st_load(pw, xs, rt * 128 + 112 + (lane & 15));
    wbar<21>();
    stage(0u);
    wbar<25>();
    stage((unsigned)SB);
    wbar<9>();
    stage((unsigned)(2 * SB));
    {
      float mb, rinv;
      st_fin(pv, mb, rinv);
      if (lane < 32) *reinterpret_cast<f32x2*>(smem + K1_ST_OFF + tr * 8) = f32x2{-mb, rinv};
    }
    wbar<9>();
    stage((unsigned)(3 * SB));
    if (wl == 0) {
      float mb, rinv;
      st_fin(pw, mb, rinv);
      if (lane < 16) *reinterpret_cast<f32x2*>(smem + K1_ST_OFF + (128 + lane) * 8) = f32x2{-mb, rinv};
    }
    wbar<9>();
    stage(0u);
    wbar<9>();
    wbar<0>();
    MK(30);
  }

  template <int T>
  DEVI void p5_fill(const char* wp) {
    const unsigned so = (unsigned)(P5_W + ((T + 3) % 8) * 18432), wo = (unsigned)(12288 + wl * 1536);
    dma16_sc1(wp, lane16 + wo + (unsigned)((T + 8) * 18432), lds0 + so + wo);
    if (lane < 32) dma16_sc1(wp, lane16 + wo + (unsigned)((T + 8) * 18432 + 1024), lds0 + so + wo + 1024u);
    pf_put<0, 12 * T, 12>(so);
  }
  template <int I>
  DEVI void p5_step(const char* wp) {
    constexpr int T = 2 * I + 1;
    wbar<((T >= 3 && T <= 11) ? 4 : 0)>();
    if constexpr (T <= 9) {
      p5_fill<T - 1>(wp);
      p5_fill<T>(wp);
    }
  }
  template <int... S>
  DEVI void p5_steps(const char* wp, std::integer_sequence<int, S...>) {
    (p5_step<S>(wp), ...);
  }

  template <int T0, int T1>
  DEVI void w5_load(const char* w5p) {
    const unsigned o = lane16 + (unsigned)(wl * 1024);
#pragma unroll
    for (int t = T0; t < T1; ++t)
      asm volatile("global_load_dwordx4 %0, %1, %2 sc1" : "=v"(pf[t]) : "v"(o + (unsigned)((t / 3 + 8) * 18432 + (t % 3) * 4096)), "s"(w5p) : "memory");
  }
  DEVI void p35(char* smem, char* ws, const u16* w_sp, int layer, int bid, int mi, int ni, int rb, int ct, int hp MK_ARG) {
    using C = K3C;
    constexpr int NS = C::NS, SB = C::SB, NDX = 4, NDW = 5, VPS = NDX + NDW;
    const char* wip = uni_ptr(ws + OFF_PI + (size_t)ni * (18 * C::WB));
    pf_put<0, 0, 18>((unsigned)C::XB);
    pf_put<0, 18, 18>((unsigned)(SB + C::XB));
    pf_put<0, 36, 18>((unsigned)(2 * SB + C::XB));
    pf_put<0, 54, 18>((unsigned)(3 * SB + C::XB));
    const char* w5p = uni_ptr(ws + OFF_PW + (size_t)ct * (36 * 9216));
    const unsigned m0x = lds0 + (unsigned)(wl * 4096), m0w = lds0 + (unsigned)(C::XB + wl * 4608);
    const unsigned wo = lane16 + (unsigned)(wl * 4608);
    const unsigned lw = lds0 + (unsigned)(LW_OFF + layer * 4), me = 1u << (8 * wl);
    auto dma_w = [&](const char* wp, unsigned so) {
#pragma unroll
      for (int i = 0; i < NDW; ++i) {
        if (i < NDW - 1 || lane < 32) dma16_sc1(wp, wo + (unsigned)(i * 1024), m0w + so + (unsigned)(i * 1024));
      }
    };
    {
      const char* xp = uni_ptr(ws + OFF_X1 + (size_t)layer * 2 * SZ_X + (size_t)(mi * 2 * 72 + wl * 2) * 1024);
      const unsigned xo1 = lane16 + 72u * 1024u;
      auto dma_x = [&](int k, unsigned so) {
        const char* p = xp + k * 8192;
        dma16_sc1(p, lane16, m0x + so);
        dma16_sc1(p, xo1, m0x + so + 1024u);
        dma16_sc1(p + 1024, lane16, m0x + so + 2048u);
        dma16_sc1(p + 1024, xo1, m0x + so + 3072u);
      };
      if (wl == 0) cnt_wait(ws, CN_X1 + layer, mi * 16 + ni, 16u);
      MK(10);
      bar();
      dma_x(0, 0u);
      dma_x(1, (unsigned)SB);
      wait_vm<4>(); MX(3, 2); wbar<4>(); MX(2, 2);
      MK(25);
      dma_x(2, (unsigned)(2 * SB));
      dma_x(3, (unsigned)(3 * SB));
      wait_vm<8>(); MX(3, 3); wbar<8>(); MX(2, 3);
      dma_x(4, (unsigned)(4 * SB));
      wait_vm<8>();
#pragma unroll 1
      for (int t = 0; t < 4; ++t) {
        MX(3, 4 + t); bar(); MX(2, 4 + t);
        dma_w(wip + (t + 4) * C::WB, (unsigned)(t * SB));
        dma_x(t + 5, (unsigned)(t * SB));
        wait_vm<NDX + VPS>();
      }
      MX(3, 8); bar(); MX(2, 8);
      dma_w(wip + 8 * C::WB, 0u);
      wait_vm<VPS>();
      MX(3, 9); bar(); MX(2, 9);
      const char* wp = wip + 9 * C::WB;
      dma_w(wp + b_k(0) * C::WB, (unsigned)SB);
      if (layer == 0) {
        const char* sp = uni_ptr((const char*)w_sp + (size_t)(bid * 4 + wl) * 8192);
        const unsigned so = (unsigned)((lane >> 5) * 4096 + (lane & 31) * 16);
#pragma unroll
        for (int i = 0; i < WSP_V; ++i) dma16(sp + i * 512, so, lds0 + (unsigned)K3_DM_OFF);
        dma16(sp + (bid >> 4) * 256, so & ~0x100u, lds0 + (unsigned)K3_DM_OFF);
      }
      w5_load<0, 4>(w5p);
      wait_vm<NDW + 4>();
      MX(3, 10); bar(); MX(2, 10);
      dma_w(wp + b_k(1) * C::WB, (unsigned)(2 * SB));
      w5_load<4, 8>(w5p);
      MK(26);
      lds_spin4(lw + LW_AD * 4, 1u);
      dma_w(wp + b_k(2) * C::WB, (unsigned)(3 * SB));
      dma_w(wp + b_k(3) * C::WB, 0u);
      w5_load<8, W5A>(w5p);
      wait_vm<W5A - 8>();
      lds_inc(lw + LW_L * 4, 4u * me);
      w5_load<W5A, 30>(w5p);
      MK(11);
#define LSL 2
      static_for<4, 7>([&](auto T_) {
        constexpr int t = T_;
        lds_spin4<LSL>(lw + LW_S * 4, (unsigned)(t - 3));
        MX(2, 12 + t);
        dma_w(wp + b_k(t) * C::WB, (unsigned)(((t + 1) % 4) * SB));
        if constexpr (t > 4) {
          wait_vm<NDW>();
          lds_inc(lw + LW_L * 4, me);
        }
      });
      MX(2, 19);
      wait_vm<0>();
      lds_inc(lw + LW_L * 4, me);
      pf_pin<0, 30>();
      MK(13);
    }
    {
      unsigned xoff[NDX];
#pragma unroll
      for (int i = 0; i < NDX; ++i) {
        const int c = wl * 256 + i * 64 + lane;
        const int row = c >> 3, cc = c & 7;
        xoff[i] = (unsigned)((mi * 128 + row) * S_ * 2) + (unsigned)((cc ^ ((row >> 1) & 7)) * 16);
      }
      auto dma_x = [&](const char* xp, unsigned so) {
#pragma unroll
        for (int i = 0; i < NDX; ++i) dma16(xp, xoff[i], m0x + so + (unsigned)(i * 1024));
      };
      const char* xp = uni_ptr((const char*)w_sp);
      const char* wp = uni_ptr(ws + OFF_Z2 + (size_t)layer * SZ_Z + (size_t)ni * S_ * 288);
#define XEARLY 0
      if (!XEARLY) lds_spin4<LSL>(lw + LW_AD * 4, 5u);
      dma_x(xp + mi * 256, (unsigned)(2 * SB));
      dma_x(xp + mi * 256 + 128, (unsigned)(3 * SB));
      if (XEARLY) lds_spin4<LSL>(lw + LW_AD * 4, 5u);
      MK(24);
      int left = 2 * mi;
      auto next = [&]() {
        const int d = --left == 0 ? 3 : 1;
        xp += d * 128;
        wp += d * C::WB;
      };
      if (mi == 0) {
        xp += 256;
        wp += 2 * C::WB;
      }
      dma_x(xp, 0u);
      dma_x(xp + 128, (unsigned)SB);
      wait_vm<2 * NDX>();
      lds_inc(lw + LW_L * 4, me);
      MK(27);
      if (wl == 0) {
        cnt_wait(ws, CN_Z + layer, mi * 16 + ni, 16u);
        lds_wr32(lw + LW_GO * 4, 1u);
      } else {
        lds_spin(lw + LW_GO * 4, 1u);
      }
      MK(14);
      lds_spin4(lw + LW_S * 4, 8u);
      dma_w(wp, 0u);
      lds_spin4(lw + LW_S * 4, 9u);
      dma_w(wp + C::WB, (unsigned)SB);
      next();
      next();
      unsigned slot = (unsigned)(2 * SB);
      auto cissue = [&]() {
        dma_x(xp, slot);
        dma_w(wp, slot);
        next();
        slot = (slot == (unsigned)((NS - 1) * SB)) ? 0u : slot + (unsigned)SB;
      };
      auto cstep = [&]() {
        bar();
        cissue();
      };
      wait_vm<NDW>();
      MK(15);
      int j = 2;
      if (layer == 0) {
        const char* sp = uni_ptr((const char*)w_sp + (size_t)(bid * 4 + wl) * 8192 + WSP_V * 512);
        unsigned d = 0u;
#pragma unroll 1
        for (; j < 2 + 32 - 4 * WSP_V; ++j) {
          cstep();
          asm volatile("s_load_dword %0, %1, 0\n\ts_load_dword %0, %1, 64\n\ts_load_dword %0, %1, 0x1000\n\ts_load_dword %0, %1, 0x1040"
                       : "+s"(d)
                       : "s"(sp));
          sp += 128;
          wait_vm<VPS>();
        }
#pragma unroll 1
        for (int e = j + 4; j < e; ++j) {
          cstep();
          asm volatile("" : "+s"(d));
          wait_vm<VPS>();
        }
        asm volatile("s_waitcnt lgkmcnt(0)" : "+s"(d));
      }
#pragma unroll 1
      for (; j <= 29; ++j) {
        cstep();
        wait_vm<VPS>();
      }
      bar();
    }
    auto w5piece = [&](unsigned off) {
      dma16_sc1(w5p, lane16 + (off >= 54u * 1024u ? off - 54u * 1024u : off + 90u * 1024u), lds0 + (unsigned)P5_W + off);
    };
#pragma unroll
    for (int i = 0; i < 15; ++i) {
      const int p = 4 * i + wl;
      if (i < 14 || wl == 0) w5piece((unsigned)((p < 34 ? 53 + p : 121 - 34 + p) * 1024));
    }
    wait_vm<14>();
    bar();
#pragma unroll
    for (int i = 0; i < 9; ++i)
      if (i < 8 || wl < 2) w5piece((unsigned)((87 + 4 * i + wl) * 1024));
    wait_vm<0>();
    bar();
    if (wl) {
#pragma unroll
      for (int i = 0; i < 18; ++i)
        if (i < 17 || wl < 3) w5piece((unsigned)((3 * i + wl - 1) * 1024));
    } else {
      cnt_wait(ws, CN_H + layer, rb * 8 + ct, 16u);
    }
    MK(20);
    bar();
    p5_steps(w5p, std::make_integer_sequence<int, 7>{});
    if (layer == 0) {
      if (bid < 24 && wl == 0 && lane < 8) pk_st16(ws + OFF_HELLO + bid * 128 + lane16, u32x4{0u, 0u, 0u, 0u});
      pf_load<0, 14, 0, 56>(uni_ptr(ws + OFF_PQ + (size_t)hp * (9 * K1C::WB)));
      wait_vm<0>();
      pf_pin<0, 14>();
      MK(22);
      bar();
    } else {
      pf_undef<0, 14>();
    }
  }
};

__global__ __launch_bounds__(768, 3) void k_main(const u16* __restrict__ x0, const u16* __restrict__ wqkv, const u16* __restrict__ wo,
                                                 const u16* __restrict__ w_in, const u16* __restrict__ w_sp,
                                                 const u16* __restrict__ b_sp, const u16* __restrict__ w_out,
                                                 u16* __restrict__ out, char* ws) {
  extern __shared__ __attribute__((aligned(16))) char smem[];
  const char* code0 = code_here();
  (void)code0;
  const unsigned lds0 = (unsigned)(size_t)smem;
  int wave = __builtin_amdgcn_readfirstlane((int)threadIdx.x >> 6);
  int bid = blockIdx.x;
#define FRESH() asm volatile("" : "+s"(bid), "+s"(ws), "+s"(wave))
#define XCD (bid & 7)
#define IDX (bid >> 3)
#define MI ((XCD >> 2) * 8 + (IDX >> 2))
#define NI ((XCD & 3) * 4 + (IDX & 3))
#define RB (XCD * 4 + (IDX >> 3))
#define CT (IDX & 7)
#define RT ((XCD >> 1) * 4 + (IDX >> 3))
#define HP ((XCD & 1) * 8 + (IDX & 7))
  MK(0);
  if (wave >= 3 && wave < 8) {
    constexpr int ST = 64;
    const int k = (7 - wave) * 64 + lane_id();
    const bool isx = k < 9216 / ST;
    int o = isx ? bid * 9216 + k * ST : bid * 7776 + (k - 9216 / ST) * ST;
    if (!isx && k - 9216 / ST > 7775 / ST) o = bid * 7776;
    const char* p = (isx ? (const char*)x0 : (const char*)wqkv) + o;
    asm volatile("s_mov_b32 m0, %1\n\ts_nop 0\n\tglobal_load_lds_dword %0, off" ::"v"(p), "s"(lds0 + (unsigned)K1R_DUMMY) : "memory");
  }

  if (__builtin_expect(wave >= 8, 1)) {
    __builtin_amdgcn_s_setprio(3);
    {
      const int lane = lane_id();
      RawLd rl;
      const bool cross = (((HP * 72) & ~15) & 127) > 48;
      const int c16 = IDX >> 1;
      const int job = cross ? (c16 < 11 ? 128 + XCD * 11 + c16 : 999) : XCD * 16 + c16;
      const int pj = cross ? (c16 >= 11 ? XCD * 5 + c16 - 11 : -1) : (XCD < 4 && (c16 & 7) == 0 ? 40 + XCD * 2 + (c16 >> 3) : -1);
      rl.front(x0, wqkv, wo, ws, bid, lds0, wave - 8, lane, RT, HP, pj MF_PASS);
      if (job < 216) k1_raw_loader<true>(rl, w_in, w_out, ws, rl.tag, bid, lds0, wave - 8, lane, RT, HP, CT, job, pj MF_PASS);
      else k1_raw_loader<false>(rl, w_in, w_out, ws, rl.tag, bid, lds0, wave - 8, lane, RT, HP, CT, job, pj MF_PASS);
    }
    Loader ld;
    ld.lds0 = lds0;
#pragma unroll 1
    for (int layer = 0; layer < 2; ++layer) {
      FRESH();
      ld.wl = wave - 8;
      ld.lane = lane_id();
      ld.lane16 = (unsigned)ld.lane * 16u;
      const char* wip = uni_ptr(ws + OFF_PI + (size_t)NI * (18 * K3C::WB));
      if (layer) {
        ld.p1(smem, ws, RT, HP MK_PASS);
        ld.pf_load<0, 21, 0, 81>(uni_ptr(ws + OFF_PO + (size_t)CT * (18 * 4608)));
        wait_vm<0>();
        ld.pf_pin<0, 21>();
        bar();
        ld.pf_put<0, 0, 40>((unsigned)P2_W);
        ld.pf_put<0, 40, 41>((unsigned)(P2_W + 40 * 1024));
      } else {
        MK(3);
        MK(4);
        bar();
        const char* wp = uni_ptr(ws + OFF_PO + (size_t)CT * (18 * 4608) + ld.wl * 1024);
#pragma unroll
        for (int i = 0; i < 21; ++i)
          if (i < 20 || ld.wl == 0) dma16_sc1(wp + i * 4096, ld.lane16, lds0 + (unsigned)(P2_W + i * 4096 + ld.wl * 1024));
      }
      if (ld.wl == 0) cnt_wait(ws, CN_O + layer, RB * 8 + CT, 16u);
      MK(6);
      asm volatile("s_waitcnt vmcnt(0) lgkmcnt(0)" ::: "memory");
      bar();
      lds_spin(lds0 + (unsigned)(LW_OFF + (LW_T + layer) * 4), 16u);
      if (layer == 0) {
        if (ld.wl == 0) {
          cnt_wait(ws, CN_PK, 8 + NI, 36u);
          lds_wr32(lds0 + (unsigned)(LW_OFF + LW_HELLO * 4), 2u);
        } else {
          lds_spin(lds0 + (unsigned)(LW_OFF + LW_HELLO * 4), 2u);
        }
        MK(7);
      }
      ld.pf_load<0, 18, 0, 72>(wip);
      if (layer == 0 && ld.wl == 0) cnt_wait(ws, CN_PK, 24 + CT, 72u);
      wait_vm<0>();
      ld.pf_pin<0, 18>();
      bar();
      FRESH();
      ld.wl = wave - 8;
      ld.p35(smem, ws, w_sp, layer, bid, MI, NI, RB, CT, HP MK_PASS);
    }
  } else {
    MF(12);
    math_start(smem, code0, x0, wqkv, ws, bid, wave, lane_id());
    MF(13);
#pragma unroll 1
    for (int layer = 0; layer < 2; ++layer) {
      FRESH();
      {
        const int lane = lane_id(), r0 = RT * 128;
        float* csq = reinterpret_cast<float*>(ws + OFF_CSQ + bid * 512);
        Attn at;
        at.setup(lds0 + (unsigned)(layer ? K1_T_OFF : K1R_T_OFF), r0, HP, wave, lane);
        if (layer) {
          if ((wave & 2) == 0) k1_gemm<2, 4>(smem, csq, HP, (wave & 1) * 2 + (wave & 4), 0, lane MK_PASS);
          else if (wave < 4) k1_gemm<3, 3>(smem, csq, HP, (wave & 1) * 3, 4, lane MK_PASS);
          else if (wave == 6) k1_gemm<2, 3>(smem, csq, HP, 6, 4, lane MK_PASS);
          else k1_gemm<1, 7>(smem, csq, HP, 8, 0, lane MK_PASS);
        } else {
          if (wave == 0) k1_gemm_raw<3, false>(smem, HP, 0, lane MF_PASS);
          else if (wave < 4) k1_gemm_raw<2, false>(smem, HP, 1 + 2 * wave, lane MF_PASS);
          else if (wave < 7) k1_gemm_raw<2, true>(smem, HP, 2 * (wave - 4), lane MF_PASS);
          else k1_gemm_raw<3, true>(smem, HP, 6, lane MF_PASS);
        }
        msync(lds0 + (unsigned)(LW_OFF + (LW_T + layer) * 4));
        MK(3);
        at.run(ws + OFF_O + (size_t)layer * SZ_X, r0, HP, wave, lane);
        if (layer == 0) {
          if (wave == 0 && lane < 32) pk_st16(csq + lane * 4, *reinterpret_cast<const u32x4*>(smem + K1R_CS_OFF + lane * 16));
          lds_spin(lds0 + (unsigned)(LW_OFF + LW_HELLO * 4), 1u);
        }
        MK(4);
        announce(lds0 + (unsigned)(LW_OFF + (LW_O + layer * 22 + (wave >> 2)) * 4), 4u, ws, CN_O + layer, (RT * 2 + (wave >> 2)) * 8 + lane,
                 lane < 8);
        MK(5);
        bar();
      }
      FRESH();
      {
        const int lane = lane_id();
        u32x2 xv[3];
        res_ldx(xv, layer ? ws + OFF_X1 + SZ_X : (const char*)x0, layer == 0, RB, CT, wave, lane);
        f32x4 acc[5];
        p2_gemm(acc, lds0, uni_ptr(ws + OFF_O + (size_t)layer * SZ_X + (size_t)RB * (72 * 1024)), layer, wave, lane MK_PASS);
        MK(7);
        res_fin(lds0, acc, xv, ws + OFF_X1 + (size_t)layer * 2 * SZ_X, ws + OFF_XS + (size_t)layer * 2 * SZ_XS, out, false,
                                RB, CT, wave, lane, 2u * layer + 1u, 0u MK_PASS);
        MK(8);
        announce(lds0 + (unsigned)(LW_OFF + (LW_X1 + layer) * 4), 8u, ws, CN_X1 + layer, (RB >> 1) * 16 + lane, lane < 16);
        MK(9);
        bar();
      }
      FRESH();
      {
        const int tid = wave * 64 + lane_id();
        float cv = 0.f;
        if (tid < 288) {
          const char* cp = uni_ptr(ws + OFF_CSP + NI * 1152);
          float a[18];
#pragma unroll
          for (int i = 0; i < 18; ++i)
            asm volatile("global_load_dword %0, %1, %2 sc1" : "=v"(a[i]) : "v"((unsigned)tid * 4u), "s"(cp + i * (F_ * 4)) : "memory");
#pragma unroll
          for (int i = 0; i < 18; ++i) asm volatile("s_waitcnt vmcnt(0)" : "+v"(a[i]));
          cv = (((a[0] + a[1]) + (a[2] + a[3])) + ((a[4] + a[5]) + (a[6] + a[7]))) +
               ((((a[8] + a[9]) + (a[10] + a[11])) + ((a[12] + a[13]) + (a[14] + a[15]))) + (a[16] + a[17]));
        } else if (tid >= 384) {
          cv = bf_lo((unsigned)b_sp[MI * 128 + tid - 384]);
        }
        p3_math(smem, ws, layer, cv, MI, NI, wave, tid MK_PASS);
      }
      FRESH();
      {
        const int lane = lane_id();
        u32x2 xv[3];
        res_ldx(xv, ws + OFF_X1 + (size_t)layer * 2 * SZ_X, false, RB, CT, wave, lane);
        f32x4 acc[5];
        ResX<36, 8, P5_W, P5_XW, P5_X0, 3, P5_BAR> gm(acc);
        gm.setup(lds0, uni_ptr(ws + OFF_H + (size_t)layer * SZ_Z + (size_t)RB * (288 * 1024)), wave, lane);
        gm.prow();
        bar();
        MK(20);
        gm.prox<0>(std::make_integer_sequence<int, P5_X0>{});
        __builtin_amdgcn_sched_barrier(0);
        gm.steps(std::make_integer_sequence<int, 36>{});
        MK(21);
        res_fin(lds0, acc, xv, ws + OFF_X1 + SZ_X, ws + OFF_XS + SZ_XS, out, layer != 0, RB, CT, wave, lane,
                               2u * layer + 2u, (unsigned)(P5_W + 5 * 18432) MK_PASS);
        MK(22);
        if (layer == 0) {
          announce(lds0 + (unsigned)(LW_OFF + LW_X2 * 4), 8u, ws, CN_X2, (RB >> 1) * 16 + lane,
                   lane < 16 || (lane < 32 && (RB & 1) && RB < 31));
          MK(23);
          bar();
        }
      }
    }
  }
}

}

extern "C" void kernel_launch(void* const* d_in, const int* in_sizes, int n_in, void* d_out, int out_size,
                              void* d_ws, size_t ws_size, hipStream_t stream) {
  const u16* x0 = (const u16*)d_in[0];
  const u16* wqkv = (const u16*)d_in[1];
  const u16* wo = (const u16*)d_in[2];
  const u16* w_in = (const u16*)d_in[4];
  const u16* w_sp = (const u16*)d_in[5];
  const u16* b_sp = (const u16*)d_in[6];
  const u16* w_out = (const u16*)d_in[7];
  char* ws = (char*)d_ws;
  (void)hipFuncSetAttribute((const void*)k_main, hipFuncAttributeMaxDynamicSharedMemorySize, LDS_ALL);
  k_main<<<256, 768, LDS_ALL, stream>>>(x0, wqkv, wo, w_in, w_sp, b_sp, w_out, (u16*)d_out, ws);
}
```

```cpp
#include <hip/hip_runtime.h>
#include <hip/hip_bf16.h>
#include <stdint.h>

#define DEVI __device__ __forceinline__

typedef unsigned short u16;
typedef __attribute__((ext_vector_type(8))) short s16x8;
typedef __attribute__((ext_vector_type(4))) short s16x4;
typedef __attribute__((ext_vector_type(4))) float f32x4;
typedef __attribute__((ext_vector_type(2))) float f32x2;
typedef __attribute__((ext_vector_type(2))) __bf16 bf16x2;
typedef __attribute__((ext_vector_type(2))) unsigned u32x2;
typedef __attribute__((ext_vector_type(4))) unsigned u32x4;

#define MK(I) ((void)0)
#define MX(S, I) ((void)0)
#define MF(I) ((void)0)
#define MF_ARG
#define MF_PASS
#define MK_ARG
#define MK_PASS

#define SPIN_LDS for (;;)
#define SPIN_MEM (1u << 22)

#define G1W 0
#define P5PACE 0
#define L0REG 0
#define RTC 0
#define P1PR 3
#define MPOLL 1
namespace {

constexpr int S_ = 2048, D_ = 576, F_ = 4608, FH_ = 2304;
constexpr int NQKV = 3 * D_;

constexpr size_t SZ_X = (size_t)S_ * D_ * 2;
constexpr size_t SZ_Z = (size_t)S_ * FH_ * 2;
constexpr size_t OFF_X1 = 0;
constexpr size_t OFF_O = OFF_X1 + 3 * SZ_X;
constexpr size_t OFF_Z2 = OFF_O + 2 * SZ_X;
constexpr size_t OFF_H = OFF_Z2 + 2 * SZ_Z;
constexpr size_t SZ_XS = (size_t)S_ * 8 * 8;
constexpr size_t OFF_XS = OFF_H + 2 * SZ_Z;
constexpr size_t OFF_CSQ = OFF_XS + 3 * SZ_XS;
constexpr size_t OFF_ZP = OFF_CSQ + 256 * 512;
constexpr size_t SZ_ZP = (size_t)16 * 16 * 1024;
constexpr size_t OFF_PQ = OFF_ZP + 2 * SZ_ZP;
constexpr size_t OFF_PO = OFF_PQ + (size_t)16 * 9 * 64 * 112 * 2;
constexpr size_t OFF_PI = OFF_PO + (size_t)D_ * D_ * 2;
constexpr size_t OFF_PW = OFF_PI + (size_t)32 * 9 * 64 * 144 * 2;
constexpr size_t OFF_CSP = OFF_PW + (size_t)FH_ * D_ * 2;
constexpr size_t OFF_CNT = OFF_CSP + (size_t)18 * F_ * 4;
constexpr int CN_X1 = 0, CN_H = 2, CN_X2 = 4, CN_O = 5, CN_Z = 7, CN_PK = 9, NCNT = 10;
constexpr size_t OFF_TAG = OFF_CNT + (size_t)NCNT * 256 * 128;
constexpr size_t OFF_HELLO = OFF_TAG + 128;
constexpr size_t OFF_POT = OFF_HELLO + 2048;
constexpr size_t WS_END = OFF_POT + 1024;

DEVI float bf_lo(unsigned u) { return __uint_as_float(u << 16); }
DEVI float bf_hi(unsigned u) { return __uint_as_float(u & 0xffff0000u); }
DEVI unsigned pack2(float a, float b) {
  f32x2 v = {a, b};
  bf16x2 r = __builtin_convertvector(v, bf16x2);
  return __builtin_bit_cast(unsigned, r);
}
DEVI float rbf(float a) {
  return bf_lo(pack2(a, 0.f));
}

DEVI float sum_g(float x) {
  const unsigned u = __float_as_uint(x);
  const auto p = __builtin_amdgcn_permlane32_swap(u, u, false, false);
  const unsigned v = __float_as_uint(__uint_as_float(p[0]) + __uint_as_float(p[1]));
  const auto q = __builtin_amdgcn_permlane16_swap(v, v, false, false);
  return __uint_as_float(q[0]) + __uint_as_float(q[1]);
}

DEVI unsigned vconst(unsigned c) {
  unsigned r;
  asm volatile("v_mov_b32 %0, %1" : "=v"(r) : "s"(c));
  return r;
}
DEVI const char* uni_ptr(const char* p) {
  unsigned long long v = (unsigned long long)p;
  unsigned lo = __builtin_amdgcn_readfirstlane((unsigned)v), hi = __builtin_amdgcn_readfirstlane((unsigned)(v >> 32));
  asm("s_nop 4" : "+s"(lo), "+s"(hi));
  return (const char*)(((unsigned long long)hi << 32) | lo);
}

DEVI void dma16(const char* sbase, unsigned voff, unsigned m0val) {
  asm volatile("s_mov_b32 m0, %2\n\ts_nop 0\n\tglobal_load_lds_dwordx4 %0, %1"
               :
               : "v"(voff), "s"(sbase), "s"(m0val)
               : "memory");
}

DEVI void ln_finish(float s, float q, float invn, float& mb, float& rinv) {
  float mean = s * invn;
  asm("" : "+v"(mean));
  float var = fmaxf(__builtin_fmaf(-mean, mean, q * invn), 0.f);
  mb = rbf(mean);
  float v = rbf(var);
  float t = rbf(v + 1e-5f);
  float sd = rbf(__builtin_amdgcn_sqrtf(t));
  rinv = __builtin_amdgcn_rcpf(sd);
}

DEVI unsigned pack2v(f32x2 v) { return pack2(v[0], v[1]); }

template <int BM_, int BN_>
struct GemmCfg {
  static constexpr int BM = BM_, BN = BN_, NS = 4;
  static constexpr int P = BN * 2;
  static constexpr int XB = BM * 128, WB = 64 * P, SB = XB + WB;
  static constexpr int LDS_BYTES = NS * SB;
};

DEVI int swap23(int r) { return (r & ~12) | ((r & 4) << 1) | ((r & 8) >> 1); }

DEVI unsigned lds_rd32(unsigned addr) {
  unsigned v;
  asm volatile("ds_read_b32 %0, %1\n\ts_waitcnt lgkmcnt(0)" : "=v"(v) : "v"(addr) : "memory");
  return v;
}
DEVI void lds_wr32(unsigned addr, unsigned v) { asm volatile("ds_write_b32 %0, %1" ::"v"(addr), "v"(v) : "memory"); }

template <class C, int MT, int NT, int D, int R, bool XCM, int WT, bool SQ = false, unsigned long long WS = 0>
struct GemmMath {
  static constexpr int WO = WT ? 1 : 2;
  static constexpr int NS = C::NS, SB = C::SB, P = C::P, NU = 2 * NT;
  static constexpr int XPU = MT <= 2 ? MT : MT - 2;
  static constexpr int XU = (MT + XPU - 1) / XPU;
  static_assert(NU % R == 0 && R > D && D <= NT && XU <= D, "shape");
  s16x4 wlo[R], whi[R];
  s16x8 wf[R];
  s16x8 xf[2][MT];
  unsigned xrd[2], wrd, xa[2], wa, so;
  f32x4 (&acc)[MT][NT];
  f32x4 accq[SQ ? MT : 1];
  unsigned long long wtab = WS;
  DEVI GemmMath(f32x4 (&a)[MT][NT]) : acc(a) {}

  template <int PU>
  DEVI void wread_lo() {
    constexpr int off = (PU / NT) * 32 * P + (PU % NT) * (WT == 2 ? 1024 : 32);
    if constexpr (WT) asm volatile("ds_read_b128 %0, %1 offset:%2" : "=v"(wf[PU % R]) : "v"(wa), "n"(off));
    else asm volatile("ds_read_b64_tr_b16 %0, %1 offset:%2" : "=v"(wlo[PU % R]) : "v"(wa), "n"(off));
  }
  template <int PU>
  DEVI void wread_hi() {
    if constexpr (!WT)
      asm volatile("ds_read_b64_tr_b16 %0, %1 offset:%2" : "=v"(whi[PU % R]) : "v"(wa), "n"((PU / NT) * 32 * P + (PU % NT) * 32 + 8 * P));
  }
  template <int H, int M>
  DEVI void xread1() {
    asm volatile("ds_read_b128 %0, %1 offset:%2" : "=v"(xf[H][M]) : "v"(xa[H]), "n"(M * (XCM ? 256 : 2048)));
  }
  static constexpr int xops(int v) {
    const int k = (v + D) % NT;
    if (k >= XU) return 0;
    const int n = MT - k * XPU;
    return n < XPU ? n : XPU;
  }
  static constexpr int ops(int v, bool last) { return (last && v + D >= NU) ? 0 : WO + xops(v); }
  static constexpr int newer(int U, bool last) {
    int n = 0;
    int v0 = U - D;
    if (U % NT == 0) v0 += XU - 1;
    else n += v0 >= 0 ? xops(v0) : xops(v0 + NU);
    for (int v = v0 + 1; v < U; ++v) n += v >= 0 ? ops(v, last) : ops(v + NU, false);
    return n;
  }
  template <int U, int M_>
  DEVI void mf() {
    constexpr int M = ((U % NT) & 1) ? MT - 1 - M_ : M_;
    s16x8 w;
    if constexpr (WT) w = wf[U % R];
    else w = __builtin_shufflevector(wlo[U % R], whi[U % R], 0, 1, 2, 3, 4, 5, 6, 7);
    acc[M][U % NT] = __builtin_amdgcn_mfma_f32_16x16x32_bf16(w, xf[U / NT][M], acc[M][U % NT], 0, 0, 0);
    __builtin_amdgcn_sched_barrier(0);
  }
  DEVI void pin() {
#pragma unroll
    for (int m = 0; m < MT; ++m)
#pragma unroll
      for (int n = 0; n < NT; ++n) asm volatile("" : "+v"(acc[m][n]));
  }
  template <int U, int I>
  DEVI void dsop() {
    constexpr int PU = (U + D) % NU;
    if constexpr (I == 0) wread_lo<PU>();
    else if constexpr (I == 1) wread_hi<PU>();
    else if constexpr (I - 2 < xops(U)) {
      constexpr int k = (U + D) % NT;
      xread1<((U + D - k) % NU) / NT, k * XPU + I - 2>();
    }
  }
  template <int U, bool LAST, bool BAR, int M>
  DEVI void slot() {
    constexpr bool PF = !(LAST && U + D >= NU);
    mf<U, M>();
    if constexpr (M == 0 && PF && U + D == NU) {
      if constexpr (BAR) __builtin_amdgcn_s_barrier();
      so = (so == (unsigned)((NS - 1) * SB)) ? 0u : so + (unsigned)SB;
      wa = wrd + (WS ? ((unsigned)wtab & 63u) << 11 : so);
      wtab >>= 6;
      xa[0] = xrd[0] + so;
      xa[1] = xrd[1] + so;
      __builtin_amdgcn_sched_barrier(0);
    }
    if constexpr (PF) {
      dsop<U, M>();
      if constexpr (M == MT - 1) {
        if constexpr (MT <= 1) dsop<U, 1>();
        if constexpr (MT <= 2) {
          dsop<U, 2>();
          dsop<U, 3>();
        }
      }
      __builtin_amdgcn_sched_barrier(0);
    }
  }
  template <int U, bool LAST, bool BAR, int... M>
  DEVI void unit(std::integer_sequence<int, M...>) {
    static_assert(newer(U, LAST) <= 15, "lgkmcnt range");
    asm volatile("s_waitcnt lgkmcnt(%0)" ::"n"(newer(U, LAST)));
    __builtin_amdgcn_sched_barrier(0);
    (slot<U, LAST, BAR, M>(), ...);
    if constexpr (SQ && (U % NT) < MT) {
      constexpr int m = U % NT;
      accq[m] = __builtin_amdgcn_mfma_f32_16x16x32_bf16(xf[U / NT][m], xf[U / NT][m], accq[m], 0, 0, 0);
      __builtin_amdgcn_sched_barrier(0);
    }
    __builtin_amdgcn_sched_barrier(0);
  }
  template <bool LAST, bool BAR, int... U>
  DEVI void stage(std::integer_sequence<int, U...>) {
    (unit<U, LAST, BAR>(std::make_integer_sequence<int, MT>{}), ...);
  }
  template <int... U>
  DEVI void prow(std::integer_sequence<int, U...>) {
    ((wread_lo<U>(), wread_hi<U>()), ...);
  }
  template <int... M>
  DEVI void prox(std::integer_sequence<int, M...>) {
    (xread1<0, M>(), ...);
  }
  DEVI void setup(unsigned lds0, int mt0, int nt0, int lane, unsigned so0) {
    const int g = lane >> 4, r = lane & 15, q = (lane >> 2) & 3, p = lane & 3;
#pragma unroll
    for (int h = 0; h < 2; ++h)
      xrd[h] = XCM ? lds0 + (unsigned)((4 * h + g) * (C::BM * 16) + (mt0 * 16 + r) * 16)
                   : lds0 + (unsigned)((mt0 * 16 + r) * 128 + (((4 * h + g) ^ ((r >> 1) & 7)) * 16));
    wrd = WT == 2   ? lds0 + (unsigned)(C::XB + nt0 * 1024 + lane * 16)
          : WT == 1 ? lds0 + (unsigned)(C::XB + (g * 8 + (r & 7)) * P + (r >> 3) * 16 + nt0 * 32)
                    : lds0 + (unsigned)(C::XB + (16 * (g >> 1) + 4 * (g & 1) + q) * P + 8 * p + nt0 * 32);
    so = so0;
    xa[0] = xrd[0] + so;
    xa[1] = xrd[1] + so;
    wa = wrd + so;
  }
  DEVI void first() {
    prox(std::make_integer_sequence<int, MT>{});
    prow(std::make_integer_sequence<int, D>{});
    asm volatile("s_waitcnt lgkmcnt(0)");
  }
  template <int N, bool BAR = true>
  DEVI void mid() {
#pragma unroll 1
    for (int s = 0; s < N; ++s) stage<false, BAR>(std::make_integer_sequence<int, NU>{});
  }
  DEVI void last() {
    stage<true, true>(std::make_integer_sequence<int, NU>{});
    pin();
  }
  template <int KS>
  DEVI void run(unsigned lds0, int mt0, int nt0, int lane, unsigned so0) {
    setup(lds0, mt0, nt0, lane, so0);
    __builtin_amdgcn_s_barrier();
    first();
    mid<KS - 1>();
    last();
  }
};

DEVI const char* code_here() {
  unsigned long long pc;
  asm volatile("s_getpc_b64 %0" : "=s"(pc));
  return (const char*)pc;
}
DEVI int okmap(int p) {
  const int ch = p >> 3, e = p & 7, c = (ch - 8) & 3;
  return ch < 8 ? (2 * ch + (e >> 2)) * 36 + 16 + (e & 3) : ((ch - 8) >> 2) * 36 + c * 8 + (c >> 1) * 4 + e;
}

DEVI void pk_st16(void* p, u32x4 v) {
  asm volatile("global_store_dwordx4 %0, %1, off sc1\n\ts_nop 1" ::"v"(p), "v"(v) : "memory");
}

using K1C = GemmCfg<144, 112>;
constexpr int K1_TP = 240;
constexpr int K1_T_OFF = K1C::SB;
constexpr int K1_ST_OFF = 163840 - 128 - 1152;
constexpr int K1_WX = 4 * K1C::SB + 2048;
constexpr unsigned long long K1_WS = 16ull | 32ull << 6 | 48ull << 12 | 56ull << 18 | 63ull << 24 | 16ull << 30 | 32ull << 36 | 48ull << 42;
static_assert(K1C::SB == 16 * 2048 && K1_WX - K1C::XB == 56 * 2048 && K1C::WB == 7 * 2048 && K1_WX + 2 * K1C::WB <= K1_ST_OFF, "K1 w places");
static_assert(K1_T_OFF + 144 * K1_TP + 64 <= 2 * K1C::SB + K1C::XB, "K1 LDS alias");
using K1R = GemmCfg<144, 144>;
constexpr int K1R_T_OFF = K1R::SB;
constexpr int K1R_CS_OFF = K1R_T_OFF + 144 * K1_TP;
constexpr int K1R_ST_OFF = K1R_CS_OFF + 512;
constexpr int K1R_DUMMY = K1R::LDS_BYTES;
constexpr int K1R_PO = K1R_DUMMY + 1024;
constexpr int K1R_CNT = 163840 - 128 + 28 * 4;
DEVI void msync(unsigned addr) {
  asm volatile("s_waitcnt lgkmcnt(0)" ::: "memory");
  asm volatile("s_mov_b64 exec, 1\n\tds_add_u32 %0, %1\n\ts_mov_b64 exec, -1" ::"v"(addr), "v"(vconst(1u)) : "memory");
  SPIN_LDS {
    if (__builtin_amdgcn_readfirstlane(lds_rd32(addr)) >= 8u) break;
  }
}
static_assert(K1R_PO + 13824 + 256 <= 163840 - 128 && K1R_ST_OFF + 144 * 8 <= 3 * K1R::SB, "K1R LDS");

DEVI void touch64(const char* sbase, unsigned voff, unsigned m0val) {
  asm volatile("s_mov_b32 m0, %2\n\ts_nop 0\n\tglobal_load_lds_dword %0, %1" : : "v"(voff), "s"(sbase), "s"(m0val) : "memory");
}

template <int MT, bool G1>
DEVI void k1_gemm_raw(char* smem, int hp, int mt0, int lane MF_ARG) {
  constexpr int NT = 4, nt0 = G1 ? 4 : 0;
  const unsigned lds0 = (unsigned)(size_t)smem;
  const int g = lane >> 4, r = lane & 15;
  unsigned ta[NT];
#pragma unroll
  for (int nt = 0; nt < NT; ++nt) {
    const int j = (nt0 + nt) * 16 + g * 4, seg = j / 40, cj = j - seg * 40 - (hp & 1) * 4;
    const int to = (seg < 3 && cj >= 0 && cj < 36) ? seg * 80 + cj * 2 + (cj >= 20 ? 8 : 0) : 40;
    ta[nt] = lds0 + (unsigned)(K1R_T_OFF + (mt0 * 16 + r) * K1_TP + to);
    asm volatile("" : "+v"(ta[nt]));
  }
  unsigned sa = lds0 + (unsigned)(K1R_ST_OFF + (mt0 * 16 + r) * 8), ca = lds0 + (unsigned)(K1R_CS_OFF + (nt0 * 16 + g * 4) * 4);
  asm volatile("" : "+v"(sa), "+v"(ca));
  f32x4 acc[MT][NT];
#pragma unroll
  for (int mt = 0; mt < MT; ++mt)
#pragma unroll
    for (int nt = 0; nt < NT; ++nt) acc[mt][nt] = f32x4{0.f, 0.f, 0.f, 0.f};
  {
    GemmMath<K1R, MT, 4, 3, 4, false, 0, G1> gm(acc);
    if constexpr (G1) {
#pragma unroll
      for (int mt = 0; mt < MT; ++mt) gm.accq[mt] = f32x4{0.f, 0.f, 0.f, 0.f};
    }
    MF(14);
    if constexpr (!G1) {
      if (mt0 < 7) asm volatile("s_waitcnt vmcnt(0)" ::: "memory");
    }
    MF(15);
    gm.template run<9>(lds0, mt0, nt0, lane, 0u);
    MF(2);
    if constexpr (G1) {
#pragma unroll
      for (int mt = 0; mt < MT; ++mt) {
        const f32x4 q = gm.accq[mt];
        const float d = (r & 2) ? ((r & 1) ? q[3] : q[2]) : ((r & 1) ? q[1] : q[0]);
        const float qq = sum_g(g == (r >> 2) ? d : 0.f);
        float mb, rinv;
        ln_finish(acc[mt][3][0], qq, 1.0f / (float)D_, mb, rinv);
        const f32x2 st = {-mb, rinv};
        if (g == 2) asm volatile("ds_write_b64 %0, %1 offset:%2" ::"v"(sa), "v"(st), "n"(mt * 128) : "memory");
      }
    }
  }
  if (mt0 == 0 && r == 0) {
#pragma unroll
    for (int nt = 0; nt < NT; ++nt) *reinterpret_cast<f32x4*>(smem + K1R_CS_OFF + ((nt0 + nt) * 16 + g * 4) * 4) = acc[0][nt];
  }
  msync(lds0 + (unsigned)K1R_CNT);
  MF(3);
  f32x2 st[MT];
  f32x4 cs[NT];
#pragma unroll
  for (int nt = 0; nt < NT; ++nt) asm volatile("ds_read_b128 %0, %1 offset:%2" : "=v"(cs[nt]) : "v"(ca), "n"(nt * 64) : "memory");
#pragma unroll
  for (int mt = 0; mt < MT; ++mt) asm volatile("ds_read_b64 %0, %1 offset:%2" : "=v"(st[mt]) : "v"(sa), "n"(mt * 128) : "memory");
#pragma unroll
  for (int i = 0; i < MT; ++i) asm volatile("s_waitcnt lgkmcnt(0)" : "+v"(st[i]));
#pragma unroll
  for (int i = 0; i < NT; ++i) asm volatile("" : "+v"(cs[i]));
#pragma unroll
  for (int mt = 0; mt < MT; ++mt) {
    f32x2 M = {st[mt][0], st[mt][0]}, R = {st[mt][1], st[mt][1]};
    asm volatile("" : "+v"(M), "+v"(R));
#pragma unroll
    for (int nt = 0; nt < NT; ++nt) {
      const f32x2 v0 = R * __builtin_elementwise_fma(M, f32x2{cs[nt][0], cs[nt][1]}, f32x2{acc[mt][nt][0], acc[mt][nt][1]});
      const f32x2 v1 = R * __builtin_elementwise_fma(M, f32x2{cs[nt][2], cs[nt][3]}, f32x2{acc[mt][nt][2], acc[mt][nt][3]});
      acc[mt][nt][0] = __uint_as_float(pack2v(v0));
      acc[mt][nt][1] = __uint_as_float(pack2v(v1));
      const f32x2 pk = {acc[mt][nt][0], acc[mt][nt][1]};
      asm volatile("ds_write_b64 %0, %1 offset:%2" ::"v"(ta[nt]), "v"(pk), "n"(mt * 16 * K1_TP) : "memory");
    }
  }
}

DEVI void dma16m(const char* sbase, unsigned voff, unsigned m0val, unsigned long long mask) {
  asm volatile("s_mov_b32 m0, %2\n\ts_mov_b64 exec, %3\n\tglobal_load_lds_dwordx4 %0, %1\n\ts_mov_b64 exec, -1"
               :
               : "v"(voff), "s"(sbase), "s"(m0val), "s"(mask)
               : "memory");
}
template <int N>
DEVI void wait_vm() {
  asm volatile("s_waitcnt vmcnt(%0)" ::"n"(N) : "memory");
}

constexpr int LDS_ALL = 163840;
constexpr int LW_OFF = LDS_ALL - 128;
constexpr int LW_GO = 0, LW_T = 2, LW_O = 4  , LW_X1 = 6, LW_H = 8, LW_X2 = 12, LW_PK = 13, LW_Z = 14, LW_PAIR = 16, LW_AD = 20,
              LW_L = 22, LW_S = 24, LW_ST = 29, LW_HELLO = 30, LW_Q = 31;
DEVI void announce(unsigned addr, unsigned n, char* ws, int kind, int who, bool act) {
  const char* cb = uni_ptr(ws + OFF_CNT + (size_t)kind * 256 * 128);
  const unsigned long long mask = __ballot(act);
  unsigned t, u;
  asm volatile(
      "s_waitcnt vmcnt(0)\n\t"
      "s_mov_b64 exec, 1\n\t"
      "ds_add_rtn_u32 %0, %2, %3\n\t"
      "s_waitcnt lgkmcnt(0)\n\t"
      "v_readfirstlane_b32 %1, %0\n\t"
      "s_cmp_eq_u32 %1, %4\n\t"
      "s_cselect_b64 exec, %5, 0\n\t"
      "global_atomic_add %6, %3, %7 sc1\n\t"
      "s_mov_b64 exec, -1\n\t"
      "s_nop 0"
      : "=&v"(t), "=&s"(u)
      : "v"(addr), "v"(vconst(1u)), "s"(n - 1u), "s"(mask), "v"((unsigned)(who & 255) * 128u), "s"(cb)
      : "memory", "scc");
}
DEVI void cnt_wait(const char* ws, int kind, int who, unsigned n) {
  const char* cp = ws + OFF_CNT + (size_t)(kind * 256 + who) * 128;
#pragma unroll 1
  for (unsigned spin = 0; spin < SPIN_MEM; ++spin) {
    unsigned f;
    asm volatile("global_load_dword %0, %1, off sc1\n\ts_waitcnt vmcnt(0)" : "=&v"(f) : "v"(cp) : "memory");
    if (__builtin_amdgcn_readfirstlane(f) == n - 1u) break;
    __builtin_amdgcn_s_sleep(1);
  }
}
DEVI void dma16_sc1(const char* sbase, unsigned voff, unsigned m0val) {
  asm volatile("s_mov_b32 m0, %2\n\ts_nop 0\n\tglobal_load_lds_dwordx4 %0, %1 sc1"
               :
               : "v"(voff), "s"(sbase), "s"(m0val)
               : "memory");
}
DEVI void bar() { __builtin_amdgcn_s_barrier(); }
DEVI void lds_inc(unsigned addr, unsigned by = 1u) {
  asm volatile("s_mov_b64 exec, 1\n\tds_add_u32 %0, %1\n\ts_mov_b64 exec, -1" ::"v"(addr), "v"(vconst(by)) : "memory");
}
DEVI void lds_spin(unsigned addr, unsigned n) {
  SPIN_LDS {
    if (__builtin_amdgcn_readfirstlane(lds_rd32(addr)) >= n) break;
    __builtin_amdgcn_s_sleep(1);
  }
}
template <int SL = 1>
DEVI void lds_spin4(unsigned addr, unsigned n) {
  SPIN_LDS {
    if (((__builtin_amdgcn_readfirstlane(lds_rd32(addr)) + (0x80u - n) * 0x01010101u) & 0x80808080u) == 0x80808080u) break;
    __builtin_amdgcn_s_sleep(SL);
  }
}
DEVI int lane_id() {
  int l;
  asm volatile("v_mbcnt_lo_u32_b32 %0, -1, 0\n\tv_mbcnt_hi_u32_b32 %0, -1, %0" : "=v"(l));
  __builtin_assume((unsigned)l < 64u);
  return l;
}

template <int I>
DEVI u32x4 tr8(const u32x4 (&v)[8]) {
  constexpr unsigned sel = (I & 1) ? 0x07060302u : 0x05040100u;
  return u32x4{__builtin_amdgcn_perm(v[1][I / 2], v[0][I / 2], sel), __builtin_amdgcn_perm(v[3][I / 2], v[2][I / 2], sel),
               __builtin_amdgcn_perm(v[5][I / 2], v[4][I / 2], sel), __builtin_amdgcn_perm(v[7][I / 2], v[6][I / 2], sel)};
}
template <int PITCH, bool SUM, int... I>
DEVI void tr8_store(const u32x4 (&v)[8], unsigned dst, float (&cs)[8], std::integer_sequence<int, I...>) {
  auto one = [&](auto I_) {
    const u32x4 o = tr8<I_>(v);
    const unsigned d_ = dst;
    asm volatile("ds_write_b128 %0, %1 offset:%2" ::"v"(d_), "v"(o), "n"(I_ * PITCH) : "memory");
    if constexpr (SUM) {
#pragma unroll
      for (int d = 0; d < 4; ++d) {
        const unsigned od = o[d];
        cs[I_] = __builtin_amdgcn_fdot2_f32_bf16(__builtin_bit_cast(bf16x2, od), __builtin_bit_cast(bf16x2, 0x3f803f80u), cs[I_], false);
      }
    }
  };
  (one(std::integral_constant<int, I>{}), ...);
}
template <int NB>
DEVI void lds_out(unsigned src, char* dst, int lane) {
  constexpr int N = (NB + 1023) / 1024;
  u32x4 t[N];
  const unsigned a = src + (unsigned)lane * 16u;
#pragma unroll
  for (int i = 0; i < N; ++i) asm volatile("ds_read_b128 %0, %1 offset:%2" : "=v"(t[i]) : "v"(a), "n"(i * 1024) : "memory");
  const char* d = uni_ptr(dst);
#pragma unroll
  for (int i = 0; i < N; ++i) {
    asm volatile("s_waitcnt lgkmcnt(%1)" : "+v"(t[i]) : "n"(N - 1 - i) : "memory");
    if (i < N - 1 || lane < (NB - (N - 1) * 1024) / 16)
      asm volatile("global_store_dwordx4 %0, %1, %2 sc1\n\ts_nop 1" ::"v"((unsigned)lane * 16u), "v"(t[i]), "s"(d + i * 1024) : "memory");
  }
}
template <int A, int B, class F>
DEVI void static_for(F f) {
  if constexpr (A < B) {
    f(std::integral_constant<int, A>{});
    static_for<A + 1, B>(f);
  }
}
constexpr int WSP_V = 3;
constexpr int RAW_S1 = 7;
constexpr int RAW_S0 = 4;
template <int E>
struct RawX {
  static constexpr int first(int s) {
    return s <= RAW_S0 ? 0 : (s > RAW_S1 ? E : (E * (s - RAW_S0) + RAW_S1 - RAW_S0) / (RAW_S1 + 1 - RAW_S0));
  }
  static constexpr int n(int s) { return first(s + 1) - first(s); }
};
struct RawLd {
  static constexpr int VPS = 10, SB = K1R::SB;
  unsigned xoff[5], woff[5], m0x;
  unsigned long long xact0, wact[5], tag;
  const char *xp, *wp;
  int qb;
  DEVI void q_hook(int st, char* ws, unsigned lds0, int hp) {
    if ((qb >> 1) == st) {
      const int lane = lane_id(), g = lane >> 4;
      const unsigned a = lds0 + (unsigned)((st & 3) * SB + K1R::XB + ((qb & 1) * 32 + 16 * (g >> 1) + 4 * (g & 1) + ((lane >> 2) & 3)) * 288 +
                                           (lane & 3) * 8 + (hp & 1) * 8);
      const unsigned a2 = a + ((lane & 3) >= 1 ? 8u : 0u), a4 = a + ((lane & 3) >= 2 ? 8u : 0u);
      u32x2 qf[7][2];
#pragma unroll
      for (int nt = 0; nt < 7; ++nt)
        asm volatile("ds_read_b64_tr_b16 %0, %2 offset:%3\n\tds_read_b64_tr_b16 %1, %2 offset:%4"
                     : "=&v"(qf[nt][0]), "=&v"(qf[nt][1])
                     : "v"(nt == 2 ? a2 : (nt == 4 ? a4 : a)), "n"(nt * 32 + (nt > 4 ? 16 : (nt > 2 ? 8 : 0))),
                       "n"(2304 + nt * 32 + (nt > 4 ? 16 : (nt > 2 ? 8 : 0)))
                     : "memory");
      const char* d = uni_ptr(ws + OFF_PQ + (size_t)(hp * 18 + qb) * 7168 + 3072);
#pragma unroll
      for (int nt = 0; nt < 7; ++nt) {
        asm volatile("s_waitcnt lgkmcnt(%2)" : "+v"(qf[nt][0]), "+v"(qf[nt][1]) : "n"(12 - 2 * nt) : "memory");
        const u32x4 v = {qf[nt][0][0], qf[nt][0][1], qf[nt][1][0], qf[nt][1][1]};
        asm volatile("global_store_dwordx4 %0, %1, %2 offset:%3 sc1\n\ts_nop 1" ::"v"((unsigned)lane * 16u), "v"(v), "s"(d), "n"(nt * 1024 - 3072) : "memory");
      }
      if (st == 8) lds_wr32(lds0 + (unsigned)(LW_OFF + LW_Q * 4), 1u);
    }
  }
  DEVI void stage_x(unsigned so) {
    dma16m(xp, xoff[0], m0x + so, xact0);
#pragma unroll
    for (int i = 1; i < 4; ++i) dma16(xp, xoff[i], m0x + so + (unsigned)(i * 1024));
    dma16m(xp, xoff[4], m0x + so + 4096u, 0xffffffffull);
    xp += 128;
  }
  DEVI void stage_w(unsigned so) {
#pragma unroll
    for (int i = 0; i < 5; ++i) dma16m(wp, woff[i], m0x + so + (unsigned)(K1R::XB + i * 1024), wact[i]);
    wp += 64 * NQKV * 2;
  }
  DEVI void stage(unsigned so) {
    stage_x(so);
    stage_w(so);
  }
  DEVI void front(const u16* x, const u16* wqkv, const u16* wo, char* ws, int bid, unsigned lds0, int wl, int lane, int rt, int hp,
                  int pj MF_ARG) {
    xp = uni_ptr((const char*)x);
    wp = uni_ptr((const char*)wqkv + ((hp * 72) & ~15));
    m0x = lds0 + (unsigned)(wl * 288 * 16);
    xact0 = wl ? ~0ull : ~0xffull;
    const int row0 = wl * 36 + (lane >> 3);
#pragma unroll
    for (int i = 0; i < 5; ++i) {
      const int row = row0 + i * 8;
      int gr = rt * 128 - 16 + row;
      if (gr < 0) gr = 0;
      xoff[i] = (unsigned)(gr * (D_ * 2)) + (unsigned)(((lane ^ (row >> 1)) & 7) * 16);
    }
    stage_x(0u);
    u32x2 tg;
    asm volatile("global_load_dwordx2 %0, %1, %2 sc1" : "=v"(tg) : "v"(0u), "s"(uni_ptr(ws + OFF_TAG)) : "memory");
    asm volatile("" : "+v"(lane));
#pragma unroll
    for (int i = 0; i < 5; ++i) {
      const unsigned c = (unsigned)(i * 64 + (lane & 63));
      const unsigned wr = (c * 3641u) >> 16, wc = c - wr * 18u, seg = (wc * 13u) >> 6;
      wact[i] = __ballot(wc < 15u) & (i < 4 ? ~0ull : 0xffffffffull);
      woff[i] = (unsigned)(wl * 16 + swap23((int)wr)) * (unsigned)(NQKV * 2) + seg * (unsigned)(D_ * 2 - 80) + wc * 16u;
    }
    stage_w(0u);
    stage((unsigned)SB);
    MF(10);
    asm volatile("s_waitcnt vmcnt(%1)" : "+v"(tg) : "n"(VPS) : "memory");
    tag = (((unsigned long long)(unsigned)__builtin_amdgcn_readfirstlane(tg[1]) << 32) | (unsigned)__builtin_amdgcn_readfirstlane(tg[0])) +
          0x9e3779b97f4a7c15ull;
    MF(1);
    __builtin_amdgcn_s_barrier();
    MF(15);
    if (wl == 0 && lane == 0)
      asm volatile("global_store_dwordx2 %0, %1, off sc1\n\ts_nop 1" ::"v"(ws + OFF_HELLO + bid * 8), "v"(tag) : "memory");
    stage((unsigned)(2 * SB));
    qb = wl == 3 ? rt : ((wl == 2 && rt < 2) ? 16 + rt : 99);
    q_hook(0, ws, lds0, hp);
    const int nb = pj;
    const bool po = pj >= 0 && wl < 2;
    u32x4 v[8];
    unsigned da = 0u;
    if (po) {
      const int l54 = lane < 54 ? lane : 53, gl = l54 / 9, c = l54 - gl * 9, kl = wl * 6 + gl, t = nb / 6, kg = (nb - t * 6) * 12 + kl;
      da = lds0 + (unsigned)(K1R_PO + (kl >> 2) * 4608 + (16 * (kl & 1) + ((kl >> 1) & 1)) * 144 + c * 16);
      const unsigned sa[2] = {(unsigned)(okmap(kg * 8) * D_ + t * 72 + c * 8) * 2u, (unsigned)(okmap(kg * 8 + 4) * D_ + t * 72 + c * 8) * 2u};
      const char* ps = uni_ptr((const char*)wo);
#pragma unroll
      for (int j = 0; j < 8; ++j)
        asm volatile("global_load_dwordx4 %0, %1, %2 offset:%3" : "=v"(v[j]) : "v"(sa[j / 4]), "s"(ps), "n"((j % 4) * D_ * 2) : "memory");
    } else {
#pragma unroll
      for (int j = 0; j < 8; ++j) asm volatile("" : "=v"(v[j]));
    }
    unsigned slot = (unsigned)(3 * SB);
#pragma unroll
    for (int S = 0; S < RAW_S0; ++S) {
      wait_vm<VPS>();
      __builtin_amdgcn_s_barrier();
      if (S == 0) MF(16);
      if (S == 1) MF(17);
      if (S == 2) MF(18);
      if (S == 3) MF(19);
      stage(slot);
      slot = (slot == (unsigned)(3 * SB)) ? 0u : slot + (unsigned)SB;
      q_hook(S + 1, ws, lds0, hp);
      if (po && S == 1) {
        float cs[8];
        asm volatile("" : "+v"(v[0]), "+v"(v[1]), "+v"(v[2]), "+v"(v[3]), "+v"(v[4]), "+v"(v[5]), "+v"(v[6]), "+v"(v[7]));
        tr8_store<288, false>(v, da, cs, std::make_integer_sequence<int, 8>{});
        asm volatile("s_waitcnt lgkmcnt(0)" ::: "memory");
      }
      if (po && S == 2) lds_out<6912>(lds0 + (unsigned)(K1R_PO + wl * 6912), ws + OFF_PO + (size_t)nb * 13824 + wl * 6912, lane);
    }
  }
};
template <bool JOB>
DEVI void k1_raw_loader(RawLd& ld, const u16* __restrict__ w_in, const u16* __restrict__ w_out, char* __restrict__ ws,
                        unsigned long long tag, int bid, unsigned lds0, int wl, int lane, int rt, int hp, int ct, int job, int pj MF_ARG) {
  constexpr int VPS = RawLd::VPS, SB = RawLd::SB, E = JOB ? 16 : 0;
  const bool isi = job < 144;
  const int kb = isi ? job >> 3 : job - 144;
  const int pitch = isi ? F_ * 2 : D_ * 2;
  const int l36 = lane < 36 ? lane : 35;
  unsigned sa = (unsigned)l36 * 16u, da;
  if (isi) da = lds0 + (unsigned)((l36 >= 18 ? 9216 - 288 : 0) + wl * 8 * 288 + l36 * 16);
  else da = lds0 + (unsigned)((l36 / 9) * (4608 - 144) + (16 * (wl & 1) + (wl >> 1)) * 144 + l36 * 16);
  asm volatile("" : "+v"(sa), "+v"(da));
  u32x4 v[2][8];
  const char* ps = uni_ptr((const char*)(isi ? w_in + (job & 7) * 576 : w_out) + (size_t)(kb * 32 + wl * 8) * pitch);
  auto extra = [&](auto J_) {
    constexpr int j = J_;
    const char* q = ps + (j / 2) * pitch;
    u32x4 t;
    asm volatile("global_load_dwordx4 %0, %1, %2 offset:%3" : "=v"(t) : "v"(sa), "s"(q), "n"((j % 2) * 576) : "memory");
    v[j % 2][j / 2] = t;
  };
  auto step = [&](auto S_) {
    constexpr int S = S_;
    wait_vm<RawX<E>::n(S - 2) + (S <= 6 ? VPS : 0) + RawX<E>::n(S - 1)>();
    __builtin_amdgcn_s_barrier();
    MF(16 + S);
    if constexpr (S == RAW_S0) {
      static_assert(RAW_S0 >= 4, "wo blocks");
      if (pj >= 0 && wl == 0 && lane == 0)
        asm volatile("global_store_dwordx2 %0, %1, off sc1\n\ts_nop 1" ::"v"(ws + OFF_POT + (pj / 6) * 128 + (pj % 6) * 8), "v"(tag) : "memory");
    }
    if constexpr (S <= 5) ld.stage((unsigned)(((S + 3) % 4) * SB));
    ld.q_hook(S + 1, ws, lds0, hp);
    static_for<RawX<E>::first(S), RawX<E>::first(S + 1)>(extra);
  };
  const char* hp_ = ws + OFF_HELLO + lane * 32;
  u32x4 a, b;
  if (wl == 1) asm volatile("global_load_dwordx4 %0, %2, off sc1\n\tglobal_load_dwordx4 %1, %2, off offset:16 sc1" : "=&v"(a), "=&v"(b) : "v"(hp_) : "memory");
  else asm volatile("" : "=v"(a), "=v"(b));
  static_for<RAW_S0, 8>(step);
  MF(2);
  if (wl == 1) {
    asm volatile("" : "+v"(a), "+v"(b));
    const unsigned t0 = (unsigned)tag, t1 = (unsigned)(tag >> 32);
#pragma unroll 1
    for (unsigned spin = 0; spin < SPIN_MEM; ++spin) {
      const unsigned bad = ((a[0] ^ t0) | (a[2] ^ t0) | (b[0] ^ t0) | (b[2] ^ t0)) | ((a[1] ^ t1) | (a[3] ^ t1) | (b[1] ^ t1) | (b[3] ^ t1));
      if (!__any(bad != 0u)) break;
      __builtin_amdgcn_s_sleep(4);
      asm volatile("global_load_dwordx4 %0, %2, off sc1\n\tglobal_load_dwordx4 %1, %2, off offset:16 sc1\n\ts_waitcnt vmcnt(0)"
                   : "=&v"(a), "=&v"(b)
                   : "v"(hp_)
                   : "memory");
    }
    lds_wr32(lds0 + (unsigned)(LW_OFF + LW_HELLO * 4), 1u);
    if (bid == 0 && lane == 0) asm volatile("global_store_dwordx2 %0, %1, off sc1\n\ts_nop 1" ::"v"(ws + OFF_TAG), "v"(tag) : "memory");
  }
  MF(11);
  if (rt < 2) lds_spin(lds0 + (unsigned)(LW_OFF + LW_Q * 4), 1u);
  lds_spin(lds0 + (unsigned)K1R_CNT, 8u);
  if constexpr (JOB) {
#pragma unroll
    for (int b = 0; b < 2; ++b)
      asm volatile("s_waitcnt vmcnt(0)"
                   : "+v"(v[b][0]), "+v"(v[b][1]), "+v"(v[b][2]), "+v"(v[b][3]), "+v"(v[b][4]), "+v"(v[b][5]), "+v"(v[b][6]), "+v"(v[b][7])
                   :
                   : "memory");
  }
  MF(12);
  const char* tp = ws + OFF_POT + ct * 128 + (lane < 6 ? lane : 5) * 8;
  u32x2 f;
  asm volatile("global_load_dwordx2 %0, %1, off sc1" : "=v"(f) : "v"(tp) : "memory");
  if constexpr (JOB) {
    static_assert(SB == 4 * 9216, "staging");
#pragma unroll
    for (int b = 0; b < 2; ++b) {
      float cs[8] = {0.f, 0.f, 0.f, 0.f, 0.f, 0.f, 0.f, 0.f};
      tr8_store<288, true>(v[b], da + (unsigned)(b * 18432), cs, std::make_integer_sequence<int, 8>{});
      const f32x4 c0 = {cs[0], cs[1], cs[2], cs[3]}, c1 = {cs[4], cs[5], cs[6], cs[7]};
      const unsigned ca = lds0 + (unsigned)(3 * SB + wl * 2304) + sa * 2u;
      asm volatile("ds_write_b128 %0, %1 offset:%2" ::"v"(ca), "v"(c0), "n"(b * 1152) : "memory");
      asm volatile("ds_write_b128 %0, %1 offset:%2" ::"v"(ca), "v"(c1), "n"(b * 1152 + 16) : "memory");
    }
    lds_inc(lds0 + (unsigned)(LW_OFF + LW_ST * 4));
    lds_spin(lds0 + (unsigned)(LW_OFF + LW_ST * 4), 4u);
    if (isi) {
      const int j = (job & 7) * 4 + wl, ti = j < 16 ? 2 * j + 1 : 2 * (j - 16);
      lds_out<9216>(lds0 + (unsigned)(wl * 9216), ws + OFF_PI + (size_t)(ti * 18 + kb) * 9216, lane);
      f32x4 c[4];
      const unsigned ca = lds0 + (unsigned)(3 * SB + wl * 576) + sa;
#pragma unroll
      for (int i = 0; i < 4; ++i) asm volatile("ds_read_b128 %0, %1 offset:%2" : "=v"(c[i]) : "v"(ca), "n"(i * 2304) : "memory");
      asm volatile("s_waitcnt lgkmcnt(0)" : "+v"(c[0]), "+v"(c[1]), "+v"(c[2]), "+v"(c[3]));
      const f32x4 t = (c[0] + c[1]) + (c[2] + c[3]);
      if (lane < 36) pk_st16(ws + OFF_CSP + ((size_t)kb * F_ + ti * 144) * 4 + sa, __builtin_bit_cast(u32x4, t));
    } else {
      lds_out<4608>(lds0 + (unsigned)(wl * 9216), ws + OFF_PW + (size_t)(wl * 144 + kb) * 4608, lane);
      lds_out<4608>(lds0 + (unsigned)(wl * 9216 + 4608), ws + OFF_PW + (size_t)(wl * 144 + 72 + kb) * 4608, lane);
    }
  }
  MF(8);
  announce(lds0 + (unsigned)(LW_OFF + LW_PK * 4), 4u, ws, CN_PK, isi ? 8 + (job & 3) * 4 + lane : 24 + lane, JOB && lane < (isi ? 4 : 8));
  MF(9);
  if (wl == 0) {
    asm volatile("" : "+v"(f));
#pragma unroll 1
    for (unsigned spin = 0; spin < SPIN_MEM; ++spin) {
      if (!__any(f[0] != (unsigned)tag || f[1] != (unsigned)(tag >> 32))) break;
      __builtin_amdgcn_s_sleep(2);
      asm volatile("global_load_dwordx2 %0, %1, off sc1\n\ts_waitcnt vmcnt(0)" : "=&v"(f) : "v"(tp) : "memory");
    }
  }
}

template <int MT, int NT>
DEVI void k1_gemm(char* smem, const char* ws, const float* __restrict__ csq, int rt, int hp, int mt0, int nt0,
                  int lane MK_ARG) {
  const unsigned lds0 = (unsigned)(size_t)smem;
  const int g = lane >> 4;
  f32x4 cs[NT];
  unsigned ta[NT];
  const char* csp = uni_ptr((const char*)(csq + (hp & 1) * 4));
#pragma unroll
  for (int nt = 0; nt < NT; ++nt) {
    int c = (nt0 + nt) * 16 + g * 4;
    if (c > 104) c = 104;
    const int part = c / 36, ci = c - part * 36;
    asm volatile("global_load_dwordx4 %0, %1, %2 sc1" : "=v"(cs[nt]) : "v"((unsigned)(part * 40 + ci) * 4u), "s"(csp));
    const int to = (nt0 + nt) * 16 + g * 4 > 104 ? 40 : part * 80 + ci * 2 + (ci >= 20 ? 8 : 0);
    ta[nt] = lds0 + (unsigned)(K1_T_OFF + (mt0 * 16 + (lane & 15)) * K1_TP + to);
    asm volatile("" : "+v"(ta[nt]));
  }
  unsigned sa = lds0 + (unsigned)(K1_ST_OFF + (mt0 * 16 + (lane & 15)) * 8);
  asm volatile("" : "+v"(sa));
  f32x4 acc[MT][NT];
#pragma unroll
  for (int mt = 0; mt < MT; ++mt)
#pragma unroll
    for (int nt = 0; nt < NT; ++nt) acc[mt][nt] = f32x4{0.f, 0.f, 0.f, 0.f};
  if constexpr (NT == 7) cnt_wait(ws, CN_X2, rt * 16 + hp, rt ? 24u : 16u);
  bar();
  MK(1);
  {
    GemmMath<K1C, MT, NT, (NT == 7 ? 5 : 3), (NT == 3 ? 6 : NT), true, 2, false, K1_WS> gm(acc);
    gm.template run<9>(lds0, mt0, nt0, lane, 0u);
  }
  asm volatile("s_waitcnt vmcnt(0)" ::: "memory");
#pragma unroll
  for (int nt = 0; nt < NT; ++nt) asm volatile("" : "+v"(cs[nt]));
  MK(2);
  f32x2 st[MT];
#pragma unroll
  for (int mt = 0; mt < MT; ++mt) asm volatile("ds_read_b64 %0, %1 offset:%2" : "=v"(st[mt]) : "v"(sa), "n"(mt * 128) : "memory");
#pragma unroll
  for (int mt = 0; mt < MT; ++mt) {
    if (mt == 0) {
#pragma unroll
      for (int i = 0; i < MT; ++i) asm volatile("s_waitcnt lgkmcnt(0)" : "+v"(st[i]));
    }
    f32x2 M = {st[mt][0], st[mt][0]}, R = {st[mt][1], st[mt][1]};
    asm volatile("" : "+v"(M), "+v"(R));
#pragma unroll
    for (int nt = 0; nt < NT; ++nt) {
      const f32x2 v0 = R * __builtin_elementwise_fma(M, f32x2{cs[nt][0], cs[nt][1]}, f32x2{acc[mt][nt][0], acc[mt][nt][1]});
      const f32x2 v1 = R * __builtin_elementwise_fma(M, f32x2{cs[nt][2], cs[nt][3]}, f32x2{acc[mt][nt][2], acc[mt][nt][3]});
      acc[mt][nt][0] = __uint_as_float(pack2v(v0));
      acc[mt][nt][1] = __uint_as_float(pack2v(v1));
      const f32x2 pk = {acc[mt][nt][0], acc[mt][nt][1]};
      asm volatile("ds_write_b64 %0, %1 offset:%2" ::"v"(ta[nt]), "v"(pk), "n"(mt * 16 * K1_TP) : "memory");
    }
  }
}

DEVI float vmax(float a, float b) {
  float r;
  asm("v_max_f32 %0, %1, %2" : "=v"(r) : "v"(a), "v"(b));
  return r;
}
DEVI float max_g(float x) {
  const unsigned u = __float_as_uint(x);
  const auto p = __builtin_amdgcn_permlane32_swap(u, u, false, false);
  const unsigned v = __float_as_uint(vmax(__uint_as_float(p[0]), __uint_as_float(p[1])));
  const auto q = __builtin_amdgcn_permlane16_swap(v, v, false, false);
  return vmax(__uint_as_float(q[0]), __uint_as_float(q[1]));
}
struct Attn {
  unsigned qa, ka, kb, va, vb, m0, m1, m2, pm[2], ok, oa;
  float fill[4];
  DEVI void setup(unsigned T, int r0, int hp, int wave, int lane) {
    const int g = lane >> 4, r = lane & 15, blk = wave >> 1, hd = wave & 1;
    const unsigned co = (unsigned)(g < 2 ? hd * 48 + g * 16 : 32);
    ka = T + (unsigned)((blk * 32 + 2 * r) * K1_TP) + co;
    kb = T + (unsigned)((blk * 32 + 2 * (r & 7)) * K1_TP) + co;
    const int vr = 4 * g + ((lane >> 2) & 3);
    va = T + (unsigned)((blk * 32 + 2 * vr) * K1_TP + (lane & 3) * 8);
    vb = T + (unsigned)((blk * 32 + 2 * (vr & 7)) * K1_TP + (lane & 3) * 8);
    m0 = (g < 2 || (g == 2 && hd == 0)) ? ~0u : 0u;
    m1 = (g < 2 || (g == 2 && hd == 1)) ? ~0u : 0u;
    m2 = g < 2 ? ~0u : 0u;
    int wmax = (r0 + blk * 32) / 2 + r;
    if (wmax > 7) wmax = 7;
    ok = 0u;
    pm[0] = pm[1] = 0u;
    const float cm = __uint_as_float(vconst(0xce6e6b28u)), ci = __uint_as_float(vconst(0xff800000u));
    const unsigned ch = vconst(0xffffu);
#pragma unroll
    for (int e = 0; e < 4; ++e) {
      const int w1 = r + 8 - 4 * g - e, w = w1 & 15;
      fill[e] = w <= 7 ? cm : ci;
      if (w <= wmax) ok |= 1u << e;
      if (w1 >= 8) pm[e >> 1] |= ch << (16 * (e & 1));
    }
    oa = (unsigned)((8 + hp * 4 + hd * 2 + (g >> 1)) * 1024 + ((blk & 1) * 32 + 2 * r + (g & 1)) * 16);
    asm volatile("" : "+v"(ka), "+v"(kb), "+v"(va), "+v"(vb), "+v"(m0), "+v"(m1), "+v"(m2), "+v"(pm[0]), "+v"(pm[1]), "+v"(ok), "+v"(oa));
    asm volatile("" : "+v"(fill[0]), "+v"(fill[1]), "+v"(fill[2]), "+v"(fill[3]));
  }
  DEVI void run(char* o, int r0, int hp, int wave, int lane) {
    const int hd = wave & 1;
    u32x4 q[2], k0[2], k1[2];
    u32x2 v[2][2][2];
#pragma unroll
    for (int p = 0; p < 2; ++p) {
      asm volatile("ds_read_b128 %0, %1 offset:%2" : "=v"(q[p]) : "v"(ka), "n"((16 + p) * K1_TP) : "memory");
      asm volatile("ds_read_b128 %0, %1 offset:%2" : "=v"(k0[p]) : "v"(ka), "n"(p * K1_TP + 80) : "memory");
      asm volatile("ds_read_b128 %0, %1 offset:%2" : "=v"(k1[p]) : "v"(kb), "n"((32 + p) * K1_TP + 80) : "memory");
    }
    const unsigned vh = (unsigned)hd * 48u;
#pragma unroll
    for (int p = 0; p < 2; ++p) {
      asm volatile("ds_read_b64_tr_b16 %0, %1 offset:%2" : "=v"(v[p][0][0]) : "v"(va + vh), "n"(p * K1_TP + 160) : "memory");
      asm volatile("ds_read_b64_tr_b16 %0, %1 offset:%2" : "=v"(v[p][0][1]) : "v"(vb + vh), "n"((32 + p) * K1_TP + 160) : "memory");
      asm volatile("ds_read_b64_tr_b16 %0, %1 offset:%2" : "=v"(v[p][1][0]) : "v"(va), "n"(p * K1_TP + 192) : "memory");
      asm volatile("ds_read_b64_tr_b16 %0, %1 offset:%2" : "=v"(v[p][1][1]) : "v"(vb), "n"((32 + p) * K1_TP + 192) : "memory");
    }
    const char* ob = uni_ptr(o + (size_t)((r0 >> 6) + (wave >> 2)) * (72 * 1024));
    const bool okb[4] = {(ok & 1u) != 0u, (ok & 2u) != 0u, (ok & 4u) != 0u, (ok & 8u) != 0u};
    const float scale = 0.23570226039551584f, l2e = 1.4426950408889634f;
    const f32x4 z = {0.f, 0.f, 0.f, 0.f};
    f32x4 sc[2][2];
#pragma unroll
    for (int p = 0; p < 2; ++p) {
      asm volatile("s_waitcnt lgkmcnt(%3)" : "+v"(q[p]), "+v"(k0[p]), "+v"(k1[p]) : "n"(11 - 3 * p));
      const s16x8 qf = __builtin_bit_cast(s16x8, q[p] & u32x4{m0, m1, m2, m2});
      sc[p][0] = __builtin_amdgcn_mfma_f32_16x16x32_bf16(__builtin_bit_cast(s16x8, k0[p] & u32x4{~0u, ~0u, m2, m2}), qf, z, 0, 0, 0);
      sc[p][1] = __builtin_amdgcn_mfma_f32_16x16x32_bf16(__builtin_bit_cast(s16x8, k1[p] & u32x4{~0u, ~0u, m2, m2}), qf, z, 0, 0, 0);
    }
    float lg[2][4], mx[2], sum[2];
#pragma unroll
    for (int p = 0; p < 2; ++p) {
#pragma unroll
      for (int h = 0; h < 2; ++h) {
        const unsigned a0 = pack2(sc[p][0][2 * h], sc[p][0][2 * h + 1]), a1 = pack2(sc[p][1][2 * h], sc[p][1][2 * h + 1]);
        const unsigned a = (a1 & pm[h]) | (a0 & ~pm[h]);
        const unsigned b = pack2(bf_lo(a) * scale, bf_hi(a) * scale);
        lg[p][2 * h] = okb[2 * h] ? bf_lo(b) : fill[2 * h];
        lg[p][2 * h + 1] = okb[2 * h + 1] ? bf_hi(b) : fill[2 * h + 1];
      }
      mx[p] = vmax(vmax(lg[p][0], lg[p][1]), vmax(lg[p][2], lg[p][3]));
    }
#pragma unroll
    for (int p = 0; p < 2; ++p) mx[p] = max_g(mx[p]);
#pragma unroll
    for (int p = 0; p < 2; ++p) {
#pragma unroll
      for (int e = 0; e < 4; ++e) lg[p][e] = __builtin_amdgcn_exp2f((lg[p][e] - mx[p]) * l2e);
      sum[p] = (lg[p][0] + lg[p][1]) + (lg[p][2] + lg[p][3]);
    }
#pragma unroll
    for (int p = 0; p < 2; ++p) sum[p] = sum_g(sum[p]);
    asm volatile("s_waitcnt lgkmcnt(0)"
                 : "+v"(v[0][0][0]), "+v"(v[0][0][1]), "+v"(v[0][1][0]), "+v"(v[0][1][1]), "+v"(v[1][0][0]), "+v"(v[1][0][1]),
                   "+v"(v[1][1][0]), "+v"(v[1][1][1]));
    u32x2 y[2], yl;
#pragma unroll
    for (int p = 0; p < 2; ++p) {
      const float inv = __builtin_amdgcn_rcpf(sum[p]);
      const unsigned p0 = pack2(lg[p][0] * inv, lg[p][1] * inv), p1 = pack2(lg[p][2] * inv, lg[p][3] * inv);
      const s16x8 pf = __builtin_bit_cast(s16x8, u32x4{p0 & ~pm[0], p1 & ~pm[1], p0 & pm[0], p1 & pm[1]});
      const u32x4 v0 = {v[p][0][0][0], v[p][0][0][1], v[p][0][1][0], v[p][0][1][1]};
      const u32x4 v1 = {v[p][1][0][0], v[p][1][0][1], v[p][1][1][0], v[p][1][1][1]};
      const f32x4 o0 = __builtin_amdgcn_mfma_f32_16x16x32_bf16(__builtin_bit_cast(s16x8, v0), pf, z, 0, 0, 0);
      const f32x4 o1 = __builtin_amdgcn_mfma_f32_16x16x32_bf16(__builtin_bit_cast(s16x8, v1), pf, z, 0, 0, 0);
      y[p] = u32x2{pack2(o0[0], o0[1]), pack2(o0[2], o0[3])};
      yl[p] = hd ? pack2(o1[2], o1[3]) : pack2(o1[0], o1[1]);
    }
    const auto s0 = __builtin_amdgcn_permlane16_swap(y[0][0], y[1][0], false, false);
    const auto s1 = __builtin_amdgcn_permlane16_swap(y[0][1], y[1][1], false, false);
    const u32x4 w = {s0[0], s1[0], s0[1], s1[1]};
    asm volatile("global_store_dwordx4 %0, %1, %2 sc1\n\ts_nop 1" ::"v"(oa), "v"(w), "s"(ob) : "memory");
    if (lane < 16)
      asm volatile("global_store_dwordx2 %0, %1, %2 sc1\n\ts_nop 1" ::"v"(((oa & 1023u) >> 2) + (unsigned)((hp * 2 + hd) * 256)), "v"(yl), "s"(ob) : "memory");
  }
};

#define NL_MAIN 701
DEVI void code_again(const char* code0, int bid) {
  constexpr int PER = (2 * NL_MAIN + 31) / 32;
  const int lane = lane_id();
  int li = (bid >> 3) * PER + (lane < PER ? lane : PER - 1);
  if (li > 2 * NL_MAIN - 1) li = 2 * NL_MAIN - 1;
  unsigned t;
  asm volatile("global_load_dword %0, %1, %2\n\ts_waitcnt vmcnt(0)" : "=&v"(t) : "v"((unsigned)li * 64u), "s"(code0) : "memory");
}
DEVI void math_start(char* smem, const char* code0, const u16* x, const u16* wqkv, char* ws, int bid, int wave, int lane) {
  const unsigned lds0 = (unsigned)(size_t)smem;
  const int tid = wave * 64 + lane, idx = bid >> 3;
  if (wave < 3) {
    static_assert(2 * SZ_ZP == (size_t)256 * 2048 && NCNT <= 16, "mark split");
    const u32x4 mk = {0xffffffffu, 0xffffffffu, 0xffffffffu, 0xffffffffu};
    char* c = ws + OFF_CNT + (size_t)((lane >> 3) * 256 + bid) * 128 + (lane & 7) * 16;
    pk_st16(wave < 2 ? ws + OFF_ZP + (size_t)((bid * 2 + wave) * 64 + lane) * 16 : c, mk);
    if (wave == 2 && lane < (NCNT - 8) * 8) pk_st16(c + 8 * 256 * 128, mk);
  }
  if (wave == 3) {
    const int li = idx * 54 + (lane < 54 ? lane : 53);
    touch64(uni_ptr((const char*)wqkv), (unsigned)(li * 1152 + 512), lds0 + (unsigned)(K1R_DUMMY + 768));
  }
  if (wave == 4) {
    constexpr int PER = (2 * NL_MAIN + 31) / 32;
    static_assert(PER <= 64, "one load");
    int li = idx * PER + (lane < PER ? lane : PER - 1);
    if (li > 2 * NL_MAIN - 1) li = 2 * NL_MAIN - 1;
    touch64(code0, (unsigned)li * 64u, lds0 + (unsigned)(K1R_DUMMY + 768));
  }
  const u32x4 ones = {0x3f803f80u, 0x3f803f80u, 0x3f803f80u, 0x3f803f80u};
  if (tid < 256) *reinterpret_cast<u32x4*>(smem + (tid >> 6) * K1R::SB + K1R::XB + (tid & 63) * 288 + 240) = ones;
  else if (tid < 288) *reinterpret_cast<u32x4*>(smem + ((tid - 256) >> 3) * K1R::SB + (tid & 7) * 16) = ones;
  else if (tid < 296) *reinterpret_cast<u32x4*>(smem + LW_OFF + (tid - 288) * 16) = u32x4{0u, 0u, 0u, 0u};
  asm volatile("s_waitcnt lgkmcnt(0)" ::: "memory");
}

#define SAFE_OUT(L)
#define NOP_(n) "=s"(n)
template <int NST, int NS, int NSW, int WOFF, int XW, int X0, int S0, unsigned BAR>
struct ResX {
  static constexpr int WS = 4 * 4608, R = 10, V1 = 5 * NS, NV = V1 + 10 * (NST - NS), NG = (NSW + 2) / 3;
  s16x8 xf[XW][2];
  s16x8 wf[R];
  unsigned wrd[2][NG], xo, ea, fa, fv, tag;
  const char* xp[3];
  f32x4 (&acc)[2][5];
  DEVI ResX(f32x4 (&a)[2][5]) : acc(a) {}
  static constexpr int st(int V) { return V < V1 ? V / 5 : NS + (V - V1) / 10; }
  static constexpr int hf(int V) { return V < V1 ? 0 : ((V - V1) / 5) & 1; }
  static constexpr bool first(int V) { return V < V1 ? V % 5 == 0 : (V - V1) % 10 == 0; }
  static constexpr int wreq(int V) {
    int n = V + 5;
    if (V >= V1 - 4) n += V < V1 ? V - (V1 - 5) : 4;
    return n < NV ? n : NV;
  }
  static constexpr int XU = V1 + 1, XF = V1 + 11, XR = V1 + 15, XC = V1 + 21;
  static constexpr int oth(int V) { return NS && ((V >= XU && V <= XF && (V - XU) % 2 == 0) || V == XR || (V > XC && V <= XC + 5)) ? 1 : 0; }
  static constexpr int newer(int V) {
    const int F = V + 1 < NV ? V + 1 : V;
    int v = V - 1, n = 0;
    for (; v >= 0 && wreq(v - 1) > F; --v) n += wreq(v) - wreq(v - 1) + oth(v);
    return n + wreq(v) - 1 - F + (v >= 0 ? oth(v) : 0);
  }
  static constexpr int xreq(int T) {
    int n = X0 + 2 * (T + 1);
    if (n > T + XW) n = T + XW;
    if (T < 0) n = X0;
    return n < NST ? n : NST;
  }
  static_assert(xreq(NS - 2) == NST && (NS == 0 || XC + 5 < NV), "the last steps only compute and swap");
  template <int T, int M>
  DEVI void xld() {
    asm volatile("global_load_dwordx4 %0, %1, %2 sc1" : "=v"(xf[T % XW][M]) : "v"(xo), "s"(xp[M == 0 ? 0 : (T < NS ? 1 : 2)]));
    if constexpr (M == 1 && T + 1 < NST) asm volatile("v_add_u32 %0, 0x2000, %0" : "+v"(xo));
  }
  template <int V>
  DEVI void rd() {
    constexpr int T = st(V), sl = (T / 2 + S0) % NSW;
    asm volatile("ds_read_b128 %0, %1 offset:%2" : "=v"(wf[V % R]) : "v"(wrd[hf(V)][sl / 3]), "n"((sl % 3) * WS + (T & 1) * 9216 + (V % 5) * 32));
  }
  template <int V, int M, int H>
  DEVI void mf() {
    acc[M][V % 5] = __builtin_amdgcn_mfma_f32_16x16x32_bf16(wf[V % R], xf[st(V) % XW][H], acc[M][V % 5], 0, 0, 0);
    __builtin_amdgcn_sched_barrier(0);
  }
  template <int V>
  DEVI void unit() {
    constexpr int T = st(V), n = V % 5, nx = xreq(T) - xreq(T - 1), nw = wreq(V) - wreq(V - 1);
    constexpr int vm = 2 * (xreq(T - 1) - 1 - T);
    static_assert(newer(V) <= 15, "lgkmcnt range");
    if constexpr (first(V) && V % 2 == 0) asm volatile("s_waitcnt vmcnt(%0) lgkmcnt(%1)" ::"n"(vm), "n"(newer(V)));
    else if constexpr (first(V)) asm volatile("s_waitcnt vmcnt(%0)" ::"n"(vm));
    else if constexpr (V % 2 == 0) asm volatile("s_waitcnt lgkmcnt(%0)" ::"n"(newer(V)));
    __builtin_amdgcn_sched_barrier(0);
    if constexpr (V < V1) mf<V, V & 1, V & 1>();
    else mf<V, 0, hf(V)>();
    if constexpr (nw > 0) rd<wreq(V - 1)>();
    __builtin_amdgcn_sched_barrier(0);
    if constexpr (V < V1) {
      mf<V, 1 - (V & 1), 1 - (V & 1)>();
      if constexpr (nw > 1) rd<wreq(V - 1) + 1>();
      if constexpr (nx == 2 && n < 4) xld<xreq(T - 1) + n / 2, n % 2>();
      if constexpr (nx == 1 && n == (T & 1)) xld<xreq(T - 1), 0>();
      if constexpr (nx == 1 && n == (T & 1) + 2) xld<xreq(T - 1), 1>();
      if constexpr (n == 4 && (T & 1) && ((BAR >> (T / 2)) & 1u)) bar();
    } else if constexpr (NS > 0) {
      if constexpr (oth(V) && V < XF) asm volatile("ds_write_b128 %0, %1 offset:%2" ::"v"(ea), "v"(acc[1][(V - XU) / 2]), "n"((V - XU) * 512));
      if constexpr (V == XF) asm volatile("ds_write_b32 %0, %1" ::"v"(fa), "v"(fv));
      if constexpr (V == XF + 2) asm volatile("v_xor_b32 %0, 4, %0" : "+v"(fa));
      if constexpr (V == XR) asm volatile("ds_read_b32 %0, %1" : "+v"(fv) : "v"(fa));
      if constexpr (V == XR + 2) asm volatile("v_xor_b32 %0, 0x2000, %0" : "+v"(ea));
      if constexpr (V == XC) {
        static_assert(wreq(XR) <= XC + 1 || true, "");
        unsigned c = 1u << 18;
        asm volatile(
            "s_waitcnt lgkmcnt(%4)\n\tv_cmp_ne_u32 vcc, %3, %0\n\ts_cbranch_vccnz .Lw%=\n\t.subsection 1\n.Lw%=:\n\tds_read_b32 %0, %2\n\t"
            "s_waitcnt lgkmcnt(0)\n\tv_cmp_ne_u32 vcc, %3, %0\n\t" SAFE_OUT(".Lr%=") "s_cbranch_vccnz .Lw%=\n\ts_branch .Lr%=\n\t.subsection 0\n.Lr%=:"
            : "+v"(fv), NOP_(c)
            : "v"(fa), "s"(tag), "n"(wreq(XC) - wreq(XR))
            : "vcc", "scc");
      }
      if constexpr (oth(V) && V > XC) asm volatile("ds_read_b128 %0, %1 offset:%2" : "=v"(acc[1][V - XC - 1]) : "v"(ea), "n"((V - XC - 1) * 1024));
    }
    __builtin_amdgcn_sched_barrier(0);
  }
  template <int... V>
  DEVI void units(std::integer_sequence<int, V...>) {
    (unit<V>(), ...);
  }
  DEVI void run() {
    units(std::make_integer_sequence<int, NV>{});
    if constexpr (NS > 0) {
      asm volatile("s_waitcnt lgkmcnt(0)" : "+v"(acc[1][0]), "+v"(acc[1][1]), "+v"(acc[1][2]), "+v"(acc[1][3]), "+v"(acc[1][4]));
#pragma unroll
      for (int i = 0; i < 5; ++i) acc[0][i] += acc[1][i];
    }
  }
  template <int... V>
  DEVI void pro(std::integer_sequence<int, V...>) {
    (rd<V>(), ...);
  }
  DEVI void prow() { pro(std::make_integer_sequence<int, wreq(-1)>{}); }
  template <int T0, int... T>
  DEVI void prox(std::integer_sequence<int, T...>) {
    ((xld<T0 + T, 0>(), xld<T0 + T, 1>()), ...);
  }
  DEVI void setup(unsigned lds0, const char* xbase, int wave, int lane, u32x2 (&xv)[5], unsigned red, unsigned nth) {
    const int g = lane >> 4, r = lane & 15, kq = wave & 1;
    xp[0] = xbase + wave * 256;
    xp[1] = xbase + (wave ^ 1) * 256;
    xp[2] = xp[0] + (kq ? -4096 : 4096);
    xo = (unsigned)((4 * kq + g) * 1024 + r * 16);
#pragma unroll
    for (int i = 0; i < 2 * NG; ++i) {
      wrd[i / NG][i % NG] = lds0 + (unsigned)(WOFF + (i % NG) * 3 * WS + (kq ^ (i / NG)) * 4608 + (2 * (r & 7) + 16 * (g & 1) + (g >> 1)) * 144 + (r >> 3) * 16);
      if (i < NG || NS == 0 || i % NG == ((NS / 2 + S0) % NSW) / 3) asm volatile("" : "+v"(wrd[i / NG][i % NG]));
    }
    asm volatile("" : "+v"(xo));
    if constexpr (NS > 0) {
      ea = lds0 + red + (unsigned)((wave ^ 1) * 8192 + lane * 16);
      fa = lds0 + (unsigned)(LW_OFF + (LW_PAIR + wave) * 4);
      fv = tag = nth;
      asm volatile("" : "+v"(ea), "+v"(fa), "+v"(fv));
    }
    asm volatile("s_waitcnt vmcnt(0)" : "+v"(xv[0]), "+v"(xv[1]), "+v"(xv[2]), "+v"(xv[3]), "+v"(xv[4]));
#pragma unroll
    for (int i = 0; i < 5; ++i) {
      acc[0][i] = f32x4{bf_lo(xv[i][0]), bf_hi(xv[i][0]), bf_lo(xv[i][1]), bf_hi(xv[i][1])};
      asm volatile("" : "+v"(acc[0][i]));
      acc[1][i] = f32x4{0.f, 0.f, 0.f, 0.f};
    }
  }
};

#define P5_XW 8
#define P5_X0 5
constexpr unsigned P5_BAR = 0x2aaau;
constexpr int P5_W = 15 * 1024;
constexpr int P2_W = K1_ST_OFF - 18 * 4608;
constexpr int P5_RED = 0;
static_assert(P5_RED % 16384 == 0 && P5_RED + 3 * 8192 + 5120 <= P5_W + 18432, "exchange area");
static_assert(P2_W >= K1_T_OFF + 144 * K1_TP + 64 && P2_W % 16 == 0 && P5_W + 8 * 18432 <= LW_OFF, "LDS placement");

DEVI void res_ldx(u32x2 (&xv)[5], const char* src, bool rm, int rb, int ct, int wave, int lane) {
  const int g = lane >> 4, g2 = g & 1, rl = wave * 16 + (lane & 15);
  const char* xp = uni_ptr(rm ? src + ((size_t)rb * 64 * D_ + ct * 72) * 2 : src + (size_t)(rb * 72 + ct * 9) * 1024);
  const unsigned o = rm ? (unsigned)(rl * (D_ * 2) + g * 8) : (unsigned)((g >> 1) * 1024 + rl * 16 + g2 * 8);
  const unsigned o2 = rm ? (unsigned)(rl * (D_ * 2) + g2 * 8 + 128) : (unsigned)(8192 + rl * 16 + g2 * 8);
#pragma unroll
  for (int i = 0; i < 4; ++i) asm volatile("global_load_dwordx2 %0, %1, %2 sc1" : "=v"(xv[i]) : "v"(o), "s"(xp + i * (rm ? 32 : 2048)));
  asm volatile("global_load_dwordx2 %0, %1, %2 sc1" : "=v"(xv[4]) : "v"(o2), "s"(xp));
}

DEVI void p2_gemm(f32x4 (&acc)[2][5], u32x2 (&xv)[5], unsigned lds0, const char* ob, int layer, int wave, int lane MK_ARG) {
  ResX<9, 0, 5, P2_W, 9, 9, 0, 0u> gm(acc);
  gm.setup(lds0, ob, wave, lane, xv, 0u, 0u);
  unsigned a = (gm.xo & ~1023u) | ((gm.xo & 1023u) >> 2);
  gm.xo += 8192u;
  asm volatile("" : "+v"(a), "+v"(gm.xo));
  const char* pp[2] = {gm.xp[0] - wave * 192, gm.xp[2] - wave * 192};
  bar();
  MK(6);
  unsigned e[2][4];
#pragma unroll
  for (int d = 0; d < 8; ++d) asm volatile("global_load_dword %0, %1, %2 offset:%3 sc1" : "=v"(e[d / 4][d % 4]) : "v"(a), "s"(pp[d / 4]), "n"((d % 4) * 256));
  gm.template prox<1>(std::make_integer_sequence<int, 8>{});
  lds_inc(lds0 + (unsigned)(LW_OFF + (LW_T + layer) * 4));
  gm.prow();
  asm volatile("s_waitcnt vmcnt(16)"
               : "+v"(e[0][0]), "+v"(e[0][1]), "+v"(e[0][2]), "+v"(e[0][3]), "+v"(e[1][0]), "+v"(e[1][1]), "+v"(e[1][2]), "+v"(e[1][3]));
  gm.xf[0][0] = __builtin_bit_cast(s16x8, u32x4{e[0][0], e[0][1], e[0][2], e[0][3]});
  gm.xf[0][1] = __builtin_bit_cast(s16x8, u32x4{e[1][0], e[1][1], e[1][2], e[1][3]});
  __builtin_amdgcn_sched_barrier(0);
  gm.run();
}

struct ResOut {
  const char *xp[3], *sp;
  unsigned xo, so;
  DEVI void setup(char* xim, char* xs, u16* out, bool fin, int rb, int ct, int wave, int lane) {
    const int g = lane >> 4, rl = wave * 16 + (lane & 15);
    xp[0] = uni_ptr(fin ? (const char*)(out + (size_t)rb * 64 * D_ + ct * 72) : xim + (size_t)(rb * 72 + ct * 9) * 1024);
    xp[1] = xp[0] + (fin ? 64 : 4096);
    xp[2] = xp[0] + (fin ? 128 : 8192);
    sp = uni_ptr(xs + (size_t)(rb * 8 + ct) * 512);
    xo = (unsigned)(((g & 1) * 2 + (g >> 1)) * (fin ? 16 : 1024) + rl * (fin ? D_ * 2 : 16));
    so = (unsigned)(rl * 8);
    asm volatile("" : "+v"(xo), "+v"(so), "+s"(xp[0]), "+s"(xp[1]), "+s"(xp[2]), "+s"(sp));
  }
  DEVI void run(const f32x4 (&a)[5], bool fin) {
    u32x2 y[5];
    u32x4 v[3];
#pragma unroll
    for (int nt = 0; nt < 5; ++nt) y[nt] = u32x2{pack2(a[nt][0], a[nt][1]), pack2(a[nt][2], a[nt][3])};
#pragma unroll
    for (int i = 0; i < 3; ++i) {
      const auto s0 = __builtin_amdgcn_permlane16_swap(y[2 * i][0], y[i < 2 ? 2 * i + 1 : 4][0], false, false);
      const auto s1 = __builtin_amdgcn_permlane16_swap(y[2 * i][1], y[i < 2 ? 2 * i + 1 : 4][1], false, false);
      v[i] = u32x4{s0[0], s1[0], s0[1], s1[1]};
    }
    if (fin) {
      asm volatile("global_store_dwordx4 %0, %1, %4\n\tglobal_store_dwordx4 %0, %2, %5\n\ts_mov_b64 exec, 0xffff\n\tglobal_store_dwordx4 %0, %3, %6\n\t"
                   "s_mov_b64 exec, -1\n\ts_nop 0" ::"v"(xo), "v"(v[0]), "v"(v[1]), "v"(v[2]), "s"(xp[0]), "s"(xp[1]), "s"(xp[2])
                   : "memory");
      return;
    }
    asm volatile("global_store_dwordx4 %0, %1, %4 sc1\n\tglobal_store_dwordx4 %0, %2, %5 sc1\n\ts_mov_b64 exec, 0xffff\n\t"
                 "global_store_dwordx4 %0, %3, %6 sc1\n\ts_mov_b64 exec, -1\n\ts_nop 0" ::"v"(xo), "v"(v[0]), "v"(v[1]), "v"(v[2]),
                 "s"(xp[0]), "s"(xp[1]), "s"(xp[2])
                 : "memory");
    const bool lo = lane_id() < 16;
    float s[2] = {0.f, 0.f}, q[2] = {0.f, 0.f};
#pragma unroll
    for (int i = 0; i < 12; ++i) {
      const unsigned u = (i < 8 || lo) ? v[i / 4][i % 4] : 0u;
      s[i & 1] = __builtin_amdgcn_fdot2_f32_bf16(__builtin_bit_cast(bf16x2, u), __builtin_bit_cast(bf16x2, 0x3f803f80u), s[i & 1], false);
      q[i & 1] = __builtin_amdgcn_fdot2_f32_bf16(__builtin_bit_cast(bf16x2, u), __builtin_bit_cast(bf16x2, u), q[i & 1], false);
    }
    const f32x2 sq = {sum_g(s[0] + s[1]), sum_g(q[0] + q[1])};
    asm volatile("s_mov_b64 exec, 0xffff\n\tglobal_store_dwordx2 %0, %1, %2 sc1\n\ts_mov_b64 exec, -1" ::"v"(so), "v"(sq), "s"(sp) : "memory");
  }
};

using K3C = GemmCfg<128, 144>;
constexpr int K3_Z1_OFF = K3C::LDS_BYTES;
constexpr int K3_C2_OFF = K3_Z1_OFF + K3C::XB;
constexpr int K3_ZC_OFF = K3_C2_OFF + 2048;
constexpr int K3_DM_OFF = K3_ZC_OFF + 1024;
constexpr int K3_ST_OFF = K3_Z1_OFF + 4 * 5120;
constexpr int K3_C1_OFF = K3_ST_OFF + 2048;
constexpr int K3_BS_OFF = K3_C1_OFF + 576;
static_assert(K3_BS_OFF + 512 <= LW_OFF, "K3 LDS");
static_assert(P5_W + 19 * 1024 == K3C::SB && K3C::SB == 34 * 1024, "w_out ring");
constexpr int K3_D = 4;

DEVI void red4(float (&t)[4], float& s, float& q) {
#pragma unroll
  for (int j = 0; j < 2; ++j) {
    const auto p = __builtin_amdgcn_permlane16_swap(__float_as_uint(t[j]), __float_as_uint(t[2 + j]), false, false);
    const unsigned v = __float_as_uint(__uint_as_float(p[0]) + __uint_as_float(p[1]));
    const auto w = __builtin_amdgcn_permlane32_swap(v, v, false, false);
    t[j] = __uint_as_float(w[0]) + __uint_as_float(w[1]);
    asm volatile("" : "+v"(t[j]));
  }
  s = t[0];
  q = t[1];
}
DEVI void red8(const f32x4& a, const f32x4& b, float& s, float& q) {
  float t[4];
#pragma unroll
  for (int j = 0; j < 4; ++j) {
    t[j] = a[j] + b[j];
    asm volatile("" : "+v"(t[j]));
  }
  red4(t, s, q);
}
DEVI void red16(const f32x4& a, const f32x4& b, const f32x4& c, const f32x4& d, float& s, float& q) {
  float t[4];
#pragma unroll
  for (int j = 0; j < 4; ++j) {
    float u = a[j] + b[j], v = c[j] + d[j];
    asm volatile("" : "+v"(u), "+v"(v));
    t[j] = u + v;
    asm volatile("" : "+v"(t[j]));
  }
  red4(t, s, q);
}

constexpr int b_k(int s) { return s == 0 ? 0 : (s < 3 ? 6 + s : (s < 5 ? 2 + s : (s == 5 ? 4 : s - 5))); }
template <bool B>
struct UMath {
  static constexpr int D = 4, R = 6, NV = 162, SB = K3C::SB, P = K3C::P;
  s16x8 wf[R], xf[2][2];
  s16x8 (&xc)[4][2][2];
  f32x4 (&acc)[2][9];
  unsigned xa, wa;
  unsigned fa, fw, fv;
  DEVI UMath(f32x4 (&a)[2][9], s16x8 (&x)[4][2][2]) : xc(x), acc(a) {}
  static constexpr int wslot(int s) { return B ? (s + 1) % 4 : s % 4; }
  static constexpr int xslot(int s) { return B ? b_k(s) % 5 : s % 5; }
  static constexpr bool res(int q) { return !B || b_k(q / 2) >= 4; }
#define BPS 9
  static constexpr bool chk(int s) { return s >= 3 && s < 7 && s != BPS; }
  static constexpr bool xop(int v) { return v + D < NV && (v + D) % 9 < 2 && res((v + D) / 9); }
  static constexpr int ops1(int v) { return (xop(v) || (B && ((v % 18 == 9 && chk(v / 18)) || (v % 18 == 0 && v > 0)))) ? 1 : 0; }
  static constexpr int newer(int V) {
    int v0 = (V % 9 == 8 ? V : V + 1) - D, n = 0;
    if (v0 >= 0 && !(V % 9 == 0 && res(V / 9))) n += ops1(v0);
    for (int v = v0 + 1; v < V; ++v)
      if (v >= 0) n += (v + D < NV ? 1 : 0) + ops1(v);
    return n;
  }
  template <int V>
  DEVI void wread() {
    asm volatile("ds_read_b128 %0, %1 offset:%2" : "=v"(wf[V % R]) : "v"(wa), "n"((wslot(V / 18) & 1) * SB + ((V % 18) / 9) * 32 * P + (V % 9) * 32));
  }
  template <int Q, int M>
  DEVI void xread() {
    asm volatile("ds_read_b128 %0, %1 offset:%2" : "=v"(xf[Q & 1][M]) : "v"(xa), "n"((xslot(Q / 2) & 1) * SB + (Q & 1) * 8192 + M * (B ? 256 : 64)));
  }
  template <int V, int M_>
  DEVI void mf() {
    constexpr int M = ((V % 9) & 1) ? 1 - M_ : M_, q = V / 9;
    if constexpr (!B) acc[M][V % 9] = __builtin_amdgcn_mfma_f32_16x16x32_bf16(xf[q & 1][M], wf[V % R], acc[M][V % 9], 0, 0, 0);
    else if constexpr (res(q)) acc[M][V % 9] = __builtin_amdgcn_mfma_f32_16x16x32_bf16(wf[V % R], xf[q & 1][M], acc[M][V % 9], 0, 0, 0);
    else acc[M][V % 9] = __builtin_amdgcn_mfma_f32_16x16x32_bf16(wf[V % R], xc[b_k(q / 2)][q & 1][M], acc[M][V % 9], 0, 0, 0);
    __builtin_amdgcn_sched_barrier(0);
  }
  template <int V>
  DEVI void unit() {
    constexpr int U = V % 18, s = V / 18;
    static_assert(newer(V) <= 15, "lgkmcnt range");
    if constexpr ((V % 9) % 2 == 0 && V > 0) asm volatile("s_waitcnt lgkmcnt(%0)" ::"n"(newer(V)));
    __builtin_amdgcn_sched_barrier(0);
    mf<V, 0>();
    if constexpr (V + D < NV) wread<V + D>();
    if constexpr (s < 8 && U == 13 && (wslot(s + 1) >> 1) != (wslot(s) >> 1))
      asm volatile("v_add_u32 %0, %1, %0" : "+v"(wa) : "n"(((wslot(s + 1) >> 1) - (wslot(s) >> 1)) * 2 * SB));
    __builtin_amdgcn_sched_barrier(0);
    mf<V, 1>();
    if constexpr (xop(V)) xread<(V + D) / 9, (V + D) % 9>();
    if constexpr (s < 8) {
      if constexpr (U == 11 && res(2 * s + 2) && (xslot(s + 1) >> 1) != (xslot(s) >> 1))
        asm volatile("v_add_u32 %0, %1, %0" : "+v"(xa) : "n"(((xslot(s + 1) >> 1) - (xslot(s) >> 1)) * 2 * SB));
      if constexpr (!B && U == 13) __builtin_amdgcn_s_barrier();
      if constexpr (B && U == 9 && chk(s)) asm volatile("ds_read_u8 %0, %1 offset:%2" : "+v"(fv) : "v"(fa), "n"(s == 6 ? 0 : 8));
      unsigned n = 1u << 18;
      if constexpr (B && U == 13 && chk(s)) {
        asm volatile(
            "v_cmp_gt_u32 vcc, %3, %0\n\ts_cbranch_vccnz .Lw%=\n\t.subsection 1\n.Lw%=:\n\tds_read_u8 %0, %2 offset:%4\n\ts_waitcnt lgkmcnt(0)\n\t"
            "v_cmp_gt_u32 vcc, %3, %0\n\t" SAFE_OUT(".Lr%=") "s_cbranch_vccnz .Lw%=\n\ts_branch .Lr%=\n\t.subsection 0\n.Lr%=:"
            : "+v"(fv), NOP_(n)
            : "v"(fa), "n"(s == 6 ? 4 : s + 2), "n"(s == 6 ? 0 : 8)
            : "vcc", "scc");
      }
      if constexpr (B && U == 13 && s == BPS) {
        asm volatile(
            ".La%=:\n\ts_sleep 1\n\tds_read_u8 %0, %2\n\ts_waitcnt lgkmcnt(0)\n\tv_cmp_gt_u32 vcc, 3, %0\n\t" SAFE_OUT(".Lr%=") "s_cbranch_vccnz .La%=\n"
            ".Lb%=:\n\tds_read_u8 %0, %2 offset:8\n\ts_waitcnt lgkmcnt(0)\n\tv_cmp_gt_u32 vcc, %3, %0\n\t" SAFE_OUT(".Lr%=") "s_cbranch_vccnz .Lb%=\n.Lr%=:"
            : "+v"(fv), NOP_(n)
            : "v"(fa), "n"(s + 2)
            : "vcc", "scc");
      }
    }
    if constexpr (B && U == 15) asm volatile("v_mov_b32 %0, %1" : "+v"(fv) : "n"(s + 1));
    if constexpr (B && ((U == 0 && s > 0) || V == NV - 1)) asm volatile("ds_write_b8 %0, %1" ::"v"(fw), "v"(fv));
    __builtin_amdgcn_sched_barrier(0);
  }
  template <int... V>
  DEVI void units(std::integer_sequence<int, V...>) {
    (unit<V>(), ...);
  }
  DEVI void first() {
    if constexpr (!B) {
      xread<0, 0>();
      xread<0, 1>();
    }
    wread<0>();
    wread<1>();
    wread<2>();
    wread<3>();
    asm volatile("s_waitcnt lgkmcnt(0)");
  }
  DEVI void run() {
    units(std::make_integer_sequence<int, NV>{});
#pragma unroll
    for (int m = 0; m < 2; ++m)
#pragma unroll
      for (int n = 0; n < 9; ++n) asm volatile("" : "+v"(acc[m][n]));
  }
};

#define PACE 0
#define G0P 2
#define ZSLEEP 6
DEVI void p3_g0(char* smem, char* __restrict__ ws, int layer, int mi, int ni, int wm, int lane MK_ARG) {
  constexpr int SB = K3C::SB;
  const unsigned lds0 = (unsigned)(size_t)smem;
  const int g = lane >> 4, r = lane & 15;
  const unsigned lw = lds0 + (unsigned)(LW_OFF + layer * 4), me = 1u << (8 * wm);
  f32x4 ac[2][9];
#pragma unroll
  for (int mt = 0; mt < 2; ++mt)
#pragma unroll
    for (int j = 0; j < 9; ++j) ac[mt][j] = f32x4{0.f, 0.f, 0.f, 0.f};
  unsigned ra = lds0 + (unsigned)(K3_ST_OFF + (wm * 32 + g * 8) * 4), ca = lds0 + (unsigned)(K3_C2_OFF + r * 8);
  asm volatile("" : "+v"(ra), "+v"(ca));
  {
    s16x8 xc[4][2][2];
    UMath<false> gm(ac, xc);
    gm.xa = lds0 + (unsigned)(g * 2048 + (wm * 32 + 8 * (r >> 2) + (r & 3)) * 16);
    gm.wa = lds0 + (unsigned)(K3C::XB + (g * 8 + (r & 7)) * K3C::P + (r >> 3) * 16);
    asm volatile("" : "+v"(gm.xa), "+v"(gm.wa));
    bar();
    gm.first();
    gm.run();
  }
  lds_inc(lw + LW_AD * 4, me);
  MK(11);
  f32x2 z[9][4];
  f32x2 sm[4], sq[4];
  {
    f32x4 rc[4][2];
    f32x2 cs[9];
#pragma unroll
    for (int i = 0; i < 8; ++i)
      asm volatile("ds_read_b128 %0, %1 offset:%2" : "=v"(rc[i / 2][i % 2]) : "v"(ra), "n"((i / 2) * 512 + (i % 2) * 16) : "memory");
#pragma unroll
    for (int j = 0; j < 9; ++j) asm volatile("ds_read_b64 %0, %1 offset:%2" : "=v"(cs[j]) : "v"(ca), "n"(j * 128) : "memory");
    asm volatile("s_waitcnt lgkmcnt(0)"
                 : "+v"(rc[0][0]), "+v"(rc[0][1]), "+v"(rc[1][0]), "+v"(rc[1][1]), "+v"(rc[2][0]), "+v"(rc[2][1]), "+v"(rc[3][0]), "+v"(rc[3][1]));
#pragma unroll
    for (int j = 0; j < 9; ++j) asm volatile("" : "+v"(cs[j]));
    auto RC = [&](int k, int i) { return f32x2{rc[k][i / 2][(i % 2) * 2], rc[k][i / 2][(i % 2) * 2 + 1]}; };
#pragma unroll
    for (int j = 0; j < 9; ++j) {
      f32x2 u[4], w[4];
#pragma unroll
      for (int i = 0; i < 4; ++i) u[i] = __builtin_elementwise_fma(RC(0, i), cs[j], f32x2{ac[i / 2][j][(i % 2) * 2], ac[i / 2][j][(i % 2) * 2 + 1]});
#pragma unroll
      for (int i = 0; i < 4; ++i) w[i] = u[i] * u[i];
#pragma unroll
      for (int i = 0; i < 4; ++i) w[i] = __builtin_elementwise_fma(w[i], RC(3, i), RC(2, i));
#pragma unroll
      for (int i = 0; i < 4; ++i) w[i] = u[i] * w[i];
      __builtin_amdgcn_sched_barrier(0);
#pragma unroll
      for (int i = 0; i < 4; ++i) w[i] = f32x2{__builtin_amdgcn_exp2f(w[i][0]), __builtin_amdgcn_exp2f(w[i][1])};
      __builtin_amdgcn_sched_barrier(0);
#pragma unroll
      for (int i = 0; i < 4; ++i) w[i] = __builtin_elementwise_fma(w[i], RC(1, i), RC(1, i));
      __builtin_amdgcn_sched_barrier(0);
#pragma unroll
      for (int i = 0; i < 4; ++i) w[i] = f32x2{__builtin_amdgcn_rcpf(w[i][0]), __builtin_amdgcn_rcpf(w[i][1])};
      __builtin_amdgcn_sched_barrier(0);
#pragma unroll
      for (int i = 0; i < 4; ++i) z[j][i] = u[i] * w[i];
#pragma unroll
      for (int i = 0; i < 4; ++i) {
        if (j == 1) sm[i] = z[0][i] + z[1][i];
        if (j > 1) sm[i] += z[j][i];
      }
#pragma unroll
      for (int i = 0; i < 4; ++i) sq[i] = j ? __builtin_elementwise_fma(z[j][i], z[j][i], sq[i]) : z[j][i] * z[j][i];
      asm volatile("" : "+v"(z[j][0]), "+v"(z[j][1]), "+v"(z[j][2]), "+v"(z[j][3]));
      __builtin_amdgcn_sched_barrier(0);
    }
  }
  f32x4 e;
  {
    float v[4][4], d[2][4], f[4];
#pragma unroll
    for (int i = 0; i < 4; ++i) {
      v[i][0] = sm[i][0];
      v[i][1] = sq[i][0];
      v[i][2] = sm[i][1];
      v[i][3] = sq[i][1];
    }
    asm volatile("s_nop 1" : "+v"(v[3][1]), "+v"(v[3][3]), "+v"(v[2][1]), "+v"(v[2][3]));
#pragma unroll
    for (int i = 0; i < 2; ++i)
#pragma unroll
      for (int c = 0; c < 4; ++c)
        asm volatile("v_add_f32_dpp %0, %1, %1 row_ror:8 row_mask:0xf bank_mask:0x3\n\tv_add_f32_dpp %0, %2, %2 row_ror:8 row_mask:0xf bank_mask:0xc"
                     : "=&v"(d[i][c])
                     : "v"(v[i][c]), "v"(v[i + 2][c]));
#pragma unroll
    for (int c = 0; c < 4; ++c)
      asm volatile("v_add_f32_dpp %0, %1, %1 row_shl:4 row_mask:0xf bank_mask:0x5\n\tv_add_f32_dpp %0, %2, %2 row_shr:4 row_mask:0xf bank_mask:0xa"
                   : "=&v"(f[c])
                   : "v"(d[0][c]), "v"(d[1][c]));
    asm volatile(
        "v_add_f32_dpp %0, %0, %0 quad_perm:[1,0,3,2] row_mask:0xf bank_mask:0xf\n\t"
        "v_add_f32_dpp %1, %1, %1 quad_perm:[1,0,3,2] row_mask:0xf bank_mask:0xf\n\t"
        "v_add_f32_dpp %2, %2, %2 quad_perm:[1,0,3,2] row_mask:0xf bank_mask:0xf\n\t"
        "v_add_f32_dpp %3, %3, %3 quad_perm:[1,0,3,2] row_mask:0xf bank_mask:0xf\n\t"
        "v_add_f32_dpp %0, %0, %0 quad_perm:[2,3,0,1] row_mask:0xf bank_mask:0xf\n\t"
        "v_add_f32_dpp %1, %1, %1 quad_perm:[2,3,0,1] row_mask:0xf bank_mask:0xf\n\t"
        "v_add_f32_dpp %2, %2, %2 quad_perm:[2,3,0,1] row_mask:0xf bank_mask:0xf\n\t"
        "v_add_f32_dpp %3, %3, %3 quad_perm:[2,3,0,1] row_mask:0xf bank_mask:0xf"
        : "+v"(f[0]), "+v"(f[1]), "+v"(f[2]), "+v"(f[3]));
    e = f32x4{f[0], __uint_as_float(__float_as_uint(f[1]) & 0x7fffffffu), f[2], __uint_as_float(__float_as_uint(f[3]) & 0x7fffffffu)};
  }
  const char* zp = uni_ptr(ws + OFF_ZP + (size_t)layer * SZ_ZP + (size_t)mi * 16384 + wm * 256);
  if ((lane & 3) == 0)
    asm volatile("global_store_dwordx4 %0, %1, %2 sc1\n\ts_nop 1" ::"v"((unsigned)(ni * 1024 + lane * 4)), "v"(e), "s"(zp) : "memory");
  lds_inc(lw + LW_AD * 4, me);
  __builtin_amdgcn_s_setprio(2);
  MK(12);
  const char* dst = uni_ptr(ws + OFF_Z2 + (size_t)layer * SZ_Z + (size_t)ni * S_ * 288 + (size_t)(mi * 4 + wm) * 9216 + 4096);
  const unsigned lane16 = (unsigned)lane * 16u;
  u32x4 wb[9];
  {
    static_assert(b_k(7) == 2 && b_k(8) == 3, "order");
    const char* wp = uni_ptr(ws + OFF_PI + (size_t)ni * (18 * K3C::WB) + 11 * K3C::WB + wm * 9216 + 4096);
#pragma unroll
    for (int j = 0; j < 8; ++j) asm volatile("global_load_dwordx4 %0, %1, %2 offset:%3 sc1" : "=v"(wb[j]) : "v"(lane16), "s"(wp), "n"(j * 1024 - 4096) : "memory");
    asm volatile("global_load_dwordx4 %0, %1, %2 sc1" : "=v"(wb[8]) : "v"(lane16 + 4096u), "s"(wp) : "memory");
  }
  u32x4 y[9];
  {
    const unsigned src = (unsigned)(g * 4096 + r * 16);
    unsigned zw = lds0 + (unsigned)(K3_ZC_OFF + (wm * 32 + 2 * r + (g & 1)) * 4), zr = lds0 + (unsigned)(K3_ZC_OFF + (wm * 32 + g * 8) * 4);
    asm volatile("" : "+v"(zw), "+v"(zr));
    f32x4 a, b, c, d;
    __builtin_amdgcn_s_sleep(ZSLEEP);
#pragma unroll 1
    for (unsigned spin = 0; spin < SPIN_MEM; ++spin) {
      asm volatile(
          "global_load_dwordx4 %0, %4, %5 sc1\n\t"
          "global_load_dwordx4 %1, %4, %5 offset:1024 sc1\n\t"
          "global_load_dwordx4 %2, %4, %5 offset:2048 sc1\n\t"
          "global_load_dwordx4 %3, %4, %5 offset:3072 sc1\n\t"
          "s_waitcnt vmcnt(0)"
          : "=&v"(a), "=&v"(b), "=&v"(c), "=&v"(d)
          : "v"(src), "s"(zp)
          : "memory");
      const unsigned bad = (__float_as_uint(a[1]) | __float_as_uint(a[3]) | __float_as_uint(b[1]) | __float_as_uint(b[3]) |
                            __float_as_uint(c[1]) | __float_as_uint(c[3]) | __float_as_uint(d[1]) | __float_as_uint(d[3])) >> 31;
      if (!__any(bad)) break;
    }
    MK(13);
    float s, q, zmb, zi;
    red16(a, b, c, d, s, q);
    ln_finish(s, q, 1.0f / (float)FH_, zmb, zi);
    f32x4 R[2], M[2];
    asm volatile(
        "ds_write2st64_b32 %4, %5, %6 offset1:2\n\t"
        "ds_read_b128 %0, %7\n\tds_read_b128 %2, %7 offset:512\n\tds_read_b128 %1, %7 offset:16\n\tds_read_b128 %3, %7 offset:528\n\t"
        "s_waitcnt lgkmcnt(0)"
        : "=&v"(R[0]), "=&v"(R[1]), "=&v"(M[0]), "=&v"(M[1])
        : "v"(zw), "v"(zi), "v"(-zmb * zi), "v"(zr)
        : "memory");
    MK(28);
#define NRM 0
    auto st = [&](int j) {
      if (j < 8) asm volatile("global_store_dwordx4 %0, %1, %2 offset:%3 sc1\n\ts_nop 1" ::"v"(lane16), "v"(y[j]), "s"(dst), "n"(j * 1024 - 4096) : "memory");
      else asm volatile("global_store_dwordx4 %0, %1, %2 sc1\n\ts_nop 1" ::"v"(lane16 + 4096u), "v"(y[j]), "s"(dst) : "memory");
    };
#pragma unroll
    for (int j = 0; j < 9; ++j) {
      if (NRM & 2) {
        f32x2 t[4];
#pragma unroll
        for (int k = 0; k < 4; ++k)
          t[k] = __builtin_elementwise_fma(z[j][k], f32x2{R[k / 2][(k % 2) * 2], R[k / 2][(k % 2) * 2 + 1]}, f32x2{M[k / 2][(k % 2) * 2], M[k / 2][(k % 2) * 2 + 1]});
        y[j] = u32x4{pack2v(t[0]), pack2v(t[1]), pack2v(t[2]), pack2v(t[3])};
      } else {
        float t[8];
#pragma unroll
        for (int k = 0; k < 8; ++k) {
          t[k] = __builtin_fmaf(z[j][k / 2][k % 2], R[k / 4][k % 4], M[k / 4][k % 4]);
          asm volatile("" : "+v"(t[k]));
        }
        y[j] = u32x4{pack2(t[0], t[1]), pack2(t[2], t[3]), pack2(t[4], t[5]), pack2(t[6], t[7])};
      }
      if (NRM & 1) asm volatile("" : "+v"(y[j]));
      else st(j);
      __builtin_amdgcn_sched_barrier(0);
    }
    MK(29);
    if (NRM & 1) {
#pragma unroll
      for (int j = 0; j < 9; ++j) st(j);
    }
  }
  lds_inc(lw + LW_AD * 4, me);
  MK(24);
  __builtin_amdgcn_s_setprio(G0P);
  lds_spin4(lw + LW_S * 4, 4u + (unsigned)(wm >> 1));
  {
    const unsigned fa = lds0 + (unsigned)((wm >> 1) * SB + K3C::XB + (wm & 1) * 9216) + lane16;
#pragma unroll
    for (int j = 0; j < 9; ++j) {
      asm volatile("ds_write_b128 %0, %1 offset:%2" ::"v"(fa), "v"(wb[j]), "n"(j * 1024) : "memory");
      if (PACE && j < 8) __builtin_amdgcn_s_sleep(PACE);
    }
    lds_inc(lw + LW_AD * 4, me);
  }
  announce(lw + LW_Z * 4, 4u, ws, CN_Z + layer, lane * 16 + ni, lane < 16);
  lds_inc(lw + LW_AD * 4, me);
  MK(27);
  f32x4 acc[2][9];
  {
    float bs[2];
    asm volatile("ds_read2_b32 %0, %1 offset1:16\n\ts_waitcnt lgkmcnt(0)"
                 : "=v"(*reinterpret_cast<f32x2*>(bs))
                 : "v"(lds0 + (unsigned)(K3_BS_OFF + (wm * 32 + r) * 4))
                 : "memory");
    lds_spin4(lw + LW_S * 4, 6u + (unsigned)(wm >> 1));
    const unsigned fa = lds0 + (unsigned)((2 + (wm >> 1)) * SB + K3C::XB + (wm & 1) * 9216) + lane16;
#pragma unroll
    for (int j = 0; j < 9; ++j) {
      asm volatile("ds_write_b128 %0, %1 offset:%2" ::"v"(fa), "v"(y[j]), "n"(j * 1024) : "memory");
      if (PACE && j < 8) __builtin_amdgcn_s_sleep(PACE);
    }
    lds_inc(lw + LW_AD * 4, me);
#pragma unroll
    for (int mt = 0; mt < 2; ++mt)
#pragma unroll
      for (int j = 0; j < 9; ++j) acc[mt][j] = f32x4{bs[mt], bs[mt], bs[mt], bs[mt]};
  }
  u32x4 z1[9];
  {
    GemmMath<K3C, 2, 9, K3_D, 6, false, 2> gm(acc);
    gm.setup(lds0, wm * 2, 0, lane, (unsigned)(2 * SB));
    MK(15);
    lds_spin4(lw + LW_AD * 4, 6u);
    lds_spin4(lw + LW_L * 4, 8u);
    MK(17);
    gm.first();
    __builtin_amdgcn_s_setprio(0);
    gm.template mid<1, false>();
    gm.template mid<11>();
    const unsigned za = gm.wrd + (unsigned)(K3_Z1_OFF - K3C::XB + wm * 5120);
#pragma unroll
    for (int j = 0; j < 5; ++j) asm volatile("ds_read_b128 %0, %1 offset:%2" : "=v"(z1[j]) : "v"(za), "n"(j * 1024));
    gm.template mid<10>();
#pragma unroll
    for (int j = 5; j < 9; ++j) asm volatile("ds_read_b128 %0, %1 offset:%2" : "=v"(z1[j]) : "v"(za), "n"((j - 5) * 1024));
    gm.template mid<9>();
    gm.last();
  }
  MK(16);
  const char* hp = uni_ptr(ws + OFF_H + (size_t)layer * SZ_Z + (size_t)((mi * 2 + (wm >> 1)) * 288 + ni * 18) * 1024);
  const int l2 = lane_id();
  const unsigned ho = (unsigned)((l2 >> 5) * 1024 + ((wm & 1) * 32 + (l2 & 31)) * 16);
#pragma unroll
  for (int j = 0; j < 9; ++j) {
    asm volatile("" : "+v"(z1[j]));
    u32x2 h[2];
#pragma unroll
    for (int mt = 0; mt < 2; ++mt) {
      const f32x4 a = acc[mt][j];
      h[mt] = u32x2{pack2v(f32x2{bf_lo(z1[j][2 * mt]), bf_hi(z1[j][2 * mt])} * f32x2{a[0], a[1]}),
                    pack2v(f32x2{bf_lo(z1[j][2 * mt + 1]), bf_hi(z1[j][2 * mt + 1])} * f32x2{a[2], a[3]})};
    }
    const auto s0 = __builtin_amdgcn_permlane16_swap(h[0][0], h[1][0], false, false);
    const auto s1 = __builtin_amdgcn_permlane16_swap(h[0][1], h[1][1], false, false);
    const u32x4 v = {s0[0], s1[0], s0[1], s1[1]};
    asm volatile("global_store_dwordx4 %0, %1, %2 offset:%3 sc1\n\ts_nop 1" ::"v"(ho), "v"(v), "s"(hp + (j / 2) * 4096), "n"((j % 2) * 2048) : "memory");
    __builtin_amdgcn_sched_barrier(0);
  }
  MK(18);
  announce(lds0 + (unsigned)(LW_OFF + (LW_H + layer * 2 + (wm >> 1)) * 4), 2u, ws, CN_H + layer, (mi * 2 + (wm >> 1)) * 8 + l2, l2 < 8);
  MK(19);
}

DEVI void p3_g1(char* smem, char* __restrict__ ws, const char* code0, int bid, int layer, int mi, int wm, int lane MK_ARG) {
  const unsigned lds0 = (unsigned)(size_t)smem;
  const int g = lane >> 4, r = lane & 15;
  const unsigned lw = lds0 + (unsigned)(LW_OFF + layer * 4);
  const unsigned xrd = lds0 + (unsigned)(g * 2048 + (wm * 32 + r) * 16);
  s16x8 xc[4][2][2];
  {
    const char* xs = uni_ptr(ws + OFF_XS + (size_t)layer * 2 * SZ_XS + (size_t)(mi * 2 + (wm >> 1)) * 4096 + (wm & 1) * 256);
    f32x4 a, b;
    auto pick = [&](int t) {
      bar();
      asm volatile(
          "ds_read_b128 %0, %4\n\tds_read_b128 %1, %4 offset:256\n\tds_read_b128 %2, %4 offset:8192\n\tds_read_b128 %3, %4 offset:8448\n\t"
          "s_waitcnt lgkmcnt(0)"
          : "=&v"(xc[t][0][0]), "=&v"(xc[t][0][1]), "=&v"(xc[t][1][0]), "=&v"(xc[t][1][1])
          : "v"(xrd + (unsigned)(t * K3C::SB))
          : "memory");
    };
#define G1P 2
    __builtin_amdgcn_s_setprio(G1P);
    pick(0);
    pick(1);
    asm volatile("global_load_dwordx4 %0, %2, %3 sc1\n\tglobal_load_dwordx4 %1, %2, %3 offset:512 sc1"
                 : "=&v"(a), "=&v"(b)
                 : "v"((unsigned)(g * 1024 + r * 16)), "s"(xs)
                 : "memory");
    pick(2);
    pick(3);
    bar();
    asm volatile("s_waitcnt vmcnt(0)" : "+v"(a), "+v"(b) : : "memory");
    float s, q, mb, rinv;
    red8(a, b, s, q);
    bar();
    asm volatile("" : "+v"(s), "+v"(q));
    ln_finish(s, q, 1.0f / (float)D_, mb, rinv);
    asm volatile("" : "+v"(mb), "+v"(rinv));
    bar();
    asm volatile("" : "+v"(mb), "+v"(rinv));
    const float k0 = -2.0f * 0.7978845608028654f * 1.4426950408889634f, k1 = k0 * 0.044715f;
    float sd = __builtin_amdgcn_rcpf(rinv), c0 = k0 * rinv, c1 = k1 * rinv * rinv * rinv;
    asm volatile("" : "+v"(sd));
    asm volatile("" : "+v"(c0));
    asm volatile("" : "+v"(c1));
    const float e0 = lane < 32 ? -mb : c0, e1 = lane < 32 ? sd : c1;
    asm volatile("ds_write2st64_b32 %0, %1, %2 offset1:2" ::"v"(
                     lds0 + (unsigned)(K3_ST_OFF + (g >> 1) * 1024 + (wm * 32 + 2 * r + (g & 1)) * 4)),
                 "v"(e0), "v"(e1)
                 : "memory");
    bar();
    asm volatile("s_waitcnt lgkmcnt(0)" ::: "memory");
    bar();
    __builtin_amdgcn_s_setprio(0);
  }
  f32x4 ac[2][9];
#pragma unroll
  for (int mt = 0; mt < 2; ++mt)
#pragma unroll
    for (int j = 0; j < 9; ++j) ac[mt][j] = f32x4{0.f, 0.f, 0.f, 0.f};
  {
    UMath<true> gm(ac, xc);
    gm.xa = xrd;
    gm.wa = lds0 + (unsigned)(K3C::XB + (g * 8 + (r & 7)) * K3C::P + (r >> 3) * 16);
    gm.fa = lw + (unsigned)(LW_AD * 4 + (lane & 3));
    gm.fw = lw + (unsigned)(LW_S * 4 + wm);
    gm.fv = 0u;
    asm volatile("" : "+v"(gm.fa), "+v"(gm.fw), "+v"(gm.fv), "+v"(gm.xa), "+v"(gm.wa));
    lds_spin4<4>(lw + LW_L * 4, 4u);
    gm.first();
    lds_spin4(lw + LW_AD * 4, 2u);
#define BPR 1
    __builtin_amdgcn_s_setprio(BPR);
    gm.run();
    __builtin_amdgcn_s_setprio(0);
  }
  MK(14);
  f32x2 rc[4];
  f32x4 cs[9];
  const int l2 = lane_id();
  {
    const unsigned ra = lds0 + (unsigned)(K3_ST_OFF + (wm * 32 + (l2 & 15)) * 4), ca = lds0 + (unsigned)(K3_C1_OFF + (l2 & 48));
#pragma unroll
    for (int k = 0; k < 4; ++k) asm volatile("ds_read2_b32 %0, %1 offset1:16" : "=v"(rc[k]) : "v"(ra + (unsigned)(k * 512)) : "memory");
#pragma unroll
    for (int j = 0; j < 9; ++j) asm volatile("ds_read_b128 %0, %1 offset:%2" : "=v"(cs[j]) : "v"(ca), "n"(j * 64) : "memory");
    asm volatile("s_waitcnt lgkmcnt(0)" : "+v"(rc[0]), "+v"(rc[1]), "+v"(rc[2]), "+v"(rc[3]));
#pragma unroll
    for (int j = 0; j < 9; ++j) asm volatile("" : "+v"(cs[j]));
  }
  const unsigned za = lds0 + (unsigned)(K3_Z1_OFF + wm * 5120 + l2 * 16);
  u32x4 z1[9];
#pragma unroll
  for (int t = 0; t < 18; ++t) {
    const int j = t / 2, mt = t % 2;
    float u[4], w[4];
#pragma unroll
    for (int i = 0; i < 4; ++i) asm volatile("v_fma_f32 %0, %1, %2, %3" : "=v"(u[i]) : "v"(rc[0][mt]), "v"(cs[j][i]), "v"(ac[mt][j][i]));
#pragma unroll
    for (int i = 0; i < 4; ++i) asm volatile("v_mul_f32 %0, %1, %1" : "=v"(w[i]) : "v"(u[i]));
#pragma unroll
    for (int i = 0; i < 4; ++i) asm volatile("v_fma_f32 %0, %0, %1, %2" : "+v"(w[i]) : "v"(rc[3][mt]), "v"(rc[2][mt]));
#pragma unroll
    for (int i = 0; i < 4; ++i) asm volatile("v_mul_f32 %0, %0, %1" : "+v"(w[i]) : "v"(u[i]));
#pragma unroll
    for (int i = 0; i < 4; ++i) asm volatile("v_exp_f32 %0, %0" : "+v"(w[i]));
#pragma unroll
    for (int i = 0; i < 4; ++i) asm volatile("v_fma_f32 %0, %0, %1, %1" : "+v"(w[i]) : "v"(rc[1][mt]));
#pragma unroll
    for (int i = 0; i < 4; ++i) asm volatile("v_rcp_f32 %0, %0" : "+v"(w[i]));
#pragma unroll
    for (int i = 0; i < 4; ++i) asm volatile("v_mul_f32 %0, %0, %1" : "+v"(w[i]) : "v"(u[i]));
    z1[j][2 * mt] = pack2(w[0], w[1]);
    z1[j][2 * mt + 1] = pack2(w[2], w[3]);
    if (mt && j < 5) asm volatile("ds_write_b128 %0, %1 offset:%2" ::"v"(za), "v"(z1[j]), "n"(j * 1024) : "memory");
    if (t == 17) {
#pragma unroll
      for (int k = 5; k < 9; ++k) asm volatile("ds_write_b128 %0, %1 offset:%2" ::"v"(za), "v"(z1[k]), "n"((k - 5) * 1024) : "memory");
    }
    if (t == 9 || t == 17) asm volatile("s_waitcnt lgkmcnt(0)" ::: "memory");
    __builtin_amdgcn_sched_barrier(0);
    bar();
    __builtin_amdgcn_sched_barrier(0);
  }
  MK(15);
#pragma unroll 1
  for (int t = 20; t < 32; ++t) bar();
  if ((RTC & 2) && wm == 2) code_again(code0, bid);
}

DEVI void p3_math(char* smem, char* __restrict__ ws, const char* code0, int bid, int layer, float cv, int mi, int ni, int wave, int tid MK_ARG) {
  if (tid < 144) *reinterpret_cast<f32x2*>(smem + K3_C2_OFF + tid * 8) = f32x2{cv, cv};
  else if (tid < 288 || tid >= 384) *reinterpret_cast<float*>(smem + (tid < 288 ? K3_C1_OFF - 576 : K3_BS_OFF - 1536) + tid * 4) = cv;
  if (MPOLL && wave == 5) cnt_wait(ws, CN_X1 + layer, mi * 16 + ni, 16u);
  bar();
  MK(10);
  asm volatile("s_waitcnt lgkmcnt(0)" ::: "memory");
  if (wave >> 2) p3_g1(smem, ws, code0, bid, layer, mi, wave & 3, lane_id() MK_PASS);
  else p3_g0(smem, ws, layer, mi, ni, wave, lane_id() MK_PASS);
  bar();
}

constexpr int W5A = 22, W5B = 26;
constexpr int NPF = 30;
struct Loader {
  u32x4 pf[NPF];
  unsigned lds0, lane16;
  int wl, lane;

  template <int R0, int N, int P0, int PE>
  DEVI void pf_load(const char* src) {
    const unsigned o = lane16 + (unsigned)(wl * 1024);
#pragma unroll
    for (int t = 0; t < N; ++t) {
      unsigned a = o + (unsigned)((P0 + 4 * t) * 1024);
      if (P0 + 4 * t + 3 >= PE && P0 + 4 * t + wl >= PE) a = lane16;
      asm volatile("global_load_dwordx4 %0, %1, %2 sc1" : "=v"(pf[R0 + t]) : "v"(a), "s"(src) : "memory");
    }
  }
  template <int R0, int N>
  DEVI void pf_pin() {
#pragma unroll
    for (int t = 0; t < N; ++t) asm volatile("" : "+v"(pf[R0 + t]));
  }
  template <int R0, int N>
  DEVI void pf_undef() {
#pragma unroll
    for (int t = 0; t < N; ++t) asm volatile("" : "=v"(pf[R0 + t]));
  }
  template <int PB, int A, int N, int SL = 0>
  DEVI void pf_put(unsigned dst) {
    constexpr int T0 = (A - PB) / 4, T1 = (A + N - 1 - PB) / 4;
    const unsigned a = lds0 + dst + lane16 + (unsigned)((wl + PB + 4 * T0 - A + 4) * 1024) - 4096u;
#pragma unroll
    for (int t = T0; t <= T1; ++t) {
      const int p0 = PB + 4 * t;
      if ((p0 >= A || p0 + wl >= A) && (p0 + 3 < A + N || p0 + wl < A + N))
        asm volatile("ds_write_b128 %0, %1 offset:%2" ::"v"(a), "v"(pf[t]), "n"((t - T0) * 4096) : "memory");
      if (SL) __builtin_amdgcn_s_sleep(SL);
    }
  }
  template <int N>
  DEVI void dma_run(const char* src, unsigned dst) {
    const unsigned o = lane16 + (unsigned)(wl * 1024), m = lds0 + dst + (unsigned)(wl * 1024);
#pragma unroll
    for (int i = 0; i < (N + 3) / 4; ++i)
      if (4 * i + 3 < N || 4 * i + wl < N) dma16_sc1(src + i * 4096, o, m + (unsigned)(i * 4096));
  }
  template <int V>
  DEVI void wbar() {
    asm volatile("s_waitcnt vmcnt(%0) lgkmcnt(0)" ::"n"(V) : "memory");
    bar();
  }

  DEVI void p1(char* smem, char* ws, int rt, int hp MK_ARG) {
    constexpr int SB = K1C::SB;
    static_assert(K1C::XB == 18 * 1024 && K1C::WB == 14 * 1024, "K1 shape");
    const char* wp = uni_ptr(ws + OFF_PQ + (size_t)hp * (9 * K1C::WB) + 4 * K1C::WB);
    static_assert(3 * SB + K1C::XB >= P5_W + 5 * 18432, "free while the GEMM before ends");
    const char* wq = wp - 4 * K1C::WB;
    dma_run<14>(wq + 3 * K1C::WB, (unsigned)(3 * SB + K1C::XB));
    dma_run<28>(wp, (unsigned)K1_WX);
    if (P1PR & 1) __builtin_amdgcn_s_setprio(0);
    if (P1PR & 2) wait_vm<0>();
    const bool lo = wl < 2;
    const int x0 = lo ? wl * 5 : wl * 4 + 2, w0 = lo ? wl * 3 : wl * 4 - 2;
    unsigned xo[5];
#pragma unroll
    for (int i = 0; i < 5; ++i) {
      const int c = (x0 + i) * 64 + lane;
      const int ch = c / 144;
      int gr = rt * 128 - 16 + (c - ch * 144);
      if (gr < 0) gr = 0;
      xo[i] = (unsigned)(((gr >> 6) * 72 + ch) * 1024 + (gr & 63) * 16);
      asm volatile("" : "+v"(xo[i]));
    }
    const char* xp = uni_ptr(ws + OFF_X1 + SZ_X);
    wp += 2 * K1C::WB + w0 * 1024;
    auto dma_x = [&](unsigned so) {
      const unsigned m = lds0 + so + (unsigned)(x0 * 1024);
#pragma unroll
      for (int i = 0; i < 4; ++i) dma16_sc1(xp, xo[i], m + (unsigned)(i * 1024));
      if (lo) dma16_sc1(xp, xo[4], m + 4096u);
      xp += 8192;
    };
    auto dma_w = [&](unsigned so) {
      const unsigned m = lds0 + so + (unsigned)(K1C::XB + w0 * 1024);
#pragma unroll
      for (int i = 0; i < 3; ++i) dma16_sc1(wp + i * 1024, lane16, m + (unsigned)(i * 1024));
      if (!lo) dma16_sc1(wp + 3072, lane16, m + 3072u);
      wp += K1C::WB;
    };
    const char* xs = uni_ptr(ws + OFF_XS + SZ_XS);
    const int g = lane >> 4, p2 = 64 + (lane & 7);
    int ra = rt * 128 - 16 + wl * 32 + 2 * (lane & 15);
    if (ra < 0) ra = 0;
    unsigned sa = (unsigned)(((ra >> 6) * 8 + g * 2) * 512 + (ra & 63) * 8);
    unsigned sb = (unsigned)(((rt * 2 + 1) * 8 + (lane >> 3)) * 512 + (2 * p2 - 80) * 8);
    unsigned sw = lds0 + (unsigned)(K1_ST_OFF + (2 * (lane < 32 ? wl * 16 + (lane & 15) : p2) + (g & 1)) * 8);
    asm volatile("" : "+v"(sa), "+v"(sb), "+v"(sw));
    MK(22);
    __builtin_amdgcn_s_setprio(3);
    bar();
    dma_run<14>(wq, (unsigned)K1C::XB);
    dma_run<14>(wq + K1C::WB, (unsigned)(SB + K1C::XB));
    dma_run<14>(wq + 2 * K1C::WB, (unsigned)(2 * SB + K1C::XB));
    MK(1);
    bar();
    dma_x(0u);
    dma_x((unsigned)SB);
    wbar<4>();
    MK(29);
    dma_x((unsigned)(2 * SB));
    dma_x((unsigned)(3 * SB));
    wbar<8>();
    MX(1, 3);
    dma_x(0u);
    wbar<8>();
    MX(1, 4);
    dma_x((unsigned)SB);
    f32x4 a, b, e;
    asm volatile("global_load_dwordx4 %0, %3, %5 sc1\n\tglobal_load_dwordx4 %1, %3, %5 offset:512 sc1\n\tglobal_load_dwordx4 %2, %4, %5 sc1"
                 : "=&v"(a), "=&v"(b), "=&v"(e)
                 : "v"(sa), "v"(sb), "s"(xs)
                 : "memory");
    wbar<11>();
    MX(1, 5);
    dma_w((unsigned)SB);
    dma_x((unsigned)(2 * SB));
    wbar<15>();
    MX(1, 6);
    dma_w((unsigned)(2 * SB));
    dma_x((unsigned)(3 * SB));
    wbar<19>();
    MX(1, 7);
    dma_w((unsigned)(3 * SB));
    dma_x(0u);
    wbar<16>();
    MX(1, 8);
    asm volatile("" : "+v"(a), "+v"(b), "+v"(e));
    float s, q;
    {
      float t[4] = {e[0], e[1], e[2], e[3]}, s2, q2;
      asm volatile(
          "v_add_f32_dpp %0, %0, %0 row_ror:8 row_mask:0xf bank_mask:0xf\n\tv_add_f32_dpp %1, %1, %1 row_ror:8 row_mask:0xf bank_mask:0xf\n\t"
          "v_add_f32_dpp %2, %2, %2 row_ror:8 row_mask:0xf bank_mask:0xf\n\tv_add_f32_dpp %3, %3, %3 row_ror:8 row_mask:0xf bank_mask:0xf\n\ts_nop 1"
          : "+v"(t[0]), "+v"(t[1]), "+v"(t[2]), "+v"(t[3]));
      red4(t, s2, q2);
      red8(a, b, s, q);
      s = lane < 32 ? s : s2;
      q = lane < 32 ? q : q2;
      asm volatile("" : "+v"(s), "+v"(q));
    }
    wbar<8>();
    MX(1, 9);
    {
      asm volatile("" : "+v"(s), "+v"(q));
      float mb, rinv;
      ln_finish(s, q, 1.0f / (float)D_, mb, rinv);
      asm volatile("ds_write_b64 %0, %1" ::"v"(sw), "v"(f32x2{-mb, rinv}) : "memory");
    }
    wbar<0>();
    MK(30);
  }

  template <int I>
  DEVI void p5_step(const char* wp) {
    constexpr int T = 2 * I + 1;
    wbar<((T >= 3 && T <= 11) ? 3 : 0)>();
    if constexpr (T <= 9) {
      if (!G1W) {
        const unsigned wo = (unsigned)(12288 + (wl & 1) * 3072), so = (unsigned)(P5_W + ((T + 2 + (wl >> 1)) & 7) * 18432);
#pragma unroll
        for (int k = 0; k < 3; ++k) dma16_sc1(wp + (T + 7) * 18432 + k * 1024, lane16 + wo + (unsigned)((wl >> 1) * 18432), lds0 + so + wo + (unsigned)(k * 1024));
      }
      pf_put<0, 12 * (T - 1), 12, P5PACE>((unsigned)(P5_W + ((T + 2) % 8) * 18432));
      pf_put<0, 12 * T, 12, P5PACE>((unsigned)(P5_W + ((T + 3) % 8) * 18432));
    }
  }
  template <int... S>
  DEVI void p5_steps(const char* wp, std::integer_sequence<int, S...>) {
    (p5_step<S>(wp), ...);
  }

  template <int T0, int T1>
  DEVI void w5_load(const char* w5p) {
    const unsigned o = lane16 + (unsigned)(wl * 1024);
#pragma unroll
    for (int t = T0; t < T1; ++t)
      asm volatile("global_load_dwordx4 %0, %1, %2 sc1" : "=v"(pf[t]) : "v"(o + (unsigned)((t / 3 + 8) * 18432 + (t % 3) * 4096)), "s"(w5p) : "memory");
  }
  DEVI void p35(char* smem, char* ws, const u16* w_sp, int layer, int bid, int mi, int ni, int rb, int ct, int hp MK_ARG) {
    using C = K3C;
    constexpr int NS = C::NS, SB = C::SB, NDX = 4, NDW = 5, VPS = NDX + NDW;
    const char* wip = uni_ptr(ws + OFF_PI + (size_t)ni * (18 * C::WB));
    if (layer || L0REG) {
      pf_put<-48, 36, 18>((unsigned)(2 * SB + C::XB));
      pf_put<-48, 54, 18>((unsigned)(3 * SB + C::XB));
    } else {
      dma_run<18>(wip + 2 * C::WB, (unsigned)(2 * SB + C::XB));
      dma_run<18>(wip + 3 * C::WB, (unsigned)(3 * SB + C::XB));
    }
    const char* w5p = uni_ptr(ws + OFF_PW + (size_t)ct * (36 * 9216));
    const unsigned m0x = lds0 + (unsigned)(wl * 4096), m0w = lds0 + (unsigned)(C::XB + wl * 4608);
    const unsigned wo = lane16 + (unsigned)(wl * 4608);
    const unsigned lw = lds0 + (unsigned)(LW_OFF + layer * 4), me = 1u << (8 * wl);
    auto dma_w = [&](const char* wp, unsigned so) {
#pragma unroll
      for (int i = 0; i < NDW; ++i) {
        if (i < NDW - 1 || lane < 32) dma16_sc1(wp, wo + (unsigned)(i * 1024), m0w + so + (unsigned)(i * 1024));
      }
    };
    {
      const char* xp = uni_ptr(ws + OFF_X1 + (size_t)layer * 2 * SZ_X + (size_t)(mi * 2 * 72 + wl * 2) * 1024);
      const unsigned xo1 = lane16 + 72u * 1024u;
      auto dma_x = [&](int k, unsigned so) {
        const char* p = xp + k * 8192;
        dma16_sc1(p, lane16, m0x + so);
        dma16_sc1(p, xo1, m0x + so + 1024u);
        dma16_sc1(p + 1024, lane16, m0x + so + 2048u);
        dma16_sc1(p + 1024, xo1, m0x + so + 3072u);
      };
      if (!MPOLL && wl == 0) cnt_wait(ws, CN_X1 + layer, mi * 16 + ni, 16u);
      MK(10);
      bar();
      dma_x(0, 0u);
      dma_x(1, (unsigned)SB);
      wait_vm<4>(); MX(3, 2); wbar<4>(); MX(2, 2);
      MK(25);
      dma_x(2, (unsigned)(2 * SB));
      dma_x(3, (unsigned)(3 * SB));
      wait_vm<8>(); MX(3, 3); wbar<8>(); MX(2, 3);
      dma_x(4, (unsigned)(4 * SB));
      wait_vm<8>();
#pragma unroll 1
      for (int t = 0; t < 4; ++t) {
        MX(3, 4 + t); bar(); MX(2, 4 + t);
        dma_w(wip + (t + 4) * C::WB, (unsigned)(t * SB));
        dma_x(t + 5, (unsigned)(t * SB));
        wait_vm<NDX + VPS>();
      }
      MX(3, 8); bar(); MX(2, 8);
      dma_w(wip + 8 * C::WB, 0u);
      wait_vm<VPS>();
      MX(3, 9); bar(); MX(2, 9);
      const char* wp = wip + 9 * C::WB;
      dma_w(wp + b_k(0) * C::WB, (unsigned)SB);
      if (layer == 0) {
        const char* sp = uni_ptr((const char*)w_sp + (size_t)(bid * 4 + wl) * 8192);
        const unsigned so = (unsigned)((lane >> 5) * 4096 + (lane & 31) * 16);
#pragma unroll
        for (int i = 0; i < WSP_V; ++i) dma16(sp + i * 512, so, lds0 + (unsigned)K3_DM_OFF);
        dma16(sp + (bid >> 4) * 256, so & ~0x100u, lds0 + (unsigned)K3_DM_OFF);
      }
      w5_load<0, 4>(w5p);
      wait_vm<NDW + 4>();
      MX(3, 10); bar(); MX(2, 10);
      dma_w(wp + b_k(1) * C::WB, (unsigned)(2 * SB));
      w5_load<4, 8>(w5p);
      MK(26);
      lds_spin4(lw + LW_AD * 4, 1u);
      dma_w(wp + b_k(2) * C::WB, (unsigned)(3 * SB));
      dma_w(wp + b_k(3) * C::WB, 0u);
#define TCH 0
      if (TCH & 1) {
        const int li = wl * 54 + (lane < 54 ? lane : 53);
        touch64(wp + C::WB, (unsigned)((mi & 7) * 13824 + li * 64), lds0 + (unsigned)K3_DM_OFF);
      }
      w5_load<8, W5A>(w5p);
      wait_vm<W5A - 8>();
      lds_inc(lw + LW_L * 4, 4u * me);
      w5_load<W5A, 30>(w5p);
      MK(11);
#define LSL 2
      static_for<4, 7>([&](auto T_) {
        constexpr int t = T_;
        lds_spin4<LSL>(lw + LW_S * 4, (unsigned)(t - 3));
        MX(2, 12 + t);
        dma_w(wp + b_k(t) * C::WB, (unsigned)(((t + 1) % 4) * SB));
        if constexpr (t > 4) {
          wait_vm<NDW>();
          lds_inc(lw + LW_L * 4, me);
        }
      });
      MX(2, 19);
      wait_vm<0>();
      lds_inc(lw + LW_L * 4, me);
      pf_pin<0, 30>();
      MK(13);
    }
    {
      unsigned xoff[NDX];
#pragma unroll
      for (int i = 0; i < NDX; ++i) {
        const int c = wl * 256 + i * 64 + lane;
        const int row = c >> 3, cc = c & 7;
        xoff[i] = (unsigned)((mi * 128 + row) * S_ * 2) + (unsigned)((cc ^ ((row >> 1) & 7)) * 16);
      }
      auto dma_x = [&](const char* xp, unsigned so) {
#pragma unroll
        for (int i = 0; i < NDX; ++i) dma16(xp, xoff[i], m0x + so + (unsigned)(i * 1024));
      };
      const char* xp = uni_ptr((const char*)w_sp);
      const char* wp = uni_ptr(ws + OFF_Z2 + (size_t)layer * SZ_Z + (size_t)ni * S_ * 288);
#define XEARLY 0
      if (!XEARLY) lds_spin4<LSL>(lw + LW_AD * 4, 5u);
      dma_x(xp + mi * 256, (unsigned)(2 * SB));
      dma_x(xp + mi * 256 + 128, (unsigned)(3 * SB));
      if (XEARLY) lds_spin4<LSL>(lw + LW_AD * 4, 5u);
      MK(24);
      int left = 2 * mi;
      auto next = [&]() {
        const int d = --left == 0 ? 3 : 1;
        xp += d * 128;
        wp += d * C::WB;
      };
      if (mi == 0) {
        xp += 256;
        wp += 2 * C::WB;
      }
      dma_x(xp, 0u);
      dma_x(xp + 128, (unsigned)SB);
      wait_vm<2 * NDX>();
      lds_inc(lw + LW_L * 4, me);
      MK(27);
      if (wl == 0) {
        cnt_wait(ws, CN_Z + layer, mi * 16 + ni, 16u);
        lds_wr32(lw + LW_GO * 4, 1u);
      } else {
        lds_spin(lw + LW_GO * 4, 1u);
      }
      MK(14);
      lds_spin4(lw + LW_S * 4, 8u);
      dma_w(wp, 0u);
      lds_spin4(lw + LW_S * 4, 9u);
      dma_w(wp + C::WB, (unsigned)SB);
      next();
      next();
      unsigned slot = (unsigned)(2 * SB);
      auto cissue = [&]() {
        dma_x(xp, slot);
        dma_w(wp, slot);
        next();
        slot = (slot == (unsigned)((NS - 1) * SB)) ? 0u : slot + (unsigned)SB;
      };
      auto cstep = [&]() {
        bar();
        cissue();
      };
      wait_vm<NDW>();
      MK(15);
      int j = 2;
      if (layer == 0) {
        const char* sp = uni_ptr((const char*)w_sp + (size_t)(bid * 4 + wl) * 8192 + WSP_V * 512);
        unsigned d = 0u;
#pragma unroll 1
        for (; j < 2 + 32 - 4 * WSP_V; ++j) {
          cstep();
          asm volatile("s_load_dword %0, %1, 0\n\ts_load_dword %0, %1, 64\n\ts_load_dword %0, %1, 0x1000\n\ts_load_dword %0, %1, 0x1040"
                       : "+s"(d)
                       : "s"(sp));
          sp += 128;
          wait_vm<VPS>();
        }
#pragma unroll 1
        for (int e = j + 4; j < e; ++j) {
          cstep();
          asm volatile("" : "+s"(d));
          wait_vm<VPS>();
        }
        asm volatile("s_waitcnt lgkmcnt(0)" : "+s"(d));
      }
#pragma unroll 1
      for (; j <= 29; ++j) {
        cstep();
        wait_vm<VPS>();
      }
      bar();
    }
    auto w5piece = [&](unsigned off) {
      dma16_sc1(w5p, lane16 + (off >= 54u * 1024u ? off - 54u * 1024u : off + 90u * 1024u), lds0 + (unsigned)P5_W + off);
    };
#pragma unroll
    for (int i = 0; i < 15; ++i) {
      const int p = 4 * i + wl;
      if (i < 14 || wl == 0) w5piece((unsigned)((p < 34 ? 53 + p : 121 - 34 + p) * 1024));
    }
    wait_vm<14>();
    bar();
#pragma unroll
    for (int i = 0; i < 9; ++i)
      if (i < 8 || wl < 2) w5piece((unsigned)((87 + 4 * i + wl) * 1024));
    wait_vm<0>();
    bar();
    if (wl) {
#pragma unroll
      for (int i = 0; i < 18; ++i)
        if (i < 17 || wl < 3) w5piece((unsigned)((3 * i + wl - 1) * 1024));
    } else {
      cnt_wait(ws, CN_H + layer, rb * 8 + ct, 16u);
    }
    MK(20);
    bar();
    p5_steps(w5p, std::make_integer_sequence<int, 7>{});
    if (layer == 0) {
      if (bid < 24 && wl == 0 && lane < 8) pk_st16(ws + OFF_HELLO + bid * 128 + lane16, u32x4{0u, 0u, 0u, 0u});
    }
  }
};

__global__ __launch_bounds__(768, 3) void k_main(const u16* __restrict__ x0, const u16* __restrict__ wqkv, const u16* __restrict__ wo,
                                                 const u16* __restrict__ w_in, const u16* __restrict__ w_sp,
                                                 const u16* __restrict__ b_sp, const u16* __restrict__ w_out,
                                                 u16* __restrict__ out, char* ws) {
  extern __shared__ __attribute__((aligned(16))) char smem[];
  const char* code0 = code_here();
  (void)code0;
  const unsigned lds0 = (unsigned)(size_t)smem;
  int wave = __builtin_amdgcn_readfirstlane((int)threadIdx.x >> 6);
  int bid = blockIdx.x;
#define FRESH() asm volatile("" : "+s"(bid), "+s"(ws), "+s"(wave))
#define XCD (bid & 7)
#define IDX (bid >> 3)
#define MI ((XCD >> 2) * 8 + (IDX >> 2))
#define NI ((XCD & 3) * 4 + (IDX & 3))
#define RB (XCD * 4 + (IDX >> 3))
#define CT (IDX & 7)
#define RT ((XCD >> 1) * 4 + (IDX >> 3))
#define HP ((XCD & 1) * 8 + (IDX & 7))
  MK(0);
  if (wave >= 3 && wave < 8) {
    constexpr int ST = 64;
    const int k = (7 - wave) * 64 + lane_id();
    const bool isx = k < 9216 / ST;
    int o = isx ? bid * 9216 + k * ST : bid * 7776 + (k - 9216 / ST) * ST;
    if (!isx && k - 9216 / ST > 7775 / ST) o = bid * 7776;
    const char* p = (isx ? (const char*)x0 : (const char*)wqkv) + o;
    asm volatile("s_mov_b32 m0, %1\n\ts_nop 0\n\tglobal_load_lds_dword %0, off" ::"v"(p), "s"(lds0 + (unsigned)K1R_DUMMY) : "memory");
  }

  if (__builtin_expect(wave >= 8, 1)) {
    __builtin_amdgcn_s_setprio(3);
    {
      const int lane = lane_id();
      RawLd rl;
      const bool cross = (((HP * 72) & ~15) & 127) > 48;
      const int c16 = IDX >> 1;
      const int job = cross ? (c16 < 11 ? 128 + XCD * 11 + c16 : 999) : XCD * 16 + c16;
      const int pj = cross ? (c16 >= 11 ? XCD * 5 + c16 - 11 : -1) : (XCD < 4 && (c16 & 7) == 0 ? 40 + XCD * 2 + (c16 >> 3) : -1);
      rl.front(x0, wqkv, wo, ws, bid, lds0, wave - 8, lane, RT, HP, pj MF_PASS);
      if (job < 216) k1_raw_loader<true>(rl, w_in, w_out, ws, rl.tag, bid, lds0, wave - 8, lane, RT, HP, CT, job, pj MF_PASS);
      else k1_raw_loader<false>(rl, w_in, w_out, ws, rl.tag, bid, lds0, wave - 8, lane, RT, HP, CT, job, pj MF_PASS);
    }
    Loader ld;
    ld.lds0 = lds0;
#pragma unroll 1
    for (int layer = 0; layer < 2; ++layer) {
      FRESH();
      ld.wl = wave - 8;
      ld.lane = lane_id();
      ld.lane16 = (unsigned)ld.lane * 16u;
      const char* wip = uni_ptr(ws + OFF_PI + (size_t)NI * (18 * K3C::WB));
      if (layer) {
        ld.p1(smem, ws, RT, HP MK_PASS);
        ld.pf_load<0, 21, 0, 81>(uni_ptr(ws + OFF_PO + (size_t)CT * (18 * 4608)));
        ld.pf_load<21, 9, 36, 72>(wip);
        wait_vm<0>();
        ld.pf_pin<0, 30>();
        bar();
        ld.pf_put<0, 0, 40>((unsigned)P2_W);
        ld.pf_put<0, 40, 41>((unsigned)(P2_W + 40 * 1024));
      } else {
        ld.pf_undef<21, 9>();
        MK(3);
        MK(4);
        bar();
        const char* wp = uni_ptr(ws + OFF_PO + (size_t)CT * (18 * 4608) + ld.wl * 1024);
#pragma unroll
        for (int i = 0; i < 21; ++i)
          if (i < 20 || ld.wl == 0) dma16_sc1(wp + i * 4096, ld.lane16, lds0 + (unsigned)(P2_W + i * 4096 + ld.wl * 1024));
      }
      if (layer == 0 && ld.wl == 0) cnt_wait(ws, CN_O + layer, RB * 8 + CT, 16u);
      MK(6);
      asm volatile("s_waitcnt vmcnt(0) lgkmcnt(0)" ::: "memory");
      bar();
      lds_spin(lds0 + (unsigned)(LW_OFF + (LW_T + layer) * 4), 12u);
      if (layer == 0) {
        if (ld.wl == 0) {
          cnt_wait(ws, CN_PK, 8 + NI, 36u);
          lds_wr32(lds0 + (unsigned)(LW_OFF + LW_HELLO * 4), 2u);
        } else {
          lds_spin(lds0 + (unsigned)(LW_OFF + LW_HELLO * 4), 2u);
        }
        MK(7);
      }
      static_assert(2 * K3C::SB <= P2_W, "free during the first residual GEMM");
      ld.dma_run<18>(wip, (unsigned)K3C::XB);
      ld.dma_run<18>(wip + K3C::WB, (unsigned)(K3C::SB + K3C::XB));
      if (L0REG && layer == 0) ld.pf_load<21, 9, 36, 72>(wip);
      if (layer == 0 && ld.wl == 0) cnt_wait(ws, CN_PK, 24 + CT, 72u);
      wait_vm<0>();
      ld.pf_pin<21, 9>();
      bar();
      FRESH();
      ld.wl = wave - 8;
      ld.p35(smem, ws, w_sp, layer, bid, MI, NI, RB, CT, HP MK_PASS);
    }
  } else {
    MF(12);
    math_start(smem, code0, x0, wqkv, ws, bid, wave, lane_id());
    MF(13);
#pragma unroll 1
    for (int layer = 0; layer < 2; ++layer) {
      FRESH();
      {
        const int lane = lane_id(), r0 = RT * 128;
        float* csq = reinterpret_cast<float*>(ws + OFF_CSQ + bid * 512);
        Attn at;
        at.setup(lds0 + (unsigned)(layer ? K1_T_OFF : K1R_T_OFF), r0, HP, wave, lane);
        if (layer) {
          if ((wave & 2) == 0) k1_gemm<2, 4>(smem, ws, csq, RT, HP, (wave & 1) * 2 + (wave & 4), 0, lane MK_PASS);
          else if (wave < 4) k1_gemm<3, 3>(smem, ws, csq, RT, HP, (wave & 1) * 3, 4, lane MK_PASS);
          else if (wave == 6) k1_gemm<2, 3>(smem, ws, csq, RT, HP, 6, 4, lane MK_PASS);
          else k1_gemm<1, 7>(smem, ws, csq, RT, HP, 8, 0, lane MK_PASS);
        } else {
          if (wave == 0) k1_gemm_raw<3, false>(smem, HP, 0, lane MF_PASS);
          else if (wave < 4) k1_gemm_raw<2, false>(smem, HP, 1 + 2 * wave, lane MF_PASS);
          else if (wave < 7) k1_gemm_raw<2, true>(smem, HP, 2 * (wave - 4), lane MF_PASS);
          else k1_gemm_raw<3, true>(smem, HP, 6, lane MF_PASS);
        }
        msync(lds0 + (unsigned)(LW_OFF + (LW_T + layer) * 4));
        MK(3);
        at.run(ws + OFF_O + (size_t)layer * SZ_X, r0, HP, wave, lane);
        if (layer == 0) {
          if (wave == 0 && lane < 32) pk_st16(csq + lane * 4, *reinterpret_cast<const u32x4*>(smem + K1R_CS_OFF + lane * 16));
          lds_spin(lds0 + (unsigned)(LW_OFF + LW_HELLO * 4), 1u);
        }
        MK(4);
        announce(lds0 + (unsigned)(LW_OFF + (LW_O + layer * 22 + (wave >> 2)) * 4), 4u, ws, CN_O + layer, (RT * 2 + (wave >> 2)) * 8 + lane,
                 lane < 8);
        MK(5);
        bar();
      }
      FRESH();
      {
        if (wave < 4) {
          const int lane = lane_id();
          u32x2 xv[5];
          res_ldx(xv, layer ? ws + OFF_X1 + SZ_X : (const char*)x0, layer == 0, RB, CT, wave, lane);
          ResOut ro;
          ro.setup(ws + OFF_X1 + (size_t)layer * 2 * SZ_X, ws + OFF_XS + (size_t)layer * 2 * SZ_XS, out, false, RB, CT, wave, lane);
          f32x4 acc[2][5];
          p2_gemm(acc, xv, lds0, uni_ptr(ws + OFF_O + (size_t)layer * SZ_X + (size_t)RB * (72 * 1024)), layer, wave, lane MK_PASS);
          MK(7);
          ro.run(acc[0], false);
          MK(8);
          announce(lds0 + (unsigned)(LW_OFF + (LW_X1 + layer) * 4), 4u, ws, CN_X1 + layer, (RB >> 1) * 16 + lane, lane < 16);
          MK(9);
        } else {
          if (layer && wave == 5) cnt_wait(ws, CN_O + layer, RB * 8 + CT, 16u);
          bar();
          if ((RTC & 1) && wave == 6) {
            __builtin_amdgcn_s_sleep(12);
            code_again(code0, bid);
          }
        }
        bar();
      }
      FRESH();
      {
        const int tid = wave * 64 + lane_id();
        float cv = 0.f;
        if (tid < 288) {
          const char* cp = uni_ptr(ws + OFF_CSP + NI * 1152);
          float a[18];
#pragma unroll
          for (int i = 0; i < 18; ++i)
            asm volatile("global_load_dword %0, %1, %2 sc1" : "=v"(a[i]) : "v"((unsigned)tid * 4u), "s"(cp + i * (F_ * 4)) : "memory");
#pragma unroll
          for (int i = 0; i < 18; ++i) asm volatile("s_waitcnt vmcnt(0)" : "+v"(a[i]));
          cv = (((a[0] + a[1]) + (a[2] + a[3])) + ((a[4] + a[5]) + (a[6] + a[7]))) +
               ((((a[8] + a[9]) + (a[10] + a[11])) + ((a[12] + a[13]) + (a[14] + a[15]))) + (a[16] + a[17]));
        } else if (tid >= 384) {
          cv = bf_lo((unsigned)b_sp[MI * 128 + tid - 384]);
        }
        p3_math(smem, ws, code0, bid, layer, cv, MI, NI, wave, tid MK_PASS);
      }
      FRESH();
      {
        const int lane = lane_id();
        if (wave < 4) {
          u32x2 xv[5];
          res_ldx(xv, ws + OFF_X1 + (size_t)layer * 2 * SZ_X, false, RB, CT, wave, lane);
          f32x4 acc[2][5];
          ResOut ro;
          ro.setup(ws + OFF_X1 + SZ_X, ws + OFF_XS + SZ_XS, out, layer != 0, RB, CT, wave, lane);
          ResX<36, 33, 8, P5_W, P5_XW, P5_X0, 3, P5_BAR> gm(acc);
          gm.setup(lds0, uni_ptr(ws + OFF_H + (size_t)layer * SZ_Z + (size_t)RB * (288 * 1024)), wave, lane, xv, (unsigned)P5_RED, layer + 1u);
          gm.prow();
          bar();
          MK(20);
          gm.prox<0>(std::make_integer_sequence<int, P5_X0>{});
          __builtin_amdgcn_sched_barrier(0);
          gm.run();
          MK(21);
          ro.run(acc[0], layer != 0);
          MK(22);
          if (layer == 0) {
            announce(lds0 + (unsigned)(LW_OFF + LW_X2 * 4), 4u, ws, CN_X2, (RB >> 1) * 16 + lane,
                     lane < 16 || (lane < 32 && (RB & 1) && RB < 31));
            MK(23);
          }
        } else {
          u32x4 wq[15];
          const int wm = wave & 3;
          const unsigned o = (unsigned)((wm >> 1) * 18432 + 12288 + (wm & 1) * 3072 + lane * 16);
          if (G1W) {
            const char* wp = uni_ptr(ws + OFF_PW + (size_t)CT * (36 * 9216) + 8 * 18432);
#pragma unroll
            for (int i = 0; i < 15; ++i)
              asm volatile("global_load_dwordx4 %0, %1, %2 offset:%3 sc1" : "=v"(wq[i]) : "v"(o), "s"(wp + (i / 3) * 36864), "n"((i % 3) * 1024) : "memory");
          }
          bar();
#pragma unroll
          for (int i = 0; i < 7; ++i) {
            if (G1W && i == 0) asm volatile("s_waitcnt vmcnt(0)" ::: "memory");
            asm volatile("s_waitcnt lgkmcnt(0)" ::: "memory");
            bar();
            if (G1W && i < 5) {
              const unsigned a = lds0 + o + (unsigned)(P5_W + ((2 * i + 3) % 8) * 18432) - ((i == 2 && wm >= 2) ? 8u * 18432u : 0u);
#pragma unroll
              for (int k = 0; k < 3; ++k) {
                if (P5PACE) __builtin_amdgcn_s_sleep(2 * P5PACE);
                asm volatile("ds_write_b128 %0, %1 offset:%2" ::"v"(a), "v"(wq[3 * i + k]), "n"(k * 1024) : "memory");
              }
            }
          }
          MK(22);
        }
        if (layer == 0) bar();
      }
    }
  }
}

}

extern "C" void kernel_launch(void* const* d_in, const int* in_sizes, int n_in, void* d_out, int out_size,
                              void* d_ws, size_t ws_size, hipStream_t stream) {
  const u16* x0 = (const u16*)d_in[0];
  const u16* wqkv = (const u16*)d_in[1];
  const u16* wo = (const u16*)d_in[2];
  const u16* w_in = (const u16*)d_in[4];
  const u16* w_sp = (const u16*)d_in[5];
  const u16* b_sp = (const u16*)d_in[6];
  const u16* w_out = (const u16*)d_in[7];
  char* ws = (char*)d_ws;
  (void)hipFuncSetAttribute((const void*)k_main, hipFuncAttributeMaxDynamicSharedMemorySize, LDS_ALL);
  k_main<<<256, 768, LDS_ALL, stream>>>(x0, wqkv, wo, w_in, w_sp, b_sp, w_out, (u16*)d_out, ws);
}
```

```cpp
#include <hip/hip_runtime.h>
#include <hip/hip_bf16.h>
#include <stdint.h>

#define DEVI __device__ __forceinline__

typedef unsigned short u16;
typedef __attribute__((ext_vector_type(8))) short s16x8;
typedef __attribute__((ext_vector_type(4))) short s16x4;
typedef __attribute__((ext_vector_type(4))) float f32x4;
typedef __attribute__((ext_vector_type(2))) float f32x2;
typedef __attribute__((ext_vector_type(2))) __bf16 bf16x2;
typedef __attribute__((ext_vector_type(2))) unsigned u32x2;
typedef __attribute__((ext_vector_type(4))) unsigned u32x4;

#define MK(I) ((void)0)
#define MX(S, I) ((void)0)
#define MF(I) ((void)0)
#define MF_ARG
#define MF_PASS
#define MK_ARG
#define MK_PASS

#define SPIN_LDS for (;;)
#define SPIN_MEM (1u << 22)

#define G1W 0
#define P5PACE 0
#define L0REG 0
#define RTC 0
#define P1PR 3
#define HSPLIT 1
#define HPOLL 1
#define XA4 1
#define MPOLL 1
namespace {

constexpr int S_ = 2048, D_ = 576, F_ = 4608, FH_ = 2304;
constexpr int NQKV = 3 * D_;

constexpr size_t SZ_X = (size_t)S_ * D_ * 2;
constexpr size_t SZ_Z = (size_t)S_ * FH_ * 2;
constexpr size_t OFF_X1 = 0;
constexpr size_t OFF_O = OFF_X1 + 3 * SZ_X;
constexpr size_t OFF_Z2 = OFF_O + 2 * SZ_X;
constexpr size_t OFF_H = OFF_Z2 + 2 * SZ_Z;
constexpr size_t SZ_XS = (size_t)S_ * 8 * 8;
constexpr size_t OFF_XS = OFF_H + 2 * SZ_Z;
constexpr size_t OFF_CSQ = OFF_XS + 3 * SZ_XS;
constexpr size_t OFF_ZP = OFF_CSQ + 256 * 512;
constexpr size_t SZ_ZP = (size_t)16 * 16 * 1024;
constexpr size_t OFF_PQ = OFF_ZP + 2 * SZ_ZP;
constexpr size_t OFF_PO = OFF_PQ + (size_t)16 * 9 * 64 * 112 * 2;
constexpr size_t OFF_PI = OFF_PO + (size_t)D_ * D_ * 2;
constexpr size_t OFF_PW = OFF_PI + (size_t)32 * 9 * 64 * 144 * 2;
constexpr size_t OFF_CSP = OFF_PW + (size_t)FH_ * D_ * 2;
constexpr size_t OFF_CNT = OFF_CSP + (size_t)18 * F_ * 4;
constexpr int CN_X1 = 0, CN_H = 2, CN_X2 = 4, CN_O = 5, CN_Z = 7, CN_PK = 9, CN_H2 = 10, NCNT = 12;
constexpr size_t OFF_TAG = OFF_CNT + (size_t)NCNT * 256 * 128;
constexpr size_t OFF_HELLO = OFF_TAG + 128;
constexpr size_t OFF_POT = OFF_HELLO + 2048;
constexpr size_t OFF_CSK = OFF_POT + 1024;
constexpr size_t WS_END = OFF_CSK + 256 * 2048;

DEVI float bf_lo(unsigned u) { return __uint_as_float(u << 16); }
DEVI float bf_hi(unsigned u) { return __uint_as_float(u & 0xffff0000u); }
DEVI unsigned pack2(float a, float b) {
  f32x2 v = {a, b};
  bf16x2 r = __builtin_convertvector(v, bf16x2);
  return __builtin_bit_cast(unsigned, r);
}
DEVI float rbf(float a) {
  return bf_lo(pack2(a, 0.f));
}

DEVI float sum_g(float x) {
  const unsigned u = __float_as_uint(x);
  const auto p = __builtin_amdgcn_permlane32_swap(u, u, false, false);
  const unsigned v = __float_as_uint(__uint_as_float(p[0]) + __uint_as_float(p[1]));
  const auto q = __builtin_amdgcn_permlane16_swap(v, v, false, false);
  return __uint_as_float(q[0]) + __uint_as_float(q[1]);
}

DEVI unsigned vconst(unsigned c) {
  unsigned r;
  asm volatile("v_mov_b32 %0, %1" : "=v"(r) : "s"(c));
  return r;
}
DEVI const char* uni_ptr(const char* p) {
  unsigned long long v = (unsigned long long)p;
  unsigned lo = __builtin_amdgcn_readfirstlane((unsigned)v), hi = __builtin_amdgcn_readfirstlane((unsigned)(v >> 32));
  asm("s_nop 4" : "+s"(lo), "+s"(hi));
  return (const char*)(((unsigned long long)hi << 32) | lo);
}

DEVI void dma16(const char* sbase, unsigned voff, unsigned m0val) {
  asm volatile("s_mov_b32 m0, %2\n\ts_nop 0\n\tglobal_load_lds_dwordx4 %0, %1"
               :
               : "v"(voff), "s"(sbase), "s"(m0val)
               : "memory");
}

DEVI void ln_finish(float s, float q, float invn, float& mb, float& rinv) {
  float mean = s * invn;
  asm("" : "+v"(mean));
  float var = fmaxf(__builtin_fmaf(-mean, mean, q * invn), 0.f);
  mb = rbf(mean);
  float v = rbf(var);
  float t = rbf(v + 1e-5f);
  float sd = rbf(__builtin_amdgcn_sqrtf(t));
  rinv = __builtin_amdgcn_rcpf(sd);
}

DEVI unsigned pack2v(f32x2 v) { return pack2(v[0], v[1]); }

template <int BM_, int BN_>
struct GemmCfg {
  static constexpr int BM = BM_, BN = BN_, NS = 4;
  static constexpr int P = BN * 2;
  static constexpr int XB = BM * 128, WB = 64 * P, SB = XB + WB;
  static constexpr int LDS_BYTES = NS * SB;
};

DEVI int swap23(int r) { return (r & ~12) | ((r & 4) << 1) | ((r & 8) >> 1); }

DEVI unsigned lds_rd32(unsigned addr) {
  unsigned v;
  asm volatile("ds_read_b32 %0, %1\n\ts_waitcnt lgkmcnt(0)" : "=v"(v) : "v"(addr) : "memory");
  return v;
}
DEVI void lds_wr32(unsigned addr, unsigned v) { asm volatile("ds_write_b32 %0, %1" ::"v"(addr), "v"(v) : "memory"); }

template <class C, int MT, int NT, int D, int R, bool XCM, int WT, bool SQ = false, unsigned long long WS = 0>
struct GemmMath {
  static constexpr int WO = WT ? 1 : 2;
  static constexpr int NS = C::NS, SB = C::SB, P = C::P, NU = 2 * NT;
  static constexpr int XPU = MT <= 2 ? MT : MT - 2;
  static constexpr int XU = (MT + XPU - 1) / XPU;
  static_assert(NU % R == 0 && R > D && D <= NT && XU <= D, "shape");
  s16x4 wlo[R], whi[R];
  s16x8 wf[R];
  s16x8 xf[2][MT];
  unsigned xrd[2], wrd, xa[2], wa, so;
  f32x4 (&acc)[MT][NT];
  f32x4 accq[SQ ? MT : 1];
  unsigned long long wtab = WS;
  DEVI GemmMath(f32x4 (&a)[MT][NT]) : acc(a) {}

  template <int PU>
  DEVI void wread_lo() {
    constexpr int off = (PU / NT) * 32 * P + (PU % NT) * (WT == 2 ? 1024 : 32);
    if constexpr (WT) asm volatile("ds_read_b128 %0, %1 offset:%2" : "=v"(wf[PU % R]) : "v"(wa), "n"(off));
    else asm volatile("ds_read_b64_tr_b16 %0, %1 offset:%2" : "=v"(wlo[PU % R]) : "v"(wa), "n"(off));
  }
  template <int PU>
  DEVI void wread_hi() {
    if constexpr (!WT)
      asm volatile("ds_read_b64_tr_b16 %0, %1 offset:%2" : "=v"(whi[PU % R]) : "v"(wa), "n"((PU / NT) * 32 * P + (PU % NT) * 32 + 8 * P));
  }
  template <int H, int M>
  DEVI void xread1() {
    asm volatile("ds_read_b128 %0, %1 offset:%2" : "=v"(xf[H][M]) : "v"(xa[H]), "n"(M * (XCM ? 256 : 2048)));
  }
  static constexpr int xops(int v) {
    const int k = (v + D) % NT;
    if (k >= XU) return 0;
    const int n = MT - k * XPU;
    return n < XPU ? n : XPU;
  }
  static constexpr int ops(int v, bool last) { return (last && v + D >= NU) ? 0 : WO + xops(v); }
  static constexpr int newer(int U, bool last) {
    int n = 0;
    int v0 = U - D;
    if (U % NT == 0) v0 += XU - 1;
    else n += v0 >= 0 ? xops(v0) : xops(v0 + NU);
    for (int v = v0 + 1; v < U; ++v) n += v >= 0 ? ops(v, last) : ops(v + NU, false);
    return n;
  }
  template <int U, int M_>
  DEVI void mf() {
    constexpr int M = ((U % NT) & 1) ? MT - 1 - M_ : M_;
    s16x8 w;
    if constexpr (WT) w = wf[U % R];
    else w = __builtin_shufflevector(wlo[U % R], whi[U % R], 0, 1, 2, 3, 4, 5, 6, 7);
    acc[M][U % NT] = __builtin_amdgcn_mfma_f32_16x16x32_bf16(w, xf[U / NT][M], acc[M][U % NT], 0, 0, 0);
    __builtin_amdgcn_sched_barrier(0);
  }
  DEVI void pin() {
#pragma unroll
    for (int m = 0; m < MT; ++m)
#pragma unroll
      for (int n = 0; n < NT; ++n) asm volatile("" : "+v"(acc[m][n]));
  }
  template <int U, int I>
  DEVI void dsop() {
    constexpr int PU = (U + D) % NU;
    if constexpr (I == 0) wread_lo<PU>();
    else if constexpr (I == 1) wread_hi<PU>();
    else if constexpr (I - 2 < xops(U)) {
      constexpr int k = (U + D) % NT;
      xread1<((U + D - k) % NU) / NT, k * XPU + I - 2>();
    }
  }
  template <int U, bool LAST, bool BAR, int M>
  DEVI void slot() {
    constexpr bool PF = !(LAST && U + D >= NU);
    mf<U, M>();
    if constexpr (M == 0 && PF && U + D == NU) {
      if constexpr (BAR) __builtin_amdgcn_s_barrier();
      so = (so == (unsigned)((NS - 1) * SB)) ? 0u : so + (unsigned)SB;
      wa = wrd + (WS ? ((unsigned)wtab & 63u) << 11 : so);
      wtab >>= 6;
      xa[0] = xrd[0] + so;
      xa[1] = xrd[1] + so;
      __builtin_amdgcn_sched_barrier(0);
    }
    if constexpr (PF) {
      dsop<U, M>();
      if constexpr (M == MT - 1) {
        if constexpr (MT <= 1) dsop<U, 1>();
        if constexpr (MT <= 2) {
          dsop<U, 2>();
          dsop<U, 3>();
        }
      }
      __builtin_amdgcn_sched_barrier(0);
    }
  }
  template <int U, bool LAST, bool BAR, int... M>
  DEVI void unit(std::integer_sequence<int, M...>) {
    static_assert(newer(U, LAST) <= 15, "lgkmcnt range");
    asm volatile("s_waitcnt lgkmcnt(%0)" ::"n"(newer(U, LAST)));
    __builtin_amdgcn_sched_barrier(0);
    (slot<U, LAST, BAR, M>(), ...);
    if constexpr (SQ && (U % NT) < MT) {
      constexpr int m = U % NT;
      accq[m] = __builtin_amdgcn_mfma_f32_16x16x32_bf16(xf[U / NT][m], xf[U / NT][m], accq[m], 0, 0, 0);
      __builtin_amdgcn_sched_barrier(0);
    }
    __builtin_amdgcn_sched_barrier(0);
  }
  template <bool LAST, bool BAR, int... U>
  DEVI void stage(std::integer_sequence<int, U...>) {
    (unit<U, LAST, BAR>(std::make_integer_sequence<int, MT>{}), ...);
  }
  template <int... U>
  DEVI void prow(std::integer_sequence<int, U...>) {
    ((wread_lo<U>(), wread_hi<U>()), ...);
  }
  template <int... M>
  DEVI void prox(std::integer_sequence<int, M...>) {
    (xread1<0, M>(), ...);
  }
  DEVI void setup(unsigned lds0, int mt0, int nt0, int lane, unsigned so0) {
    const int g = lane >> 4, r = lane & 15, q = (lane >> 2) & 3, p = lane & 3;
#pragma unroll
    for (int h = 0; h < 2; ++h)
      xrd[h] = XCM ? lds0 + (unsigned)((4 * h + g) * (C::BM * 16) + (mt0 * 16 + r) * 16)
                   : lds0 + (unsigned)((mt0 * 16 + r) * 128 + (((4 * h + g) ^ ((r >> 1) & 7)) * 16));
    wrd = WT == 2   ? lds0 + (unsigned)(C::XB + nt0 * 1024 + lane * 16)
          : WT == 1 ? lds0 + (unsigned)(C::XB + (g * 8 + (r & 7)) * P + (r >> 3) * 16 + nt0 * 32)
                    : lds0 + (unsigned)(C::XB + (16 * (g >> 1) + 4 * (g & 1) + q) * P + 8 * p + nt0 * 32);
    so = so0;
    xa[0] = xrd[0] + so;
    xa[1] = xrd[1] + so;
    wa = wrd + so;
  }
  DEVI void first() {
    prox(std::make_integer_sequence<int, MT>{});
    prow(std::make_integer_sequence<int, D>{});
    asm volatile("s_waitcnt lgkmcnt(0)");
  }
  template <int N, bool BAR = true>
  DEVI void mid() {
#pragma unroll 1
    for (int s = 0; s < N; ++s) stage<false, BAR>(std::make_integer_sequence<int, NU>{});
  }
  DEVI void last() {
    stage<true, true>(std::make_integer_sequence<int, NU>{});
    pin();
  }
  template <int KS>
  DEVI void run(unsigned lds0, int mt0, int nt0, int lane, unsigned so0) {
    setup(lds0, mt0, nt0, lane, so0);
    __builtin_amdgcn_s_barrier();
    first();
    mid<KS - 1>();
    last();
  }
};

DEVI const char* code_here() {
  unsigned long long pc;
  asm volatile("s_getpc_b64 %0" : "=s"(pc));
  return (const char*)pc;
}
DEVI int okmap(int p) {
  const int ch = p >> 3, e = p & 7, c = (ch - 8) & 3;
  return ch < 8 ? (2 * ch + (e >> 2)) * 36 + 16 + (e & 3) : ((ch - 8) >> 2) * 36 + c * 8 + (c >> 1) * 4 + e;
}

DEVI void pk_st16(void* p, u32x4 v) {
  asm volatile("global_store_dwordx4 %0, %1, off sc1\n\ts_nop 1" ::"v"(p), "v"(v) : "memory");
}

using K1C = GemmCfg<144, 112>;
constexpr int K1_TP = 240;
constexpr int K1_T_OFF = K1C::SB;
constexpr int K1_ST_OFF = 163840 - 128 - 1152;
constexpr int K1_WX = 4 * K1C::SB + 2048;
constexpr unsigned long long K1_WS = 16ull | 32ull << 6 | 48ull << 12 | 56ull << 18 | 63ull << 24 | 16ull << 30 | 32ull << 36 | 48ull << 42;
static_assert(K1C::SB == 16 * 2048 && K1_WX - K1C::XB == 56 * 2048 && K1C::WB == 7 * 2048 && K1_WX + 2 * K1C::WB <= K1_ST_OFF, "K1 w places");
static_assert(K1_T_OFF + 144 * K1_TP + 64 <= 2 * K1C::SB + K1C::XB, "K1 LDS alias");
using K1R = GemmCfg<144, 144>;
constexpr int K1R_T_OFF = K1R::SB;
constexpr int K1R_CS_OFF = K1R_T_OFF + 144 * K1_TP;
constexpr int K1R_ST_OFF = K1R_CS_OFF + 512;
constexpr int K1R_DUMMY = K1R::LDS_BYTES;
constexpr int K1R_PO = K1R_DUMMY + 1024;
constexpr int K1R_CNT = 163840 - 128 + 28 * 4;
DEVI void msync(unsigned addr) {
  asm volatile("s_waitcnt lgkmcnt(0)" ::: "memory");
  asm volatile("s_mov_b64 exec, 1\n\tds_add_u32 %0, %1\n\ts_mov_b64 exec, -1" ::"v"(addr), "v"(vconst(1u)) : "memory");
  SPIN_LDS {
    if (__builtin_amdgcn_readfirstlane(lds_rd32(addr)) >= 8u) break;
  }
}
static_assert(K1R_PO + 13824 + 256 <= 163840 - 128 && K1R_ST_OFF + 144 * 8 <= 3 * K1R::SB, "K1R LDS");

DEVI void touch64(const char* sbase, unsigned voff, unsigned m0val) {
  asm volatile("s_mov_b32 m0, %2\n\ts_nop 0\n\tglobal_load_lds_dword %0, %1" : : "v"(voff), "s"(sbase), "s"(m0val) : "memory");
}

template <int MT, bool G1>
DEVI void k1_gemm_raw(char* smem, int hp, int mt0, int lane MF_ARG) {
  constexpr int NT = 4, nt0 = G1 ? 4 : 0;
  const unsigned lds0 = (unsigned)(size_t)smem;
  const int g = lane >> 4, r = lane & 15;
  unsigned ta[NT];
#pragma unroll
  for (int nt = 0; nt < NT; ++nt) {
    const int j = (nt0 + nt) * 16 + g * 4, seg = j / 40, cj = j - seg * 40 - (hp & 1) * 4;
    const int to = (seg < 3 && cj >= 0 && cj < 36) ? seg * 80 + cj * 2 + (cj >= 20 ? 8 : 0) : 40;
    ta[nt] = lds0 + (unsigned)(K1R_T_OFF + (mt0 * 16 + r) * K1_TP + to);
    asm volatile("" : "+v"(ta[nt]));
  }
  unsigned sa = lds0 + (unsigned)(K1R_ST_OFF + (mt0 * 16 + r) * 8), ca = lds0 + (unsigned)(K1R_CS_OFF + (nt0 * 16 + g * 4) * 4);
  asm volatile("" : "+v"(sa), "+v"(ca));
  f32x4 acc[MT][NT];
#pragma unroll
  for (int mt = 0; mt < MT; ++mt)
#pragma unroll
    for (int nt = 0; nt < NT; ++nt) acc[mt][nt] = f32x4{0.f, 0.f, 0.f, 0.f};
  {
    GemmMath<K1R, MT, 4, 3, 4, false, 0, G1> gm(acc);
    if constexpr (G1) {
#pragma unroll
      for (int mt = 0; mt < MT; ++mt) gm.accq[mt] = f32x4{0.f, 0.f, 0.f, 0.f};
    }
    MF(14);
    if constexpr (!G1) {
      if (mt0 < 7) asm volatile("s_waitcnt vmcnt(0)" ::: "memory");
    }
    MF(15);
    gm.template run<9>(lds0, mt0, nt0, lane, 0u);
    MF(2);
    if constexpr (G1) {
#pragma unroll
      for (int mt = 0; mt < MT; ++mt) {
        const f32x4 q = gm.accq[mt];
        const float d = (r & 2) ? ((r & 1) ? q[3] : q[2]) : ((r & 1) ? q[1] : q[0]);
        const float qq = sum_g(g == (r >> 2) ? d : 0.f);
        float mb, rinv;
        ln_finish(acc[mt][3][0], qq, 1.0f / (float)D_, mb, rinv);
        const f32x2 st = {-mb, rinv};
        if (g == 2) asm volatile("ds_write_b64 %0, %1 offset:%2" ::"v"(sa), "v"(st), "n"(mt * 128) : "memory");
      }
    }
  }
  if (mt0 == 0 && r == 0) {
#pragma unroll
    for (int nt = 0; nt < NT; ++nt) *reinterpret_cast<f32x4*>(smem + K1R_CS_OFF + ((nt0 + nt) * 16 + g * 4) * 4) = acc[0][nt];
  }
  msync(lds0 + (unsigned)K1R_CNT);
  MF(3);
  f32x2 st[MT];
  f32x4 cs[NT];
#pragma unroll
  for (int nt = 0; nt < NT; ++nt) asm volatile("ds_read_b128 %0, %1 offset:%2" : "=v"(cs[nt]) : "v"(ca), "n"(nt * 64) : "memory");
#pragma unroll
  for (int mt = 0; mt < MT; ++mt) asm volatile("ds_read_b64 %0, %1 offset:%2" : "=v"(st[mt]) : "v"(sa), "n"(mt * 128) : "memory");
#pragma unroll
  for (int i = 0; i < MT; ++i) asm volatile("s_waitcnt lgkmcnt(0)" : "+v"(st[i]));
#pragma unroll
  for (int i = 0; i < NT; ++i) asm volatile("" : "+v"(cs[i]));
#pragma unroll
  for (int mt = 0; mt < MT; ++mt) {
    f32x2 M = {st[mt][0], st[mt][0]}, R = {st[mt][1], st[mt][1]};
    asm volatile("" : "+v"(M), "+v"(R));
#pragma unroll
    for (int nt = 0; nt < NT; ++nt) {
      const f32x2 v0 = R * __builtin_elementwise_fma(M, f32x2{cs[nt][0], cs[nt][1]}, f32x2{acc[mt][nt][0], acc[mt][nt][1]});
      const f32x2 v1 = R * __builtin_elementwise_fma(M, f32x2{cs[nt][2], cs[nt][3]}, f32x2{acc[mt][nt][2], acc[mt][nt][3]});
      acc[mt][nt][0] = __uint_as_float(pack2v(v0));
      acc[mt][nt][1] = __uint_as_float(pack2v(v1));
      const f32x2 pk = {acc[mt][nt][0], acc[mt][nt][1]};
      asm volatile("ds_write_b64 %0, %1 offset:%2" ::"v"(ta[nt]), "v"(pk), "n"(mt * 16 * K1_TP) : "memory");
    }
  }
}

DEVI void dma16m(const char* sbase, unsigned voff, unsigned m0val, unsigned long long mask) {
  asm volatile("s_mov_b32 m0, %2\n\ts_mov_b64 exec, %3\n\tglobal_load_lds_dwordx4 %0, %1\n\ts_mov_b64 exec, -1"
               :
               : "v"(voff), "s"(sbase), "s"(m0val), "s"(mask)
               : "memory");
}
template <int N>
DEVI void wait_vm() {
  asm volatile("s_waitcnt vmcnt(%0)" ::"n"(N) : "memory");
}

constexpr int LDS_ALL = 163840;
constexpr int LW_OFF = LDS_ALL - 128;
constexpr int LW_GO = 0, LW_T = 2, LW_O = 4  , LW_X1 = 6, LW_H = 8, LW_X2 = 12, LW_PK = 13, LW_Z = 14, LW_PAIR = 16, LW_AD = 20,
              LW_L = 22, LW_S = 24, LW_ST = 29, LW_HELLO = 30, LW_Q = 31;
DEVI void announce(unsigned addr, unsigned n, char* ws, int kind, int who, bool act) {
  const char* cb = uni_ptr(ws + OFF_CNT + (size_t)kind * 256 * 128);
  const unsigned long long mask = __ballot(act);
  unsigned t, u;
  asm volatile(
      "s_waitcnt vmcnt(0)\n\t"
      "s_mov_b64 exec, 1\n\t"
      "ds_add_rtn_u32 %0, %2, %3\n\t"
      "s_waitcnt lgkmcnt(0)\n\t"
      "v_readfirstlane_b32 %1, %0\n\t"
      "s_cmp_eq_u32 %1, %4\n\t"
      "s_cselect_b64 exec, %5, 0\n\t"
      "global_atomic_add %6, %3, %7 sc1\n\t"
      "s_mov_b64 exec, -1\n\t"
      "s_nop 0"
      : "=&v"(t), "=&s"(u)
      : "v"(addr), "v"(vconst(1u)), "s"(n - 1u), "s"(mask), "v"((unsigned)(who & 255) * 128u), "s"(cb)
      : "memory", "scc");
}
DEVI void cnt_wait(const char* ws, int kind, int who, unsigned n) {
  const char* cp = ws + OFF_CNT + (size_t)(kind * 256 + who) * 128;
#pragma unroll 1
  for (unsigned spin = 0; spin < SPIN_MEM; ++spin) {
    unsigned f;
    asm volatile("global_load_dword %0, %1, off sc1\n\ts_waitcnt vmcnt(0)" : "=&v"(f) : "v"(cp) : "memory");
    if (__builtin_amdgcn_readfirstlane(f) == n - 1u) break;
    __builtin_amdgcn_s_sleep(1);
  }
}
DEVI void dma16_sc1(const char* sbase, unsigned voff, unsigned m0val) {
  asm volatile("s_mov_b32 m0, %2\n\ts_nop 0\n\tglobal_load_lds_dwordx4 %0, %1 sc1"
               :
               : "v"(voff), "s"(sbase), "s"(m0val)
               : "memory");
}
DEVI void bar() { __builtin_amdgcn_s_barrier(); }
DEVI void lds_inc(unsigned addr, unsigned by = 1u) {
  asm volatile("s_mov_b64 exec, 1\n\tds_add_u32 %0, %1\n\ts_mov_b64 exec, -1" ::"v"(addr), "v"(vconst(by)) : "memory");
}
DEVI void lds_spin(unsigned addr, unsigned n) {
  SPIN_LDS {
    if (__builtin_amdgcn_readfirstlane(lds_rd32(addr)) >= n) break;
    __builtin_amdgcn_s_sleep(1);
  }
}
template <int SL = 1>
DEVI void lds_spin4(unsigned addr, unsigned n) {
  SPIN_LDS {
    if (((__builtin_amdgcn_readfirstlane(lds_rd32(addr)) + (0x80u - n) * 0x01010101u) & 0x80808080u) == 0x80808080u) break;
    __builtin_amdgcn_s_sleep(SL);
  }
}
DEVI int lane_id() {
  int l;
  asm volatile("v_mbcnt_lo_u32_b32 %0, -1, 0\n\tv_mbcnt_hi_u32_b32 %0, -1, %0" : "=v"(l));
  __builtin_assume((unsigned)l < 64u);
  return l;
}

template <int I>
DEVI u32x4 tr8(const u32x4 (&v)[8]) {
  constexpr unsigned sel = (I & 1) ? 0x07060302u : 0x05040100u;
  return u32x4{__builtin_amdgcn_perm(v[1][I / 2], v[0][I / 2], sel), __builtin_amdgcn_perm(v[3][I / 2], v[2][I / 2], sel),
               __builtin_amdgcn_perm(v[5][I / 2], v[4][I / 2], sel), __builtin_amdgcn_perm(v[7][I / 2], v[6][I / 2], sel)};
}
template <int PITCH, bool SUM, int... I>
DEVI void tr8_store(const u32x4 (&v)[8], unsigned dst, float (&cs)[8], std::integer_sequence<int, I...>) {
  auto one = [&](auto I_) {
    const u32x4 o = tr8<I_>(v);
    const unsigned d_ = dst;
    asm volatile("ds_write_b128 %0, %1 offset:%2" ::"v"(d_), "v"(o), "n"(I_ * PITCH) : "memory");
    if constexpr (SUM) {
#pragma unroll
      for (int d = 0; d < 4; ++d) {
        const unsigned od = o[d];
        cs[I_] = __builtin_amdgcn_fdot2_f32_bf16(__builtin_bit_cast(bf16x2, od), __builtin_bit_cast(bf16x2, 0x3f803f80u), cs[I_], false);
      }
    }
  };
  (one(std::integral_constant<int, I>{}), ...);
}
template <int NB>
DEVI void lds_out(unsigned src, char* dst, int lane) {
  constexpr int N = (NB + 1023) / 1024;
  u32x4 t[N];
  const unsigned a = src + (unsigned)lane * 16u;
#pragma unroll
  for (int i = 0; i < N; ++i) asm volatile("ds_read_b128 %0, %1 offset:%2" : "=v"(t[i]) : "v"(a), "n"(i * 1024) : "memory");
  const char* d = uni_ptr(dst);
#pragma unroll
  for (int i = 0; i < N; ++i) {
    asm volatile("s_waitcnt lgkmcnt(%1)" : "+v"(t[i]) : "n"(N - 1 - i) : "memory");
    if (i < N - 1 || lane < (NB - (N - 1) * 1024) / 16)
      asm volatile("global_store_dwordx4 %0, %1, %2 sc1\n\ts_nop 1" ::"v"((unsigned)lane * 16u), "v"(t[i]), "s"(d + i * 1024) : "memory");
  }
}
template <int A, int B, class F>
DEVI void static_for(F f) {
  if constexpr (A < B) {
    f(std::integral_constant<int, A>{});
    static_for<A + 1, B>(f);
  }
}
constexpr int WSP_V = 3;
constexpr int RAW_S1 = 7;
constexpr int RAW_S0 = 4;
template <int E>
struct RawX {
  static constexpr int first(int s) {
    return s <= RAW_S0 ? 0 : (s > RAW_S1 ? E : (E * (s - RAW_S0) + RAW_S1 - RAW_S0) / (RAW_S1 + 1 - RAW_S0));
  }
  static constexpr int n(int s) { return first(s + 1) - first(s); }
};
struct RawLd {
  static constexpr int VPS = 10, SB = K1R::SB;
  unsigned xoff[5], woff[5], m0x;
  unsigned long long xact0, wact[5], tag;
  const char *xp, *wp;
  int qb;
  DEVI void q_hook(int st, char* ws, unsigned lds0, int hp) {
    if ((qb >> 1) == st) {
      const int lane = lane_id(), g = lane >> 4;
      const unsigned a = lds0 + (unsigned)((st & 3) * SB + K1R::XB + ((qb & 1) * 32 + 16 * (g >> 1) + 4 * (g & 1) + ((lane >> 2) & 3)) * 288 +
                                           (lane & 3) * 8 + (hp & 1) * 8);
      const unsigned a2 = a + ((lane & 3) >= 1 ? 8u : 0u), a4 = a + ((lane & 3) >= 2 ? 8u : 0u);
      u32x2 qf[7][2];
#pragma unroll
      for (int nt = 0; nt < 7; ++nt)
        asm volatile("ds_read_b64_tr_b16 %0, %2 offset:%3\n\tds_read_b64_tr_b16 %1, %2 offset:%4"
                     : "=&v"(qf[nt][0]), "=&v"(qf[nt][1])
                     : "v"(nt == 2 ? a2 : (nt == 4 ? a4 : a)), "n"(nt * 32 + (nt > 4 ? 16 : (nt > 2 ? 8 : 0))),
                       "n"(2304 + nt * 32 + (nt > 4 ? 16 : (nt > 2 ? 8 : 0)))
                     : "memory");
      const char* d = uni_ptr(ws + OFF_PQ + (size_t)(hp * 18 + qb) * 7168 + 3072);
#pragma unroll
      for (int nt = 0; nt < 7; ++nt) {
        asm volatile("s_waitcnt lgkmcnt(%2)" : "+v"(qf[nt][0]), "+v"(qf[nt][1]) : "n"(12 - 2 * nt) : "memory");
        const u32x4 v = {qf[nt][0][0], qf[nt][0][1], qf[nt][1][0], qf[nt][1][1]};
        asm volatile("global_store_dwordx4 %0, %1, %2 offset:%3 sc1\n\ts_nop 1" ::"v"((unsigned)lane * 16u), "v"(v), "s"(d), "n"(nt * 1024 - 3072) : "memory");
      }
      if (st == 8) lds_wr32(lds0 + (unsigned)(LW_OFF + LW_Q * 4), 1u);
    }
  }
  DEVI void stage_x(unsigned so) {
    dma16m(xp, xoff[0], m0x + so, xact0);
#pragma unroll
    for (int i = 1; i < 4; ++i) dma16(xp, xoff[i], m0x + so + (unsigned)(i * 1024));
    dma16m(xp, xoff[4], m0x + so + 4096u, 0xffffffffull);
    xp += 128;
  }
  DEVI void stage_w(unsigned so) {
#pragma unroll
    for (int i = 0; i < 5; ++i) dma16m(wp, woff[i], m0x + so + (unsigned)(K1R::XB + i * 1024), wact[i]);
    wp += 64 * NQKV * 2;
  }
  DEVI void stage(unsigned so) {
    stage_x(so);
    stage_w(so);
  }
  DEVI void front(const u16* x, const u16* wqkv, const u16* wo, char* ws, int bid, unsigned lds0, int wl, int lane, int rt, int hp,
                  int pj MF_ARG) {
    xp = uni_ptr((const char*)x);
    wp = uni_ptr((const char*)wqkv + ((hp * 72) & ~15));
    m0x = lds0 + (unsigned)(wl * 288 * 16);
    xact0 = wl ? ~0ull : ~0xffull;
    const int row0 = wl * 36 + (lane >> 3);
#pragma unroll
    for (int i = 0; i < 5; ++i) {
      const int row = row0 + i * 8;
      int gr = rt * 128 - 16 + row;
      if (gr < 0) gr = 0;
      xoff[i] = (unsigned)(gr * (D_ * 2)) + (unsigned)(((lane ^ (row >> 1)) & 7) * 16);
    }
    stage_x(0u);
    u32x2 tg;
    asm volatile("global_load_dwordx2 %0, %1, %2 sc1" : "=v"(tg) : "v"(0u), "s"(uni_ptr(ws + OFF_TAG)) : "memory");
    asm volatile("" : "+v"(lane));
#pragma unroll
    for (int i = 0; i < 5; ++i) {
      const unsigned c = (unsigned)(i * 64 + (lane & 63));
      const unsigned wr = (c * 3641u) >> 16, wc = c - wr * 18u, seg = (wc * 13u) >> 6;
      wact[i] = __ballot(wc < 15u) & (i < 4 ? ~0ull : 0xffffffffull);
      woff[i] = (unsigned)(wl * 16 + swap23((int)wr)) * (unsigned)(NQKV * 2) + seg * (unsigned)(D_ * 2 - 80) + wc * 16u;
    }
    stage_w(0u);
    stage((unsigned)SB);
    MF(10);
    asm volatile("s_waitcnt vmcnt(%1)" : "+v"(tg) : "n"(VPS) : "memory");
    tag = (((unsigned long long)(unsigned)__builtin_amdgcn_readfirstlane(tg[1]) << 32) | (unsigned)__builtin_amdgcn_readfirstlane(tg[0])) +
          0x9e3779b97f4a7c15ull;
    MF(1);
    __builtin_amdgcn_s_barrier();
    MF(15);
    if (wl == 0 && lane == 0)
      asm volatile("global_store_dwordx2 %0, %1, off sc1\n\ts_nop 1" ::"v"(ws + OFF_HELLO + bid * 8), "v"(tag) : "memory");
    stage((unsigned)(2 * SB));
    qb = wl == 3 ? rt : ((wl == 2 && rt < 2) ? 16 + rt : 99);
    q_hook(0, ws, lds0, hp);
    const int nb = pj;
    const bool po = pj >= 0 && wl < 2;
    u32x4 v[8];
    unsigned da = 0u;
    if (po) {
      const int l54 = lane < 54 ? lane : 53, gl = l54 / 9, c = l54 - gl * 9, kl = wl * 6 + gl, t = nb / 6, kg = (nb - t * 6) * 12 + kl;
      da = lds0 + (unsigned)(K1R_PO + (kl >> 2) * 4608 + (16 * (kl & 1) + ((kl >> 1) & 1)) * 144 + c * 16);
      const unsigned sa[2] = {(unsigned)(okmap(kg * 8) * D_ + t * 72 + c * 8) * 2u, (unsigned)(okmap(kg * 8 + 4) * D_ + t * 72 + c * 8) * 2u};
      const char* ps = uni_ptr((const char*)wo);
#pragma unroll
      for (int j = 0; j < 8; ++j)
        asm volatile("global_load_dwordx4 %0, %1, %2 offset:%3" : "=v"(v[j]) : "v"(sa[j / 4]), "s"(ps), "n"((j % 4) * D_ * 2) : "memory");
    } else {
#pragma unroll
      for (int j = 0; j < 8; ++j) asm volatile("" : "=v"(v[j]));
    }
    unsigned slot = (unsigned)(3 * SB);
#pragma unroll
    for (int S = 0; S < RAW_S0; ++S) {
      wait_vm<VPS>();
      __builtin_amdgcn_s_barrier();
      if (S == 0) MF(16);
      if (S == 1) MF(17);
      if (S == 2) MF(18);
      if (S == 3) MF(19);
      stage(slot);
      slot = (slot == (unsigned)(3 * SB)) ? 0u : slot + (unsigned)SB;
      q_hook(S + 1, ws, lds0, hp);
      if (po && S == 1) {
        float cs[8];
        asm volatile("" : "+v"(v[0]), "+v"(v[1]), "+v"(v[2]), "+v"(v[3]), "+v"(v[4]), "+v"(v[5]), "+v"(v[6]), "+v"(v[7]));
        tr8_store<288, false>(v, da, cs, std::make_integer_sequence<int, 8>{});
        asm volatile("s_waitcnt lgkmcnt(0)" ::: "memory");
      }
      if (po && S == 2) lds_out<6912>(lds0 + (unsigned)(K1R_PO + wl * 6912), ws + OFF_PO + (size_t)nb * 13824 + wl * 6912, lane);
    }
  }
};
template <bool JOB>
DEVI void k1_raw_loader(RawLd& ld, const u16* __restrict__ w_in, const u16* __restrict__ w_out, char* __restrict__ ws,
                        unsigned long long tag, int bid, unsigned lds0, int wl, int lane, int rt, int hp, int ct, int job, int pj MF_ARG) {
  constexpr int VPS = RawLd::VPS, SB = RawLd::SB, E = JOB ? 16 : 0;
  const bool isi = job < 144;
  const int kb = isi ? job >> 3 : job - 144;
  const int pitch = isi ? F_ * 2 : D_ * 2;
  const int l36 = lane < 36 ? lane : 35;
  unsigned sa = (unsigned)l36 * 16u, da;
  if (isi) da = lds0 + (unsigned)((l36 >= 18 ? 9216 - 288 : 0) + wl * 8 * 288 + l36 * 16);
  else da = lds0 + (unsigned)((l36 / 9) * (4608 - 144) + (16 * (wl & 1) + (wl >> 1)) * 144 + l36 * 16);
  asm volatile("" : "+v"(sa), "+v"(da));
  u32x4 v[2][8];
  const char* ps = uni_ptr((const char*)(isi ? w_in + (job & 7) * 576 : w_out) + (size_t)(kb * 32 + wl * 8) * pitch);
  auto extra = [&](auto J_) {
    constexpr int j = J_;
    const char* q = ps + (j / 2) * pitch;
    u32x4 t;
    asm volatile("global_load_dwordx4 %0, %1, %2 offset:%3" : "=v"(t) : "v"(sa), "s"(q), "n"((j % 2) * 576) : "memory");
    v[j % 2][j / 2] = t;
  };
  auto step = [&](auto S_) {
    constexpr int S = S_;
    wait_vm<RawX<E>::n(S - 2) + (S <= 6 ? VPS : 0) + RawX<E>::n(S - 1)>();
    __builtin_amdgcn_s_barrier();
    MF(16 + S);
    if constexpr (S == RAW_S0) {
      static_assert(RAW_S0 >= 4, "wo blocks");
      if (pj >= 0 && wl == 0 && lane == 0)
        asm volatile("global_store_dwordx2 %0, %1, off sc1\n\ts_nop 1" ::"v"(ws + OFF_POT + (pj / 6) * 128 + (pj % 6) * 8), "v"(tag) : "memory");
    }
    if constexpr (S <= 5) ld.stage((unsigned)(((S + 3) % 4) * SB));
    ld.q_hook(S + 1, ws, lds0, hp);
    static_for<RawX<E>::first(S), RawX<E>::first(S + 1)>(extra);
  };
  const char* hp_ = ws + OFF_HELLO + lane * 32;
  u32x4 a, b;
  if (wl == 1) asm volatile("global_load_dwordx4 %0, %2, off sc1\n\tglobal_load_dwordx4 %1, %2, off offset:16 sc1" : "=&v"(a), "=&v"(b) : "v"(hp_) : "memory");
  else asm volatile("" : "=v"(a), "=v"(b));
  static_for<RAW_S0, 8>(step);
  MF(2);
  if (wl == 1) {
    asm volatile("" : "+v"(a), "+v"(b));
    const unsigned t0 = (unsigned)tag, t1 = (unsigned)(tag >> 32);
#pragma unroll 1
    for (unsigned spin = 0; spin < SPIN_MEM; ++spin) {
      const unsigned bad = ((a[0] ^ t0) | (a[2] ^ t0) | (b[0] ^ t0) | (b[2] ^ t0)) | ((a[1] ^ t1) | (a[3] ^ t1) | (b[1] ^ t1) | (b[3] ^ t1));
      if (!__any(bad != 0u)) break;
      __builtin_amdgcn_s_sleep(4);
      asm volatile("global_load_dwordx4 %0, %2, off sc1\n\tglobal_load_dwordx4 %1, %2, off offset:16 sc1\n\ts_waitcnt vmcnt(0)"
                   : "=&v"(a), "=&v"(b)
                   : "v"(hp_)
                   : "memory");
    }
    lds_wr32(lds0 + (unsigned)(LW_OFF + LW_HELLO * 4), 1u);
    if (bid == 0 && lane == 0) asm volatile("global_store_dwordx2 %0, %1, off sc1\n\ts_nop 1" ::"v"(ws + OFF_TAG), "v"(tag) : "memory");
  }
  MF(11);
  if (rt < 2) lds_spin(lds0 + (unsigned)(LW_OFF + LW_Q * 4), 1u);
  lds_spin(lds0 + (unsigned)K1R_CNT, 8u);
  if constexpr (JOB) {
#pragma unroll
    for (int b = 0; b < 2; ++b)
      asm volatile("s_waitcnt vmcnt(0)"
                   : "+v"(v[b][0]), "+v"(v[b][1]), "+v"(v[b][2]), "+v"(v[b][3]), "+v"(v[b][4]), "+v"(v[b][5]), "+v"(v[b][6]), "+v"(v[b][7])
                   :
                   : "memory");
  }
  MF(12);
  const char* tp = ws + OFF_POT + ct * 128 + (lane < 6 ? lane : 5) * 8;
  u32x2 f;
  asm volatile("global_load_dwordx2 %0, %1, off sc1" : "=v"(f) : "v"(tp) : "memory");
  if constexpr (JOB) {
    static_assert(SB == 4 * 9216, "staging");
#pragma unroll
    for (int b = 0; b < 2; ++b) {
      float cs[8] = {0.f, 0.f, 0.f, 0.f, 0.f, 0.f, 0.f, 0.f};
      tr8_store<288, true>(v[b], da + (unsigned)(b * 18432), cs, std::make_integer_sequence<int, 8>{});
      const f32x4 c0 = {cs[0], cs[1], cs[2], cs[3]}, c1 = {cs[4], cs[5], cs[6], cs[7]};
      const unsigned ca = lds0 + (unsigned)(3 * SB + wl * 2304) + sa * 2u;
      asm volatile("ds_write_b128 %0, %1 offset:%2" ::"v"(ca), "v"(c0), "n"(b * 1152) : "memory");
      asm volatile("ds_write_b128 %0, %1 offset:%2" ::"v"(ca), "v"(c1), "n"(b * 1152 + 16) : "memory");
    }
    lds_inc(lds0 + (unsigned)(LW_OFF + LW_ST * 4));
    lds_spin(lds0 + (unsigned)(LW_OFF + LW_ST * 4), 4u);
    if (isi) {
      const int j = (job & 7) * 4 + wl, ti = j < 16 ? 2 * j + 1 : 2 * (j - 16);
      lds_out<9216>(lds0 + (unsigned)(wl * 9216), ws + OFF_PI + (size_t)(ti * 18 + kb) * 9216, lane);
      f32x4 c[4];
      const unsigned ca = lds0 + (unsigned)(3 * SB + wl * 576) + sa;
#pragma unroll
      for (int i = 0; i < 4; ++i) asm volatile("ds_read_b128 %0, %1 offset:%2" : "=v"(c[i]) : "v"(ca), "n"(i * 2304) : "memory");
      asm volatile("s_waitcnt lgkmcnt(0)" : "+v"(c[0]), "+v"(c[1]), "+v"(c[2]), "+v"(c[3]));
      const f32x4 t = (c[0] + c[1]) + (c[2] + c[3]);
      if (lane < 36) pk_st16(ws + OFF_CSP + ((size_t)kb * F_ + ti * 144) * 4 + sa, __builtin_bit_cast(u32x4, t));
    } else {
      lds_out<4608>(lds0 + (unsigned)(wl * 9216), ws + OFF_PW + (size_t)(wl * 144 + kb) * 4608, lane);
      lds_out<4608>(lds0 + (unsigned)(wl * 9216 + 4608), ws + OFF_PW + (size_t)(wl * 144 + 72 + kb) * 4608, lane);
    }
  }
  MF(8);
  announce(lds0 + (unsigned)(LW_OFF + LW_PK * 4), 4u, ws, CN_PK, isi ? 8 + (job & 3) * 4 + lane : 24 + lane, JOB && lane < (isi ? 4 : 8));
  MF(9);
  if (wl == 0) {
    asm volatile("" : "+v"(f));
#pragma unroll 1
    for (unsigned spin = 0; spin < SPIN_MEM; ++spin) {
      if (!__any(f[0] != (unsigned)tag || f[1] != (unsigned)(tag >> 32))) break;
      __builtin_amdgcn_s_sleep(2);
      asm volatile("global_load_dwordx2 %0, %1, off sc1\n\ts_waitcnt vmcnt(0)" : "=&v"(f) : "v"(tp) : "memory");
    }
  }
}

template <int MT, int NT>
DEVI void k1_gemm(char* smem, const char* ws, const float* __restrict__ csq, int rt, int hp, int mt0, int nt0,
                  int lane MK_ARG) {
  const unsigned lds0 = (unsigned)(size_t)smem;
  const int g = lane >> 4;
  f32x4 cs[NT];
  unsigned ta[NT];
  const char* csp = uni_ptr((const char*)(csq + (hp & 1) * 4));
#pragma unroll
  for (int nt = 0; nt < NT; ++nt) {
    int c = (nt0 + nt) * 16 + g * 4;
    if (c > 104) c = 104;
    const int part = c / 36, ci = c - part * 36;
    asm volatile("global_load_dwordx4 %0, %1, %2 sc1" : "=v"(cs[nt]) : "v"((unsigned)(part * 40 + ci) * 4u), "s"(csp));
    const int to = (nt0 + nt) * 16 + g * 4 > 104 ? 40 : part * 80 + ci * 2 + (ci >= 20 ? 8 : 0);
    ta[nt] = lds0 + (unsigned)(K1_T_OFF + (mt0 * 16 + (lane & 15)) * K1_TP + to);
    asm volatile("" : "+v"(ta[nt]));
  }
  unsigned sa = lds0 + (unsigned)(K1_ST_OFF + (mt0 * 16 + (lane & 15)) * 8);
  asm volatile("" : "+v"(sa));
  f32x4 acc[MT][NT];
#pragma unroll
  for (int mt = 0; mt < MT; ++mt)
#pragma unroll
    for (int nt = 0; nt < NT; ++nt) acc[mt][nt] = f32x4{0.f, 0.f, 0.f, 0.f};
  if constexpr (NT == 7) cnt_wait(ws, CN_X2, rt * 16 + hp, rt ? 24u : 16u);
  bar();
  MK(1);
  {
    GemmMath<K1C, MT, NT, (NT == 7 ? 5 : 3), (NT == 3 ? 6 : NT), true, 2, false, K1_WS> gm(acc);
    gm.template run<9>(lds0, mt0, nt0, lane, 0u);
  }
  asm volatile("s_waitcnt vmcnt(0)" ::: "memory");
#pragma unroll
  for (int nt = 0; nt < NT; ++nt) asm volatile("" : "+v"(cs[nt]));
  MK(2);
  f32x2 st[MT];
#pragma unroll
  for (int mt = 0; mt < MT; ++mt) asm volatile("ds_read_b64 %0, %1 offset:%2" : "=v"(st[mt]) : "v"(sa), "n"(mt * 128) : "memory");
#pragma unroll
  for (int mt = 0; mt < MT; ++mt) {
    if (mt == 0) {
#pragma unroll
      for (int i = 0; i < MT; ++i) asm volatile("s_waitcnt lgkmcnt(0)" : "+v"(st[i]));
    }
    f32x2 M = {st[mt][0], st[mt][0]}, R = {st[mt][1], st[mt][1]};
    asm volatile("" : "+v"(M), "+v"(R));
#pragma unroll
    for (int nt = 0; nt < NT; ++nt) {
      const f32x2 v0 = R * __builtin_elementwise_fma(M, f32x2{cs[nt][0], cs[nt][1]}, f32x2{acc[mt][nt][0], acc[mt][nt][1]});
      const f32x2 v1 = R * __builtin_elementwise_fma(M, f32x2{cs[nt][2], cs[nt][3]}, f32x2{acc[mt][nt][2], acc[mt][nt][3]});
      acc[mt][nt][0] = __uint_as_float(pack2v(v0));
      acc[mt][nt][1] = __uint_as_float(pack2v(v1));
      const f32x2 pk = {acc[mt][nt][0], acc[mt][nt][1]};
      asm volatile("ds_write_b64 %0, %1 offset:%2" ::"v"(ta[nt]), "v"(pk), "n"(mt * 16 * K1_TP) : "memory");
    }
  }
}

DEVI float vmax(float a, float b) {
  float r;
  asm("v_max_f32 %0, %1, %2" : "=v"(r) : "v"(a), "v"(b));
  return r;
}
DEVI float max_g(float x) {
  const unsigned u = __float_as_uint(x);
  const auto p = __builtin_amdgcn_permlane32_swap(u, u, false, false);
  const unsigned v = __float_as_uint(vmax(__uint_as_float(p[0]), __uint_as_float(p[1])));
  const auto q = __builtin_amdgcn_permlane16_swap(v, v, false, false);
  return vmax(__uint_as_float(q[0]), __uint_as_float(q[1]));
}
struct Attn {
  unsigned qa, ka, kb, va, vb, m0, m1, m2, pm[2], ok, oa;
  float fill[4];
  DEVI void setup(unsigned T, int r0, int hp, int wave, int lane) {
    const int g = lane >> 4, r = lane & 15, blk = wave >> 1, hd = wave & 1;
    const unsigned co = (unsigned)(g < 2 ? hd * 48 + g * 16 : 32);
    ka = T + (unsigned)((blk * 32 + 2 * r) * K1_TP) + co;
    kb = T + (unsigned)((blk * 32 + 2 * (r & 7)) * K1_TP) + co;
    const int vr = 4 * g + ((lane >> 2) & 3);
    va = T + (unsigned)((blk * 32 + 2 * vr) * K1_TP + (lane & 3) * 8);
    vb = T + (unsigned)((blk * 32 + 2 * (vr & 7)) * K1_TP + (lane & 3) * 8);
    m0 = (g < 2 || (g == 2 && hd == 0)) ? ~0u : 0u;
    m1 = (g < 2 || (g == 2 && hd == 1)) ? ~0u : 0u;
    m2 = g < 2 ? ~0u : 0u;
    int wmax = (r0 + blk * 32) / 2 + r;
    if (wmax > 7) wmax = 7;
    ok = 0u;
    pm[0] = pm[1] = 0u;
    const float cm = __uint_as_float(vconst(0xce6e6b28u)), ci = __uint_as_float(vconst(0xff800000u));
    const unsigned ch = vconst(0xffffu);
#pragma unroll
    for (int e = 0; e < 4; ++e) {
      const int w1 = r + 8 - 4 * g - e, w = w1 & 15;
      fill[e] = w <= 7 ? cm : ci;
      if (w <= wmax) ok |= 1u << e;
      if (w1 >= 8) pm[e >> 1] |= ch << (16 * (e & 1));
    }
    oa = (unsigned)((8 + hp * 4 + hd * 2 + (g >> 1)) * 1024 + ((blk & 1) * 32 + 2 * r + (g & 1)) * 16);
    asm volatile("" : "+v"(ka), "+v"(kb), "+v"(va), "+v"(vb), "+v"(m0), "+v"(m1), "+v"(m2), "+v"(pm[0]), "+v"(pm[1]), "+v"(ok), "+v"(oa));
    asm volatile("" : "+v"(fill[0]), "+v"(fill[1]), "+v"(fill[2]), "+v"(fill[3]));
  }
  DEVI void run(char* o, int r0, int hp, int wave, int lane) {
    const int hd = wave & 1;
    u32x4 q[2], k0[2], k1[2];
    u32x2 v[2][2][2];
#pragma unroll
    for (int p = 0; p < 2; ++p) {
      asm volatile("ds_read_b128 %0, %1 offset:%2" : "=v"(q[p]) : "v"(ka), "n"((16 + p) * K1_TP) : "memory");
      asm volatile("ds_read_b128 %0, %1 offset:%2" : "=v"(k0[p]) : "v"(ka), "n"(p * K1_TP + 80) : "memory");
      asm volatile("ds_read_b128 %0, %1 offset:%2" : "=v"(k1[p]) : "v"(kb), "n"((32 + p) * K1_TP + 80) : "memory");
    }
    const unsigned vh = (unsigned)hd * 48u;
#pragma unroll
    for (int p = 0; p < 2; ++p) {
      asm volatile("ds_read_b64_tr_b16 %0, %1 offset:%2" : "=v"(v[p][0][0]) : "v"(va + vh), "n"(p * K1_TP + 160) : "memory");
      asm volatile("ds_read_b64_tr_b16 %0, %1 offset:%2" : "=v"(v[p][0][1]) : "v"(vb + vh), "n"((32 + p) * K1_TP + 160) : "memory");
      asm volatile("ds_read_b64_tr_b16 %0, %1 offset:%2" : "=v"(v[p][1][0]) : "v"(va), "n"(p * K1_TP + 192) : "memory");
      asm volatile("ds_read_b64_tr_b16 %0, %1 offset:%2" : "=v"(v[p][1][1]) : "v"(vb), "n"((32 + p) * K1_TP + 192) : "memory");
    }
    const char* ob = uni_ptr(o + (size_t)((r0 >> 6) + (wave >> 2)) * (72 * 1024));
    const bool okb[4] = {(ok & 1u) != 0u, (ok & 2u) != 0u, (ok & 4u) != 0u, (ok & 8u) != 0u};
    const float scale = 0.23570226039551584f, l2e = 1.4426950408889634f;
    const f32x4 z = {0.f, 0.f, 0.f, 0.f};
    f32x4 sc[2][2];
#pragma unroll
    for (int p = 0; p < 2; ++p) {
      asm volatile("s_waitcnt lgkmcnt(%3)" : "+v"(q[p]), "+v"(k0[p]), "+v"(k1[p]) : "n"(11 - 3 * p));
      const s16x8 qf = __builtin_bit_cast(s16x8, q[p] & u32x4{m0, m1, m2, m2});
      sc[p][0] = __builtin_amdgcn_mfma_f32_16x16x32_bf16(__builtin_bit_cast(s16x8, k0[p] & u32x4{~0u, ~0u, m2, m2}), qf, z, 0, 0, 0);
      sc[p][1] = __builtin_amdgcn_mfma_f32_16x16x32_bf16(__builtin_bit_cast(s16x8, k1[p] & u32x4{~0u, ~0u, m2, m2}), qf, z, 0, 0, 0);
    }
    float lg[2][4], mx[2], sum[2];
#pragma unroll
    for (int p = 0; p < 2; ++p) {
#pragma unroll
      for (int h = 0; h < 2; ++h) {
        const unsigned a0 = pack2(sc[p][0][2 * h], sc[p][0][2 * h + 1]), a1 = pack2(sc[p][1][2 * h], sc[p][1][2 * h + 1]);
        const unsigned a = (a1 & pm[h]) | (a0 & ~pm[h]);
        const unsigned b = pack2(bf_lo(a) * scale, bf_hi(a) * scale);
        lg[p][2 * h] = okb[2 * h] ? bf_lo(b) : fill[2 * h];
        lg[p][2 * h + 1] = okb[2 * h + 1] ? bf_hi(b) : fill[2 * h + 1];
      }
      mx[p] = vmax(vmax(lg[p][0], lg[p][1]), vmax(lg[p][2], lg[p][3]));
    }
#pragma unroll
    for (int p = 0; p < 2; ++p) mx[p] = max_g(mx[p]);
#pragma unroll
    for (int p = 0; p < 2; ++p) {
#pragma unroll
      for (int e = 0; e < 4; ++e) lg[p][e] = __builtin_amdgcn_exp2f((lg[p][e] - mx[p]) * l2e);
      sum[p] = (lg[p][0] + lg[p][1]) + (lg[p][2] + lg[p][3]);
    }
#pragma unroll
    for (int p = 0; p < 2; ++p) sum[p] = sum_g(sum[p]);
    asm volatile("s_waitcnt lgkmcnt(0)"
                 : "+v"(v[0][0][0]), "+v"(v[0][0][1]), "+v"(v[0][1][0]), "+v"(v[0][1][1]), "+v"(v[1][0][0]), "+v"(v[1][0][1]),
                   "+v"(v[1][1][0]), "+v"(v[1][1][1]));
    u32x2 y[2], yl;
#pragma unroll
    for (int p = 0; p < 2; ++p) {
      const float inv = __builtin_amdgcn_rcpf(sum[p]);
      const unsigned p0 = pack2(lg[p][0] * inv, lg[p][1] * inv), p1 = pack2(lg[p][2] * inv, lg[p][3] * inv);
      const s16x8 pf = __builtin_bit_cast(s16x8, u32x4{p0 & ~pm[0], p1 & ~pm[1], p0 & pm[0], p1 & pm[1]});
      const u32x4 v0 = {v[p][0][0][0], v[p][0][0][1], v[p][0][1][0], v[p][0][1][1]};
      const u32x4 v1 = {v[p][1][0][0], v[p][1][0][1], v[p][1][1][0], v[p][1][1][1]};
      const f32x4 o0 = __builtin_amdgcn_mfma_f32_16x16x32_bf16(__builtin_bit_cast(s16x8, v0), pf, z, 0, 0, 0);
      const f32x4 o1 = __builtin_amdgcn_mfma_f32_16x16x32_bf16(__builtin_bit_cast(s16x8, v1), pf, z, 0, 0, 0);
      y[p] = u32x2{pack2(o0[0], o0[1]), pack2(o0[2], o0[3])};
      yl[p] = hd ? pack2(o1[2], o1[3]) : pack2(o1[0], o1[1]);
    }
    const auto s0 = __builtin_amdgcn_permlane16_swap(y[0][0], y[1][0], false, false);
    const auto s1 = __builtin_amdgcn_permlane16_swap(y[0][1], y[1][1], false, false);
    const u32x4 w = {s0[0], s1[0], s0[1], s1[1]};
    asm volatile("global_store_dwordx4 %0, %1, %2 sc1\n\ts_nop 1" ::"v"(oa), "v"(w), "s"(ob) : "memory");
    if (lane < 16)
      asm volatile("global_store_dwordx2 %0, %1, %2 sc1\n\ts_nop 1" ::"v"(((oa & 1023u) >> 2) + (unsigned)((hp * 2 + hd) * 256)), "v"(yl), "s"(ob) : "memory");
  }
};

#define NL_MAIN 709
DEVI void code_again(const char* code0, int bid) {
  constexpr int PER = (2 * NL_MAIN + 31) / 32;
  const int lane = lane_id();
  int li = (bid >> 3) * PER + (lane < PER ? lane : PER - 1);
  if (li > 2 * NL_MAIN - 1) li = 2 * NL_MAIN - 1;
  unsigned t;
  asm volatile("global_load_dword %0, %1, %2\n\ts_waitcnt vmcnt(0)" : "=&v"(t) : "v"((unsigned)li * 64u), "s"(code0) : "memory");
}
DEVI void math_start(char* smem, const char* code0, const u16* b_sp, const u16* wqkv, char* ws, int bid, int wave, int lane) {
  const unsigned lds0 = (unsigned)(size_t)smem;
  const int tid = wave * 64 + lane, idx = bid >> 3;
  if (wave < 3) {
    static_assert(2 * SZ_ZP == (size_t)256 * 2048 && NCNT <= 16, "mark split");
    const u32x4 mk = {0xffffffffu, 0xffffffffu, 0xffffffffu, 0xffffffffu};
    char* c = ws + OFF_CNT + (size_t)((lane >> 3) * 256 + bid) * 128 + (lane & 7) * 16;
    pk_st16(wave < 2 ? ws + OFF_ZP + (size_t)((bid * 2 + wave) * 64 + lane) * 16 : c, mk);
    if (wave == 2 && lane < (NCNT - 8) * 8) pk_st16(c + 8 * 256 * 128, mk);
  }
  if (wave == 3) {
    const int li = idx * 54 + (lane < 54 ? lane : 53);
    touch64(uni_ptr((const char*)wqkv), (unsigned)(li * 1152 + 512), lds0 + (unsigned)(K1R_DUMMY + 768));
  }
  if (wave == 4) {
    constexpr int PER = (2 * NL_MAIN + 31) / 32;
    static_assert(PER <= 64, "one load");
    int li = idx * PER + (lane < PER ? lane : PER - 1);
    if (li > 2 * NL_MAIN - 1) li = 2 * NL_MAIN - 1;
    touch64(code0, (unsigned)li * 64u, lds0 + (unsigned)(K1R_DUMMY + 768));
  }
  if (wave == 5 && idx == 31) touch64(uni_ptr((const char*)b_sp), (unsigned)lane * 64u, lds0 + (unsigned)(K1R_DUMMY + 768));
  const u32x4 ones = {0x3f803f80u, 0x3f803f80u, 0x3f803f80u, 0x3f803f80u};
  if (tid < 256) *reinterpret_cast<u32x4*>(smem + (tid >> 6) * K1R::SB + K1R::XB + (tid & 63) * 288 + 240) = ones;
  else if (tid < 288) *reinterpret_cast<u32x4*>(smem + ((tid - 256) >> 3) * K1R::SB + (tid & 7) * 16) = ones;
  else if (tid < 296) *reinterpret_cast<u32x4*>(smem + LW_OFF + (tid - 288) * 16) = u32x4{0u, 0u, 0u, 0u};
  asm volatile("s_waitcnt lgkmcnt(0)" ::: "memory");
}

#define SAFE_OUT(L)
#define NOP_(n) "=s"(n)
template <int NST, int NS, int NSW, int WOFF, int XW, int X0, int S0, unsigned BAR>
struct ResX {
  static constexpr int WS = 4 * 4608, R = 10, V1 = 5 * NS, NV = V1 + 10 * (NST - NS), NG = (NSW + 2) / 3;
  s16x8 xf[XW][2];
  s16x8 wf[R];
  unsigned wrd[2][NG], xo, ea, fa, fv, tag;
  const char* xp[3];
  f32x4 (&acc)[2][5];
  DEVI ResX(f32x4 (&a)[2][5]) : acc(a) {}
  static constexpr int st(int V) { return V < V1 ? V / 5 : NS + (V - V1) / 10; }
  static constexpr int hf(int V) { return V < V1 ? 0 : ((V - V1) / 5) & 1; }
  static constexpr bool first(int V) { return V < V1 ? V % 5 == 0 : (V - V1) % 10 == 0; }
  static constexpr int wreq(int V) {
    int n = V + 5;
    if (V >= V1 - 4) n += V < V1 ? V - (V1 - 5) : 4;
    return n < NV ? n : NV;
  }
  static constexpr int XU = V1 + 1, XF = V1 + 11, XR = V1 + 15, XC = V1 + 21;
  static constexpr int oth(int V) { return NS && ((V >= XU && V <= XF && (V - XU) % 2 == 0) || V == XR || (V > XC && V <= XC + 5)) ? 1 : 0; }
  static constexpr int newer(int V) {
    const int F = V + 1 < NV ? V + 1 : V;
    int v = V - 1, n = 0;
    for (; v >= 0 && wreq(v - 1) > F; --v) n += wreq(v) - wreq(v - 1) + oth(v);
    return n + wreq(v) - 1 - F + (v >= 0 ? oth(v) : 0);
  }
  static constexpr int xreq(int T) {
    int n = X0 + 2 * (T + 1);
    if (n > T + XW) n = T + XW;
    if (NS && T <= 11 && n > 18) n = 18;
    if (T < 0) n = X0;
    return n < NST ? n : NST;
  }
  static_assert(xreq(NS - 2) == NST && (NS == 0 || XC + 5 < NV), "the last steps only compute and swap");
  template <int T, int M>
  DEVI void xld() {
    asm volatile("global_load_dwordx4 %0, %1, %2 sc1" : "=v"(xf[T % XW][M]) : "v"(xo), "s"(xp[M == 0 ? 0 : (T < NS ? 1 : 2)]));
    if constexpr (M == 1 && T + 1 < NST) asm volatile("v_add_u32 %0, 0x2000, %0" : "+v"(xo));
  }
  template <int V>
  DEVI void rd() {
    constexpr int T = st(V), sl = (T / 2 + S0) % NSW;
    asm volatile("ds_read_b128 %0, %1 offset:%2" : "=v"(wf[V % R]) : "v"(wrd[hf(V)][sl / 3]), "n"((sl % 3) * WS + (T & 1) * 9216 + (V % 5) * 32));
  }
  template <int V, int M, int H>
  DEVI void mf() {
    acc[M][V % 5] = __builtin_amdgcn_mfma_f32_16x16x32_bf16(wf[V % R], xf[st(V) % XW][H], acc[M][V % 5], 0, 0, 0);
    __builtin_amdgcn_sched_barrier(0);
  }
  template <int V>
  DEVI void unit() {
    constexpr int T = st(V), n = V % 5, nx = xreq(T) - xreq(T - 1), nw = wreq(V) - wreq(V - 1);
    constexpr int vm = 2 * (xreq(T - 1) - 1 - T);
    static_assert(newer(V) <= 15, "lgkmcnt range");
    if constexpr (first(V) && V % 2 == 0) asm volatile("s_waitcnt vmcnt(%0) lgkmcnt(%1)" ::"n"(vm), "n"(newer(V)));
    else if constexpr (first(V)) asm volatile("s_waitcnt vmcnt(%0)" ::"n"(vm));
    else if constexpr (V % 2 == 0) asm volatile("s_waitcnt lgkmcnt(%0)" ::"n"(newer(V)));
    __builtin_amdgcn_sched_barrier(0);
    if constexpr (V < V1) mf<V, V & 1, V & 1>();
    else mf<V, 0, hf(V)>();
    if constexpr (nw > 0) rd<wreq(V - 1)>();
    __builtin_amdgcn_sched_barrier(0);
    if constexpr (V < V1) {
      mf<V, 1 - (V & 1), 1 - (V & 1)>();
      if constexpr (nw > 1) rd<wreq(V - 1) + 1>();
      if constexpr (nx == 2 && n < 4) xld<xreq(T - 1) + n / 2, n % 2>();
      if constexpr (nx == 1 && n == (T & 1)) xld<xreq(T - 1), 0>();
      if constexpr (nx == 1 && n == (T & 1) + 2) xld<xreq(T - 1), 1>();
      if constexpr (n == 4 && (T & 1) && ((BAR >> (T / 2)) & 1u)) bar();
    } else if constexpr (NS > 0) {
      if constexpr (oth(V) && V < XF) asm volatile("ds_write_b128 %0, %1 offset:%2" ::"v"(ea), "v"(acc[1][(V - XU) / 2]), "n"((V - XU) * 512));
      if constexpr (V == XF) asm volatile("ds_write_b32 %0, %1" ::"v"(fa), "v"(fv));
      if constexpr (V == XF + 2) asm volatile("v_xor_b32 %0, 4, %0" : "+v"(fa));
      if constexpr (V == XR) asm volatile("ds_read_b32 %0, %1" : "+v"(fv) : "v"(fa));
      if constexpr (V == XR + 2) asm volatile("v_xor_b32 %0, 0x2000, %0" : "+v"(ea));
      if constexpr (V == XC) {
        static_assert(wreq(XR) <= XC + 1 || true, "");
        unsigned c = 1u << 18;
        asm volatile(
            "s_waitcnt lgkmcnt(%4)\n\tv_cmp_ne_u32 vcc, %3, %0\n\ts_cbranch_vccnz .Lw%=\n\t.subsection 1\n.Lw%=:\n\tds_read_b32 %0, %2\n\t"
            "s_waitcnt lgkmcnt(0)\n\tv_cmp_ne_u32 vcc, %3, %0\n\t" SAFE_OUT(".Lr%=") "s_cbranch_vccnz .Lw%=\n\ts_branch .Lr%=\n\t.subsection 0\n.Lr%=:"
            : "+v"(fv), NOP_(c)
            : "v"(fa), "s"(tag), "n"(wreq(XC) - wreq(XR))
            : "vcc", "scc");
      }
      if constexpr (oth(V) && V > XC) asm volatile("ds_read_b128 %0, %1 offset:%2" : "=v"(acc[1][V - XC - 1]) : "v"(ea), "n"((V - XC - 1) * 1024));
    }
    __builtin_amdgcn_sched_barrier(0);
  }
  template <int... V>
  DEVI void units(std::integer_sequence<int, V...>) {
    (unit<V>(), ...);
  }
  DEVI void run() {
    units(std::make_integer_sequence<int, NV>{});
    if constexpr (NS > 0) {
      asm volatile("s_waitcnt lgkmcnt(0)" : "+v"(acc[1][0]), "+v"(acc[1][1]), "+v"(acc[1][2]), "+v"(acc[1][3]), "+v"(acc[1][4]));
#pragma unroll
      for (int i = 0; i < 5; ++i) acc[0][i] += acc[1][i];
    }
  }
  template <int... V>
  DEVI void pro(std::integer_sequence<int, V...>) {
    (rd<V>(), ...);
  }
  DEVI void prow() { pro(std::make_integer_sequence<int, wreq(-1)>{}); }
  template <int T0, int... T>
  DEVI void prox(std::integer_sequence<int, T...>) {
    ((xld<T0 + T, 0>(), xld<T0 + T, 1>()), ...);
  }
  DEVI void setup(unsigned lds0, const char* xbase, int wave, int lane, unsigned red, unsigned nth) {
    const int g = lane >> 4, r = lane & 15, kq = wave & 1;
    xp[0] = xbase + wave * 256;
    xp[1] = xbase + (wave ^ 1) * 256;
    xp[2] = xp[0] + (kq ? -4096 : 4096);
    xo = (unsigned)((4 * kq + g) * 1024 + r * 16);
#pragma unroll
    for (int i = 0; i < 2 * NG; ++i) {
      wrd[i / NG][i % NG] = lds0 + (unsigned)(WOFF + (i % NG) * 3 * WS + (kq ^ (i / NG)) * 4608 + (2 * (r & 7) + 16 * (g & 1) + (g >> 1)) * 144 + (r >> 3) * 16);
      if (i < NG || NS == 0 || i % NG == ((NS / 2 + S0) % NSW) / 3) asm volatile("" : "+v"(wrd[i / NG][i % NG]));
    }
    asm volatile("" : "+v"(xo));
    if constexpr (NS > 0) {
      ea = lds0 + red + (unsigned)((wave ^ 1) * 8192 + lane * 16);
      fa = lds0 + (unsigned)(LW_OFF + (LW_PAIR + wave) * 4);
      fv = tag = nth;
      asm volatile("" : "+v"(ea), "+v"(fa), "+v"(fv));
    }
  }
  template <int N>
  DEVI void init(u32x2 (&xv)[5]) {
    asm volatile("s_waitcnt vmcnt(%5)" : "+v"(xv[0]), "+v"(xv[1]), "+v"(xv[2]), "+v"(xv[3]), "+v"(xv[4]) : "n"(N));
#pragma unroll
    for (int i = 0; i < 5; ++i) {
      acc[0][i] = f32x4{bf_lo(xv[i][0]), bf_hi(xv[i][0]), bf_lo(xv[i][1]), bf_hi(xv[i][1])};
      asm volatile("" : "+v"(acc[0][i]));
      acc[1][i] = f32x4{0.f, 0.f, 0.f, 0.f};
    }
  }
};

#define P5_XW 8
#define P5_X0 5
constexpr unsigned P5_BAR = 0x2aaau;
constexpr int P5_W = 15 * 1024;
constexpr int P2_W = K1_ST_OFF - 18 * 4608;
constexpr int P5_RED = 0;
static_assert(P5_RED % 16384 == 0 && P5_RED + 3 * 8192 + 5120 <= P5_W + 18432, "exchange area");
static_assert(P2_W >= K1_T_OFF + 144 * K1_TP + 64 && P2_W % 16 == 0 && P5_W + 8 * 18432 <= LW_OFF, "LDS placement");

DEVI void res_ldx(u32x2 (&xv)[5], const char* src, bool rm, int rb, int ct, int wave, int lane) {
  const int g = lane >> 4, g2 = g & 1, rl = wave * 16 + (lane & 15);
  const char* xp = uni_ptr(rm ? src + ((size_t)rb * 64 * D_ + ct * 72) * 2 : src + (size_t)(rb * 72 + ct * 9) * 1024);
  const unsigned o = rm ? (unsigned)(rl * (D_ * 2) + g * 8) : (unsigned)((g >> 1) * 1024 + rl * 16 + g2 * 8);
  const unsigned o2 = rm ? (unsigned)(rl * (D_ * 2) + g2 * 8 + 128) : (unsigned)(8192 + rl * 16 + g2 * 8);
#pragma unroll
  for (int i = 0; i < 4; ++i) asm volatile("global_load_dwordx2 %0, %1, %2 sc1" : "=v"(xv[i]) : "v"(o), "s"(xp + i * (rm ? 32 : 2048)));
  asm volatile("global_load_dwordx2 %0, %1, %2 sc1" : "=v"(xv[4]) : "v"(o2), "s"(xp));
}

DEVI void p2_gemm(f32x4 (&acc)[2][5], u32x2 (&xv)[5], unsigned lds0, const char* ob, int layer, int wave, int lane MK_ARG) {
  ResX<9, 0, 5, P2_W, 9, 9, 0, 0u> gm(acc);
  gm.setup(lds0, ob, wave, lane, 0u, 0u);
  unsigned a = (gm.xo & ~1023u) | ((gm.xo & 1023u) >> 2);
  gm.xo += 8192u;
  asm volatile("" : "+v"(a), "+v"(gm.xo));
  const char* pp[2] = {gm.xp[0] - wave * 192, gm.xp[2] - wave * 192};
  bar();
  MK(6);
  unsigned e[2][4];
#pragma unroll
  for (int d = 0; d < 8; ++d) asm volatile("global_load_dword %0, %1, %2 offset:%3 sc1" : "=v"(e[d / 4][d % 4]) : "v"(a), "s"(pp[d / 4]), "n"((d % 4) * 256));
  gm.template prox<1>(std::make_integer_sequence<int, 8>{});
  lds_inc(lds0 + (unsigned)(LW_OFF + (LW_T + layer) * 4));
  gm.prow();
  gm.template init<24>(xv);
  asm volatile("s_waitcnt vmcnt(16)"
               : "+v"(e[0][0]), "+v"(e[0][1]), "+v"(e[0][2]), "+v"(e[0][3]), "+v"(e[1][0]), "+v"(e[1][1]), "+v"(e[1][2]), "+v"(e[1][3]));
  gm.xf[0][0] = __builtin_bit_cast(s16x8, u32x4{e[0][0], e[0][1], e[0][2], e[0][3]});
  gm.xf[0][1] = __builtin_bit_cast(s16x8, u32x4{e[1][0], e[1][1], e[1][2], e[1][3]});
  __builtin_amdgcn_sched_barrier(0);
  gm.run();
}

struct ResOut {
  const char *xp[3], *sp;
  unsigned xo, so;
  DEVI void setup(char* xim, char* xs, u16* out, bool fin, int rb, int ct, int wave, int lane) {
    const int g = lane >> 4, rl = wave * 16 + (lane & 15);
    xp[0] = uni_ptr(fin ? (const char*)(out + (size_t)rb * 64 * D_ + ct * 72) : xim + (size_t)(rb * 72 + ct * 9) * 1024);
    xp[1] = xp[0] + (fin ? 64 : 4096);
    xp[2] = xp[0] + (fin ? 128 : 8192);
    sp = uni_ptr(xs + (size_t)(rb * 8 + ct) * 512);
    xo = (unsigned)(((g & 1) * 2 + (g >> 1)) * (fin ? 16 : 1024) + rl * (fin ? D_ * 2 : 16));
    so = (unsigned)(rl * 8);
    asm volatile("" : "+v"(xo), "+v"(so), "+s"(xp[0]), "+s"(xp[1]), "+s"(xp[2]), "+s"(sp));
  }
  DEVI void run(const f32x4 (&a)[5], bool fin) {
    u32x2 y[5];
    u32x4 v[3];
#pragma unroll
    for (int nt = 0; nt < 5; ++nt) y[nt] = u32x2{pack2(a[nt][0], a[nt][1]), pack2(a[nt][2], a[nt][3])};
#pragma unroll
    for (int i = 0; i < 3; ++i) {
      const auto s0 = __builtin_amdgcn_permlane16_swap(y[2 * i][0], y[i < 2 ? 2 * i + 1 : 4][0], false, false);
      const auto s1 = __builtin_amdgcn_permlane16_swap(y[2 * i][1], y[i < 2 ? 2 * i + 1 : 4][1], false, false);
      v[i] = u32x4{s0[0], s1[0], s0[1], s1[1]};
    }
    if (fin) {
      asm volatile("global_store_dwordx4 %0, %1, %4\n\tglobal_store_dwordx4 %0, %2, %5\n\ts_mov_b64 exec, 0xffff\n\tglobal_store_dwordx4 %0, %3, %6\n\t"
                   "s_mov_b64 exec, -1\n\ts_nop 0" ::"v"(xo), "v"(v[0]), "v"(v[1]), "v"(v[2]), "s"(xp[0]), "s"(xp[1]), "s"(xp[2])
                   : "memory");
      return;
    }
    asm volatile("global_store_dwordx4 %0, %1, %4 sc1\n\tglobal_store_dwordx4 %0, %2, %5 sc1\n\ts_mov_b64 exec, 0xffff\n\t"
                 "global_store_dwordx4 %0, %3, %6 sc1\n\ts_mov_b64 exec, -1\n\ts_nop 0" ::"v"(xo), "v"(v[0]), "v"(v[1]), "v"(v[2]),
                 "s"(xp[0]), "s"(xp[1]), "s"(xp[2])
                 : "memory");
    const bool lo = lane_id() < 16;
    float s[2] = {0.f, 0.f}, q[2] = {0.f, 0.f};
#pragma unroll
    for (int i = 0; i < 12; ++i) {
      const unsigned u = (i < 8 || lo) ? v[i / 4][i % 4] : 0u;
      s[i & 1] = __builtin_amdgcn_fdot2_f32_bf16(__builtin_bit_cast(bf16x2, u), __builtin_bit_cast(bf16x2, 0x3f803f80u), s[i & 1], false);
      q[i & 1] = __builtin_amdgcn_fdot2_f32_bf16(__builtin_bit_cast(bf16x2, u), __builtin_bit_cast(bf16x2, u), q[i & 1], false);
    }
    const f32x2 sq = {sum_g(s[0] + s[1]), sum_g(q[0] + q[1])};
    asm volatile("s_mov_b64 exec, 0xffff\n\tglobal_store_dwordx2 %0, %1, %2 sc1\n\ts_mov_b64 exec, -1" ::"v"(so), "v"(sq), "s"(sp) : "memory");
  }
};

using K3C = GemmCfg<128, 144>;
constexpr int K3_Z1_OFF = K3C::LDS_BYTES;
constexpr int K3_C2_OFF = K3_Z1_OFF + K3C::XB;
constexpr int K3_ZC_OFF = K3_C2_OFF + 2048;
constexpr int K3_DM_OFF = K3_ZC_OFF + 1024;
constexpr int K3_ST_OFF = K3_Z1_OFF + 4 * 5120;
constexpr int K3_C1_OFF = K3_ST_OFF + 2048;
constexpr int K3_BS_OFF = K3_C1_OFF + 576;
static_assert(K3_BS_OFF + 512 <= LW_OFF, "K3 LDS");
static_assert(P5_W + 19 * 1024 == K3C::SB && K3C::SB == 34 * 1024, "w_out ring");
constexpr int K3_D = 4;

DEVI void red4(float (&t)[4], float& s, float& q) {
#pragma unroll
  for (int j = 0; j < 2; ++j) {
    const auto p = __builtin_amdgcn_permlane16_swap(__float_as_uint(t[j]), __float_as_uint(t[2 + j]), false, false);
    const unsigned v = __float_as_uint(__uint_as_float(p[0]) + __uint_as_float(p[1]));
    const auto w = __builtin_amdgcn_permlane32_swap(v, v, false, false);
    t[j] = __uint_as_float(w[0]) + __uint_as_float(w[1]);
    asm volatile("" : "+v"(t[j]));
  }
  s = t[0];
  q = t[1];
}
DEVI void red8(const f32x4& a, const f32x4& b, float& s, float& q) {
  float t[4];
#pragma unroll
  for (int j = 0; j < 4; ++j) {
    t[j] = a[j] + b[j];
    asm volatile("" : "+v"(t[j]));
  }
  red4(t, s, q);
}
DEVI void red16(const f32x4& a, const f32x4& b, const f32x4& c, const f32x4& d, float& s, float& q) {
  float t[4];
#pragma unroll
  for (int j = 0; j < 4; ++j) {
    float u = a[j] + b[j], v = c[j] + d[j];
    asm volatile("" : "+v"(u), "+v"(v));
    t[j] = u + v;
    asm volatile("" : "+v"(t[j]));
  }
  red4(t, s, q);
}

constexpr int b_k(int s) { return s == 0 ? 0 : (s < 3 ? 6 + s : (s < 5 ? 2 + s : (s == 5 ? 4 : s - 5))); }
template <bool B>
struct UMath {
  static constexpr int D = 4, R = 6, NV = 162, SB = K3C::SB, P = K3C::P;
  s16x8 wf[R], xf[2][2];
  s16x8 (&xc)[4][2][2];
  f32x4 (&acc)[2][9];
  unsigned xa, wa;
  unsigned fa, fw, fv;
  DEVI UMath(f32x4 (&a)[2][9], s16x8 (&x)[4][2][2]) : xc(x), acc(a) {}
  static constexpr int wslot(int s) { return B ? (s + 1) % 4 : s % 4; }
  static constexpr int xslot(int s) { return B ? b_k(s) % 5 : s % 5; }
  static constexpr bool res(int q) { return !B || b_k(q / 2) >= 4; }
#define BPS 9
  static constexpr bool chk(int s) { return s >= 3 && s < 7 && s != BPS; }
  static constexpr bool xop(int v) { return v + D < NV && (v + D) % 9 < 2 && res((v + D) / 9); }
  static constexpr int ops1(int v) { return (xop(v) || (B && ((v % 18 == 9 && chk(v / 18)) || (v % 18 == 0 && v > 0)))) ? 1 : 0; }
  static constexpr int newer(int V) {
    int v0 = (V % 9 == 8 ? V : V + 1) - D, n = 0;
    if (v0 >= 0 && !(V % 9 == 0 && res(V / 9))) n += ops1(v0);
    for (int v = v0 + 1; v < V; ++v)
      if (v >= 0) n += (v + D < NV ? 1 : 0) + ops1(v);
    return n;
  }
  template <int V>
  DEVI void wread() {
    asm volatile("ds_read_b128 %0, %1 offset:%2" : "=v"(wf[V % R]) : "v"(wa), "n"((wslot(V / 18) & 1) * SB + ((V % 18) / 9) * 32 * P + (V % 9) * 32));
  }
  template <int Q, int M>
  DEVI void xread() {
    asm volatile("ds_read_b128 %0, %1 offset:%2" : "=v"(xf[Q & 1][M]) : "v"(xa), "n"((xslot(Q / 2) & 1) * SB + (Q & 1) * 8192 + M * (B ? 256 : 64)));
  }
  template <int V, int M_>
  DEVI void mf() {
    constexpr int M = ((V % 9) & 1) ? 1 - M_ : M_, q = V / 9;
    if constexpr (!B) acc[M][V % 9] = __builtin_amdgcn_mfma_f32_16x16x32_bf16(xf[q & 1][M], wf[V % R], acc[M][V % 9], 0, 0, 0);
    else if constexpr (res(q)) acc[M][V % 9] = __builtin_amdgcn_mfma_f32_16x16x32_bf16(wf[V % R], xf[q & 1][M], acc[M][V % 9], 0, 0, 0);
    else acc[M][V % 9] = __builtin_amdgcn_mfma_f32_16x16x32_bf16(wf[V % R], xc[b_k(q / 2)][q & 1][M], acc[M][V % 9], 0, 0, 0);
    __builtin_amdgcn_sched_barrier(0);
  }
  template <int V>
  DEVI void unit() {
    constexpr int U = V % 18, s = V / 18;
    static_assert(newer(V) <= 15, "lgkmcnt range");
    if constexpr ((V % 9) % 2 == 0 && V > 0) asm volatile("s_waitcnt lgkmcnt(%0)" ::"n"(newer(V)));
    __builtin_amdgcn_sched_barrier(0);
    mf<V, 0>();
    if constexpr (V + D < NV) wread<V + D>();
    if constexpr (s < 8 && U == 13 && (wslot(s + 1) >> 1) != (wslot(s) >> 1))
      asm volatile("v_add_u32 %0, %1, %0" : "+v"(wa) : "n"(((wslot(s + 1) >> 1) - (wslot(s) >> 1)) * 2 * SB));
    __builtin_amdgcn_sched_barrier(0);
    mf<V, 1>();
    if constexpr (xop(V)) xread<(V + D) / 9, (V + D) % 9>();
    if constexpr (s < 8) {
      if constexpr (U == 11 && res(2 * s + 2) && (xslot(s + 1) >> 1) != (xslot(s) >> 1))
        asm volatile("v_add_u32 %0, %1, %0" : "+v"(xa) : "n"(((xslot(s + 1) >> 1) - (xslot(s) >> 1)) * 2 * SB));
      if constexpr (!B && U == 13) __builtin_amdgcn_s_barrier();
      if constexpr (B && U == 9 && chk(s)) asm volatile("ds_read_u8 %0, %1 offset:%2" : "+v"(fv) : "v"(fa), "n"(s == 6 ? 0 : 8));
      unsigned n = 1u << 18;
      if constexpr (B && U == 13 && chk(s)) {
        asm volatile(
            "v_cmp_gt_u32 vcc, %3, %0\n\ts_cbranch_vccnz .Lw%=\n\t.subsection 1\n.Lw%=:\n\tds_read_u8 %0, %2 offset:%4\n\ts_waitcnt lgkmcnt(0)\n\t"
            "v_cmp_gt_u32 vcc, %3, %0\n\t" SAFE_OUT(".Lr%=") "s_cbranch_vccnz .Lw%=\n\ts_branch .Lr%=\n\t.subsection 0\n.Lr%=:"
            : "+v"(fv), NOP_(n)
            : "v"(fa), "n"(s == 6 ? 4 : s + 2), "n"(s == 6 ? 0 : 8)
            : "vcc", "scc");
      }
      if constexpr (B && U == 13 && s == BPS) {
        asm volatile(
            ".La%=:\n\ts_sleep 1\n\tds_read_u8 %0, %2\n\ts_waitcnt lgkmcnt(0)\n\tv_cmp_gt_u32 vcc, 3, %0\n\t" SAFE_OUT(".Lr%=") "s_cbranch_vccnz .La%=\n"
            ".Lb%=:\n\tds_read_u8 %0, %2 offset:8\n\ts_waitcnt lgkmcnt(0)\n\tv_cmp_gt_u32 vcc, %3, %0\n\t" SAFE_OUT(".Lr%=") "s_cbranch_vccnz .Lb%=\n.Lr%=:"
            : "+v"(fv), NOP_(n)
            : "v"(fa), "n"(s + 2)
            : "vcc", "scc");
      }
    }
    if constexpr (B && U == 15) asm volatile("v_mov_b32 %0, %1" : "+v"(fv) : "n"(s + 1));
    if constexpr (B && ((U == 0 && s > 0) || V == NV - 1)) asm volatile("ds_write_b8 %0, %1" ::"v"(fw), "v"(fv));
    __builtin_amdgcn_sched_barrier(0);
  }
  template <int... V>
  DEVI void units(std::integer_sequence<int, V...>) {
    (unit<V>(), ...);
  }
  DEVI void first() {
    if constexpr (!B) {
      xread<0, 0>();
      xread<0, 1>();
    }
    wread<0>();
    wread<1>();
    wread<2>();
    wread<3>();
    asm volatile("s_waitcnt lgkmcnt(0)");
  }
  DEVI void run() {
    units(std::make_integer_sequence<int, NV>{});
#pragma unroll
    for (int m = 0; m < 2; ++m)
#pragma unroll
      for (int n = 0; n < 9; ++n) asm volatile("" : "+v"(acc[m][n]));
  }
};

#define PACE 0
#define G0P 2
#define ZSLEEP 6
DEVI void p3_g0(char* smem, char* __restrict__ ws, int layer, int mi, int ni, int wm, int lane MK_ARG) {
  constexpr int SB = K3C::SB;
  const unsigned lds0 = (unsigned)(size_t)smem;
  const int g = lane >> 4, r = lane & 15;
  const unsigned lw = lds0 + (unsigned)(LW_OFF + layer * 4), me = 1u << (8 * wm);
  f32x4 ac[2][9];
#pragma unroll
  for (int mt = 0; mt < 2; ++mt)
#pragma unroll
    for (int j = 0; j < 9; ++j) ac[mt][j] = f32x4{0.f, 0.f, 0.f, 0.f};
  unsigned ra = lds0 + (unsigned)(K3_ST_OFF + (wm * 32 + g * 8) * 4), ca = lds0 + (unsigned)(K3_C2_OFF + r * 8);
  asm volatile("" : "+v"(ra), "+v"(ca));
  {
    s16x8 xc[4][2][2];
    UMath<false> gm(ac, xc);
    gm.xa = lds0 + (unsigned)(g * 2048 + (wm * 32 + 8 * (r >> 2) + (r & 3)) * 16);
    gm.wa = lds0 + (unsigned)(K3C::XB + (g * 8 + (r & 7)) * K3C::P + (r >> 3) * 16);
    asm volatile("" : "+v"(gm.xa), "+v"(gm.wa));
    bar();
    gm.first();
    gm.run();
  }
  lds_inc(lw + LW_AD * 4, me);
  MK(11);
  f32x2 z[9][4];
  f32x2 sm[4], sq[4];
  {
    f32x4 rc[4][2];
    f32x2 cs[9];
#pragma unroll
    for (int i = 0; i < 8; ++i)
      asm volatile("ds_read_b128 %0, %1 offset:%2" : "=v"(rc[i / 2][i % 2]) : "v"(ra), "n"((i / 2) * 512 + (i % 2) * 16) : "memory");
#pragma unroll
    for (int j = 0; j < 9; ++j) asm volatile("ds_read_b64 %0, %1 offset:%2" : "=v"(cs[j]) : "v"(ca), "n"(j * 128) : "memory");
    asm volatile("s_waitcnt lgkmcnt(0)"
                 : "+v"(rc[0][0]), "+v"(rc[0][1]), "+v"(rc[1][0]), "+v"(rc[1][1]), "+v"(rc[2][0]), "+v"(rc[2][1]), "+v"(rc[3][0]), "+v"(rc[3][1]));
#pragma unroll
    for (int j = 0; j < 9; ++j) asm volatile("" : "+v"(cs[j]));
    auto RC = [&](int k, int i) { return f32x2{rc[k][i / 2][(i % 2) * 2], rc[k][i / 2][(i % 2) * 2 + 1]}; };
#pragma unroll
    for (int j = 0; j < 9; ++j) {
      f32x2 u[4], w[4];
#pragma unroll
      for (int i = 0; i < 4; ++i) u[i] = __builtin_elementwise_fma(RC(0, i), cs[j], f32x2{ac[i / 2][j][(i % 2) * 2], ac[i / 2][j][(i % 2) * 2 + 1]});
#pragma unroll
      for (int i = 0; i < 4; ++i) w[i] = u[i] * u[i];
#pragma unroll
      for (int i = 0; i < 4; ++i) w[i] = __builtin_elementwise_fma(w[i], RC(3, i), RC(2, i));
#pragma unroll
      for (int i = 0; i < 4; ++i) w[i] = u[i] * w[i];
      __builtin_amdgcn_sched_barrier(0);
#pragma unroll
      for (int i = 0; i < 4; ++i) w[i] = f32x2{__builtin_amdgcn_exp2f(w[i][0]), __builtin_amdgcn_exp2f(w[i][1])};
      __builtin_amdgcn_sched_barrier(0);
#pragma unroll
      for (int i = 0; i < 4; ++i) w[i] = __builtin_elementwise_fma(w[i], RC(1, i), RC(1, i));
      __builtin_amdgcn_sched_barrier(0);
#pragma unroll
      for (int i = 0; i < 4; ++i) w[i] = f32x2{__builtin_amdgcn_rcpf(w[i][0]), __builtin_amdgcn_rcpf(w[i][1])};
      __builtin_amdgcn_sched_barrier(0);
#pragma unroll
      for (int i = 0; i < 4; ++i) z[j][i] = u[i] * w[i];
#pragma unroll
      for (int i = 0; i < 4; ++i) {
        if (j == 1) sm[i] = z[0][i] + z[1][i];
        if (j > 1) sm[i] += z[j][i];
      }
#pragma unroll
      for (int i = 0; i < 4; ++i) sq[i] = j ? __builtin_elementwise_fma(z[j][i], z[j][i], sq[i]) : z[j][i] * z[j][i];
      asm volatile("" : "+v"(z[j][0]), "+v"(z[j][1]), "+v"(z[j][2]), "+v"(z[j][3]));
      __builtin_amdgcn_sched_barrier(0);
    }
  }
  f32x4 e;
  {
    float v[4][4], d[2][4], f[4];
#pragma unroll
    for (int i = 0; i < 4; ++i) {
      v[i][0] = sm[i][0];
      v[i][1] = sq[i][0];
      v[i][2] = sm[i][1];
      v[i][3] = sq[i][1];
    }
    asm volatile("s_nop 1" : "+v"(v[3][1]), "+v"(v[3][3]), "+v"(v[2][1]), "+v"(v[2][3]));
#pragma unroll
    for (int i = 0; i < 2; ++i)
#pragma unroll
      for (int c = 0; c < 4; ++c)
        asm volatile("v_add_f32_dpp %0, %1, %1 row_ror:8 row_mask:0xf bank_mask:0x3\n\tv_add_f32_dpp %0, %2, %2 row_ror:8 row_mask:0xf bank_mask:0xc"
                     : "=&v"(d[i][c])
                     : "v"(v[i][c]), "v"(v[i + 2][c]));
#pragma unroll
    for (int c = 0; c < 4; ++c)
      asm volatile("v_add_f32_dpp %0, %1, %1 row_shl:4 row_mask:0xf bank_mask:0x5\n\tv_add_f32_dpp %0, %2, %2 row_shr:4 row_mask:0xf bank_mask:0xa"
                   : "=&v"(f[c])
                   : "v"(d[0][c]), "v"(d[1][c]));
    asm volatile(
        "v_add_f32_dpp %0, %0, %0 quad_perm:[1,0,3,2] row_mask:0xf bank_mask:0xf\n\t"
        "v_add_f32_dpp %1, %1, %1 quad_perm:[1,0,3,2] row_mask:0xf bank_mask:0xf\n\t"
        "v_add_f32_dpp %2, %2, %2 quad_perm:[1,0,3,2] row_mask:0xf bank_mask:0xf\n\t"
        "v_add_f32_dpp %3, %3, %3 quad_perm:[1,0,3,2] row_mask:0xf bank_mask:0xf\n\t"
        "v_add_f32_dpp %0, %0, %0 quad_perm:[2,3,0,1] row_mask:0xf bank_mask:0xf\n\t"
        "v_add_f32_dpp %1, %1, %1 quad_perm:[2,3,0,1] row_mask:0xf bank_mask:0xf\n\t"
        "v_add_f32_dpp %2, %2, %2 quad_perm:[2,3,0,1] row_mask:0xf bank_mask:0xf\n\t"
        "v_add_f32_dpp %3, %3, %3 quad_perm:[2,3,0,1] row_mask:0xf bank_mask:0xf"
        : "+v"(f[0]), "+v"(f[1]), "+v"(f[2]), "+v"(f[3]));
    e = f32x4{f[0], __uint_as_float(__float_as_uint(f[1]) & 0x7fffffffu), f[2], __uint_as_float(__float_as_uint(f[3]) & 0x7fffffffu)};
  }
  const char* zp = uni_ptr(ws + OFF_ZP + (size_t)layer * SZ_ZP + (size_t)mi * 16384 + wm * 256);
  if ((lane & 3) == 0)
    asm volatile("global_store_dwordx4 %0, %1, %2 sc1\n\ts_nop 1" ::"v"((unsigned)(ni * 1024 + lane * 4)), "v"(e), "s"(zp) : "memory");
  lds_inc(lw + LW_AD * 4, me);
  __builtin_amdgcn_s_setprio(2);
  MK(12);
  const char* dst = uni_ptr(ws + OFF_Z2 + (size_t)layer * SZ_Z + (size_t)ni * S_ * 288 + (size_t)(mi * 4 + wm) * 9216 + 4096);
  const unsigned lane16 = (unsigned)lane * 16u;
  u32x4 wb[9];
  {
    static_assert(b_k(7) == 2 && b_k(8) == 3, "order");
    const char* wp = uni_ptr(ws + OFF_PI + (size_t)ni * (18 * K3C::WB) + 11 * K3C::WB + wm * 9216 + 4096);
#pragma unroll
    for (int j = 0; j < 8; ++j) asm volatile("global_load_dwordx4 %0, %1, %2 offset:%3 sc1" : "=v"(wb[j]) : "v"(lane16), "s"(wp), "n"(j * 1024 - 4096) : "memory");
    asm volatile("global_load_dwordx4 %0, %1, %2 sc1" : "=v"(wb[8]) : "v"(lane16 + 4096u), "s"(wp) : "memory");
  }
  u32x4 y[9];
  {
    const unsigned src = (unsigned)(g * 4096 + r * 16);
    unsigned zw = lds0 + (unsigned)(K3_ZC_OFF + (wm * 32 + 2 * r + (g & 1)) * 4), zr = lds0 + (unsigned)(K3_ZC_OFF + (wm * 32 + g * 8) * 4);
    asm volatile("" : "+v"(zw), "+v"(zr));
    f32x4 a, b, c, d;
    __builtin_amdgcn_s_sleep(ZSLEEP);
#pragma unroll 1
    for (unsigned spin = 0; spin < SPIN_MEM; ++spin) {
      asm volatile(
          "global_load_dwordx4 %0, %4, %5 sc1\n\t"
          "global_load_dwordx4 %1, %4, %5 offset:1024 sc1\n\t"
          "global_load_dwordx4 %2, %4, %5 offset:2048 sc1\n\t"
          "global_load_dwordx4 %3, %4, %5 offset:3072 sc1\n\t"
          "s_waitcnt vmcnt(0)"
          : "=&v"(a), "=&v"(b), "=&v"(c), "=&v"(d)
          : "v"(src), "s"(zp)
          : "memory");
      const unsigned bad = (__float_as_uint(a[1]) | __float_as_uint(a[3]) | __float_as_uint(b[1]) | __float_as_uint(b[3]) |
                            __float_as_uint(c[1]) | __float_as_uint(c[3]) | __float_as_uint(d[1]) | __float_as_uint(d[3])) >> 31;
      if (!__any(bad)) break;
    }
    MK(13);
    float s, q, zmb, zi;
    red16(a, b, c, d, s, q);
    ln_finish(s, q, 1.0f / (float)FH_, zmb, zi);
    f32x4 R[2], M[2];
    asm volatile(
        "ds_write2st64_b32 %4, %5, %6 offset1:2\n\t"
        "ds_read_b128 %0, %7\n\tds_read_b128 %2, %7 offset:512\n\tds_read_b128 %1, %7 offset:16\n\tds_read_b128 %3, %7 offset:528\n\t"
        "s_waitcnt lgkmcnt(0)"
        : "=&v"(R[0]), "=&v"(R[1]), "=&v"(M[0]), "=&v"(M[1])
        : "v"(zw), "v"(zi), "v"(-zmb * zi), "v"(zr)
        : "memory");
    MK(28);
#define NRM 0
    auto st = [&](int j) {
      if (j < 8) asm volatile("global_store_dwordx4 %0, %1, %2 offset:%3 sc1\n\ts_nop 1" ::"v"(lane16), "v"(y[j]), "s"(dst), "n"(j * 1024 - 4096) : "memory");
      else asm volatile("global_store_dwordx4 %0, %1, %2 sc1\n\ts_nop 1" ::"v"(lane16 + 4096u), "v"(y[j]), "s"(dst) : "memory");
    };
#pragma unroll
    for (int j = 0; j < 9; ++j) {
      if (NRM & 2) {
        f32x2 t[4];
#pragma unroll
        for (int k = 0; k < 4; ++k)
          t[k] = __builtin_elementwise_fma(z[j][k], f32x2{R[k / 2][(k % 2) * 2], R[k / 2][(k % 2) * 2 + 1]}, f32x2{M[k / 2][(k % 2) * 2], M[k / 2][(k % 2) * 2 + 1]});
        y[j] = u32x4{pack2v(t[0]), pack2v(t[1]), pack2v(t[2]), pack2v(t[3])};
      } else {
        float t[8];
#pragma unroll
        for (int k = 0; k < 8; ++k) {
          t[k] = __builtin_fmaf(z[j][k / 2][k % 2], R[k / 4][k % 4], M[k / 4][k % 4]);
          asm volatile("" : "+v"(t[k]));
        }
        y[j] = u32x4{pack2(t[0], t[1]), pack2(t[2], t[3]), pack2(t[4], t[5]), pack2(t[6], t[7])};
      }
      if (NRM & 1) asm volatile("" : "+v"(y[j]));
      else st(j);
      __builtin_amdgcn_sched_barrier(0);
    }
    MK(29);
    if (NRM & 1) {
#pragma unroll
      for (int j = 0; j < 9; ++j) st(j);
    }
  }
  lds_inc(lw + LW_AD * 4, me);
  MK(24);
  __builtin_amdgcn_s_setprio(G0P);
  lds_spin4(lw + LW_S * 4, 4u + (unsigned)(wm >> 1));
  {
    const unsigned fa = lds0 + (unsigned)((wm >> 1) * SB + K3C::XB + (wm & 1) * 9216) + lane16;
#pragma unroll
    for (int j = 0; j < 9; ++j) {
      asm volatile("ds_write_b128 %0, %1 offset:%2" ::"v"(fa), "v"(wb[j]), "n"(j * 1024) : "memory");
      if (PACE && j < 8) __builtin_amdgcn_s_sleep(PACE);
    }
    lds_inc(lw + LW_AD * 4, me);
  }
  announce(lw + LW_Z * 4, 4u, ws, CN_Z + layer, lane * 16 + ni, lane < 16);
  lds_inc(lw + LW_AD * 4, me);
  MK(27);
  f32x4 acc[2][9];
  {
    float bs[2];
    asm volatile("ds_read2_b32 %0, %1 offset1:16\n\ts_waitcnt lgkmcnt(0)"
                 : "=v"(*reinterpret_cast<f32x2*>(bs))
                 : "v"(lds0 + (unsigned)(K3_BS_OFF + (wm * 32 + r) * 4))
                 : "memory");
    lds_spin4(lw + LW_S * 4, 6u + (unsigned)(wm >> 1));
    const unsigned fa = lds0 + (unsigned)((2 + (wm >> 1)) * SB + K3C::XB + (wm & 1) * 9216) + lane16;
#pragma unroll
    for (int j = 0; j < 9; ++j) {
      asm volatile("ds_write_b128 %0, %1 offset:%2" ::"v"(fa), "v"(y[j]), "n"(j * 1024) : "memory");
      if (PACE && j < 8) __builtin_amdgcn_s_sleep(PACE);
    }
    lds_inc(lw + LW_AD * 4, me);
#pragma unroll
    for (int mt = 0; mt < 2; ++mt)
#pragma unroll
      for (int j = 0; j < 9; ++j) acc[mt][j] = f32x4{bs[mt], bs[mt], bs[mt], bs[mt]};
  }
  u32x4 z1[9];
  {
    GemmMath<K3C, 2, 9, K3_D, 6, false, 2> gm(acc);
    gm.setup(lds0, wm * 2, 0, lane, (unsigned)(2 * SB));
    MK(15);
    lds_spin4(lw + LW_AD * 4, 6u);
    lds_spin4(lw + LW_L * 4, 8u);
    MK(17);
    gm.first();
    __builtin_amdgcn_s_setprio(0);
    gm.template mid<1, false>();
    gm.template mid<11>();
    const unsigned za = gm.wrd + (unsigned)(K3_Z1_OFF - K3C::XB + wm * 5120);
#pragma unroll
    for (int j = 0; j < 5; ++j) asm volatile("ds_read_b128 %0, %1 offset:%2" : "=v"(z1[j]) : "v"(za), "n"(j * 1024));
    gm.template mid<10>();
#pragma unroll
    for (int j = 5; j < 9; ++j) asm volatile("ds_read_b128 %0, %1 offset:%2" : "=v"(z1[j]) : "v"(za), "n"((j - 5) * 1024));
    gm.template mid<9>();
    gm.last();
  }
  MK(16);
  const char* hp = uni_ptr(ws + OFF_H + (size_t)layer * SZ_Z + (size_t)((mi * 2 + (wm >> 1)) * 288 + ni * 18) * 1024);
  const int l2 = lane_id();
  const unsigned ho = (unsigned)((l2 >> 5) * 1024 + ((wm & 1) * 32 + (l2 & 31)) * 16);
#pragma unroll
  for (int j = 0; j < 9; ++j) {
    asm volatile("" : "+v"(z1[j]));
    u32x2 h[2];
#pragma unroll
    for (int mt = 0; mt < 2; ++mt) {
      const f32x4 a = acc[mt][j];
      h[mt] = u32x2{pack2v(f32x2{bf_lo(z1[j][2 * mt]), bf_hi(z1[j][2 * mt])} * f32x2{a[0], a[1]}),
                    pack2v(f32x2{bf_lo(z1[j][2 * mt + 1]), bf_hi(z1[j][2 * mt + 1])} * f32x2{a[2], a[3]})};
    }
    const auto s0 = __builtin_amdgcn_permlane16_swap(h[0][0], h[1][0], false, false);
    const auto s1 = __builtin_amdgcn_permlane16_swap(h[0][1], h[1][1], false, false);
    const u32x4 v = {s0[0], s1[0], s0[1], s1[1]};
    asm volatile("global_store_dwordx4 %0, %1, %2 offset:%3 sc1\n\ts_nop 1" ::"v"(ho), "v"(v), "s"(hp + (j / 2) * 4096), "n"((j % 2) * 2048) : "memory");
    __builtin_amdgcn_sched_barrier(0);
  }
  MK(18);
  announce(lds0 + (unsigned)(LW_OFF + (LW_H + layer * 2 + (wm >> 1)) * 4), 2u, ws, ((HSPLIT && ni >= 8) ? CN_H2 : CN_H) + layer,
           (mi * 2 + (wm >> 1)) * 8 + l2, l2 < 8);
  MK(19);
}

DEVI void k3_const(char* smem, char* ws, const u16* b_sp, int layer, int bid, int mi, int ni, int wm) {
  const int lane = lane_id(), l = lane < 54 ? lane : 53, p = l / 18;
  const char* kp = uni_ptr(ws + OFF_CSK + bid * 2048 + wm * 512);
  const unsigned q16 = (unsigned)(l - p * 18) * 16u;
  unsigned bs;
  f32x4 t;
  asm volatile("global_load_ushort %0, %1, %2" : "=v"(bs) : "v"((unsigned)(lane & 31) * 2u), "s"(uni_ptr((const char*)(b_sp + mi * 128 + wm * 32))) : "memory");
  if (layer) {
    asm volatile("global_load_dwordx4 %0, %1, %2 sc1\n\ts_waitcnt vmcnt(0)" : "=&v"(t) : "v"(q16), "s"(kp) : "memory");
  } else {
    const char* cp = uni_ptr(ws + OFF_CSP + ni * 1152 + wm * 288);
    f32x4 a[6];
#pragma unroll
    for (int i = 0; i < 6; ++i)
      asm volatile("global_load_dwordx4 %0, %1, %2 sc1" : "=v"(a[i]) : "v"(q16 + (unsigned)(p * (6 * F_ * 4))), "s"(cp + i * (F_ * 4)) : "memory");
    asm volatile("s_waitcnt vmcnt(0)" : "+v"(a[0]), "+v"(a[1]), "+v"(a[2]), "+v"(a[3]), "+v"(a[4]), "+v"(a[5]));
    t = ((a[0] + a[1]) + (a[2] + a[3])) + (a[4] + a[5]);
#pragma unroll
    for (int j = 0; j < 4; ++j)
      t[j] += __int_as_float(__builtin_amdgcn_ds_bpermute((lane + 18) * 4, __float_as_int(t[j]))) +
              __int_as_float(__builtin_amdgcn_ds_bpermute((lane + 36) * 4, __float_as_int(t[j])));
    if (lane < 24) asm volatile("global_store_dwordx4 %0, %1, %2\n\ts_nop 1" ::"v"((unsigned)lane * 16u), "v"(t), "s"(kp) : "memory");
  }
  asm volatile("" : "+v"(bs));
  if (lane < 18) {
    if (wm < 2) {
      *reinterpret_cast<f32x4*>(smem + K3_C2_OFF + wm * 576 + lane * 32) = f32x4{t[0], t[0], t[1], t[1]};
      *reinterpret_cast<f32x4*>(smem + K3_C2_OFF + wm * 576 + lane * 32 + 16) = f32x4{t[2], t[2], t[3], t[3]};
    } else {
      *reinterpret_cast<f32x4*>(smem + K3_C1_OFF + (wm - 2) * 288 + lane * 16) = t;
    }
  }
  if (lane < 32) *reinterpret_cast<float*>(smem + K3_BS_OFF + (wm * 32 + lane) * 4) = bf_lo(bs);
}

DEVI void p3_g1(char* smem, char* __restrict__ ws, const char* code0, int bid, int layer, int mi, int wm, int lane MK_ARG) {
  const unsigned lds0 = (unsigned)(size_t)smem;
  const int g = lane >> 4, r = lane & 15;
  const unsigned lw = lds0 + (unsigned)(LW_OFF + layer * 4);
  const unsigned xrd = lds0 + (unsigned)(g * 2048 + (wm * 32 + r) * 16);
  s16x8 xc[4][2][2];
  {
    const char* xs = uni_ptr(ws + OFF_XS + (size_t)layer * 2 * SZ_XS + (size_t)(mi * 2 + (wm >> 1)) * 4096 + (wm & 1) * 256);
    f32x4 a, b;
    auto pick = [&](int t) {
      bar();
      asm volatile(
          "ds_read_b128 %0, %4\n\tds_read_b128 %1, %4 offset:256\n\tds_read_b128 %2, %4 offset:8192\n\tds_read_b128 %3, %4 offset:8448\n\t"
          "s_waitcnt lgkmcnt(0)"
          : "=&v"(xc[t][0][0]), "=&v"(xc[t][0][1]), "=&v"(xc[t][1][0]), "=&v"(xc[t][1][1])
          : "v"(xrd + (unsigned)(t * K3C::SB))
          : "memory");
    };
#define G1P 2
    __builtin_amdgcn_s_setprio(G1P);
    pick(0);
    pick(1);
    asm volatile("global_load_dwordx4 %0, %2, %3 sc1\n\tglobal_load_dwordx4 %1, %2, %3 offset:512 sc1"
                 : "=&v"(a), "=&v"(b)
                 : "v"((unsigned)(g * 1024 + r * 16)), "s"(xs)
                 : "memory");
    pick(2);
    pick(3);
    bar();
    asm volatile("s_waitcnt vmcnt(0)" : "+v"(a), "+v"(b) : : "memory");
    float s, q, mb, rinv;
    red8(a, b, s, q);
    bar();
    asm volatile("" : "+v"(s), "+v"(q));
    ln_finish(s, q, 1.0f / (float)D_, mb, rinv);
    asm volatile("" : "+v"(mb), "+v"(rinv));
    bar();
    asm volatile("" : "+v"(mb), "+v"(rinv));
    const float k0 = -2.0f * 0.7978845608028654f * 1.4426950408889634f, k1 = k0 * 0.044715f;
    float sd = __builtin_amdgcn_rcpf(rinv), c0 = k0 * rinv, c1 = k1 * rinv * rinv * rinv;
    asm volatile("" : "+v"(sd));
    asm volatile("" : "+v"(c0));
    asm volatile("" : "+v"(c1));
    const float e0 = lane < 32 ? -mb : c0, e1 = lane < 32 ? sd : c1;
    asm volatile("ds_write2st64_b32 %0, %1, %2 offset1:2" ::"v"(
                     lds0 + (unsigned)(K3_ST_OFF + (g >> 1) * 1024 + (wm * 32 + 2 * r + (g & 1)) * 4)),
                 "v"(e0), "v"(e1)
                 : "memory");
    bar();
    asm volatile("s_waitcnt lgkmcnt(0)" ::: "memory");
    bar();
    __builtin_amdgcn_s_setprio(0);
  }
  f32x4 ac[2][9];
#pragma unroll
  for (int mt = 0; mt < 2; ++mt)
#pragma unroll
    for (int j = 0; j < 9; ++j) ac[mt][j] = f32x4{0.f, 0.f, 0.f, 0.f};
  {
    UMath<true> gm(ac, xc);
    gm.xa = xrd;
    gm.wa = lds0 + (unsigned)(K3C::XB + (g * 8 + (r & 7)) * K3C::P + (r >> 3) * 16);
    gm.fa = lw + (unsigned)(LW_AD * 4 + (lane & 3));
    gm.fw = lw + (unsigned)(LW_S * 4 + wm);
    gm.fv = 0u;
    asm volatile("" : "+v"(gm.fa), "+v"(gm.fw), "+v"(gm.fv), "+v"(gm.xa), "+v"(gm.wa));
    lds_spin4<4>(lw + LW_L * 4, 4u);
    gm.first();
    lds_spin4(lw + LW_AD * 4, 2u);
#define BPR 1
    __builtin_amdgcn_s_setprio(BPR);
    gm.run();
    __builtin_amdgcn_s_setprio(0);
  }
  MK(14);
  f32x2 rc[4];
  f32x4 cs[9];
  const int l2 = lane_id();
  {
    const unsigned ra = lds0 + (unsigned)(K3_ST_OFF + (wm * 32 + (l2 & 15)) * 4), ca = lds0 + (unsigned)(K3_C1_OFF + (l2 & 48));
#pragma unroll
    for (int k = 0; k < 4; ++k) asm volatile("ds_read2_b32 %0, %1 offset1:16" : "=v"(rc[k]) : "v"(ra + (unsigned)(k * 512)) : "memory");
#pragma unroll
    for (int j = 0; j < 9; ++j) asm volatile("ds_read_b128 %0, %1 offset:%2" : "=v"(cs[j]) : "v"(ca), "n"(j * 64) : "memory");
    asm volatile("s_waitcnt lgkmcnt(0)" : "+v"(rc[0]), "+v"(rc[1]), "+v"(rc[2]), "+v"(rc[3]));
#pragma unroll
    for (int j = 0; j < 9; ++j) asm volatile("" : "+v"(cs[j]));
  }
  const unsigned za = lds0 + (unsigned)(K3_Z1_OFF + wm * 5120 + l2 * 16);
  u32x4 z1[9];
#pragma unroll
  for (int t = 0; t < 18; ++t) {
    const int j = t / 2, mt = t % 2;
    float u[4], w[4];
#pragma unroll
    for (int i = 0; i < 4; ++i) asm volatile("v_fma_f32 %0, %1, %2, %3" : "=v"(u[i]) : "v"(rc[0][mt]), "v"(cs[j][i]), "v"(ac[mt][j][i]));
#pragma unroll
    for (int i = 0; i < 4; ++i) asm volatile("v_mul_f32 %0, %1, %1" : "=v"(w[i]) : "v"(u[i]));
#pragma unroll
    for (int i = 0; i < 4; ++i) asm volatile("v_fma_f32 %0, %0, %1, %2" : "+v"(w[i]) : "v"(rc[3][mt]), "v"(rc[2][mt]));
#pragma unroll
    for (int i = 0; i < 4; ++i) asm volatile("v_mul_f32 %0, %0, %1" : "+v"(w[i]) : "v"(u[i]));
#pragma unroll
    for (int i = 0; i < 4; ++i) asm volatile("v_exp_f32 %0, %0" : "+v"(w[i]));
#pragma unroll
    for (int i = 0; i < 4; ++i) asm volatile("v_fma_f32 %0, %0, %1, %1" : "+v"(w[i]) : "v"(rc[1][mt]));
#pragma unroll
    for (int i = 0; i < 4; ++i) asm volatile("v_rcp_f32 %0, %0" : "+v"(w[i]));
#pragma unroll
    for (int i = 0; i < 4; ++i) asm volatile("v_mul_f32 %0, %0, %1" : "+v"(w[i]) : "v"(u[i]));
    z1[j][2 * mt] = pack2(w[0], w[1]);
    z1[j][2 * mt + 1] = pack2(w[2], w[3]);
    if (mt && j < 5) asm volatile("ds_write_b128 %0, %1 offset:%2" ::"v"(za), "v"(z1[j]), "n"(j * 1024) : "memory");
    if (t == 17) {
#pragma unroll
      for (int k = 5; k < 9; ++k) asm volatile("ds_write_b128 %0, %1 offset:%2" ::"v"(za), "v"(z1[k]), "n"((k - 5) * 1024) : "memory");
    }
    if (t == 9 || t == 17) asm volatile("s_waitcnt lgkmcnt(0)" ::: "memory");
    __builtin_amdgcn_sched_barrier(0);
    bar();
    __builtin_amdgcn_sched_barrier(0);
  }
  MK(15);
#pragma unroll 1
  for (int t = 20; t < 32; ++t) bar();
  if ((RTC & 2) && wm == 2) code_again(code0, bid);
}

constexpr int W5A = 22, W5B = 26;
constexpr int NPF = 30;
struct Loader {
  u32x4 pf[NPF];
  unsigned lds0, lane16;
  int wl, lane;

  template <int R0, int N, int P0, int PE>
  DEVI void pf_load(const char* src) {
    const unsigned o = lane16 + (unsigned)(wl * 1024);
#pragma unroll
    for (int t = 0; t < N; ++t) {
      unsigned a = o + (unsigned)((P0 + 4 * t) * 1024);
      if (P0 + 4 * t + 3 >= PE && P0 + 4 * t + wl >= PE) a = lane16;
      asm volatile("global_load_dwordx4 %0, %1, %2 sc1" : "=v"(pf[R0 + t]) : "v"(a), "s"(src) : "memory");
    }
  }
  template <int R0, int N>
  DEVI void pf_pin() {
#pragma unroll
    for (int t = 0; t < N; ++t) asm volatile("" : "+v"(pf[R0 + t]));
  }
  template <int R0, int N>
  DEVI void pf_undef() {
#pragma unroll
    for (int t = 0; t < N; ++t) asm volatile("" : "=v"(pf[R0 + t]));
  }
  template <int PB, int A, int N, int SL = 0>
  DEVI void pf_put(unsigned dst) {
    constexpr int T0 = (A - PB) / 4, T1 = (A + N - 1 - PB) / 4;
    const unsigned a = lds0 + dst + lane16 + (unsigned)((wl + PB + 4 * T0 - A + 4) * 1024) - 4096u;
#pragma unroll
    for (int t = T0; t <= T1; ++t) {
      const int p0 = PB + 4 * t;
      if ((p0 >= A || p0 + wl >= A) && (p0 + 3 < A + N || p0 + wl < A + N))
        asm volatile("ds_write_b128 %0, %1 offset:%2" ::"v"(a), "v"(pf[t]), "n"((t - T0) * 4096) : "memory");
      if (SL) __builtin_amdgcn_s_sleep(SL);
    }
  }
  template <int N>
  DEVI void dma_run(const char* src, unsigned dst) {
    const unsigned o = lane16 + (unsigned)(wl * 1024), m = lds0 + dst + (unsigned)(wl * 1024);
#pragma unroll
    for (int i = 0; i < (N + 3) / 4; ++i)
      if (4 * i + 3 < N || 4 * i + wl < N) dma16_sc1(src + i * 4096, o, m + (unsigned)(i * 4096));
  }
  template <int V>
  DEVI void wait_w() {
    asm volatile("s_cmp_lt_u32 %0, 2\n\ts_cbranch_scc1 1f\n\ts_waitcnt vmcnt(%1)\n1:\n\ts_waitcnt vmcnt(%2)" ::"s"(wl), "n"(V - 1), "n"(V) : "memory", "scc");
  }
  template <int V>
  DEVI void wbar() {
    asm volatile("s_waitcnt vmcnt(%0) lgkmcnt(0)" ::"n"(V) : "memory");
    bar();
  }

  DEVI void p1(char* smem, char* ws, int rt, int hp MK_ARG) {
    constexpr int SB = K1C::SB;
    static_assert(K1C::XB == 18 * 1024 && K1C::WB == 14 * 1024, "K1 shape");
    const char* wp = uni_ptr(ws + OFF_PQ + (size_t)hp * (9 * K1C::WB) + 4 * K1C::WB);
    static_assert(3 * SB + K1C::XB >= P5_W + 5 * 18432, "free while the GEMM before ends");
    const char* wq = wp - 4 * K1C::WB;
    dma_run<14>(wq + 3 * K1C::WB, (unsigned)(3 * SB + K1C::XB));
    dma_run<28>(wp, (unsigned)K1_WX);
    if (P1PR & 1) __builtin_amdgcn_s_setprio(0);
    if (P1PR & 2) wait_vm<0>();
    const bool lo = wl < 2;
    const int x0 = lo ? wl * 5 : wl * 4 + 2, w0 = lo ? wl * 3 : wl * 4 - 2;
    unsigned xo[5];
#pragma unroll
    for (int i = 0; i < 5; ++i) {
      const int c = (x0 + i) * 64 + lane;
      const int ch = c / 144;
      int gr = rt * 128 - 16 + (c - ch * 144);
      if (gr < 0) gr = 0;
      xo[i] = (unsigned)(((gr >> 6) * 72 + ch) * 1024 + (gr & 63) * 16);
      asm volatile("" : "+v"(xo[i]));
    }
    const char* xp = uni_ptr(ws + OFF_X1 + SZ_X);
    wp += 2 * K1C::WB + w0 * 1024;
    auto dma_x = [&](unsigned so) {
      const unsigned m = lds0 + so + (unsigned)(x0 * 1024);
#pragma unroll
      for (int i = 0; i < 4; ++i) dma16_sc1(xp, xo[i], m + (unsigned)(i * 1024));
      if (lo) dma16_sc1(xp, xo[4], m + 4096u);
      xp += 8192;
    };
    auto dma_w = [&](unsigned so) {
      const unsigned m = lds0 + so + (unsigned)(K1C::XB + w0 * 1024);
#pragma unroll
      for (int i = 0; i < 3; ++i) dma16_sc1(wp + i * 1024, lane16, m + (unsigned)(i * 1024));
      if (!lo) dma16_sc1(wp + 3072, lane16, m + 3072u);
      wp += K1C::WB;
    };
    const char* xs = uni_ptr(ws + OFF_XS + SZ_XS);
    const int g = lane >> 4, p2 = 64 + (lane & 7);
    int ra = rt * 128 - 16 + wl * 32 + 2 * (lane & 15);
    if (ra < 0) ra = 0;
    unsigned sa = (unsigned)(((ra >> 6) * 8 + g * 2) * 512 + (ra & 63) * 8);
    unsigned sb = (unsigned)(((rt * 2 + 1) * 8 + (lane >> 3)) * 512 + (2 * p2 - 80) * 8);
    unsigned sw = lds0 + (unsigned)(K1_ST_OFF + (2 * (lane < 32 ? wl * 16 + (lane & 15) : p2) + (g & 1)) * 8);
    asm volatile("" : "+v"(sa), "+v"(sb), "+v"(sw));
    MK(22);
    __builtin_amdgcn_s_setprio(3);
    bar();
    dma_run<14>(wq, (unsigned)K1C::XB);
    dma_run<14>(wq + K1C::WB, (unsigned)(SB + K1C::XB));
    dma_run<14>(wq + 2 * K1C::WB, (unsigned)(2 * SB + K1C::XB));
    MK(1);
    bar();
    dma_x(0u);
    dma_x((unsigned)SB);
    wbar<4>();
    MK(29);
    dma_x((unsigned)(2 * SB));
    dma_x((unsigned)(3 * SB));
    wbar<8>();
    MX(1, 3);
    dma_x(0u);
    wbar<8>();
    MX(1, 4);
    dma_x((unsigned)SB);
    f32x4 a, b, e;
    asm volatile("global_load_dwordx4 %0, %3, %5 sc1\n\tglobal_load_dwordx4 %1, %3, %5 offset:512 sc1\n\tglobal_load_dwordx4 %2, %4, %5 sc1"
                 : "=&v"(a), "=&v"(b), "=&v"(e)
                 : "v"(sa), "v"(sb), "s"(xs)
                 : "memory");
    wbar<11>();
    MX(1, 5);
    dma_w((unsigned)SB);
    dma_x((unsigned)(2 * SB));
    wbar<15>();
    MX(1, 6);
    dma_w((unsigned)(2 * SB));
    dma_x((unsigned)(3 * SB));
    wbar<19>();
    MX(1, 7);
    dma_w((unsigned)(3 * SB));
    dma_x(0u);
    wbar<16>();
    MX(1, 8);
    asm volatile("" : "+v"(a), "+v"(b), "+v"(e));
    float s, q;
    {
      float t[4] = {e[0], e[1], e[2], e[3]}, s2, q2;
      asm volatile(
          "v_add_f32_dpp %0, %0, %0 row_ror:8 row_mask:0xf bank_mask:0xf\n\tv_add_f32_dpp %1, %1, %1 row_ror:8 row_mask:0xf bank_mask:0xf\n\t"
          "v_add_f32_dpp %2, %2, %2 row_ror:8 row_mask:0xf bank_mask:0xf\n\tv_add_f32_dpp %3, %3, %3 row_ror:8 row_mask:0xf bank_mask:0xf\n\ts_nop 1"
          : "+v"(t[0]), "+v"(t[1]), "+v"(t[2]), "+v"(t[3]));
      red4(t, s2, q2);
      red8(a, b, s, q);
      s = lane < 32 ? s : s2;
      q = lane < 32 ? q : q2;
      asm volatile("" : "+v"(s), "+v"(q));
    }
    wbar<8>();
    MX(1, 9);
    {
      asm volatile("" : "+v"(s), "+v"(q));
      float mb, rinv;
      ln_finish(s, q, 1.0f / (float)D_, mb, rinv);
      asm volatile("ds_write_b64 %0, %1" ::"v"(sw), "v"(f32x2{-mb, rinv}) : "memory");
    }
    wbar<0>();
    MK(30);
  }

  template <int I>
  DEVI void p5_step(const char* wp) {
    constexpr int T = 2 * I + 1;
    wbar<(T == 1 ? 63 : (T <= 11 ? 3 : 0))>();
    if constexpr (T <= 9) {
      if (!G1W) {
        const unsigned wo = (unsigned)(12288 + (wl & 1) * 3072), so = (unsigned)(P5_W + ((T + 2 + (wl >> 1)) & 7) * 18432);
#pragma unroll
        for (int k = 0; k < 3; ++k) dma16_sc1(wp + (T + 7) * 18432 + k * 1024, lane16 + wo + (unsigned)((wl >> 1) * 18432), lds0 + so + wo + (unsigned)(k * 1024));
      }
      pf_put<0, 12 * (T - 1), 12, P5PACE>((unsigned)(P5_W + ((T + 2) % 8) * 18432));
      pf_put<0, 12 * T, 12, P5PACE>((unsigned)(P5_W + ((T + 3) % 8) * 18432));
    }
  }
  template <int... S>
  DEVI void p5_steps(const char* wp, std::integer_sequence<int, S...>) {
    (p5_step<S>(wp), ...);
  }

  template <int T0, int T1>
  DEVI void w5_load(const char* w5p) {
    const unsigned o = lane16 + (unsigned)(wl * 1024);
#pragma unroll
    for (int t = T0; t < T1; ++t)
      asm volatile("global_load_dwordx4 %0, %1, %2 sc1" : "=v"(pf[t]) : "v"(o + (unsigned)((t / 3 + 8) * 18432 + (t % 3) * 4096)), "s"(w5p) : "memory");
  }
  DEVI void p35(char* smem, char* ws, const u16* w_sp, int layer, int bid, int mi, int ni, int rb, int ct, int hp MK_ARG) {
    using C = K3C;
    constexpr int NS = C::NS, SB = C::SB, NDX = 4, NDW = 5, VPS = NDX + NDW;
    const char* wip = uni_ptr(ws + OFF_PI + (size_t)ni * (18 * C::WB));
    if (layer || L0REG) {
      pf_put<-48, 36, 18>((unsigned)(2 * SB + C::XB));
      pf_put<-48, 54, 18>((unsigned)(3 * SB + C::XB));
    } else {
      dma_run<18>(wip + 2 * C::WB, (unsigned)(2 * SB + C::XB));
      dma_run<18>(wip + 3 * C::WB, (unsigned)(3 * SB + C::XB));
    }
    const char* w5p = uni_ptr(ws + OFF_PW + (size_t)ct * (36 * 9216));
    const int w0 = wl < 2 ? wl * 5120 : wl * 4096 + 2048;
    const unsigned m0x = lds0 + (unsigned)(wl * 4096), m0w = lds0 + (unsigned)(C::XB + w0);
    const unsigned wo = lane16 + (unsigned)w0;
    const unsigned lw = lds0 + (unsigned)(LW_OFF + layer * 4), me = 1u << (8 * wl);
    auto dma_w = [&](const char* wp, unsigned so) {
#pragma unroll
      for (int i = 0; i < NDW; ++i) {
        if (i < NDW - 1 || wl < 2) dma16_sc1(wp, wo + (unsigned)(i * 1024), m0w + so + (unsigned)(i * 1024));
      }
    };
    {
      const char* xp = uni_ptr(ws + OFF_X1 + (size_t)layer * 2 * SZ_X + (size_t)(mi * 2 * 72 + wl * 2) * 1024);
      const unsigned xo1 = lane16 + 72u * 1024u;
      auto dma_x = [&](int k, unsigned so) {
        const char* p = xp + k * 8192;
        dma16_sc1(p, lane16, m0x + so);
        dma16_sc1(p, xo1, m0x + so + 1024u);
        dma16_sc1(p + 1024, lane16, m0x + so + 2048u);
        dma16_sc1(p + 1024, xo1, m0x + so + 3072u);
      };
      if (!MPOLL && wl == 0) cnt_wait(ws, CN_X1 + layer, mi * 16 + ni, 16u);
      MK(10);
      bar();
      dma_x(0, 0u);
      dma_x(1, (unsigned)SB);
      wait_vm<4>(); MX(3, 2); wbar<4>(); MX(2, 2);
      MK(25);
      dma_x(2, (unsigned)(2 * SB));
      dma_x(3, (unsigned)(3 * SB));
      dma_x(4, (unsigned)(4 * SB));
      wbar<12>(); MX(2, 3);
      dma_x(5, 0u);
      wait_vm<12>();
      bar(); MX(2, 4);
      dma_w(wip + 4 * C::WB, 0u);
      dma_x(6, (unsigned)SB);
      wait_w<8 + VPS>();
#pragma unroll 1
      for (int t = 1; t < 3; ++t) {
        bar(); MX(2, 4 + t);
        dma_w(wip + (t + 4) * C::WB, (unsigned)(t * SB));
        dma_x(t + 6, (unsigned)((t + 1) * SB));
        wait_w<NDX + VPS>();
      }
      bar(); MX(2, 7);
      dma_w(wip + 7 * C::WB, (unsigned)(3 * SB));
      wait_w<VPS>();
      bar(); MX(2, 8);
      dma_w(wip + 8 * C::WB, 0u);
      wait_w<NDW>();
      bar(); MX(2, 9);
      const char* wp = wip + 9 * C::WB;
      dma_w(wp + b_k(0) * C::WB, (unsigned)SB);
      if (layer == 0) {
        const char* sp = uni_ptr((const char*)w_sp + (size_t)(bid * 4 + wl) * 8192);
        const unsigned so = (unsigned)((lane >> 5) * 4096 + (lane & 31) * 16);
#pragma unroll
        for (int i = 0; i < WSP_V; ++i) dma16(sp + i * 512, so, lds0 + (unsigned)K3_DM_OFF);
        dma16(sp + (bid >> 4) * 256, so & ~0x100u, lds0 + (unsigned)K3_DM_OFF);
      }
      w5_load<0, 4>(w5p);
      wait_w<NDW + 4>();
      MX(3, 10); bar(); MX(2, 10);
      dma_w(wp + b_k(1) * C::WB, (unsigned)(2 * SB));
      w5_load<4, 8>(w5p);
      MK(26);
      lds_spin4(lw + LW_AD * 4, 1u);
      dma_w(wp + b_k(2) * C::WB, (unsigned)(3 * SB));
      dma_w(wp + b_k(3) * C::WB, 0u);
#define TCH 0
      if (TCH & 1) {
        const int li = wl * 54 + (lane < 54 ? lane : 53);
        touch64(wp + C::WB, (unsigned)((mi & 7) * 13824 + li * 64), lds0 + (unsigned)K3_DM_OFF);
      }
      w5_load<8, W5A>(w5p);
      wait_vm<W5A - 8>();
      lds_inc(lw + LW_L * 4, 4u * me);
      w5_load<W5A, 30>(w5p);
      MK(11);
#define LSL 2
      static_for<4, 7>([&](auto T_) {
        constexpr int t = T_;
        lds_spin4<LSL>(lw + LW_S * 4, (unsigned)(t - 3));
        MX(2, 12 + t);
        dma_w(wp + b_k(t) * C::WB, (unsigned)(((t + 1) % 4) * SB));
        if constexpr (t > 4) {
          wait_w<NDW>();
          lds_inc(lw + LW_L * 4, me);
        }
      });
      MX(2, 19);
      wait_vm<0>();
      lds_inc(lw + LW_L * 4, me);
      pf_pin<0, 30>();
      MK(13);
    }
    {
      unsigned xoff[NDX];
#pragma unroll
      for (int i = 0; i < NDX; ++i) {
        const int c = wl * 256 + i * 64 + lane;
        const int row = c >> 3, cc = c & 7;
        xoff[i] = (unsigned)((mi * 128 + row) * S_ * 2) + (unsigned)((cc ^ ((row >> 1) & 7)) * 16);
      }
      auto dma_x = [&](const char* xp, unsigned so) {
#pragma unroll
        for (int i = 0; i < NDX; ++i) dma16(xp, xoff[i], m0x + so + (unsigned)(i * 1024));
      };
      const char* xp = uni_ptr((const char*)w_sp);
      const char* wp = uni_ptr(ws + OFF_Z2 + (size_t)layer * SZ_Z + (size_t)ni * S_ * 288);
#define XEARLY 0
      if (!XEARLY) lds_spin4<LSL>(lw + LW_AD * 4, 5u);
      dma_x(xp + mi * 256, (unsigned)(2 * SB));
      dma_x(xp + mi * 256 + 128, (unsigned)(3 * SB));
      if (XEARLY) lds_spin4<LSL>(lw + LW_AD * 4, 5u);
      MK(24);
      int left = 2 * mi;
      auto next = [&]() {
        const int d = --left == 0 ? 3 : 1;
        xp += d * 128;
        wp += d * C::WB;
      };
      if (mi == 0) {
        xp += 256;
        wp += 2 * C::WB;
      }
      dma_x(xp, 0u);
      dma_x(xp + 128, (unsigned)SB);
      wait_vm<2 * NDX>();
      lds_inc(lw + LW_L * 4, me);
      MK(27);
      if (wl == 0) {
        cnt_wait(ws, CN_Z + layer, mi * 16 + ni, 16u);
        lds_wr32(lw + LW_GO * 4, 1u);
      } else {
        lds_spin(lw + LW_GO * 4, 1u);
      }
      MK(14);
      lds_spin4(lw + LW_S * 4, 8u);
      dma_w(wp, 0u);
      lds_spin4(lw + LW_S * 4, 9u);
      dma_w(wp + C::WB, (unsigned)SB);
      next();
      next();
      unsigned slot = (unsigned)(2 * SB);
      auto cissue = [&]() {
        dma_x(xp, slot);
        dma_w(wp, slot);
        next();
        slot = (slot == (unsigned)((NS - 1) * SB)) ? 0u : slot + (unsigned)SB;
      };
      auto cstep = [&]() {
        bar();
        cissue();
      };
      wait_w<NDW>();
      MK(15);
      int j = 2;
      if (layer == 0) {
        const char* sp = uni_ptr((const char*)w_sp + (size_t)(bid * 4 + wl) * 8192 + WSP_V * 512);
        unsigned d = 0u;
#pragma unroll 1
        for (; j < 2 + 32 - 4 * WSP_V; ++j) {
          cstep();
          asm volatile("s_load_dword %0, %1, 0\n\ts_load_dword %0, %1, 64\n\ts_load_dword %0, %1, 0x1000\n\ts_load_dword %0, %1, 0x1040"
                       : "+s"(d)
                       : "s"(sp));
          sp += 128;
          wait_w<VPS>();
        }
#pragma unroll 1
        for (int e = j + 4; j < e; ++j) {
          cstep();
          asm volatile("" : "+s"(d));
          wait_w<VPS>();
        }
        asm volatile("s_waitcnt lgkmcnt(0)" : "+s"(d));
      }
#pragma unroll 1
      for (; j <= 29; ++j) {
        cstep();
        wait_w<VPS>();
      }
      bar();
    }
    auto w5piece = [&](unsigned off) {
      dma16_sc1(w5p, lane16 + (off >= 54u * 1024u ? off - 54u * 1024u : off + 90u * 1024u), lds0 + (unsigned)P5_W + off);
    };
#pragma unroll
    for (int i = 0; i < 15; ++i) {
      const int p = 4 * i + wl;
      if (i < 14 || wl == 0) w5piece((unsigned)((p < 34 ? 53 + p : 121 - 34 + p) * 1024));
    }
    wait_vm<14>();
    bar();
#pragma unroll
    for (int i = 0; i < 9; ++i)
      if (i < 8 || wl < 2) w5piece((unsigned)((87 + 4 * i + wl) * 1024));
    wait_vm<0>();
    bar();
    if (wl) {
#pragma unroll
      for (int i = 0; i < 18; ++i)
        if (i < 17 || wl < 3) w5piece((unsigned)((3 * i + wl - 1) * 1024));
    } else {
      cnt_wait(ws, CN_H + layer, rb * 8 + ct, HSPLIT ? 8u : 16u);
    }
    MK(20);
    bar();
    p5_steps(w5p, std::make_integer_sequence<int, 7>{});
    if (layer == 0) {
      if (bid < 24 && wl == 0 && lane < 8) pk_st16(ws + OFF_HELLO + bid * 128 + lane16, u32x4{0u, 0u, 0u, 0u});
    }
  }
};

__global__ __launch_bounds__(768, 3) void k_main(const u16* __restrict__ x0, const u16* __restrict__ wqkv, const u16* __restrict__ wo,
                                                 const u16* __restrict__ w_in, const u16* __restrict__ w_sp,
                                                 const u16* __restrict__ b_sp, const u16* __restrict__ w_out,
                                                 u16* __restrict__ out, char* ws) {
  extern __shared__ __attribute__((aligned(16))) char smem[];
  const char* code0 = code_here();
  (void)code0;
  const unsigned lds0 = (unsigned)(size_t)smem;
  int wave = __builtin_amdgcn_readfirstlane((int)threadIdx.x >> 6);
  int bid = blockIdx.x;
#define FRESH() asm volatile("" : "+s"(bid), "+s"(ws), "+s"(wave))
#define XCD (bid & 7)
#define IDX (bid >> 3)
#define MI ((XCD >> 2) * 8 + (IDX >> 2))
#define NI ((XCD & 1) * 8 + (XCD & 2) * 2 + (IDX & 3))
#define RB (XCD * 4 + (IDX >> 3))
#define CT (IDX & 7)
#define RT ((XCD >> 1) * 4 + (IDX >> 3))
#define HP ((XCD & 1) * 8 + (IDX & 7))
  MK(0);
  if (wave >= 3 && wave < 8) {
    constexpr int ST = 64;
    const int k = (7 - wave) * 64 + lane_id();
    const bool isx = k < 9216 / ST;
    int o = isx ? bid * 9216 + k * ST : bid * 7776 + (k - 9216 / ST) * ST;
    if (!isx && k - 9216 / ST > 7775 / ST) o = bid * 7776;
    const char* p = (isx ? (const char*)x0 : (const char*)wqkv) + o;
    asm volatile("s_mov_b32 m0, %1\n\ts_nop 0\n\tglobal_load_lds_dword %0, off" ::"v"(p), "s"(lds0 + (unsigned)K1R_DUMMY) : "memory");
  }

  if (__builtin_expect(wave >= 8, 1)) {
    __builtin_amdgcn_s_setprio(3);
    {
      const int lane = lane_id();
      RawLd rl;
      const bool cross = (((HP * 72) & ~15) & 127) > 48;
      const int c16 = IDX >> 1;
      const int job = cross ? (c16 < 11 ? 128 + XCD * 11 + c16 : 999) : XCD * 16 + c16;
      const int pj = cross ? (c16 >= 11 ? XCD * 5 + c16 - 11 : -1) : (XCD < 4 && (c16 & 7) == 0 ? 40 + XCD * 2 + (c16 >> 3) : -1);
      rl.front(x0, wqkv, wo, ws, bid, lds0, wave - 8, lane, RT, HP, pj MF_PASS);
      if (job < 216) k1_raw_loader<true>(rl, w_in, w_out, ws, rl.tag, bid, lds0, wave - 8, lane, RT, HP, CT, job, pj MF_PASS);
      else k1_raw_loader<false>(rl, w_in, w_out, ws, rl.tag, bid, lds0, wave - 8, lane, RT, HP, CT, job, pj MF_PASS);
    }
    Loader ld;
    ld.lds0 = lds0;
#pragma unroll 1
    for (int layer = 0; layer < 2; ++layer) {
      FRESH();
      ld.wl = wave - 8;
      ld.lane = lane_id();
      ld.lane16 = (unsigned)ld.lane * 16u;
      const char* wip = uni_ptr(ws + OFF_PI + (size_t)NI * (18 * K3C::WB));
      if (layer) {
        ld.p1(smem, ws, RT, HP MK_PASS);
        ld.pf_load<0, 21, 0, 81>(uni_ptr(ws + OFF_PO + (size_t)CT * (18 * 4608)));
        ld.pf_load<21, 9, 36, 72>(wip);
        wait_vm<0>();
        ld.pf_pin<0, 30>();
        bar();
        ld.pf_put<0, 0, 40>((unsigned)P2_W);
        ld.pf_put<0, 40, 41>((unsigned)(P2_W + 40 * 1024));
      } else {
        ld.pf_undef<21, 9>();
        MK(3);
        MK(4);
        bar();
        const char* wp = uni_ptr(ws + OFF_PO + (size_t)CT * (18 * 4608) + ld.wl * 1024);
#pragma unroll
        for (int i = 0; i < 21; ++i)
          if (i < 20 || ld.wl == 0) dma16_sc1(wp + i * 4096, ld.lane16, lds0 + (unsigned)(P2_W + i * 4096 + ld.wl * 1024));
      }
      if (layer == 0 && ld.wl == 0) cnt_wait(ws, CN_O + layer, RB * 8 + CT, 16u);
      MK(6);
      asm volatile("s_waitcnt vmcnt(0) lgkmcnt(0)" ::: "memory");
      bar();
      lds_spin(lds0 + (unsigned)(LW_OFF + (LW_T + layer) * 4), 12u);
      if (layer == 0) {
        if (ld.wl == 0) {
          cnt_wait(ws, CN_PK, 8 + NI, 36u);
          lds_wr32(lds0 + (unsigned)(LW_OFF + LW_HELLO * 4), 2u);
        } else {
          lds_spin(lds0 + (unsigned)(LW_OFF + LW_HELLO * 4), 2u);
        }
        MK(7);
      }
      static_assert(2 * K3C::SB <= P2_W, "free during the first residual GEMM");
      ld.dma_run<18>(wip, (unsigned)K3C::XB);
      ld.dma_run<18>(wip + K3C::WB, (unsigned)(K3C::SB + K3C::XB));
      if (L0REG && layer == 0) ld.pf_load<21, 9, 36, 72>(wip);
      if (layer == 0 && ld.wl == 0) cnt_wait(ws, CN_PK, 24 + CT, 72u);
      wait_vm<0>();
      ld.pf_pin<21, 9>();
      bar();
      FRESH();
      ld.wl = wave - 8;
      ld.p35(smem, ws, w_sp, layer, bid, MI, NI, RB, CT, HP MK_PASS);
    }
  } else {
    MF(12);
    math_start(smem, code0, b_sp, wqkv, ws, bid, wave, lane_id());
    MF(13);
#pragma unroll 1
    for (int layer = 0; layer < 2; ++layer) {
      FRESH();
      {
        const int lane = lane_id(), r0 = RT * 128;
        float* csq = reinterpret_cast<float*>(ws + OFF_CSQ + bid * 512);
        Attn at;
        at.setup(lds0 + (unsigned)(layer ? K1_T_OFF : K1R_T_OFF), r0, HP, wave, lane);
        if (layer) {
          if ((wave & 2) == 0) k1_gemm<2, 4>(smem, ws, csq, RT, HP, (wave & 1) * 2 + (wave & 4), 0, lane MK_PASS);
          else if (wave < 4) k1_gemm<3, 3>(smem, ws, csq, RT, HP, (wave & 1) * 3, 4, lane MK_PASS);
          else if (wave == 6) k1_gemm<2, 3>(smem, ws, csq, RT, HP, 6, 4, lane MK_PASS);
          else k1_gemm<1, 7>(smem, ws, csq, RT, HP, 8, 0, lane MK_PASS);
        } else {
          if (wave == 0) k1_gemm_raw<3, false>(smem, HP, 0, lane MF_PASS);
          else if (wave < 4) k1_gemm_raw<2, false>(smem, HP, 1 + 2 * wave, lane MF_PASS);
          else if (wave < 7) k1_gemm_raw<2, true>(smem, HP, 2 * (wave - 4), lane MF_PASS);
          else k1_gemm_raw<3, true>(smem, HP, 6, lane MF_PASS);
        }
        msync(lds0 + (unsigned)(LW_OFF + (LW_T + layer) * 4));
        MK(3);
        at.run(ws + OFF_O + (size_t)layer * SZ_X, r0, HP, wave, lane);
        if (layer == 0) {
          if (wave == 0 && lane < 32) pk_st16(csq + lane * 4, *reinterpret_cast<const u32x4*>(smem + K1R_CS_OFF + lane * 16));
          lds_spin(lds0 + (unsigned)(LW_OFF + LW_HELLO * 4), 1u);
        }
        MK(4);
        announce(lds0 + (unsigned)(LW_OFF + (LW_O + layer * 22 + (wave >> 2)) * 4), 4u, ws, CN_O + layer, (RT * 2 + (wave >> 2)) * 8 + lane,
                 lane < 8);
        MK(5);
        bar();
      }
      FRESH();
      {
        if (wave < 4) {
          const int lane = lane_id();
          u32x2 xv[5];
          res_ldx(xv, layer ? ws + OFF_X1 + SZ_X : (const char*)x0, layer == 0, RB, CT, wave, lane);
          ResOut ro;
          ro.setup(ws + OFF_X1 + (size_t)layer * 2 * SZ_X, ws + OFF_XS + (size_t)layer * 2 * SZ_XS, out, false, RB, CT, wave, lane);
          f32x4 acc[2][5];
          p2_gemm(acc, xv, lds0, uni_ptr(ws + OFF_O + (size_t)layer * SZ_X + (size_t)RB * (72 * 1024)), layer, wave, lane MK_PASS);
          MK(7);
          ro.run(acc[0], false);
          MK(8);
          announce(lds0 + (unsigned)(LW_OFF + (LW_X1 + layer) * 4), 4u, ws, CN_X1 + layer, (RB >> 1) * 16 + lane, lane < 16);
          MK(9);
          bar();
          FRESH();
          if (wave == 3) cnt_wait(ws, CN_X1 + layer, MI * 16 + NI, 16u);
          bar();
          MK(10);
          p3_g0(smem, ws, layer, MI, NI, wave, lane_id() MK_PASS);
        } else {
          if (layer && wave == 5) cnt_wait(ws, CN_O + layer, RB * 8 + CT, 16u);
          bar();
          bar();
          k3_const(smem, ws, b_sp, layer, bid, MI, NI, wave & 3);
          bar();
          MK(10);
          FRESH();
          p3_g1(smem, ws, code0, bid, layer, MI, wave & 3, lane_id() MK_PASS);
        }
        bar();
      }
      FRESH();
      {
        const int lane = lane_id();
        if (wave < 4) {
          u32x2 xv[5];
          res_ldx(xv, ws + OFF_X1 + (size_t)layer * 2 * SZ_X, false, RB, CT, wave, lane);
          f32x4 acc[2][5];
          ResOut ro;
          ro.setup(ws + OFF_X1 + SZ_X, ws + OFF_XS + SZ_XS, out, layer != 0, RB, CT, wave, lane);
          ResX<36, 33, 8, P5_W, P5_XW, P5_X0, 3, P5_BAR> gm(acc);
          gm.setup(lds0, uni_ptr(ws + OFF_H + (size_t)layer * SZ_Z + (size_t)RB * (288 * 1024)), wave, lane, (unsigned)P5_RED, layer + 1u);
          gm.prow();
          bar();
          MK(20);
          gm.prox<0>(std::make_integer_sequence<int, P5_X0>{});
          gm.init<2 * P5_X0>(xv);
          __builtin_amdgcn_sched_barrier(0);
          gm.run();
          MK(21);
          ro.run(acc[0], layer != 0);
          MK(22);
          if (layer == 0) {
            announce(lds0 + (unsigned)(LW_OFF + LW_X2 * 4), 4u, ws, CN_X2, (RB >> 1) * 16 + lane,
                     lane < 16 || (lane < 32 && (RB & 1) && RB < 31));
            MK(23);
          }
        } else {
          u32x4 wq[15];
          const int wm = wave & 3;
          const unsigned o = (unsigned)((wm >> 1) * 18432 + 12288 + (wm & 1) * 3072 + lane * 16);
          if (G1W) {
            const char* wp = uni_ptr(ws + OFF_PW + (size_t)CT * (36 * 9216) + 8 * 18432);
#pragma unroll
            for (int i = 0; i < 15; ++i)
              asm volatile("global_load_dwordx4 %0, %1, %2 offset:%3 sc1" : "=v"(wq[i]) : "v"(o), "s"(wp + (i / 3) * 36864), "n"((i % 3) * 1024) : "memory");
          }
          bar();
#pragma unroll
          for (int i = 0; i < 7; ++i) {
            if (G1W && i == 0) asm volatile("s_waitcnt vmcnt(0)" ::: "memory");
            if (HSPLIT && i == HPOLL && wave == 5) {
              if (HPOLL < 2) {
                unsigned f;
                const char* cp = uni_ptr(ws + OFF_CNT + (size_t)((CN_H2 + layer) * 256 + RB * 8 + CT) * 128);
                asm volatile("global_load_dword %0, %1, %2 sc1" : "=v"(f) : "v"(vconst(0u)), "s"(cp) : "memory");
                bar();
                asm volatile("s_waitcnt vmcnt(0)" : "+v"(f) : : "memory");
                if (__builtin_amdgcn_readfirstlane(f) != 7u) cnt_wait(ws, CN_H2 + layer, RB * 8 + CT, 8u);
                continue;
              }
              cnt_wait(ws, CN_H2 + layer, RB * 8 + CT, 8u);
            }
            asm volatile("s_waitcnt lgkmcnt(0)" ::: "memory");
            bar();
            if (G1W && i < 5) {
              const unsigned a = lds0 + o + (unsigned)(P5_W + ((2 * i + 3) % 8) * 18432) - ((i == 2 && wm >= 2) ? 8u * 18432u : 0u);
#pragma unroll
              for (int k = 0; k < 3; ++k) {
                if (P5PACE) __builtin_amdgcn_s_sleep(2 * P5PACE);
                asm volatile("ds_write_b128 %0, %1 offset:%2" ::"v"(a), "v"(wq[3 * i + k]), "n"(k * 1024) : "memory");
              }
            }
          }
          MK(22);
        }
        if (layer == 0) bar();
      }
    }
  }
}

}

extern "C" void kernel_launch(void* const* d_in, const int* in_sizes, int n_in, void* d_out, int out_size,
                              void* d_ws, size_t ws_size, hipStream_t stream) {
  const u16* x0 = (const u16*)d_in[0];
  const u16* wqkv = (const u16*)d_in[1];
  const u16* wo = (const u16*)d_in[2];
  const u16* w_in = (const u16*)d_in[4];
  const u16* w_sp = (const u16*)d_in[5];
  const u16* b_sp = (const u16*)d_in[6];
  const u16* w_out = (const u16*)d_in[7];
  char* ws = (char*)d_ws;
  (void)hipFuncSetAttribute((const void*)k_main, hipFuncAttributeMaxDynamicSharedMemorySize, LDS_ALL);
  k_main<<<256, 768, LDS_ALL, stream>>>(x0, wqkv, wo, w_in, w_sp, b_sp, w_out, (u16*)d_out, ws);
}
```

```cpp
#include <hip/hip_runtime.h>
#include <hip/hip_bf16.h>
#include <stdint.h>

#define DEVI __device__ __forceinline__

typedef unsigned short u16;
typedef __attribute__((ext_vector_type(8))) short s16x8;
typedef __attribute__((ext_vector_type(4))) short s16x4;
typedef __attribute__((ext_vector_type(4))) float f32x4;
typedef __attribute__((ext_vector_type(2))) float f32x2;
typedef __attribute__((ext_vector_type(2))) __bf16 bf16x2;
typedef __attribute__((ext_vector_type(2))) unsigned u32x2;
typedef __attribute__((ext_vector_type(4))) unsigned u32x4;

#define MK(I) ((void)0)
#define MX(S, I) ((void)0)
#define MF(I) ((void)0)
#define MF_ARG
#define MF_PASS
#define MK_ARG
#define MK_PASS

#define SPIN_LDS for (;;)
#define SPIN_MEM (1u << 22)

#define G1W 0
#define P5PACE 0
#define L0REG 0
#define RTC 0
#define P1PR 3
#define HSPLIT 1
#define HPOLL 1
#define XA4 1
#define P5ORD 0
#define CT0 1
#define AL_A 0
#define AL_B 0
#define AL_C 0
#define AL_P5 0
#define AL_P2 0
#define AL_G -1
#define AL_K 0
#define AL_L 0
#define MPOLL 1
namespace {

constexpr int S_ = 2048, D_ = 576, F_ = 4608, FH_ = 2304;
constexpr int NQKV = 3 * D_;

constexpr size_t SZ_X = (size_t)S_ * D_ * 2;
constexpr size_t SZ_Z = (size_t)S_ * FH_ * 2;
constexpr size_t OFF_X1 = 0;
constexpr size_t OFF_O = OFF_X1 + 3 * SZ_X;
constexpr size_t OFF_Z2 = OFF_O + 2 * SZ_X;
constexpr size_t OFF_H = OFF_Z2 + 2 * SZ_Z;
constexpr size_t SZ_XS = (size_t)S_ * 8 * 8;
constexpr size_t OFF_XS = OFF_H + 2 * SZ_Z;
constexpr size_t OFF_CSQ = OFF_XS + 3 * SZ_XS;
constexpr size_t OFF_ZP = OFF_CSQ + 256 * 512;
constexpr size_t SZ_ZP = (size_t)16 * 16 * 1024;
constexpr size_t OFF_PQ = OFF_ZP + 2 * SZ_ZP;
constexpr size_t OFF_PO = OFF_PQ + (size_t)16 * 9 * 64 * 112 * 2;
constexpr size_t OFF_PI = OFF_PO + (size_t)D_ * D_ * 2;
constexpr size_t OFF_PW = OFF_PI + (size_t)32 * 9 * 64 * 144 * 2;
constexpr size_t OFF_CSP = OFF_PW + (size_t)FH_ * D_ * 2;
constexpr size_t OFF_CNT = OFF_CSP + (size_t)18 * F_ * 4;
constexpr int CN_X1 = 0, CN_H = 2, CN_X2 = 4, CN_O = 5, CN_Z = 7, CN_PK = 9, CN_H2 = 10, NCNT = 12;
constexpr size_t OFF_TAG = OFF_CNT + (size_t)NCNT * 256 * 128;
constexpr size_t OFF_HELLO = OFF_TAG + 128;
constexpr size_t OFF_POT = OFF_HELLO + 2048;
constexpr size_t OFF_CSK = OFF_POT + 1024;
constexpr size_t WS_END = OFF_CSK + 256 * 2048;

DEVI float bf_lo(unsigned u) { return __uint_as_float(u << 16); }
DEVI float bf_hi(unsigned u) { return __uint_as_float(u & 0xffff0000u); }
DEVI unsigned pack2(float a, float b) {
  f32x2 v = {a, b};
  bf16x2 r = __builtin_convertvector(v, bf16x2);
  return __builtin_bit_cast(unsigned, r);
}
DEVI float rbf(float a) {
  return bf_lo(pack2(a, 0.f));
}

DEVI float sum_g(float x) {
  const unsigned u = __float_as_uint(x);
  const auto p = __builtin_amdgcn_permlane32_swap(u, u, false, false);
  const unsigned v = __float_as_uint(__uint_as_float(p[0]) + __uint_as_float(p[1]));
  const auto q = __builtin_amdgcn_permlane16_swap(v, v, false, false);
  return __uint_as_float(q[0]) + __uint_as_float(q[1]);
}

DEVI unsigned vconst(unsigned c) {
  unsigned r;
  asm volatile("v_mov_b32 %0, %1" : "=v"(r) : "s"(c));
  return r;
}
template <int K>
DEVI void place() {
  if constexpr (K >= 0) {
    __builtin_amdgcn_sched_barrier(0);
    asm volatile(".p2align 6\n\t.fill %0, 4, 0xbf800000" ::"n"(K));
    __builtin_amdgcn_sched_barrier(0);
  }
}
DEVI const char* uni_ptr(const char* p) {
  unsigned long long v = (unsigned long long)p;
  unsigned lo = __builtin_amdgcn_readfirstlane((unsigned)v), hi = __builtin_amdgcn_readfirstlane((unsigned)(v >> 32));
  asm("s_nop 4" : "+s"(lo), "+s"(hi));
  return (const char*)(((unsigned long long)hi << 32) | lo);
}

DEVI void dma16(const char* sbase, unsigned voff, unsigned m0val) {
  asm volatile("s_mov_b32 m0, %2\n\ts_nop 0\n\tglobal_load_lds_dwordx4 %0, %1"
               :
               : "v"(voff), "s"(sbase), "s"(m0val)
               : "memory");
}

DEVI void ln_finish(float s, float q, float invn, float& mb, float& rinv) {
  float mean = s * invn;
  asm("" : "+v"(mean));
  float var = fmaxf(__builtin_fmaf(-mean, mean, q * invn), 0.f);
  mb = rbf(mean);
  float v = rbf(var);
  float t = rbf(v + 1e-5f);
  float sd = rbf(__builtin_amdgcn_sqrtf(t));
  rinv = __builtin_amdgcn_rcpf(sd);
}

DEVI unsigned pack2v(f32x2 v) { return pack2(v[0], v[1]); }

template <int BM_, int BN_>
struct GemmCfg {
  static constexpr int BM = BM_, BN = BN_, NS = 4;
  static constexpr int P = BN * 2;
  static constexpr int XB = BM * 128, WB = 64 * P, SB = XB + WB;
  static constexpr int LDS_BYTES = NS * SB;
};

DEVI int swap23(int r) { return (r & ~12) | ((r & 4) << 1) | ((r & 8) >> 1); }

DEVI unsigned lds_rd32(unsigned addr) {
  unsigned v;
  asm volatile("ds_read_b32 %0, %1\n\ts_waitcnt lgkmcnt(0)" : "=v"(v) : "v"(addr) : "memory");
  return v;
}
DEVI void lds_wr32(unsigned addr, unsigned v) { asm volatile("ds_write_b32 %0, %1" ::"v"(addr), "v"(v) : "memory"); }

template <class C, int MT, int NT, int D, int R, bool XCM, int WT, bool SQ = false, unsigned long long WS = 0>
struct GemmMath {
  static constexpr int WO = WT ? 1 : 2;
  static constexpr int NS = C::NS, SB = C::SB, P = C::P, NU = 2 * NT;
  static constexpr int XPU = MT <= 2 ? MT : MT - 2;
  static constexpr int XU = (MT + XPU - 1) / XPU;
  static_assert(NU % R == 0 && R > D && D <= NT && XU <= D, "shape");
  s16x4 wlo[R], whi[R];
  s16x8 wf[R];
  s16x8 xf[2][MT];
  unsigned xrd[2], wrd, xa[2], wa, so;
  f32x4 (&acc)[MT][NT];
  f32x4 accq[SQ ? MT : 1];
  unsigned long long wtab = WS;
  DEVI GemmMath(f32x4 (&a)[MT][NT]) : acc(a) {}

  template <int PU>
  DEVI void wread_lo() {
    constexpr int off = (PU / NT) * 32 * P + (PU % NT) * (WT == 2 ? 1024 : 32);
    if constexpr (WT) asm volatile("ds_read_b128 %0, %1 offset:%2" : "=v"(wf[PU % R]) : "v"(wa), "n"(off));
    else asm volatile("ds_read_b64_tr_b16 %0, %1 offset:%2" : "=v"(wlo[PU % R]) : "v"(wa), "n"(off));
  }
  template <int PU>
  DEVI void wread_hi() {
    if constexpr (!WT)
      asm volatile("ds_read_b64_tr_b16 %0, %1 offset:%2" : "=v"(whi[PU % R]) : "v"(wa), "n"((PU / NT) * 32 * P + (PU % NT) * 32 + 8 * P));
  }
  template <int H, int M>
  DEVI void xread1() {
    asm volatile("ds_read_b128 %0, %1 offset:%2" : "=v"(xf[H][M]) : "v"(xa[H]), "n"(M * (XCM ? 256 : 2048)));
  }
  static constexpr int xops(int v) {
    const int k = (v + D) % NT;
    if (k >= XU) return 0;
    const int n = MT - k * XPU;
    return n < XPU ? n : XPU;
  }
  static constexpr int ops(int v, bool last) { return (last && v + D >= NU) ? 0 : WO + xops(v); }
  static constexpr int newer(int U, bool last) {
    int n = 0;
    int v0 = U - D;
    if (U % NT == 0) v0 += XU - 1;
    else n += v0 >= 0 ? xops(v0) : xops(v0 + NU);
    for (int v = v0 + 1; v < U; ++v) n += v >= 0 ? ops(v, last) : ops(v + NU, false);
    return n;
  }
  template <int U, int M_>
  DEVI void mf() {
    constexpr int M = ((U % NT) & 1) ? MT - 1 - M_ : M_;
    s16x8 w;
    if constexpr (WT) w = wf[U % R];
    else w = __builtin_shufflevector(wlo[U % R], whi[U % R], 0, 1, 2, 3, 4, 5, 6, 7);
    acc[M][U % NT] = __builtin_amdgcn_mfma_f32_16x16x32_bf16(w, xf[U / NT][M], acc[M][U % NT], 0, 0, 0);
    __builtin_amdgcn_sched_barrier(0);
  }
  DEVI void pin() {
#pragma unroll
    for (int m = 0; m < MT; ++m)
#pragma unroll
      for (int n = 0; n < NT; ++n) asm volatile("" : "+v"(acc[m][n]));
  }
  template <int U, int I>
  DEVI void dsop() {
    constexpr int PU = (U + D) % NU;
    if constexpr (I == 0) wread_lo<PU>();
    else if constexpr (I == 1) wread_hi<PU>();
    else if constexpr (I - 2 < xops(U)) {
      constexpr int k = (U + D) % NT;
      xread1<((U + D - k) % NU) / NT, k * XPU + I - 2>();
    }
  }
  template <int U, bool LAST, bool BAR, int M>
  DEVI void slot() {
    constexpr bool PF = !(LAST && U + D >= NU);
    mf<U, M>();
    if constexpr (M == 0 && PF && U + D == NU) {
      if constexpr (BAR) __builtin_amdgcn_s_barrier();
      so = (so == (unsigned)((NS - 1) * SB)) ? 0u : so + (unsigned)SB;
      wa = wrd + (WS ? ((unsigned)wtab & 63u) << 11 : so);
      wtab >>= 6;
      xa[0] = xrd[0] + so;
      xa[1] = xrd[1] + so;
      __builtin_amdgcn_sched_barrier(0);
    }
    if constexpr (PF) {
      dsop<U, M>();
      if constexpr (M == MT - 1) {
        if constexpr (MT <= 1) dsop<U, 1>();
        if constexpr (MT <= 2) {
          dsop<U, 2>();
          dsop<U, 3>();
        }
      }
      __builtin_amdgcn_sched_barrier(0);
    }
  }
  template <int U, bool LAST, bool BAR, int... M>
  DEVI void unit(std::integer_sequence<int, M...>) {
    static_assert(newer(U, LAST) <= 15, "lgkmcnt range");
    asm volatile("s_waitcnt lgkmcnt(%0)" ::"n"(newer(U, LAST)));
    __builtin_amdgcn_sched_barrier(0);
    (slot<U, LAST, BAR, M>(), ...);
    if constexpr (SQ && (U % NT) < MT) {
      constexpr int m = U % NT;
      accq[m] = __builtin_amdgcn_mfma_f32_16x16x32_bf16(xf[U / NT][m], xf[U / NT][m], accq[m], 0, 0, 0);
      __builtin_amdgcn_sched_barrier(0);
    }
    __builtin_amdgcn_sched_barrier(0);
  }
  template <bool LAST, bool BAR, int... U>
  DEVI void stage(std::integer_sequence<int, U...>) {
    (unit<U, LAST, BAR>(std::make_integer_sequence<int, MT>{}), ...);
  }
  template <int... U>
  DEVI void prow(std::integer_sequence<int, U...>) {
    ((wread_lo<U>(), wread_hi<U>()), ...);
  }
  template <int... M>
  DEVI void prox(std::integer_sequence<int, M...>) {
    (xread1<0, M>(), ...);
  }
  DEVI void setup(unsigned lds0, int mt0, int nt0, int lane, unsigned so0) {
    const int g = lane >> 4, r = lane & 15, q = (lane >> 2) & 3, p = lane & 3;
#pragma unroll
    for (int h = 0; h < 2; ++h)
      xrd[h] = XCM ? lds0 + (unsigned)((4 * h + g) * (C::BM * 16) + (mt0 * 16 + r) * 16)
                   : lds0 + (unsigned)((mt0 * 16 + r) * 128 + (((4 * h + g) ^ ((r >> 1) & 7)) * 16));
    wrd = WT == 2   ? lds0 + (unsigned)(C::XB + nt0 * 1024 + lane * 16)
          : WT == 1 ? lds0 + (unsigned)(C::XB + (g * 8 + (r & 7)) * P + (r >> 3) * 16 + nt0 * 32)
                    : lds0 + (unsigned)(C::XB + (16 * (g >> 1) + 4 * (g & 1) + q) * P + 8 * p + nt0 * 32);
    so = so0;
    xa[0] = xrd[0] + so;
    xa[1] = xrd[1] + so;
    wa = wrd + so;
  }
  DEVI void first() {
    prox(std::make_integer_sequence<int, MT>{});
    prow(std::make_integer_sequence<int, D>{});
    asm volatile("s_waitcnt lgkmcnt(0)");
  }
  template <int N, bool BAR = true, int AL = -1>
  DEVI void mid() {
    place<AL>();
#pragma unroll 1
    for (int s = 0; s < N; ++s) stage<false, BAR>(std::make_integer_sequence<int, NU>{});
  }
  DEVI void last() {
    stage<true, true>(std::make_integer_sequence<int, NU>{});
    pin();
  }
  template <int KS>
  DEVI void run(unsigned lds0, int mt0, int nt0, int lane, unsigned so0) {
    setup(lds0, mt0, nt0, lane, so0);
    __builtin_amdgcn_s_barrier();
    first();
    mid<KS - 1, true, AL_K>();
    last();
  }
};

DEVI const char* code_here() {
  unsigned long long pc;
  asm volatile("s_getpc_b64 %0" : "=s"(pc));
  return (const char*)pc;
}
DEVI int okmap(int p) {
  const int ch = p >> 3, e = p & 7, c = (ch - 8) & 3;
  return ch < 8 ? (2 * ch + (e >> 2)) * 36 + 16 + (e & 3) : ((ch - 8) >> 2) * 36 + c * 8 + (c >> 1) * 4 + e;
}

DEVI void pk_st16(void* p, u32x4 v) {
  asm volatile("global_store_dwordx4 %0, %1, off sc1\n\ts_nop 1" ::"v"(p), "v"(v) : "memory");
}

using K1C = GemmCfg<144, 112>;
constexpr int K1_TP = 240;
constexpr int K1_T_OFF = K1C::SB;
constexpr int K1_ST_OFF = 163840 - 128 - 1152;
constexpr int K1_WX = 4 * K1C::SB + 2048;
constexpr unsigned long long K1_WS = 16ull | 32ull << 6 | 48ull << 12 | 56ull << 18 | 63ull << 24 | 16ull << 30 | 32ull << 36 | 48ull << 42;
static_assert(K1C::SB == 16 * 2048 && K1_WX - K1C::XB == 56 * 2048 && K1C::WB == 7 * 2048 && K1_WX + 2 * K1C::WB <= K1_ST_OFF, "K1 w places");
static_assert(K1_T_OFF + 144 * K1_TP + 64 <= 2 * K1C::SB + K1C::XB, "K1 LDS alias");
using K1R = GemmCfg<144, 144>;
constexpr int K1R_T_OFF = K1R::SB;
constexpr int K1R_CS_OFF = K1R_T_OFF + 144 * K1_TP;
constexpr int K1R_ST_OFF = K1R_CS_OFF + 512;
constexpr int K1R_DUMMY = K1R::LDS_BYTES;
constexpr int K1R_PO = K1R_DUMMY + 1024;
constexpr int K1R_CNT = 163840 - 128 + 28 * 4;
DEVI void msync(unsigned addr) {
  asm volatile("s_waitcnt lgkmcnt(0)" ::: "memory");
  asm volatile("s_mov_b64 exec, 1\n\tds_add_u32 %0, %1\n\ts_mov_b64 exec, -1" ::"v"(addr), "v"(vconst(1u)) : "memory");
  SPIN_LDS {
    if (__builtin_amdgcn_readfirstlane(lds_rd32(addr)) >= 8u) break;
  }
}
static_assert(K1R_PO + 13824 + 256 <= 163840 - 128 && K1R_ST_OFF + 144 * 8 <= 3 * K1R::SB, "K1R LDS");

DEVI void touch64(const char* sbase, unsigned voff, unsigned m0val) {
  asm volatile("s_mov_b32 m0, %2\n\ts_nop 0\n\tglobal_load_lds_dword %0, %1" : : "v"(voff), "s"(sbase), "s"(m0val) : "memory");
}

template <int MT, bool G1>
DEVI void k1_gemm_raw(char* smem, int hp, int mt0, int lane MF_ARG) {
  constexpr int NT = 4, nt0 = G1 ? 4 : 0;
  const unsigned lds0 = (unsigned)(size_t)smem;
  const int g = lane >> 4, r = lane & 15;
  unsigned ta[NT];
#pragma unroll
  for (int nt = 0; nt < NT; ++nt) {
    const int j = (nt0 + nt) * 16 + g * 4, seg = j / 40, cj = j - seg * 40 - (hp & 1) * 4;
    const int to = (seg < 3 && cj >= 0 && cj < 36) ? seg * 80 + cj * 2 + (cj >= 20 ? 8 : 0) : (seg < 3 ? 40 : 120);
    ta[nt] = lds0 + (unsigned)(K1R_T_OFF + (mt0 * 16 + r) * K1_TP + to);
    asm volatile("" : "+v"(ta[nt]));
  }
  unsigned sa = lds0 + (unsigned)(K1R_ST_OFF + (mt0 * 16 + r) * 8), ca = lds0 + (unsigned)(K1R_CS_OFF + (nt0 * 16 + g * 4) * 4);
  asm volatile("" : "+v"(sa), "+v"(ca));
  f32x4 acc[MT][NT];
#pragma unroll
  for (int mt = 0; mt < MT; ++mt)
#pragma unroll
    for (int nt = 0; nt < NT; ++nt) acc[mt][nt] = f32x4{0.f, 0.f, 0.f, 0.f};
  {
    GemmMath<K1R, MT, 4, 3, 4, false, 0, G1> gm(acc);
    if constexpr (G1) {
#pragma unroll
      for (int mt = 0; mt < MT; ++mt) gm.accq[mt] = f32x4{0.f, 0.f, 0.f, 0.f};
    }
    MF(14);
    if constexpr (!G1) {
      if (mt0 < 7) asm volatile("s_waitcnt vmcnt(0)" ::: "memory");
    }
    MF(15);
    gm.template run<9>(lds0, mt0, nt0, lane, 0u);
    MF(2);
    if constexpr (G1) {
#pragma unroll
      for (int mt = 0; mt < MT; ++mt) {
        const f32x4 q = gm.accq[mt];
        const float d = (r & 2) ? ((r & 1) ? q[3] : q[2]) : ((r & 1) ? q[1] : q[0]);
        const float qq = sum_g(g == (r >> 2) ? d : 0.f);
        float mb, rinv;
        ln_finish(acc[mt][3][0], qq, 1.0f / (float)D_, mb, rinv);
        const f32x2 st = {-mb, rinv};
        if (g == 2) asm volatile("ds_write_b64 %0, %1 offset:%2" ::"v"(sa), "v"(st), "n"(mt * 128) : "memory");
      }
    }
  }
  if (mt0 == 0 && r == 0) {
#pragma unroll
    for (int nt = 0; nt < NT; ++nt) *reinterpret_cast<f32x4*>(smem + K1R_CS_OFF + ((nt0 + nt) * 16 + g * 4) * 4) = acc[0][nt];
  }
  msync(lds0 + (unsigned)K1R_CNT);
  MF(3);
  f32x2 st[MT];
  f32x4 cs[NT];
#pragma unroll
  for (int nt = 0; nt < NT; ++nt) asm volatile("ds_read_b128 %0, %1 offset:%2" : "=v"(cs[nt]) : "v"(ca), "n"(nt * 64) : "memory");
#pragma unroll
  for (int mt = 0; mt < MT; ++mt) asm volatile("ds_read_b64 %0, %1 offset:%2" : "=v"(st[mt]) : "v"(sa), "n"(mt * 128) : "memory");
#pragma unroll
  for (int i = 0; i < MT; ++i) asm volatile("s_waitcnt lgkmcnt(0)" : "+v"(st[i]));
#pragma unroll
  for (int i = 0; i < NT; ++i) asm volatile("" : "+v"(cs[i]));
#pragma unroll
  for (int mt = 0; mt < MT; ++mt) {
    f32x2 M = {st[mt][0], st[mt][0]}, R = {st[mt][1], st[mt][1]};
    asm volatile("" : "+v"(M), "+v"(R));
#pragma unroll
    for (int nt = 0; nt < NT; ++nt) {
      const f32x2 v0 = R * __builtin_elementwise_fma(M, f32x2{cs[nt][0], cs[nt][1]}, f32x2{acc[mt][nt][0], acc[mt][nt][1]});
      const f32x2 v1 = R * __builtin_elementwise_fma(M, f32x2{cs[nt][2], cs[nt][3]}, f32x2{acc[mt][nt][2], acc[mt][nt][3]});
      acc[mt][nt][0] = __uint_as_float(pack2v(v0));
      acc[mt][nt][1] = __uint_as_float(pack2v(v1));
      const f32x2 pk = {acc[mt][nt][0], acc[mt][nt][1]};
      asm volatile("ds_write_b64 %0, %1 offset:%2" ::"v"(ta[nt]), "v"(pk), "n"(mt * 16 * K1_TP) : "memory");
    }
  }
}

DEVI void dma16m(const char* sbase, unsigned voff, unsigned m0val, unsigned long long mask) {
  asm volatile("s_mov_b32 m0, %2\n\ts_mov_b64 exec, %3\n\tglobal_load_lds_dwordx4 %0, %1\n\ts_mov_b64 exec, -1"
               :
               : "v"(voff), "s"(sbase), "s"(m0val), "s"(mask)
               : "memory");
}
template <int N>
DEVI void wait_vm() {
  asm volatile("s_waitcnt vmcnt(%0)" ::"n"(N) : "memory");
}

constexpr int LDS_ALL = 163840;
constexpr int LW_OFF = LDS_ALL - 128;
constexpr int LW_GO = 0, LW_T = 2, LW_O = 4  , LW_X1 = 6, LW_H = 8, LW_X2 = 12, LW_PK = 13, LW_Z = 14, LW_PAIR = 16, LW_AD = 20,
              LW_L = 22, LW_S = 24, LW_ST = 29, LW_HELLO = 30, LW_Q = 31;
DEVI void announce(unsigned addr, unsigned n, char* ws, int kind, int who, bool act) {
  const char* cb = uni_ptr(ws + OFF_CNT + (size_t)kind * 256 * 128);
  const unsigned long long mask = __ballot(act);
  unsigned t, u;
  asm volatile(
      "s_waitcnt vmcnt(0)\n\t"
      "s_mov_b64 exec, 1\n\t"
      "ds_add_rtn_u32 %0, %2, %3\n\t"
      "s_waitcnt lgkmcnt(0)\n\t"
      "v_readfirstlane_b32 %1, %0\n\t"
      "s_cmp_eq_u32 %1, %4\n\t"
      "s_cselect_b64 exec, %5, 0\n\t"
      "global_atomic_add %6, %3, %7 sc1\n\t"
      "s_mov_b64 exec, -1\n\t"
      "s_nop 0"
      : "=&v"(t), "=&s"(u)
      : "v"(addr), "v"(vconst(1u)), "s"(n - 1u), "s"(mask), "v"((unsigned)(who & 255) * 128u), "s"(cb)
      : "memory", "scc");
}
DEVI void cnt_wait(const char* ws, int kind, int who, unsigned n) {
  const char* cp = ws + OFF_CNT + (size_t)(kind * 256 + who) * 128;
#pragma unroll 1
  for (unsigned spin = 0; spin < SPIN_MEM; ++spin) {
    unsigned f;
    asm volatile("global_load_dword %0, %1, off sc1\n\ts_waitcnt vmcnt(0)" : "=&v"(f) : "v"(cp) : "memory");
    if (__builtin_amdgcn_readfirstlane(f) == n - 1u) break;
    __builtin_amdgcn_s_sleep(1);
  }
}
DEVI void dma16_sc1(const char* sbase, unsigned voff, unsigned m0val) {
  asm volatile("s_mov_b32 m0, %2\n\ts_nop 0\n\tglobal_load_lds_dwordx4 %0, %1 sc1"
               :
               : "v"(voff), "s"(sbase), "s"(m0val)
               : "memory");
}
DEVI void bar() { __builtin_amdgcn_s_barrier(); }
DEVI void lds_inc(unsigned addr, unsigned by = 1u) {
  asm volatile("s_mov_b64 exec, 1\n\tds_add_u32 %0, %1\n\ts_mov_b64 exec, -1" ::"v"(addr), "v"(vconst(by)) : "memory");
}
DEVI void lds_spin(unsigned addr, unsigned n) {
  SPIN_LDS {
    if (__builtin_amdgcn_readfirstlane(lds_rd32(addr)) >= n) break;
    __builtin_amdgcn_s_sleep(1);
  }
}
template <int SL = 1>
DEVI void lds_spin4(unsigned addr, unsigned n) {
  SPIN_LDS {
    if (((__builtin_amdgcn_readfirstlane(lds_rd32(addr)) + (0x80u - n) * 0x01010101u) & 0x80808080u) == 0x80808080u) break;
    __builtin_amdgcn_s_sleep(SL);
  }
}
DEVI int lane_id() {
  int l;
  asm volatile("v_mbcnt_lo_u32_b32 %0, -1, 0\n\tv_mbcnt_hi_u32_b32 %0, -1, %0" : "=v"(l));
  __builtin_assume((unsigned)l < 64u);
  return l;
}

template <int I>
DEVI u32x4 tr8(const u32x4 (&v)[8]) {
  constexpr unsigned sel = (I & 1) ? 0x07060302u : 0x05040100u;
  return u32x4{__builtin_amdgcn_perm(v[1][I / 2], v[0][I / 2], sel), __builtin_amdgcn_perm(v[3][I / 2], v[2][I / 2], sel),
               __builtin_amdgcn_perm(v[5][I / 2], v[4][I / 2], sel), __builtin_amdgcn_perm(v[7][I / 2], v[6][I / 2], sel)};
}
template <int PITCH, bool SUM, int... I>
DEVI void tr8_store(const u32x4 (&v)[8], unsigned dst, float (&cs)[8], std::integer_sequence<int, I...>) {
  auto one = [&](auto I_) {
    const u32x4 o = tr8<I_>(v);
    const unsigned d_ = dst;
    asm volatile("ds_write_b128 %0, %1 offset:%2" ::"v"(d_), "v"(o), "n"(I_ * PITCH) : "memory");
    if constexpr (SUM) {
#pragma unroll
      for (int d = 0; d < 4; ++d) {
        const unsigned od = o[d];
        cs[I_] = __builtin_amdgcn_fdot2_f32_bf16(__builtin_bit_cast(bf16x2, od), __builtin_bit_cast(bf16x2, 0x3f803f80u), cs[I_], false);
      }
    }
  };
  (one(std::integral_constant<int, I>{}), ...);
}
template <int NB>
DEVI void lds_out(unsigned src, char* dst, int lane) {
  constexpr int N = (NB + 1023) / 1024;
  u32x4 t[N];
  const unsigned a = src + (unsigned)lane * 16u;
#pragma unroll
  for (int i = 0; i < N; ++i) asm volatile("ds_read_b128 %0, %1 offset:%2" : "=v"(t[i]) : "v"(a), "n"(i * 1024) : "memory");
  const char* d = uni_ptr(dst);
#pragma unroll
  for (int i = 0; i < N; ++i) {
    asm volatile("s_waitcnt lgkmcnt(%1)" : "+v"(t[i]) : "n"(N - 1 - i) : "memory");
    if (i < N - 1 || lane < (NB - (N - 1) * 1024) / 16)
      asm volatile("global_store_dwordx4 %0, %1, %2 sc1\n\ts_nop 1" ::"v"((unsigned)lane * 16u), "v"(t[i]), "s"(d + i * 1024) : "memory");
  }
}
template <int A, int B, class F>
DEVI void static_for(F f) {
  if constexpr (A < B) {
    f(std::integral_constant<int, A>{});
    static_for<A + 1, B>(f);
  }
}
constexpr int WSP_V = 3;
#define RS0 4
#define RS1 7
#define RX0 RS0
constexpr int RAW_S1 = RS1;
constexpr int RAW_S0 = RS0;
template <int E>
struct RawX {
  static constexpr int first(int s) {
    return s <= RX0 ? 0 : (s > RAW_S1 ? E : (E * (s - RX0) + RAW_S1 - RX0) / (RAW_S1 + 1 - RX0));
  }
  static constexpr int n(int s) { return first(s + 1) - first(s); }
};
struct RawLd {
  static constexpr int VPS = 10, SB = K1R::SB;
  unsigned xoff[5], woff[5], m0x;
  unsigned long long xact0, wact[5], tag;
  const char *xp, *wp;
  int qb;
  DEVI void q_hook(int st, char* ws, unsigned lds0, int hp) {
    if ((qb >> 1) == st) {
      const int lane = lane_id(), g = lane >> 4;
      const unsigned a = lds0 + (unsigned)((st & 3) * SB + K1R::XB + ((qb & 1) * 32 + 16 * (g >> 1) + 4 * (g & 1) + ((lane >> 2) & 3)) * 288 +
                                           (lane & 3) * 8 + (hp & 1) * 8);
      const unsigned a2 = a + ((lane & 3) >= 1 ? 8u : 0u), a4 = a + ((lane & 3) >= 2 ? 8u : 0u);
      u32x2 qf[7][2];
#pragma unroll
      for (int nt = 0; nt < 7; ++nt)
        asm volatile("ds_read_b64_tr_b16 %0, %2 offset:%3\n\tds_read_b64_tr_b16 %1, %2 offset:%4"
                     : "=&v"(qf[nt][0]), "=&v"(qf[nt][1])
                     : "v"(nt == 2 ? a2 : (nt == 4 ? a4 : a)), "n"(nt * 32 + (nt > 4 ? 16 : (nt > 2 ? 8 : 0))),
                       "n"(2304 + nt * 32 + (nt > 4 ? 16 : (nt > 2 ? 8 : 0)))
                     : "memory");
      const char* d = uni_ptr(ws + OFF_PQ + (size_t)(hp * 18 + qb) * 7168 + 3072);
#pragma unroll
      for (int nt = 0; nt < 7; ++nt) {
        asm volatile("s_waitcnt lgkmcnt(%2)" : "+v"(qf[nt][0]), "+v"(qf[nt][1]) : "n"(12 - 2 * nt) : "memory");
        const u32x4 v = {qf[nt][0][0], qf[nt][0][1], qf[nt][1][0], qf[nt][1][1]};
        asm volatile("global_store_dwordx4 %0, %1, %2 offset:%3 sc1\n\ts_nop 1" ::"v"((unsigned)lane * 16u), "v"(v), "s"(d), "n"(nt * 1024 - 3072) : "memory");
      }
      if (st == 8) lds_wr32(lds0 + (unsigned)(LW_OFF + LW_Q * 4), 1u);
    }
  }
  DEVI void stage_x(unsigned so) {
    dma16m(xp, xoff[0], m0x + so, xact0);
#pragma unroll
    for (int i = 1; i < 4; ++i) dma16(xp, xoff[i], m0x + so + (unsigned)(i * 1024));
    dma16m(xp, xoff[4], m0x + so + 4096u, 0xffffffffull);
    xp += 128;
  }
  DEVI void stage_w(unsigned so) {
#pragma unroll
    for (int i = 0; i < 5; ++i) dma16m(wp, woff[i], m0x + so + (unsigned)(K1R::XB + i * 1024), wact[i]);
    wp += 64 * NQKV * 2;
  }
  DEVI void stage(unsigned so) {
    stage_x(so);
    stage_w(so);
  }
  DEVI void front(const u16* x, const u16* wqkv, const u16* wo, char* ws, int bid, unsigned lds0, int wl, int lane, int rt, int hp,
                  int pj MF_ARG) {
    xp = uni_ptr((const char*)x);
    wp = uni_ptr((const char*)wqkv + ((hp * 72) & ~15));
    m0x = lds0 + (unsigned)(wl * 288 * 16);
    xact0 = wl ? ~0ull : ~0xffull;
    const int row0 = wl * 36 + (lane >> 3);
#pragma unroll
    for (int i = 0; i < 5; ++i) {
      const int row = row0 + i * 8;
      int gr = rt * 128 - 16 + row;
      if (gr < 0) gr = 0;
      xoff[i] = (unsigned)(gr * (D_ * 2)) + (unsigned)(((lane ^ (row >> 1)) & 7) * 16);
    }
    stage_x(0u);
    u32x2 tg;
    asm volatile("global_load_dwordx2 %0, %1, %2 sc1" : "=v"(tg) : "v"(0u), "s"(uni_ptr(ws + OFF_TAG)) : "memory");
    asm volatile("" : "+v"(lane));
#pragma unroll
    for (int i = 0; i < 5; ++i) {
      const unsigned c = (unsigned)(i * 64 + (lane & 63));
      const unsigned wr = (c * 3641u) >> 16, wc = c - wr * 18u, seg = (wc * 13u) >> 6;
      wact[i] = __ballot(wc < 15u) & (i < 4 ? ~0ull : 0xffffffffull);
      woff[i] = (unsigned)(wl * 16 + swap23((int)wr)) * (unsigned)(NQKV * 2) + seg * (unsigned)(D_ * 2 - 80) + wc * 16u;
    }
    stage_w(0u);
    stage((unsigned)SB);
    MF(10);
    asm volatile("s_waitcnt vmcnt(%1)" : "+v"(tg) : "n"(VPS) : "memory");
    tag = (((unsigned long long)(unsigned)__builtin_amdgcn_readfirstlane(tg[1]) << 32) | (unsigned)__builtin_amdgcn_readfirstlane(tg[0])) +
          0x9e3779b97f4a7c15ull;
    MF(1);
    __builtin_amdgcn_s_barrier();
    MF(15);
    if (wl == 0 && lane == 0)
      asm volatile("global_store_dwordx2 %0, %1, off sc1\n\ts_nop 1" ::"v"(ws + OFF_HELLO + bid * 8), "v"(tag) : "memory");
    stage((unsigned)(2 * SB));
    qb = wl == 3 ? rt : ((wl == 2 && rt < 2) ? 16 + rt : 99);
    q_hook(0, ws, lds0, hp);
    const int nb = pj;
    const bool po = pj >= 0 && wl < 2;
    u32x4 v[8];
    unsigned da = 0u;
    if (po) {
      const int l54 = lane < 54 ? lane : 53, gl = l54 / 9, c = l54 - gl * 9, kl = wl * 6 + gl, t = nb / 6, kg = (nb - t * 6) * 12 + kl;
      da = lds0 + (unsigned)(K1R_PO + (kl >> 2) * 4608 + (16 * (kl & 1) + ((kl >> 1) & 1)) * 144 + c * 16);
      const unsigned sa[2] = {(unsigned)(okmap(kg * 8) * D_ + t * 72 + c * 8) * 2u, (unsigned)(okmap(kg * 8 + 4) * D_ + t * 72 + c * 8) * 2u};
      const char* ps = uni_ptr((const char*)wo);
#pragma unroll
      for (int j = 0; j < 8; ++j)
        asm volatile("global_load_dwordx4 %0, %1, %2 offset:%3" : "=v"(v[j]) : "v"(sa[j / 4]), "s"(ps), "n"((j % 4) * D_ * 2) : "memory");
    } else {
#pragma unroll
      for (int j = 0; j < 8; ++j) asm volatile("" : "=v"(v[j]));
    }
    unsigned slot = (unsigned)(3 * SB);
#pragma unroll
    for (int S = 0; S < RAW_S0; ++S) {
      wait_vm<VPS>();
      __builtin_amdgcn_s_barrier();
      if (S == 0) MF(16);
      if (S == 1) MF(17);
      if (S == 2) MF(18);
      if (S == 3) MF(19);
      stage(slot);
      slot = (slot == (unsigned)(3 * SB)) ? 0u : slot + (unsigned)SB;
      q_hook(S + 1, ws, lds0, hp);
      if (po && S == 1) {
        float cs[8];
        asm volatile("" : "+v"(v[0]), "+v"(v[1]), "+v"(v[2]), "+v"(v[3]), "+v"(v[4]), "+v"(v[5]), "+v"(v[6]), "+v"(v[7]));
        tr8_store<288, false>(v, da, cs, std::make_integer_sequence<int, 8>{});
        asm volatile("s_waitcnt lgkmcnt(0)" ::: "memory");
      }
      if (po && S == 2) lds_out<6912>(lds0 + (unsigned)(K1R_PO + wl * 6912), ws + OFF_PO + (size_t)nb * 13824 + wl * 6912, lane);
    }
  }
};
template <bool JOB>
DEVI void k1_raw_loader(RawLd& ld, const u16* __restrict__ w_in, const u16* __restrict__ w_out, char* __restrict__ ws,
                        unsigned long long tag, int bid, unsigned lds0, int wl, int lane, int rt, int hp, int ct, int job, int pj MF_ARG) {
  constexpr int VPS = RawLd::VPS, SB = RawLd::SB, E = JOB ? 16 : 0;
  const bool isi = job < 144;
  const int kb = isi ? job >> 3 : job - 144;
  const int pitch = isi ? F_ * 2 : D_ * 2;
  const int l36 = lane < 36 ? lane : 35;
  unsigned sa = (unsigned)l36 * 16u, da;
  if (isi) da = lds0 + (unsigned)((l36 >= 18 ? 9216 - 288 : 0) + wl * 8 * 288 + l36 * 16);
  else da = lds0 + (unsigned)((l36 / 9) * (4608 - 144) + (16 * (wl & 1) + (wl >> 1)) * 144 + l36 * 16);
  asm volatile("" : "+v"(sa), "+v"(da));
  u32x4 v[2][8];
  const char* ps = uni_ptr((const char*)(isi ? w_in + (job & 7) * 576 : w_out) + (size_t)(kb * 32 + wl * 8) * pitch);
  auto extra = [&](auto J_) {
    constexpr int j = J_;
    const char* q = ps + (j / 2) * pitch;
    u32x4 t;
    asm volatile("global_load_dwordx4 %0, %1, %2 offset:%3" : "=v"(t) : "v"(sa), "s"(q), "n"((j % 2) * 576) : "memory");
    v[j % 2][j / 2] = t;
  };
  auto step = [&](auto S_) {
    constexpr int S = S_;
    wait_vm<RawX<E>::n(S - 2) + (S <= 6 ? VPS : 0) + RawX<E>::n(S - 1)>();
    __builtin_amdgcn_s_barrier();
    MF(16 + S);
    if constexpr (S == RAW_S0) {
      static_assert(RAW_S0 >= 4, "wo blocks");
      if (pj >= 0 && wl == 0 && lane == 0)
        asm volatile("global_store_dwordx2 %0, %1, off sc1\n\ts_nop 1" ::"v"(ws + OFF_POT + (pj / 6) * 128 + (pj % 6) * 8), "v"(tag) : "memory");
    }
    if constexpr (S <= 5) ld.stage((unsigned)(((S + 3) % 4) * SB));
    ld.q_hook(S + 1, ws, lds0, hp);
    static_for<RawX<E>::first(S), RawX<E>::first(S + 1)>(extra);
  };
  const char* hp_ = ws + OFF_HELLO + lane * 32;
  u32x4 a, b;
  if (wl == 1) asm volatile("global_load_dwordx4 %0, %2, off sc1\n\tglobal_load_dwordx4 %1, %2, off offset:16 sc1" : "=&v"(a), "=&v"(b) : "v"(hp_) : "memory");
  else asm volatile("" : "=v"(a), "=v"(b));
  static_for<RAW_S0, 8>(step);
  MF(2);
  if (wl == 1) {
    asm volatile("" : "+v"(a), "+v"(b));
    const unsigned t0 = (unsigned)tag, t1 = (unsigned)(tag >> 32);
#pragma unroll 1
    for (unsigned spin = 0; spin < SPIN_MEM; ++spin) {
      const unsigned bad = ((a[0] ^ t0) | (a[2] ^ t0) | (b[0] ^ t0) | (b[2] ^ t0)) | ((a[1] ^ t1) | (a[3] ^ t1) | (b[1] ^ t1) | (b[3] ^ t1));
      if (!__any(bad != 0u)) break;
      __builtin_amdgcn_s_sleep(4);
      asm volatile("global_load_dwordx4 %0, %2, off sc1\n\tglobal_load_dwordx4 %1, %2, off offset:16 sc1\n\ts_waitcnt vmcnt(0)"
                   : "=&v"(a), "=&v"(b)
                   : "v"(hp_)
                   : "memory");
    }
    lds_wr32(lds0 + (unsigned)(LW_OFF + LW_HELLO * 4), 1u);
    if (bid == 0 && lane == 0) asm volatile("global_store_dwordx2 %0, %1, off sc1\n\ts_nop 1" ::"v"(ws + OFF_TAG), "v"(tag) : "memory");
  }
  MF(11);
  if (rt < 2) lds_spin(lds0 + (unsigned)(LW_OFF + LW_Q * 4), 1u);
  lds_spin(lds0 + (unsigned)K1R_CNT, 8u);
  if constexpr (JOB) {
#pragma unroll
    for (int b = 0; b < 2; ++b)
      asm volatile("s_waitcnt vmcnt(0)"
                   : "+v"(v[b][0]), "+v"(v[b][1]), "+v"(v[b][2]), "+v"(v[b][3]), "+v"(v[b][4]), "+v"(v[b][5]), "+v"(v[b][6]), "+v"(v[b][7])
                   :
                   : "memory");
  }
  MF(12);
  const char* tp = ws + OFF_POT + ct * 128 + (lane < 6 ? lane : 5) * 8;
  u32x2 f;
  asm volatile("global_load_dwordx2 %0, %1, off sc1" : "=v"(f) : "v"(tp) : "memory");
  if constexpr (JOB) {
    static_assert(SB == 4 * 9216, "staging");
#pragma unroll
    for (int b = 0; b < 2; ++b) {
      float cs[8] = {0.f, 0.f, 0.f, 0.f, 0.f, 0.f, 0.f, 0.f};
      tr8_store<288, true>(v[b], da + (unsigned)(b * 18432), cs, std::make_integer_sequence<int, 8>{});
      const f32x4 c0 = {cs[0], cs[1], cs[2], cs[3]}, c1 = {cs[4], cs[5], cs[6], cs[7]};
      const unsigned ca = lds0 + (unsigned)(3 * SB + wl * 2304) + sa * 2u;
      asm volatile("ds_write_b128 %0, %1 offset:%2" ::"v"(ca), "v"(c0), "n"(b * 1152) : "memory");
      asm volatile("ds_write_b128 %0, %1 offset:%2" ::"v"(ca), "v"(c1), "n"(b * 1152 + 16) : "memory");
    }
    lds_inc(lds0 + (unsigned)(LW_OFF + LW_ST * 4));
    lds_spin(lds0 + (unsigned)(LW_OFF + LW_ST * 4), 4u);
    if (isi) {
      const int j = (job & 7) * 4 + wl, ti = j < 16 ? 2 * j + 1 : 2 * (j - 16);
      lds_out<9216>(lds0 + (unsigned)(wl * 9216), ws + OFF_PI + (size_t)(ti * 18 + kb) * 9216, lane);
      f32x4 c[4];
      const unsigned ca = lds0 + (unsigned)(3 * SB + wl * 576) + sa;
#pragma unroll
      for (int i = 0; i < 4; ++i) asm volatile("ds_read_b128 %0, %1 offset:%2" : "=v"(c[i]) : "v"(ca), "n"(i * 2304) : "memory");
      asm volatile("s_waitcnt lgkmcnt(0)" : "+v"(c[0]), "+v"(c[1]), "+v"(c[2]), "+v"(c[3]));
      const f32x4 t = (c[0] + c[1]) + (c[2] + c[3]);
      if (lane < 36) pk_st16(ws + OFF_CSP + ((size_t)kb * F_ + ti * 144) * 4 + sa, __builtin_bit_cast(u32x4, t));
    } else {
      lds_out<4608>(lds0 + (unsigned)(wl * 9216), ws + OFF_PW + (size_t)(wl * 144 + kb) * 4608, lane);
      lds_out<4608>(lds0 + (unsigned)(wl * 9216 + 4608), ws + OFF_PW + (size_t)(wl * 144 + 72 + kb) * 4608, lane);
    }
  }
  MF(8);
  announce(lds0 + (unsigned)(LW_OFF + LW_PK * 4), 4u, ws, CN_PK, isi ? 8 + (job & 3) * 4 + lane : 24 + lane, JOB && lane < (isi ? 4 : 8));
  MF(9);
  if (wl == 0) {
    asm volatile("" : "+v"(f));
#pragma unroll 1
    for (unsigned spin = 0; spin < SPIN_MEM; ++spin) {
      if (!__any(f[0] != (unsigned)tag || f[1] != (unsigned)(tag >> 32))) break;
      __builtin_amdgcn_s_sleep(2);
      asm volatile("global_load_dwordx2 %0, %1, off sc1\n\ts_waitcnt vmcnt(0)" : "=&v"(f) : "v"(tp) : "memory");
    }
  }
}

template <int MT, int NT>
DEVI void k1_gemm(char* smem, const char* ws, const float* __restrict__ csq, int rt, int hp, int mt0, int nt0,
                  int lane MK_ARG) {
  const unsigned lds0 = (unsigned)(size_t)smem;
  const int g = lane >> 4;
  f32x4 cs[NT];
  unsigned ta[NT];
  const char* csp = uni_ptr((const char*)(csq + (hp & 1) * 4));
#pragma unroll
  for (int nt = 0; nt < NT; ++nt) {
    int c = (nt0 + nt) * 16 + g * 4;
    if (c > 104) c = 104;
    const int part = c / 36, ci = c - part * 36;
    asm volatile("global_load_dwordx4 %0, %1, %2 sc1" : "=v"(cs[nt]) : "v"((unsigned)(part * 40 + ci) * 4u), "s"(csp));
    const int to = (nt0 + nt) * 16 + g * 4 > 104 ? 120 : part * 80 + ci * 2 + (ci >= 20 ? 8 : 0);
    ta[nt] = lds0 + (unsigned)(K1_T_OFF + (mt0 * 16 + (lane & 15)) * K1_TP + to);
    asm volatile("" : "+v"(ta[nt]));
  }
  unsigned sa = lds0 + (unsigned)(K1_ST_OFF + (mt0 * 16 + (lane & 15)) * 8);
  asm volatile("" : "+v"(sa));
  f32x4 acc[MT][NT];
#pragma unroll
  for (int mt = 0; mt < MT; ++mt)
#pragma unroll
    for (int nt = 0; nt < NT; ++nt) acc[mt][nt] = f32x4{0.f, 0.f, 0.f, 0.f};
  f32x2 st[MT];
  if constexpr (NT == 7) cnt_wait(ws, CN_X2, rt * 16 + hp, rt ? 24u : 16u);
  bar();
  MK(1);
  {
    GemmMath<K1C, MT, NT, (NT == 7 ? 5 : 3), (NT == 3 ? 6 : NT), true, 2, false, K1_WS> gm(acc);
    gm.setup(lds0, mt0, nt0, lane, 0u);
    bar();
    gm.first();
    gm.template mid<8, true, AL_K>();
#pragma unroll
    for (int mt = 0; mt < MT; ++mt) asm volatile("ds_read_b64 %0, %1 offset:%2" : "=v"(st[mt]) : "v"(sa), "n"(mt * 128));
    asm volatile("s_waitcnt vmcnt(0)" ::: "memory");
    gm.last();
  }
#pragma unroll
  for (int nt = 0; nt < NT; ++nt) asm volatile("" : "+v"(cs[nt]));
#pragma unroll
  for (int mt = 0; mt < MT; ++mt) asm volatile("" : "+v"(st[mt]));
  MK(2);
#pragma unroll
  for (int mt = 0; mt < MT; ++mt) {
    f32x2 M = {st[mt][0], st[mt][0]}, R = {st[mt][1], st[mt][1]};
    asm volatile("" : "+v"(M), "+v"(R));
#pragma unroll
    for (int nt = 0; nt < NT; ++nt) {
      const f32x2 v0 = R * __builtin_elementwise_fma(M, f32x2{cs[nt][0], cs[nt][1]}, f32x2{acc[mt][nt][0], acc[mt][nt][1]});
      const f32x2 v1 = R * __builtin_elementwise_fma(M, f32x2{cs[nt][2], cs[nt][3]}, f32x2{acc[mt][nt][2], acc[mt][nt][3]});
      acc[mt][nt][0] = __uint_as_float(pack2v(v0));
      acc[mt][nt][1] = __uint_as_float(pack2v(v1));
      const f32x2 pk = {acc[mt][nt][0], acc[mt][nt][1]};
      asm volatile("ds_write_b64 %0, %1 offset:%2" ::"v"(ta[nt]), "v"(pk), "n"(mt * 16 * K1_TP) : "memory");
    }
  }
}

DEVI float vmax(float a, float b) {
  float r;
  asm("v_max_f32 %0, %1, %2" : "=v"(r) : "v"(a), "v"(b));
  return r;
}
struct Attn {
  unsigned ka, kb, va, vb, m0, m1, m2, pm[2], oa, pa;
  f32x4 mk;
  unsigned long long sel[4];
  const char* ob;
  DEVI void setup(unsigned T, char* o, int r0, int hp, int wave, int lane) {
    const int g = lane >> 4, r = lane & 15, blk = wave >> 1, hd = wave & 1;
    const unsigned co = (unsigned)(g < 2 ? hd * 48 + g * 16 : 32);
    ka = T + (unsigned)((blk * 32 + 2 * r) * K1_TP) + co;
    kb = T + (unsigned)((blk * 32 + 2 * (r & 7)) * K1_TP) + co;
    const int vr = 4 * g + ((lane >> 2) & 3);
    va = T + (unsigned)((blk * 32 + 2 * vr) * K1_TP + (lane & 3) * 8);
    vb = T + (unsigned)((blk * 32 + 2 * (vr & 7)) * K1_TP + (lane & 3) * 8);
    m0 = (g < 2 || (g == 2 && hd == 0)) ? ~0u : 0u;
    m1 = (g < 2 || (g == 2 && hd == 1)) ? ~0u : 0u;
    m2 = g < 2 ? ~0u : 0u;
    pm[0] = pm[1] = 0u;
    const float ci = __uint_as_float(vconst(0xff800000u));
    const unsigned ch = vconst(0xffffu);
#pragma unroll
    for (int e = 0; e < 4; ++e) {
      const int w1 = r + 8 - 4 * g - e;
      mk[e] = (w1 & 15) <= 7 ? 0.f : ci;
      sel[e] = __ballot(w1 >= 8);
      if (w1 >= 8) pm[e >> 1] |= ch << (16 * (e & 1));
    }
    oa = (unsigned)((8 + hp * 4 + hd * 2 + (g >> 1)) * 1024 + ((blk & 1) * 32 + 2 * r + (g & 1)) * 16);
    pa = ((oa & 1023u) >> 2) + (unsigned)((hp * 2 + hd) * 256);
    ob = uni_ptr(o + (size_t)((r0 >> 6) + (wave >> 2)) * (72 * 1024));
    asm volatile("" : "+v"(ka), "+v"(kb), "+v"(va), "+v"(vb), "+v"(m0), "+v"(m1), "+v"(m2), "+v"(pm[0]), "+v"(pm[1]), "+v"(oa));
    asm volatile("" : "+v"(mk), "+v"(pa), "+s"(ob), "+s"(sel[0]), "+s"(sel[1]), "+s"(sel[2]), "+s"(sel[3]));
  }
  DEVI void run(int r0, int hp, int wave, int lane) {
    const int hd = wave & 1;
    u32x4 q[2], k0[2], k1[2];
    u32x2 v[2][2][2];
#pragma unroll
    for (int p = 0; p < 2; ++p) {
      asm volatile("ds_read_b128 %0, %1 offset:%2" : "=v"(q[p]) : "v"(ka), "n"((16 + p) * K1_TP) : "memory");
      asm volatile("ds_read_b128 %0, %1 offset:%2" : "=v"(k0[p]) : "v"(ka), "n"(p * K1_TP + 80) : "memory");
      asm volatile("ds_read_b128 %0, %1 offset:%2" : "=v"(k1[p]) : "v"(kb), "n"((32 + p) * K1_TP + 80) : "memory");
    }
    const unsigned vh = (unsigned)hd * 48u;
#pragma unroll
    for (int p = 0; p < 2; ++p) {
      asm volatile("ds_read_b64_tr_b16 %0, %1 offset:%2" : "=v"(v[p][0][0]) : "v"(va + vh), "n"(p * K1_TP + 160) : "memory");
      asm volatile("ds_read_b64_tr_b16 %0, %1 offset:%2" : "=v"(v[p][0][1]) : "v"(vb + vh), "n"((32 + p) * K1_TP + 160) : "memory");
      asm volatile("ds_read_b64_tr_b16 %0, %1 offset:%2" : "=v"(v[p][1][0]) : "v"(va), "n"(p * K1_TP + 192) : "memory");
      asm volatile("ds_read_b64_tr_b16 %0, %1 offset:%2" : "=v"(v[p][1][1]) : "v"(vb), "n"((32 + p) * K1_TP + 192) : "memory");
    }
    const u32x4 one4 = {vconst(0x3f803f80u), vconst(0x3f803f80u), vconst(0x3f803f80u), vconst(0x3f803f80u)};
    const s16x8 ones = __builtin_bit_cast(s16x8, one4);
    const float c = 0.23570226039551584f * 1.4426950408889634f;
    const f32x4 z = {0.f, 0.f, 0.f, 0.f};
    f32x4 sc[2][2];
#pragma unroll
    for (int p = 0; p < 2; ++p) {
      asm volatile("s_waitcnt lgkmcnt(%3)" : "+v"(q[p]), "+v"(k0[p]), "+v"(k1[p]) : "n"(11 - 3 * p));
      const s16x8 qf = __builtin_bit_cast(s16x8, q[p] & u32x4{m0, m1, m2, m2});
      sc[p][0] = __builtin_amdgcn_mfma_f32_16x16x32_bf16(__builtin_bit_cast(s16x8, k0[p]), qf, mk, 0, 0, 0);
      sc[p][1] = __builtin_amdgcn_mfma_f32_16x16x32_bf16(__builtin_bit_cast(s16x8, k1[p]), qf, mk, 0, 0, 0);
      __builtin_amdgcn_sched_barrier(0);
    }
    if (__builtin_expect(r0 == 0 && wave < 2, 0)) {
#pragma unroll
      for (int p = 0; p < 2; ++p)
#pragma unroll
        for (int e = 0; e < 4; ++e)
          if (lane < 32) sc[p][0][e] = mk[e] - 4.242640687e9f;
    }
    float lg[2][4], mx[2];
#pragma unroll
    for (int p = 0; p < 2; ++p) {
#pragma unroll
      for (int e = 0; e < 4; ++e) lg[p][e] = __builtin_amdgcn_inverse_ballot_w64(sel[e]) ? sc[p][1][e] : sc[p][0][e];
      asm("v_max3_f32 %0, %1, %2, %3\n\tv_max_f32 %0, %0, %4" : "=&v"(mx[p]) : "v"(lg[p][0]), "v"(lg[p][1]), "v"(lg[p][2]), "v"(lg[p][3]));
    }
    {
      const auto a = __builtin_amdgcn_permlane32_swap(__float_as_uint(mx[0]), __float_as_uint(mx[1]), false, false);
      const unsigned b = __float_as_uint(vmax(__uint_as_float(a[0]), __uint_as_float(a[1])));
      const auto d = __builtin_amdgcn_permlane16_swap(b, b, false, false);
      const unsigned f = __float_as_uint(vmax(__uint_as_float(d[0]), __uint_as_float(d[1])) * -c);
      const auto h = __builtin_amdgcn_permlane32_swap(f, f, false, false);
      mx[0] = __uint_as_float(h[0]);
      mx[1] = __uint_as_float(h[1]);
    }
#pragma unroll
    for (int p = 0; p < 2; ++p)
#pragma unroll
      for (int e = 0; e < 4; ++e) lg[p][e] = __builtin_amdgcn_exp2f(__builtin_fmaf(lg[p][e], c, mx[p]));
    asm volatile("s_waitcnt lgkmcnt(0)"
                 : "+v"(v[0][0][0]), "+v"(v[0][0][1]), "+v"(v[0][1][0]), "+v"(v[0][1][1]), "+v"(v[1][0][0]), "+v"(v[1][0][1]),
                   "+v"(v[1][1][0]), "+v"(v[1][1][1]));
    __builtin_amdgcn_sched_barrier(0);
    f32x4 su[2], o0[2], o1[2];
    s16x8 pp[2];
#pragma unroll
    for (int p = 0; p < 2; ++p) {
      const unsigned p0 = pack2(lg[p][0], lg[p][1]), p1 = pack2(lg[p][2], lg[p][3]);
      pp[p] = __builtin_bit_cast(s16x8, u32x4{p0 & ~pm[0], p1 & ~pm[1], p0 & pm[0], p1 & pm[1]});
    }
    __builtin_amdgcn_sched_barrier(0);
#pragma unroll
    for (int p = 0; p < 2; ++p) {
      const s16x8 pf = pp[p];
      const u32x4 v0 = {v[p][0][0][0], v[p][0][0][1], v[p][0][1][0], v[p][0][1][1]};
      const u32x4 v1 = {v[p][1][0][0], v[p][1][0][1], v[p][1][1][0], v[p][1][1][1]};
      su[p] = __builtin_amdgcn_mfma_f32_16x16x32_bf16(ones, pf, z, 0, 0, 0);
      o0[p] = __builtin_amdgcn_mfma_f32_16x16x32_bf16(__builtin_bit_cast(s16x8, v0), pf, z, 0, 0, 0);
      o1[p] = __builtin_amdgcn_mfma_f32_16x16x32_bf16(__builtin_bit_cast(s16x8, v1), pf, z, 0, 0, 0);
    }
    __builtin_amdgcn_sched_barrier(0);
    u32x2 y[2], yl;
    unsigned hm = (unsigned)hd - 1u;
    asm volatile("" : "+s"(hm));
#pragma unroll
    for (int p = 0; p < 2; ++p) {
      const float inv = __builtin_amdgcn_rcpf(su[p][0]);
      y[p] = u32x2{pack2(o0[p][0] * inv, o0[p][1] * inv), pack2(o0[p][2] * inv, o0[p][3] * inv)};
      const unsigned la = pack2(o1[p][0] * inv, o1[p][1] * inv), lb = pack2(o1[p][2] * inv, o1[p][3] * inv);
      yl[p] = (la & hm) | (lb & ~hm);
    }
    const auto s0 = __builtin_amdgcn_permlane16_swap(y[0][0], y[1][0], false, false);
    const auto s1 = __builtin_amdgcn_permlane16_swap(y[0][1], y[1][1], false, false);
    const u32x4 w = {s0[0], s1[0], s0[1], s1[1]};
    asm volatile("global_store_dwordx4 %0, %1, %4 sc1\n\ts_mov_b64 exec, 0xffff\n\tglobal_store_dwordx2 %2, %3, %4 sc1\n\ts_mov_b64 exec, -1\n\ts_nop 0" ::"v"(oa),
                 "v"(w), "v"(pa), "v"(yl), "s"(ob)
                 : "memory");
  }
};

#define NL_MAIN 707
DEVI void code_again(const char* code0, int bid) {
  constexpr int PER = (2 * NL_MAIN + 31) / 32;
  const int lane = lane_id();
  int li = (bid >> 3) * PER + (lane < PER ? lane : PER - 1);
  if (li > 2 * NL_MAIN - 1) li = 2 * NL_MAIN - 1;
  unsigned t;
  asm volatile("global_load_dword %0, %1, %2\n\ts_waitcnt vmcnt(0)" : "=&v"(t) : "v"((unsigned)li * 64u), "s"(code0) : "memory");
}
DEVI void math_start(char* smem, const char* code0, const u16* b_sp, const u16* wqkv, char* ws, int bid, int wave, int lane) {
  const unsigned lds0 = (unsigned)(size_t)smem;
  const int tid = wave * 64 + lane, idx = bid >> 3;
  if (wave < 3) {
    static_assert(2 * SZ_ZP == (size_t)256 * 2048 && NCNT <= 16, "mark split");
    const u32x4 mk = {0xffffffffu, 0xffffffffu, 0xffffffffu, 0xffffffffu};
    char* c = ws + OFF_CNT + (size_t)((lane >> 3) * 256 + bid) * 128 + (lane & 7) * 16;
    pk_st16(wave < 2 ? ws + OFF_ZP + (size_t)((bid * 2 + wave) * 64 + lane) * 16 : c, mk);
    if (wave == 2 && lane < (NCNT - 8) * 8) pk_st16(c + 8 * 256 * 128, mk);
  }
  if (wave == 3) {
    const int li = idx * 54 + (lane < 54 ? lane : 53);
    touch64(uni_ptr((const char*)wqkv), (unsigned)(li * 1152 + 512), lds0 + (unsigned)(K1R_DUMMY + 768));
  }
  if (!CT0 && wave == 4) {
    constexpr int PER = (2 * NL_MAIN + 31) / 32;
    static_assert(PER <= 64, "one load");
    int li = idx * PER + (lane < PER ? lane : PER - 1);
    if (li > 2 * NL_MAIN - 1) li = 2 * NL_MAIN - 1;
    touch64(code0, (unsigned)li * 64u, lds0 + (unsigned)(K1R_DUMMY + 768));
  }
  if (wave == 5 && idx == 31) touch64(uni_ptr((const char*)b_sp), (unsigned)lane * 64u, lds0 + (unsigned)(K1R_DUMMY + 768));
  const u32x4 ones = {0x3f803f80u, 0x3f803f80u, 0x3f803f80u, 0x3f803f80u};
  if (tid < 256) *reinterpret_cast<u32x4*>(smem + (tid >> 6) * K1R::SB + K1R::XB + (tid & 63) * 288 + 240) = ones;
  else if (tid < 288) *reinterpret_cast<u32x4*>(smem + ((tid - 256) >> 3) * K1R::SB + (tid & 7) * 16) = ones;
  else if (tid < 296) *reinterpret_cast<u32x4*>(smem + LW_OFF + (tid - 288) * 16) = u32x4{0u, 0u, 0u, 0u};
  asm volatile("s_waitcnt lgkmcnt(0)" ::: "memory");
}

#define SAFE_OUT(L)
#define NOP_(n) "=s"(n)
template <int NST, int NS, int NSW, int WOFF, int XW, int X0, int S0, unsigned BAR>
struct ResX {
  static constexpr int WS = 4 * 4608, R = 10, V1 = 5 * NS, NV = V1 + 10 * (NST - NS), NG = (NSW + 2) / 3;
  s16x8 xf[XW][2];
  s16x8 wf[R];
  unsigned wrd[2][NG], xo, ea, fa, fv, tag;
  const char* xp[3];
  f32x4 (&acc)[2][5];
  DEVI ResX(f32x4 (&a)[2][5]) : acc(a) {}
  static constexpr int st(int V) { return V < V1 ? V / 5 : NS + (V - V1) / 10; }
  static constexpr int hf(int V) { return V < V1 ? 0 : ((V - V1) / 5) & 1; }
  static constexpr bool first(int V) { return V < V1 ? V % 5 == 0 : (V - V1) % 10 == 0; }
  static constexpr int wreq(int V) {
    int n = V + 5;
    if (V >= V1 - 4) n += V < V1 ? V - (V1 - 5) : 4;
    return n < NV ? n : NV;
  }
  static constexpr int XU = V1 + 1, XF = V1 + 11, XR = V1 + 15, XC = V1 + 21;
  static constexpr int oth(int V) { return NS && ((V >= XU && V <= XF && (V - XU) % 2 == 0) || V == XR || (V > XC && V <= XC + 5)) ? 1 : 0; }
  static constexpr int newer(int V) {
    const int F = V + 1 < NV ? V + 1 : V;
    int v = V - 1, n = 0;
    for (; v >= 0 && wreq(v - 1) > F; --v) n += wreq(v) - wreq(v - 1) + oth(v);
    return n + wreq(v) - 1 - F + (v >= 0 ? oth(v) : 0);
  }
  static constexpr int xreq(int T) {
    int n = X0 + 2 * (T + 1);
    if (n > T + XW) n = T + XW;
    if (NS && T <= 11 && n > 18) n = 18;
    if (T < 0) n = X0;
    return n < NST ? n : NST;
  }
  static_assert(xreq(NS - 2) == NST && (NS == 0 || XC + 5 < NV), "the last steps only compute and swap");
  template <int T, int M>
  DEVI void xld() {
    asm volatile("global_load_dwordx4 %0, %1, %2 sc1" : "=v"(xf[T % XW][M]) : "v"(xo), "s"(xp[M == 0 ? 0 : (T < NS ? 1 : 2)]));
    if constexpr (M == 1 && T + 1 < NST) asm volatile("v_add_u32 %0, 0x2000, %0" : "+v"(xo));
  }
  template <int V>
  DEVI void rd() {
    constexpr int T = st(V), sl = (T / 2 + S0) % NSW;
    asm volatile("ds_read_b128 %0, %1 offset:%2" : "=v"(wf[V % R]) : "v"(wrd[hf(V)][sl / 3]), "n"((sl % 3) * WS + (T & 1) * 9216 + (V % 5) * 32));
  }
  template <int V, int M, int H>
  DEVI void mf() {
    acc[M][V % 5] = __builtin_amdgcn_mfma_f32_16x16x32_bf16(wf[V % R], xf[st(V) % XW][H], acc[M][V % 5], 0, 0, 0);
    __builtin_amdgcn_sched_barrier(0);
  }
  template <int V>
  DEVI void unit() {
    constexpr int T = st(V), n = V % 5, nx = xreq(T) - xreq(T - 1), nw = wreq(V) - wreq(V - 1);
    constexpr int vm = 2 * (xreq(T - 1) - 1 - T);
    static_assert(newer(V) <= 15, "lgkmcnt range");
    if constexpr (first(V) && V % 2 == 0) asm volatile("s_waitcnt vmcnt(%0) lgkmcnt(%1)" ::"n"(vm), "n"(newer(V)));
    else if constexpr (first(V)) asm volatile("s_waitcnt vmcnt(%0)" ::"n"(vm));
    else if constexpr (V % 2 == 0) asm volatile("s_waitcnt lgkmcnt(%0)" ::"n"(newer(V)));
    __builtin_amdgcn_sched_barrier(0);
    if constexpr (V < V1) mf<V, V & 1, V & 1>();
    else mf<V, 0, hf(V)>();
    if constexpr (nw > 0) rd<wreq(V - 1)>();
    __builtin_amdgcn_sched_barrier(0);
    if constexpr (V < V1) {
      mf<V, 1 - (V & 1), 1 - (V & 1)>();
      if constexpr (nw > 1) rd<wreq(V - 1) + 1>();
      if constexpr (nx == 2 && n < 4) xld<xreq(T - 1) + n / 2, n % 2>();
      if constexpr (nx == 1 && n == (T & 1)) xld<xreq(T - 1), 0>();
      if constexpr (nx == 1 && n == (T & 1) + 2) xld<xreq(T - 1), 1>();
      if constexpr (n == 4 && (T & 1) && ((BAR >> (T / 2)) & 1u)) bar();
    } else if constexpr (NS > 0) {
      if constexpr (oth(V) && V < XF) asm volatile("ds_write_b128 %0, %1 offset:%2" ::"v"(ea), "v"(acc[1][(V - XU) / 2]), "n"((V - XU) * 512));
      if constexpr (V == XF) asm volatile("ds_write_b32 %0, %1" ::"v"(fa), "v"(fv));
      if constexpr (V == XF + 2) asm volatile("v_xor_b32 %0, 4, %0" : "+v"(fa));
      if constexpr (V == XR) asm volatile("ds_read_b32 %0, %1" : "+v"(fv) : "v"(fa));
      if constexpr (V == XR + 2) asm volatile("v_xor_b32 %0, 0x2000, %0" : "+v"(ea));
      if constexpr (V == XC) {
        static_assert(wreq(XR) <= XC + 1 || true, "");
        unsigned c = 1u << 18;
        asm volatile(
            "s_waitcnt lgkmcnt(%4)\n\tv_cmp_ne_u32 vcc, %3, %0\n\ts_cbranch_vccnz .Lw%=\n\t.subsection 1\n.Lw%=:\n\tds_read_b32 %0, %2\n\t"
            "s_waitcnt lgkmcnt(0)\n\tv_cmp_ne_u32 vcc, %3, %0\n\t" SAFE_OUT(".Lr%=") "s_cbranch_vccnz .Lw%=\n\ts_branch .Lr%=\n\t.subsection 0\n.Lr%=:"
            : "+v"(fv), NOP_(c)
            : "v"(fa), "s"(tag), "n"(wreq(XC) - wreq(XR))
            : "vcc", "scc");
      }
      if constexpr (oth(V) && V > XC) asm volatile("ds_read_b128 %0, %1 offset:%2" : "=v"(acc[1][V - XC - 1]) : "v"(ea), "n"((V - XC - 1) * 1024));
    }
    __builtin_amdgcn_sched_barrier(0);
  }
  template <int... V>
  DEVI void units(std::integer_sequence<int, V...>) {
    (unit<V>(), ...);
  }
  DEVI void run() {
    units(std::make_integer_sequence<int, NV>{});
    if constexpr (NS > 0) {
      asm volatile("s_waitcnt lgkmcnt(0)" : "+v"(acc[1][0]), "+v"(acc[1][1]), "+v"(acc[1][2]), "+v"(acc[1][3]), "+v"(acc[1][4]));
#pragma unroll
      for (int i = 0; i < 5; ++i) acc[0][i] += acc[1][i];
    }
  }
  template <int... V>
  DEVI void pro(std::integer_sequence<int, V...>) {
    (rd<V>(), ...);
  }
  DEVI void prow() { pro(std::make_integer_sequence<int, wreq(-1)>{}); }
  template <int T0, int... T>
  DEVI void prox(std::integer_sequence<int, T...>) {
    ((xld<T0 + T, 0>(), xld<T0 + T, 1>()), ...);
  }
  DEVI void setup(unsigned lds0, const char* xbase, int wave, int lane, unsigned red, unsigned nth) {
    const int g = lane >> 4, r = lane & 15, kq = wave & 1;
    xp[0] = xbase + wave * 256;
    xp[1] = xbase + (wave ^ 1) * 256;
    xp[2] = xp[0] + (kq ? -4096 : 4096);
    xo = (unsigned)((4 * kq + g) * 1024 + r * 16);
#pragma unroll
    for (int i = 0; i < 2 * NG; ++i) {
      wrd[i / NG][i % NG] = lds0 + (unsigned)(WOFF + (i % NG) * 3 * WS + (kq ^ (i / NG)) * 4608 + (2 * (r & 7) + 16 * (g & 1) + (g >> 1)) * 144 + (r >> 3) * 16);
      if (i < NG || NS == 0 || i % NG == ((NS / 2 + S0) % NSW) / 3) asm volatile("" : "+v"(wrd[i / NG][i % NG]));
    }
    asm volatile("" : "+v"(xo));
    if constexpr (NS > 0) {
      ea = lds0 + red + (unsigned)((wave ^ 1) * 8192 + lane * 16);
      fa = lds0 + (unsigned)(LW_OFF + (LW_PAIR + wave) * 4);
      fv = tag = nth;
      asm volatile("" : "+v"(ea), "+v"(fa), "+v"(fv));
    }
  }
  template <int N>
  DEVI void init(u32x2 (&xv)[5]) {
    asm volatile("s_waitcnt vmcnt(%5)" : "+v"(xv[0]), "+v"(xv[1]), "+v"(xv[2]), "+v"(xv[3]), "+v"(xv[4]) : "n"(N));
#pragma unroll
    for (int i = 0; i < 5; ++i) {
      acc[0][i] = f32x4{bf_lo(xv[i][0]), bf_hi(xv[i][0]), bf_lo(xv[i][1]), bf_hi(xv[i][1])};
      asm volatile("" : "+v"(acc[0][i]));
      acc[1][i] = f32x4{0.f, 0.f, 0.f, 0.f};
    }
  }
};

#define P5_XW 8
#define P5_X0 5
constexpr unsigned P5_BAR = 0x2aaau;
constexpr int P5_W = 15 * 1024;
constexpr int P2_W = K1_ST_OFF - 18 * 4608;
constexpr int P5_RED = 0;
static_assert(P5_RED % 16384 == 0 && P5_RED + 3 * 8192 + 5120 <= P5_W + 18432, "exchange area");
static_assert(P2_W >= K1_T_OFF + 144 * K1_TP + 64 && P2_W % 16 == 0 && P5_W + 8 * 18432 <= LW_OFF, "LDS placement");

DEVI void res_ldx(u32x2 (&xv)[5], const char* src, bool rm, int rb, int ct, int wave, int lane) {
  const int g = lane >> 4, g2 = g & 1, rl = wave * 16 + (lane & 15);
  const char* xp = uni_ptr(rm ? src + ((size_t)rb * 64 * D_ + ct * 72) * 2 : src + (size_t)(rb * 72 + ct * 9) * 1024);
  const unsigned o = rm ? (unsigned)(rl * (D_ * 2) + g * 8) : (unsigned)((g >> 1) * 1024 + rl * 16 + g2 * 8);
  const unsigned o2 = rm ? (unsigned)(rl * (D_ * 2) + g2 * 8 + 128) : (unsigned)(8192 + rl * 16 + g2 * 8);
#pragma unroll
  for (int i = 0; i < 4; ++i) asm volatile("global_load_dwordx2 %0, %1, %2 sc1" : "=v"(xv[i]) : "v"(o), "s"(xp + i * (rm ? 32 : 2048)));
  asm volatile("global_load_dwordx2 %0, %1, %2 sc1" : "=v"(xv[4]) : "v"(o2), "s"(xp));
}

DEVI void p2_gemm(f32x4 (&acc)[2][5], u32x2 (&xv)[5], unsigned lds0, const char* ob, int layer, int wave, int lane MK_ARG) {
  ResX<9, 0, 5, P2_W, 9, 9, 0, 0u> gm(acc);
  gm.setup(lds0, ob, wave, lane, 0u, 0u);
  unsigned a = (gm.xo & ~1023u) | ((gm.xo & 1023u) >> 2);
  gm.xo += 8192u;
  asm volatile("" : "+v"(a), "+v"(gm.xo));
  const char* pp[2] = {gm.xp[0] - wave * 192, gm.xp[2] - wave * 192};
  place<AL_P2>();
  bar();
  MK(6);
  unsigned e[2][4];
#pragma unroll
  for (int d = 0; d < 8; ++d) asm volatile("global_load_dword %0, %1, %2 offset:%3 sc1" : "=v"(e[d / 4][d % 4]) : "v"(a), "s"(pp[d / 4]), "n"((d % 4) * 256));
  gm.template prox<1>(std::make_integer_sequence<int, 8>{});
  lds_inc(lds0 + (unsigned)(LW_OFF + (LW_T + layer) * 4));
  gm.prow();
  gm.template init<24>(xv);
  asm volatile("s_waitcnt vmcnt(16)"
               : "+v"(e[0][0]), "+v"(e[0][1]), "+v"(e[0][2]), "+v"(e[0][3]), "+v"(e[1][0]), "+v"(e[1][1]), "+v"(e[1][2]), "+v"(e[1][3]));
  gm.xf[0][0] = __builtin_bit_cast(s16x8, u32x4{e[0][0], e[0][1], e[0][2], e[0][3]});
  gm.xf[0][1] = __builtin_bit_cast(s16x8, u32x4{e[1][0], e[1][1], e[1][2], e[1][3]});
  __builtin_amdgcn_sched_barrier(0);
  gm.run();
}

struct ResOut {
  const char *xp[3], *sp;
  unsigned xo, so;
  DEVI void setup(char* xim, char* xs, u16* out, bool fin, int rb, int ct, int wave, int lane) {
    const int g = lane >> 4, rl = wave * 16 + (lane & 15);
    xp[0] = uni_ptr(fin ? (const char*)(out + (size_t)rb * 64 * D_ + ct * 72) : xim + (size_t)(rb * 72 + ct * 9) * 1024);
    xp[1] = xp[0] + (fin ? 64 : 4096);
    xp[2] = xp[0] + (fin ? 128 : 8192);
    sp = uni_ptr(xs + (size_t)(rb * 8 + ct) * 512);
    xo = (unsigned)(((g & 1) * 2 + (g >> 1)) * (fin ? 16 : 1024) + rl * (fin ? D_ * 2 : 16));
    so = (unsigned)(rl * 8);
    asm volatile("" : "+v"(xo), "+v"(so), "+s"(xp[0]), "+s"(xp[1]), "+s"(xp[2]), "+s"(sp));
  }
  DEVI void run(const f32x4 (&a)[5], bool fin) {
    u32x2 y[5];
    u32x4 v[3];
#pragma unroll
    for (int nt = 0; nt < 5; ++nt) y[nt] = u32x2{pack2(a[nt][0], a[nt][1]), pack2(a[nt][2], a[nt][3])};
#pragma unroll
    for (int i = 0; i < 3; ++i) {
      const auto s0 = __builtin_amdgcn_permlane16_swap(y[2 * i][0], y[i < 2 ? 2 * i + 1 : 4][0], false, false);
      const auto s1 = __builtin_amdgcn_permlane16_swap(y[2 * i][1], y[i < 2 ? 2 * i + 1 : 4][1], false, false);
      v[i] = u32x4{s0[0], s1[0], s0[1], s1[1]};
    }
    if (fin) {
#define OUTM 0
#define OUTST ""
      asm volatile("global_store_dwordx4 %0, %1, %4" OUTST "\n\tglobal_store_dwordx4 %0, %2, %5" OUTST "\n\ts_mov_b64 exec, 0xffff\n\tglobal_store_dwordx4 %0, %3, %6" OUTST "\n\t"
                   "s_mov_b64 exec, -1\n\ts_nop 0" ::"v"(xo), "v"(v[0]), "v"(v[1]), "v"(v[2]), "s"(xp[0]), "s"(xp[1]), "s"(xp[2])
                   : "memory");
      return;
    }
    asm volatile("global_store_dwordx4 %0, %1, %4 sc1\n\tglobal_store_dwordx4 %0, %2, %5 sc1\n\ts_mov_b64 exec, 0xffff\n\t"
                 "global_store_dwordx4 %0, %3, %6 sc1\n\ts_mov_b64 exec, -1\n\ts_nop 0" ::"v"(xo), "v"(v[0]), "v"(v[1]), "v"(v[2]),
                 "s"(xp[0]), "s"(xp[1]), "s"(xp[2])
                 : "memory");
    const u32x4 one4 = {vconst(0x3f803f80u), vconst(0x3f803f80u), vconst(0x3f803f80u), vconst(0x3f803f80u)};
    const bool lo = __builtin_amdgcn_inverse_ballot_w64(0xffffull);
    v[2] = u32x4{lo ? v[2][0] : 0u, lo ? v[2][1] : 0u, lo ? v[2][2] : 0u, lo ? v[2][3] : 0u};
    f32x4 s = {0.f, 0.f, 0.f, 0.f}, q = s;
#pragma unroll
    for (int i = 0; i < 3; ++i) {
      const s16x8 u = __builtin_bit_cast(s16x8, v[i]);
      q = __builtin_amdgcn_mfma_f32_16x16x32_bf16(u, u, q, 0, 0, 0);
      s = __builtin_amdgcn_mfma_f32_16x16x32_bf16(__builtin_bit_cast(s16x8, one4), u, s, 0, 0, 0);
    }
    float q0 = q[0], q1 = q[1], q2 = q[2], q3 = q[3];
    const bool b0 = __builtin_amdgcn_inverse_ballot_w64(0xaaaaaaaaaaaaaaaaull), b1 = __builtin_amdgcn_inverse_ballot_w64(0xccccccccccccccccull);
    q0 = b0 ? q1 : q0;
    q2 = b0 ? q3 : q2;
    asm("" : "+v"(q0), "+v"(q2));
    const f32x2 sq = {s[0], b1 ? q2 : q0};
    asm volatile("s_mov_b64 exec, %3\n\tglobal_store_dwordx2 %0, %1, %2 sc1\n\ts_mov_b64 exec, -1" ::"v"(so), "v"(sq), "s"(sp), "s"(0xf0000f0000f0000full) : "memory");
  }
};

using K3C = GemmCfg<128, 144>;
constexpr int K3_Z1_OFF = K3C::LDS_BYTES;
constexpr int K3_C2_OFF = K3_Z1_OFF + K3C::XB;
constexpr int K3_ZC_OFF = K3_C2_OFF + 2048;
constexpr int K3_DM_OFF = K3_ZC_OFF + 1024;
constexpr int K3_ST_OFF = K3_Z1_OFF + 4 * 5120;
constexpr int K3_C1_OFF = K3_ST_OFF + 2048;
constexpr int K3_BS_OFF = K3_C1_OFF + 576;
static_assert(K3_BS_OFF + 512 <= LW_OFF, "K3 LDS");
static_assert(P5_W + 19 * 1024 == K3C::SB && K3C::SB == 34 * 1024, "w_out ring");
constexpr int K3_D = 4;

DEVI void red4(float (&t)[4], float& s, float& q) {
#pragma unroll
  for (int j = 0; j < 2; ++j) {
    const auto p = __builtin_amdgcn_permlane16_swap(__float_as_uint(t[j]), __float_as_uint(t[2 + j]), false, false);
    const unsigned v = __float_as_uint(__uint_as_float(p[0]) + __uint_as_float(p[1]));
    const auto w = __builtin_amdgcn_permlane32_swap(v, v, false, false);
    t[j] = __uint_as_float(w[0]) + __uint_as_float(w[1]);
    asm volatile("" : "+v"(t[j]));
  }
  s = t[0];
  q = t[1];
}
DEVI void red8(const f32x4& a, const f32x4& b, float& s, float& q) {
  float t[4];
#pragma unroll
  for (int j = 0; j < 4; ++j) {
    t[j] = a[j] + b[j];
    asm volatile("" : "+v"(t[j]));
  }
  red4(t, s, q);
}
DEVI void red16(const f32x4& a, const f32x4& b, const f32x4& c, const f32x4& d, float& s, float& q) {
  float t[4];
#pragma unroll
  for (int j = 0; j < 4; ++j) {
    float u = a[j] + b[j], v = c[j] + d[j];
    asm volatile("" : "+v"(u), "+v"(v));
    t[j] = u + v;
    asm volatile("" : "+v"(t[j]));
  }
  red4(t, s, q);
}

constexpr int b_k(int s) { return s == 0 ? 0 : (s < 3 ? 6 + s : (s < 5 ? 2 + s : (s == 5 ? 4 : s - 5))); }
template <bool B>
struct UMath {
  static constexpr int D = 4, R = 6, NV = 162, SB = K3C::SB, P = K3C::P;
  s16x8 wf[R], xf[2][2];
  s16x8 (&xc)[4][2][2];
  f32x4 (&acc)[2][9];
  unsigned xa, wa;
  unsigned fa, fw, fv;
  DEVI UMath(f32x4 (&a)[2][9], s16x8 (&x)[4][2][2]) : xc(x), acc(a) {}
  static constexpr int wslot(int s) { return B ? (s + 1) % 4 : s % 4; }
  static constexpr int xslot(int s) { return B ? b_k(s) % 5 : s % 5; }
  static constexpr bool res(int q) { return !B || b_k(q / 2) >= 4; }
#define BPS 9
  static constexpr bool chk(int s) { return s >= 3 && s < 7 && s != BPS; }
  static constexpr bool xop(int v) { return v + D < NV && (v + D) % 9 < 2 && res((v + D) / 9); }
  static constexpr int ops1(int v) { return (xop(v) || (B && ((v % 18 == 9 && chk(v / 18)) || (v % 18 == 0 && v > 0)))) ? 1 : 0; }
  static constexpr int newer(int V) {
    int v0 = (V % 9 == 8 ? V : V + 1) - D, n = 0;
    if (v0 >= 0 && !(V % 9 == 0 && res(V / 9))) n += ops1(v0);
    for (int v = v0 + 1; v < V; ++v)
      if (v >= 0) n += (v + D < NV ? 1 : 0) + ops1(v);
    return n;
  }
  template <int V>
  DEVI void wread() {
    asm volatile("ds_read_b128 %0, %1 offset:%2" : "=v"(wf[V % R]) : "v"(wa), "n"((wslot(V / 18) & 1) * SB + ((V % 18) / 9) * 32 * P + (V % 9) * 32));
  }
  template <int Q, int M>
  DEVI void xread() {
    asm volatile("ds_read_b128 %0, %1 offset:%2" : "=v"(xf[Q & 1][M]) : "v"(xa), "n"((xslot(Q / 2) & 1) * SB + (Q & 1) * 8192 + M * (B ? 256 : 64)));
  }
  template <int V, int M_>
  DEVI void mf() {
    constexpr int M = ((V % 9) & 1) ? 1 - M_ : M_, q = V / 9;
    if constexpr (!B) acc[M][V % 9] = __builtin_amdgcn_mfma_f32_16x16x32_bf16(xf[q & 1][M], wf[V % R], acc[M][V % 9], 0, 0, 0);
    else if constexpr (res(q)) acc[M][V % 9] = __builtin_amdgcn_mfma_f32_16x16x32_bf16(wf[V % R], xf[q & 1][M], acc[M][V % 9], 0, 0, 0);
    else acc[M][V % 9] = __builtin_amdgcn_mfma_f32_16x16x32_bf16(wf[V % R], xc[b_k(q / 2)][q & 1][M], acc[M][V % 9], 0, 0, 0);
    __builtin_amdgcn_sched_barrier(0);
  }
  template <int V>
  DEVI void unit() {
    constexpr int U = V % 18, s = V / 18;
    static_assert(newer(V) <= 15, "lgkmcnt range");
    if constexpr ((V % 9) % 2 == 0 && V > 0) asm volatile("s_waitcnt lgkmcnt(%0)" ::"n"(newer(V)));
    __builtin_amdgcn_sched_barrier(0);
    mf<V, 0>();
    if constexpr (V + D < NV) wread<V + D>();
    if constexpr (s < 8 && U == 13 && (wslot(s + 1) >> 1) != (wslot(s) >> 1))
      asm volatile("v_add_u32 %0, %1, %0" : "+v"(wa) : "n"(((wslot(s + 1) >> 1) - (wslot(s) >> 1)) * 2 * SB));
    __builtin_amdgcn_sched_barrier(0);
    mf<V, 1>();
    if constexpr (xop(V)) xread<(V + D) / 9, (V + D) % 9>();
    if constexpr (s < 8) {
      if constexpr (U == 11 && res(2 * s + 2) && (xslot(s + 1) >> 1) != (xslot(s) >> 1))
        asm volatile("v_add_u32 %0, %1, %0" : "+v"(xa) : "n"(((xslot(s + 1) >> 1) - (xslot(s) >> 1)) * 2 * SB));
      if constexpr (!B && U == 13) __builtin_amdgcn_s_barrier();
      if constexpr (B && U == 9 && chk(s)) asm volatile("ds_read_u8 %0, %1 offset:%2" : "+v"(fv) : "v"(fa), "n"(s == 6 ? 0 : 8));
      unsigned n = 1u << 18;
      if constexpr (B && U == 13 && chk(s)) {
        asm volatile(
            "v_cmp_gt_u32 vcc, %3, %0\n\ts_cbranch_vccnz .Lw%=\n\t.subsection 1\n.Lw%=:\n\tds_read_u8 %0, %2 offset:%4\n\ts_waitcnt lgkmcnt(0)\n\t"
            "v_cmp_gt_u32 vcc, %3, %0\n\t" SAFE_OUT(".Lr%=") "s_cbranch_vccnz .Lw%=\n\ts_branch .Lr%=\n\t.subsection 0\n.Lr%=:"
            : "+v"(fv), NOP_(n)
            : "v"(fa), "n"(s == 6 ? 4 : s + 2), "n"(s == 6 ? 0 : 8)
            : "vcc", "scc");
      }
      if constexpr (B && U == 13 && s == BPS) {
        asm volatile(
            ".La%=:\n\ts_sleep 1\n\tds_read_u8 %0, %2\n\ts_waitcnt lgkmcnt(0)\n\tv_cmp_gt_u32 vcc, 3, %0\n\t" SAFE_OUT(".Lr%=") "s_cbranch_vccnz .La%=\n"
            ".Lb%=:\n\tds_read_u8 %0, %2 offset:8\n\ts_waitcnt lgkmcnt(0)\n\tv_cmp_gt_u32 vcc, %3, %0\n\t" SAFE_OUT(".Lr%=") "s_cbranch_vccnz .Lb%=\n.Lr%=:"
            : "+v"(fv), NOP_(n)
            : "v"(fa), "n"(s + 2)
            : "vcc", "scc");
      }
    }
    if constexpr (B && U == 15) asm volatile("v_mov_b32 %0, %1" : "+v"(fv) : "n"(s + 1));
    if constexpr (B && ((U == 0 && s > 0) || V == NV - 1)) asm volatile("ds_write_b8 %0, %1" ::"v"(fw), "v"(fv));
    __builtin_amdgcn_sched_barrier(0);
  }
  template <int... V>
  DEVI void units(std::integer_sequence<int, V...>) {
    (unit<V>(), ...);
  }
  DEVI void first() {
    if constexpr (!B) {
      xread<0, 0>();
      xread<0, 1>();
    }
    wread<0>();
    wread<1>();
    wread<2>();
    wread<3>();
    asm volatile("s_waitcnt lgkmcnt(0)");
  }
  DEVI void run() {
    units(std::make_integer_sequence<int, NV>{});
#pragma unroll
    for (int m = 0; m < 2; ++m)
#pragma unroll
      for (int n = 0; n < 9; ++n) asm volatile("" : "+v"(acc[m][n]));
  }
};

#define PACE 0
#define G0P 2
#define ZSLEEP 6
DEVI void p3_g0(char* smem, char* __restrict__ ws, int layer, int mi, int ni, int wm, int lane MK_ARG) {
  constexpr int SB = K3C::SB;
  const unsigned lds0 = (unsigned)(size_t)smem;
  const int g = lane >> 4, r = lane & 15;
  const unsigned lw = lds0 + (unsigned)(LW_OFF + layer * 4), me = 1u << (8 * wm);
  f32x4 ac[2][9];
#pragma unroll
  for (int mt = 0; mt < 2; ++mt)
#pragma unroll
    for (int j = 0; j < 9; ++j) ac[mt][j] = f32x4{0.f, 0.f, 0.f, 0.f};
  unsigned ra = lds0 + (unsigned)(K3_ST_OFF + (wm * 32 + g * 8) * 4), ca = lds0 + (unsigned)(K3_C2_OFF + r * 8);
  asm volatile("" : "+v"(ra), "+v"(ca));
  {
    s16x8 xc[4][2][2];
    UMath<false> gm(ac, xc);
    gm.xa = lds0 + (unsigned)(g * 2048 + (wm * 32 + 8 * (r >> 2) + (r & 3)) * 16);
    gm.wa = lds0 + (unsigned)(K3C::XB + (g * 8 + (r & 7)) * K3C::P + (r >> 3) * 16);
    asm volatile("" : "+v"(gm.xa), "+v"(gm.wa));
    place<AL_A>();
    bar();
    gm.first();
    gm.run();
  }
  lds_inc(lw + LW_AD * 4, me);
  MK(11);
  f32x2 z[9][4];
  f32x2 sm[4], sq[4];
  {
    f32x4 rc[4][2];
    f32x2 cs[9];
#pragma unroll
    for (int i = 0; i < 8; ++i)
      asm volatile("ds_read_b128 %0, %1 offset:%2" : "=v"(rc[i / 2][i % 2]) : "v"(ra), "n"((i / 2) * 512 + (i % 2) * 16) : "memory");
#pragma unroll
    for (int j = 0; j < 9; ++j) asm volatile("ds_read_b64 %0, %1 offset:%2" : "=v"(cs[j]) : "v"(ca), "n"(j * 128) : "memory");
    asm volatile("s_waitcnt lgkmcnt(0)"
                 : "+v"(rc[0][0]), "+v"(rc[0][1]), "+v"(rc[1][0]), "+v"(rc[1][1]), "+v"(rc[2][0]), "+v"(rc[2][1]), "+v"(rc[3][0]), "+v"(rc[3][1]));
#pragma unroll
    for (int j = 0; j < 9; ++j) asm volatile("" : "+v"(cs[j]));
    auto RC = [&](int k, int i) { return f32x2{rc[k][i / 2][(i % 2) * 2], rc[k][i / 2][(i % 2) * 2 + 1]}; };
#pragma unroll
    for (int j = 0; j < 9; ++j) {
      f32x2 u[4], w[4];
#pragma unroll
      for (int i = 0; i < 4; ++i) u[i] = __builtin_elementwise_fma(RC(0, i), cs[j], f32x2{ac[i / 2][j][(i % 2) * 2], ac[i / 2][j][(i % 2) * 2 + 1]});
#pragma unroll
      for (int i = 0; i < 4; ++i) w[i] = u[i] * u[i];
#pragma unroll
      for (int i = 0; i < 4; ++i) w[i] = __builtin_elementwise_fma(w[i], RC(3, i), RC(2, i));
#pragma unroll
      for (int i = 0; i < 4; ++i) w[i] = u[i] * w[i];
      __builtin_amdgcn_sched_barrier(0);
#pragma unroll
      for (int i = 0; i < 4; ++i) w[i] = f32x2{__builtin_amdgcn_exp2f(w[i][0]), __builtin_amdgcn_exp2f(w[i][1])};
      __builtin_amdgcn_sched_barrier(0);
#pragma unroll
      for (int i = 0; i < 4; ++i) w[i] = __builtin_elementwise_fma(w[i], RC(1, i), RC(1, i));
      __builtin_amdgcn_sched_barrier(0);
#pragma unroll
      for (int i = 0; i < 4; ++i) w[i] = f32x2{__builtin_amdgcn_rcpf(w[i][0]), __builtin_amdgcn_rcpf(w[i][1])};
      __builtin_amdgcn_sched_barrier(0);
#pragma unroll
      for (int i = 0; i < 4; ++i) z[j][i] = u[i] * w[i];
#pragma unroll
      for (int i = 0; i < 4; ++i) {
        if (j == 1) sm[i] = z[0][i] + z[1][i];
        if (j > 1) sm[i] += z[j][i];
      }
#pragma unroll
      for (int i = 0; i < 4; ++i) sq[i] = j ? __builtin_elementwise_fma(z[j][i], z[j][i], sq[i]) : z[j][i] * z[j][i];
      asm volatile("" : "+v"(z[j][0]), "+v"(z[j][1]), "+v"(z[j][2]), "+v"(z[j][3]));
      __builtin_amdgcn_sched_barrier(0);
    }
  }
  f32x4 e;
  {
    float v[4][4], d[2][4], f[4];
#pragma unroll
    for (int i = 0; i < 4; ++i) {
      v[i][0] = sm[i][0];
      v[i][1] = sq[i][0];
      v[i][2] = sm[i][1];
      v[i][3] = sq[i][1];
    }
    asm volatile("s_nop 1" : "+v"(v[3][1]), "+v"(v[3][3]), "+v"(v[2][1]), "+v"(v[2][3]));
#pragma unroll
    for (int i = 0; i < 2; ++i)
#pragma unroll
      for (int c = 0; c < 4; ++c)
        asm volatile("v_add_f32_dpp %0, %1, %1 row_ror:8 row_mask:0xf bank_mask:0x3\n\tv_add_f32_dpp %0, %2, %2 row_ror:8 row_mask:0xf bank_mask:0xc"
                     : "=&v"(d[i][c])
                     : "v"(v[i][c]), "v"(v[i + 2][c]));
#pragma unroll
    for (int c = 0; c < 4; ++c)
      asm volatile("v_add_f32_dpp %0, %1, %1 row_shl:4 row_mask:0xf bank_mask:0x5\n\tv_add_f32_dpp %0, %2, %2 row_shr:4 row_mask:0xf bank_mask:0xa"
                   : "=&v"(f[c])
                   : "v"(d[0][c]), "v"(d[1][c]));
    asm volatile(
        "v_add_f32_dpp %0, %0, %0 quad_perm:[1,0,3,2] row_mask:0xf bank_mask:0xf\n\t"
        "v_add_f32_dpp %1, %1, %1 quad_perm:[1,0,3,2] row_mask:0xf bank_mask:0xf\n\t"
        "v_add_f32_dpp %2, %2, %2 quad_perm:[1,0,3,2] row_mask:0xf bank_mask:0xf\n\t"
        "v_add_f32_dpp %3, %3, %3 quad_perm:[1,0,3,2] row_mask:0xf bank_mask:0xf\n\t"
        "v_add_f32_dpp %0, %0, %0 quad_perm:[2,3,0,1] row_mask:0xf bank_mask:0xf\n\t"
        "v_add_f32_dpp %1, %1, %1 quad_perm:[2,3,0,1] row_mask:0xf bank_mask:0xf\n\t"
        "v_add_f32_dpp %2, %2, %2 quad_perm:[2,3,0,1] row_mask:0xf bank_mask:0xf\n\t"
        "v_add_f32_dpp %3, %3, %3 quad_perm:[2,3,0,1] row_mask:0xf bank_mask:0xf"
        : "+v"(f[0]), "+v"(f[1]), "+v"(f[2]), "+v"(f[3]));
    e = f32x4{f[0], __uint_as_float(__float_as_uint(f[1]) & 0x7fffffffu), f[2], __uint_as_float(__float_as_uint(f[3]) & 0x7fffffffu)};
  }
  const char* zp = uni_ptr(ws + OFF_ZP + (size_t)layer * SZ_ZP + (size_t)mi * 16384 + wm * 256);
  if ((lane & 3) == 0)
    asm volatile("global_store_dwordx4 %0, %1, %2 sc1\n\ts_nop 1" ::"v"((unsigned)(ni * 1024 + lane * 4)), "v"(e), "s"(zp) : "memory");
  lds_inc(lw + LW_AD * 4, me);
  __builtin_amdgcn_s_setprio(2);
  MK(12);
  const char* dst = uni_ptr(ws + OFF_Z2 + (size_t)layer * SZ_Z + (size_t)ni * S_ * 288 + (size_t)(mi * 4 + wm) * 9216 + 4096);
  const unsigned lane16 = (unsigned)lane * 16u;
  u32x4 wb[9];
  {
    static_assert(b_k(7) == 2 && b_k(8) == 3, "order");
    const char* wp = uni_ptr(ws + OFF_PI + (size_t)ni * (18 * K3C::WB) + 11 * K3C::WB + wm * 9216 + 4096);
#pragma unroll
    for (int j = 0; j < 8; ++j) asm volatile("global_load_dwordx4 %0, %1, %2 offset:%3 sc1" : "=v"(wb[j]) : "v"(lane16), "s"(wp), "n"(j * 1024 - 4096) : "memory");
    asm volatile("global_load_dwordx4 %0, %1, %2 sc1" : "=v"(wb[8]) : "v"(lane16 + 4096u), "s"(wp) : "memory");
  }
  u32x4 y[9];
  {
    const unsigned src = (unsigned)(g * 4096 + r * 16);
    unsigned zw = lds0 + (unsigned)(K3_ZC_OFF + (wm * 32 + 2 * r + (g & 1)) * 4), zr = lds0 + (unsigned)(K3_ZC_OFF + (wm * 32 + g * 8) * 4);
    asm volatile("" : "+v"(zw), "+v"(zr));
    f32x4 a, b, c, d;
    __builtin_amdgcn_s_sleep(ZSLEEP);
#pragma unroll 1
    for (unsigned spin = 0; spin < SPIN_MEM; ++spin) {
      asm volatile(
          "global_load_dwordx4 %0, %4, %5 sc1\n\t"
          "global_load_dwordx4 %1, %4, %5 offset:1024 sc1\n\t"
          "global_load_dwordx4 %2, %4, %5 offset:2048 sc1\n\t"
          "global_load_dwordx4 %3, %4, %5 offset:3072 sc1\n\t"
          "s_waitcnt vmcnt(0)"
          : "=&v"(a), "=&v"(b), "=&v"(c), "=&v"(d)
          : "v"(src), "s"(zp)
          : "memory");
      const unsigned bad = (__float_as_uint(a[1]) | __float_as_uint(a[3]) | __float_as_uint(b[1]) | __float_as_uint(b[3]) |
                            __float_as_uint(c[1]) | __float_as_uint(c[3]) | __float_as_uint(d[1]) | __float_as_uint(d[3])) >> 31;
      if (!__any(bad)) break;
    }
    MK(13);
    float s, q, zmb, zi;
    red16(a, b, c, d, s, q);
    ln_finish(s, q, 1.0f / (float)FH_, zmb, zi);
    f32x4 R[2], M[2];
    asm volatile(
        "ds_write2st64_b32 %4, %5, %6 offset1:2\n\t"
        "ds_read_b128 %0, %7\n\tds_read_b128 %2, %7 offset:512\n\tds_read_b128 %1, %7 offset:16\n\tds_read_b128 %3, %7 offset:528\n\t"
        "s_waitcnt lgkmcnt(0)"
        : "=&v"(R[0]), "=&v"(R[1]), "=&v"(M[0]), "=&v"(M[1])
        : "v"(zw), "v"(zi), "v"(-zmb * zi), "v"(zr)
        : "memory");
    MK(28);
#define NRM 0
    auto st = [&](int j) {
      if (j < 8) asm volatile("global_store_dwordx4 %0, %1, %2 offset:%3 sc1\n\ts_nop 1" ::"v"(lane16), "v"(y[j]), "s"(dst), "n"(j * 1024 - 4096) : "memory");
      else asm volatile("global_store_dwordx4 %0, %1, %2 sc1\n\ts_nop 1" ::"v"(lane16 + 4096u), "v"(y[j]), "s"(dst) : "memory");
    };
#pragma unroll
    for (int j = 0; j < 9; ++j) {
      if (NRM & 2) {
        f32x2 t[4];
#pragma unroll
        for (int k = 0; k < 4; ++k)
          t[k] = __builtin_elementwise_fma(z[j][k], f32x2{R[k / 2][(k % 2) * 2], R[k / 2][(k % 2) * 2 + 1]}, f32x2{M[k / 2][(k % 2) * 2], M[k / 2][(k % 2) * 2 + 1]});
        y[j] = u32x4{pack2v(t[0]), pack2v(t[1]), pack2v(t[2]), pack2v(t[3])};
      } else {
        float t[8];
#pragma unroll
        for (int k = 0; k < 8; ++k) {
          t[k] = __builtin_fmaf(z[j][k / 2][k % 2], R[k / 4][k % 4], M[k / 4][k % 4]);
          asm volatile("" : "+v"(t[k]));
        }
        y[j] = u32x4{pack2(t[0], t[1]), pack2(t[2], t[3]), pack2(t[4], t[5]), pack2(t[6], t[7])};
      }
      if (NRM & 1) asm volatile("" : "+v"(y[j]));
      else st(j);
      __builtin_amdgcn_sched_barrier(0);
    }
    MK(29);
    if (NRM & 1) {
#pragma unroll
      for (int j = 0; j < 9; ++j) st(j);
    }
  }
  lds_inc(lw + LW_AD * 4, me);
  MK(24);
  __builtin_amdgcn_s_setprio(G0P);
  lds_spin4(lw + LW_S * 4, 4u + (unsigned)(wm >> 1));
  {
    const unsigned fa = lds0 + (unsigned)((wm >> 1) * SB + K3C::XB + (wm & 1) * 9216) + lane16;
#pragma unroll
    for (int j = 0; j < 9; ++j) {
      asm volatile("ds_write_b128 %0, %1 offset:%2" ::"v"(fa), "v"(wb[j]), "n"(j * 1024) : "memory");
      if (PACE && j < 8) __builtin_amdgcn_s_sleep(PACE);
    }
    lds_inc(lw + LW_AD * 4, me);
  }
  announce(lw + LW_Z * 4, 4u, ws, CN_Z + layer, lane * 16 + ni, lane < 16);
  lds_inc(lw + LW_AD * 4, me);
  MK(27);
  f32x4 acc[2][9];
  {
    float bs[2];
    asm volatile("ds_read2_b32 %0, %1 offset1:16\n\ts_waitcnt lgkmcnt(0)"
                 : "=v"(*reinterpret_cast<f32x2*>(bs))
                 : "v"(lds0 + (unsigned)(K3_BS_OFF + (wm * 32 + r) * 4))
                 : "memory");
    lds_spin4(lw + LW_S * 4, 6u + (unsigned)(wm >> 1));
    const unsigned fa = lds0 + (unsigned)((2 + (wm >> 1)) * SB + K3C::XB + (wm & 1) * 9216) + lane16;
#pragma unroll
    for (int j = 0; j < 9; ++j) {
      asm volatile("ds_write_b128 %0, %1 offset:%2" ::"v"(fa), "v"(y[j]), "n"(j * 1024) : "memory");
      if (PACE && j < 8) __builtin_amdgcn_s_sleep(PACE);
    }
    lds_inc(lw + LW_AD * 4, me);
#pragma unroll
    for (int mt = 0; mt < 2; ++mt)
#pragma unroll
      for (int j = 0; j < 9; ++j) acc[mt][j] = f32x4{bs[mt], bs[mt], bs[mt], bs[mt]};
  }
  u32x4 z1[9];
  {
    GemmMath<K3C, 2, 9, K3_D, 6, false, 2> gm(acc);
    gm.setup(lds0, wm * 2, 0, lane, (unsigned)(2 * SB));
    MK(15);
    lds_spin4(lw + LW_AD * 4, 6u);
    lds_spin4(lw + LW_L * 4, 8u);
    MK(17);
    gm.first();
    __builtin_amdgcn_s_setprio(0);
    gm.template mid<1, false>();
    gm.template mid<11, true, AL_C>();
    const unsigned za = gm.wrd + (unsigned)(K3_Z1_OFF - K3C::XB + wm * 5120);
#pragma unroll
    for (int j = 0; j < 5; ++j) asm volatile("ds_read_b128 %0, %1 offset:%2" : "=v"(z1[j]) : "v"(za), "n"(j * 1024));
    gm.template mid<10, true, AL_C>();
#pragma unroll
    for (int j = 5; j < 9; ++j) asm volatile("ds_read_b128 %0, %1 offset:%2" : "=v"(z1[j]) : "v"(za), "n"((j - 5) * 1024));
    gm.template mid<9, true, AL_C>();
    gm.last();
  }
  MK(16);
  const char* hp = uni_ptr(ws + OFF_H + (size_t)layer * SZ_Z + (size_t)((mi * 2 + (wm >> 1)) * 288 + ni * 18) * 1024);
  const int l2 = lane_id();
  const unsigned ho = (unsigned)((l2 >> 5) * 1024 + ((wm & 1) * 32 + (l2 & 31)) * 16);
#pragma unroll
  for (int j = 0; j < 9; ++j) {
    asm volatile("" : "+v"(z1[j]));
    u32x2 h[2];
#pragma unroll
    for (int mt = 0; mt < 2; ++mt) {
      const f32x4 a = acc[mt][j];
      h[mt] = u32x2{pack2v(f32x2{bf_lo(z1[j][2 * mt]), bf_hi(z1[j][2 * mt])} * f32x2{a[0], a[1]}),
                    pack2v(f32x2{bf_lo(z1[j][2 * mt + 1]), bf_hi(z1[j][2 * mt + 1])} * f32x2{a[2], a[3]})};
    }
    const auto s0 = __builtin_amdgcn_permlane16_swap(h[0][0], h[1][0], false, false);
    const auto s1 = __builtin_amdgcn_permlane16_swap(h[0][1], h[1][1], false, false);
    const u32x4 v = {s0[0], s1[0], s0[1], s1[1]};
    asm volatile("global_store_dwordx4 %0, %1, %2 offset:%3 sc1\n\ts_nop 1" ::"v"(ho), "v"(v), "s"(hp + (j / 2) * 4096), "n"((j % 2) * 2048) : "memory");
    __builtin_amdgcn_sched_barrier(0);
  }
  MK(18);
  announce(lds0 + (unsigned)(LW_OFF + (LW_H + layer * 2 + (wm >> 1)) * 4), 2u, ws, ((HSPLIT && ni >= 8) ? CN_H2 : CN_H) + layer,
           (mi * 2 + (wm >> 1)) * 8 + l2, l2 < 8);
  MK(19);
}

DEVI void k3_const(char* smem, char* ws, const u16* b_sp, int layer, int bid, int mi, int ni, int wm) {
  const int lane = lane_id(), l = lane < 54 ? lane : 53, p = l / 18;
  const char* kp = uni_ptr(ws + OFF_CSK + bid * 2048 + wm * 512);
  const unsigned q16 = (unsigned)(l - p * 18) * 16u;
  unsigned bs;
  f32x4 t;
  asm volatile("global_load_ushort %0, %1, %2" : "=v"(bs) : "v"((unsigned)(lane & 31) * 2u), "s"(uni_ptr((const char*)(b_sp + mi * 128 + wm * 32))) : "memory");
  if (layer) {
    asm volatile("global_load_dwordx4 %0, %1, %2 sc1\n\ts_waitcnt vmcnt(0)" : "=&v"(t) : "v"(q16), "s"(kp) : "memory");
  } else {
    const char* cp = uni_ptr(ws + OFF_CSP + ni * 1152 + wm * 288);
    f32x4 a[6];
#pragma unroll
    for (int i = 0; i < 6; ++i)
      asm volatile("global_load_dwordx4 %0, %1, %2 sc1" : "=v"(a[i]) : "v"(q16 + (unsigned)(p * (6 * F_ * 4))), "s"(cp + i * (F_ * 4)) : "memory");
    asm volatile("s_waitcnt vmcnt(0)" : "+v"(a[0]), "+v"(a[1]), "+v"(a[2]), "+v"(a[3]), "+v"(a[4]), "+v"(a[5]));
    t = ((a[0] + a[1]) + (a[2] + a[3])) + (a[4] + a[5]);
#pragma unroll
    for (int j = 0; j < 4; ++j)
      t[j] += __int_as_float(__builtin_amdgcn_ds_bpermute((lane + 18) * 4, __float_as_int(t[j]))) +
              __int_as_float(__builtin_amdgcn_ds_bpermute((lane + 36) * 4, __float_as_int(t[j])));
    if (lane < 24) asm volatile("global_store_dwordx4 %0, %1, %2\n\ts_nop 1" ::"v"((unsigned)lane * 16u), "v"(t), "s"(kp) : "memory");
  }
  asm volatile("" : "+v"(bs));
  if (lane < 18) {
    if (wm < 2) {
      *reinterpret_cast<f32x4*>(smem + K3_C2_OFF + wm * 576 + lane * 32) = f32x4{t[0], t[0], t[1], t[1]};
      *reinterpret_cast<f32x4*>(smem + K3_C2_OFF + wm * 576 + lane * 32 + 16) = f32x4{t[2], t[2], t[3], t[3]};
    } else {
      *reinterpret_cast<f32x4*>(smem + K3_C1_OFF + (wm - 2) * 288 + lane * 16) = t;
    }
  }
  if (lane < 32) *reinterpret_cast<float*>(smem + K3_BS_OFF + (wm * 32 + lane) * 4) = bf_lo(bs);
}

DEVI void p3_g1(char* smem, char* __restrict__ ws, const char* code0, int bid, int layer, int mi, int wm, int lane MK_ARG) {
  const unsigned lds0 = (unsigned)(size_t)smem;
  const int g = lane >> 4, r = lane & 15;
  const unsigned lw = lds0 + (unsigned)(LW_OFF + layer * 4);
  const unsigned xrd = lds0 + (unsigned)(g * 2048 + (wm * 32 + r) * 16);
  s16x8 xc[4][2][2];
  {
    const char* xs = uni_ptr(ws + OFF_XS + (size_t)layer * 2 * SZ_XS + (size_t)(mi * 2 + (wm >> 1)) * 4096 + (wm & 1) * 256);
    f32x4 a, b;
    auto pick = [&](int t) {
      bar();
      asm volatile(
          "ds_read_b128 %0, %4\n\tds_read_b128 %1, %4 offset:256\n\tds_read_b128 %2, %4 offset:8192\n\tds_read_b128 %3, %4 offset:8448\n\t"
          "s_waitcnt lgkmcnt(0)"
          : "=&v"(xc[t][0][0]), "=&v"(xc[t][0][1]), "=&v"(xc[t][1][0]), "=&v"(xc[t][1][1])
          : "v"(xrd + (unsigned)(t * K3C::SB))
          : "memory");
    };
#define G1P 2
    __builtin_amdgcn_s_setprio(G1P);
    pick(0);
    pick(1);
    asm volatile("global_load_dwordx4 %0, %2, %3 sc1\n\tglobal_load_dwordx4 %1, %2, %3 offset:512 sc1"
                 : "=&v"(a), "=&v"(b)
                 : "v"((unsigned)(g * 1024 + r * 16)), "s"(xs)
                 : "memory");
    pick(2);
    pick(3);
    bar();
    asm volatile("s_waitcnt vmcnt(0)" : "+v"(a), "+v"(b) : : "memory");
    float s, q, mb, rinv;
    red8(a, b, s, q);
    bar();
    asm volatile("" : "+v"(s), "+v"(q));
    ln_finish(s, q, 1.0f / (float)D_, mb, rinv);
    asm volatile("" : "+v"(mb), "+v"(rinv));
    bar();
    asm volatile("" : "+v"(mb), "+v"(rinv));
    const float k0 = -2.0f * 0.7978845608028654f * 1.4426950408889634f, k1 = k0 * 0.044715f;
    float sd = __builtin_amdgcn_rcpf(rinv), c0 = k0 * rinv, c1 = k1 * rinv * rinv * rinv;
    asm volatile("" : "+v"(sd));
    asm volatile("" : "+v"(c0));
    asm volatile("" : "+v"(c1));
    const float e0 = lane < 32 ? -mb : c0, e1 = lane < 32 ? sd : c1;
    asm volatile("ds_write2st64_b32 %0, %1, %2 offset1:2" ::"v"(
                     lds0 + (unsigned)(K3_ST_OFF + (g >> 1) * 1024 + (wm * 32 + 2 * r + (g & 1)) * 4)),
                 "v"(e0), "v"(e1)
                 : "memory");
    bar();
    asm volatile("s_waitcnt lgkmcnt(0)" ::: "memory");
    bar();
    __builtin_amdgcn_s_setprio(0);
  }
  f32x4 ac[2][9];
#pragma unroll
  for (int mt = 0; mt < 2; ++mt)
#pragma unroll
    for (int j = 0; j < 9; ++j) ac[mt][j] = f32x4{0.f, 0.f, 0.f, 0.f};
  {
    UMath<true> gm(ac, xc);
    gm.xa = xrd;
    gm.wa = lds0 + (unsigned)(K3C::XB + (g * 8 + (r & 7)) * K3C::P + (r >> 3) * 16);
    gm.fa = lw + (unsigned)(LW_AD * 4 + (lane & 3));
    gm.fw = lw + (unsigned)(LW_S * 4 + wm);
    gm.fv = 0u;
    asm volatile("" : "+v"(gm.fa), "+v"(gm.fw), "+v"(gm.fv), "+v"(gm.xa), "+v"(gm.wa));
    lds_spin4<4>(lw + LW_L * 4, 4u);
    gm.first();
    place<AL_B>();
    lds_spin4(lw + LW_AD * 4, 2u);
#define BPR 1
    __builtin_amdgcn_s_setprio(BPR);
    gm.run();
    __builtin_amdgcn_s_setprio(0);
  }
  MK(14);
  f32x2 rc[4];
  f32x4 cs[9];
  const int l2 = lane_id();
  {
    const unsigned ra = lds0 + (unsigned)(K3_ST_OFF + (wm * 32 + (l2 & 15)) * 4), ca = lds0 + (unsigned)(K3_C1_OFF + (l2 & 48));
#pragma unroll
    for (int k = 0; k < 4; ++k) asm volatile("ds_read2_b32 %0, %1 offset1:16" : "=v"(rc[k]) : "v"(ra + (unsigned)(k * 512)) : "memory");
#pragma unroll
    for (int j = 0; j < 9; ++j) asm volatile("ds_read_b128 %0, %1 offset:%2" : "=v"(cs[j]) : "v"(ca), "n"(j * 64) : "memory");
    asm volatile("s_waitcnt lgkmcnt(0)" : "+v"(rc[0]), "+v"(rc[1]), "+v"(rc[2]), "+v"(rc[3]));
#pragma unroll
    for (int j = 0; j < 9; ++j) asm volatile("" : "+v"(cs[j]));
  }
  const unsigned za = lds0 + (unsigned)(K3_Z1_OFF + wm * 5120 + l2 * 16);
  u32x4 z1[9];
#pragma unroll
  for (int t = 0; t < 18; ++t) {
    const int j = t / 2, mt = t % 2;
    float u[4], w[4];
#pragma unroll
    for (int i = 0; i < 4; ++i) asm volatile("v_fma_f32 %0, %1, %2, %3" : "=v"(u[i]) : "v"(rc[0][mt]), "v"(cs[j][i]), "v"(ac[mt][j][i]));
#pragma unroll
    for (int i = 0; i < 4; ++i) asm volatile("v_mul_f32 %0, %1, %1" : "=v"(w[i]) : "v"(u[i]));
#pragma unroll
    for (int i = 0; i < 4; ++i) asm volatile("v_fma_f32 %0, %0, %1, %2" : "+v"(w[i]) : "v"(rc[3][mt]), "v"(rc[2][mt]));
#pragma unroll
    for (int i = 0; i < 4; ++i) asm volatile("v_mul_f32 %0, %0, %1" : "+v"(w[i]) : "v"(u[i]));
#pragma unroll
    for (int i = 0; i < 4; ++i) asm volatile("v_exp_f32 %0, %0" : "+v"(w[i]));
#pragma unroll
    for (int i = 0; i < 4; ++i) asm volatile("v_fma_f32 %0, %0, %1, %1" : "+v"(w[i]) : "v"(rc[1][mt]));
#pragma unroll
    for (int i = 0; i < 4; ++i) asm volatile("v_rcp_f32 %0, %0" : "+v"(w[i]));
#pragma unroll
    for (int i = 0; i < 4; ++i) asm volatile("v_mul_f32 %0, %0, %1" : "+v"(w[i]) : "v"(u[i]));
    z1[j][2 * mt] = pack2(w[0], w[1]);
    z1[j][2 * mt + 1] = pack2(w[2], w[3]);
    if (mt && j < 5) asm volatile("ds_write_b128 %0, %1 offset:%2" ::"v"(za), "v"(z1[j]), "n"(j * 1024) : "memory");
    if (t == 17) {
#pragma unroll
      for (int k = 5; k < 9; ++k) asm volatile("ds_write_b128 %0, %1 offset:%2" ::"v"(za), "v"(z1[k]), "n"((k - 5) * 1024) : "memory");
    }
    if (t == 9 || t == 17) asm volatile("s_waitcnt lgkmcnt(0)" ::: "memory");
    __builtin_amdgcn_sched_barrier(0);
    bar();
    __builtin_amdgcn_sched_barrier(0);
  }
  MK(15);
#pragma unroll 1
  for (int t = 20; t < 32; ++t) bar();
  if ((RTC & 2) && wm == 2) code_again(code0, bid);
}

constexpr int W5A = 22, W5B = 26;
constexpr int NPF = 30;
struct Loader {
  u32x4 pf[NPF];
  unsigned lds0, lane16;
  int wl, lane;

  template <int R0, int N, int P0, int PE>
  DEVI void pf_load(const char* src) {
    const unsigned o = lane16 + (unsigned)(wl * 1024);
#pragma unroll
    for (int t = 0; t < N; ++t) {
      unsigned a = o + (unsigned)((P0 + 4 * t) * 1024);
      if (P0 + 4 * t + 3 >= PE && P0 + 4 * t + wl >= PE) a = lane16;
      asm volatile("global_load_dwordx4 %0, %1, %2 sc1" : "=v"(pf[R0 + t]) : "v"(a), "s"(src) : "memory");
    }
  }
  template <int R0, int N>
  DEVI void pf_pin() {
#pragma unroll
    for (int t = 0; t < N; ++t) asm volatile("" : "+v"(pf[R0 + t]));
  }
  template <int R0, int N>
  DEVI void pf_undef() {
#pragma unroll
    for (int t = 0; t < N; ++t) asm volatile("" : "=v"(pf[R0 + t]));
  }
  template <int PB, int A, int N, int SL = 0>
  DEVI void pf_put(unsigned dst) {
    constexpr int T0 = (A - PB) / 4, T1 = (A + N - 1 - PB) / 4;
    const unsigned a = lds0 + dst + lane16 + (unsigned)((wl + PB + 4 * T0 - A + 4) * 1024) - 4096u;
#pragma unroll
    for (int t = T0; t <= T1; ++t) {
      const int p0 = PB + 4 * t;
      if ((p0 >= A || p0 + wl >= A) && (p0 + 3 < A + N || p0 + wl < A + N))
        asm volatile("ds_write_b128 %0, %1 offset:%2" ::"v"(a), "v"(pf[t]), "n"((t - T0) * 4096) : "memory");
      if (SL) __builtin_amdgcn_s_sleep(SL);
    }
  }
  template <int N>
  DEVI void dma_run(const char* src, unsigned dst) {
    const unsigned o = lane16 + (unsigned)(wl * 1024), m = lds0 + dst + (unsigned)(wl * 1024);
#pragma unroll
    for (int i = 0; i < (N + 3) / 4; ++i)
      if (4 * i + 3 < N || 4 * i + wl < N) dma16_sc1(src + i * 4096, o, m + (unsigned)(i * 4096));
  }
  template <int V>
  DEVI void wait_w() {
    asm volatile("s_cmp_lt_u32 %0, 2\n\ts_cbranch_scc1 1f\n\ts_waitcnt vmcnt(%1)\n1:\n\ts_waitcnt vmcnt(%2)" ::"s"(wl), "n"(V - 1), "n"(V) : "memory", "scc");
  }
  template <int V>
  DEVI void wbar() {
    asm volatile("s_waitcnt vmcnt(%0) lgkmcnt(0)" ::"n"(V) : "memory");
    bar();
  }

  DEVI void p1(char* smem, char* ws, int rt, int hp MK_ARG) {
    constexpr int SB = K1C::SB;
    static_assert(K1C::XB == 18 * 1024 && K1C::WB == 14 * 1024, "K1 shape");
    const char* wp = uni_ptr(ws + OFF_PQ + (size_t)hp * (9 * K1C::WB) + 4 * K1C::WB);
    static_assert(3 * SB + K1C::XB >= P5_W + 5 * 18432, "free while the GEMM before ends");
    const char* wq = wp - 4 * K1C::WB;
    dma_run<14>(wq + 3 * K1C::WB, (unsigned)(3 * SB + K1C::XB));
    dma_run<28>(wp, (unsigned)K1_WX);
    if (P1PR & 1) __builtin_amdgcn_s_setprio(0);
    if (P1PR & 2) wait_vm<0>();
    const bool lo = wl < 2;
    const int x0 = lo ? wl * 5 : wl * 4 + 2, w0 = lo ? wl * 3 : wl * 4 - 2;
    unsigned xo[5];
#pragma unroll
    for (int i = 0; i < 5; ++i) {
      const int c = (x0 + i) * 64 + lane;
      const int ch = c / 144;
      int gr = rt * 128 - 16 + (c - ch * 144);
      if (gr < 0) gr = 0;
      xo[i] = (unsigned)(((gr >> 6) * 72 + ch) * 1024 + (gr & 63) * 16);
      asm volatile("" : "+v"(xo[i]));
    }
    const char* xp = uni_ptr(ws + OFF_X1 + SZ_X);
    wp += 2 * K1C::WB + w0 * 1024;
    auto dma_x = [&](unsigned so) {
      const unsigned m = lds0 + so + (unsigned)(x0 * 1024);
#pragma unroll
      for (int i = 0; i < 4; ++i) dma16_sc1(xp, xo[i], m + (unsigned)(i * 1024));
      if (lo) dma16_sc1(xp, xo[4], m + 4096u);
      xp += 8192;
    };
    auto dma_w = [&](unsigned so) {
      const unsigned m = lds0 + so + (unsigned)(K1C::XB + w0 * 1024);
#pragma unroll
      for (int i = 0; i < 3; ++i) dma16_sc1(wp + i * 1024, lane16, m + (unsigned)(i * 1024));
      if (!lo) dma16_sc1(wp + 3072, lane16, m + 3072u);
      wp += K1C::WB;
    };
    const char* xs = uni_ptr(ws + OFF_XS + SZ_XS);
    const int g = lane >> 4, p2 = 64 + (lane & 7);
    int ra = rt * 128 - 16 + wl * 32 + 2 * (lane & 15);
    if (ra < 0) ra = 0;
    unsigned sa = (unsigned)(((ra >> 6) * 8 + g * 2) * 512 + (ra & 63) * 8);
    unsigned sb = (unsigned)(((rt * 2 + 1) * 8 + (lane >> 3)) * 512 + (2 * p2 - 80) * 8);
    unsigned sw = lds0 + (unsigned)(K1_ST_OFF + (2 * (lane < 32 ? wl * 16 + (lane & 15) : p2) + (g & 1)) * 8);
    asm volatile("" : "+v"(sa), "+v"(sb), "+v"(sw));
    MK(22);
    __builtin_amdgcn_s_setprio(3);
    bar();
    dma_run<14>(wq, (unsigned)K1C::XB);
    dma_run<14>(wq + K1C::WB, (unsigned)(SB + K1C::XB));
    dma_run<14>(wq + 2 * K1C::WB, (unsigned)(2 * SB + K1C::XB));
    MK(1);
    bar();
    dma_x(0u);
    dma_x((unsigned)SB);
    wbar<4>();
    MK(29);
    dma_x((unsigned)(2 * SB));
    dma_x((unsigned)(3 * SB));
    wbar<8>();
    MX(1, 3);
    dma_x(0u);
    wbar<8>();
    MX(1, 4);
    dma_x((unsigned)SB);
    f32x4 a, b, e;
    asm volatile("global_load_dwordx4 %0, %3, %5 sc1\n\tglobal_load_dwordx4 %1, %3, %5 offset:512 sc1\n\tglobal_load_dwordx4 %2, %4, %5 sc1"
                 : "=&v"(a), "=&v"(b), "=&v"(e)
                 : "v"(sa), "v"(sb), "s"(xs)
                 : "memory");
    wbar<11>();
    MX(1, 5);
    dma_w((unsigned)SB);
    dma_x((unsigned)(2 * SB));
    wbar<15>();
    MX(1, 6);
    dma_w((unsigned)(2 * SB));
    dma_x((unsigned)(3 * SB));
    wbar<19>();
    MX(1, 7);
    dma_w((unsigned)(3 * SB));
    dma_x(0u);
    wbar<16>();
    MX(1, 8);
    asm volatile("" : "+v"(a), "+v"(b), "+v"(e));
    float s, q;
    {
      float t[4] = {e[0], e[1], e[2], e[3]}, s2, q2;
      asm volatile(
          "v_add_f32_dpp %0, %0, %0 row_ror:8 row_mask:0xf bank_mask:0xf\n\tv_add_f32_dpp %1, %1, %1 row_ror:8 row_mask:0xf bank_mask:0xf\n\t"
          "v_add_f32_dpp %2, %2, %2 row_ror:8 row_mask:0xf bank_mask:0xf\n\tv_add_f32_dpp %3, %3, %3 row_ror:8 row_mask:0xf bank_mask:0xf\n\ts_nop 1"
          : "+v"(t[0]), "+v"(t[1]), "+v"(t[2]), "+v"(t[3]));
      red4(t, s2, q2);
      red8(a, b, s, q);
      s = lane < 32 ? s : s2;
      q = lane < 32 ? q : q2;
      asm volatile("" : "+v"(s), "+v"(q));
    }
    wbar<8>();
    MX(1, 9);
    {
      asm volatile("" : "+v"(s), "+v"(q));
      float mb, rinv;
      ln_finish(s, q, 1.0f / (float)D_, mb, rinv);
      asm volatile("ds_write_b64 %0, %1" ::"v"(sw), "v"(f32x2{-mb, rinv}) : "memory");
    }
    wbar<0>();
    MK(30);
  }

  template <int I>
  DEVI void p5_step(const char* wp) {
    constexpr int T = 2 * I + 1;
    wbar<(T == 1 ? 63 : (T <= 11 ? 3 : 0))>();
    if constexpr (T <= 9) {
      auto dm = [&]() {
      if (!G1W) {
        const unsigned wo = (unsigned)(12288 + (wl & 1) * 3072), so = (unsigned)(P5_W + ((T + 2 + (wl >> 1)) & 7) * 18432);
#pragma unroll
        for (int k = 0; k < 3; ++k) dma16_sc1(wp + (T + 7) * 18432 + k * 1024, lane16 + wo + (unsigned)((wl >> 1) * 18432), lds0 + so + wo + (unsigned)(k * 1024));
      }
      };
      if (!P5ORD) dm();
      pf_put<0, 12 * (T - 1), 12, P5PACE>((unsigned)(P5_W + ((T + 2) % 8) * 18432));
      pf_put<0, 12 * T, 12, P5PACE>((unsigned)(P5_W + ((T + 3) % 8) * 18432));
      if (P5ORD == 2) asm volatile("s_waitcnt lgkmcnt(0)" ::: "memory");
      if (P5ORD) dm();
    }
  }
  template <int... S>
  DEVI void p5_steps(const char* wp, std::integer_sequence<int, S...>) {
    (p5_step<S>(wp), ...);
  }

  template <int T0, int T1>
  DEVI void w5_load(const char* w5p) {
    const unsigned o = lane16 + (unsigned)(wl * 1024);
#pragma unroll
    for (int t = T0; t < T1; ++t)
      asm volatile("global_load_dwordx4 %0, %1, %2 sc1" : "=v"(pf[t]) : "v"(o + (unsigned)((t / 3 + 8) * 18432 + (t % 3) * 4096)), "s"(w5p) : "memory");
  }
  DEVI void p35(char* smem, char* ws, const u16* w_sp, int layer, int bid, int mi, int ni, int rb, int ct, int hp MK_ARG) {
    using C = K3C;
    constexpr int NS = C::NS, SB = C::SB, NDX = 4, NDW = 5, VPS = NDX + NDW;
    const char* wip = uni_ptr(ws + OFF_PI + (size_t)ni * (18 * C::WB));
    if (layer || L0REG) {
      pf_put<-48, 36, 18>((unsigned)(2 * SB + C::XB));
      pf_put<-48, 54, 18>((unsigned)(3 * SB + C::XB));
    } else {
      dma_run<18>(wip + 2 * C::WB, (unsigned)(2 * SB + C::XB));
      dma_run<18>(wip + 3 * C::WB, (unsigned)(3 * SB + C::XB));
    }
    const char* w5p = uni_ptr(ws + OFF_PW + (size_t)ct * (36 * 9216));
    const int w0 = wl < 2 ? wl * 5120 : wl * 4096 + 2048;
    const unsigned m0x = lds0 + (unsigned)(wl * 4096), m0w = lds0 + (unsigned)(C::XB + w0);
    const unsigned wo = lane16 + (unsigned)w0;
    const unsigned lw = lds0 + (unsigned)(LW_OFF + layer * 4), me = 1u << (8 * wl);
    auto dma_w = [&](const char* wp, unsigned so) {
#pragma unroll
      for (int i = 0; i < NDW; ++i) {
        if (i < NDW - 1 || wl < 2) dma16_sc1(wp, wo + (unsigned)(i * 1024), m0w + so + (unsigned)(i * 1024));
      }
    };
    {
      const char* xp = uni_ptr(ws + OFF_X1 + (size_t)layer * 2 * SZ_X + (size_t)(mi * 2 * 72 + wl * 2) * 1024);
      const unsigned xo1 = lane16 + 72u * 1024u;
      auto dma_x = [&](int k, unsigned so) {
        const char* p = xp + k * 8192;
        dma16_sc1(p, lane16, m0x + so);
        dma16_sc1(p, xo1, m0x + so + 1024u);
        dma16_sc1(p + 1024, lane16, m0x + so + 2048u);
        dma16_sc1(p + 1024, xo1, m0x + so + 3072u);
      };
      if (!MPOLL && wl == 0) cnt_wait(ws, CN_X1 + layer, mi * 16 + ni, 16u);
      MK(10);
      bar();
      dma_x(0, 0u);
      dma_x(1, (unsigned)SB);
      wait_vm<4>(); MX(3, 2); wbar<4>(); MX(2, 2);
      MK(25);
      dma_x(2, (unsigned)(2 * SB));
      dma_x(3, (unsigned)(3 * SB));
      dma_x(4, (unsigned)(4 * SB));
      wbar<12>(); MX(2, 3);
      dma_x(5, 0u);
      wait_vm<12>();
      bar(); MX(2, 4);
      dma_w(wip + 4 * C::WB, 0u);
      dma_x(6, (unsigned)SB);
      wait_w<8 + VPS>();
#pragma unroll 1
      for (int t = 1; t < 3; ++t) {
        bar(); MX(2, 4 + t);
        dma_w(wip + (t + 4) * C::WB, (unsigned)(t * SB));
        dma_x(t + 6, (unsigned)((t + 1) * SB));
        wait_w<NDX + VPS>();
      }
      bar(); MX(2, 7);
      dma_w(wip + 7 * C::WB, (unsigned)(3 * SB));
      wait_w<VPS>();
      bar(); MX(2, 8);
      dma_w(wip + 8 * C::WB, 0u);
      wait_w<NDW>();
      bar(); MX(2, 9);
      const char* wp = wip + 9 * C::WB;
      dma_w(wp + b_k(0) * C::WB, (unsigned)SB);
      if (layer == 0) {
        const char* sp = uni_ptr((const char*)w_sp + (size_t)(bid * 4 + wl) * 8192);
        const unsigned so = (unsigned)((lane >> 5) * 4096 + (lane & 31) * 16);
#pragma unroll
        for (int i = 0; i < WSP_V; ++i) dma16(sp + i * 512, so, lds0 + (unsigned)K3_DM_OFF);
        dma16(sp + (bid >> 4) * 256, so & ~0x100u, lds0 + (unsigned)K3_DM_OFF);
      }
      w5_load<0, 4>(w5p);
      wait_w<NDW + 4>();
      MX(3, 10); bar(); MX(2, 10);
      dma_w(wp + b_k(1) * C::WB, (unsigned)(2 * SB));
      w5_load<4, 8>(w5p);
      MK(26);
      lds_spin4(lw + LW_AD * 4, 1u);
      dma_w(wp + b_k(2) * C::WB, (unsigned)(3 * SB));
      dma_w(wp + b_k(3) * C::WB, 0u);
#define TCH 0
      if (TCH & 1) {
        const int li = wl * 54 + (lane < 54 ? lane : 53);
        touch64(wp + C::WB, (unsigned)((mi & 7) * 13824 + li * 64), lds0 + (unsigned)K3_DM_OFF);
      }
      w5_load<8, W5A>(w5p);
      wait_vm<W5A - 8>();
      lds_inc(lw + LW_L * 4, 4u * me);
      w5_load<W5A, 30>(w5p);
      MK(11);
#define LSL 2
      static_for<4, 7>([&](auto T_) {
        constexpr int t = T_;
        lds_spin4<LSL>(lw + LW_S * 4, (unsigned)(t - 3));
        MX(2, 12 + t);
        dma_w(wp + b_k(t) * C::WB, (unsigned)(((t + 1) % 4) * SB));
        if constexpr (t > 4) {
          wait_w<NDW>();
          lds_inc(lw + LW_L * 4, me);
        }
      });
      MX(2, 19);
      wait_vm<0>();
      lds_inc(lw + LW_L * 4, me);
      pf_pin<0, 30>();
      MK(13);
    }
    {
      unsigned xoff[NDX];
#pragma unroll
      for (int i = 0; i < NDX; ++i) {
        const int c = wl * 256 + i * 64 + lane;
        const int row = c >> 3, cc = c & 7;
        xoff[i] = (unsigned)((mi * 128 + row) * S_ * 2) + (unsigned)((cc ^ ((row >> 1) & 7)) * 16);
      }
      auto dma_x = [&](const char* xp, unsigned so) {
#pragma unroll
        for (int i = 0; i < NDX; ++i) dma16(xp, xoff[i], m0x + so + (unsigned)(i * 1024));
      };
      const char* xp = uni_ptr((const char*)w_sp);
      const char* wp = uni_ptr(ws + OFF_Z2 + (size_t)layer * SZ_Z + (size_t)ni * S_ * 288);
#define XEARLY 0
      if (!XEARLY) lds_spin4<LSL>(lw + LW_AD * 4, 5u);
      dma_x(xp + mi * 256, (unsigned)(2 * SB));
      dma_x(xp + mi * 256 + 128, (unsigned)(3 * SB));
      if (XEARLY) lds_spin4<LSL>(lw + LW_AD * 4, 5u);
      MK(24);
      int left = 2 * mi;
      auto next = [&]() {
        const int d = --left == 0 ? 3 : 1;
        xp += d * 128;
        wp += d * C::WB;
      };
      if (mi == 0) {
        xp += 256;
        wp += 2 * C::WB;
      }
      dma_x(xp, 0u);
      dma_x(xp + 128, (unsigned)SB);
      wait_vm<2 * NDX>();
      lds_inc(lw + LW_L * 4, me);
      MK(27);
      if (wl == 0) {
        cnt_wait(ws, CN_Z + layer, mi * 16 + ni, 16u);
        lds_wr32(lw + LW_GO * 4, 1u);
      } else {
        lds_spin(lw + LW_GO * 4, 1u);
      }
      MK(14);
      lds_spin4(lw + LW_S * 4, 8u);
      dma_w(wp, 0u);
      lds_spin4(lw + LW_S * 4, 9u);
      dma_w(wp + C::WB, (unsigned)SB);
      next();
      next();
      unsigned slot = (unsigned)(2 * SB);
      auto cissue = [&]() {
        dma_x(xp, slot);
        dma_w(wp, slot);
        next();
        slot = (slot == (unsigned)((NS - 1) * SB)) ? 0u : slot + (unsigned)SB;
      };
      auto cstep = [&]() {
        bar();
        cissue();
      };
      wait_w<NDW>();
      MK(15);
      int j = 2;
      if (layer == 0) {
        const char* sp = uni_ptr((const char*)w_sp + (size_t)(bid * 4 + wl) * 8192 + WSP_V * 512);
        unsigned d = 0u;
#pragma unroll 1
        for (; j < 2 + 32 - 4 * WSP_V; ++j) {
          cstep();
          asm volatile("s_load_dword %0, %1, 0\n\ts_load_dword %0, %1, 64\n\ts_load_dword %0, %1, 0x1000\n\ts_load_dword %0, %1, 0x1040"
                       : "+s"(d)
                       : "s"(sp));
          sp += 128;
          wait_w<VPS>();
        }
#pragma unroll 1
        for (int e = j + 4; j < e; ++j) {
          cstep();
          asm volatile("" : "+s"(d));
          wait_w<VPS>();
        }
        asm volatile("s_waitcnt lgkmcnt(0)" : "+s"(d));
      }
      place<AL_L>();
#pragma unroll 1
      for (; j <= 29; ++j) {
        cstep();
        wait_w<VPS>();
      }
      bar();
    }
    auto w5piece = [&](unsigned off) {
      dma16_sc1(w5p, lane16 + (off >= 54u * 1024u ? off - 54u * 1024u : off + 90u * 1024u), lds0 + (unsigned)P5_W + off);
    };
#pragma unroll
    for (int i = 0; i < 15; ++i) {
      const int p = 4 * i + wl;
      if (i < 14 || wl == 0) w5piece((unsigned)((p < 34 ? 53 + p : 121 - 34 + p) * 1024));
    }
    wait_vm<14>();
    bar();
#pragma unroll
    for (int i = 0; i < 9; ++i)
      if (i < 8 || wl < 2) w5piece((unsigned)((87 + 4 * i + wl) * 1024));
    wait_vm<0>();
    bar();
    if (wl) {
#pragma unroll
      for (int i = 0; i < 18; ++i)
        if (i < 17 || wl < 3) w5piece((unsigned)((3 * i + wl - 1) * 1024));
    } else {
      cnt_wait(ws, CN_H + layer, rb * 8 + ct, HSPLIT ? 8u : 16u);
    }
    MK(20);
    bar();
    p5_steps(w5p, std::make_integer_sequence<int, 7>{});
    if (layer == 0) {
      if (bid < 24 && wl == 0 && lane < 8) pk_st16(ws + OFF_HELLO + bid * 128 + lane16, u32x4{0u, 0u, 0u, 0u});
    }
  }
};

__global__ __launch_bounds__(768, 3) void k_main(const u16* __restrict__ x0, const u16* __restrict__ wqkv, const u16* __restrict__ wo,
                                                 const u16* __restrict__ w_in, const u16* __restrict__ w_sp,
                                                 const u16* __restrict__ b_sp, const u16* __restrict__ w_out,
                                                 u16* __restrict__ out, char* ws) {
  extern __shared__ __attribute__((aligned(16))) char smem[];
  const char* code0 = code_here();
  (void)code0;
  const unsigned lds0 = (unsigned)(size_t)smem;
  int wave = __builtin_amdgcn_readfirstlane((int)threadIdx.x >> 6);
  int bid = blockIdx.x;
#define FRESH() asm volatile("" : "+s"(bid), "+s"(ws), "+s"(wave))
#define XCD (bid & 7)
#define IDX (bid >> 3)
#define MI ((XCD >> 2) * 8 + (IDX >> 2))
#define NI ((XCD & 1) * 8 + (XCD & 2) * 2 + (IDX & 3))
#define RB (XCD * 4 + (IDX >> 3))
#define CT (IDX & 7)
#define P1MAP 0
#define RT ((XCD >> 1) * 4 + (IDX >> 3))
#define HP ((XCD & 1) * 8 + (IDX & 7))
  MK(0);
  if (CT0 && wave == 2) {
    constexpr int PER = (2 * NL_MAIN + 31) / 32;
    const int lane = lane_id();
    int li = (CT0 == 2 ? lane * 32 + IDX : IDX * PER + (lane < PER ? lane : PER - 1));
    if (li > 2 * NL_MAIN - 1) li = 2 * NL_MAIN - 1;
    touch64(code0, (unsigned)li * 64u, lds0 + (unsigned)(K1R_DUMMY + 768));
  }
  if (wave >= 3 && wave < 8) {
    constexpr int ST = 64;
    const int k = (7 - wave) * 64 + lane_id();
    const bool isx = k < 9216 / ST;
    int o = isx ? bid * 9216 + k * ST : bid * 7776 + (k - 9216 / ST) * ST;
    if (!isx && k - 9216 / ST > 7775 / ST) o = bid * 7776;
    const char* p = (isx ? (const char*)x0 : (const char*)wqkv) + o;
    asm volatile("s_mov_b32 m0, %1\n\ts_nop 0\n\tglobal_load_lds_dword %0, off" ::"v"(p), "s"(lds0 + (unsigned)K1R_DUMMY) : "memory");
  }

  if (__builtin_expect(wave >= 8, 1)) {
    __builtin_amdgcn_s_setprio(3);
    {
      const int lane = lane_id();
      RawLd rl;
      const bool cross = (((HP * 72) & ~15) & 127) > 48;
      const int c16 = IDX >> 1;
      const int job = cross ? (c16 < 11 ? 128 + XCD * 11 + c16 : 999) : XCD * 16 + c16;
      const int pj = cross ? (c16 >= 11 ? XCD * 5 + c16 - 11 : -1) : (XCD < 4 && (c16 & 7) == 0 ? 40 + XCD * 2 + (c16 >> 3) : -1);
      rl.front(x0, wqkv, wo, ws, bid, lds0, wave - 8, lane, RT, HP, pj MF_PASS);
      if (job < 216) k1_raw_loader<true>(rl, w_in, w_out, ws, rl.tag, bid, lds0, wave - 8, lane, RT, HP, CT, job, pj MF_PASS);
      else k1_raw_loader<false>(rl, w_in, w_out, ws, rl.tag, bid, lds0, wave - 8, lane, RT, HP, CT, job, pj MF_PASS);
    }
    Loader ld;
    ld.lds0 = lds0;
#pragma unroll 1
    for (int layer = 0; layer < 2; ++layer) {
      FRESH();
      ld.wl = wave - 8;
      ld.lane = lane_id();
      ld.lane16 = (unsigned)ld.lane * 16u;
      const char* wip = uni_ptr(ws + OFF_PI + (size_t)NI * (18 * K3C::WB));
      if (layer) {
        ld.p1(smem, ws, RT, HP MK_PASS);
        ld.pf_load<0, 21, 0, 81>(uni_ptr(ws + OFF_PO + (size_t)CT * (18 * 4608)));
        ld.pf_load<21, 9, 36, 72>(wip);
        wait_vm<0>();
        ld.pf_pin<0, 30>();
        bar();
        ld.pf_put<0, 0, 40>((unsigned)P2_W);
        ld.pf_put<0, 40, 41>((unsigned)(P2_W + 40 * 1024));
      } else {
        ld.pf_undef<21, 9>();
        MK(3);
        MK(4);
        bar();
        const char* wp = uni_ptr(ws + OFF_PO + (size_t)CT * (18 * 4608) + ld.wl * 1024);
#pragma unroll
        for (int i = 0; i < 21; ++i)
          if (i < 20 || ld.wl == 0) dma16_sc1(wp + i * 4096, ld.lane16, lds0 + (unsigned)(P2_W + i * 4096 + ld.wl * 1024));
      }
      if (layer == 0 && ld.wl == 0) cnt_wait(ws, CN_O + layer, RB * 8 + CT, 16u);
      MK(6);
      asm volatile("s_waitcnt vmcnt(0) lgkmcnt(0)" ::: "memory");
      bar();
      lds_spin(lds0 + (unsigned)(LW_OFF + (LW_T + layer) * 4), 12u);
      if (layer == 0) {
        if (ld.wl == 0) {
          cnt_wait(ws, CN_PK, 8 + NI, 36u);
          lds_wr32(lds0 + (unsigned)(LW_OFF + LW_HELLO * 4), 2u);
        } else {
          lds_spin(lds0 + (unsigned)(LW_OFF + LW_HELLO * 4), 2u);
        }
        MK(7);
      }
      static_assert(2 * K3C::SB <= P2_W, "free during the first residual GEMM");
      ld.dma_run<18>(wip, (unsigned)K3C::XB);
      ld.dma_run<18>(wip + K3C::WB, (unsigned)(K3C::SB + K3C::XB));
      if (L0REG && layer == 0) ld.pf_load<21, 9, 36, 72>(wip);
      if (layer == 0 && ld.wl == 0) cnt_wait(ws, CN_PK, 24 + CT, 72u);
      wait_vm<0>();
      ld.pf_pin<21, 9>();
      bar();
      FRESH();
      ld.wl = wave - 8;
      ld.p35(smem, ws, w_sp, layer, bid, MI, NI, RB, CT, HP MK_PASS);
    }
  } else {
    MF(12);
    math_start(smem, code0, b_sp, wqkv, ws, bid, wave, lane_id());
    MF(13);
#pragma unroll 1
    for (int layer = 0; layer < 2; ++layer) {
      FRESH();
      {
        const int lane = lane_id(), r0 = RT * 128;
        float* csq = reinterpret_cast<float*>(ws + OFF_CSQ + bid * 512);
        Attn at;
        at.setup(lds0 + (unsigned)(layer ? K1_T_OFF : K1R_T_OFF), ws + OFF_O + (size_t)layer * SZ_X, r0, HP, wave, lane);
        if (layer) {
          if ((wave & 2) == 0) k1_gemm<2, 4>(smem, ws, csq, RT, HP, (wave & 1) * 2 + (wave & 4), 0, lane MK_PASS);
          else if (wave < 4) k1_gemm<3, 3>(smem, ws, csq, RT, HP, (wave & 1) * 3, 4, lane MK_PASS);
          else if (wave == 6) k1_gemm<2, 3>(smem, ws, csq, RT, HP, 6, 4, lane MK_PASS);
          else k1_gemm<1, 7>(smem, ws, csq, RT, HP, 8, 0, lane MK_PASS);
        } else {
          if (wave == 0) k1_gemm_raw<3, false>(smem, HP, 0, lane MF_PASS);
          else if (wave < 4) k1_gemm_raw<2, false>(smem, HP, 1 + 2 * wave, lane MF_PASS);
          else if (wave < 7) k1_gemm_raw<2, true>(smem, HP, 2 * (wave - 4), lane MF_PASS);
          else k1_gemm_raw<3, true>(smem, HP, 6, lane MF_PASS);
        }
        msync(lds0 + (unsigned)(LW_OFF + (LW_T + layer) * 4));
        MK(3);
        at.run(r0, HP, wave, lane);
        if (layer == 0) {
          if (wave == 0 && lane < 32)
            asm volatile("global_store_dwordx4 %0, %1, %2 sc1\n\ts_nop 1" ::"v"((unsigned)lane * 16u),
                         "v"(*reinterpret_cast<const u32x4*>(smem + K1R_CS_OFF + lane * 16)), "s"(uni_ptr((const char*)csq))
                         : "memory");
          lds_spin(lds0 + (unsigned)(LW_OFF + LW_HELLO * 4), 1u);
        }
        MK(4);
        announce(lds0 + (unsigned)(LW_OFF + (LW_O + layer * 22 + (wave >> 2)) * 4), 4u, ws, CN_O + layer, (RT * 2 + (wave >> 2)) * 8 + lane,
                 lane < 8);
        MK(5);
        bar();
      }
      FRESH();
      {
        if (wave < 4) {
          const int lane = lane_id();
          u32x2 xv[5];
          res_ldx(xv, layer ? ws + OFF_X1 + SZ_X : (const char*)x0, layer == 0, RB, CT, wave, lane);
          ResOut ro;
          ro.setup(ws + OFF_X1 + (size_t)layer * 2 * SZ_X, ws + OFF_XS + (size_t)layer * 2 * SZ_XS, out, false, RB, CT, wave, lane);
          f32x4 acc[2][5];
          p2_gemm(acc, xv, lds0, uni_ptr(ws + OFF_O + (size_t)layer * SZ_X + (size_t)RB * (72 * 1024)), layer, wave, lane MK_PASS);
          MK(7);
          ro.run(acc[0], false);
          MK(8);
          announce(lds0 + (unsigned)(LW_OFF + (LW_X1 + layer) * 4), 4u, ws, CN_X1 + layer, (RB >> 1) * 16 + lane, lane < 16);
          MK(9);
          bar();
          FRESH();
          if (wave == 3) cnt_wait(ws, CN_X1 + layer, MI * 16 + NI, 16u);
          bar();
          MK(10);
          p3_g0(smem, ws, layer, MI, NI, wave, lane_id() MK_PASS);
        } else {
          if (layer && wave == 5) cnt_wait(ws, CN_O + layer, RB * 8 + CT, 16u);
          bar();
          bar();
          k3_const(smem, ws, b_sp, layer, bid, MI, NI, wave & 3);
          bar();
          MK(10);
          FRESH();
          p3_g1(smem, ws, code0, bid, layer, MI, wave & 3, lane_id() MK_PASS);
        }
        bar();
      }
      FRESH();
      {
        const int lane = lane_id();
        if (wave < 4) {
          u32x2 xv[5];
          res_ldx(xv, ws + OFF_X1 + (size_t)layer * 2 * SZ_X, false, RB, CT, wave, lane);
          f32x4 acc[2][5];
          ResOut ro;
          ro.setup(ws + OFF_X1 + SZ_X, ws + OFF_XS + SZ_XS, out, layer != 0, RB, CT, wave, lane);
          ResX<36, 33, 8, P5_W, P5_XW, P5_X0, 3, P5_BAR> gm(acc);
          gm.setup(lds0, uni_ptr(ws + OFF_H + (size_t)layer * SZ_Z + (size_t)RB * (288 * 1024)), wave, lane, (unsigned)P5_RED, layer + 1u);
          gm.prow();
          place<AL_P5>();
          bar();
          MK(20);
          gm.prox<0>(std::make_integer_sequence<int, P5_X0>{});
          gm.init<2 * P5_X0>(xv);
          __builtin_amdgcn_sched_barrier(0);
          gm.run();
          MK(21);
          ro.run(acc[0], layer != 0);
          MK(22);
          if (layer == 0) {
            announce(lds0 + (unsigned)(LW_OFF + LW_X2 * 4), 4u, ws, CN_X2, (RB >> 1) * 16 + lane,
                     lane < 16 || (lane < 32 && (RB & 1) && RB < 31));
            MK(23);
          }
        } else {
          u32x4 wq[15];
          const int wm = wave & 3;
          const unsigned o = (unsigned)((wm >> 1) * 18432 + 12288 + (wm & 1) * 3072 + lane * 16);
          if (G1W) {
            const char* wp = uni_ptr(ws + OFF_PW + (size_t)CT * (36 * 9216) + 8 * 18432);
#pragma unroll
            for (int i = 0; i < 15; ++i)
              asm volatile("global_load_dwordx4 %0, %1, %2 offset:%3 sc1" : "=v"(wq[i]) : "v"(o), "s"(wp + (i / 3) * 36864), "n"((i % 3) * 1024) : "memory");
          }
          bar();
#pragma unroll
          for (int i = 0; i < 7; ++i) {
            if (G1W && i == 0) asm volatile("s_waitcnt vmcnt(0)" ::: "memory");
            if (HSPLIT && i == HPOLL && wave == 5) {
              if (HPOLL < 2) {
                unsigned f;
                const char* cp = uni_ptr(ws + OFF_CNT + (size_t)((CN_H2 + layer) * 256 + RB * 8 + CT) * 128);
                asm volatile("global_load_dword %0, %1, %2 sc1" : "=v"(f) : "v"(vconst(0u)), "s"(cp) : "memory");
                bar();
                asm volatile("s_waitcnt vmcnt(0)" : "+v"(f) : : "memory");
                if (__builtin_amdgcn_readfirstlane(f) != 7u) cnt_wait(ws, CN_H2 + layer, RB * 8 + CT, 8u);
                continue;
              }
              cnt_wait(ws, CN_H2 + layer, RB * 8 + CT, 8u);
            }
            asm volatile("s_waitcnt lgkmcnt(0)" ::: "memory");
            bar();
            if (G1W && i < 5) {
              const unsigned a = lds0 + o + (unsigned)(P5_W + ((2 * i + 3) % 8) * 18432) - ((i == 2 && wm >= 2) ? 8u * 18432u : 0u);
#pragma unroll
              for (int k = 0; k < 3; ++k) {
                if (P5PACE) __builtin_amdgcn_s_sleep(2 * P5PACE);
                asm volatile("ds_write_b128 %0, %1 offset:%2" ::"v"(a), "v"(wq[3 * i + k]), "n"(k * 1024) : "memory");
              }
            }
          }
          MK(22);
        }
        if (layer == 0) bar();
      }
    }
  }
}

}

extern "C" void kernel_launch(void* const* d_in, const int* in_sizes, int n_in, void* d_out, int out_size,
                              void* d_ws, size_t ws_size, hipStream_t stream) {
  const u16* x0 = (const u16*)d_in[0];
  const u16* wqkv = (const u16*)d_in[1];
  const u16* wo = (const u16*)d_in[2];
  const u16* w_in = (const u16*)d_in[4];
  const u16* w_sp = (const u16*)d_in[5];
  const u16* b_sp = (const u16*)d_in[6];
  const u16* w_out = (const u16*)d_in[7];
  char* ws = (char*)d_ws;
  (void)hipFuncSetAttribute((const void*)k_main, hipFuncAttributeMaxDynamicSharedMemorySize, LDS_ALL);
  k_main<<<256, 768, LDS_ALL, stream>>>(x0, wqkv, wo, w_in, w_sp, b_sp, w_out, (u16*)d_out, ws);
}
```

```cpp
#include <hip/hip_runtime.h>
#include <hip/hip_bf16.h>
#include <stdint.h>

#define DEVI __device__ __forceinline__

typedef unsigned short u16;
typedef __attribute__((ext_vector_type(8))) short s16x8;
typedef __attribute__((ext_vector_type(4))) short s16x4;
typedef __attribute__((ext_vector_type(4))) float f32x4;
typedef __attribute__((ext_vector_type(2))) float f32x2;
typedef __attribute__((ext_vector_type(2))) __bf16 bf16x2;
typedef __attribute__((ext_vector_type(2))) unsigned u32x2;
typedef __attribute__((ext_vector_type(4))) unsigned u32x4;

#define MK(I) ((void)0)
#define MX(S, I) ((void)0)
#define MF(I) ((void)0)
#define MF_ARG
#define MF_PASS
#define MK_ARG
#define MK_PASS

#define SPIN_LDS for (;;)
#define SPIN_MEM (1u << 22)

#define G1W 0
#define P5PACE 0
#define L0REG 0
#define RTC 0
#define P1PR 3
#define HSPLIT 1
#define HPOLL 1
#define XA4 1
#define P5ORD 0
#define CT0 1
#define AL_A 0
#define AL_B 0
#define AL_C 0
#define AL_P5 0
#define AL_P2 0
#define AL_G -1
#define AL_K 0
#define AL_L 0
#define CSL 1
#define NOPL 0
#define G1E 1
#define MPOLL 1
namespace {

constexpr int S_ = 2048, D_ = 576, F_ = 4608, FH_ = 2304;
constexpr int NQKV = 3 * D_;

constexpr size_t SZ_X = (size_t)S_ * D_ * 2;
constexpr size_t SZ_Z = (size_t)S_ * FH_ * 2;
constexpr size_t OFF_X1 = 0;
constexpr size_t OFF_O = OFF_X1 + 3 * SZ_X;
constexpr size_t OFF_Z2 = OFF_O + 2 * SZ_X;
constexpr size_t OFF_H = OFF_Z2 + 2 * SZ_Z;
constexpr size_t SZ_XS = (size_t)S_ * 8 * 8;
constexpr size_t OFF_XS = OFF_H + 2 * SZ_Z;
constexpr size_t OFF_CSQ = OFF_XS + 3 * SZ_XS;
constexpr size_t OFF_ZP = OFF_CSQ + 256 * 512;
constexpr size_t SZ_ZP = (size_t)16 * 16 * 1024;
constexpr size_t OFF_PQ = OFF_ZP + 2 * SZ_ZP;
constexpr size_t OFF_PO = OFF_PQ + (size_t)16 * 9 * 64 * 112 * 2;
constexpr size_t OFF_PI = OFF_PO + (size_t)D_ * D_ * 2;
constexpr size_t OFF_PW = OFF_PI + (size_t)32 * 9 * 64 * 144 * 2;
constexpr size_t OFF_CSP = OFF_PW + (size_t)FH_ * D_ * 2;
constexpr size_t OFF_CNT = OFF_CSP + (size_t)18 * F_ * 4;
constexpr int CN_X1 = 0, CN_H = 2, CN_X2 = 4, CN_O = 5, CN_Z = 7, CN_PK = 9, CN_H2 = 10, NCNT = 12;
constexpr size_t OFF_TAG = OFF_CNT + (size_t)NCNT * 256 * 128;
constexpr size_t OFF_HELLO = OFF_TAG + 128;
constexpr size_t OFF_POT = OFF_HELLO + 2048;
constexpr size_t OFF_CSK = OFF_POT + 1024;
constexpr size_t WS_END = OFF_CSK + 256 * 2048;

DEVI float bf_lo(unsigned u) { return __uint_as_float(u << 16); }
DEVI float bf_hi(unsigned u) { return __uint_as_float(u & 0xffff0000u); }
DEVI unsigned pack2(float a, float b) {
  f32x2 v = {a, b};
  bf16x2 r = __builtin_convertvector(v, bf16x2);
  return __builtin_bit_cast(unsigned, r);
}
DEVI float rbf(float a) {
  return bf_lo(pack2(a, 0.f));
}

DEVI float sum_g(float x) {
  const unsigned u = __float_as_uint(x);
  const auto p = __builtin_amdgcn_permlane32_swap(u, u, false, false);
  const unsigned v = __float_as_uint(__uint_as_float(p[0]) + __uint_as_float(p[1]));
  const auto q = __builtin_amdgcn_permlane16_swap(v, v, false, false);
  return __uint_as_float(q[0]) + __uint_as_float(q[1]);
}

DEVI unsigned vconst(unsigned c) {
  unsigned r;
  asm volatile("v_mov_b32 %0, %1" : "=v"(r) : "s"(c));
  return r;
}
template <int K>
DEVI void place() {
  if constexpr (K >= 0) {
    __builtin_amdgcn_sched_barrier(0);
    asm volatile(".p2align 6\n\t.fill %0, 4, 0xbf800000" ::"n"(K));
    __builtin_amdgcn_sched_barrier(0);
  }
}
DEVI const char* uni_ptr(const char* p) {
  unsigned long long v = (unsigned long long)p;
  unsigned lo = __builtin_amdgcn_readfirstlane((unsigned)v), hi = __builtin_amdgcn_readfirstlane((unsigned)(v >> 32));
  asm("s_nop 4" : "+s"(lo), "+s"(hi));
  return (const char*)(((unsigned long long)hi << 32) | lo);
}

DEVI void dma16(const char* sbase, unsigned voff, unsigned m0val) {
  asm volatile("s_mov_b32 m0, %2\n\ts_nop 0\n\tglobal_load_lds_dwordx4 %0, %1"
               :
               : "v"(voff), "s"(sbase), "s"(m0val)
               : "memory");
}

DEVI void ln_finish(float s, float q, float invn, float& mb, float& rinv) {
  float mean = s * invn;
  asm("" : "+v"(mean));
  float var = fmaxf(__builtin_fmaf(-mean, mean, q * invn), 0.f);
  mb = rbf(mean);
  float v = rbf(var);
  float t = rbf(v + 1e-5f);
  float sd = rbf(__builtin_amdgcn_sqrtf(t));
  rinv = __builtin_amdgcn_rcpf(sd);
}

DEVI unsigned pack2v(f32x2 v) { return pack2(v[0], v[1]); }

template <int BM_, int BN_>
struct GemmCfg {
  static constexpr int BM = BM_, BN = BN_, NS = 4;
  static constexpr int P = BN * 2;
  static constexpr int XB = BM * 128, WB = 64 * P, SB = XB + WB;
  static constexpr int LDS_BYTES = NS * SB;
};

DEVI int swap23(int r) { return (r & ~12) | ((r & 4) << 1) | ((r & 8) >> 1); }

DEVI unsigned lds_rd32(unsigned addr) {
  unsigned v;
  asm volatile("ds_read_b32 %0, %1\n\ts_waitcnt lgkmcnt(0)" : "=v"(v) : "v"(addr) : "memory");
  return v;
}
DEVI void lds_wr32(unsigned addr, unsigned v) { asm volatile("ds_write_b32 %0, %1" ::"v"(addr), "v"(v) : "memory"); }

template <class C, int MT, int NT, int D, int R, bool XCM, int WT, bool SQ = false, unsigned long long WS = 0>
struct GemmMath {
  static constexpr int WO = WT ? 1 : 2;
  static constexpr int NS = C::NS, SB = C::SB, P = C::P, NU = 2 * NT;
  static constexpr int XPU = MT <= 2 ? MT : MT - 2;
  static constexpr int XU = (MT + XPU - 1) / XPU;
  static_assert(NU % R == 0 && R > D && D <= NT && XU <= D, "shape");
  s16x4 wlo[R], whi[R];
  s16x8 wf[R];
  s16x8 xf[2][MT];
  unsigned xrd[2], wrd, xa[2], wa, so;
  f32x4 (&acc)[MT][NT];
  f32x4 accq[SQ ? MT : 1];
  unsigned long long wtab = WS;
  DEVI GemmMath(f32x4 (&a)[MT][NT]) : acc(a) {}

  template <int PU>
  DEVI void wread_lo() {
    constexpr int off = (PU / NT) * 32 * P + (PU % NT) * (WT == 2 ? 1024 : 32);
    if constexpr (WT) asm volatile("ds_read_b128 %0, %1 offset:%2" : "=v"(wf[PU % R]) : "v"(wa), "n"(off));
    else asm volatile("ds_read_b64_tr_b16 %0, %1 offset:%2" : "=v"(wlo[PU % R]) : "v"(wa), "n"(off));
  }
  template <int PU>
  DEVI void wread_hi() {
    if constexpr (!WT)
      asm volatile("ds_read_b64_tr_b16 %0, %1 offset:%2" : "=v"(whi[PU % R]) : "v"(wa), "n"((PU / NT) * 32 * P + (PU % NT) * 32 + 8 * P));
  }
  template <int H, int M>
  DEVI void xread1() {
    asm volatile("ds_read_b128 %0, %1 offset:%2" : "=v"(xf[H][M]) : "v"(xa[H]), "n"(M * (XCM ? 256 : 2048)));
  }
  static constexpr int xops(int v) {
    const int k = (v + D) % NT;
    if (k >= XU) return 0;
    const int n = MT - k * XPU;
    return n < XPU ? n : XPU;
  }
  static constexpr int ops(int v, bool last) { return (last && v + D >= NU) ? 0 : WO + xops(v); }
  static constexpr int newer(int U, bool last) {
    int n = 0;
    int v0 = U - D;
    if (U % NT == 0) v0 += XU - 1;
    else n += v0 >= 0 ? xops(v0) : xops(v0 + NU);
    for (int v = v0 + 1; v < U; ++v) n += v >= 0 ? ops(v, last) : ops(v + NU, false);
    return n;
  }
  template <int U, int M_>
  DEVI void mf() {
    constexpr int M = ((U % NT) & 1) ? MT - 1 - M_ : M_;
    s16x8 w;
    if constexpr (WT) w = wf[U % R];
    else w = __builtin_shufflevector(wlo[U % R], whi[U % R], 0, 1, 2, 3, 4, 5, 6, 7);
    acc[M][U % NT] = __builtin_amdgcn_mfma_f32_16x16x32_bf16(w, xf[U / NT][M], acc[M][U % NT], 0, 0, 0);
    __builtin_amdgcn_sched_barrier(0);
  }
  DEVI void pin() {
#pragma unroll
    for (int m = 0; m < MT; ++m)
#pragma unroll
      for (int n = 0; n < NT; ++n) asm volatile("" : "+v"(acc[m][n]));
  }
  template <int U, int I>
  DEVI void dsop() {
    constexpr int PU = (U + D) % NU;
    if constexpr (I == 0) wread_lo<PU>();
    else if constexpr (I == 1) wread_hi<PU>();
    else if constexpr (I - 2 < xops(U)) {
      constexpr int k = (U + D) % NT;
      xread1<((U + D - k) % NU) / NT, k * XPU + I - 2>();
    }
  }
  template <int U, bool LAST, bool BAR, int M>
  DEVI void slot() {
    constexpr bool PF = !(LAST && U + D >= NU);
    mf<U, M>();
    if constexpr (M == 0 && PF && U + D == NU) {
      if constexpr (BAR) __builtin_amdgcn_s_barrier();
      so = (so == (unsigned)((NS - 1) * SB)) ? 0u : so + (unsigned)SB;
      wa = wrd + (WS ? ((unsigned)wtab & 63u) << 11 : so);
      wtab >>= 6;
      xa[0] = xrd[0] + so;
      xa[1] = xrd[1] + so;
      __builtin_amdgcn_sched_barrier(0);
    }
    if constexpr (PF) {
      dsop<U, M>();
      if constexpr (M == MT - 1) {
        if constexpr (MT <= 1) dsop<U, 1>();
        if constexpr (MT <= 2) {
          dsop<U, 2>();
          dsop<U, 3>();
        }
      }
      __builtin_amdgcn_sched_barrier(0);
    }
  }
  template <int U, bool LAST, bool BAR, int... M>
  DEVI void unit(std::integer_sequence<int, M...>) {
    static_assert(newer(U, LAST) <= 15, "lgkmcnt range");
    asm volatile("s_waitcnt lgkmcnt(%0)" ::"n"(newer(U, LAST)));
    __builtin_amdgcn_sched_barrier(0);
    (slot<U, LAST, BAR, M>(), ...);
    if constexpr (SQ && (U % NT) < MT) {
      constexpr int m = U % NT;
      accq[m] = __builtin_amdgcn_mfma_f32_16x16x32_bf16(xf[U / NT][m], xf[U / NT][m], accq[m], 0, 0, 0);
      __builtin_amdgcn_sched_barrier(0);
    }
    __builtin_amdgcn_sched_barrier(0);
  }
  template <bool LAST, bool BAR, int... U>
  DEVI void stage(std::integer_sequence<int, U...>) {
    (unit<U, LAST, BAR>(std::make_integer_sequence<int, MT>{}), ...);
  }
  template <int... U>
  DEVI void prow(std::integer_sequence<int, U...>) {
    ((wread_lo<U>(), wread_hi<U>()), ...);
  }
  template <int... M>
  DEVI void prox(std::integer_sequence<int, M...>) {
    (xread1<0, M>(), ...);
  }
  DEVI void setup(unsigned lds0, int mt0, int nt0, int lane, unsigned so0) {
    const int g = lane >> 4, r = lane & 15, q = (lane >> 2) & 3, p = lane & 3;
#pragma unroll
    for (int h = 0; h < 2; ++h)
      xrd[h] = XCM ? lds0 + (unsigned)((4 * h + g) * (C::BM * 16) + (mt0 * 16 + r) * 16)
                   : lds0 + (unsigned)((mt0 * 16 + r) * 128 + (((4 * h + g) ^ ((r >> 1) & 7)) * 16));
    wrd = WT == 2   ? lds0 + (unsigned)(C::XB + nt0 * 1024 + lane * 16)
          : WT == 1 ? lds0 + (unsigned)(C::XB + (g * 8 + (r & 7)) * P + (r >> 3) * 16 + nt0 * 32)
                    : lds0 + (unsigned)(C::XB + (16 * (g >> 1) + 4 * (g & 1) + q) * P + 8 * p + nt0 * 32);
    so = so0;
    xa[0] = xrd[0] + so;
    xa[1] = xrd[1] + so;
    wa = wrd + so;
  }
  DEVI void first() {
    prox(std::make_integer_sequence<int, MT>{});
    prow(std::make_integer_sequence<int, D>{});
    asm volatile("s_waitcnt lgkmcnt(0)");
  }
  template <int N, bool BAR = true, int AL = -1>
  DEVI void mid() {
    place<AL>();
#pragma unroll 1
    for (int s = 0; s < N; ++s) stage<false, BAR>(std::make_integer_sequence<int, NU>{});
  }
  DEVI void last() {
    stage<true, true>(std::make_integer_sequence<int, NU>{});
    pin();
  }
  template <int KS>
  DEVI void run(unsigned lds0, int mt0, int nt0, int lane, unsigned so0) {
    setup(lds0, mt0, nt0, lane, so0);
    __builtin_amdgcn_s_barrier();
    first();
    mid<KS - 1, true, AL_K>();
    last();
  }
};

DEVI const char* code_here() {
  unsigned long long pc;
  asm volatile("s_getpc_b64 %0" : "=s"(pc));
  return (const char*)pc;
}
DEVI int okmap(int p) {
  const int ch = p >> 3, e = p & 7, c = (ch - 8) & 3;
  return ch < 8 ? (2 * ch + (e >> 2)) * 36 + 16 + (e & 3) : ((ch - 8) >> 2) * 36 + c * 8 + (c >> 1) * 4 + e;
}

DEVI void pk_st16(void* p, u32x4 v) {
  asm volatile("global_store_dwordx4 %0, %1, off sc1\n\ts_nop 1" ::"v"(p), "v"(v) : "memory");
}

using K1C = GemmCfg<144, 112>;
constexpr int K1_TP = 240;
constexpr int K1_T_OFF = K1C::SB;
constexpr int K1_ST_OFF = 163840 - 128 - 1152;
constexpr int K1_CS_OFF = 4 * K1C::SB;
constexpr int K1_WX = 4 * K1C::SB + 2048;
constexpr unsigned long long K1_WS = 16ull | 32ull << 6 | 48ull << 12 | 56ull << 18 | 63ull << 24 | 16ull << 30 | 32ull << 36 | 48ull << 42;
static_assert(K1C::SB == 16 * 2048 && K1_WX - K1C::XB == 56 * 2048 && K1C::WB == 7 * 2048 && K1_WX + 2 * K1C::WB <= K1_ST_OFF, "K1 w places");
static_assert(K1_T_OFF + 144 * K1_TP + 64 <= 2 * K1C::SB + K1C::XB, "K1 LDS alias");
using K1R = GemmCfg<144, 144>;
constexpr int K1R_T_OFF = K1R::SB;
constexpr int K1R_CS_OFF = K1R_T_OFF + 144 * K1_TP;
constexpr int K1R_ST_OFF = K1R_CS_OFF + 512;
constexpr int K1R_DUMMY = K1R::LDS_BYTES;
constexpr int K1R_PO = K1R_DUMMY + 1024;
constexpr int K1R_CNT = 163840 - 128 + 28 * 4;
DEVI void msync(unsigned addr) {
  asm volatile("s_waitcnt lgkmcnt(0)" ::: "memory");
  asm volatile("s_mov_b64 exec, 1\n\tds_add_u32 %0, %1\n\ts_mov_b64 exec, -1" ::"v"(addr), "v"(vconst(1u)) : "memory");
  SPIN_LDS {
    if (__builtin_amdgcn_readfirstlane(lds_rd32(addr)) >= 8u) break;
  }
}
static_assert(K1R_PO + 13824 + 256 <= 163840 - 128 && K1R_ST_OFF + 144 * 8 <= 3 * K1R::SB, "K1R LDS");

DEVI void touch64(const char* sbase, unsigned voff, unsigned m0val) {
  asm volatile("s_mov_b32 m0, %2\n\ts_nop 0\n\tglobal_load_lds_dword %0, %1" : : "v"(voff), "s"(sbase), "s"(m0val) : "memory");
}

template <int MT, bool G1>
DEVI void k1_gemm_raw(char* smem, int hp, int mt0, int lane MF_ARG) {
  constexpr int NT = 4, nt0 = G1 ? 4 : 0;
  const unsigned lds0 = (unsigned)(size_t)smem;
  const int g = lane >> 4, r = lane & 15;
  unsigned ta[NT];
#pragma unroll
  for (int nt = 0; nt < NT; ++nt) {
    const int j = (nt0 + nt) * 16 + g * 4, seg = j / 40, cj = j - seg * 40 - (hp & 1) * 4;
    const int to = (seg < 3 && cj >= 0 && cj < 36) ? seg * 80 + cj * 2 + (cj >= 20 ? 8 : 0) : (seg < 3 ? 40 : 120);
    ta[nt] = lds0 + (unsigned)(K1R_T_OFF + (mt0 * 16 + r) * K1_TP + to);
    asm volatile("" : "+v"(ta[nt]));
  }
  unsigned sa = lds0 + (unsigned)(K1R_ST_OFF + (mt0 * 16 + r) * 8), ca = lds0 + (unsigned)(K1R_CS_OFF + (nt0 * 16 + g * 4) * 4);
  asm volatile("" : "+v"(sa), "+v"(ca));
  f32x4 acc[MT][NT];
#pragma unroll
  for (int mt = 0; mt < MT; ++mt)
#pragma unroll
    for (int nt = 0; nt < NT; ++nt) acc[mt][nt] = f32x4{0.f, 0.f, 0.f, 0.f};
  {
    GemmMath<K1R, MT, 4, 3, 4, false, 0, G1> gm(acc);
    if constexpr (G1) {
#pragma unroll
      for (int mt = 0; mt < MT; ++mt) gm.accq[mt] = f32x4{0.f, 0.f, 0.f, 0.f};
    }
    MF(14);
    if constexpr (!G1) {
      if (mt0 < 7) asm volatile("s_waitcnt vmcnt(0)" ::: "memory");
    }
    MF(15);
    gm.template run<9>(lds0, mt0, nt0, lane, 0u);
    MF(2);
    if constexpr (G1) {
#pragma unroll
      for (int mt = 0; mt < MT; ++mt) {
        const f32x4 q = gm.accq[mt];
        const float d = (r & 2) ? ((r & 1) ? q[3] : q[2]) : ((r & 1) ? q[1] : q[0]);
        const float qq = sum_g(g == (r >> 2) ? d : 0.f);
        float mb, rinv;
        ln_finish(acc[mt][3][0], qq, 1.0f / (float)D_, mb, rinv);
        const f32x2 st = {-mb, rinv};
        if (g == 2) asm volatile("ds_write_b64 %0, %1 offset:%2" ::"v"(sa), "v"(st), "n"(mt * 128) : "memory");
      }
    }
  }
  if (mt0 == 0 && r == 0) {
#pragma unroll
    for (int nt = 0; nt < NT; ++nt) *reinterpret_cast<f32x4*>(smem + K1R_CS_OFF + ((nt0 + nt) * 16 + g * 4) * 4) = acc[0][nt];
  }
  msync(lds0 + (unsigned)K1R_CNT);
  MF(3);
  f32x2 st[MT];
  f32x4 cs[NT];
#pragma unroll
  for (int nt = 0; nt < NT; ++nt) asm volatile("ds_read_b128 %0, %1 offset:%2" : "=v"(cs[nt]) : "v"(ca), "n"(nt * 64) : "memory");
#pragma unroll
  for (int mt = 0; mt < MT; ++mt) asm volatile("ds_read_b64 %0, %1 offset:%2" : "=v"(st[mt]) : "v"(sa), "n"(mt * 128) : "memory");
#pragma unroll
  for (int i = 0; i < MT; ++i) asm volatile("s_waitcnt lgkmcnt(0)" : "+v"(st[i]));
#pragma unroll
  for (int i = 0; i < NT; ++i) asm volatile("" : "+v"(cs[i]));
#pragma unroll
  for (int mt = 0; mt < MT; ++mt) {
    f32x2 M = {st[mt][0], st[mt][0]}, R = {st[mt][1], st[mt][1]};
    asm volatile("" : "+v"(M), "+v"(R));
#pragma unroll
    for (int nt = 0; nt < NT; ++nt) {
      const f32x2 v0 = R * __builtin_elementwise_fma(M, f32x2{cs[nt][0], cs[nt][1]}, f32x2{acc[mt][nt][0], acc[mt][nt][1]});
      const f32x2 v1 = R * __builtin_elementwise_fma(M, f32x2{cs[nt][2], cs[nt][3]}, f32x2{acc[mt][nt][2], acc[mt][nt][3]});
      acc[mt][nt][0] = __uint_as_float(pack2v(v0));
      acc[mt][nt][1] = __uint_as_float(pack2v(v1));
      const f32x2 pk = {acc[mt][nt][0], acc[mt][nt][1]};
      asm volatile("ds_write_b64 %0, %1 offset:%2" ::"v"(ta[nt]), "v"(pk), "n"(mt * 16 * K1_TP) : "memory");
    }
  }
}

DEVI void dma16m(const char* sbase, unsigned voff, unsigned m0val, unsigned long long mask) {
  asm volatile("s_mov_b32 m0, %2\n\ts_mov_b64 exec, %3\n\tglobal_load_lds_dwordx4 %0, %1\n\ts_mov_b64 exec, -1"
               :
               : "v"(voff), "s"(sbase), "s"(m0val), "s"(mask)
               : "memory");
}
template <int N>
DEVI void wait_vm() {
  asm volatile("s_waitcnt vmcnt(%0)" ::"n"(N) : "memory");
}

constexpr int LDS_ALL = 163840;
constexpr int LW_OFF = LDS_ALL - 128;
constexpr int LW_GO = 0, LW_T = 2, LW_O = 4  , LW_X1 = 6, LW_H = 8, LW_X2 = 12, LW_PK = 13, LW_Z = 14, LW_PAIR = 16, LW_AD = 20,
              LW_L = 22, LW_S = 24, LW_ST = 29, LW_HELLO = 30, LW_Q = 31;
DEVI void announce(unsigned addr, unsigned n, char* ws, int kind, int who, bool act) {
  const char* cb = uni_ptr(ws + OFF_CNT + (size_t)kind * 256 * 128);
  const unsigned long long mask = __ballot(act);
  unsigned t, u;
  asm volatile(
      "s_waitcnt vmcnt(0)\n\t"
      "s_mov_b64 exec, 1\n\t"
      "ds_add_rtn_u32 %0, %2, %3\n\t"
      "s_waitcnt lgkmcnt(0)\n\t"
      "v_readfirstlane_b32 %1, %0\n\t"
      "s_cmp_eq_u32 %1, %4\n\t"
      "s_cselect_b64 exec, %5, 0\n\t"
      "global_atomic_add %6, %3, %7 sc1\n\t"
      "s_mov_b64 exec, -1\n\t"
      "s_nop 0"
      : "=&v"(t), "=&s"(u)
      : "v"(addr), "v"(vconst(1u)), "s"(n - 1u), "s"(mask), "v"((unsigned)(who & 255) * 128u), "s"(cb)
      : "memory", "scc");
}
DEVI void cnt_wait(const char* ws, int kind, int who, unsigned n) {
  const char* cp = ws + OFF_CNT + (size_t)(kind * 256 + who) * 128;
#pragma unroll 1
  for (unsigned spin = 0; spin < SPIN_MEM; ++spin) {
    unsigned f;
    asm volatile("global_load_dword %0, %1, off sc1\n\ts_waitcnt vmcnt(0)" : "=&v"(f) : "v"(cp) : "memory");
    if (__builtin_amdgcn_readfirstlane(f) == n - 1u) break;
    __builtin_amdgcn_s_sleep(1);
  }
}
DEVI void dma16_sc1(const char* sbase, unsigned voff, unsigned m0val) {
  asm volatile("s_mov_b32 m0, %2\n\ts_nop 0\n\tglobal_load_lds_dwordx4 %0, %1 sc1"
               :
               : "v"(voff), "s"(sbase), "s"(m0val)
               : "memory");
}
DEVI void bar() { __builtin_amdgcn_s_barrier(); }
DEVI void lds_inc(unsigned addr, unsigned by = 1u) {
  asm volatile("s_mov_b64 exec, 1\n\tds_add_u32 %0, %1\n\ts_mov_b64 exec, -1" ::"v"(addr), "v"(vconst(by)) : "memory");
}
DEVI void lds_spin(unsigned addr, unsigned n) {
  SPIN_LDS {
    if (__builtin_amdgcn_readfirstlane(lds_rd32(addr)) >= n) break;
    __builtin_amdgcn_s_sleep(1);
  }
}
template <int SL = 1>
DEVI void lds_spin4(unsigned addr, unsigned n) {
  SPIN_LDS {
    if (((__builtin_amdgcn_readfirstlane(lds_rd32(addr)) + (0x80u - n) * 0x01010101u) & 0x80808080u) == 0x80808080u) break;
    __builtin_amdgcn_s_sleep(SL);
  }
}
DEVI int lane_id() {
  int l;
  asm volatile("v_mbcnt_lo_u32_b32 %0, -1, 0\n\tv_mbcnt_hi_u32_b32 %0, -1, %0" : "=v"(l));
  __builtin_assume((unsigned)l < 64u);
  return l;
}

template <int I>
DEVI u32x4 tr8(const u32x4 (&v)[8]) {
  constexpr unsigned sel = (I & 1) ? 0x07060302u : 0x05040100u;
  return u32x4{__builtin_amdgcn_perm(v[1][I / 2], v[0][I / 2], sel), __builtin_amdgcn_perm(v[3][I / 2], v[2][I / 2], sel),
               __builtin_amdgcn_perm(v[5][I / 2], v[4][I / 2], sel), __builtin_amdgcn_perm(v[7][I / 2], v[6][I / 2], sel)};
}
template <int PITCH, bool SUM, int... I>
DEVI void tr8_store(const u32x4 (&v)[8], unsigned dst, float (&cs)[8], std::integer_sequence<int, I...>) {
  auto one = [&](auto I_) {
    const u32x4 o = tr8<I_>(v);
    const unsigned d_ = dst;
    asm volatile("ds_write_b128 %0, %1 offset:%2" ::"v"(d_), "v"(o), "n"(I_ * PITCH) : "memory");
    if constexpr (SUM) {
#pragma unroll
      for (int d = 0; d < 4; ++d) {
        const unsigned od = o[d];
        cs[I_] = __builtin_amdgcn_fdot2_f32_bf16(__builtin_bit_cast(bf16x2, od), __builtin_bit_cast(bf16x2, 0x3f803f80u), cs[I_], false);
      }
    }
  };
  (one(std::integral_constant<int, I>{}), ...);
}
template <int NB>
DEVI void lds_out(unsigned src, char* dst, int lane) {
  constexpr int N = (NB + 1023) / 1024;
  u32x4 t[N];
  const unsigned a = src + (unsigned)lane * 16u;
#pragma unroll
  for (int i = 0; i < N; ++i) asm volatile("ds_read_b128 %0, %1 offset:%2" : "=v"(t[i]) : "v"(a), "n"(i * 1024) : "memory");
  const char* d = uni_ptr(dst);
#pragma unroll
  for (int i = 0; i < N; ++i) {
    asm volatile("s_waitcnt lgkmcnt(%1)" : "+v"(t[i]) : "n"(N - 1 - i) : "memory");
    if (i < N - 1 || lane < (NB - (N - 1) * 1024) / 16)
      asm volatile("global_store_dwordx4 %0, %1, %2 sc1\n\ts_nop 1" ::"v"((unsigned)lane * 16u), "v"(t[i]), "s"(d + i * 1024) : "memory");
  }
}
template <int A, int B, class F>
DEVI void static_for(F f) {
  if constexpr (A < B) {
    f(std::integral_constant<int, A>{});
    static_for<A + 1, B>(f);
  }
}
constexpr int WSP_V = 3;
#define RS0 4
#define RS1 7
#define RX0 RS0
constexpr int RAW_S1 = RS1;
constexpr int RAW_S0 = RS0;
template <int E>
struct RawX {
  static constexpr int first(int s) {
    return s <= RX0 ? 0 : (s > RAW_S1 ? E : (E * (s - RX0) + RAW_S1 - RX0) / (RAW_S1 + 1 - RX0));
  }
  static constexpr int n(int s) { return first(s + 1) - first(s); }
};
struct RawLd {
  static constexpr int VPS = 10, SB = K1R::SB;
  unsigned xoff[5], woff[5], m0x;
  unsigned long long xact0, wact[5], tag;
  const char *xp, *wp;
  int qb;
  DEVI void q_hook(int st, char* ws, unsigned lds0, int hp) {
    if ((qb >> 1) == st) {
      const int lane = lane_id(), g = lane >> 4;
      const unsigned a = lds0 + (unsigned)((st & 3) * SB + K1R::XB + ((qb & 1) * 32 + 16 * (g >> 1) + 4 * (g & 1) + ((lane >> 2) & 3)) * 288 +
                                           (lane & 3) * 8 + (hp & 1) * 8);
      const unsigned a2 = a + ((lane & 3) >= 1 ? 8u : 0u), a4 = a + ((lane & 3) >= 2 ? 8u : 0u);
      u32x2 qf[7][2];
#pragma unroll
      for (int nt = 0; nt < 7; ++nt)
        asm volatile("ds_read_b64_tr_b16 %0, %2 offset:%3\n\tds_read_b64_tr_b16 %1, %2 offset:%4"
                     : "=&v"(qf[nt][0]), "=&v"(qf[nt][1])
                     : "v"(nt == 2 ? a2 : (nt == 4 ? a4 : a)), "n"(nt * 32 + (nt > 4 ? 16 : (nt > 2 ? 8 : 0))),
                       "n"(2304 + nt * 32 + (nt > 4 ? 16 : (nt > 2 ? 8 : 0)))
                     : "memory");
      const char* d = uni_ptr(ws + OFF_PQ + (size_t)(hp * 18 + qb) * 7168 + 3072);
#pragma unroll
      for (int nt = 0; nt < 7; ++nt) {
        asm volatile("s_waitcnt lgkmcnt(%2)" : "+v"(qf[nt][0]), "+v"(qf[nt][1]) : "n"(12 - 2 * nt) : "memory");
        const u32x4 v = {qf[nt][0][0], qf[nt][0][1], qf[nt][1][0], qf[nt][1][1]};
        asm volatile("global_store_dwordx4 %0, %1, %2 offset:%3 sc1\n\ts_nop 1" ::"v"((unsigned)lane * 16u), "v"(v), "s"(d), "n"(nt * 1024 - 3072) : "memory");
      }
      if (st == 8) lds_wr32(lds0 + (unsigned)(LW_OFF + LW_Q * 4), 1u);
    }
  }
  DEVI void stage_x(unsigned so) {
    dma16m(xp, xoff[0], m0x + so, xact0);
#pragma unroll
    for (int i = 1; i < 4; ++i) dma16(xp, xoff[i], m0x + so + (unsigned)(i * 1024));
    dma16m(xp, xoff[4], m0x + so + 4096u, 0xffffffffull);
    xp += 128;
  }
  DEVI void stage_w(unsigned so) {
#pragma unroll
    for (int i = 0; i < 5; ++i) dma16m(wp, woff[i], m0x + so + (unsigned)(K1R::XB + i * 1024), wact[i]);
    wp += 64 * NQKV * 2;
  }
  DEVI void stage(unsigned so) {
    stage_x(so);
    stage_w(so);
  }
  DEVI void front(const u16* x, const u16* wqkv, const u16* wo, char* ws, int bid, unsigned lds0, int wl, int lane, int rt, int hp,
                  int pj MF_ARG) {
    xp = uni_ptr((const char*)x);
    wp = uni_ptr((const char*)wqkv + ((hp * 72) & ~15));
    m0x = lds0 + (unsigned)(wl * 288 * 16);
    xact0 = wl ? ~0ull : ~0xffull;
    const int row0 = wl * 36 + (lane >> 3);
#pragma unroll
    for (int i = 0; i < 5; ++i) {
      const int row = row0 + i * 8;
      int gr = rt * 128 - 16 + row;
      if (gr < 0) gr = 0;
      xoff[i] = (unsigned)(gr * (D_ * 2)) + (unsigned)(((lane ^ (row >> 1)) & 7) * 16);
    }
    stage_x(0u);
    u32x2 tg;
    asm volatile("global_load_dwordx2 %0, %1, %2 sc1" : "=v"(tg) : "v"(0u), "s"(uni_ptr(ws + OFF_TAG)) : "memory");
    asm volatile("" : "+v"(lane));
#pragma unroll
    for (int i = 0; i < 5; ++i) {
      const unsigned c = (unsigned)(i * 64 + (lane & 63));
      const unsigned wr = (c * 3641u) >> 16, wc = c - wr * 18u, seg = (wc * 13u) >> 6;
      wact[i] = __ballot(wc < 15u) & (i < 4 ? ~0ull : 0xffffffffull);
      woff[i] = (unsigned)(wl * 16 + swap23((int)wr)) * (unsigned)(NQKV * 2) + seg * (unsigned)(D_ * 2 - 80) + wc * 16u;
    }
    stage_w(0u);
    stage((unsigned)SB);
    MF(10);
    asm volatile("s_waitcnt vmcnt(%1)" : "+v"(tg) : "n"(VPS) : "memory");
    tag = (((unsigned long long)(unsigned)__builtin_amdgcn_readfirstlane(tg[1]) << 32) | (unsigned)__builtin_amdgcn_readfirstlane(tg[0])) +
          0x9e3779b97f4a7c15ull;
    MF(1);
    __builtin_amdgcn_s_barrier();
    MF(15);
    if (wl == 0 && lane == 0)
      asm volatile("global_store_dwordx2 %0, %1, off sc1\n\ts_nop 1" ::"v"(ws + OFF_HELLO + bid * 8), "v"(tag) : "memory");
    stage((unsigned)(2 * SB));
    qb = wl == 3 ? rt : ((wl == 2 && rt < 2) ? 16 + rt : 99);
    q_hook(0, ws, lds0, hp);
    const int nb = pj;
    const bool po = pj >= 0 && wl < 2;
    u32x4 v[8];
    unsigned da = 0u;
    if (po) {
      const int l54 = lane < 54 ? lane : 53, gl = l54 / 9, c = l54 - gl * 9, kl = wl * 6 + gl, t = nb / 6, kg = (nb - t * 6) * 12 + kl;
      da = lds0 + (unsigned)(K1R_PO + (kl >> 2) * 4608 + (16 * (kl & 1) + ((kl >> 1) & 1)) * 144 + c * 16);
      const unsigned sa[2] = {(unsigned)(okmap(kg * 8) * D_ + t * 72 + c * 8) * 2u, (unsigned)(okmap(kg * 8 + 4) * D_ + t * 72 + c * 8) * 2u};
      const char* ps = uni_ptr((const char*)wo);
#pragma unroll
      for (int j = 0; j < 8; ++j)
        asm volatile("global_load_dwordx4 %0, %1, %2 offset:%3" : "=v"(v[j]) : "v"(sa[j / 4]), "s"(ps), "n"((j % 4) * D_ * 2) : "memory");
    } else {
#pragma unroll
      for (int j = 0; j < 8; ++j) asm volatile("" : "=v"(v[j]));
    }
    unsigned slot = (unsigned)(3 * SB);
#pragma unroll
    for (int S = 0; S < RAW_S0; ++S) {
      wait_vm<VPS>();
      __builtin_amdgcn_s_barrier();
      if (S == 0) MF(16);
      if (S == 1) MF(17);
      if (S == 2) MF(18);
      if (S == 3) MF(19);
      stage(slot);
      slot = (slot == (unsigned)(3 * SB)) ? 0u : slot + (unsigned)SB;
      q_hook(S + 1, ws, lds0, hp);
      if (po && S == 1) {
        float cs[8];
        asm volatile("" : "+v"(v[0]), "+v"(v[1]), "+v"(v[2]), "+v"(v[3]), "+v"(v[4]), "+v"(v[5]), "+v"(v[6]), "+v"(v[7]));
        tr8_store<288, false>(v, da, cs, std::make_integer_sequence<int, 8>{});
        asm volatile("s_waitcnt lgkmcnt(0)" ::: "memory");
      }
      if (po && S == 2) lds_out<6912>(lds0 + (unsigned)(K1R_PO + wl * 6912), ws + OFF_PO + (size_t)nb * 13824 + wl * 6912, lane);
    }
  }
};
template <bool JOB>
DEVI void k1_raw_loader(RawLd& ld, const u16* __restrict__ w_in, const u16* __restrict__ w_out, char* __restrict__ ws,
                        unsigned long long tag, int bid, unsigned lds0, int wl, int lane, int rt, int hp, int ct, int job, int pj MF_ARG) {
  constexpr int VPS = RawLd::VPS, SB = RawLd::SB, E = JOB ? 16 : 0;
  const bool isi = job < 144;
  const int kb = isi ? job >> 3 : job - 144;
  const int pitch = isi ? F_ * 2 : D_ * 2;
  const int l36 = lane < 36 ? lane : 35;
  unsigned sa = (unsigned)l36 * 16u, da;
  if (isi) da = lds0 + (unsigned)((l36 >= 18 ? 9216 - 288 : 0) + wl * 8 * 288 + l36 * 16);
  else da = lds0 + (unsigned)((l36 / 9) * (4608 - 144) + (16 * (wl & 1) + (wl >> 1)) * 144 + l36 * 16);
  asm volatile("" : "+v"(sa), "+v"(da));
  u32x4 v[2][8];
  const char* ps = uni_ptr((const char*)(isi ? w_in + (job & 7) * 576 : w_out) + (size_t)(kb * 32 + wl * 8) * pitch);
  auto extra = [&](auto J_) {
    constexpr int j = J_;
    const char* q = ps + (j / 2) * pitch;
    u32x4 t;
    asm volatile("global_load_dwordx4 %0, %1, %2 offset:%3" : "=v"(t) : "v"(sa), "s"(q), "n"((j % 2) * 576) : "memory");
    v[j % 2][j / 2] = t;
  };
  auto step = [&](auto S_) {
    constexpr int S = S_;
    wait_vm<RawX<E>::n(S - 2) + (S <= 6 ? VPS : 0) + RawX<E>::n(S - 1)>();
    __builtin_amdgcn_s_barrier();
    MF(16 + S);
    if constexpr (S == RAW_S0) {
      static_assert(RAW_S0 >= 4, "wo blocks");
      if (pj >= 0 && wl == 0 && lane == 0)
        asm volatile("global_store_dwordx2 %0, %1, off sc1\n\ts_nop 1" ::"v"(ws + OFF_POT + (pj / 6) * 128 + (pj % 6) * 8), "v"(tag) : "memory");
    }
    if constexpr (S <= 5) ld.stage((unsigned)(((S + 3) % 4) * SB));
    ld.q_hook(S + 1, ws, lds0, hp);
    static_for<RawX<E>::first(S), RawX<E>::first(S + 1)>(extra);
  };
  const char* hp_ = ws + OFF_HELLO + lane * 32;
  u32x4 a, b;
  if (wl == 1) asm volatile("global_load_dwordx4 %0, %2, off sc1\n\tglobal_load_dwordx4 %1, %2, off offset:16 sc1" : "=&v"(a), "=&v"(b) : "v"(hp_) : "memory");
  else asm volatile("" : "=v"(a), "=v"(b));
  static_for<RAW_S0, 8>(step);
  MF(2);
  if (wl == 1) {
    asm volatile("" : "+v"(a), "+v"(b));
    const unsigned t0 = (unsigned)tag, t1 = (unsigned)(tag >> 32);
#pragma unroll 1
    for (unsigned spin = 0; spin < SPIN_MEM; ++spin) {
      const unsigned bad = ((a[0] ^ t0) | (a[2] ^ t0) | (b[0] ^ t0) | (b[2] ^ t0)) | ((a[1] ^ t1) | (a[3] ^ t1) | (b[1] ^ t1) | (b[3] ^ t1));
      if (!__any(bad != 0u)) break;
      __builtin_amdgcn_s_sleep(4);
      asm volatile("global_load_dwordx4 %0, %2, off sc1\n\tglobal_load_dwordx4 %1, %2, off offset:16 sc1\n\ts_waitcnt vmcnt(0)"
                   : "=&v"(a), "=&v"(b)
                   : "v"(hp_)
                   : "memory");
    }
    lds_wr32(lds0 + (unsigned)(LW_OFF + LW_HELLO * 4), 1u);
    if (bid == 0 && lane == 0) asm volatile("global_store_dwordx2 %0, %1, off sc1\n\ts_nop 1" ::"v"(ws + OFF_TAG), "v"(tag) : "memory");
  }
  MF(11);
  if (rt < 2) lds_spin(lds0 + (unsigned)(LW_OFF + LW_Q * 4), 1u);
  lds_spin(lds0 + (unsigned)K1R_CNT, 8u);
  if constexpr (JOB) {
#pragma unroll
    for (int b = 0; b < 2; ++b)
      asm volatile("s_waitcnt vmcnt(0)"
                   : "+v"(v[b][0]), "+v"(v[b][1]), "+v"(v[b][2]), "+v"(v[b][3]), "+v"(v[b][4]), "+v"(v[b][5]), "+v"(v[b][6]), "+v"(v[b][7])
                   :
                   : "memory");
  }
  MF(12);
  const char* tp = ws + OFF_POT + ct * 128 + (lane < 6 ? lane : 5) * 8;
  u32x2 f;
  asm volatile("global_load_dwordx2 %0, %1, off sc1" : "=v"(f) : "v"(tp) : "memory");
  if constexpr (JOB) {
    static_assert(SB == 4 * 9216, "staging");
#pragma unroll
    for (int b = 0; b < 2; ++b) {
      float cs[8] = {0.f, 0.f, 0.f, 0.f, 0.f, 0.f, 0.f, 0.f};
      tr8_store<288, true>(v[b], da + (unsigned)(b * 18432), cs, std::make_integer_sequence<int, 8>{});
      const f32x4 c0 = {cs[0], cs[1], cs[2], cs[3]}, c1 = {cs[4], cs[5], cs[6], cs[7]};
      const unsigned ca = lds0 + (unsigned)(3 * SB + wl * 2304) + sa * 2u;
      asm volatile("ds_write_b128 %0, %1 offset:%2" ::"v"(ca), "v"(c0), "n"(b * 1152) : "memory");
      asm volatile("ds_write_b128 %0, %1 offset:%2" ::"v"(ca), "v"(c1), "n"(b * 1152 + 16) : "memory");
    }
    lds_inc(lds0 + (unsigned)(LW_OFF + LW_ST * 4));
    lds_spin(lds0 + (unsigned)(LW_OFF + LW_ST * 4), 4u);
    if (isi) {
      const int j = (job & 7) * 4 + wl, ti = j < 16 ? 2 * j + 1 : 2 * (j - 16);
      lds_out<9216>(lds0 + (unsigned)(wl * 9216), ws + OFF_PI + (size_t)(ti * 18 + kb) * 9216, lane);
      f32x4 c[4];
      const unsigned ca = lds0 + (unsigned)(3 * SB + wl * 576) + sa;
#pragma unroll
      for (int i = 0; i < 4; ++i) asm volatile("ds_read_b128 %0, %1 offset:%2" : "=v"(c[i]) : "v"(ca), "n"(i * 2304) : "memory");
      asm volatile("s_waitcnt lgkmcnt(0)" : "+v"(c[0]), "+v"(c[1]), "+v"(c[2]), "+v"(c[3]));
      const f32x4 t = (c[0] + c[1]) + (c[2] + c[3]);
      if (lane < 36) pk_st16(ws + OFF_CSP + ((size_t)kb * F_ + ti * 144) * 4 + sa, __builtin_bit_cast(u32x4, t));
    } else {
      lds_out<4608>(lds0 + (unsigned)(wl * 9216), ws + OFF_PW + (size_t)(wl * 144 + kb) * 4608, lane);
      lds_out<4608>(lds0 + (unsigned)(wl * 9216 + 4608), ws + OFF_PW + (size_t)(wl * 144 + 72 + kb) * 4608, lane);
    }
  }
  MF(8);
  announce(lds0 + (unsigned)(LW_OFF + LW_PK * 4), 4u, ws, CN_PK, isi ? 8 + (job & 3) * 4 + lane : 24 + lane, JOB && lane < (isi ? 4 : 8));
  MF(9);
  if (wl == 0) {
    asm volatile("" : "+v"(f));
#pragma unroll 1
    for (unsigned spin = 0; spin < SPIN_MEM; ++spin) {
      if (!__any(f[0] != (unsigned)tag || f[1] != (unsigned)(tag >> 32))) break;
      __builtin_amdgcn_s_sleep(2);
      asm volatile("global_load_dwordx2 %0, %1, off sc1\n\ts_waitcnt vmcnt(0)" : "=&v"(f) : "v"(tp) : "memory");
    }
  }
}

template <int MT, int NT>
DEVI void k1_gemm(char* smem, const char* ws, const float* __restrict__ csq, int rt, int hp, int mt0, int nt0,
                  bool late, int lane MK_ARG) {
  const unsigned lds0 = (unsigned)(size_t)smem;
  const int g = lane >> 4;
  f32x4 cs[NT];
  unsigned ta[NT];
  const char* csp = uni_ptr((const char*)(csq + (hp & 1) * 4));
  unsigned cad[NT];
#pragma unroll
  for (int nt = 0; nt < NT; ++nt) {
    int c = (nt0 + nt) * 16 + g * 4;
    if (c > 104) c = 104;
    const int part = c / 36, ci = c - part * 36;
    if (CSL) {
      cad[nt] = lds0 + (unsigned)(K1_CS_OFF + (part * 40 + (hp & 1) * 4 + ci) * 4);
      asm volatile("" : "+v"(cad[nt]));
    } else
    asm volatile("global_load_dwordx4 %0, %1, %2 sc1" : "=v"(cs[nt]) : "v"((unsigned)(part * 40 + ci) * 4u), "s"(csp));
    const int to = (nt0 + nt) * 16 + g * 4 > 104 ? 120 : part * 80 + ci * 2 + (ci >= 20 ? 8 : 0);
    ta[nt] = lds0 + (unsigned)(K1_T_OFF + (mt0 * 16 + (lane & 15)) * K1_TP + to);
    asm volatile("" : "+v"(ta[nt]));
  }
  unsigned sa = lds0 + (unsigned)(K1_ST_OFF + (mt0 * 16 + (lane & 15)) * 8);
  asm volatile("" : "+v"(sa));
  f32x4 acc[MT][NT];
#pragma unroll
  for (int mt = 0; mt < MT; ++mt)
#pragma unroll
    for (int nt = 0; nt < NT; ++nt) acc[mt][nt] = f32x4{0.f, 0.f, 0.f, 0.f};
  f32x2 st[MT];
  if (G1E && late) bar();
  if constexpr (NT == 7) cnt_wait(ws, CN_X2, rt * 16 + hp, rt ? 24u : 16u);
  bar();
  MK(1);
  {
    GemmMath<K1C, MT, NT, (NT == 7 ? 5 : 3), (NT == 3 ? 6 : NT), true, 2, false, K1_WS> gm(acc);
    gm.setup(lds0, mt0, nt0, lane, 0u);
    bar();
    if (CSL) {
#pragma unroll
      for (int nt = 0; nt < NT; ++nt) asm volatile("ds_read_b128 %0, %1" : "=v"(cs[nt]) : "v"(cad[nt]));
    }
    gm.first();
    gm.template mid<8, true, AL_K>();
#pragma unroll
    for (int mt = 0; mt < MT; ++mt) asm volatile("ds_read_b64 %0, %1 offset:%2" : "=v"(st[mt]) : "v"(sa), "n"(mt * 128));
    asm volatile("s_waitcnt vmcnt(0)" ::: "memory");
    gm.last();
  }
#pragma unroll
  for (int nt = 0; nt < NT; ++nt) asm volatile("" : "+v"(cs[nt]));
#pragma unroll
  for (int mt = 0; mt < MT; ++mt) asm volatile("" : "+v"(st[mt]));
  MK(2);
#pragma unroll
  for (int mt = 0; mt < MT; ++mt) {
    f32x2 M = {st[mt][0], st[mt][0]}, R = {st[mt][1], st[mt][1]};
    asm volatile("" : "+v"(M), "+v"(R));
#pragma unroll
    for (int nt = 0; nt < NT; ++nt) {
      const f32x2 v0 = R * __builtin_elementwise_fma(M, f32x2{cs[nt][0], cs[nt][1]}, f32x2{acc[mt][nt][0], acc[mt][nt][1]});
      const f32x2 v1 = R * __builtin_elementwise_fma(M, f32x2{cs[nt][2], cs[nt][3]}, f32x2{acc[mt][nt][2], acc[mt][nt][3]});
      acc[mt][nt][0] = __uint_as_float(pack2v(v0));
      acc[mt][nt][1] = __uint_as_float(pack2v(v1));
      const f32x2 pk = {acc[mt][nt][0], acc[mt][nt][1]};
      asm volatile("ds_write_b64 %0, %1 offset:%2" ::"v"(ta[nt]), "v"(pk), "n"(mt * 16 * K1_TP) : "memory");
    }
  }
}

DEVI float vmax(float a, float b) {
  float r;
  asm("v_max_f32 %0, %1, %2" : "=v"(r) : "v"(a), "v"(b));
  return r;
}
struct Attn {
  unsigned ka, kb, va, vb, m0, m1, m2, pm[2], oa, pa;
  f32x4 mk;
  unsigned long long sel[4];
  const char* ob;
  DEVI void setup(unsigned T, char* o, int r0, int hp, int wave, int lane) {
    const int g = lane >> 4, r = lane & 15, blk = wave >> 1, hd = wave & 1;
    const unsigned co = (unsigned)(g < 2 ? hd * 48 + g * 16 : 32);
    ka = T + (unsigned)((blk * 32 + 2 * r) * K1_TP) + co;
    kb = T + (unsigned)((blk * 32 + 2 * (r & 7)) * K1_TP) + co;
    const int vr = 4 * g + ((lane >> 2) & 3);
    va = T + (unsigned)((blk * 32 + 2 * vr) * K1_TP + (lane & 3) * 8);
    vb = T + (unsigned)((blk * 32 + 2 * (vr & 7)) * K1_TP + (lane & 3) * 8);
    m0 = (g < 2 || (g == 2 && hd == 0)) ? ~0u : 0u;
    m1 = (g < 2 || (g == 2 && hd == 1)) ? ~0u : 0u;
    m2 = g < 2 ? ~0u : 0u;
    pm[0] = pm[1] = 0u;
    const float ci = __uint_as_float(vconst(0xff800000u));
    const unsigned ch = vconst(0xffffu);
#pragma unroll
    for (int e = 0; e < 4; ++e) {
      const int w1 = r + 8 - 4 * g - e;
      mk[e] = (w1 & 15) <= 7 ? 0.f : ci;
      sel[e] = __ballot(w1 >= 8);
      if (w1 >= 8) pm[e >> 1] |= ch << (16 * (e & 1));
    }
    oa = (unsigned)((8 + hp * 4 + hd * 2 + (g >> 1)) * 1024 + ((blk & 1) * 32 + 2 * r + (g & 1)) * 16);
    pa = ((oa & 1023u) >> 2) + (unsigned)((hp * 2 + hd) * 256);
    ob = uni_ptr(o + (size_t)((r0 >> 6) + (wave >> 2)) * (72 * 1024));
    asm volatile("" : "+v"(ka), "+v"(kb), "+v"(va), "+v"(vb), "+v"(m0), "+v"(m1), "+v"(m2), "+v"(pm[0]), "+v"(pm[1]), "+v"(oa));
    asm volatile("" : "+v"(mk), "+v"(pa), "+s"(ob), "+s"(sel[0]), "+s"(sel[1]), "+s"(sel[2]), "+s"(sel[3]));
  }
  DEVI void run(int r0, int hp, int wave, int lane) {
    const int hd = wave & 1;
    u32x4 q[2], k0[2], k1[2];
    u32x2 v[2][2][2];
#pragma unroll
    for (int p = 0; p < 2; ++p) {
      asm volatile("ds_read_b128 %0, %1 offset:%2" : "=v"(q[p]) : "v"(ka), "n"((16 + p) * K1_TP) : "memory");
      asm volatile("ds_read_b128 %0, %1 offset:%2" : "=v"(k0[p]) : "v"(ka), "n"(p * K1_TP + 80) : "memory");
      asm volatile("ds_read_b128 %0, %1 offset:%2" : "=v"(k1[p]) : "v"(kb), "n"((32 + p) * K1_TP + 80) : "memory");
    }
    const unsigned vh = (unsigned)hd * 48u;
#pragma unroll
    for (int p = 0; p < 2; ++p) {
      asm volatile("ds_read_b64_tr_b16 %0, %1 offset:%2" : "=v"(v[p][0][0]) : "v"(va + vh), "n"(p * K1_TP + 160) : "memory");
      asm volatile("ds_read_b64_tr_b16 %0, %1 offset:%2" : "=v"(v[p][0][1]) : "v"(vb + vh), "n"((32 + p) * K1_TP + 160) : "memory");
      asm volatile("ds_read_b64_tr_b16 %0, %1 offset:%2" : "=v"(v[p][1][0]) : "v"(va), "n"(p * K1_TP + 192) : "memory");
      asm volatile("ds_read_b64_tr_b16 %0, %1 offset:%2" : "=v"(v[p][1][1]) : "v"(vb), "n"((32 + p) * K1_TP + 192) : "memory");
    }
    const u32x4 one4 = {vconst(0x3f803f80u), vconst(0x3f803f80u), vconst(0x3f803f80u), vconst(0x3f803f80u)};
    const s16x8 ones = __builtin_bit_cast(s16x8, one4);
    const float c = 0.23570226039551584f * 1.4426950408889634f;
    const f32x4 z = {0.f, 0.f, 0.f, 0.f};
    f32x4 sc[2][2];
#pragma unroll
    for (int p = 0; p < 2; ++p) {
      asm volatile("s_waitcnt lgkmcnt(%3)" : "+v"(q[p]), "+v"(k0[p]), "+v"(k1[p]) : "n"(11 - 3 * p));
      const s16x8 qf = __builtin_bit_cast(s16x8, q[p] & u32x4{m0, m1, m2, m2});
      sc[p][0] = __builtin_amdgcn_mfma_f32_16x16x32_bf16(__builtin_bit_cast(s16x8, k0[p]), qf, mk, 0, 0, 0);
      sc[p][1] = __builtin_amdgcn_mfma_f32_16x16x32_bf16(__builtin_bit_cast(s16x8, k1[p]), qf, mk, 0, 0, 0);
      __builtin_amdgcn_sched_barrier(0);
    }
    if (__builtin_expect(r0 == 0 && wave < 2, 0)) {
#pragma unroll
      for (int p = 0; p < 2; ++p)
#pragma unroll
        for (int e = 0; e < 4; ++e)
          if (lane < 32) sc[p][0][e] = mk[e] - 4.242640687e9f;
    }
    float lg[2][4], mx[2];
#pragma unroll
    for (int p = 0; p < 2; ++p) {
#pragma unroll
      for (int e = 0; e < 4; ++e) lg[p][e] = __builtin_amdgcn_inverse_ballot_w64(sel[e]) ? sc[p][1][e] : sc[p][0][e];
      asm("v_max3_f32 %0, %1, %2, %3\n\tv_max_f32 %0, %0, %4" : "=&v"(mx[p]) : "v"(lg[p][0]), "v"(lg[p][1]), "v"(lg[p][2]), "v"(lg[p][3]));
    }
    {
      const auto a = __builtin_amdgcn_permlane32_swap(__float_as_uint(mx[0]), __float_as_uint(mx[1]), false, false);
      const unsigned b = __float_as_uint(vmax(__uint_as_float(a[0]), __uint_as_float(a[1])));
      const auto d = __builtin_amdgcn_permlane16_swap(b, b, false, false);
      const unsigned f = __float_as_uint(vmax(__uint_as_float(d[0]), __uint_as_float(d[1])) * -c);
      const auto h = __builtin_amdgcn_permlane32_swap(f, f, false, false);
      mx[0] = __uint_as_float(h[0]);
      mx[1] = __uint_as_float(h[1]);
    }
#pragma unroll
    for (int p = 0; p < 2; ++p)
#pragma unroll
      for (int e = 0; e < 4; ++e) lg[p][e] = __builtin_amdgcn_exp2f(__builtin_fmaf(lg[p][e], c, mx[p]));
    asm volatile("s_waitcnt lgkmcnt(0)"
                 : "+v"(v[0][0][0]), "+v"(v[0][0][1]), "+v"(v[0][1][0]), "+v"(v[0][1][1]), "+v"(v[1][0][0]), "+v"(v[1][0][1]),
                   "+v"(v[1][1][0]), "+v"(v[1][1][1]));
    __builtin_amdgcn_sched_barrier(0);
    f32x4 su[2], o0[2], o1[2];
    s16x8 pp[2];
#pragma unroll
    for (int p = 0; p < 2; ++p) {
      const unsigned p0 = pack2(lg[p][0], lg[p][1]), p1 = pack2(lg[p][2], lg[p][3]);
      pp[p] = __builtin_bit_cast(s16x8, u32x4{p0 & ~pm[0], p1 & ~pm[1], p0 & pm[0], p1 & pm[1]});
    }
    __builtin_amdgcn_sched_barrier(0);
#pragma unroll
    for (int p = 0; p < 2; ++p) {
      const s16x8 pf = pp[p];
      const u32x4 v0 = {v[p][0][0][0], v[p][0][0][1], v[p][0][1][0], v[p][0][1][1]};
      const u32x4 v1 = {v[p][1][0][0], v[p][1][0][1], v[p][1][1][0], v[p][1][1][1]};
      su[p] = __builtin_amdgcn_mfma_f32_16x16x32_bf16(ones, pf, z, 0, 0, 0);
      o0[p] = __builtin_amdgcn_mfma_f32_16x16x32_bf16(__builtin_bit_cast(s16x8, v0), pf, z, 0, 0, 0);
      o1[p] = __builtin_amdgcn_mfma_f32_16x16x32_bf16(__builtin_bit_cast(s16x8, v1), pf, z, 0, 0, 0);
    }
    __builtin_amdgcn_sched_barrier(0);
    u32x2 y[2], yl;
    unsigned hm = (unsigned)hd - 1u;
    asm volatile("" : "+s"(hm));
#pragma unroll
    for (int p = 0; p < 2; ++p) {
      const float inv = __builtin_amdgcn_rcpf(su[p][0]);
      y[p] = u32x2{pack2(o0[p][0] * inv, o0[p][1] * inv), pack2(o0[p][2] * inv, o0[p][3] * inv)};
      const unsigned la = pack2(o1[p][0] * inv, o1[p][1] * inv), lb = pack2(o1[p][2] * inv, o1[p][3] * inv);
      yl[p] = (la & hm) | (lb & ~hm);
    }
    const auto s0 = __builtin_amdgcn_permlane16_swap(y[0][0], y[1][0], false, false);
    const auto s1 = __builtin_amdgcn_permlane16_swap(y[0][1], y[1][1], false, false);
    const u32x4 w = {s0[0], s1[0], s0[1], s1[1]};
    asm volatile("global_store_dwordx4 %0, %1, %4 sc1\n\ts_mov_b64 exec, 0xffff\n\tglobal_store_dwordx2 %2, %3, %4 sc1\n\ts_mov_b64 exec, -1\n\ts_nop 0" ::"v"(oa),
                 "v"(w), "v"(pa), "v"(yl), "s"(ob)
                 : "memory");
  }
};

#define NL_MAIN 708
DEVI void code_again(const char* code0, int bid) {
  constexpr int PER = (2 * NL_MAIN + 31) / 32;
  const int lane = lane_id();
  int li = (bid >> 3) * PER + (lane < PER ? lane : PER - 1);
  if (li > 2 * NL_MAIN - 1) li = 2 * NL_MAIN - 1;
  unsigned t;
  asm volatile("global_load_dword %0, %1, %2\n\ts_waitcnt vmcnt(0)" : "=&v"(t) : "v"((unsigned)li * 64u), "s"(code0) : "memory");
}
DEVI void math_start(char* smem, const char* code0, const u16* b_sp, const u16* wqkv, char* ws, int bid, int wave, int lane) {
  const unsigned lds0 = (unsigned)(size_t)smem;
  const int tid = wave * 64 + lane, idx = bid >> 3;
  if (wave < 3) {
    static_assert(2 * SZ_ZP == (size_t)256 * 2048 && NCNT <= 16, "mark split");
    const u32x4 mk = {0xffffffffu, 0xffffffffu, 0xffffffffu, 0xffffffffu};
    char* c = ws + OFF_CNT + (size_t)((lane >> 3) * 256 + bid) * 128 + (lane & 7) * 16;
    pk_st16(wave < 2 ? ws + OFF_ZP + (size_t)((bid * 2 + wave) * 64 + lane) * 16 : c, mk);
    if (wave == 2 && lane < (NCNT - 8) * 8) pk_st16(c + 8 * 256 * 128, mk);
  }
  if (wave == 3) {
    const int li = idx * 54 + (lane < 54 ? lane : 53);
    touch64(uni_ptr((const char*)wqkv), (unsigned)(li * 1152 + 512), lds0 + (unsigned)(K1R_DUMMY + 768));
  }
  if (!CT0 && wave == 4) {
    constexpr int PER = (2 * NL_MAIN + 31) / 32;
    static_assert(PER <= 64, "one load");
    int li = idx * PER + (lane < PER ? lane : PER - 1);
    if (li > 2 * NL_MAIN - 1) li = 2 * NL_MAIN - 1;
    touch64(code0, (unsigned)li * 64u, lds0 + (unsigned)(K1R_DUMMY + 768));
  }
  if (wave == 5 && idx == 31) touch64(uni_ptr((const char*)b_sp), (unsigned)lane * 64u, lds0 + (unsigned)(K1R_DUMMY + 768));
  const u32x4 ones = {0x3f803f80u, 0x3f803f80u, 0x3f803f80u, 0x3f803f80u};
  if (tid < 256) *reinterpret_cast<u32x4*>(smem + (tid >> 6) * K1R::SB + K1R::XB + (tid & 63) * 288 + 240) = ones;
  else if (tid < 288) *reinterpret_cast<u32x4*>(smem + ((tid - 256) >> 3) * K1R::SB + (tid & 7) * 16) = ones;
  else if (tid < 296) *reinterpret_cast<u32x4*>(smem + LW_OFF + (tid - 288) * 16) = u32x4{0u, 0u, 0u, 0u};
  asm volatile("s_waitcnt lgkmcnt(0)" ::: "memory");
}

#define SAFE_OUT(L)
#define NOP_(n) "=s"(n)
template <int NST, int NS, int NSW, int WOFF, int XW, int X0, int S0, unsigned BAR>
struct ResX {
  static constexpr int WS = 4 * 4608, R = 10, V1 = 5 * NS, NV = V1 + 10 * (NST - NS), NG = (NSW + 2) / 3;
  s16x8 xf[XW][2];
  s16x8 wf[R];
  unsigned wrd[2][NG], xo, ea, fa, fv, tag;
  const char* xp[3];
  f32x4 (&acc)[2][5];
  DEVI ResX(f32x4 (&a)[2][5]) : acc(a) {}
  static constexpr int st(int V) { return V < V1 ? V / 5 : NS + (V - V1) / 10; }
  static constexpr int hf(int V) { return V < V1 ? 0 : ((V - V1) / 5) & 1; }
  static constexpr bool first(int V) { return V < V1 ? V % 5 == 0 : (V - V1) % 10 == 0; }
  static constexpr int wreq(int V) {
    int n = V + 5;
    if (V >= V1 - 4) n += V < V1 ? V - (V1 - 5) : 4;
    return n < NV ? n : NV;
  }
  static constexpr int XU = V1 + 1, XF = V1 + 11, XR = V1 + 15, XC = V1 + 21;
  static constexpr int oth(int V) { return NS && ((V >= XU && V <= XF && (V - XU) % 2 == 0) || V == XR || (V > XC && V <= XC + 5)) ? 1 : 0; }
  static constexpr int newer(int V) {
    const int F = V + 1 < NV ? V + 1 : V;
    int v = V - 1, n = 0;
    for (; v >= 0 && wreq(v - 1) > F; --v) n += wreq(v) - wreq(v - 1) + oth(v);
    return n + wreq(v) - 1 - F + (v >= 0 ? oth(v) : 0);
  }
  static constexpr int xreq(int T) {
    int n = X0 + 2 * (T + 1);
    if (n > T + XW) n = T + XW;
    if (NS && T <= 11 && n > 18) n = 18;
    if (T < 0) n = X0;
    return n < NST ? n : NST;
  }
  static_assert(xreq(NS - 2) == NST && (NS == 0 || XC + 5 < NV), "the last steps only compute and swap");
  template <int T, int M>
  DEVI void xld() {
    asm volatile("global_load_dwordx4 %0, %1, %2 sc1" : "=v"(xf[T % XW][M]) : "v"(xo), "s"(xp[M == 0 ? 0 : (T < NS ? 1 : 2)]));
    if constexpr (M == 1 && T + 1 < NST) asm volatile("v_add_u32 %0, 0x2000, %0" : "+v"(xo));
  }
  template <int V>
  DEVI void rd() {
    constexpr int T = st(V), sl = (T / 2 + S0) % NSW;
    asm volatile("ds_read_b128 %0, %1 offset:%2" : "=v"(wf[V % R]) : "v"(wrd[hf(V)][sl / 3]), "n"((sl % 3) * WS + (T & 1) * 9216 + (V % 5) * 32));
  }
  template <int V, int M, int H>
  DEVI void mf() {
    acc[M][V % 5] = __builtin_amdgcn_mfma_f32_16x16x32_bf16(wf[V % R], xf[st(V) % XW][H], acc[M][V % 5], 0, 0, 0);
    __builtin_amdgcn_sched_barrier(0);
  }
  template <int V>
  DEVI void unit() {
    constexpr int T = st(V), n = V % 5, nx = xreq(T) - xreq(T - 1), nw = wreq(V) - wreq(V - 1);
    constexpr int vm = 2 * (xreq(T - 1) - 1 - T);
    static_assert(newer(V) <= 15, "lgkmcnt range");
    if constexpr (first(V) && V % 2 == 0) asm volatile("s_waitcnt vmcnt(%0) lgkmcnt(%1)" ::"n"(vm), "n"(newer(V)));
    else if constexpr (first(V)) asm volatile("s_waitcnt vmcnt(%0)" ::"n"(vm));
    else if constexpr (V % 2 == 0) asm volatile("s_waitcnt lgkmcnt(%0)" ::"n"(newer(V)));
    __builtin_amdgcn_sched_barrier(0);
    if constexpr (V < V1) mf<V, V & 1, V & 1>();
    else mf<V, 0, hf(V)>();
    if constexpr (nw > 0) rd<wreq(V - 1)>();
    __builtin_amdgcn_sched_barrier(0);
    if constexpr (V < V1) {
      mf<V, 1 - (V & 1), 1 - (V & 1)>();
      if constexpr (nw > 1) rd<wreq(V - 1) + 1>();
      if constexpr (nx == 2 && n < 4) xld<xreq(T - 1) + n / 2, n % 2>();
      if constexpr (nx == 1 && n == (T & 1)) xld<xreq(T - 1), 0>();
      if constexpr (nx == 1 && n == (T & 1) + 2) xld<xreq(T - 1), 1>();
      if constexpr (n == 4 && (T & 1) && ((BAR >> (T / 2)) & 1u)) bar();
    } else if constexpr (NS > 0) {
      if constexpr (oth(V) && V < XF) asm volatile("ds_write_b128 %0, %1 offset:%2" ::"v"(ea), "v"(acc[1][(V - XU) / 2]), "n"((V - XU) * 512));
      if constexpr (V == XF) asm volatile("ds_write_b32 %0, %1" ::"v"(fa), "v"(fv));
      if constexpr (V == XF + 2) asm volatile("v_xor_b32 %0, 4, %0" : "+v"(fa));
      if constexpr (V == XR) asm volatile("ds_read_b32 %0, %1" : "+v"(fv) : "v"(fa));
      if constexpr (V == XR + 2) asm volatile("v_xor_b32 %0, 0x2000, %0" : "+v"(ea));
      if constexpr (V == XC) {
        static_assert(wreq(XR) <= XC + 1 || true, "");
        unsigned c = 1u << 18;
        asm volatile(
            "s_waitcnt lgkmcnt(%4)\n\tv_cmp_ne_u32 vcc, %3, %0\n\ts_cbranch_vccnz .Lw%=\n\t.subsection 1\n.Lw%=:\n\tds_read_b32 %0, %2\n\t"
            "s_waitcnt lgkmcnt(0)\n\tv_cmp_ne_u32 vcc, %3, %0\n\t" SAFE_OUT(".Lr%=") "s_cbranch_vccnz .Lw%=\n\ts_branch .Lr%=\n\t.subsection 0\n.Lr%=:"
            : "+v"(fv), NOP_(c)
            : "v"(fa), "s"(tag), "n"(wreq(XC) - wreq(XR))
            : "vcc", "scc");
      }
      if constexpr (oth(V) && V > XC) asm volatile("ds_read_b128 %0, %1 offset:%2" : "=v"(acc[1][V - XC - 1]) : "v"(ea), "n"((V - XC - 1) * 1024));
    }
    __builtin_amdgcn_sched_barrier(0);
  }
  template <int... V>
  DEVI void units(std::integer_sequence<int, V...>) {
    (unit<V>(), ...);
  }
  DEVI void run() {
    units(std::make_integer_sequence<int, NV>{});
    if constexpr (NS > 0) {
      asm volatile("s_waitcnt lgkmcnt(0)" : "+v"(acc[1][0]), "+v"(acc[1][1]), "+v"(acc[1][2]), "+v"(acc[1][3]), "+v"(acc[1][4]));
#pragma unroll
      for (int i = 0; i < 5; ++i) acc[0][i] += acc[1][i];
    }
  }
  template <int... V>
  DEVI void pro(std::integer_sequence<int, V...>) {
    (rd<V>(), ...);
  }
  DEVI void prow() { pro(std::make_integer_sequence<int, wreq(-1)>{}); }
  template <int T0, int... T>
  DEVI void prox(std::integer_sequence<int, T...>) {
    ((xld<T0 + T, 0>(), xld<T0 + T, 1>()), ...);
  }
  DEVI void setup(unsigned lds0, const char* xbase, int wave, int lane, unsigned red, unsigned nth) {
    const int g = lane >> 4, r = lane & 15, kq = wave & 1;
    xp[0] = xbase + wave * 256;
    xp[1] = xbase + (wave ^ 1) * 256;
    xp[2] = xp[0] + (kq ? -4096 : 4096);
    xo = (unsigned)((4 * kq + g) * 1024 + r * 16);
#pragma unroll
    for (int i = 0; i < 2 * NG; ++i) {
      wrd[i / NG][i % NG] = lds0 + (unsigned)(WOFF + (i % NG) * 3 * WS + (kq ^ (i / NG)) * 4608 + (2 * (r & 7) + 16 * (g & 1) + (g >> 1)) * 144 + (r >> 3) * 16);
      if (i < NG || NS == 0 || i % NG == ((NS / 2 + S0) % NSW) / 3) asm volatile("" : "+v"(wrd[i / NG][i % NG]));
    }
    asm volatile("" : "+v"(xo));
    if constexpr (NS > 0) {
      ea = lds0 + red + (unsigned)((wave ^ 1) * 8192 + lane * 16);
      fa = lds0 + (unsigned)(LW_OFF + (LW_PAIR + wave) * 4);
      fv = tag = nth;
      asm volatile("" : "+v"(ea), "+v"(fa), "+v"(fv));
    }
  }
  template <int N>
  DEVI void init(u32x2 (&xv)[5]) {
    asm volatile("s_waitcnt vmcnt(%5)" : "+v"(xv[0]), "+v"(xv[1]), "+v"(xv[2]), "+v"(xv[3]), "+v"(xv[4]) : "n"(N));
#pragma unroll
    for (int i = 0; i < 5; ++i) {
      acc[0][i] = f32x4{bf_lo(xv[i][0]), bf_hi(xv[i][0]), bf_lo(xv[i][1]), bf_hi(xv[i][1])};
      asm volatile("" : "+v"(acc[0][i]));
      acc[1][i] = f32x4{0.f, 0.f, 0.f, 0.f};
    }
  }
};

#define P5_XW 8
#define P5_X0 5
constexpr unsigned P5_BAR = 0x2aaau;
constexpr int P5_W = 15 * 1024;
constexpr int P2_W = K1_ST_OFF - 18 * 4608;
constexpr int P5_RED = 0;
static_assert(P5_RED % 16384 == 0 && P5_RED + 3 * 8192 + 5120 <= P5_W + 18432, "exchange area");
static_assert(P2_W >= K1_T_OFF + 144 * K1_TP + 64 && P2_W % 16 == 0 && P5_W + 8 * 18432 <= LW_OFF, "LDS placement");

DEVI void res_ldx(u32x2 (&xv)[5], const char* src, bool rm, int rb, int ct, int wave, int lane) {
  const int g = lane >> 4, g2 = g & 1, rl = wave * 16 + (lane & 15);
  const char* xp = uni_ptr(rm ? src + ((size_t)rb * 64 * D_ + ct * 72) * 2 : src + (size_t)(rb * 72 + ct * 9) * 1024);
  const unsigned o = rm ? (unsigned)(rl * (D_ * 2) + g * 8) : (unsigned)((g >> 1) * 1024 + rl * 16 + g2 * 8);
  const unsigned o2 = rm ? (unsigned)(rl * (D_ * 2) + g2 * 8 + 128) : (unsigned)(8192 + rl * 16 + g2 * 8);
#pragma unroll
  for (int i = 0; i < 4; ++i) asm volatile("global_load_dwordx2 %0, %1, %2 sc1" : "=v"(xv[i]) : "v"(o), "s"(xp + i * (rm ? 32 : 2048)));
  asm volatile("global_load_dwordx2 %0, %1, %2 sc1" : "=v"(xv[4]) : "v"(o2), "s"(xp));
}

DEVI void p2_gemm(f32x4 (&acc)[2][5], u32x2 (&xv)[5], unsigned lds0, const char* ob, int layer, int wave, int lane MK_ARG) {
  ResX<9, 0, 5, P2_W, 9, 9, 0, 0u> gm(acc);
  gm.setup(lds0, ob, wave, lane, 0u, 0u);
  unsigned a = (gm.xo & ~1023u) | ((gm.xo & 1023u) >> 2);
  gm.xo += 8192u;
  asm volatile("" : "+v"(a), "+v"(gm.xo));
  const char* pp[2] = {gm.xp[0] - wave * 192, gm.xp[2] - wave * 192};
  place<AL_P2>();
  bar();
  MK(6);
  unsigned e[2][4];
#pragma unroll
  for (int d = 0; d < 8; ++d) {
    if (NOPL && (d & 3)) e[d / 4][d % 4] = 0x3c003c00u;
    else asm volatile("global_load_dword %0, %1, %2 offset:%3 sc1" : "=v"(e[d / 4][d % 4]) : "v"(a), "s"(pp[d / 4]), "n"((d % 4) * 256));
  }
  gm.template prox<1>(std::make_integer_sequence<int, 8>{});
  lds_inc(lds0 + (unsigned)(LW_OFF + (LW_T + layer) * 4));
  gm.prow();
  gm.template init<(NOPL ? 18 : 24)>(xv);
  asm volatile("s_waitcnt vmcnt(16)"
               : "+v"(e[0][0]), "+v"(e[0][1]), "+v"(e[0][2]), "+v"(e[0][3]), "+v"(e[1][0]), "+v"(e[1][1]), "+v"(e[1][2]), "+v"(e[1][3]));
  gm.xf[0][0] = __builtin_bit_cast(s16x8, u32x4{e[0][0], e[0][1], e[0][2], e[0][3]});
  gm.xf[0][1] = __builtin_bit_cast(s16x8, u32x4{e[1][0], e[1][1], e[1][2], e[1][3]});
  __builtin_amdgcn_sched_barrier(0);
  gm.run();
}

struct ResOut {
  const char *xp[3], *sp;
  unsigned xo, so;
  DEVI void setup(char* xim, char* xs, u16* out, bool fin, int rb, int ct, int wave, int lane) {
    const int g = lane >> 4, rl = wave * 16 + (lane & 15);
    xp[0] = uni_ptr(fin ? (const char*)(out + (size_t)rb * 64 * D_ + ct * 72) : xim + (size_t)(rb * 72 + ct * 9) * 1024);
    xp[1] = xp[0] + (fin ? 64 : 4096);
    xp[2] = xp[0] + (fin ? 128 : 8192);
    sp = uni_ptr(xs + (size_t)(rb * 8 + ct) * 512);
    xo = (unsigned)(((g & 1) * 2 + (g >> 1)) * (fin ? 16 : 1024) + rl * (fin ? D_ * 2 : 16));
    so = (unsigned)(rl * 8);
    asm volatile("" : "+v"(xo), "+v"(so), "+s"(xp[0]), "+s"(xp[1]), "+s"(xp[2]), "+s"(sp));
  }
  DEVI void run(const f32x4 (&a)[5], bool fin) {
    u32x2 y[5];
    u32x4 v[3];
#pragma unroll
    for (int nt = 0; nt < 5; ++nt) y[nt] = u32x2{pack2(a[nt][0], a[nt][1]), pack2(a[nt][2], a[nt][3])};
#pragma unroll
    for (int i = 0; i < 3; ++i) {
      const auto s0 = __builtin_amdgcn_permlane16_swap(y[2 * i][0], y[i < 2 ? 2 * i + 1 : 4][0], false, false);
      const auto s1 = __builtin_amdgcn_permlane16_swap(y[2 * i][1], y[i < 2 ? 2 * i + 1 : 4][1], false, false);
      v[i] = u32x4{s0[0], s1[0], s0[1], s1[1]};
    }
    if (fin) {
#define OUTM 0
#define OUTST ""
      asm volatile("global_store_dwordx4 %0, %1, %4" OUTST "\n\tglobal_store_dwordx4 %0, %2, %5" OUTST "\n\ts_mov_b64 exec, 0xffff\n\tglobal_store_dwordx4 %0, %3, %6" OUTST "\n\t"
                   "s_mov_b64 exec, -1\n\ts_nop 0" ::"v"(xo), "v"(v[0]), "v"(v[1]), "v"(v[2]), "s"(xp[0]), "s"(xp[1]), "s"(xp[2])
                   : "memory");
      return;
    }
    asm volatile("global_store_dwordx4 %0, %1, %4 sc1\n\tglobal_store_dwordx4 %0, %2, %5 sc1\n\ts_mov_b64 exec, 0xffff\n\t"
                 "global_store_dwordx4 %0, %3, %6 sc1\n\ts_mov_b64 exec, -1\n\ts_nop 0" ::"v"(xo), "v"(v[0]), "v"(v[1]), "v"(v[2]),
                 "s"(xp[0]), "s"(xp[1]), "s"(xp[2])
                 : "memory");
    const u32x4 one4 = {vconst(0x3f803f80u), vconst(0x3f803f80u), vconst(0x3f803f80u), vconst(0x3f803f80u)};
    const bool lo = __builtin_amdgcn_inverse_ballot_w64(0xffffull);
    v[2] = u32x4{lo ? v[2][0] : 0u, lo ? v[2][1] : 0u, lo ? v[2][2] : 0u, lo ? v[2][3] : 0u};
    f32x4 s = {0.f, 0.f, 0.f, 0.f}, q = s;
#pragma unroll
    for (int i = 0; i < 3; ++i) {
      const s16x8 u = __builtin_bit_cast(s16x8, v[i]);
      q = __builtin_amdgcn_mfma_f32_16x16x32_bf16(u, u, q, 0, 0, 0);
      s = __builtin_amdgcn_mfma_f32_16x16x32_bf16(__builtin_bit_cast(s16x8, one4), u, s, 0, 0, 0);
    }
    float q0 = q[0], q1 = q[1], q2 = q[2], q3 = q[3];
    const bool b0 = __builtin_amdgcn_inverse_ballot_w64(0xaaaaaaaaaaaaaaaaull), b1 = __builtin_amdgcn_inverse_ballot_w64(0xccccccccccccccccull);
    q0 = b0 ? q1 : q0;
    q2 = b0 ? q3 : q2;
    asm("" : "+v"(q0), "+v"(q2));
    const f32x2 sq = {s[0], b1 ? q2 : q0};
    asm volatile("s_mov_b64 exec, %3\n\tglobal_store_dwordx2 %0, %1, %2 sc1\n\ts_mov_b64 exec, -1" ::"v"(so), "v"(sq), "s"(sp), "s"(0xf0000f0000f0000full) : "memory");
  }
};

using K3C = GemmCfg<128, 144>;
constexpr int K3_Z1_OFF = K3C::LDS_BYTES;
constexpr int K3_C2_OFF = K3_Z1_OFF + K3C::XB;
constexpr int K3_ZC_OFF = K3_C2_OFF + 2048;
constexpr int K3_DM_OFF = K3_ZC_OFF + 1024;
constexpr int K3_ST_OFF = K3_Z1_OFF + 4 * 5120;
constexpr int K3_C1_OFF = K3_ST_OFF + 2048;
constexpr int K3_BS_OFF = K3_C1_OFF + 576;
static_assert(K3_BS_OFF + 512 <= LW_OFF, "K3 LDS");
static_assert(P5_W + 19 * 1024 == K3C::SB && K3C::SB == 34 * 1024, "w_out ring");
constexpr int K3_D = 4;

DEVI void red4(float (&t)[4], float& s, float& q) {
#pragma unroll
  for (int j = 0; j < 2; ++j) {
    const auto p = __builtin_amdgcn_permlane16_swap(__float_as_uint(t[j]), __float_as_uint(t[2 + j]), false, false);
    const unsigned v = __float_as_uint(__uint_as_float(p[0]) + __uint_as_float(p[1]));
    const auto w = __builtin_amdgcn_permlane32_swap(v, v, false, false);
    t[j] = __uint_as_float(w[0]) + __uint_as_float(w[1]);
    asm volatile("" : "+v"(t[j]));
  }
  s = t[0];
  q = t[1];
}
DEVI void red8(const f32x4& a, const f32x4& b, float& s, float& q) {
  float t[4];
#pragma unroll
  for (int j = 0; j < 4; ++j) {
    t[j] = a[j] + b[j];
    asm volatile("" : "+v"(t[j]));
  }
  red4(t, s, q);
}
DEVI void red16(const f32x4& a, const f32x4& b, const f32x4& c, const f32x4& d, float& s, float& q) {
  float t[4];
#pragma unroll
  for (int j = 0; j < 4; ++j) {
    float u = a[j] + b[j], v = c[j] + d[j];
    asm volatile("" : "+v"(u), "+v"(v));
    t[j] = u + v;
    asm volatile("" : "+v"(t[j]));
  }
  red4(t, s, q);
}

constexpr int b_k(int s) { return s == 0 ? 0 : (s < 3 ? 6 + s : (s < 5 ? 2 + s : (s == 5 ? 4 : s - 5))); }
template <bool B>
struct UMath {
  static constexpr int D = 4, R = 6, NV = 162, SB = K3C::SB, P = K3C::P;
  s16x8 wf[R], xf[2][2];
  s16x8 (&xc)[4][2][2];
  f32x4 (&acc)[2][9];
  unsigned xa, wa;
  unsigned fa, fw, fv;
  DEVI UMath(f32x4 (&a)[2][9], s16x8 (&x)[4][2][2]) : xc(x), acc(a) {}
  static constexpr int wslot(int s) { return B ? (s + 1) % 4 : s % 4; }
  static constexpr int xslot(int s) { return B ? b_k(s) % 5 : s % 5; }
  static constexpr bool res(int q) { return !B || b_k(q / 2) >= 4; }
#define BPS 9
  static constexpr bool chk(int s) { return s >= 3 && s < 7 && s != BPS; }
  static constexpr bool xop(int v) { return v + D < NV && (v + D) % 9 < 2 && res((v + D) / 9); }
  static constexpr int ops1(int v) { return (xop(v) || (B && ((v % 18 == 9 && chk(v / 18)) || (v % 18 == 0 && v > 0)))) ? 1 : 0; }
  static constexpr int newer(int V) {
    int v0 = (V % 9 == 8 ? V : V + 1) - D, n = 0;
    if (v0 >= 0 && !(V % 9 == 0 && res(V / 9))) n += ops1(v0);
    for (int v = v0 + 1; v < V; ++v)
      if (v >= 0) n += (v + D < NV ? 1 : 0) + ops1(v);
    return n;
  }
  template <int V>
  DEVI void wread() {
    asm volatile("ds_read_b128 %0, %1 offset:%2" : "=v"(wf[V % R]) : "v"(wa), "n"((wslot(V / 18) & 1) * SB + ((V % 18) / 9) * 32 * P + (V % 9) * 32));
  }
  template <int Q, int M>
  DEVI void xread() {
    asm volatile("ds_read_b128 %0, %1 offset:%2" : "=v"(xf[Q & 1][M]) : "v"(xa), "n"((xslot(Q / 2) & 1) * SB + (Q & 1) * 8192 + M * (B ? 256 : 64)));
  }
  template <int V, int M_>
  DEVI void mf() {
    constexpr int M = ((V % 9) & 1) ? 1 - M_ : M_, q = V / 9;
    if constexpr (!B) acc[M][V % 9] = __builtin_amdgcn_mfma_f32_16x16x32_bf16(xf[q & 1][M], wf[V % R], acc[M][V % 9], 0, 0, 0);
    else if constexpr (res(q)) acc[M][V % 9] = __builtin_amdgcn_mfma_f32_16x16x32_bf16(wf[V % R], xf[q & 1][M], acc[M][V % 9], 0, 0, 0);
    else acc[M][V % 9] = __builtin_amdgcn_mfma_f32_16x16x32_bf16(wf[V % R], xc[b_k(q / 2)][q & 1][M], acc[M][V % 9], 0, 0, 0);
    __builtin_amdgcn_sched_barrier(0);
  }
  template <int V>
  DEVI void unit() {
    constexpr int U = V % 18, s = V / 18;
    static_assert(newer(V) <= 15, "lgkmcnt range");
    if constexpr ((V % 9) % 2 == 0 && V > 0) asm volatile("s_waitcnt lgkmcnt(%0)" ::"n"(newer(V)));
    __builtin_amdgcn_sched_barrier(0);
    mf<V, 0>();
    if constexpr (V + D < NV) wread<V + D>();
    if constexpr (s < 8 && U == 13 && (wslot(s + 1) >> 1) != (wslot(s) >> 1))
      asm volatile("v_add_u32 %0, %1, %0" : "+v"(wa) : "n"(((wslot(s + 1) >> 1) - (wslot(s) >> 1)) * 2 * SB));
    __builtin_amdgcn_sched_barrier(0);
    mf<V, 1>();
    if constexpr (xop(V)) xread<(V + D) / 9, (V + D) % 9>();
    if constexpr (s < 8) {
      if constexpr (U == 11 && res(2 * s + 2) && (xslot(s + 1) >> 1) != (xslot(s) >> 1))
        asm volatile("v_add_u32 %0, %1, %0" : "+v"(xa) : "n"(((xslot(s + 1) >> 1) - (xslot(s) >> 1)) * 2 * SB));
      if constexpr (!B && U == 13) __builtin_amdgcn_s_barrier();
      if constexpr (B && U == 9 && chk(s)) asm volatile("ds_read_u8 %0, %1 offset:%2" : "+v"(fv) : "v"(fa), "n"(s == 6 ? 0 : 8));
      unsigned n = 1u << 18;
      if constexpr (B && U == 13 && chk(s)) {
        asm volatile(
            "v_cmp_gt_u32 vcc, %3, %0\n\ts_cbranch_vccnz .Lw%=\n\t.subsection 1\n.Lw%=:\n\tds_read_u8 %0, %2 offset:%4\n\ts_waitcnt lgkmcnt(0)\n\t"
            "v_cmp_gt_u32 vcc, %3, %0\n\t" SAFE_OUT(".Lr%=") "s_cbranch_vccnz .Lw%=\n\ts_branch .Lr%=\n\t.subsection 0\n.Lr%=:"
            : "+v"(fv), NOP_(n)
            : "v"(fa), "n"(s == 6 ? 4 : s + 2), "n"(s == 6 ? 0 : 8)
            : "vcc", "scc");
      }
      if constexpr (B && U == 13 && s == BPS) {
        asm volatile(
            ".La%=:\n\ts_sleep 1\n\tds_read_u8 %0, %2\n\ts_waitcnt lgkmcnt(0)\n\tv_cmp_gt_u32 vcc, 3, %0\n\t" SAFE_OUT(".Lr%=") "s_cbranch_vccnz .La%=\n"
            ".Lb%=:\n\tds_read_u8 %0, %2 offset:8\n\ts_waitcnt lgkmcnt(0)\n\tv_cmp_gt_u32 vcc, %3, %0\n\t" SAFE_OUT(".Lr%=") "s_cbranch_vccnz .Lb%=\n.Lr%=:"
            : "+v"(fv), NOP_(n)
            : "v"(fa), "n"(s + 2)
            : "vcc", "scc");
      }
    }
    if constexpr (B && U == 15) asm volatile("v_mov_b32 %0, %1" : "+v"(fv) : "n"(s + 1));
    if constexpr (B && ((U == 0 && s > 0) || V == NV - 1)) asm volatile("ds_write_b8 %0, %1" ::"v"(fw), "v"(fv));
    __builtin_amdgcn_sched_barrier(0);
  }
  template <int... V>
  DEVI void units(std::integer_sequence<int, V...>) {
    (unit<V>(), ...);
  }
  DEVI void first() {
    if constexpr (!B) {
      xread<0, 0>();
      xread<0, 1>();
    }
    wread<0>();
    wread<1>();
    wread<2>();
    wread<3>();
    asm volatile("s_waitcnt lgkmcnt(0)");
  }
  DEVI void run() {
    units(std::make_integer_sequence<int, NV>{});
#pragma unroll
    for (int m = 0; m < 2; ++m)
#pragma unroll
      for (int n = 0; n < 9; ++n) asm volatile("" : "+v"(acc[m][n]));
  }
};

#define PACE 0
#define G0P 2
#define ZSLEEP 6
DEVI void p3_g0(char* smem, char* __restrict__ ws, int layer, int mi, int ni, int wm, int lane MK_ARG) {
  constexpr int SB = K3C::SB;
  const unsigned lds0 = (unsigned)(size_t)smem;
  const int g = lane >> 4, r = lane & 15;
  const unsigned lw = lds0 + (unsigned)(LW_OFF + layer * 4), me = 1u << (8 * wm);
  f32x4 ac[2][9];
#pragma unroll
  for (int mt = 0; mt < 2; ++mt)
#pragma unroll
    for (int j = 0; j < 9; ++j) ac[mt][j] = f32x4{0.f, 0.f, 0.f, 0.f};
  unsigned ra = lds0 + (unsigned)(K3_ST_OFF + (wm * 32 + g * 8) * 4), ca = lds0 + (unsigned)(K3_C2_OFF + r * 8);
  asm volatile("" : "+v"(ra), "+v"(ca));
  {
    s16x8 xc[4][2][2];
    UMath<false> gm(ac, xc);
    gm.xa = lds0 + (unsigned)(g * 2048 + (wm * 32 + 8 * (r >> 2) + (r & 3)) * 16);
    gm.wa = lds0 + (unsigned)(K3C::XB + (g * 8 + (r & 7)) * K3C::P + (r >> 3) * 16);
    asm volatile("" : "+v"(gm.xa), "+v"(gm.wa));
    place<AL_A>();
    bar();
    gm.first();
    gm.run();
  }
  lds_inc(lw + LW_AD * 4, me);
  MK(11);
  f32x2 z[9][4];
  f32x2 sm[4], sq[4];
  {
    f32x4 rc[4][2];
    f32x2 cs[9];
#pragma unroll
    for (int i = 0; i < 8; ++i)
      asm volatile("ds_read_b128 %0, %1 offset:%2" : "=v"(rc[i / 2][i % 2]) : "v"(ra), "n"((i / 2) * 512 + (i % 2) * 16) : "memory");
#pragma unroll
    for (int j = 0; j < 9; ++j) asm volatile("ds_read_b64 %0, %1 offset:%2" : "=v"(cs[j]) : "v"(ca), "n"(j * 128) : "memory");
    asm volatile("s_waitcnt lgkmcnt(0)"
                 : "+v"(rc[0][0]), "+v"(rc[0][1]), "+v"(rc[1][0]), "+v"(rc[1][1]), "+v"(rc[2][0]), "+v"(rc[2][1]), "+v"(rc[3][0]), "+v"(rc[3][1]));
#pragma unroll
    for (int j = 0; j < 9; ++j) asm volatile("" : "+v"(cs[j]));
    auto RC = [&](int k, int i) { return f32x2{rc[k][i / 2][(i % 2) * 2], rc[k][i / 2][(i % 2) * 2 + 1]}; };
#pragma unroll
    for (int j = 0; j < 9; ++j) {
      f32x2 u[4], w[4];
#pragma unroll
      for (int i = 0; i < 4; ++i) u[i] = __builtin_elementwise_fma(RC(0, i), cs[j], f32x2{ac[i / 2][j][(i % 2) * 2], ac[i / 2][j][(i % 2) * 2 + 1]});
#pragma unroll
      for (int i = 0; i < 4; ++i) w[i] = u[i] * u[i];
#pragma unroll
      for (int i = 0; i < 4; ++i) w[i] = __builtin_elementwise_fma(w[i], RC(3, i), RC(2, i));
#pragma unroll
      for (int i = 0; i < 4; ++i) w[i] = u[i] * w[i];
      __builtin_amdgcn_sched_barrier(0);
#pragma unroll
      for (int i = 0; i < 4; ++i) w[i] = f32x2{__builtin_amdgcn_exp2f(w[i][0]), __builtin_amdgcn_exp2f(w[i][1])};
      __builtin_amdgcn_sched_barrier(0);
#pragma unroll
      for (int i = 0; i < 4; ++i) w[i] = __builtin_elementwise_fma(w[i], RC(1, i), RC(1, i));
      __builtin_amdgcn_sched_barrier(0);
#pragma unroll
      for (int i = 0; i < 4; ++i) w[i] = f32x2{__builtin_amdgcn_rcpf(w[i][0]), __builtin_amdgcn_rcpf(w[i][1])};
      __builtin_amdgcn_sched_barrier(0);
#pragma unroll
      for (int i = 0; i < 4; ++i) z[j][i] = u[i] * w[i];
#pragma unroll
      for (int i = 0; i < 4; ++i) {
        if (j == 1) sm[i] = z[0][i] + z[1][i];
        if (j > 1) sm[i] += z[j][i];
      }
#pragma unroll
      for (int i = 0; i < 4; ++i) sq[i] = j ? __builtin_elementwise_fma(z[j][i], z[j][i], sq[i]) : z[j][i] * z[j][i];
      asm volatile("" : "+v"(z[j][0]), "+v"(z[j][1]), "+v"(z[j][2]), "+v"(z[j][3]));
      __builtin_amdgcn_sched_barrier(0);
    }
  }
  f32x4 e;
  {
    float v[4][4], d[2][4], f[4];
#pragma unroll
    for (int i = 0; i < 4; ++i) {
      v[i][0] = sm[i][0];
      v[i][1] = sq[i][0];
      v[i][2] = sm[i][1];
      v[i][3] = sq[i][1];
    }
    asm volatile("s_nop 1" : "+v"(v[3][1]), "+v"(v[3][3]), "+v"(v[2][1]), "+v"(v[2][3]));
#pragma unroll
    for (int i = 0; i < 2; ++i)
#pragma unroll
      for (int c = 0; c < 4; ++c)
        asm volatile("v_add_f32_dpp %0, %1, %1 row_ror:8 row_mask:0xf bank_mask:0x3\n\tv_add_f32_dpp %0, %2, %2 row_ror:8 row_mask:0xf bank_mask:0xc"
                     : "=&v"(d[i][c])
                     : "v"(v[i][c]), "v"(v[i + 2][c]));
#pragma unroll
    for (int c = 0; c < 4; ++c)
      asm volatile("v_add_f32_dpp %0, %1, %1 row_shl:4 row_mask:0xf bank_mask:0x5\n\tv_add_f32_dpp %0, %2, %2 row_shr:4 row_mask:0xf bank_mask:0xa"
                   : "=&v"(f[c])
                   : "v"(d[0][c]), "v"(d[1][c]));
    asm volatile(
        "v_add_f32_dpp %0, %0, %0 quad_perm:[1,0,3,2] row_mask:0xf bank_mask:0xf\n\t"
        "v_add_f32_dpp %1, %1, %1 quad_perm:[1,0,3,2] row_mask:0xf bank_mask:0xf\n\t"
        "v_add_f32_dpp %2, %2, %2 quad_perm:[1,0,3,2] row_mask:0xf bank_mask:0xf\n\t"
        "v_add_f32_dpp %3, %3, %3 quad_perm:[1,0,3,2] row_mask:0xf bank_mask:0xf\n\t"
        "v_add_f32_dpp %0, %0, %0 quad_perm:[2,3,0,1] row_mask:0xf bank_mask:0xf\n\t"
        "v_add_f32_dpp %1, %1, %1 quad_perm:[2,3,0,1] row_mask:0xf bank_mask:0xf\n\t"
        "v_add_f32_dpp %2, %2, %2 quad_perm:[2,3,0,1] row_mask:0xf bank_mask:0xf\n\t"
        "v_add_f32_dpp %3, %3, %3 quad_perm:[2,3,0,1] row_mask:0xf bank_mask:0xf"
        : "+v"(f[0]), "+v"(f[1]), "+v"(f[2]), "+v"(f[3]));
    e = f32x4{f[0], __uint_as_float(__float_as_uint(f[1]) & 0x7fffffffu), f[2], __uint_as_float(__float_as_uint(f[3]) & 0x7fffffffu)};
  }
  const char* zp = uni_ptr(ws + OFF_ZP + (size_t)layer * SZ_ZP + (size_t)mi * 16384 + wm * 256);
  if ((lane & 3) == 0)
    asm volatile("global_store_dwordx4 %0, %1, %2 sc1\n\ts_nop 1" ::"v"((unsigned)(ni * 1024 + lane * 4)), "v"(e), "s"(zp) : "memory");
  lds_inc(lw + LW_AD * 4, me);
  __builtin_amdgcn_s_setprio(2);
  MK(12);
  const char* dst = uni_ptr(ws + OFF_Z2 + (size_t)layer * SZ_Z + (size_t)ni * S_ * 288 + (size_t)(mi * 4 + wm) * 9216 + 4096);
  const unsigned lane16 = (unsigned)lane * 16u;
  u32x4 wb[9];
  {
    static_assert(b_k(7) == 2 && b_k(8) == 3, "order");
    const char* wp = uni_ptr(ws + OFF_PI + (size_t)ni * (18 * K3C::WB) + 11 * K3C::WB + wm * 9216 + 4096);
#pragma unroll
    for (int j = 0; j < 8; ++j) asm volatile("global_load_dwordx4 %0, %1, %2 offset:%3 sc1" : "=v"(wb[j]) : "v"(lane16), "s"(wp), "n"(j * 1024 - 4096) : "memory");
    asm volatile("global_load_dwordx4 %0, %1, %2 sc1" : "=v"(wb[8]) : "v"(lane16 + 4096u), "s"(wp) : "memory");
  }
  u32x4 y[9];
  {
    const unsigned src = (unsigned)(g * 4096 + r * 16);
    unsigned zw = lds0 + (unsigned)(K3_ZC_OFF + (wm * 32 + 2 * r + (g & 1)) * 4), zr = lds0 + (unsigned)(K3_ZC_OFF + (wm * 32 + g * 8) * 4);
    asm volatile("" : "+v"(zw), "+v"(zr));
    f32x4 a, b, c, d;
    __builtin_amdgcn_s_sleep(ZSLEEP);
#pragma unroll 1
    for (unsigned spin = 0; spin < SPIN_MEM; ++spin) {
      asm volatile(
          "global_load_dwordx4 %0, %4, %5 sc1\n\t"
          "global_load_dwordx4 %1, %4, %5 offset:1024 sc1\n\t"
          "global_load_dwordx4 %2, %4, %5 offset:2048 sc1\n\t"
          "global_load_dwordx4 %3, %4, %5 offset:3072 sc1\n\t"
          "s_waitcnt vmcnt(0)"
          : "=&v"(a), "=&v"(b), "=&v"(c), "=&v"(d)
          : "v"(src), "s"(zp)
          : "memory");
      const unsigned bad = (__float_as_uint(a[1]) | __float_as_uint(a[3]) | __float_as_uint(b[1]) | __float_as_uint(b[3]) |
                            __float_as_uint(c[1]) | __float_as_uint(c[3]) | __float_as_uint(d[1]) | __float_as_uint(d[3])) >> 31;
      if (!__any(bad)) break;
    }
    MK(13);
    float s, q, zmb, zi;
    red16(a, b, c, d, s, q);
    ln_finish(s, q, 1.0f / (float)FH_, zmb, zi);
    f32x4 R[2], M[2];
    asm volatile(
        "ds_write2st64_b32 %4, %5, %6 offset1:2\n\t"
        "ds_read_b128 %0, %7\n\tds_read_b128 %2, %7 offset:512\n\tds_read_b128 %1, %7 offset:16\n\tds_read_b128 %3, %7 offset:528\n\t"
        "s_waitcnt lgkmcnt(0)"
        : "=&v"(R[0]), "=&v"(R[1]), "=&v"(M[0]), "=&v"(M[1])
        : "v"(zw), "v"(zi), "v"(-zmb * zi), "v"(zr)
        : "memory");
    MK(28);
#define NRM 0
    auto st = [&](int j) {
      if (j < 8) asm volatile("global_store_dwordx4 %0, %1, %2 offset:%3 sc1\n\ts_nop 1" ::"v"(lane16), "v"(y[j]), "s"(dst), "n"(j * 1024 - 4096) : "memory");
      else asm volatile("global_store_dwordx4 %0, %1, %2 sc1\n\ts_nop 1" ::"v"(lane16 + 4096u), "v"(y[j]), "s"(dst) : "memory");
    };
#pragma unroll
    for (int j = 0; j < 9; ++j) {
      if (NRM & 2) {
        f32x2 t[4];
#pragma unroll
        for (int k = 0; k < 4; ++k)
          t[k] = __builtin_elementwise_fma(z[j][k], f32x2{R[k / 2][(k % 2) * 2], R[k / 2][(k % 2) * 2 + 1]}, f32x2{M[k / 2][(k % 2) * 2], M[k / 2][(k % 2) * 2 + 1]});
        y[j] = u32x4{pack2v(t[0]), pack2v(t[1]), pack2v(t[2]), pack2v(t[3])};
      } else {
        float t[8];
#pragma unroll
        for (int k = 0; k < 8; ++k) {
          t[k] = __builtin_fmaf(z[j][k / 2][k % 2], R[k / 4][k % 4], M[k / 4][k % 4]);
          asm volatile("" : "+v"(t[k]));
        }
        y[j] = u32x4{pack2(t[0], t[1]), pack2(t[2], t[3]), pack2(t[4], t[5]), pack2(t[6], t[7])};
      }
      if (NRM & 1) asm volatile("" : "+v"(y[j]));
      else st(j);
      __builtin_amdgcn_sched_barrier(0);
    }
    MK(29);
    if (NRM & 1) {
#pragma unroll
      for (int j = 0; j < 9; ++j) st(j);
    }
  }
  lds_inc(lw + LW_AD * 4, me);
  MK(24);
  __builtin_amdgcn_s_setprio(G0P);
  lds_spin4(lw + LW_S * 4, 4u + (unsigned)(wm >> 1));
  {
    const unsigned fa = lds0 + (unsigned)((wm >> 1) * SB + K3C::XB + (wm & 1) * 9216) + lane16;
#pragma unroll
    for (int j = 0; j < 9; ++j) {
      asm volatile("ds_write_b128 %0, %1 offset:%2" ::"v"(fa), "v"(wb[j]), "n"(j * 1024) : "memory");
      if (PACE && j < 8) __builtin_amdgcn_s_sleep(PACE);
    }
    lds_inc(lw + LW_AD * 4, me);
  }
  announce(lw + LW_Z * 4, 4u, ws, CN_Z + layer, lane * 16 + ni, lane < 16);
  lds_inc(lw + LW_AD * 4, me);
  MK(27);
  f32x4 acc[2][9];
  {
    float bs[2];
    asm volatile("ds_read2_b32 %0, %1 offset1:16\n\ts_waitcnt lgkmcnt(0)"
                 : "=v"(*reinterpret_cast<f32x2*>(bs))
                 : "v"(lds0 + (unsigned)(K3_BS_OFF + (wm * 32 + r) * 4))
                 : "memory");
    lds_spin4(lw + LW_S * 4, 6u + (unsigned)(wm >> 1));
    const unsigned fa = lds0 + (unsigned)((2 + (wm >> 1)) * SB + K3C::XB + (wm & 1) * 9216) + lane16;
#pragma unroll
    for (int j = 0; j < 9; ++j) {
      asm volatile("ds_write_b128 %0, %1 offset:%2" ::"v"(fa), "v"(y[j]), "n"(j * 1024) : "memory");
      if (PACE && j < 8) __builtin_amdgcn_s_sleep(PACE);
    }
    lds_inc(lw + LW_AD * 4, me);
#pragma unroll
    for (int mt = 0; mt < 2; ++mt)
#pragma unroll
      for (int j = 0; j < 9; ++j) acc[mt][j] = f32x4{bs[mt], bs[mt], bs[mt], bs[mt]};
  }
  u32x4 z1[9];
  {
    GemmMath<K3C, 2, 9, K3_D, 6, false, 2> gm(acc);
    gm.setup(lds0, wm * 2, 0, lane, (unsigned)(2 * SB));
    MK(15);
    lds_spin4(lw + LW_AD * 4, 6u);
    lds_spin4(lw + LW_L * 4, 8u);
    MK(17);
    gm.first();
    __builtin_amdgcn_s_setprio(0);
    gm.template mid<1, false>();
    gm.template mid<11, true, AL_C>();
    const unsigned za = gm.wrd + (unsigned)(K3_Z1_OFF - K3C::XB + wm * 5120);
#pragma unroll
    for (int j = 0; j < 5; ++j) asm volatile("ds_read_b128 %0, %1 offset:%2" : "=v"(z1[j]) : "v"(za), "n"(j * 1024));
    gm.template mid<10, true, AL_C>();
#pragma unroll
    for (int j = 5; j < 9; ++j) asm volatile("ds_read_b128 %0, %1 offset:%2" : "=v"(z1[j]) : "v"(za), "n"((j - 5) * 1024));
    gm.template mid<9, true, AL_C>();
    gm.last();
  }
  MK(16);
  const char* hp = uni_ptr(ws + OFF_H + (size_t)layer * SZ_Z + (size_t)((mi * 2 + (wm >> 1)) * 288 + ni * 18) * 1024);
  const int l2 = lane_id();
  const unsigned ho = (unsigned)((l2 >> 5) * 1024 + ((wm & 1) * 32 + (l2 & 31)) * 16);
#pragma unroll
  for (int j = 0; j < 9; ++j) {
    asm volatile("" : "+v"(z1[j]));
    u32x2 h[2];
#pragma unroll
    for (int mt = 0; mt < 2; ++mt) {
      const f32x4 a = acc[mt][j];
      h[mt] = u32x2{pack2v(f32x2{bf_lo(z1[j][2 * mt]), bf_hi(z1[j][2 * mt])} * f32x2{a[0], a[1]}),
                    pack2v(f32x2{bf_lo(z1[j][2 * mt + 1]), bf_hi(z1[j][2 * mt + 1])} * f32x2{a[2], a[3]})};
    }
    const auto s0 = __builtin_amdgcn_permlane16_swap(h[0][0], h[1][0], false, false);
    const auto s1 = __builtin_amdgcn_permlane16_swap(h[0][1], h[1][1], false, false);
    const u32x4 v = {s0[0], s1[0], s0[1], s1[1]};
    asm volatile("global_store_dwordx4 %0, %1, %2 offset:%3 sc1\n\ts_nop 1" ::"v"(ho), "v"(v), "s"(hp + (j / 2) * 4096), "n"((j % 2) * 2048) : "memory");
    __builtin_amdgcn_sched_barrier(0);
  }
  MK(18);
  announce(lds0 + (unsigned)(LW_OFF + (LW_H + layer * 2 + (wm >> 1)) * 4), 2u, ws, ((HSPLIT && ni >= 8) ? CN_H2 : CN_H) + layer,
           (mi * 2 + (wm >> 1)) * 8 + l2, l2 < 8);
  MK(19);
}

DEVI void k3_const(char* smem, char* ws, const u16* b_sp, int layer, int bid, int mi, int ni, int wm) {
  const int lane = lane_id(), l = lane < 54 ? lane : 53, p = l / 18;
  const char* kp = uni_ptr(ws + OFF_CSK + bid * 2048 + wm * 512);
  const unsigned q16 = (unsigned)(l - p * 18) * 16u;
  unsigned bs;
  f32x4 t;
  asm volatile("global_load_ushort %0, %1, %2" : "=v"(bs) : "v"((unsigned)(lane & 31) * 2u), "s"(uni_ptr((const char*)(b_sp + mi * 128 + wm * 32))) : "memory");
  if (layer) {
    asm volatile("global_load_dwordx4 %0, %1, %2 sc1\n\ts_waitcnt vmcnt(0)" : "=&v"(t) : "v"(q16), "s"(kp) : "memory");
  } else {
    const char* cp = uni_ptr(ws + OFF_CSP + ni * 1152 + wm * 288);
    f32x4 a[6];
#pragma unroll
    for (int i = 0; i < 6; ++i)
      asm volatile("global_load_dwordx4 %0, %1, %2 sc1" : "=v"(a[i]) : "v"(q16 + (unsigned)(p * (6 * F_ * 4))), "s"(cp + i * (F_ * 4)) : "memory");
    asm volatile("s_waitcnt vmcnt(0)" : "+v"(a[0]), "+v"(a[1]), "+v"(a[2]), "+v"(a[3]), "+v"(a[4]), "+v"(a[5]));
    t = ((a[0] + a[1]) + (a[2] + a[3])) + (a[4] + a[5]);
#pragma unroll
    for (int j = 0; j < 4; ++j)
      t[j] += __int_as_float(__builtin_amdgcn_ds_bpermute((lane + 18) * 4, __float_as_int(t[j]))) +
              __int_as_float(__builtin_amdgcn_ds_bpermute((lane + 36) * 4, __float_as_int(t[j])));
    if (lane < 24) asm volatile("global_store_dwordx4 %0, %1, %2\n\ts_nop 1" ::"v"((unsigned)lane * 16u), "v"(t), "s"(kp) : "memory");
  }
  asm volatile("" : "+v"(bs));
  if (lane < 18) {
    if (wm < 2) {
      *reinterpret_cast<f32x4*>(smem + K3_C2_OFF + wm * 576 + lane * 32) = f32x4{t[0], t[0], t[1], t[1]};
      *reinterpret_cast<f32x4*>(smem + K3_C2_OFF + wm * 576 + lane * 32 + 16) = f32x4{t[2], t[2], t[3], t[3]};
    } else {
      *reinterpret_cast<f32x4*>(smem + K3_C1_OFF + (wm - 2) * 288 + lane * 16) = t;
    }
  }
  if (lane < 32) *reinterpret_cast<float*>(smem + K3_BS_OFF + (wm * 32 + lane) * 4) = bf_lo(bs);
}

DEVI void p3_g1(char* smem, char* __restrict__ ws, const char* code0, int bid, int layer, int mi, int wm, int lane MK_ARG) {
  const unsigned lds0 = (unsigned)(size_t)smem;
  const int g = lane >> 4, r = lane & 15;
  const unsigned lw = lds0 + (unsigned)(LW_OFF + layer * 4);
  const unsigned xrd = lds0 + (unsigned)(g * 2048 + (wm * 32 + r) * 16);
  s16x8 xc[4][2][2];
  {
    const char* xs = uni_ptr(ws + OFF_XS + (size_t)layer * 2 * SZ_XS + (size_t)(mi * 2 + (wm >> 1)) * 4096 + (wm & 1) * 256);
    f32x4 a, b;
    auto pick = [&](int t) {
      bar();
      asm volatile(
          "ds_read_b128 %0, %4\n\tds_read_b128 %1, %4 offset:256\n\tds_read_b128 %2, %4 offset:8192\n\tds_read_b128 %3, %4 offset:8448\n\t"
          "s_waitcnt lgkmcnt(0)"
          : "=&v"(xc[t][0][0]), "=&v"(xc[t][0][1]), "=&v"(xc[t][1][0]), "=&v"(xc[t][1][1])
          : "v"(xrd + (unsigned)(t * K3C::SB))
          : "memory");
    };
#define G1P 2
    __builtin_amdgcn_s_setprio(G1P);
    pick(0);
    pick(1);
    asm volatile("global_load_dwordx4 %0, %2, %3 sc1\n\tglobal_load_dwordx4 %1, %2, %3 offset:512 sc1"
                 : "=&v"(a), "=&v"(b)
                 : "v"((unsigned)(g * 1024 + r * 16)), "s"(xs)
                 : "memory");
    pick(2);
    pick(3);
    bar();
    asm volatile("s_waitcnt vmcnt(0)" : "+v"(a), "+v"(b) : : "memory");
    float s, q, mb, rinv;
    red8(a, b, s, q);
    bar();
    asm volatile("" : "+v"(s), "+v"(q));
    ln_finish(s, q, 1.0f / (float)D_, mb, rinv);
    asm volatile("" : "+v"(mb), "+v"(rinv));
    bar();
    asm volatile("" : "+v"(mb), "+v"(rinv));
    const float k0 = -2.0f * 0.7978845608028654f * 1.4426950408889634f, k1 = k0 * 0.044715f;
    float sd = __builtin_amdgcn_rcpf(rinv), c0 = k0 * rinv, c1 = k1 * rinv * rinv * rinv;
    asm volatile("" : "+v"(sd));
    asm volatile("" : "+v"(c0));
    asm volatile("" : "+v"(c1));
    const float e0 = lane < 32 ? -mb : c0, e1 = lane < 32 ? sd : c1;
    asm volatile("ds_write2st64_b32 %0, %1, %2 offset1:2" ::"v"(
                     lds0 + (unsigned)(K3_ST_OFF + (g >> 1) * 1024 + (wm * 32 + 2 * r + (g & 1)) * 4)),
                 "v"(e0), "v"(e1)
                 : "memory");
    bar();
    asm volatile("s_waitcnt lgkmcnt(0)" ::: "memory");
    bar();
    __builtin_amdgcn_s_setprio(0);
  }
  f32x4 ac[2][9];
#pragma unroll
  for (int mt = 0; mt < 2; ++mt)
#pragma unroll
    for (int j = 0; j < 9; ++j) ac[mt][j] = f32x4{0.f, 0.f, 0.f, 0.f};
  {
    UMath<true> gm(ac, xc);
    gm.xa = xrd;
    gm.wa = lds0 + (unsigned)(K3C::XB + (g * 8 + (r & 7)) * K3C::P + (r >> 3) * 16);
    gm.fa = lw + (unsigned)(LW_AD * 4 + (lane & 3));
    gm.fw = lw + (unsigned)(LW_S * 4 + wm);
    gm.fv = 0u;
    asm volatile("" : "+v"(gm.fa), "+v"(gm.fw), "+v"(gm.fv), "+v"(gm.xa), "+v"(gm.wa));
    lds_spin4<4>(lw + LW_L * 4, 4u);
    gm.first();
    place<AL_B>();
    lds_spin4(lw + LW_AD * 4, 2u);
#define BPR 1
    __builtin_amdgcn_s_setprio(BPR);
    gm.run();
    __builtin_amdgcn_s_setprio(0);
  }
  MK(14);
  f32x2 rc[4];
  f32x4 cs[9];
  const int l2 = lane_id();
  {
    const unsigned ra = lds0 + (unsigned)(K3_ST_OFF + (wm * 32 + (l2 & 15)) * 4), ca = lds0 + (unsigned)(K3_C1_OFF + (l2 & 48));
#pragma unroll
    for (int k = 0; k < 4; ++k) asm volatile("ds_read2_b32 %0, %1 offset1:16" : "=v"(rc[k]) : "v"(ra + (unsigned)(k * 512)) : "memory");
#pragma unroll
    for (int j = 0; j < 9; ++j) asm volatile("ds_read_b128 %0, %1 offset:%2" : "=v"(cs[j]) : "v"(ca), "n"(j * 64) : "memory");
    asm volatile("s_waitcnt lgkmcnt(0)" : "+v"(rc[0]), "+v"(rc[1]), "+v"(rc[2]), "+v"(rc[3]));
#pragma unroll
    for (int j = 0; j < 9; ++j) asm volatile("" : "+v"(cs[j]));
  }
  const unsigned za = lds0 + (unsigned)(K3_Z1_OFF + wm * 5120 + l2 * 16);
  u32x4 z1[9];
#pragma unroll
  for (int t = 0; t < 18; ++t) {
    const int j = t / 2, mt = t % 2;
    float u[4], w[4];
#pragma unroll
    for (int i = 0; i < 4; ++i) asm volatile("v_fma_f32 %0, %1, %2, %3" : "=v"(u[i]) : "v"(rc[0][mt]), "v"(cs[j][i]), "v"(ac[mt][j][i]));
#pragma unroll
    for (int i = 0; i < 4; ++i) asm volatile("v_mul_f32 %0, %1, %1" : "=v"(w[i]) : "v"(u[i]));
#pragma unroll
    for (int i = 0; i < 4; ++i) asm volatile("v_fma_f32 %0, %0, %1, %2" : "+v"(w[i]) : "v"(rc[3][mt]), "v"(rc[2][mt]));
#pragma unroll
    for (int i = 0; i < 4; ++i) asm volatile("v_mul_f32 %0, %0, %1" : "+v"(w[i]) : "v"(u[i]));
#pragma unroll
    for (int i = 0; i < 4; ++i) asm volatile("v_exp_f32 %0, %0" : "+v"(w[i]));
#pragma unroll
    for (int i = 0; i < 4; ++i) asm volatile("v_fma_f32 %0, %0, %1, %1" : "+v"(w[i]) : "v"(rc[1][mt]));
#pragma unroll
    for (int i = 0; i < 4; ++i) asm volatile("v_rcp_f32 %0, %0" : "+v"(w[i]));
#pragma unroll
    for (int i = 0; i < 4; ++i) asm volatile("v_mul_f32 %0, %0, %1" : "+v"(w[i]) : "v"(u[i]));
    z1[j][2 * mt] = pack2(w[0], w[1]);
    z1[j][2 * mt + 1] = pack2(w[2], w[3]);
    if (mt && j < 5) asm volatile("ds_write_b128 %0, %1 offset:%2" ::"v"(za), "v"(z1[j]), "n"(j * 1024) : "memory");
    if (t == 17) {
#pragma unroll
      for (int k = 5; k < 9; ++k) asm volatile("ds_write_b128 %0, %1 offset:%2" ::"v"(za), "v"(z1[k]), "n"((k - 5) * 1024) : "memory");
    }
    if (t == 9 || t == 17) asm volatile("s_waitcnt lgkmcnt(0)" ::: "memory");
    __builtin_amdgcn_sched_barrier(0);
    bar();
    __builtin_amdgcn_sched_barrier(0);
  }
  MK(15);
#pragma unroll 1
  for (int t = 20; t < 32; ++t) bar();
  if ((RTC & 2) && wm == 2) code_again(code0, bid);
}

constexpr int W5A = 22, W5B = 26;
constexpr int NPF = 30;
struct Loader {
  u32x4 pf[NPF];
  unsigned lds0, lane16;
  int wl, lane;

  template <int R0, int N, int P0, int PE>
  DEVI void pf_load(const char* src) {
    const unsigned o = lane16 + (unsigned)(wl * 1024);
#pragma unroll
    for (int t = 0; t < N; ++t) {
      unsigned a = o + (unsigned)((P0 + 4 * t) * 1024);
      if (P0 + 4 * t + 3 >= PE && P0 + 4 * t + wl >= PE) a = lane16;
      asm volatile("global_load_dwordx4 %0, %1, %2 sc1" : "=v"(pf[R0 + t]) : "v"(a), "s"(src) : "memory");
    }
  }
  template <int R0, int N>
  DEVI void pf_pin() {
#pragma unroll
    for (int t = 0; t < N; ++t) asm volatile("" : "+v"(pf[R0 + t]));
  }
  template <int R0, int N>
  DEVI void pf_undef() {
#pragma unroll
    for (int t = 0; t < N; ++t) asm volatile("" : "=v"(pf[R0 + t]));
  }
  template <int PB, int A, int N, int SL = 0>
  DEVI void pf_put(unsigned dst) {
    constexpr int T0 = (A - PB) / 4, T1 = (A + N - 1 - PB) / 4;
    const unsigned a = lds0 + dst + lane16 + (unsigned)((wl + PB + 4 * T0 - A + 4) * 1024) - 4096u;
#pragma unroll
    for (int t = T0; t <= T1; ++t) {
      const int p0 = PB + 4 * t;
      if ((p0 >= A || p0 + wl >= A) && (p0 + 3 < A + N || p0 + wl < A + N))
        asm volatile("ds_write_b128 %0, %1 offset:%2" ::"v"(a), "v"(pf[t]), "n"((t - T0) * 4096) : "memory");
      if (SL) __builtin_amdgcn_s_sleep(SL);
    }
  }
  template <int N>
  DEVI void dma_run(const char* src, unsigned dst) {
    const unsigned o = lane16 + (unsigned)(wl * 1024), m = lds0 + dst + (unsigned)(wl * 1024);
#pragma unroll
    for (int i = 0; i < (N + 3) / 4; ++i)
      if (4 * i + 3 < N || 4 * i + wl < N) dma16_sc1(src + i * 4096, o, m + (unsigned)(i * 4096));
  }
  template <int V>
  DEVI void wait_w() {
    asm volatile("s_cmp_lt_u32 %0, 2\n\ts_cbranch_scc1 1f\n\ts_waitcnt vmcnt(%1)\n1:\n\ts_waitcnt vmcnt(%2)" ::"s"(wl), "n"(V - 1), "n"(V) : "memory", "scc");
  }
  template <int V>
  DEVI void wbar() {
    asm volatile("s_waitcnt vmcnt(%0) lgkmcnt(0)" ::"n"(V) : "memory");
    bar();
  }

  DEVI void p1(char* smem, char* ws, int bid, int rt, int hp MK_ARG) {
    constexpr int SB = K1C::SB;
    static_assert(K1C::XB == 18 * 1024 && K1C::WB == 14 * 1024, "K1 shape");
    const char* wp = uni_ptr(ws + OFF_PQ + (size_t)hp * (9 * K1C::WB) + 4 * K1C::WB);
    static_assert(3 * SB + K1C::XB >= P5_W + 5 * 18432, "free while the GEMM before ends");
    const char* wq = wp - 4 * K1C::WB;
    dma_run<14>(wq + 3 * K1C::WB, (unsigned)(3 * SB + K1C::XB));
    dma_run<28>(wp, (unsigned)K1_WX);
    if (CSL && wl == 3) dma16_sc1(uni_ptr(ws + OFF_CSQ + bid * 512), lane16 & 511u, lds0 + (unsigned)K1_CS_OFF);
    if (P1PR & 1) __builtin_amdgcn_s_setprio(0);
    if (P1PR & 2) wait_vm<0>();
    const bool lo = wl < 2;
    const int x0 = lo ? wl * 5 : wl * 4 + 2, w0 = lo ? wl * 3 : wl * 4 - 2;
    unsigned xo[5];
#pragma unroll
    for (int i = 0; i < 5; ++i) {
      const int c = (x0 + i) * 64 + lane;
      const int ch = c / 144;
      int gr = rt * 128 - 16 + (c - ch * 144);
      if (gr < 0) gr = 0;
      xo[i] = (unsigned)(((gr >> 6) * 72 + ch) * 1024 + (gr & 63) * 16);
      asm volatile("" : "+v"(xo[i]));
    }
    const char* xp = uni_ptr(ws + OFF_X1 + SZ_X);
    wp += 2 * K1C::WB + w0 * 1024;
    auto dma_x = [&](unsigned so) {
      const unsigned m = lds0 + so + (unsigned)(x0 * 1024);
#pragma unroll
      for (int i = 0; i < 4; ++i) dma16_sc1(xp, xo[i], m + (unsigned)(i * 1024));
      if (lo) dma16_sc1(xp, xo[4], m + 4096u);
      xp += 8192;
    };
    auto dma_w = [&](unsigned so) {
      const unsigned m = lds0 + so + (unsigned)(K1C::XB + w0 * 1024);
#pragma unroll
      for (int i = 0; i < 3; ++i) dma16_sc1(wp + i * 1024, lane16, m + (unsigned)(i * 1024));
      if (!lo) dma16_sc1(wp + 3072, lane16, m + 3072u);
      wp += K1C::WB;
    };
    const char* xs = uni_ptr(ws + OFF_XS + SZ_XS);
    const int g = lane >> 4, p2 = 64 + (lane & 7);
    int ra = rt * 128 - 16 + wl * 32 + 2 * (lane & 15);
    if (ra < 0) ra = 0;
    unsigned sa = (unsigned)(((ra >> 6) * 8 + g * 2) * 512 + (ra & 63) * 8);
    unsigned sb = (unsigned)(((rt * 2 + 1) * 8 + (lane >> 3)) * 512 + (2 * p2 - 80) * 8);
    unsigned sw = lds0 + (unsigned)(K1_ST_OFF + (2 * (lane < 32 ? wl * 16 + (lane & 15) : p2) + (g & 1)) * 8);
    asm volatile("" : "+v"(sa), "+v"(sb), "+v"(sw));
    MK(22);
    __builtin_amdgcn_s_setprio(3);
    bar();
    dma_run<14>(wq, (unsigned)K1C::XB);
    dma_run<14>(wq + K1C::WB, (unsigned)(SB + K1C::XB));
    dma_run<14>(wq + 2 * K1C::WB, (unsigned)(2 * SB + K1C::XB));
    MK(1);
    bar();
    dma_x(0u);
    dma_x((unsigned)SB);
    wbar<4>();
    MK(29);
    dma_x((unsigned)(2 * SB));
    dma_x((unsigned)(3 * SB));
    wbar<8>();
    MX(1, 3);
    dma_x(0u);
    wbar<8>();
    MX(1, 4);
    dma_x((unsigned)SB);
    f32x4 a, b, e;
    asm volatile("global_load_dwordx4 %0, %3, %5 sc1\n\tglobal_load_dwordx4 %1, %3, %5 offset:512 sc1\n\tglobal_load_dwordx4 %2, %4, %5 sc1"
                 : "=&v"(a), "=&v"(b), "=&v"(e)
                 : "v"(sa), "v"(sb), "s"(xs)
                 : "memory");
    wbar<11>();
    MX(1, 5);
    dma_w((unsigned)SB);
    dma_x((unsigned)(2 * SB));
    wbar<15>();
    MX(1, 6);
    dma_w((unsigned)(2 * SB));
    dma_x((unsigned)(3 * SB));
    wbar<19>();
    MX(1, 7);
    dma_w((unsigned)(3 * SB));
    dma_x(0u);
    wbar<16>();
    MX(1, 8);
    asm volatile("" : "+v"(a), "+v"(b), "+v"(e));
    float s, q;
    {
      float t[4] = {e[0], e[1], e[2], e[3]}, s2, q2;
      asm volatile(
          "v_add_f32_dpp %0, %0, %0 row_ror:8 row_mask:0xf bank_mask:0xf\n\tv_add_f32_dpp %1, %1, %1 row_ror:8 row_mask:0xf bank_mask:0xf\n\t"
          "v_add_f32_dpp %2, %2, %2 row_ror:8 row_mask:0xf bank_mask:0xf\n\tv_add_f32_dpp %3, %3, %3 row_ror:8 row_mask:0xf bank_mask:0xf\n\ts_nop 1"
          : "+v"(t[0]), "+v"(t[1]), "+v"(t[2]), "+v"(t[3]));
      red4(t, s2, q2);
      red8(a, b, s, q);
      s = lane < 32 ? s : s2;
      q = lane < 32 ? q : q2;
      asm volatile("" : "+v"(s), "+v"(q));
    }
    wbar<8>();
    MX(1, 9);
    {
      asm volatile("" : "+v"(s), "+v"(q));
      float mb, rinv;
      ln_finish(s, q, 1.0f / (float)D_, mb, rinv);
      asm volatile("ds_write_b64 %0, %1" ::"v"(sw), "v"(f32x2{-mb, rinv}) : "memory");
    }
    wbar<0>();
    MK(30);
  }

  template <int I>
  DEVI void p5_step(const char* wp) {
    constexpr int T = 2 * I + 1;
    wbar<(T == 1 ? 63 : (T <= 11 ? 3 : 0))>();
    if constexpr (T <= 9) {
      auto dm = [&]() {
      if (!G1W) {
        const unsigned wo = (unsigned)(12288 + (wl & 1) * 3072), so = (unsigned)(P5_W + ((T + 2 + (wl >> 1)) & 7) * 18432);
#pragma unroll
        for (int k = 0; k < 3; ++k) dma16_sc1(wp + (T + 7) * 18432 + k * 1024, lane16 + wo + (unsigned)((wl >> 1) * 18432), lds0 + so + wo + (unsigned)(k * 1024));
      }
      };
      if (!P5ORD) dm();
      pf_put<0, 12 * (T - 1), 12, P5PACE>((unsigned)(P5_W + ((T + 2) % 8) * 18432));
      pf_put<0, 12 * T, 12, P5PACE>((unsigned)(P5_W + ((T + 3) % 8) * 18432));
      if (P5ORD == 2) asm volatile("s_waitcnt lgkmcnt(0)" ::: "memory");
      if (P5ORD) dm();
    }
  }
  template <int... S>
  DEVI void p5_steps(const char* wp, std::integer_sequence<int, S...>) {
    (p5_step<S>(wp), ...);
  }

  template <int T0, int T1>
  DEVI void w5_load(const char* w5p) {
    const unsigned o = lane16 + (unsigned)(wl * 1024);
#pragma unroll
    for (int t = T0; t < T1; ++t)
      asm volatile("global_load_dwordx4 %0, %1, %2 sc1" : "=v"(pf[t]) : "v"(o + (unsigned)((t / 3 + 8) * 18432 + (t % 3) * 4096)), "s"(w5p) : "memory");
  }
  DEVI void p35(char* smem, char* ws, const u16* w_sp, int layer, int bid, int mi, int ni, int rb, int ct, int hp MK_ARG) {
    using C = K3C;
    constexpr int NS = C::NS, SB = C::SB, NDX = 4, NDW = 5, VPS = NDX + NDW;
    const char* wip = uni_ptr(ws + OFF_PI + (size_t)ni * (18 * C::WB));
    if (layer || L0REG) {
      pf_put<-48, 36, 18>((unsigned)(2 * SB + C::XB));
      pf_put<-48, 54, 18>((unsigned)(3 * SB + C::XB));
    } else {
      dma_run<18>(wip + 2 * C::WB, (unsigned)(2 * SB + C::XB));
      dma_run<18>(wip + 3 * C::WB, (unsigned)(3 * SB + C::XB));
    }
    const char* w5p = uni_ptr(ws + OFF_PW + (size_t)ct * (36 * 9216));
    const int w0 = wl < 2 ? wl * 5120 : wl * 4096 + 2048;
    const unsigned m0x = lds0 + (unsigned)(wl * 4096), m0w = lds0 + (unsigned)(C::XB + w0);
    const unsigned wo = lane16 + (unsigned)w0;
    const unsigned lw = lds0 + (unsigned)(LW_OFF + layer * 4), me = 1u << (8 * wl);
    auto dma_w = [&](const char* wp, unsigned so) {
#pragma unroll
      for (int i = 0; i < NDW; ++i) {
        if (i < NDW - 1 || wl < 2) dma16_sc1(wp, wo + (unsigned)(i * 1024), m0w + so + (unsigned)(i * 1024));
      }
    };
    {
      const char* xp = uni_ptr(ws + OFF_X1 + (size_t)layer * 2 * SZ_X + (size_t)(mi * 2 * 72 + wl * 2) * 1024);
      const unsigned xo1 = lane16 + 72u * 1024u;
      auto dma_x = [&](int k, unsigned so) {
        const char* p = xp + k * 8192;
        dma16_sc1(p, lane16, m0x + so);
        dma16_sc1(p, xo1, m0x + so + 1024u);
        dma16_sc1(p + 1024, lane16, m0x + so + 2048u);
        dma16_sc1(p + 1024, xo1, m0x + so + 3072u);
      };
      if (!MPOLL && wl == 0) cnt_wait(ws, CN_X1 + layer, mi * 16 + ni, 16u);
      MK(10);
      bar();
      dma_x(0, 0u);
      dma_x(1, (unsigned)SB);
      wait_vm<4>(); MX(3, 2); wbar<4>(); MX(2, 2);
      MK(25);
      dma_x(2, (unsigned)(2 * SB));
      dma_x(3, (unsigned)(3 * SB));
      dma_x(4, (unsigned)(4 * SB));
      wbar<12>(); MX(2, 3);
      dma_x(5, 0u);
      wait_vm<12>();
      bar(); MX(2, 4);
      dma_w(wip + 4 * C::WB, 0u);
      dma_x(6, (unsigned)SB);
      wait_w<8 + VPS>();
#pragma unroll 1
      for (int t = 1; t < 3; ++t) {
        bar(); MX(2, 4 + t);
        dma_w(wip + (t + 4) * C::WB, (unsigned)(t * SB));
        dma_x(t + 6, (unsigned)((t + 1) * SB));
        wait_w<NDX + VPS>();
      }
      bar(); MX(2, 7);
      dma_w(wip + 7 * C::WB, (unsigned)(3 * SB));
      wait_w<VPS>();
      bar(); MX(2, 8);
      dma_w(wip + 8 * C::WB, 0u);
      wait_w<NDW>();
      bar(); MX(2, 9);
      const char* wp = wip + 9 * C::WB;
      dma_w(wp + b_k(0) * C::WB, (unsigned)SB);
      if (layer == 0) {
        const char* sp = uni_ptr((const char*)w_sp + (size_t)(bid * 4 + wl) * 8192);
        const unsigned so = (unsigned)((lane >> 5) * 4096 + (lane & 31) * 16);
#pragma unroll
        for (int i = 0; i < WSP_V; ++i) dma16(sp + i * 512, so, lds0 + (unsigned)K3_DM_OFF);
        dma16(sp + (bid >> 4) * 256, so & ~0x100u, lds0 + (unsigned)K3_DM_OFF);
      }
      w5_load<0, 4>(w5p);
      wait_w<NDW + 4>();
      MX(3, 10); bar(); MX(2, 10);
      dma_w(wp + b_k(1) * C::WB, (unsigned)(2 * SB));
      w5_load<4, 8>(w5p);
      MK(26);
      lds_spin4(lw + LW_AD * 4, 1u);
      dma_w(wp + b_k(2) * C::WB, (unsigned)(3 * SB));
      dma_w(wp + b_k(3) * C::WB, 0u);
#define TCH 0
      if (TCH & 1) {
        const int li = wl * 54 + (lane < 54 ? lane : 53);
        touch64(wp + C::WB, (unsigned)((mi & 7) * 13824 + li * 64), lds0 + (unsigned)K3_DM_OFF);
      }
      w5_load<8, W5A>(w5p);
      wait_vm<W5A - 8>();
      lds_inc(lw + LW_L * 4, 4u * me);
      w5_load<W5A, 30>(w5p);
      MK(11);
#define LSL 2
      static_for<4, 7>([&](auto T_) {
        constexpr int t = T_;
        lds_spin4<LSL>(lw + LW_S * 4, (unsigned)(t - 3));
        MX(2, 12 + t);
        dma_w(wp + b_k(t) * C::WB, (unsigned)(((t + 1) % 4) * SB));
        if constexpr (t > 4) {
          wait_w<NDW>();
          lds_inc(lw + LW_L * 4, me);
        }
      });
      MX(2, 19);
      wait_vm<0>();
      lds_inc(lw + LW_L * 4, me);
      pf_pin<0, 30>();
      MK(13);
    }
    {
      unsigned xoff[NDX];
#pragma unroll
      for (int i = 0; i < NDX; ++i) {
        const int c = wl * 256 + i * 64 + lane;
        const int row = c >> 3, cc = c & 7;
        xoff[i] = (unsigned)((mi * 128 + row) * S_ * 2) + (unsigned)((cc ^ ((row >> 1) & 7)) * 16);
      }
      auto dma_x = [&](const char* xp, unsigned so) {
#pragma unroll
        for (int i = 0; i < NDX; ++i) dma16(xp, xoff[i], m0x + so + (unsigned)(i * 1024));
      };
      const char* xp = uni_ptr((const char*)w_sp);
      const char* wp = uni_ptr(ws + OFF_Z2 + (size_t)layer * SZ_Z + (size_t)ni * S_ * 288);
#define XEARLY 0
      if (!XEARLY) lds_spin4<LSL>(lw + LW_AD * 4, 5u);
      dma_x(xp + mi * 256, (unsigned)(2 * SB));
      dma_x(xp + mi * 256 + 128, (unsigned)(3 * SB));
      if (XEARLY) lds_spin4<LSL>(lw + LW_AD * 4, 5u);
      MK(24);
      int left = 2 * mi;
      auto next = [&]() {
        const int d = --left == 0 ? 3 : 1;
        xp += d * 128;
        wp += d * C::WB;
      };
      if (mi == 0) {
        xp += 256;
        wp += 2 * C::WB;
      }
      dma_x(xp, 0u);
      dma_x(xp + 128, (unsigned)SB);
      wait_vm<2 * NDX>();
      lds_inc(lw + LW_L * 4, me);
      MK(27);
      if (wl == 0) {
        cnt_wait(ws, CN_Z + layer, mi * 16 + ni, 16u);
        lds_wr32(lw + LW_GO * 4, 1u);
      } else {
        lds_spin(lw + LW_GO * 4, 1u);
      }
      MK(14);
      lds_spin4(lw + LW_S * 4, 8u);
      dma_w(wp, 0u);
      lds_spin4(lw + LW_S * 4, 9u);
      dma_w(wp + C::WB, (unsigned)SB);
      next();
      next();
      unsigned slot = (unsigned)(2 * SB);
      auto cissue = [&]() {
        dma_x(xp, slot);
        dma_w(wp, slot);
        next();
        slot = (slot == (unsigned)((NS - 1) * SB)) ? 0u : slot + (unsigned)SB;
      };
      auto cstep = [&]() {
        bar();
        cissue();
      };
      wait_w<NDW>();
      MK(15);
      int j = 2;
      if (layer == 0) {
        const char* sp = uni_ptr((const char*)w_sp + (size_t)(bid * 4 + wl) * 8192 + WSP_V * 512);
        unsigned d = 0u;
#pragma unroll 1
        for (; j < 2 + 32 - 4 * WSP_V; ++j) {
          cstep();
          asm volatile("s_load_dword %0, %1, 0\n\ts_load_dword %0, %1, 64\n\ts_load_dword %0, %1, 0x1000\n\ts_load_dword %0, %1, 0x1040"
                       : "+s"(d)
                       : "s"(sp));
          sp += 128;
          wait_w<VPS>();
        }
#pragma unroll 1
        for (int e = j + 4; j < e; ++j) {
          cstep();
          asm volatile("" : "+s"(d));
          wait_w<VPS>();
        }
        asm volatile("s_waitcnt lgkmcnt(0)" : "+s"(d));
      }
      place<AL_L>();
#pragma unroll 1
      for (; j <= 29; ++j) {
        cstep();
        wait_w<VPS>();
      }
      bar();
    }
    auto w5piece = [&](unsigned off) {
      dma16_sc1(w5p, lane16 + (off >= 54u * 1024u ? off - 54u * 1024u : off + 90u * 1024u), lds0 + (unsigned)P5_W + off);
    };
#pragma unroll
    for (int i = 0; i < 15; ++i) {
      const int p = 4 * i + wl;
      if (i < 14 || wl == 0) w5piece((unsigned)((p < 34 ? 53 + p : 121 - 34 + p) * 1024));
    }
    wait_vm<14>();
    bar();
#pragma unroll
    for (int i = 0; i < 9; ++i)
      if (i < 8 || wl < 2) w5piece((unsigned)((87 + 4 * i + wl) * 1024));
    wait_vm<0>();
    bar();
    if (wl) {
#pragma unroll
      for (int i = 0; i < 18; ++i)
        if (i < 17 || wl < 3) w5piece((unsigned)((3 * i + wl - 1) * 1024));
    } else {
      cnt_wait(ws, CN_H + layer, rb * 8 + ct, HSPLIT ? 8u : 16u);
    }
    MK(20);
    bar();
    p5_steps(w5p, std::make_integer_sequence<int, 7>{});
    if (layer == 0) {
      if (bid < 24 && wl == 0 && lane < 8) pk_st16(ws + OFF_HELLO + bid * 128 + lane16, u32x4{0u, 0u, 0u, 0u});
    }
  }
};

__global__ __launch_bounds__(768, 3) void k_main(const u16* __restrict__ x0, const u16* __restrict__ wqkv, const u16* __restrict__ wo,
                                                 const u16* __restrict__ w_in, const u16* __restrict__ w_sp,
                                                 const u16* __restrict__ b_sp, const u16* __restrict__ w_out,
                                                 u16* __restrict__ out, char* ws) {
  extern __shared__ __attribute__((aligned(16))) char smem[];
  const char* code0 = code_here();
  (void)code0;
  const unsigned lds0 = (unsigned)(size_t)smem;
  int wave = __builtin_amdgcn_readfirstlane((int)threadIdx.x >> 6);
  int bid = blockIdx.x;
#define FRESH() asm volatile("" : "+s"(bid), "+s"(ws), "+s"(wave))
#define XCD (bid & 7)
#define IDX (bid >> 3)
#define MI ((XCD >> 2) * 8 + (IDX >> 2))
#define NI ((XCD & 1) * 8 + (XCD & 2) * 2 + (IDX & 3))
#define RB (XCD * 4 + (IDX >> 3))
#define CT (IDX & 7)
#define P1MAP 0
#define RT ((XCD >> 1) * 4 + (IDX >> 3))
#define HP ((XCD & 1) * 8 + (IDX & 7))
  MK(0);
  if (CT0 && wave == 2) {
    constexpr int PER = (2 * NL_MAIN + 31) / 32;
    const int lane = lane_id();
    int li = (CT0 == 2 ? lane * 32 + IDX : IDX * PER + (lane < PER ? lane : PER - 1));
    if (li > 2 * NL_MAIN - 1) li = 2 * NL_MAIN - 1;
    touch64(code0, (unsigned)li * 64u, lds0 + (unsigned)(K1R_DUMMY + 768));
  }
  if (wave >= 3 && wave < 8) {
    constexpr int ST = 64;
    const int k = (7 - wave) * 64 + lane_id();
    const bool isx = k < 9216 / ST;
    int o = isx ? bid * 9216 + k * ST : bid * 7776 + (k - 9216 / ST) * ST;
    if (!isx && k - 9216 / ST > 7775 / ST) o = bid * 7776;
    const char* p = (isx ? (const char*)x0 : (const char*)wqkv) + o;
    asm volatile("s_mov_b32 m0, %1\n\ts_nop 0\n\tglobal_load_lds_dword %0, off" ::"v"(p), "s"(lds0 + (unsigned)K1R_DUMMY) : "memory");
  }

  if (__builtin_expect(wave >= 8, 1)) {
    __builtin_amdgcn_s_setprio(3);
    {
      const int lane = lane_id();
      RawLd rl;
      const bool cross = (((HP * 72) & ~15) & 127) > 48;
      const int c16 = IDX >> 1;
      const int job = cross ? (c16 < 11 ? 128 + XCD * 11 + c16 : 999) : XCD * 16 + c16;
      const int pj = cross ? (c16 >= 11 ? XCD * 5 + c16 - 11 : -1) : (XCD < 4 && (c16 & 7) == 0 ? 40 + XCD * 2 + (c16 >> 3) : -1);
      rl.front(x0, wqkv, wo, ws, bid, lds0, wave - 8, lane, RT, HP, pj MF_PASS);
      if (job < 216) k1_raw_loader<true>(rl, w_in, w_out, ws, rl.tag, bid, lds0, wave - 8, lane, RT, HP, CT, job, pj MF_PASS);
      else k1_raw_loader<false>(rl, w_in, w_out, ws, rl.tag, bid, lds0, wave - 8, lane, RT, HP, CT, job, pj MF_PASS);
    }
    Loader ld;
    ld.lds0 = lds0;
#pragma unroll 1
    for (int layer = 0; layer < 2; ++layer) {
      FRESH();
      ld.wl = wave - 8;
      ld.lane = lane_id();
      ld.lane16 = (unsigned)ld.lane * 16u;
      const char* wip = uni_ptr(ws + OFF_PI + (size_t)NI * (18 * K3C::WB));
      if (layer) {
        ld.p1(smem, ws, bid, RT, HP MK_PASS);
        ld.pf_load<0, 21, 0, 81>(uni_ptr(ws + OFF_PO + (size_t)CT * (18 * 4608)));
        ld.pf_load<21, 9, 36, 72>(wip);
        wait_vm<0>();
        ld.pf_pin<0, 30>();
        bar();
        ld.pf_put<0, 0, 40>((unsigned)P2_W);
        ld.pf_put<0, 40, 41>((unsigned)(P2_W + 40 * 1024));
      } else {
        ld.pf_undef<21, 9>();
        MK(3);
        MK(4);
        bar();
        const char* wp = uni_ptr(ws + OFF_PO + (size_t)CT * (18 * 4608) + ld.wl * 1024);
#pragma unroll
        for (int i = 0; i < 21; ++i)
          if (i < 20 || ld.wl == 0) dma16_sc1(wp + i * 4096, ld.lane16, lds0 + (unsigned)(P2_W + i * 4096 + ld.wl * 1024));
      }
      if (layer == 0 && ld.wl == 0) cnt_wait(ws, CN_O + layer, RB * 8 + CT, 16u);
      MK(6);
      asm volatile("s_waitcnt vmcnt(0) lgkmcnt(0)" ::: "memory");
      bar();
      lds_spin(lds0 + (unsigned)(LW_OFF + (LW_T + layer) * 4), 12u);
      if (layer == 0) {
        if (ld.wl == 0) {
          cnt_wait(ws, CN_PK, 8 + NI, 36u);
          lds_wr32(lds0 + (unsigned)(LW_OFF + LW_HELLO * 4), 2u);
        } else {
          lds_spin(lds0 + (unsigned)(LW_OFF + LW_HELLO * 4), 2u);
        }
        MK(7);
      }
      static_assert(2 * K3C::SB <= P2_W, "free during the first residual GEMM");
      ld.dma_run<18>(wip, (unsigned)K3C::XB);
      ld.dma_run<18>(wip + K3C::WB, (unsigned)(K3C::SB + K3C::XB));
      if (L0REG && layer == 0) ld.pf_load<21, 9, 36, 72>(wip);
      if (layer == 0 && ld.wl == 0) cnt_wait(ws, CN_PK, 24 + CT, 72u);
      wait_vm<0>();
      ld.pf_pin<21, 9>();
      bar();
      FRESH();
      ld.wl = wave - 8;
      ld.p35(smem, ws, w_sp, layer, bid, MI, NI, RB, CT, HP MK_PASS);
    }
  } else {
    MF(12);
    math_start(smem, code0, b_sp, wqkv, ws, bid, wave, lane_id());
    MF(13);
#pragma unroll 1
    for (int layer = 0; layer < 2; ++layer) {
      FRESH();
      {
        const int lane = lane_id(), r0 = RT * 128;
        float* csq = reinterpret_cast<float*>(ws + OFF_CSQ + bid * 512);
        Attn at;
        at.setup(lds0 + (unsigned)(layer ? K1_T_OFF : K1R_T_OFF), ws + OFF_O + (size_t)layer * SZ_X, r0, HP, wave, lane);
        if (layer) {
          if ((wave & 2) == 0) k1_gemm<2, 4>(smem, ws, csq, RT, HP, (wave & 1) * 2 + (wave & 4), 0, wave >= 4, lane MK_PASS);
          else if (wave < 4) k1_gemm<3, 3>(smem, ws, csq, RT, HP, (wave & 1) * 3, 4, false, lane MK_PASS);
          else if (wave == 6) k1_gemm<2, 3>(smem, ws, csq, RT, HP, 6, 4, true, lane MK_PASS);
          else k1_gemm<1, 7>(smem, ws, csq, RT, HP, 8, 0, true, lane MK_PASS);
        } else {
          if (wave == 0) k1_gemm_raw<3, false>(smem, HP, 0, lane MF_PASS);
          else if (wave < 4) k1_gemm_raw<2, false>(smem, HP, 1 + 2 * wave, lane MF_PASS);
          else if (wave < 7) k1_gemm_raw<2, true>(smem, HP, 2 * (wave - 4), lane MF_PASS);
          else k1_gemm_raw<3, true>(smem, HP, 6, lane MF_PASS);
        }
        msync(lds0 + (unsigned)(LW_OFF + (LW_T + layer) * 4));
        MK(3);
        at.run(r0, HP, wave, lane);
        if (layer == 0) {
          if (wave == 0 && lane < 32)
            asm volatile("global_store_dwordx4 %0, %1, %2 sc1\n\ts_nop 1" ::"v"((unsigned)lane * 16u),
                         "v"(*reinterpret_cast<const u32x4*>(smem + K1R_CS_OFF + lane * 16)), "s"(uni_ptr((const char*)csq))
                         : "memory");
          lds_spin(lds0 + (unsigned)(LW_OFF + LW_HELLO * 4), 1u);
        }
        MK(4);
        announce(lds0 + (unsigned)(LW_OFF + (LW_O + layer * 22 + (wave >> 2)) * 4), 4u, ws, CN_O + layer, (RT * 2 + (wave >> 2)) * 8 + lane,
                 lane < 8);
        MK(5);
        bar();
      }
      FRESH();
      {
        if (wave < 4) {
          const int lane = lane_id();
          u32x2 xv[5];
          res_ldx(xv, layer ? ws + OFF_X1 + SZ_X : (const char*)x0, layer == 0, RB, CT, wave, lane);
          ResOut ro;
          ro.setup(ws + OFF_X1 + (size_t)layer * 2 * SZ_X, ws + OFF_XS + (size_t)layer * 2 * SZ_XS, out, false, RB, CT, wave, lane);
          f32x4 acc[2][5];
          p2_gemm(acc, xv, lds0, uni_ptr(ws + OFF_O + (size_t)layer * SZ_X + (size_t)RB * (72 * 1024)), layer, wave, lane MK_PASS);
          MK(7);
          ro.run(acc[0], false);
          MK(8);
          announce(lds0 + (unsigned)(LW_OFF + (LW_X1 + layer) * 4), 4u, ws, CN_X1 + layer, (RB >> 1) * 16 + lane, lane < 16);
          MK(9);
          bar();
          FRESH();
          if (wave == 3) cnt_wait(ws, CN_X1 + layer, MI * 16 + NI, 16u);
          bar();
          MK(10);
          p3_g0(smem, ws, layer, MI, NI, wave, lane_id() MK_PASS);
        } else {
          if (layer && wave == 5) cnt_wait(ws, CN_O + layer, RB * 8 + CT, 16u);
          bar();
          bar();
          k3_const(smem, ws, b_sp, layer, bid, MI, NI, wave & 3);
          bar();
          MK(10);
          FRESH();
          p3_g1(smem, ws, code0, bid, layer, MI, wave & 3, lane_id() MK_PASS);
        }
        bar();
      }
      FRESH();
      {
        const int lane = lane_id();
        if (wave < 4) {
          u32x2 xv[5];
          res_ldx(xv, ws + OFF_X1 + (size_t)layer * 2 * SZ_X, false, RB, CT, wave, lane);
          f32x4 acc[2][5];
          ResOut ro;
          ro.setup(ws + OFF_X1 + SZ_X, ws + OFF_XS + SZ_XS, out, layer != 0, RB, CT, wave, lane);
          ResX<36, 33, 8, P5_W, P5_XW, P5_X0, 3, P5_BAR> gm(acc);
          gm.setup(lds0, uni_ptr(ws + OFF_H + (size_t)layer * SZ_Z + (size_t)RB * (288 * 1024)), wave, lane, (unsigned)P5_RED, layer + 1u);
          gm.prow();
          place<AL_P5>();
          bar();
          MK(20);
          gm.prox<0>(std::make_integer_sequence<int, P5_X0>{});
          gm.init<2 * P5_X0>(xv);
          __builtin_amdgcn_sched_barrier(0);
          gm.run();
          MK(21);
          ro.run(acc[0], layer != 0);
          MK(22);
          if (layer == 0) {
            announce(lds0 + (unsigned)(LW_OFF + LW_X2 * 4), 4u, ws, CN_X2, (RB >> 1) * 16 + lane,
                     lane < 16 || (lane < 32 && (RB & 1) && RB < 31));
            MK(23);
          }
        } else {
          u32x4 wq[15];
          const int wm = wave & 3;
          const unsigned o = (unsigned)((wm >> 1) * 18432 + 12288 + (wm & 1) * 3072 + lane * 16);
          if (G1W) {
            const char* wp = uni_ptr(ws + OFF_PW + (size_t)CT * (36 * 9216) + 8 * 18432);
#pragma unroll
            for (int i = 0; i < 15; ++i)
              asm volatile("global_load_dwordx4 %0, %1, %2 offset:%3 sc1" : "=v"(wq[i]) : "v"(o), "s"(wp + (i / 3) * 36864), "n"((i % 3) * 1024) : "memory");
          }
          bar();
#pragma unroll
          for (int i = 0; i < 7; ++i) {
            if (G1W && i == 0) asm volatile("s_waitcnt vmcnt(0)" ::: "memory");
            if (HSPLIT && i == HPOLL && wave == 5) {
              if (HPOLL < 2) {
                unsigned f;
                const char* cp = uni_ptr(ws + OFF_CNT + (size_t)((CN_H2 + layer) * 256 + RB * 8 + CT) * 128);
                asm volatile("global_load_dword %0, %1, %2 sc1" : "=v"(f) : "v"(vconst(0u)), "s"(cp) : "memory");
                bar();
                asm volatile("s_waitcnt vmcnt(0)" : "+v"(f) : : "memory");
                if (__builtin_amdgcn_readfirstlane(f) != 7u) cnt_wait(ws, CN_H2 + layer, RB * 8 + CT, 8u);
                continue;
              }
              cnt_wait(ws, CN_H2 + layer, RB * 8 + CT, 8u);
            }
            asm volatile("s_waitcnt lgkmcnt(0)" ::: "memory");
            bar();
            if (G1W && i < 5) {
              const unsigned a = lds0 + o + (unsigned)(P5_W + ((2 * i + 3) % 8) * 18432) - ((i == 2 && wm >= 2) ? 8u * 18432u : 0u);
#pragma unroll
              for (int k = 0; k < 3; ++k) {
                if (P5PACE) __builtin_amdgcn_s_sleep(2 * P5PACE);
                asm volatile("ds_write_b128 %0, %1 offset:%2" ::"v"(a), "v"(wq[3 * i + k]), "n"(k * 1024) : "memory");
              }
            }
          }
          MK(22);
        }
        if (layer == 0 && (!G1E || wave < 4)) bar();
      }
    }
  }
}

}

extern "C" void kernel_launch(void* const* d_in, const int* in_sizes, int n_in, void* d_out, int out_size,
                              void* d_ws, size_t ws_size, hipStream_t stream) {
  const u16* x0 = (const u16*)d_in[0];
  const u16* wqkv = (const u16*)d_in[1];
  const u16* wo = (const u16*)d_in[2];
  const u16* w_in = (const u16*)d_in[4];
  const u16* w_sp = (const u16*)d_in[5];
  const u16* b_sp = (const u16*)d_in[6];
  const u16* w_out = (const u16*)d_in[7];
  char* ws = (char*)d_ws;
  (void)hipFuncSetAttribute((const void*)k_main, hipFuncAttributeMaxDynamicSharedMemorySize, LDS_ALL);
  k_main<<<256, 768, LDS_ALL, stream>>>(x0, wqkv, wo, w_in, w_sp, b_sp, w_out, (u16*)d_out, ws);
}
```

```cpp
#include <hip/hip_runtime.h>
#include <hip/hip_bf16.h>
#include <stdint.h>

#define DEVI __device__ __forceinline__

typedef unsigned short u16;
typedef __attribute__((ext_vector_type(8))) short s16x8;
typedef __attribute__((ext_vector_type(4))) short s16x4;
typedef __attribute__((ext_vector_type(4))) float f32x4;
typedef __attribute__((ext_vector_type(2))) float f32x2;
typedef __attribute__((ext_vector_type(2))) __bf16 bf16x2;
typedef __attribute__((ext_vector_type(2))) unsigned u32x2;
typedef __attribute__((ext_vector_type(4))) unsigned u32x4;

#define MK(I) ((void)0)
#define MX(S, I) ((void)0)
#define MF(I) ((void)0)
#define MF_ARG
#define MF_PASS
#define MK_ARG
#define MK_PASS

#define SPIN_LDS for (;;)
#define SPIN_MEM (1u << 22)

#define G1W 0
#define P5PACE 0
#define L0REG 0
#define RTC 0
#define P1PR 3
#define HSPLIT 1
#define HPOLL 1
#define XA4 1
#define P5ORD 0
#define CT0 1
#define AL_A 0
#define AL_B 0
#define AL_C 0
#define AL_P5 0
#define AL_P2 0
#define AL_G -1
#define AL_K 0
#define AL_L 0
#define CSL 1
#define NOPL 0
#define G1E 1
#define XVE 0
#define ZSL 11
#define NR1 3
#define MPOLL 1
namespace {

constexpr int S_ = 2048, D_ = 576, F_ = 4608, FH_ = 2304;
constexpr int NQKV = 3 * D_;

constexpr size_t SZ_X = (size_t)S_ * D_ * 2;
constexpr size_t SZ_Z = (size_t)S_ * FH_ * 2;
constexpr size_t OFF_X1 = 0;
constexpr size_t OFF_O = OFF_X1 + 3 * SZ_X;
constexpr size_t OFF_Z2 = OFF_O + 2 * SZ_X;
constexpr size_t OFF_H = OFF_Z2 + 2 * SZ_Z;
constexpr size_t SZ_XS = (size_t)S_ * 8 * 8;
constexpr size_t OFF_XS = OFF_H + 2 * SZ_Z;
constexpr size_t OFF_CSQ = OFF_XS + 3 * SZ_XS;
constexpr size_t OFF_ZP = OFF_CSQ + 256 * 512;
constexpr size_t SZ_ZP = (size_t)16 * 16 * 1024;
constexpr size_t OFF_PQ = OFF_ZP + 2 * SZ_ZP;
constexpr size_t OFF_PO = OFF_PQ + (size_t)16 * 9 * 64 * 112 * 2;
constexpr size_t OFF_PI = OFF_PO + (size_t)D_ * D_ * 2;
constexpr size_t OFF_PW = OFF_PI + (size_t)32 * 9 * 64 * 144 * 2;
constexpr size_t OFF_CSP = OFF_PW + (size_t)FH_ * D_ * 2;
constexpr size_t OFF_CNT = OFF_CSP + (size_t)18 * F_ * 4;
constexpr int CN_X1 = 0, CN_H = 2, CN_X2 = 4, CN_O = 5, CN_Z = 7, CN_PK = 9, CN_H2 = 10, NCNT = 12;
constexpr size_t OFF_TAG = OFF_CNT + (size_t)NCNT * 256 * 128;
constexpr size_t OFF_HELLO = OFF_TAG + 128;
constexpr size_t OFF_POT = OFF_HELLO + 2048;
constexpr size_t OFF_CSK = OFF_POT + 1024;
constexpr size_t WS_END = OFF_CSK + 256 * 2048;

DEVI float bf_lo(unsigned u) { return __uint_as_float(u << 16); }
DEVI float bf_hi(unsigned u) { return __uint_as_float(u & 0xffff0000u); }
DEVI unsigned pack2(float a, float b) {
  f32x2 v = {a, b};
  bf16x2 r = __builtin_convertvector(v, bf16x2);
  return __builtin_bit_cast(unsigned, r);
}
DEVI float rbf(float a) {
  return bf_lo(pack2(a, 0.f));
}

DEVI float sum_g(float x) {
  const unsigned u = __float_as_uint(x);
  const auto p = __builtin_amdgcn_permlane32_swap(u, u, false, false);
  const unsigned v = __float_as_uint(__uint_as_float(p[0]) + __uint_as_float(p[1]));
  const auto q = __builtin_amdgcn_permlane16_swap(v, v, false, false);
  return __uint_as_float(q[0]) + __uint_as_float(q[1]);
}

DEVI unsigned vconst(unsigned c) {
  unsigned r;
  asm volatile("v_mov_b32 %0, %1" : "=v"(r) : "s"(c));
  return r;
}
template <int K>
DEVI void place() {
  if constexpr (K >= 0) {
    __builtin_amdgcn_sched_barrier(0);
    asm volatile(".p2align 6\n\t.fill %0, 4, 0xbf800000" ::"n"(K));
    __builtin_amdgcn_sched_barrier(0);
  }
}
DEVI const char* uni_ptr(const char* p) {
  unsigned long long v = (unsigned long long)p;
  unsigned lo = __builtin_amdgcn_readfirstlane((unsigned)v), hi = __builtin_amdgcn_readfirstlane((unsigned)(v >> 32));
  asm("s_nop 4" : "+s"(lo), "+s"(hi));
  return (const char*)(((unsigned long long)hi << 32) | lo);
}

DEVI void dma16(const char* sbase, unsigned voff, unsigned m0val) {
  asm volatile("s_mov_b32 m0, %2\n\ts_nop 0\n\tglobal_load_lds_dwordx4 %0, %1"
               :
               : "v"(voff), "s"(sbase), "s"(m0val)
               : "memory");
}

DEVI void ln_finish(float s, float q, float invn, float& mb, float& rinv) {
  float mean = s * invn;
  asm("" : "+v"(mean));
  float var = fmaxf(__builtin_fmaf(-mean, mean, q * invn), 0.f);
  mb = rbf(mean);
  float v = rbf(var);
  float t = rbf(v + 1e-5f);
  float sd = rbf(__builtin_amdgcn_sqrtf(t));
  rinv = __builtin_amdgcn_rcpf(sd);
}

DEVI unsigned pack2v(f32x2 v) { return pack2(v[0], v[1]); }

template <int BM_, int BN_>
struct GemmCfg {
  static constexpr int BM = BM_, BN = BN_, NS = 4;
  static constexpr int P = BN * 2;
  static constexpr int XB = BM * 128, WB = 64 * P, SB = XB + WB;
  static constexpr int LDS_BYTES = NS * SB;
};

DEVI int swap23(int r) { return (r & ~12) | ((r & 4) << 1) | ((r & 8) >> 1); }

DEVI unsigned lds_rd32(unsigned addr) {
  unsigned v;
  asm volatile("ds_read_b32 %0, %1\n\ts_waitcnt lgkmcnt(0)" : "=v"(v) : "v"(addr) : "memory");
  return v;
}
DEVI void lds_wr32(unsigned addr, unsigned v) { asm volatile("ds_write_b32 %0, %1" ::"v"(addr), "v"(v) : "memory"); }

template <class C, int MT, int NT, int D, int R, bool XCM, int WT, bool SQ = false, unsigned long long WS = 0>
struct GemmMath {
  static constexpr int WO = WT ? 1 : 2;
  static constexpr int NS = C::NS, SB = C::SB, P = C::P, NU = 2 * NT;
  static constexpr int XPU = MT <= 2 ? MT : MT - 2;
  static constexpr int XU = (MT + XPU - 1) / XPU;
  static_assert(NU % R == 0 && R > D && D <= NT && XU <= D, "shape");
  s16x4 wlo[R], whi[R];
  s16x8 wf[R];
  s16x8 xf[2][MT];
  unsigned xrd[2], wrd, xa[2], wa, so;
  f32x4 (&acc)[MT][NT];
  f32x4 accq[SQ ? MT : 1];
  unsigned long long wtab = WS;
  DEVI GemmMath(f32x4 (&a)[MT][NT]) : acc(a) {}

  template <int PU>
  DEVI void wread_lo() {
    constexpr int off = (PU / NT) * 32 * P + (PU % NT) * (WT == 2 ? 1024 : 32);
    if constexpr (WT) asm volatile("ds_read_b128 %0, %1 offset:%2" : "=v"(wf[PU % R]) : "v"(wa), "n"(off));
    else asm volatile("ds_read_b64_tr_b16 %0, %1 offset:%2" : "=v"(wlo[PU % R]) : "v"(wa), "n"(off));
  }
  template <int PU>
  DEVI void wread_hi() {
    if constexpr (!WT)
      asm volatile("ds_read_b64_tr_b16 %0, %1 offset:%2" : "=v"(whi[PU % R]) : "v"(wa), "n"((PU / NT) * 32 * P + (PU % NT) * 32 + 8 * P));
  }
  template <int H, int M>
  DEVI void xread1() {
    asm volatile("ds_read_b128 %0, %1 offset:%2" : "=v"(xf[H][M]) : "v"(xa[H]), "n"(M * (XCM ? 256 : 2048)));
  }
  static constexpr int xops(int v) {
    const int k = (v + D) % NT;
    if (k >= XU) return 0;
    const int n = MT - k * XPU;
    return n < XPU ? n : XPU;
  }
  static constexpr int ops(int v, bool last) { return (last && v + D >= NU) ? 0 : WO + xops(v); }
  static constexpr int newer(int U, bool last) {
    int n = 0;
    int v0 = U - D;
    if (U % NT == 0) v0 += XU - 1;
    else n += v0 >= 0 ? xops(v0) : xops(v0 + NU);
    for (int v = v0 + 1; v < U; ++v) n += v >= 0 ? ops(v, last) : ops(v + NU, false);
    return n;
  }
  template <int U, int M_>
  DEVI void mf() {
    constexpr int M = ((U % NT) & 1) ? MT - 1 - M_ : M_;
    s16x8 w;
    if constexpr (WT) w = wf[U % R];
    else w = __builtin_shufflevector(wlo[U % R], whi[U % R], 0, 1, 2, 3, 4, 5, 6, 7);
    acc[M][U % NT] = __builtin_amdgcn_mfma_f32_16x16x32_bf16(w, xf[U / NT][M], acc[M][U % NT], 0, 0, 0);
    __builtin_amdgcn_sched_barrier(0);
  }
  DEVI void pin() {
#pragma unroll
    for (int m = 0; m < MT; ++m)
#pragma unroll
      for (int n = 0; n < NT; ++n) asm volatile("" : "+v"(acc[m][n]));
  }
  template <int U, int I>
  DEVI void dsop() {
    constexpr int PU = (U + D) % NU;
    if constexpr (I == 0) wread_lo<PU>();
    else if constexpr (I == 1) wread_hi<PU>();
    else if constexpr (I - 2 < xops(U)) {
      constexpr int k = (U + D) % NT;
      xread1<((U + D - k) % NU) / NT, k * XPU + I - 2>();
    }
  }
  template <int U, bool LAST, bool BAR, int M>
  DEVI void slot() {
    constexpr bool PF = !(LAST && U + D >= NU);
    mf<U, M>();
    if constexpr (M == 0 && PF && U + D == NU) {
      if constexpr (BAR) __builtin_amdgcn_s_barrier();
      so = (so == (unsigned)((NS - 1) * SB)) ? 0u : so + (unsigned)SB;
      wa = wrd + (WS ? ((unsigned)wtab & 63u) << 11 : so);
      wtab >>= 6;
      xa[0] = xrd[0] + so;
      xa[1] = xrd[1] + so;
      __builtin_amdgcn_sched_barrier(0);
    }
    if constexpr (PF) {
      dsop<U, M>();
      if constexpr (M == MT - 1) {
        if constexpr (MT <= 1) dsop<U, 1>();
        if constexpr (MT <= 2) {
          dsop<U, 2>();
          dsop<U, 3>();
        }
      }
      __builtin_amdgcn_sched_barrier(0);
    }
  }
  template <int U, bool LAST, bool BAR, int... M>
  DEVI void unit(std::integer_sequence<int, M...>) {
    static_assert(newer(U, LAST) <= 15, "lgkmcnt range");
    asm volatile("s_waitcnt lgkmcnt(%0)" ::"n"(newer(U, LAST)));
    __builtin_amdgcn_sched_barrier(0);
    (slot<U, LAST, BAR, M>(), ...);
    if constexpr (SQ && (U % NT) < MT) {
      constexpr int m = U % NT;
      accq[m] = __builtin_amdgcn_mfma_f32_16x16x32_bf16(xf[U / NT][m], xf[U / NT][m], accq[m], 0, 0, 0);
      __builtin_amdgcn_sched_barrier(0);
    }
    __builtin_amdgcn_sched_barrier(0);
  }
  template <bool LAST, bool BAR, int... U>
  DEVI void stage(std::integer_sequence<int, U...>) {
    (unit<U, LAST, BAR>(std::make_integer_sequence<int, MT>{}), ...);
  }
  template <int... U>
  DEVI void prow(std::integer_sequence<int, U...>) {
    ((wread_lo<U>(), wread_hi<U>()), ...);
  }
  template <int... M>
  DEVI void prox(std::integer_sequence<int, M...>) {
    (xread1<0, M>(), ...);
  }
  DEVI void setup(unsigned lds0, int mt0, int nt0, int lane, unsigned so0) {
    const int g = lane >> 4, r = lane & 15, q = (lane >> 2) & 3, p = lane & 3;
#pragma unroll
    for (int h = 0; h < 2; ++h)
      xrd[h] = XCM ? lds0 + (unsigned)((4 * h + g) * (C::BM * 16) + (mt0 * 16 + r) * 16)
                   : lds0 + (unsigned)((mt0 * 16 + r) * 128 + (((4 * h + g) ^ ((r >> 1) & 7)) * 16));
    wrd = WT == 2   ? lds0 + (unsigned)(C::XB + nt0 * 1024 + lane * 16)
          : WT == 1 ? lds0 + (unsigned)(C::XB + (g * 8 + (r & 7)) * P + (r >> 3) * 16 + nt0 * 32)
                    : lds0 + (unsigned)(C::XB + (16 * (g >> 1) + 4 * (g & 1) + q) * P + 8 * p + nt0 * 32);
    so = so0;
    xa[0] = xrd[0] + so;
    xa[1] = xrd[1] + so;
    wa = wrd + so;
  }
  DEVI void first() {
    prox(std::make_integer_sequence<int, MT>{});
    prow(std::make_integer_sequence<int, D>{});
    asm volatile("s_waitcnt lgkmcnt(0)");
  }
  template <int N, bool BAR = true, int AL = -1>
  DEVI void mid() {
    place<AL>();
#pragma unroll 1
    for (int s = 0; s < N; ++s) stage<false, BAR>(std::make_integer_sequence<int, NU>{});
  }
  DEVI void last() {
    stage<true, true>(std::make_integer_sequence<int, NU>{});
    pin();
  }
  template <int KS>
  DEVI void run(unsigned lds0, int mt0, int nt0, int lane, unsigned so0) {
    setup(lds0, mt0, nt0, lane, so0);
    __builtin_amdgcn_s_barrier();
    first();
    mid<KS - 1, true, AL_K>();
    last();
  }
};

DEVI const char* code_here() {
  unsigned long long pc;
  asm volatile("s_getpc_b64 %0" : "=s"(pc));
  return (const char*)pc;
}
DEVI int okmap(int p) {
  const int ch = p >> 3, e = p & 7, c = (ch - 8) & 3;
  return ch < 8 ? (2 * ch + (e >> 2)) * 36 + 16 + (e & 3) : ((ch - 8) >> 2) * 36 + c * 8 + (c >> 1) * 4 + e;
}

DEVI void pk_st16(void* p, u32x4 v) {
  asm volatile("global_store_dwordx4 %0, %1, off sc1\n\ts_nop 1" ::"v"(p), "v"(v) : "memory");
}

using K1C = GemmCfg<144, 112>;
constexpr int K1_TP = 240;
constexpr int K1_T_OFF = K1C::SB;
constexpr int K1_ST_OFF = 163840 - 128 - 1152;
constexpr int K1_CS_OFF = 4 * K1C::SB;
constexpr int K1_WX = 4 * K1C::SB + 2048;
constexpr unsigned long long K1_WS = 16ull | 32ull << 6 | 48ull << 12 | 56ull << 18 | 63ull << 24 | 16ull << 30 | 32ull << 36 | 48ull << 42;
static_assert(K1C::SB == 16 * 2048 && K1_WX - K1C::XB == 56 * 2048 && K1C::WB == 7 * 2048 && K1_WX + 2 * K1C::WB <= K1_ST_OFF, "K1 w places");
static_assert(K1_T_OFF + 144 * K1_TP + 64 <= 2 * K1C::SB + K1C::XB, "K1 LDS alias");
using K1R = GemmCfg<144, 144>;
constexpr int K1R_T_OFF = K1R::SB;
constexpr int K1R_CS_OFF = K1R_T_OFF + 144 * K1_TP;
constexpr int K1R_ST_OFF = K1R_CS_OFF + 512;
constexpr int K1R_DUMMY = K1R::LDS_BYTES;
constexpr int K1R_PO = K1R_DUMMY + 1024;
constexpr int K1R_CNT = 163840 - 128 + 28 * 4;
DEVI void msync(unsigned addr) {
  asm volatile("s_waitcnt lgkmcnt(0)" ::: "memory");
  asm volatile("s_mov_b64 exec, 1\n\tds_add_u32 %0, %1\n\ts_mov_b64 exec, -1" ::"v"(addr), "v"(vconst(1u)) : "memory");
  SPIN_LDS {
    if (__builtin_amdgcn_readfirstlane(lds_rd32(addr)) >= 8u) break;
  }
}
static_assert(K1R_PO + 13824 + 256 <= 163840 - 128 && K1R_ST_OFF + 144 * 8 <= 3 * K1R::SB, "K1R LDS");

DEVI void touch64(const char* sbase, unsigned voff, unsigned m0val) {
  asm volatile("s_mov_b32 m0, %2\n\ts_nop 0\n\tglobal_load_lds_dword %0, %1" : : "v"(voff), "s"(sbase), "s"(m0val) : "memory");
}

template <int MT, bool G1>
DEVI void k1_gemm_raw(char* smem, int hp, int mt0, int lane MF_ARG) {
  constexpr int NT = 4, nt0 = G1 ? 4 : 0;
  const unsigned lds0 = (unsigned)(size_t)smem;
  const int g = lane >> 4, r = lane & 15;
  unsigned ta[NT];
#pragma unroll
  for (int nt = 0; nt < NT; ++nt) {
    const int j = (nt0 + nt) * 16 + g * 4, seg = j / 40, cj = j - seg * 40 - (hp & 1) * 4;
    const int to = (seg < 3 && cj >= 0 && cj < 36) ? seg * 80 + cj * 2 + (cj >= 20 ? 8 : 0) : (seg < 3 ? 40 : 120);
    ta[nt] = lds0 + (unsigned)(K1R_T_OFF + (mt0 * 16 + r) * K1_TP + to);
    asm volatile("" : "+v"(ta[nt]));
  }
  unsigned sa = lds0 + (unsigned)(K1R_ST_OFF + (mt0 * 16 + r) * 8), ca = lds0 + (unsigned)(K1R_CS_OFF + (nt0 * 16 + g * 4) * 4);
  asm volatile("" : "+v"(sa), "+v"(ca));
  f32x4 acc[MT][NT];
#pragma unroll
  for (int mt = 0; mt < MT; ++mt)
#pragma unroll
    for (int nt = 0; nt < NT; ++nt) acc[mt][nt] = f32x4{0.f, 0.f, 0.f, 0.f};
  {
    GemmMath<K1R, MT, 4, 3, 4, false, 0, G1> gm(acc);
    if constexpr (G1) {
#pragma unroll
      for (int mt = 0; mt < MT; ++mt) gm.accq[mt] = f32x4{0.f, 0.f, 0.f, 0.f};
    }
    MF(14);
    if constexpr (!G1) {
      if (mt0 < 7) asm volatile("s_waitcnt vmcnt(0)" ::: "memory");
    }
    MF(15);
    gm.template run<9>(lds0, mt0, nt0, lane, 0u);
    MF(2);
    if constexpr (G1) {
#pragma unroll
      for (int mt = 0; mt < MT; ++mt) {
        const f32x4 q = gm.accq[mt];
        const float d = (r & 2) ? ((r & 1) ? q[3] : q[2]) : ((r & 1) ? q[1] : q[0]);
        const float qq = sum_g(g == (r >> 2) ? d : 0.f);
        float mb, rinv;
        ln_finish(acc[mt][3][0], qq, 1.0f / (float)D_, mb, rinv);
        const f32x2 st = {-mb, rinv};
        if (g == 2) asm volatile("ds_write_b64 %0, %1 offset:%2" ::"v"(sa), "v"(st), "n"(mt * 128) : "memory");
      }
    }
  }
  if (mt0 == 0 && r == 0) {
#pragma unroll
    for (int nt = 0; nt < NT; ++nt) *reinterpret_cast<f32x4*>(smem + K1R_CS_OFF + ((nt0 + nt) * 16 + g * 4) * 4) = acc[0][nt];
  }
  msync(lds0 + (unsigned)K1R_CNT);
  MF(3);
  f32x2 st[MT];
  f32x4 cs[NT];
#pragma unroll
  for (int nt = 0; nt < NT; ++nt) asm volatile("ds_read_b128 %0, %1 offset:%2" : "=v"(cs[nt]) : "v"(ca), "n"(nt * 64) : "memory");
#pragma unroll
  for (int mt = 0; mt < MT; ++mt) asm volatile("ds_read_b64 %0, %1 offset:%2" : "=v"(st[mt]) : "v"(sa), "n"(mt * 128) : "memory");
#pragma unroll
  for (int i = 0; i < MT; ++i) asm volatile("s_waitcnt lgkmcnt(0)" : "+v"(st[i]));
#pragma unroll
  for (int i = 0; i < NT; ++i) asm volatile("" : "+v"(cs[i]));
#pragma unroll
  for (int mt = 0; mt < MT; ++mt) {
    f32x2 M = {st[mt][0], st[mt][0]}, R = {st[mt][1], st[mt][1]};
    asm volatile("" : "+v"(M), "+v"(R));
#pragma unroll
    for (int nt = 0; nt < NT; ++nt) {
      const f32x2 v0 = R * __builtin_elementwise_fma(M, f32x2{cs[nt][0], cs[nt][1]}, f32x2{acc[mt][nt][0], acc[mt][nt][1]});
      const f32x2 v1 = R * __builtin_elementwise_fma(M, f32x2{cs[nt][2], cs[nt][3]}, f32x2{acc[mt][nt][2], acc[mt][nt][3]});
      acc[mt][nt][0] = __uint_as_float(pack2v(v0));
      acc[mt][nt][1] = __uint_as_float(pack2v(v1));
      const f32x2 pk = {acc[mt][nt][0], acc[mt][nt][1]};
      asm volatile("ds_write_b64 %0, %1 offset:%2" ::"v"(ta[nt]), "v"(pk), "n"(mt * 16 * K1_TP) : "memory");
    }
  }
}

DEVI void dma16m(const char* sbase, unsigned voff, unsigned m0val, unsigned long long mask) {
  asm volatile("s_mov_b32 m0, %2\n\ts_mov_b64 exec, %3\n\tglobal_load_lds_dwordx4 %0, %1\n\ts_mov_b64 exec, -1"
               :
               : "v"(voff), "s"(sbase), "s"(m0val), "s"(mask)
               : "memory");
}
template <int N>
DEVI void wait_vm() {
  asm volatile("s_waitcnt vmcnt(%0)" ::"n"(N) : "memory");
}

constexpr int LDS_ALL = 163840;
constexpr int LW_OFF = LDS_ALL - 128;
constexpr int LW_GO = 0, LW_T = 2, LW_O = 4  , LW_X1 = 6, LW_H = 8, LW_X2 = 12, LW_PK = 13, LW_Z = 14, LW_PAIR = 16, LW_AD = 20,
              LW_L = 22, LW_S = 24, LW_ST = 29, LW_HELLO = 30, LW_Q = 31;
DEVI void announce(unsigned addr, unsigned n, char* ws, int kind, int who, bool act) {
  const char* cb = uni_ptr(ws + OFF_CNT + (size_t)kind * 256 * 128);
  const unsigned long long mask = __ballot(act);
  unsigned t, u;
  asm volatile(
      "s_waitcnt vmcnt(0)\n\t"
      "s_mov_b64 exec, 1\n\t"
      "ds_add_rtn_u32 %0, %2, %3\n\t"
      "s_waitcnt lgkmcnt(0)\n\t"
      "v_readfirstlane_b32 %1, %0\n\t"
      "s_cmp_eq_u32 %1, %4\n\t"
      "s_cselect_b64 exec, %5, 0\n\t"
      "global_atomic_add %6, %3, %7 sc1\n\t"
      "s_mov_b64 exec, -1\n\t"
      "s_nop 0"
      : "=&v"(t), "=&s"(u)
      : "v"(addr), "v"(vconst(1u)), "s"(n - 1u), "s"(mask), "v"((unsigned)(who & 255) * 128u), "s"(cb)
      : "memory", "scc");
}
DEVI void cnt_wait(const char* ws, int kind, int who, unsigned n) {
  const char* cp = ws + OFF_CNT + (size_t)(kind * 256 + who) * 128;
#pragma unroll 1
  for (unsigned spin = 0; spin < SPIN_MEM; ++spin) {
    unsigned f;
    asm volatile("global_load_dword %0, %1, off sc1\n\ts_waitcnt vmcnt(0)" : "=&v"(f) : "v"(cp) : "memory");
    if (__builtin_amdgcn_readfirstlane(f) == n - 1u) break;
    __builtin_amdgcn_s_sleep(1);
  }
}
DEVI void dma16_sc1(const char* sbase, unsigned voff, unsigned m0val) {
  asm volatile("s_mov_b32 m0, %2\n\ts_nop 0\n\tglobal_load_lds_dwordx4 %0, %1 sc1"
               :
               : "v"(voff), "s"(sbase), "s"(m0val)
               : "memory");
}
DEVI void bar() { __builtin_amdgcn_s_barrier(); }
DEVI void lds_inc(unsigned addr, unsigned by = 1u) {
  asm volatile("s_mov_b64 exec, 1\n\tds_add_u32 %0, %1\n\ts_mov_b64 exec, -1" ::"v"(addr), "v"(vconst(by)) : "memory");
}
DEVI void lds_spin(unsigned addr, unsigned n) {
  SPIN_LDS {
    if (__builtin_amdgcn_readfirstlane(lds_rd32(addr)) >= n) break;
    __builtin_amdgcn_s_sleep(1);
  }
}
template <int SL = 1>
DEVI void lds_spin4(unsigned addr, unsigned n) {
  SPIN_LDS {
    if (((__builtin_amdgcn_readfirstlane(lds_rd32(addr)) + (0x80u - n) * 0x01010101u) & 0x80808080u) == 0x80808080u) break;
    __builtin_amdgcn_s_sleep(SL);
  }
}
DEVI int lane_id() {
  int l;
  asm volatile("v_mbcnt_lo_u32_b32 %0, -1, 0\n\tv_mbcnt_hi_u32_b32 %0, -1, %0" : "=v"(l));
  __builtin_assume((unsigned)l < 64u);
  return l;
}

template <int I>
DEVI u32x4 tr8(const u32x4 (&v)[8]) {
  constexpr unsigned sel = (I & 1) ? 0x07060302u : 0x05040100u;
  return u32x4{__builtin_amdgcn_perm(v[1][I / 2], v[0][I / 2], sel), __builtin_amdgcn_perm(v[3][I / 2], v[2][I / 2], sel),
               __builtin_amdgcn_perm(v[5][I / 2], v[4][I / 2], sel), __builtin_amdgcn_perm(v[7][I / 2], v[6][I / 2], sel)};
}
template <int PITCH, bool SUM, int... I>
DEVI void tr8_store(const u32x4 (&v)[8], unsigned dst, float (&cs)[8], std::integer_sequence<int, I...>) {
  auto one = [&](auto I_) {
    const u32x4 o = tr8<I_>(v);
    const unsigned d_ = dst;
    asm volatile("ds_write_b128 %0, %1 offset:%2" ::"v"(d_), "v"(o), "n"(I_ * PITCH) : "memory");
    if constexpr (SUM) {
#pragma unroll
      for (int d = 0; d < 4; ++d) {
        const unsigned od = o[d];
        cs[I_] = __builtin_amdgcn_fdot2_f32_bf16(__builtin_bit_cast(bf16x2, od), __builtin_bit_cast(bf16x2, 0x3f803f80u), cs[I_], false);
      }
    }
  };
  (one(std::integral_constant<int, I>{}), ...);
}
template <int NB>
DEVI void lds_out(unsigned src, char* dst, int lane) {
  constexpr int N = (NB + 1023) / 1024;
  u32x4 t[N];
  const unsigned a = src + (unsigned)lane * 16u;
#pragma unroll
  for (int i = 0; i < N; ++i) asm volatile("ds_read_b128 %0, %1 offset:%2" : "=v"(t[i]) : "v"(a), "n"(i * 1024) : "memory");
  const char* d = uni_ptr(dst);
#pragma unroll
  for (int i = 0; i < N; ++i) {
    asm volatile("s_waitcnt lgkmcnt(%1)" : "+v"(t[i]) : "n"(N - 1 - i) : "memory");
    if (i < N - 1 || lane < (NB - (N - 1) * 1024) / 16)
      asm volatile("global_store_dwordx4 %0, %1, %2 sc1\n\ts_nop 1" ::"v"((unsigned)lane * 16u), "v"(t[i]), "s"(d + i * 1024) : "memory");
  }
}
template <int A, int B, class F>
DEVI void static_for(F f) {
  if constexpr (A < B) {
    f(std::integral_constant<int, A>{});
    static_for<A + 1, B>(f);
  }
}
constexpr int WSP_V = 3;
#define RS0 4
#define RS1 7
#define RX0 RS0
constexpr int RAW_S1 = RS1;
constexpr int RAW_S0 = RS0;
template <int E>
struct RawX {
  static constexpr int first(int s) {
    return s <= RX0 ? 0 : (s > RAW_S1 ? E : (E * (s - RX0) + RAW_S1 - RX0) / (RAW_S1 + 1 - RX0));
  }
  static constexpr int n(int s) { return first(s + 1) - first(s); }
};
struct RawLd {
  static constexpr int VPS = 10, SB = K1R::SB;
  unsigned xoff[5], woff[5], m0x;
  unsigned long long xact0, wact[5], tag;
  const char *xp, *wp;
  int qb;
  DEVI void q_hook(int st, char* ws, unsigned lds0, int hp) {
    if ((qb >> 1) == st) {
      const int lane = lane_id(), g = lane >> 4;
      const unsigned a = lds0 + (unsigned)((st & 3) * SB + K1R::XB + ((qb & 1) * 32 + 16 * (g >> 1) + 4 * (g & 1) + ((lane >> 2) & 3)) * 288 +
                                           (lane & 3) * 8 + (hp & 1) * 8);
      const unsigned a2 = a + ((lane & 3) >= 1 ? 8u : 0u), a4 = a + ((lane & 3) >= 2 ? 8u : 0u);
      u32x2 qf[7][2];
#pragma unroll
      for (int nt = 0; nt < 7; ++nt)
        asm volatile("ds_read_b64_tr_b16 %0, %2 offset:%3\n\tds_read_b64_tr_b16 %1, %2 offset:%4"
                     : "=&v"(qf[nt][0]), "=&v"(qf[nt][1])
                     : "v"(nt == 2 ? a2 : (nt == 4 ? a4 : a)), "n"(nt * 32 + (nt > 4 ? 16 : (nt > 2 ? 8 : 0))),
                       "n"(2304 + nt * 32 + (nt > 4 ? 16 : (nt > 2 ? 8 : 0)))
                     : "memory");
      const char* d = uni_ptr(ws + OFF_PQ + (size_t)(hp * 18 + qb) * 7168 + 3072);
#pragma unroll
      for (int nt = 0; nt < 7; ++nt) {
        asm volatile("s_waitcnt lgkmcnt(%2)" : "+v"(qf[nt][0]), "+v"(qf[nt][1]) : "n"(12 - 2 * nt) : "memory");
        const u32x4 v = {qf[nt][0][0], qf[nt][0][1], qf[nt][1][0], qf[nt][1][1]};
        asm volatile("global_store_dwordx4 %0, %1, %2 offset:%3 sc1\n\ts_nop 1" ::"v"((unsigned)lane * 16u), "v"(v), "s"(d), "n"(nt * 1024 - 3072) : "memory");
      }
      if (st == 8) lds_wr32(lds0 + (unsigned)(LW_OFF + LW_Q * 4), 1u);
    }
  }
  DEVI void stage_x(unsigned so) {
    dma16m(xp, xoff[0], m0x + so, xact0);
#pragma unroll
    for (int i = 1; i < 4; ++i) dma16(xp, xoff[i], m0x + so + (unsigned)(i * 1024));
    dma16m(xp, xoff[4], m0x + so + 4096u, 0xffffffffull);
    xp += 128;
  }
  DEVI void stage_w(unsigned so) {
#pragma unroll
    for (int i = 0; i < 5; ++i) dma16m(wp, woff[i], m0x + so + (unsigned)(K1R::XB + i * 1024), wact[i]);
    wp += 64 * NQKV * 2;
  }
  DEVI void stage(unsigned so) {
    stage_x(so);
    stage_w(so);
  }
  DEVI void front(const u16* x, const u16* wqkv, const u16* wo, char* ws, int bid, unsigned lds0, int wl, int lane, int rt, int hp,
                  int pj MF_ARG) {
    xp = uni_ptr((const char*)x);
    wp = uni_ptr((const char*)wqkv + ((hp * 72) & ~15));
    m0x = lds0 + (unsigned)(wl * 288 * 16);
    xact0 = wl ? ~0ull : ~0xffull;
    const int row0 = wl * 36 + (lane >> 3);
#pragma unroll
    for (int i = 0; i < 5; ++i) {
      const int row = row0 + i * 8;
      int gr = rt * 128 - 16 + row;
      if (gr < 0) gr = 0;
      xoff[i] = (unsigned)(gr * (D_ * 2)) + (unsigned)(((lane ^ (row >> 1)) & 7) * 16);
    }
    stage_x(0u);
    u32x2 tg;
    asm volatile("global_load_dwordx2 %0, %1, %2 sc1" : "=v"(tg) : "v"(0u), "s"(uni_ptr(ws + OFF_TAG)) : "memory");
    asm volatile("" : "+v"(lane));
#pragma unroll
    for (int i = 0; i < 5; ++i) {
      const unsigned c = (unsigned)(i * 64 + (lane & 63));
      const unsigned wr = (c * 3641u) >> 16, wc = c - wr * 18u, seg = (wc * 13u) >> 6;
      wact[i] = __ballot(wc < 15u) & (i < 4 ? ~0ull : 0xffffffffull);
      woff[i] = (unsigned)(wl * 16 + swap23((int)wr)) * (unsigned)(NQKV * 2) + seg * (unsigned)(D_ * 2 - 80) + wc * 16u;
    }
    stage_w(0u);
    stage((unsigned)SB);
    MF(10);
    asm volatile("s_waitcnt vmcnt(%1)" : "+v"(tg) : "n"(VPS) : "memory");
    tag = (((unsigned long long)(unsigned)__builtin_amdgcn_readfirstlane(tg[1]) << 32) | (unsigned)__builtin_amdgcn_readfirstlane(tg[0])) +
          0x9e3779b97f4a7c15ull;
    MF(1);
    __builtin_amdgcn_s_barrier();
    MF(15);
    if (wl == 0 && lane == 0)
      asm volatile("global_store_dwordx2 %0, %1, off sc1\n\ts_nop 1" ::"v"(ws + OFF_HELLO + bid * 8), "v"(tag) : "memory");
    stage((unsigned)(2 * SB));
    qb = wl == 3 ? rt : ((wl == 2 && rt < 2) ? 16 + rt : 99);
    q_hook(0, ws, lds0, hp);
    const int nb = pj;
    const bool po = pj >= 0 && wl < 2;
    u32x4 v[8];
    unsigned da = 0u;
    if (po) {
      const int l54 = lane < 54 ? lane : 53, gl = l54 / 9, c = l54 - gl * 9, kl = wl * 6 + gl, t = nb / 6, kg = (nb - t * 6) * 12 + kl;
      da = lds0 + (unsigned)(K1R_PO + (kl >> 2) * 4608 + (16 * (kl & 1) + ((kl >> 1) & 1)) * 144 + c * 16);
      const unsigned sa[2] = {(unsigned)(okmap(kg * 8) * D_ + t * 72 + c * 8) * 2u, (unsigned)(okmap(kg * 8 + 4) * D_ + t * 72 + c * 8) * 2u};
      const char* ps = uni_ptr((const char*)wo);
#pragma unroll
      for (int j = 0; j < 8; ++j)
        asm volatile("global_load_dwordx4 %0, %1, %2 offset:%3" : "=v"(v[j]) : "v"(sa[j / 4]), "s"(ps), "n"((j % 4) * D_ * 2) : "memory");
    } else {
#pragma unroll
      for (int j = 0; j < 8; ++j) asm volatile("" : "=v"(v[j]));
    }
    unsigned slot = (unsigned)(3 * SB);
#pragma unroll
    for (int S = 0; S < RAW_S0; ++S) {
      wait_vm<VPS>();
      __builtin_amdgcn_s_barrier();
      if (S == 0) MF(16);
      if (S == 1) MF(17);
      if (S == 2) MF(18);
      if (S == 3) MF(19);
      stage(slot);
      slot = (slot == (unsigned)(3 * SB)) ? 0u : slot + (unsigned)SB;
      q_hook(S + 1, ws, lds0, hp);
      if (po && S == 1) {
        float cs[8];
        asm volatile("" : "+v"(v[0]), "+v"(v[1]), "+v"(v[2]), "+v"(v[3]), "+v"(v[4]), "+v"(v[5]), "+v"(v[6]), "+v"(v[7]));
        tr8_store<288, false>(v, da, cs, std::make_integer_sequence<int, 8>{});
        asm volatile("s_waitcnt lgkmcnt(0)" ::: "memory");
      }
      if (po && S == 2) lds_out<6912>(lds0 + (unsigned)(K1R_PO + wl * 6912), ws + OFF_PO + (size_t)nb * 13824 + wl * 6912, lane);
    }
  }
};
template <bool JOB>
DEVI void k1_raw_loader(RawLd& ld, const u16* __restrict__ w_in, const u16* __restrict__ w_out, char* __restrict__ ws,
                        unsigned long long tag, int bid, unsigned lds0, int wl, int lane, int rt, int hp, int ct, int job, int pj MF_ARG) {
  constexpr int VPS = RawLd::VPS, SB = RawLd::SB, E = JOB ? 16 : 0;
  const bool isi = job < 144;
  const int kb = isi ? job >> 3 : job - 144;
  const int pitch = isi ? F_ * 2 : D_ * 2;
  const int l36 = lane < 36 ? lane : 35;
  unsigned sa = (unsigned)l36 * 16u, da;
  if (isi) da = lds0 + (unsigned)((l36 >= 18 ? 9216 - 288 : 0) + wl * 8 * 288 + l36 * 16);
  else da = lds0 + (unsigned)((l36 / 9) * (4608 - 144) + (16 * (wl & 1) + (wl >> 1)) * 144 + l36 * 16);
  asm volatile("" : "+v"(sa), "+v"(da));
  u32x4 v[2][8];
  const char* ps = uni_ptr((const char*)(isi ? w_in + (job & 7) * 576 : w_out) + (size_t)(kb * 32 + wl * 8) * pitch);
  auto extra = [&](auto J_) {
    constexpr int j = J_;
    const char* q = ps + (j / 2) * pitch;
    u32x4 t;
    asm volatile("global_load_dwordx4 %0, %1, %2 offset:%3" : "=v"(t) : "v"(sa), "s"(q), "n"((j % 2) * 576) : "memory");
    v[j % 2][j / 2] = t;
  };
  auto step = [&](auto S_) {
    constexpr int S = S_;
    wait_vm<RawX<E>::n(S - 2) + (S <= 6 ? VPS : 0) + RawX<E>::n(S - 1)>();
    __builtin_amdgcn_s_barrier();
    MF(16 + S);
    if constexpr (S == RAW_S0) {
      static_assert(RAW_S0 >= 4, "wo blocks");
      if (pj >= 0 && wl == 0 && lane == 0)
        asm volatile("global_store_dwordx2 %0, %1, off sc1\n\ts_nop 1" ::"v"(ws + OFF_POT + (pj / 6) * 128 + (pj % 6) * 8), "v"(tag) : "memory");
    }
    if constexpr (S <= 5) ld.stage((unsigned)(((S + 3) % 4) * SB));
    ld.q_hook(S + 1, ws, lds0, hp);
    static_for<RawX<E>::first(S), RawX<E>::first(S + 1)>(extra);
  };
  const char* hp_ = ws + OFF_HELLO + lane * 32;
  u32x4 a, b;
  if (wl == 1) asm volatile("global_load_dwordx4 %0, %2, off sc1\n\tglobal_load_dwordx4 %1, %2, off offset:16 sc1" : "=&v"(a), "=&v"(b) : "v"(hp_) : "memory");
  else asm volatile("" : "=v"(a), "=v"(b));
  static_for<RAW_S0, 8>(step);
  MF(2);
  if (wl == 1) {
    asm volatile("" : "+v"(a), "+v"(b));
    const unsigned t0 = (unsigned)tag, t1 = (unsigned)(tag >> 32);
#pragma unroll 1
    for (unsigned spin = 0; spin < SPIN_MEM; ++spin) {
      const unsigned bad = ((a[0] ^ t0) | (a[2] ^ t0) | (b[0] ^ t0) | (b[2] ^ t0)) | ((a[1] ^ t1) | (a[3] ^ t1) | (b[1] ^ t1) | (b[3] ^ t1));
      if (!__any(bad != 0u)) break;
      __builtin_amdgcn_s_sleep(4);
      asm volatile("global_load_dwordx4 %0, %2, off sc1\n\tglobal_load_dwordx4 %1, %2, off offset:16 sc1\n\ts_waitcnt vmcnt(0)"
                   : "=&v"(a), "=&v"(b)
                   : "v"(hp_)
                   : "memory");
    }
    lds_wr32(lds0 + (unsigned)(LW_OFF + LW_HELLO * 4), 1u);
    if (bid == 0 && lane == 0) asm volatile("global_store_dwordx2 %0, %1, off sc1\n\ts_nop 1" ::"v"(ws + OFF_TAG), "v"(tag) : "memory");
  }
  MF(11);
  if (rt < 2) lds_spin(lds0 + (unsigned)(LW_OFF + LW_Q * 4), 1u);
  lds_spin(lds0 + (unsigned)K1R_CNT, 8u);
  if constexpr (JOB) {
#pragma unroll
    for (int b = 0; b < 2; ++b)
      asm volatile("s_waitcnt vmcnt(0)"
                   : "+v"(v[b][0]), "+v"(v[b][1]), "+v"(v[b][2]), "+v"(v[b][3]), "+v"(v[b][4]), "+v"(v[b][5]), "+v"(v[b][6]), "+v"(v[b][7])
                   :
                   : "memory");
  }
  MF(12);
  const char* tp = ws + OFF_POT + ct * 128 + (lane < 6 ? lane : 5) * 8;
  u32x2 f;
  asm volatile("global_load_dwordx2 %0, %1, off sc1" : "=v"(f) : "v"(tp) : "memory");
  if constexpr (JOB) {
    static_assert(SB == 4 * 9216, "staging");
#pragma unroll
    for (int b = 0; b < 2; ++b) {
      float cs[8] = {0.f, 0.f, 0.f, 0.f, 0.f, 0.f, 0.f, 0.f};
      tr8_store<288, true>(v[b], da + (unsigned)(b * 18432), cs, std::make_integer_sequence<int, 8>{});
      const f32x4 c0 = {cs[0], cs[1], cs[2], cs[3]}, c1 = {cs[4], cs[5], cs[6], cs[7]};
      const unsigned ca = lds0 + (unsigned)(3 * SB + wl * 2304) + sa * 2u;
      asm volatile("ds_write_b128 %0, %1 offset:%2" ::"v"(ca), "v"(c0), "n"(b * 1152) : "memory");
      asm volatile("ds_write_b128 %0, %1 offset:%2" ::"v"(ca), "v"(c1), "n"(b * 1152 + 16) : "memory");
    }
    lds_inc(lds0 + (unsigned)(LW_OFF + LW_ST * 4));
    lds_spin(lds0 + (unsigned)(LW_OFF + LW_ST * 4), 4u);
    if (isi) {
      const int j = (job & 7) * 4 + wl, ti = j < 16 ? 2 * j + 1 : 2 * (j - 16);
      lds_out<9216>(lds0 + (unsigned)(wl * 9216), ws + OFF_PI + (size_t)(ti * 18 + kb) * 9216, lane);
      f32x4 c[4];
      const unsigned ca = lds0 + (unsigned)(3 * SB + wl * 576) + sa;
#pragma unroll
      for (int i = 0; i < 4; ++i) asm volatile("ds_read_b128 %0, %1 offset:%2" : "=v"(c[i]) : "v"(ca), "n"(i * 2304) : "memory");
      asm volatile("s_waitcnt lgkmcnt(0)" : "+v"(c[0]), "+v"(c[1]), "+v"(c[2]), "+v"(c[3]));
      const f32x4 t = (c[0] + c[1]) + (c[2] + c[3]);
      if (lane < 36) pk_st16(ws + OFF_CSP + ((size_t)kb * F_ + ti * 144) * 4 + sa, __builtin_bit_cast(u32x4, t));
    } else {
      lds_out<4608>(lds0 + (unsigned)(wl * 9216), ws + OFF_PW + (size_t)(wl * 144 + kb) * 4608, lane);
      lds_out<4608>(lds0 + (unsigned)(wl * 9216 + 4608), ws + OFF_PW + (size_t)(wl * 144 + 72 + kb) * 4608, lane);
    }
  }
  MF(8);
  announce(lds0 + (unsigned)(LW_OFF + LW_PK * 4), 4u, ws, CN_PK, isi ? 8 + (job & 3) * 4 + lane : 24 + lane, JOB && lane < (isi ? 4 : 8));
  MF(9);
  if (wl == 0) {
    asm volatile("" : "+v"(f));
#pragma unroll 1
    for (unsigned spin = 0; spin < SPIN_MEM; ++spin) {
      if (!__any(f[0] != (unsigned)tag || f[1] != (unsigned)(tag >> 32))) break;
      __builtin_amdgcn_s_sleep(2);
      asm volatile("global_load_dwordx2 %0, %1, off sc1\n\ts_waitcnt vmcnt(0)" : "=&v"(f) : "v"(tp) : "memory");
    }
  }
}

template <int MT, int NT>
DEVI void k1_gemm(char* smem, const char* ws, const float* __restrict__ csq, int rt, int hp, int mt0, int nt0,
                  bool late, int lane MK_ARG) {
  const unsigned lds0 = (unsigned)(size_t)smem;
  const int g = lane >> 4;
  f32x4 cs[NT];
  unsigned ta[NT];
  const char* csp = uni_ptr((const char*)(csq + (hp & 1) * 4));
  unsigned cad[NT];
#pragma unroll
  for (int nt = 0; nt < NT; ++nt) {
    int c = (nt0 + nt) * 16 + g * 4;
    if (c > 104) c = 104;
    const int part = c / 36, ci = c - part * 36;
    if (CSL) {
      cad[nt] = lds0 + (unsigned)(K1_CS_OFF + (part * 40 + (hp & 1) * 4 + ci) * 4);
      asm volatile("" : "+v"(cad[nt]));
    } else
    asm volatile("global_load_dwordx4 %0, %1, %2 sc1" : "=v"(cs[nt]) : "v"((unsigned)(part * 40 + ci) * 4u), "s"(csp));
    const int to = (nt0 + nt) * 16 + g * 4 > 104 ? 120 : part * 80 + ci * 2 + (ci >= 20 ? 8 : 0);
    ta[nt] = lds0 + (unsigned)(K1_T_OFF + (mt0 * 16 + (lane & 15)) * K1_TP + to);
    asm volatile("" : "+v"(ta[nt]));
  }
  unsigned sa = lds0 + (unsigned)(K1_ST_OFF + (mt0 * 16 + (lane & 15)) * 8);
  asm volatile("" : "+v"(sa));
  f32x4 acc[MT][NT];
#pragma unroll
  for (int mt = 0; mt < MT; ++mt)
#pragma unroll
    for (int nt = 0; nt < NT; ++nt) acc[mt][nt] = f32x4{0.f, 0.f, 0.f, 0.f};
  f32x2 st[MT];
  if (G1E && late) bar();
  if constexpr (NT == 7) cnt_wait(ws, CN_X2, rt * 16 + hp, rt ? 24u : 16u);
  bar();
  MK(1);
  {
    GemmMath<K1C, MT, NT, (NT == 7 ? 5 : 3), (NT == 3 ? 6 : NT), true, 2, false, K1_WS> gm(acc);
    gm.setup(lds0, mt0, nt0, lane, 0u);
    bar();
    if (CSL) {
#pragma unroll
      for (int nt = 0; nt < NT; ++nt) asm volatile("ds_read_b128 %0, %1" : "=v"(cs[nt]) : "v"(cad[nt]));
    }
    gm.first();
    gm.template mid<8, true, AL_K>();
#pragma unroll
    for (int mt = 0; mt < MT; ++mt) asm volatile("ds_read_b64 %0, %1 offset:%2" : "=v"(st[mt]) : "v"(sa), "n"(mt * 128));
    asm volatile("s_waitcnt vmcnt(0)" ::: "memory");
    gm.last();
  }
#pragma unroll
  for (int nt = 0; nt < NT; ++nt) asm volatile("" : "+v"(cs[nt]));
#pragma unroll
  for (int mt = 0; mt < MT; ++mt) asm volatile("" : "+v"(st[mt]));
  MK(2);
#pragma unroll
  for (int mt = 0; mt < MT; ++mt) {
    f32x2 M = {st[mt][0], st[mt][0]}, R = {st[mt][1], st[mt][1]};
    asm volatile("" : "+v"(M), "+v"(R));
#pragma unroll
    for (int nt = 0; nt < NT; ++nt) {
      const f32x2 v0 = R * __builtin_elementwise_fma(M, f32x2{cs[nt][0], cs[nt][1]}, f32x2{acc[mt][nt][0], acc[mt][nt][1]});
      const f32x2 v1 = R * __builtin_elementwise_fma(M, f32x2{cs[nt][2], cs[nt][3]}, f32x2{acc[mt][nt][2], acc[mt][nt][3]});
      acc[mt][nt][0] = __uint_as_float(pack2v(v0));
      acc[mt][nt][1] = __uint_as_float(pack2v(v1));
      const f32x2 pk = {acc[mt][nt][0], acc[mt][nt][1]};
      asm volatile("ds_write_b64 %0, %1 offset:%2" ::"v"(ta[nt]), "v"(pk), "n"(mt * 16 * K1_TP) : "memory");
    }
  }
}

DEVI float vmax(float a, float b) {
  float r;
  asm("v_max_f32 %0, %1, %2" : "=v"(r) : "v"(a), "v"(b));
  return r;
}
struct Attn {
  unsigned ka, kb, va, vb, m0, m1, m2, pm[2], oa, pa;
  f32x4 mk;
  unsigned long long sel[4];
  const char* ob;
  DEVI void setup(unsigned T, char* o, int r0, int hp, int wave, int lane) {
    const int g = lane >> 4, r = lane & 15, blk = wave >> 1, hd = wave & 1;
    const unsigned co = (unsigned)(g < 2 ? hd * 48 + g * 16 : 32);
    ka = T + (unsigned)((blk * 32 + 2 * r) * K1_TP) + co;
    kb = T + (unsigned)((blk * 32 + 2 * (r & 7)) * K1_TP) + co;
    const int vr = 4 * g + ((lane >> 2) & 3);
    va = T + (unsigned)((blk * 32 + 2 * vr) * K1_TP + (lane & 3) * 8);
    vb = T + (unsigned)((blk * 32 + 2 * (vr & 7)) * K1_TP + (lane & 3) * 8);
    m0 = (g < 2 || (g == 2 && hd == 0)) ? ~0u : 0u;
    m1 = (g < 2 || (g == 2 && hd == 1)) ? ~0u : 0u;
    m2 = g < 2 ? ~0u : 0u;
    pm[0] = pm[1] = 0u;
    const float ci = __uint_as_float(vconst(0xff800000u));
    const unsigned ch = vconst(0xffffu);
#pragma unroll
    for (int e = 0; e < 4; ++e) {
      const int w1 = r + 8 - 4 * g - e;
      mk[e] = (w1 & 15) <= 7 ? 0.f : ci;
      sel[e] = __ballot(w1 >= 8);
      if (w1 >= 8) pm[e >> 1] |= ch << (16 * (e & 1));
    }
    oa = (unsigned)((8 + hp * 4 + hd * 2 + (g >> 1)) * 1024 + ((blk & 1) * 32 + 2 * r + (g & 1)) * 16);
    pa = ((oa & 1023u) >> 2) + (unsigned)((hp * 2 + hd) * 256);
    ob = uni_ptr(o + (size_t)((r0 >> 6) + (wave >> 2)) * (72 * 1024));
    asm volatile("" : "+v"(ka), "+v"(kb), "+v"(va), "+v"(vb), "+v"(m0), "+v"(m1), "+v"(m2), "+v"(pm[0]), "+v"(pm[1]), "+v"(oa));
    asm volatile("" : "+v"(mk), "+v"(pa), "+s"(ob), "+s"(sel[0]), "+s"(sel[1]), "+s"(sel[2]), "+s"(sel[3]));
  }
  DEVI void run(int r0, int hp, int wave, int lane) {
    const int hd = wave & 1;
    u32x4 q[2], k0[2], k1[2];
    u32x2 v[2][2][2];
#pragma unroll
    for (int p = 0; p < 2; ++p) {
      asm volatile("ds_read_b128 %0, %1 offset:%2" : "=v"(q[p]) : "v"(ka), "n"((16 + p) * K1_TP) : "memory");
      asm volatile("ds_read_b128 %0, %1 offset:%2" : "=v"(k0[p]) : "v"(ka), "n"(p * K1_TP + 80) : "memory");
      asm volatile("ds_read_b128 %0, %1 offset:%2" : "=v"(k1[p]) : "v"(kb), "n"((32 + p) * K1_TP + 80) : "memory");
    }
    const unsigned vh = (unsigned)hd * 48u;
#pragma unroll
    for (int p = 0; p < 2; ++p) {
      asm volatile("ds_read_b64_tr_b16 %0, %1 offset:%2" : "=v"(v[p][0][0]) : "v"(va + vh), "n"(p * K1_TP + 160) : "memory");
      asm volatile("ds_read_b64_tr_b16 %0, %1 offset:%2" : "=v"(v[p][0][1]) : "v"(vb + vh), "n"((32 + p) * K1_TP + 160) : "memory");
      asm volatile("ds_read_b64_tr_b16 %0, %1 offset:%2" : "=v"(v[p][1][0]) : "v"(va), "n"(p * K1_TP + 192) : "memory");
      asm volatile("ds_read_b64_tr_b16 %0, %1 offset:%2" : "=v"(v[p][1][1]) : "v"(vb), "n"((32 + p) * K1_TP + 192) : "memory");
    }
    const u32x4 one4 = {vconst(0x3f803f80u), vconst(0x3f803f80u), vconst(0x3f803f80u), vconst(0x3f803f80u)};
    const s16x8 ones = __builtin_bit_cast(s16x8, one4);
    const float c = 0.23570226039551584f * 1.4426950408889634f;
    const f32x4 z = {0.f, 0.f, 0.f, 0.f};
    f32x4 sc[2][2];
#pragma unroll
    for (int p = 0; p < 2; ++p) {
      asm volatile("s_waitcnt lgkmcnt(%3)" : "+v"(q[p]), "+v"(k0[p]), "+v"(k1[p]) : "n"(11 - 3 * p));
      const s16x8 qf = __builtin_bit_cast(s16x8, q[p] & u32x4{m0, m1, m2, m2});
      sc[p][0] = __builtin_amdgcn_mfma_f32_16x16x32_bf16(__builtin_bit_cast(s16x8, k0[p]), qf, mk, 0, 0, 0);
      sc[p][1] = __builtin_amdgcn_mfma_f32_16x16x32_bf16(__builtin_bit_cast(s16x8, k1[p]), qf, mk, 0, 0, 0);
      __builtin_amdgcn_sched_barrier(0);
    }
    if (__builtin_expect(r0 == 0 && wave < 2, 0)) {
#pragma unroll
      for (int p = 0; p < 2; ++p)
#pragma unroll
        for (int e = 0; e < 4; ++e)
          if (lane < 32) sc[p][0][e] = mk[e] - 4.242640687e9f;
    }
    float lg[2][4], mx[2];
#pragma unroll
    for (int p = 0; p < 2; ++p) {
#pragma unroll
      for (int e = 0; e < 4; ++e) lg[p][e] = __builtin_amdgcn_inverse_ballot_w64(sel[e]) ? sc[p][1][e] : sc[p][0][e];
      asm("v_max3_f32 %0, %1, %2, %3\n\tv_max_f32 %0, %0, %4" : "=&v"(mx[p]) : "v"(lg[p][0]), "v"(lg[p][1]), "v"(lg[p][2]), "v"(lg[p][3]));
    }
    {
      const auto a = __builtin_amdgcn_permlane32_swap(__float_as_uint(mx[0]), __float_as_uint(mx[1]), false, false);
      const unsigned b = __float_as_uint(vmax(__uint_as_float(a[0]), __uint_as_float(a[1])));
      const auto d = __builtin_amdgcn_permlane16_swap(b, b, false, false);
      const unsigned f = __float_as_uint(vmax(__uint_as_float(d[0]), __uint_as_float(d[1])) * -c);
      const auto h = __builtin_amdgcn_permlane32_swap(f, f, false, false);
      mx[0] = __uint_as_float(h[0]);
      mx[1] = __uint_as_float(h[1]);
    }
#pragma unroll
    for (int p = 0; p < 2; ++p)
#pragma unroll
      for (int e = 0; e < 4; ++e) lg[p][e] = __builtin_amdgcn_exp2f(__builtin_fmaf(lg[p][e], c, mx[p]));
    asm volatile("s_waitcnt lgkmcnt(0)"
                 : "+v"(v[0][0][0]), "+v"(v[0][0][1]), "+v"(v[0][1][0]), "+v"(v[0][1][1]), "+v"(v[1][0][0]), "+v"(v[1][0][1]),
                   "+v"(v[1][1][0]), "+v"(v[1][1][1]));
    __builtin_amdgcn_sched_barrier(0);
    f32x4 su[2], o0[2], o1[2];
    s16x8 pp[2];
#pragma unroll
    for (int p = 0; p < 2; ++p) {
      const unsigned p0 = pack2(lg[p][0], lg[p][1]), p1 = pack2(lg[p][2], lg[p][3]);
      pp[p] = __builtin_bit_cast(s16x8, u32x4{p0 & ~pm[0], p1 & ~pm[1], p0 & pm[0], p1 & pm[1]});
    }
    __builtin_amdgcn_sched_barrier(0);
#pragma unroll
    for (int p = 0; p < 2; ++p) {
      const s16x8 pf = pp[p];
      const u32x4 v0 = {v[p][0][0][0], v[p][0][0][1], v[p][0][1][0], v[p][0][1][1]};
      const u32x4 v1 = {v[p][1][0][0], v[p][1][0][1], v[p][1][1][0], v[p][1][1][1]};
      su[p] = __builtin_amdgcn_mfma_f32_16x16x32_bf16(ones, pf, z, 0, 0, 0);
      o0[p] = __builtin_amdgcn_mfma_f32_16x16x32_bf16(__builtin_bit_cast(s16x8, v0), pf, z, 0, 0, 0);
      o1[p] = __builtin_amdgcn_mfma_f32_16x16x32_bf16(__builtin_bit_cast(s16x8, v1), pf, z, 0, 0, 0);
    }
    __builtin_amdgcn_sched_barrier(0);
    u32x2 y[2], yl;
    unsigned hm = (unsigned)hd - 1u;
    asm volatile("" : "+s"(hm));
#pragma unroll
    for (int p = 0; p < 2; ++p) {
      const float inv = __builtin_amdgcn_rcpf(su[p][0]);
      y[p] = u32x2{pack2(o0[p][0] * inv, o0[p][1] * inv), pack2(o0[p][2] * inv, o0[p][3] * inv)};
      const unsigned la = pack2(o1[p][0] * inv, o1[p][1] * inv), lb = pack2(o1[p][2] * inv, o1[p][3] * inv);
      yl[p] = (la & hm) | (lb & ~hm);
    }
    const auto s0 = __builtin_amdgcn_permlane16_swap(y[0][0], y[1][0], false, false);
    const auto s1 = __builtin_amdgcn_permlane16_swap(y[0][1], y[1][1], false, false);
    const u32x4 w = {s0[0], s1[0], s0[1], s1[1]};
    asm volatile("global_store_dwordx4 %0, %1, %4 sc1\n\ts_mov_b64 exec, 0xffff\n\tglobal_store_dwordx2 %2, %3, %4 sc1\n\ts_mov_b64 exec, -1\n\ts_nop 0" ::"v"(oa),
                 "v"(w), "v"(pa), "v"(yl), "s"(ob)
                 : "memory");
  }
};

#define NL_MAIN 706
DEVI void code_again(const char* code0, int bid) {
  constexpr int PER = (2 * NL_MAIN + 31) / 32;
  const int lane = lane_id();
  int li = (bid >> 3) * PER + (lane < PER ? lane : PER - 1);
  if (li > 2 * NL_MAIN - 1) li = 2 * NL_MAIN - 1;
  unsigned t;
  asm volatile("global_load_dword %0, %1, %2\n\ts_waitcnt vmcnt(0)" : "=&v"(t) : "v"((unsigned)li * 64u), "s"(code0) : "memory");
}
DEVI void math_start(char* smem, const char* code0, const u16* b_sp, const u16* wqkv, char* ws, int bid, int wave, int lane) {
  const unsigned lds0 = (unsigned)(size_t)smem;
  const int tid = wave * 64 + lane, idx = bid >> 3;
  if (wave < 3) {
    static_assert(2 * SZ_ZP == (size_t)256 * 2048 && NCNT <= 16, "mark split");
    const u32x4 mk = {0xffffffffu, 0xffffffffu, 0xffffffffu, 0xffffffffu};
    char* c = ws + OFF_CNT + (size_t)((lane >> 3) * 256 + bid) * 128 + (lane & 7) * 16;
    pk_st16(wave < 2 ? ws + OFF_ZP + (size_t)((bid * 2 + wave) * 64 + lane) * 16 : c, mk);
    if (wave == 2 && lane < (NCNT - 8) * 8) pk_st16(c + 8 * 256 * 128, mk);
  }
  if (wave == 3) {
    const int li = idx * 54 + (lane < 54 ? lane : 53);
    touch64(uni_ptr((const char*)wqkv), (unsigned)(li * 1152 + 512), lds0 + (unsigned)(K1R_DUMMY + 768));
  }
  if (!CT0 && wave == 4) {
    constexpr int PER = (2 * NL_MAIN + 31) / 32;
    static_assert(PER <= 64, "one load");
    int li = idx * PER + (lane < PER ? lane : PER - 1);
    if (li > 2 * NL_MAIN - 1) li = 2 * NL_MAIN - 1;
    touch64(code0, (unsigned)li * 64u, lds0 + (unsigned)(K1R_DUMMY + 768));
  }
  if (wave == 5 && idx == 31) touch64(uni_ptr((const char*)b_sp), (unsigned)lane * 64u, lds0 + (unsigned)(K1R_DUMMY + 768));
  const u32x4 ones = {0x3f803f80u, 0x3f803f80u, 0x3f803f80u, 0x3f803f80u};
  if (tid < 256) *reinterpret_cast<u32x4*>(smem + (tid >> 6) * K1R::SB + K1R::XB + (tid & 63) * 288 + 240) = ones;
  else if (tid < 288) *reinterpret_cast<u32x4*>(smem + ((tid - 256) >> 3) * K1R::SB + (tid & 7) * 16) = ones;
  else if (tid < 296) *reinterpret_cast<u32x4*>(smem + LW_OFF + (tid - 288) * 16) = u32x4{0u, 0u, 0u, 0u};
  asm volatile("s_waitcnt lgkmcnt(0)" ::: "memory");
}

#define SAFE_OUT(L)
#define NOP_(n) "=s"(n)
template <int NST, int NS, int NSW, int WOFF, int XW, int X0, int S0, unsigned BAR>
struct ResX {
  static constexpr int WS = 4 * 4608, R = 10, V1 = 5 * NS, NV = V1 + 10 * (NST - NS), NG = (NSW + 2) / 3;
  s16x8 xf[XW][2];
  s16x8 wf[R];
  unsigned wrd[2][NG], xo, ea, fa, fv, tag;
  const char* xp[3];
  f32x4 (&acc)[2][5];
  DEVI ResX(f32x4 (&a)[2][5]) : acc(a) {}
  static constexpr int st(int V) { return V < V1 ? V / 5 : NS + (V - V1) / 10; }
  static constexpr int hf(int V) { return V < V1 ? 0 : ((V - V1) / 5) & 1; }
  static constexpr bool first(int V) { return V < V1 ? V % 5 == 0 : (V - V1) % 10 == 0; }
  static constexpr int wreq(int V) {
    int n = V + 5;
    if (V >= V1 - 4) n += V < V1 ? V - (V1 - 5) : 4;
    return n < NV ? n : NV;
  }
  static constexpr int XU = V1 + 1, XF = V1 + 11, XR = V1 + 15, XC = V1 + 21;
  static constexpr int oth(int V) { return NS && ((V >= XU && V <= XF && (V - XU) % 2 == 0) || V == XR || (V > XC && V <= XC + 5)) ? 1 : 0; }
  static constexpr int newer(int V) {
    const int F = V + 1 < NV ? V + 1 : V;
    int v = V - 1, n = 0;
    for (; v >= 0 && wreq(v - 1) > F; --v) n += wreq(v) - wreq(v - 1) + oth(v);
    return n + wreq(v) - 1 - F + (v >= 0 ? oth(v) : 0);
  }
  static constexpr int xreq(int T) {
    int n = X0 + 2 * (T + 1);
    if (n > T + XW) n = T + XW;
    if (NS && T <= 11 && n > 18) n = 18;
    if (T < 0) n = X0;
    return n < NST ? n : NST;
  }
  static_assert(xreq(NS - 2) == NST && (NS == 0 || XC + 5 < NV), "the last steps only compute and swap");
  template <int T, int M>
  DEVI void xld() {
    asm volatile("global_load_dwordx4 %0, %1, %2 sc1" : "=v"(xf[T % XW][M]) : "v"(xo), "s"(xp[M == 0 ? 0 : (T < NS ? 1 : 2)]));
    if constexpr (M == 1 && T + 1 < NST) asm volatile("v_add_u32 %0, 0x2000, %0" : "+v"(xo));
  }
  template <int V>
  DEVI void rd() {
    constexpr int T = st(V), sl = (T / 2 + S0) % NSW;
    asm volatile("ds_read_b128 %0, %1 offset:%2" : "=v"(wf[V % R]) : "v"(wrd[hf(V)][sl / 3]), "n"((sl % 3) * WS + (T & 1) * 9216 + (V % 5) * 32));
  }
  template <int V, int M, int H>
  DEVI void mf() {
    acc[M][V % 5] = __builtin_amdgcn_mfma_f32_16x16x32_bf16(wf[V % R], xf[st(V) % XW][H], acc[M][V % 5], 0, 0, 0);
    __builtin_amdgcn_sched_barrier(0);
  }
  template <int V>
  DEVI void unit() {
    constexpr int T = st(V), n = V % 5, nx = xreq(T) - xreq(T - 1), nw = wreq(V) - wreq(V - 1);
    constexpr int vm = 2 * (xreq(T - 1) - 1 - T);
    static_assert(newer(V) <= 15, "lgkmcnt range");
    if constexpr (first(V) && V % 2 == 0) asm volatile("s_waitcnt vmcnt(%0) lgkmcnt(%1)" ::"n"(vm), "n"(newer(V)));
    else if constexpr (first(V)) asm volatile("s_waitcnt vmcnt(%0)" ::"n"(vm));
    else if constexpr (V % 2 == 0) asm volatile("s_waitcnt lgkmcnt(%0)" ::"n"(newer(V)));
    __builtin_amdgcn_sched_barrier(0);
    if constexpr (V < V1) mf<V, V & 1, V & 1>();
    else mf<V, 0, hf(V)>();
    if constexpr (nw > 0) rd<wreq(V - 1)>();
    __builtin_amdgcn_sched_barrier(0);
    if constexpr (V < V1) {
      mf<V, 1 - (V & 1), 1 - (V & 1)>();
      if constexpr (nw > 1) rd<wreq(V - 1) + 1>();
      if constexpr (nx == 2 && n < 4) xld<xreq(T - 1) + n / 2, n % 2>();
      if constexpr (nx == 1 && n == (T & 1)) xld<xreq(T - 1), 0>();
      if constexpr (nx == 1 && n == (T & 1) + 2) xld<xreq(T - 1), 1>();
      if constexpr (n == 4 && (T & 1) && ((BAR >> (T / 2)) & 1u)) bar();
    } else if constexpr (NS > 0) {
      if constexpr (oth(V) && V < XF) asm volatile("ds_write_b128 %0, %1 offset:%2" ::"v"(ea), "v"(acc[1][(V - XU) / 2]), "n"((V - XU) * 512));
      if constexpr (V == XF) asm volatile("ds_write_b32 %0, %1" ::"v"(fa), "v"(fv));
      if constexpr (V == XF + 2) asm volatile("v_xor_b32 %0, 4, %0" : "+v"(fa));
      if constexpr (V == XR) asm volatile("ds_read_b32 %0, %1" : "+v"(fv) : "v"(fa));
      if constexpr (V == XR + 2) asm volatile("v_xor_b32 %0, 0x2000, %0" : "+v"(ea));
      if constexpr (V == XC) {
        static_assert(wreq(XR) <= XC + 1 || true, "");
        unsigned c = 1u << 18;
        asm volatile(
            "s_waitcnt lgkmcnt(%4)\n\tv_cmp_ne_u32 vcc, %3, %0\n\ts_cbranch_vccnz .Lw%=\n\t.subsection 1\n.Lw%=:\n\tds_read_b32 %0, %2\n\t"
            "s_waitcnt lgkmcnt(0)\n\tv_cmp_ne_u32 vcc, %3, %0\n\t" SAFE_OUT(".Lr%=") "s_cbranch_vccnz .Lw%=\n\ts_branch .Lr%=\n\t.subsection 0\n.Lr%=:"
            : "+v"(fv), NOP_(c)
            : "v"(fa), "s"(tag), "n"(wreq(XC) - wreq(XR))
            : "vcc", "scc");
      }
      if constexpr (oth(V) && V > XC) asm volatile("ds_read_b128 %0, %1 offset:%2" : "=v"(acc[1][V - XC - 1]) : "v"(ea), "n"((V - XC - 1) * 1024));
    }
    __builtin_amdgcn_sched_barrier(0);
  }
  template <int... V>
  DEVI void units(std::integer_sequence<int, V...>) {
    (unit<V>(), ...);
  }
  DEVI void run() {
    units(std::make_integer_sequence<int, NV>{});
    if constexpr (NS > 0) {
      asm volatile("s_waitcnt lgkmcnt(0)" : "+v"(acc[1][0]), "+v"(acc[1][1]), "+v"(acc[1][2]), "+v"(acc[1][3]), "+v"(acc[1][4]));
#pragma unroll
      for (int i = 0; i < 5; ++i) acc[0][i] += acc[1][i];
    }
  }
  template <int... V>
  DEVI void pro(std::integer_sequence<int, V...>) {
    (rd<V>(), ...);
  }
  DEVI void prow() { pro(std::make_integer_sequence<int, wreq(-1)>{}); }
  template <int T0, int... T>
  DEVI void prox(std::integer_sequence<int, T...>) {
    ((xld<T0 + T, 0>(), xld<T0 + T, 1>()), ...);
  }
  DEVI void setup(unsigned lds0, const char* xbase, int wave, int lane, unsigned red, unsigned nth) {
    const int g = lane >> 4, r = lane & 15, kq = wave & 1;
    xp[0] = xbase + wave * 256;
    xp[1] = xbase + (wave ^ 1) * 256;
    xp[2] = xp[0] + (kq ? -4096 : 4096);
    xo = (unsigned)((4 * kq + g) * 1024 + r * 16);
#pragma unroll
    for (int i = 0; i < 2 * NG; ++i) {
      wrd[i / NG][i % NG] = lds0 + (unsigned)(WOFF + (i % NG) * 3 * WS + (kq ^ (i / NG)) * 4608 + (2 * (r & 7) + 16 * (g & 1) + (g >> 1)) * 144 + (r >> 3) * 16);
      if (i < NG || NS == 0 || i % NG == ((NS / 2 + S0) % NSW) / 3) asm volatile("" : "+v"(wrd[i / NG][i % NG]));
    }
    asm volatile("" : "+v"(xo));
    if constexpr (NS > 0) {
      ea = lds0 + red + (unsigned)((wave ^ 1) * 8192 + lane * 16);
      fa = lds0 + (unsigned)(LW_OFF + (LW_PAIR + wave) * 4);
      fv = tag = nth;
      asm volatile("" : "+v"(ea), "+v"(fa), "+v"(fv));
    }
  }
  template <int N>
  DEVI void init(u32x2 (&xv)[5]) {
    asm volatile("s_waitcnt vmcnt(%5)" : "+v"(xv[0]), "+v"(xv[1]), "+v"(xv[2]), "+v"(xv[3]), "+v"(xv[4]) : "n"(N));
#pragma unroll
    for (int i = 0; i < 5; ++i) {
      acc[0][i] = f32x4{bf_lo(xv[i][0]), bf_hi(xv[i][0]), bf_lo(xv[i][1]), bf_hi(xv[i][1])};
      asm volatile("" : "+v"(acc[0][i]));
      acc[1][i] = f32x4{0.f, 0.f, 0.f, 0.f};
    }
  }
};

#define P5_XW 8
#define P5_X0 5
constexpr unsigned P5_BAR = 0x2aaau;
constexpr int P5_W = 15 * 1024;
constexpr int P2_W = K1_ST_OFF - 18 * 4608;
constexpr int P5_RED = 0;
static_assert(P5_RED % 16384 == 0 && P5_RED + 3 * 8192 + 5120 <= P5_W + 18432, "exchange area");
static_assert(P2_W >= K1_T_OFF + 144 * K1_TP + 64 && P2_W % 16 == 0 && P5_W + 8 * 18432 <= LW_OFF, "LDS placement");

struct ResIn {
  const char* xp;
  unsigned o, o2, st;
  DEVI void setup(const char* src, bool rm, int rb, int ct, int wave, int lane) {
    const int g = lane >> 4, g2 = g & 1, rl = wave * 16 + (lane & 15);
    xp = uni_ptr(rm ? src + ((size_t)rb * 64 * D_ + ct * 72) * 2 : src + (size_t)(rb * 72 + ct * 9) * 1024);
    o = rm ? (unsigned)(rl * (D_ * 2) + g * 8) : (unsigned)((g >> 1) * 1024 + rl * 16 + g2 * 8);
    o2 = rm ? (unsigned)(rl * (D_ * 2) + g2 * 8 + 128) : (unsigned)(8192 + rl * 16 + g2 * 8);
    st = rm ? 32u : 2048u;
    asm volatile("" : "+v"(o), "+v"(o2));
    asm volatile("" : "+s"(xp));
  }
  DEVI void load(u32x2 (&xv)[5]) {
#pragma unroll
    for (int i = 0; i < 4; ++i) asm volatile("global_load_dwordx2 %0, %1, %2 sc1" : "=v"(xv[i]) : "v"(o), "s"(xp + i * st));
    asm volatile("global_load_dwordx2 %0, %1, %2 sc1" : "=v"(xv[4]) : "v"(o2), "s"(xp));
  }
};

DEVI void p2_gemm(f32x4 (&acc)[2][5], u32x2 (&xv)[5], unsigned lds0, const char* ob, int layer, int wave, int lane MK_ARG) {
  ResX<9, 0, 5, P2_W, 9, 9, 0, 0u> gm(acc);
  gm.setup(lds0, ob, wave, lane, 0u, 0u);
  unsigned a = (gm.xo & ~1023u) | ((gm.xo & 1023u) >> 2);
  gm.xo += 8192u;
  asm volatile("" : "+v"(a), "+v"(gm.xo));
  const char* pp[2] = {gm.xp[0] - wave * 192, gm.xp[2] - wave * 192};
  place<AL_P2>();
  bar();
  MK(6);
  unsigned e[2][4];
#pragma unroll
  for (int d = 0; d < 8; ++d) {
    if (NOPL && (d & 3)) e[d / 4][d % 4] = 0x3c003c00u;
    else asm volatile("global_load_dword %0, %1, %2 offset:%3 sc1" : "=v"(e[d / 4][d % 4]) : "v"(a), "s"(pp[d / 4]), "n"((d % 4) * 256));
  }
  gm.template prox<1>(std::make_integer_sequence<int, 8>{});
  lds_inc(lds0 + (unsigned)(LW_OFF + (LW_T + layer) * 4));
  gm.prow();
  gm.template init<(NOPL ? 18 : 24)>(xv);
  asm volatile("s_waitcnt vmcnt(16)"
               : "+v"(e[0][0]), "+v"(e[0][1]), "+v"(e[0][2]), "+v"(e[0][3]), "+v"(e[1][0]), "+v"(e[1][1]), "+v"(e[1][2]), "+v"(e[1][3]));
  gm.xf[0][0] = __builtin_bit_cast(s16x8, u32x4{e[0][0], e[0][1], e[0][2], e[0][3]});
  gm.xf[0][1] = __builtin_bit_cast(s16x8, u32x4{e[1][0], e[1][1], e[1][2], e[1][3]});
  __builtin_amdgcn_sched_barrier(0);
  gm.run();
}

struct ResOut {
  const char *xp[3], *sp;
  unsigned xo, so;
  DEVI void setup(char* xim, char* xs, u16* out, bool fin, int rb, int ct, int wave, int lane) {
    const int g = lane >> 4, rl = wave * 16 + (lane & 15);
    xp[0] = uni_ptr(fin ? (const char*)(out + (size_t)rb * 64 * D_ + ct * 72) : xim + (size_t)(rb * 72 + ct * 9) * 1024);
    xp[1] = xp[0] + (fin ? 64 : 4096);
    xp[2] = xp[0] + (fin ? 128 : 8192);
    sp = uni_ptr(xs + (size_t)(rb * 8 + ct) * 512);
    xo = (unsigned)(((g & 1) * 2 + (g >> 1)) * (fin ? 16 : 1024) + rl * (fin ? D_ * 2 : 16));
    so = (unsigned)(rl * 8);
    asm volatile("" : "+v"(xo), "+v"(so), "+s"(xp[0]), "+s"(xp[1]), "+s"(xp[2]), "+s"(sp));
  }
  DEVI void run(const f32x4 (&a)[5], bool fin) {
    u32x2 y[5];
    u32x4 v[3];
#pragma unroll
    for (int nt = 0; nt < 5; ++nt) y[nt] = u32x2{pack2(a[nt][0], a[nt][1]), pack2(a[nt][2], a[nt][3])};
#pragma unroll
    for (int i = 0; i < 3; ++i) {
      const auto s0 = __builtin_amdgcn_permlane16_swap(y[2 * i][0], y[i < 2 ? 2 * i + 1 : 4][0], false, false);
      const auto s1 = __builtin_amdgcn_permlane16_swap(y[2 * i][1], y[i < 2 ? 2 * i + 1 : 4][1], false, false);
      v[i] = u32x4{s0[0], s1[0], s0[1], s1[1]};
    }
    if (fin) {
#define OUTM 0
#define OUTST ""
      asm volatile("global_store_dwordx4 %0, %1, %4" OUTST "\n\tglobal_store_dwordx4 %0, %2, %5" OUTST "\n\ts_mov_b64 exec, 0xffff\n\tglobal_store_dwordx4 %0, %3, %6" OUTST "\n\t"
                   "s_mov_b64 exec, -1\n\ts_nop 0" ::"v"(xo), "v"(v[0]), "v"(v[1]), "v"(v[2]), "s"(xp[0]), "s"(xp[1]), "s"(xp[2])
                   : "memory");
      return;
    }
    asm volatile("global_store_dwordx4 %0, %1, %4 sc1\n\tglobal_store_dwordx4 %0, %2, %5 sc1\n\ts_mov_b64 exec, 0xffff\n\t"
                 "global_store_dwordx4 %0, %3, %6 sc1\n\ts_mov_b64 exec, -1\n\ts_nop 0" ::"v"(xo), "v"(v[0]), "v"(v[1]), "v"(v[2]),
                 "s"(xp[0]), "s"(xp[1]), "s"(xp[2])
                 : "memory");
    const u32x4 one4 = {vconst(0x3f803f80u), vconst(0x3f803f80u), vconst(0x3f803f80u), vconst(0x3f803f80u)};
    const bool lo = __builtin_amdgcn_inverse_ballot_w64(0xffffull);
    v[2] = u32x4{lo ? v[2][0] : 0u, lo ? v[2][1] : 0u, lo ? v[2][2] : 0u, lo ? v[2][3] : 0u};
    f32x4 s = {0.f, 0.f, 0.f, 0.f}, q = s;
#pragma unroll
    for (int i = 0; i < 3; ++i) {
      const s16x8 u = __builtin_bit_cast(s16x8, v[i]);
      q = __builtin_amdgcn_mfma_f32_16x16x32_bf16(u, u, q, 0, 0, 0);
      s = __builtin_amdgcn_mfma_f32_16x16x32_bf16(__builtin_bit_cast(s16x8, one4), u, s, 0, 0, 0);
    }
    float q0 = q[0], q1 = q[1], q2 = q[2], q3 = q[3];
    const bool b0 = __builtin_amdgcn_inverse_ballot_w64(0xaaaaaaaaaaaaaaaaull), b1 = __builtin_amdgcn_inverse_ballot_w64(0xccccccccccccccccull);
    q0 = b0 ? q1 : q0;
    q2 = b0 ? q3 : q2;
    asm("" : "+v"(q0), "+v"(q2));
    const f32x2 sq = {s[0], b1 ? q2 : q0};
    asm volatile("s_mov_b64 exec, %3\n\tglobal_store_dwordx2 %0, %1, %2 sc1\n\ts_mov_b64 exec, -1" ::"v"(so), "v"(sq), "s"(sp), "s"(0xf0000f0000f0000full) : "memory");
  }
};

using K3C = GemmCfg<128, 144>;
constexpr int K3_Z1_OFF = K3C::LDS_BYTES;
constexpr int K3_C2_OFF = K3_Z1_OFF + K3C::XB;
constexpr int K3_ZC_OFF = K3_C2_OFF + 2048;
constexpr int K3_DM_OFF = K3_ZC_OFF + 1024;
constexpr int K3_ST_OFF = K3_Z1_OFF + 4 * 5120;
constexpr int K3_C1_OFF = K3_ST_OFF + 2048;
constexpr int K3_BS_OFF = K3_C1_OFF + 576;
static_assert(K3_BS_OFF + 512 <= LW_OFF, "K3 LDS");
static_assert(P5_W + 19 * 1024 == K3C::SB && K3C::SB == 34 * 1024, "w_out ring");
constexpr int K3_D = 4;

DEVI void red4(float (&t)[4], float& s, float& q) {
#pragma unroll
  for (int j = 0; j < 2; ++j) {
    const auto p = __builtin_amdgcn_permlane16_swap(__float_as_uint(t[j]), __float_as_uint(t[2 + j]), false, false);
    const unsigned v = __float_as_uint(__uint_as_float(p[0]) + __uint_as_float(p[1]));
    const auto w = __builtin_amdgcn_permlane32_swap(v, v, false, false);
    t[j] = __uint_as_float(w[0]) + __uint_as_float(w[1]);
    asm volatile("" : "+v"(t[j]));
  }
  s = t[0];
  q = t[1];
}
DEVI void red8(const f32x4& a, const f32x4& b, float& s, float& q) {
  float t[4];
#pragma unroll
  for (int j = 0; j < 4; ++j) {
    t[j] = a[j] + b[j];
    asm volatile("" : "+v"(t[j]));
  }
  red4(t, s, q);
}
DEVI void red16(const f32x4& a, const f32x4& b, const f32x4& c, const f32x4& d, float& s, float& q) {
  float t[4];
#pragma unroll
  for (int j = 0; j < 4; ++j) {
    float u = a[j] + b[j], v = c[j] + d[j];
    asm volatile("" : "+v"(u), "+v"(v));
    t[j] = u + v;
    asm volatile("" : "+v"(t[j]));
  }
  red4(t, s, q);
}

constexpr int b_k(int s) { return s < 2 ? s : (s < 4 ? 5 + s : (s < 6 ? 1 + s : (s == 6 ? 4 : s - 5))); }
template <bool B>
struct UMath {
  static constexpr int D = 4, R = 6, NV = 162, SB = K3C::SB, P = K3C::P;
  s16x8 wf[R], xf[2][2];
  s16x8 (&xc)[4][2][2];
  f32x4 (&acc)[2][9];
  unsigned xa, wa;
  unsigned fa, fw, fv;
  DEVI UMath(f32x4 (&a)[2][9], s16x8 (&x)[4][2][2]) : xc(x), acc(a) {}
  static constexpr int wslot(int s) { return B ? (s + 1) % 4 : s % 4; }
  static constexpr int xslot(int s) { return B ? b_k(s) % 5 : s % 5; }
  static constexpr bool res(int q) { return !B || b_k(q / 2) >= 4; }
#define BPS 9
  static constexpr bool chk(int s) { return s >= 3 && s < 7 && s != BPS; }
  static constexpr bool xop(int v) { return v + D < NV && (v + D) % 9 < 2 && res((v + D) / 9); }
  static constexpr int ops1(int v) { return (xop(v) || (B && ((v % 18 == 9 && chk(v / 18)) || (v % 18 == 0 && v > 0)))) ? 1 : 0; }
  static constexpr int newer(int V) {
    int v0 = (V % 9 == 8 ? V : V + 1) - D, n = 0;
    if (v0 >= 0 && !(V % 9 == 0 && res(V / 9))) n += ops1(v0);
    for (int v = v0 + 1; v < V; ++v)
      if (v >= 0) n += (v + D < NV ? 1 : 0) + ops1(v);
    return n;
  }
  template <int V>
  DEVI void wread() {
    asm volatile("ds_read_b128 %0, %1 offset:%2" : "=v"(wf[V % R]) : "v"(wa), "n"((wslot(V / 18) & 1) * SB + ((V % 18) / 9) * 32 * P + (V % 9) * 32));
  }
  template <int Q, int M>
  DEVI void xread() {
    asm volatile("ds_read_b128 %0, %1 offset:%2" : "=v"(xf[Q & 1][M]) : "v"(xa), "n"((xslot(Q / 2) & 1) * SB + (Q & 1) * 8192 + M * (B ? 256 : 64)));
  }
  template <int V, int M_>
  DEVI void mf() {
    constexpr int M = ((V % 9) & 1) ? 1 - M_ : M_, q = V / 9;
    if constexpr (!B) acc[M][V % 9] = __builtin_amdgcn_mfma_f32_16x16x32_bf16(xf[q & 1][M], wf[V % R], acc[M][V % 9], 0, 0, 0);
    else if constexpr (res(q)) acc[M][V % 9] = __builtin_amdgcn_mfma_f32_16x16x32_bf16(wf[V % R], xf[q & 1][M], acc[M][V % 9], 0, 0, 0);
    else acc[M][V % 9] = __builtin_amdgcn_mfma_f32_16x16x32_bf16(wf[V % R], xc[b_k(q / 2)][q & 1][M], acc[M][V % 9], 0, 0, 0);
    __builtin_amdgcn_sched_barrier(0);
  }
  template <int V>
  DEVI void unit() {
    constexpr int U = V % 18, s = V / 18;
    static_assert(newer(V) <= 15, "lgkmcnt range");
    if constexpr ((V % 9) % 2 == 0 && V > 0) asm volatile("s_waitcnt lgkmcnt(%0)" ::"n"(newer(V)));
    __builtin_amdgcn_sched_barrier(0);
    mf<V, 0>();
    if constexpr (V + D < NV) wread<V + D>();
    if constexpr (s < 8 && U == 13 && (wslot(s + 1) >> 1) != (wslot(s) >> 1))
      asm volatile("v_add_u32 %0, %1, %0" : "+v"(wa) : "n"(((wslot(s + 1) >> 1) - (wslot(s) >> 1)) * 2 * SB));
    __builtin_amdgcn_sched_barrier(0);
    mf<V, 1>();
    if constexpr (xop(V)) xread<(V + D) / 9, (V + D) % 9>();
    if constexpr (s < 8) {
      if constexpr (U == 11 && res(2 * s + 2) && (xslot(s + 1) >> 1) != (xslot(s) >> 1))
        asm volatile("v_add_u32 %0, %1, %0" : "+v"(xa) : "n"(((xslot(s + 1) >> 1) - (xslot(s) >> 1)) * 2 * SB));
      if constexpr (!B && U == 13) __builtin_amdgcn_s_barrier();
      if constexpr (B && U == 9 && chk(s)) asm volatile("ds_read_u8 %0, %1 offset:%2" : "+v"(fv) : "v"(fa), "n"(s == 6 ? 0 : 8));
      unsigned n = 1u << 18;
      if constexpr (B && U == 13 && chk(s)) {
        asm volatile(
            "v_cmp_gt_u32 vcc, %3, %0\n\ts_cbranch_vccnz .Lw%=\n\t.subsection 1\n.Lw%=:\n\tds_read_u8 %0, %2 offset:%4\n\ts_waitcnt lgkmcnt(0)\n\t"
            "v_cmp_gt_u32 vcc, %3, %0\n\t" SAFE_OUT(".Lr%=") "s_cbranch_vccnz .Lw%=\n\ts_branch .Lr%=\n\t.subsection 0\n.Lr%=:"
            : "+v"(fv), NOP_(n)
            : "v"(fa), "n"(s == 6 ? 4 : s + 2), "n"(s == 6 ? 0 : 8)
            : "vcc", "scc");
      }
      if constexpr (B && U == 13 && s == BPS) {
        asm volatile(
            ".La%=:\n\ts_sleep 1\n\tds_read_u8 %0, %2\n\ts_waitcnt lgkmcnt(0)\n\tv_cmp_gt_u32 vcc, 3, %0\n\t" SAFE_OUT(".Lr%=") "s_cbranch_vccnz .La%=\n"
            ".Lb%=:\n\tds_read_u8 %0, %2 offset:8\n\ts_waitcnt lgkmcnt(0)\n\tv_cmp_gt_u32 vcc, %3, %0\n\t" SAFE_OUT(".Lr%=") "s_cbranch_vccnz .Lb%=\n.Lr%=:"
            : "+v"(fv), NOP_(n)
            : "v"(fa), "n"(s + 2)
            : "vcc", "scc");
      }
    }
    if constexpr (B && U == 15) asm volatile("v_mov_b32 %0, %1" : "+v"(fv) : "n"(s + 1));
    if constexpr (B && ((U == 0 && s > 0) || V == NV - 1)) asm volatile("ds_write_b8 %0, %1" ::"v"(fw), "v"(fv));
    __builtin_amdgcn_sched_barrier(0);
  }
  template <int... V>
  DEVI void units(std::integer_sequence<int, V...>) {
    (unit<V>(), ...);
  }
  DEVI void first() {
    if constexpr (!B) {
      xread<0, 0>();
      xread<0, 1>();
    }
    wread<0>();
    wread<1>();
    wread<2>();
    wread<3>();
    asm volatile("s_waitcnt lgkmcnt(0)");
  }
  DEVI void run() {
    units(std::make_integer_sequence<int, NV>{});
#pragma unroll
    for (int m = 0; m < 2; ++m)
#pragma unroll
      for (int n = 0; n < 9; ++n) asm volatile("" : "+v"(acc[m][n]));
  }
};

#define PACE 0
#define G0P 2
#define ZSLEEP 6
DEVI void p3_g0(char* smem, char* __restrict__ ws, int layer, int mi, int ni, int wm, int lane MK_ARG) {
  constexpr int SB = K3C::SB;
  const unsigned lds0 = (unsigned)(size_t)smem;
  const int g = lane >> 4, r = lane & 15;
  const unsigned lw = lds0 + (unsigned)(LW_OFF + layer * 4), me = 1u << (8 * wm);
  f32x4 ac[2][9];
#pragma unroll
  for (int mt = 0; mt < 2; ++mt)
#pragma unroll
    for (int j = 0; j < 9; ++j) ac[mt][j] = f32x4{0.f, 0.f, 0.f, 0.f};
  unsigned ra = lds0 + (unsigned)(K3_ST_OFF + (wm * 32 + g * 8) * 4), ca = lds0 + (unsigned)(K3_C2_OFF + r * 8);
  asm volatile("" : "+v"(ra), "+v"(ca));
  {
    s16x8 xc[4][2][2];
    UMath<false> gm(ac, xc);
    gm.xa = lds0 + (unsigned)(g * 2048 + (wm * 32 + 8 * (r >> 2) + (r & 3)) * 16);
    gm.wa = lds0 + (unsigned)(K3C::XB + (g * 8 + (r & 7)) * K3C::P + (r >> 3) * 16);
    asm volatile("" : "+v"(gm.xa), "+v"(gm.wa));
    place<AL_A>();
    bar();
    gm.first();
    gm.run();
  }
  lds_inc(lw + LW_AD * 4, me);
  MK(11);
  f32x2 z[9][4];
  f32x2 sm[4], sq[4];
  {
    f32x4 rc[4][2];
    f32x2 cs[9];
#pragma unroll
    for (int i = 0; i < 8; ++i)
      asm volatile("ds_read_b128 %0, %1 offset:%2" : "=v"(rc[i / 2][i % 2]) : "v"(ra), "n"((i / 2) * 512 + (i % 2) * 16) : "memory");
#pragma unroll
    for (int j = 0; j < 9; ++j) asm volatile("ds_read_b64 %0, %1 offset:%2" : "=v"(cs[j]) : "v"(ca), "n"(j * 128) : "memory");
    asm volatile("s_waitcnt lgkmcnt(0)"
                 : "+v"(rc[0][0]), "+v"(rc[0][1]), "+v"(rc[1][0]), "+v"(rc[1][1]), "+v"(rc[2][0]), "+v"(rc[2][1]), "+v"(rc[3][0]), "+v"(rc[3][1]));
#pragma unroll
    for (int j = 0; j < 9; ++j) asm volatile("" : "+v"(cs[j]));
    auto RC = [&](int k, int i) { return f32x2{rc[k][i / 2][(i % 2) * 2], rc[k][i / 2][(i % 2) * 2 + 1]}; };
#pragma unroll
    for (int j = 0; j < 9; ++j) {
      f32x2 u[4], w[4];
#pragma unroll
      for (int i = 0; i < 4; ++i) u[i] = __builtin_elementwise_fma(RC(0, i), cs[j], f32x2{ac[i / 2][j][(i % 2) * 2], ac[i / 2][j][(i % 2) * 2 + 1]});
#pragma unroll
      for (int i = 0; i < 4; ++i) w[i] = u[i] * u[i];
#pragma unroll
      for (int i = 0; i < 4; ++i) w[i] = __builtin_elementwise_fma(w[i], RC(3, i), RC(2, i));
#pragma unroll
      for (int i = 0; i < 4; ++i) w[i] = u[i] * w[i];
      __builtin_amdgcn_sched_barrier(0);
#pragma unroll
      for (int i = 0; i < 4; ++i) w[i] = f32x2{__builtin_amdgcn_exp2f(w[i][0]), __builtin_amdgcn_exp2f(w[i][1])};
      __builtin_amdgcn_sched_barrier(0);
#pragma unroll
      for (int i = 0; i < 4; ++i) w[i] = __builtin_elementwise_fma(w[i], RC(1, i), RC(1, i));
      __builtin_amdgcn_sched_barrier(0);
#pragma unroll
      for (int i = 0; i < 4; ++i) w[i] = f32x2{__builtin_amdgcn_rcpf(w[i][0]), __builtin_amdgcn_rcpf(w[i][1])};
      __builtin_amdgcn_sched_barrier(0);
#pragma unroll
      for (int i = 0; i < 4; ++i) z[j][i] = u[i] * w[i];
#pragma unroll
      for (int i = 0; i < 4; ++i) {
        if (j == 1) sm[i] = z[0][i] + z[1][i];
        if (j > 1) sm[i] += z[j][i];
      }
#pragma unroll
      for (int i = 0; i < 4; ++i) sq[i] = j ? __builtin_elementwise_fma(z[j][i], z[j][i], sq[i]) : z[j][i] * z[j][i];
      asm volatile("" : "+v"(z[j][0]), "+v"(z[j][1]), "+v"(z[j][2]), "+v"(z[j][3]));
      __builtin_amdgcn_sched_barrier(0);
    }
  }
  f32x4 e;
  {
    float v[4][4], d[2][4], f[4];
#pragma unroll
    for (int i = 0; i < 4; ++i) {
      v[i][0] = sm[i][0];
      v[i][1] = sq[i][0];
      v[i][2] = sm[i][1];
      v[i][3] = sq[i][1];
    }
    asm volatile("s_nop 1" : "+v"(v[3][1]), "+v"(v[3][3]), "+v"(v[2][1]), "+v"(v[2][3]));
#pragma unroll
    for (int i = 0; i < 2; ++i)
#pragma unroll
      for (int c = 0; c < 4; ++c)
        asm volatile("v_add_f32_dpp %0, %1, %1 row_ror:8 row_mask:0xf bank_mask:0x3\n\tv_add_f32_dpp %0, %2, %2 row_ror:8 row_mask:0xf bank_mask:0xc"
                     : "=&v"(d[i][c])
                     : "v"(v[i][c]), "v"(v[i + 2][c]));
#pragma unroll
    for (int c = 0; c < 4; ++c)
      asm volatile("v_add_f32_dpp %0, %1, %1 row_shl:4 row_mask:0xf bank_mask:0x5\n\tv_add_f32_dpp %0, %2, %2 row_shr:4 row_mask:0xf bank_mask:0xa"
                   : "=&v"(f[c])
                   : "v"(d[0][c]), "v"(d[1][c]));
    asm volatile(
        "v_add_f32_dpp %0, %0, %0 quad_perm:[1,0,3,2] row_mask:0xf bank_mask:0xf\n\t"
        "v_add_f32_dpp %1, %1, %1 quad_perm:[1,0,3,2] row_mask:0xf bank_mask:0xf\n\t"
        "v_add_f32_dpp %2, %2, %2 quad_perm:[1,0,3,2] row_mask:0xf bank_mask:0xf\n\t"
        "v_add_f32_dpp %3, %3, %3 quad_perm:[1,0,3,2] row_mask:0xf bank_mask:0xf\n\t"
        "v_add_f32_dpp %0, %0, %0 quad_perm:[2,3,0,1] row_mask:0xf bank_mask:0xf\n\t"
        "v_add_f32_dpp %1, %1, %1 quad_perm:[2,3,0,1] row_mask:0xf bank_mask:0xf\n\t"
        "v_add_f32_dpp %2, %2, %2 quad_perm:[2,3,0,1] row_mask:0xf bank_mask:0xf\n\t"
        "v_add_f32_dpp %3, %3, %3 quad_perm:[2,3,0,1] row_mask:0xf bank_mask:0xf"
        : "+v"(f[0]), "+v"(f[1]), "+v"(f[2]), "+v"(f[3]));
    e = f32x4{f[0], __uint_as_float(__float_as_uint(f[1]) & 0x7fffffffu), f[2], __uint_as_float(__float_as_uint(f[3]) & 0x7fffffffu)};
  }
  const char* zp = uni_ptr(ws + OFF_ZP + (size_t)layer * SZ_ZP + (size_t)mi * 16384 + wm * 256);
  if ((lane & 3) == 0)
    asm volatile("global_store_dwordx4 %0, %1, %2 sc1\n\ts_nop 1" ::"v"((unsigned)(ni * 1024 + lane * 4)), "v"(e), "s"(zp) : "memory");
  lds_inc(lw + LW_AD * 4, me);
  __builtin_amdgcn_s_setprio(2);
  MK(12);
  const char* dst = uni_ptr(ws + OFF_Z2 + (size_t)layer * SZ_Z + (size_t)ni * S_ * 288 + (size_t)(mi * 4 + wm) * 9216 + 4096);
  const unsigned lane16 = (unsigned)lane * 16u;
  u32x4 wb[9];
  {
    static_assert(b_k(7) == 2 && b_k(8) == 3, "order");
    const char* wp = uni_ptr(ws + OFF_PI + (size_t)ni * (18 * K3C::WB) + 11 * K3C::WB + wm * 9216 + 4096);
#pragma unroll
    for (int j = 0; j < 8; ++j) asm volatile("global_load_dwordx4 %0, %1, %2 offset:%3 sc1" : "=v"(wb[j]) : "v"(lane16), "s"(wp), "n"(j * 1024 - 4096) : "memory");
    asm volatile("global_load_dwordx4 %0, %1, %2 sc1" : "=v"(wb[8]) : "v"(lane16 + 4096u), "s"(wp) : "memory");
  }
  u32x4 y[9];
  {
    const unsigned src = (unsigned)(g * 4096 + r * 16);
    unsigned zw = lds0 + (unsigned)(K3_ZC_OFF + (wm * 32 + 2 * r + (g & 1)) * 4), zr = lds0 + (unsigned)(K3_ZC_OFF + (wm * 32 + g * 8) * 4);
    asm volatile("" : "+v"(zw), "+v"(zr));
    f32x4 a, b, c, d;
    __builtin_amdgcn_s_sleep(ZSLEEP);
#pragma unroll 1
    for (unsigned spin = 0; spin < SPIN_MEM; ++spin) {
      asm volatile(
          "global_load_dwordx4 %0, %4, %5 sc1\n\t"
          "global_load_dwordx4 %1, %4, %5 offset:1024 sc1\n\t"
          "global_load_dwordx4 %2, %4, %5 offset:2048 sc1\n\t"
          "global_load_dwordx4 %3, %4, %5 offset:3072 sc1\n\t"
          "s_waitcnt vmcnt(0)"
          : "=&v"(a), "=&v"(b), "=&v"(c), "=&v"(d)
          : "v"(src), "s"(zp)
          : "memory");
      const unsigned bad = (__float_as_uint(a[1]) | __float_as_uint(a[3]) | __float_as_uint(b[1]) | __float_as_uint(b[3]) |
                            __float_as_uint(c[1]) | __float_as_uint(c[3]) | __float_as_uint(d[1]) | __float_as_uint(d[3])) >> 31;
      if (!__any(bad)) break;
    }
    MK(13);
    float s, q, zmb, zi;
    red16(a, b, c, d, s, q);
    ln_finish(s, q, 1.0f / (float)FH_, zmb, zi);
    f32x4 R[2], M[2];
    asm volatile(
        "ds_write2st64_b32 %4, %5, %6 offset1:2\n\t"
        "ds_read_b128 %0, %7\n\tds_read_b128 %2, %7 offset:512\n\tds_read_b128 %1, %7 offset:16\n\tds_read_b128 %3, %7 offset:528\n\t"
        "s_waitcnt lgkmcnt(0)"
        : "=&v"(R[0]), "=&v"(R[1]), "=&v"(M[0]), "=&v"(M[1])
        : "v"(zw), "v"(zi), "v"(-zmb * zi), "v"(zr)
        : "memory");
    MK(28);
#define NRM 0
    auto st = [&](int j) {
      if (j < 8) asm volatile("global_store_dwordx4 %0, %1, %2 offset:%3 sc1\n\ts_nop 1" ::"v"(lane16), "v"(y[j]), "s"(dst), "n"(j * 1024 - 4096) : "memory");
      else asm volatile("global_store_dwordx4 %0, %1, %2 sc1\n\ts_nop 1" ::"v"(lane16 + 4096u), "v"(y[j]), "s"(dst) : "memory");
    };
#pragma unroll
    for (int j = 0; j < 9; ++j) {
      if (NRM & 2) {
        f32x2 t[4];
#pragma unroll
        for (int k = 0; k < 4; ++k)
          t[k] = __builtin_elementwise_fma(z[j][k], f32x2{R[k / 2][(k % 2) * 2], R[k / 2][(k % 2) * 2 + 1]}, f32x2{M[k / 2][(k % 2) * 2], M[k / 2][(k % 2) * 2 + 1]});
        y[j] = u32x4{pack2v(t[0]), pack2v(t[1]), pack2v(t[2]), pack2v(t[3])};
      } else {
        float t[8];
#pragma unroll
        for (int k = 0; k < 8; ++k) {
          t[k] = __builtin_fmaf(z[j][k / 2][k % 2], R[k / 4][k % 4], M[k / 4][k % 4]);
          asm volatile("" : "+v"(t[k]));
        }
        y[j] = u32x4{pack2(t[0], t[1]), pack2(t[2], t[3]), pack2(t[4], t[5]), pack2(t[6], t[7])};
      }
      if (NRM & 1) asm volatile("" : "+v"(y[j]));
      else st(j);
      __builtin_amdgcn_sched_barrier(0);
    }
    MK(29);
    if (NRM & 1) {
#pragma unroll
      for (int j = 0; j < 9; ++j) st(j);
    }
  }
  lds_inc(lw + LW_AD * 4, me);
  MK(24);
  __builtin_amdgcn_s_setprio(G0P);
  lds_spin4(lw + LW_S * 4, 4u + (unsigned)(wm >> 1));
  {
    const unsigned fa = lds0 + (unsigned)((wm >> 1) * SB + K3C::XB + (wm & 1) * 9216) + lane16;
#pragma unroll
    for (int j = 0; j < 9; ++j) {
      asm volatile("ds_write_b128 %0, %1 offset:%2" ::"v"(fa), "v"(wb[j]), "n"(j * 1024) : "memory");
      if (PACE && j < 8) __builtin_amdgcn_s_sleep(PACE);
    }
    lds_inc(lw + LW_AD * 4, me);
  }
  announce(lw + LW_Z * 4, 4u, ws, CN_Z + layer, lane * 16 + ni, lane < 16);
  lds_inc(lw + LW_AD * 4, me);
  MK(27);
  f32x4 acc[2][9];
  {
    float bs[2];
    asm volatile("ds_read2_b32 %0, %1 offset1:16\n\ts_waitcnt lgkmcnt(0)"
                 : "=v"(*reinterpret_cast<f32x2*>(bs))
                 : "v"(lds0 + (unsigned)(K3_BS_OFF + (wm * 32 + r) * 4))
                 : "memory");
    lds_spin4(lw + LW_S * 4, 6u + (unsigned)(wm >> 1));
    const unsigned fa = lds0 + (unsigned)((2 + (wm >> 1)) * SB + K3C::XB + (wm & 1) * 9216) + lane16;
#pragma unroll
    for (int j = 0; j < 9; ++j) {
      asm volatile("ds_write_b128 %0, %1 offset:%2" ::"v"(fa), "v"(y[j]), "n"(j * 1024) : "memory");
      if (PACE && j < 8) __builtin_amdgcn_s_sleep(PACE);
    }
    lds_inc(lw + LW_AD * 4, me);
#pragma unroll
    for (int mt = 0; mt < 2; ++mt)
#pragma unroll
      for (int j = 0; j < 9; ++j) acc[mt][j] = f32x4{bs[mt], bs[mt], bs[mt], bs[mt]};
  }
  u32x4 z1[9];
  const char* hp = uni_ptr(ws + OFF_H + (size_t)layer * SZ_Z + (size_t)((mi * 2 + (wm >> 1)) * 288 + ni * 18) * 1024);
  unsigned ho = (unsigned)((lane >> 5) * 1024 + ((wm & 1) * 32 + (lane & 31)) * 16);
  unsigned zb = lds0 + (unsigned)(wm * 4096) + lane16, zf = lw + (unsigned)(LW_S * 4 + wm);
  asm volatile("" : "+v"(ho), "+v"(zb), "+v"(zf));
  asm volatile("" : "+s"(hp));
  {
    GemmMath<K3C, 2, 9, K3_D, 6, false, 2> gm(acc);
    gm.setup(lds0, wm * 2, 0, lane, (unsigned)(2 * SB));
    MK(15);
    lds_spin4(lw + LW_AD * 4, 6u);
    lds_spin4(lw + LW_L * 4, 8u);
    MK(17);
    gm.first();
    __builtin_amdgcn_s_setprio(0);
    gm.template mid<1, false>();
    gm.template mid<11, true, AL_C>();
    const unsigned za = gm.wrd + (unsigned)(K3_Z1_OFF - K3C::XB + wm * 5120);
#pragma unroll
    for (int j = 0; j < NR1; ++j) asm volatile("ds_read_b128 %0, %1 offset:%2" : "=v"(z1[j]) : "v"(za), "n"(j * 1024));
    gm.template mid<19, true, AL_C>();
    gm.last();
  }
  MK(16);
  if (NR1 < 5) lds_inc(lw + LW_AD * 4, me);
  unsigned zv;
  u32x4 zl[4];
#pragma unroll
  for (int j = 0; j < 9; ++j) {
    const bool sec = j >= NR1 + 3;
    if (j == NR1 || j == NR1 + 4) {
      unsigned n = 1u << 18;
      asm volatile(
          "s_waitcnt lgkmcnt(0)\n\tv_cmp_gt_u32 vcc, %8, %0\n\ts_cbranch_vccnz .Lw%=\n\t.subsection 1\n.Lw%=:\n\tds_read_u8 %0, %6\n\tds_read_b128 %2, %7 offset:%9\n\t"
          "ds_read_b128 %3, %7 offset:%9+1024\n\tds_read_b128 %4, %7 offset:%9+2048\n\tds_read_b128 %5, %7 offset:%9+3072\n\ts_waitcnt lgkmcnt(0)\n\t"
          "v_cmp_gt_u32 vcc, %8, %0\n\t" SAFE_OUT(".Lr%=") "s_cbranch_vccnz .Lw%=\n\ts_branch .Lr%=\n\t.subsection 0\n.Lr%=:"
          : "+v"(zv), NOP_(n), "+v"(zl[0]), "+v"(zl[1]), "+v"(zl[2]), "+v"(zl[3])
          : "v"(zf), "v"(zb), "n"(sec ? 11 : 10), "n"(sec ? SB : 0)
          : "memory", "vcc", "scc");
#pragma unroll
      for (int k = 0; k < 4; ++k)
        if (j + k < 9) z1[j + k] = zl[k];
    }
    if (j == NR1 - 1 || (j == NR1 + 3 && j < 8))
      asm volatile("ds_read_u8 %0, %5\n\tds_read_b128 %1, %6 offset:%7\n\tds_read_b128 %2, %6 offset:%7+1024\n\tds_read_b128 %3, %6 offset:%7+2048\n\t"
                   "ds_read_b128 %4, %6 offset:%7+3072"
                   : "=&v"(zv), "=&v"(zl[0]), "=&v"(zl[1]), "=&v"(zl[2]), "=&v"(zl[3])
                   : "v"(zf), "v"(zb), "n"(sec ? SB : 0)
                   : "memory");
    asm volatile("" : "+v"(z1[j]));
    u32x2 h[2];
#pragma unroll
    for (int mt = 0; mt < 2; ++mt) {
      const f32x4 a = acc[mt][j];
      h[mt] = u32x2{pack2v(f32x2{bf_lo(z1[j][2 * mt]), bf_hi(z1[j][2 * mt])} * f32x2{a[0], a[1]}),
                    pack2v(f32x2{bf_lo(z1[j][2 * mt + 1]), bf_hi(z1[j][2 * mt + 1])} * f32x2{a[2], a[3]})};
    }
    const auto s0 = __builtin_amdgcn_permlane16_swap(h[0][0], h[1][0], false, false);
    const auto s1 = __builtin_amdgcn_permlane16_swap(h[0][1], h[1][1], false, false);
    const u32x4 v = {s0[0], s1[0], s0[1], s1[1]};
    asm volatile("global_store_dwordx4 %0, %1, %2 offset:%3 sc1\n\ts_nop 1" ::"v"(ho), "v"(v), "s"(hp + (j / 2) * 4096), "n"((j % 2) * 2048) : "memory");
    __builtin_amdgcn_sched_barrier(0);
  }
  MK(18);
}

DEVI void k3_const(char* smem, char* ws, const u16* b_sp, int layer, int bid, int mi, int ni, int wm) {
  const int lane = lane_id(), l = lane < 54 ? lane : 53, p = l / 18;
  const char* kp = uni_ptr(ws + OFF_CSK + bid * 2048 + wm * 512);
  const unsigned q16 = (unsigned)(l - p * 18) * 16u;
  unsigned bs;
  f32x4 t;
  asm volatile("global_load_ushort %0, %1, %2" : "=v"(bs) : "v"((unsigned)(lane & 31) * 2u), "s"(uni_ptr((const char*)(b_sp + mi * 128 + wm * 32))) : "memory");
  if (layer) {
    asm volatile("global_load_dwordx4 %0, %1, %2 sc1\n\ts_waitcnt vmcnt(0)" : "=&v"(t) : "v"(q16), "s"(kp) : "memory");
  } else {
    const char* cp = uni_ptr(ws + OFF_CSP + ni * 1152 + wm * 288);
    f32x4 a[6];
#pragma unroll
    for (int i = 0; i < 6; ++i)
      asm volatile("global_load_dwordx4 %0, %1, %2 sc1" : "=v"(a[i]) : "v"(q16 + (unsigned)(p * (6 * F_ * 4))), "s"(cp + i * (F_ * 4)) : "memory");
    asm volatile("s_waitcnt vmcnt(0)" : "+v"(a[0]), "+v"(a[1]), "+v"(a[2]), "+v"(a[3]), "+v"(a[4]), "+v"(a[5]));
    t = ((a[0] + a[1]) + (a[2] + a[3])) + (a[4] + a[5]);
#pragma unroll
    for (int j = 0; j < 4; ++j)
      t[j] += __int_as_float(__builtin_amdgcn_ds_bpermute((lane + 18) * 4, __float_as_int(t[j]))) +
              __int_as_float(__builtin_amdgcn_ds_bpermute((lane + 36) * 4, __float_as_int(t[j])));
    if (lane < 24) asm volatile("global_store_dwordx4 %0, %1, %2\n\ts_nop 1" ::"v"((unsigned)lane * 16u), "v"(t), "s"(kp) : "memory");
  }
  asm volatile("" : "+v"(bs));
  if (lane < 18) {
    if (wm < 2) {
      *reinterpret_cast<f32x4*>(smem + K3_C2_OFF + wm * 576 + lane * 32) = f32x4{t[0], t[0], t[1], t[1]};
      *reinterpret_cast<f32x4*>(smem + K3_C2_OFF + wm * 576 + lane * 32 + 16) = f32x4{t[2], t[2], t[3], t[3]};
    } else {
      *reinterpret_cast<f32x4*>(smem + K3_C1_OFF + (wm - 2) * 288 + lane * 16) = t;
    }
  }
  if (lane < 32) *reinterpret_cast<float*>(smem + K3_BS_OFF + (wm * 32 + lane) * 4) = bf_lo(bs);
}

DEVI void p3_g1(char* smem, char* __restrict__ ws, const char* code0, int bid, int layer, int mi, int wm, int lane MK_ARG) {
  const unsigned lds0 = (unsigned)(size_t)smem;
  const int g = lane >> 4, r = lane & 15;
  const unsigned lw = lds0 + (unsigned)(LW_OFF + layer * 4);
  const unsigned xrd = lds0 + (unsigned)(g * 2048 + (wm * 32 + r) * 16);
  s16x8 xc[4][2][2];
  {
    const char* xs = uni_ptr(ws + OFF_XS + (size_t)layer * 2 * SZ_XS + (size_t)(mi * 2 + (wm >> 1)) * 4096 + (wm & 1) * 256);
    f32x4 a, b;
    auto pick = [&](int t) {
      bar();
      asm volatile(
          "ds_read_b128 %0, %4\n\tds_read_b128 %1, %4 offset:256\n\tds_read_b128 %2, %4 offset:8192\n\tds_read_b128 %3, %4 offset:8448\n\t"
          "s_waitcnt lgkmcnt(0)"
          : "=&v"(xc[t][0][0]), "=&v"(xc[t][0][1]), "=&v"(xc[t][1][0]), "=&v"(xc[t][1][1])
          : "v"(xrd + (unsigned)(t * K3C::SB))
          : "memory");
    };
#define G1P 2
    __builtin_amdgcn_s_setprio(G1P);
    pick(0);
    pick(1);
    asm volatile("global_load_dwordx4 %0, %2, %3 sc1\n\tglobal_load_dwordx4 %1, %2, %3 offset:512 sc1"
                 : "=&v"(a), "=&v"(b)
                 : "v"((unsigned)(g * 1024 + r * 16)), "s"(xs)
                 : "memory");
    pick(2);
    pick(3);
    bar();
    asm volatile("s_waitcnt vmcnt(0)" : "+v"(a), "+v"(b) : : "memory");
    float s, q, mb, rinv;
    red8(a, b, s, q);
    bar();
    asm volatile("" : "+v"(s), "+v"(q));
    ln_finish(s, q, 1.0f / (float)D_, mb, rinv);
    asm volatile("" : "+v"(mb), "+v"(rinv));
    bar();
    asm volatile("" : "+v"(mb), "+v"(rinv));
    const float k0 = -2.0f * 0.7978845608028654f * 1.4426950408889634f, k1 = k0 * 0.044715f;
    float sd = __builtin_amdgcn_rcpf(rinv), c0 = k0 * rinv, c1 = k1 * rinv * rinv * rinv;
    asm volatile("" : "+v"(sd));
    asm volatile("" : "+v"(c0));
    asm volatile("" : "+v"(c1));
    const float e0 = lane < 32 ? -mb : c0, e1 = lane < 32 ? sd : c1;
    asm volatile("ds_write2st64_b32 %0, %1, %2 offset1:2" ::"v"(
                     lds0 + (unsigned)(K3_ST_OFF + (g >> 1) * 1024 + (wm * 32 + 2 * r + (g & 1)) * 4)),
                 "v"(e0), "v"(e1)
                 : "memory");
    bar();
    asm volatile("s_waitcnt lgkmcnt(0)" ::: "memory");
    bar();
    __builtin_amdgcn_s_setprio(0);
  }
  f32x4 ac[2][9];
#pragma unroll
  for (int mt = 0; mt < 2; ++mt)
#pragma unroll
    for (int j = 0; j < 9; ++j) ac[mt][j] = f32x4{0.f, 0.f, 0.f, 0.f};
  {
    UMath<true> gm(ac, xc);
    gm.xa = xrd;
    gm.wa = lds0 + (unsigned)(K3C::XB + (g * 8 + (r & 7)) * K3C::P + (r >> 3) * 16);
    gm.fa = lw + (unsigned)(LW_AD * 4 + (lane & 3));
    gm.fw = lw + (unsigned)(LW_S * 4 + wm);
    gm.fv = 0u;
    asm volatile("" : "+v"(gm.fa), "+v"(gm.fw), "+v"(gm.fv), "+v"(gm.xa), "+v"(gm.wa));
    lds_spin4<4>(lw + LW_L * 4, 4u);
    gm.first();
    place<AL_B>();
    lds_spin4(lw + LW_AD * 4, 2u);
#define BPR 1
    __builtin_amdgcn_s_setprio(BPR);
    gm.run();
    __builtin_amdgcn_s_setprio(0);
  }
  MK(14);
  f32x2 rc[4];
  f32x4 cs[9];
  const int l2 = lane_id();
  {
    const unsigned ra = lds0 + (unsigned)(K3_ST_OFF + (wm * 32 + (l2 & 15)) * 4), ca = lds0 + (unsigned)(K3_C1_OFF + (l2 & 48));
#pragma unroll
    for (int k = 0; k < 4; ++k) asm volatile("ds_read2_b32 %0, %1 offset1:16" : "=v"(rc[k]) : "v"(ra + (unsigned)(k * 512)) : "memory");
#pragma unroll
    for (int j = 0; j < 9; ++j) asm volatile("ds_read_b128 %0, %1 offset:%2" : "=v"(cs[j]) : "v"(ca), "n"(j * 64) : "memory");
    asm volatile("s_waitcnt lgkmcnt(0)" : "+v"(rc[0]), "+v"(rc[1]), "+v"(rc[2]), "+v"(rc[3]));
#pragma unroll
    for (int j = 0; j < 9; ++j) asm volatile("" : "+v"(cs[j]));
  }
  const unsigned za = lds0 + (unsigned)(K3_Z1_OFF + wm * 5120 + l2 * 16);
  u32x4 z1[9];
#pragma unroll
  for (int t = 0; t < 18; ++t) {
    const int j = t / 2, mt = t % 2;
    float u[4], w[4];
#pragma unroll
    for (int i = 0; i < 4; ++i) asm volatile("v_fma_f32 %0, %1, %2, %3" : "=v"(u[i]) : "v"(rc[0][mt]), "v"(cs[j][i]), "v"(ac[mt][j][i]));
#pragma unroll
    for (int i = 0; i < 4; ++i) asm volatile("v_mul_f32 %0, %1, %1" : "=v"(w[i]) : "v"(u[i]));
#pragma unroll
    for (int i = 0; i < 4; ++i) asm volatile("v_fma_f32 %0, %0, %1, %2" : "+v"(w[i]) : "v"(rc[3][mt]), "v"(rc[2][mt]));
#pragma unroll
    for (int i = 0; i < 4; ++i) asm volatile("v_mul_f32 %0, %0, %1" : "+v"(w[i]) : "v"(u[i]));
#pragma unroll
    for (int i = 0; i < 4; ++i) asm volatile("v_exp_f32 %0, %0" : "+v"(w[i]));
#pragma unroll
    for (int i = 0; i < 4; ++i) asm volatile("v_fma_f32 %0, %0, %1, %1" : "+v"(w[i]) : "v"(rc[1][mt]));
#pragma unroll
    for (int i = 0; i < 4; ++i) asm volatile("v_rcp_f32 %0, %0" : "+v"(w[i]));
#pragma unroll
    for (int i = 0; i < 4; ++i) asm volatile("v_mul_f32 %0, %0, %1" : "+v"(w[i]) : "v"(u[i]));
    z1[j][2 * mt] = pack2(w[0], w[1]);
    z1[j][2 * mt + 1] = pack2(w[2], w[3]);
    if (mt && j < NR1) asm volatile("ds_write_b128 %0, %1 offset:%2" ::"v"(za), "v"(z1[j]), "n"(j * 1024) : "memory");
    if (t == 9) asm volatile("s_waitcnt lgkmcnt(0)" ::: "memory");
    __builtin_amdgcn_sched_barrier(0);
    bar();
    __builtin_amdgcn_sched_barrier(0);
  }
  MK(15);
#pragma unroll 1
  for (int t = 20; t < 32; ++t) bar();
  __builtin_amdgcn_s_sleep(ZSL);
#pragma unroll
  for (int k = NR1; k < 9; ++k) {
    if (k == NR1 + 4) lds_spin4(lw + LW_AD * 4, 7u);
    asm volatile("ds_write_b128 %0, %1 offset:%2" ::"v"(lds0 + (unsigned)(wm * 4096 + l2 * 16)), "v"(z1[k]),
                 "n"(((k - NR1) / 4) * K3C::SB + ((k - NR1) % 4) * 1024)
                 : "memory");
    if (k == NR1 + 3 || k == 8) asm volatile("ds_write_b8 %0, %1" ::"v"(lw + (unsigned)(LW_S * 4 + wm)), "v"(vconst(k == NR1 + 3 ? 10u : 11u)) : "memory");
  }
  if ((RTC & 2) && wm == 2) code_again(code0, bid);
}

constexpr int W5A = 22, W5B = 26;
constexpr int NPF = 30;
struct Loader {
  u32x4 pf[NPF];
  unsigned lds0, lane16;
  int wl, lane;

  template <int R0, int N, int P0, int PE>
  DEVI void pf_load(const char* src) {
    const unsigned o = lane16 + (unsigned)(wl * 1024);
#pragma unroll
    for (int t = 0; t < N; ++t) {
      unsigned a = o + (unsigned)((P0 + 4 * t) * 1024);
      if (P0 + 4 * t + 3 >= PE && P0 + 4 * t + wl >= PE) a = lane16;
      asm volatile("global_load_dwordx4 %0, %1, %2 sc1" : "=v"(pf[R0 + t]) : "v"(a), "s"(src) : "memory");
    }
  }
  template <int R0, int N>
  DEVI void pf_pin() {
#pragma unroll
    for (int t = 0; t < N; ++t) asm volatile("" : "+v"(pf[R0 + t]));
  }
  template <int R0, int N>
  DEVI void pf_undef() {
#pragma unroll
    for (int t = 0; t < N; ++t) asm volatile("" : "=v"(pf[R0 + t]));
  }
  template <int PB, int A, int N, int SL = 0>
  DEVI void pf_put(unsigned dst) {
    constexpr int T0 = (A - PB) / 4, T1 = (A + N - 1 - PB) / 4;
    const unsigned a = lds0 + dst + lane16 + (unsigned)((wl + PB + 4 * T0 - A + 4) * 1024) - 4096u;
#pragma unroll
    for (int t = T0; t <= T1; ++t) {
      const int p0 = PB + 4 * t;
      if ((p0 >= A || p0 + wl >= A) && (p0 + 3 < A + N || p0 + wl < A + N))
        asm volatile("ds_write_b128 %0, %1 offset:%2" ::"v"(a), "v"(pf[t]), "n"((t - T0) * 4096) : "memory");
      if (SL) __builtin_amdgcn_s_sleep(SL);
    }
  }
  template <int N>
  DEVI void dma_run(const char* src, unsigned dst) {
    const unsigned o = lane16 + (unsigned)(wl * 1024), m = lds0 + dst + (unsigned)(wl * 1024);
#pragma unroll
    for (int i = 0; i < (N + 3) / 4; ++i)
      if (4 * i + 3 < N || 4 * i + wl < N) dma16_sc1(src + i * 4096, o, m + (unsigned)(i * 4096));
  }
  template <int V>
  DEVI void wait_w() {
    asm volatile("s_cmp_lt_u32 %0, 2\n\ts_cbranch_scc1 1f\n\ts_waitcnt vmcnt(%1)\n1:\n\ts_waitcnt vmcnt(%2)" ::"s"(wl), "n"(V - 1), "n"(V) : "memory", "scc");
  }
  template <int V>
  DEVI void wbar() {
    asm volatile("s_waitcnt vmcnt(%0) lgkmcnt(0)" ::"n"(V) : "memory");
    bar();
  }

  DEVI void p1(char* smem, char* ws, int bid, int rt, int hp MK_ARG) {
    constexpr int SB = K1C::SB;
    static_assert(K1C::XB == 18 * 1024 && K1C::WB == 14 * 1024, "K1 shape");
    const char* wp = uni_ptr(ws + OFF_PQ + (size_t)hp * (9 * K1C::WB) + 4 * K1C::WB);
    static_assert(3 * SB + K1C::XB >= P5_W + 5 * 18432, "free while the GEMM before ends");
    const char* wq = wp - 4 * K1C::WB;
    dma_run<14>(wq + 3 * K1C::WB, (unsigned)(3 * SB + K1C::XB));
    dma_run<28>(wp, (unsigned)K1_WX);
    if (CSL && wl == 3) dma16_sc1(uni_ptr(ws + OFF_CSQ + bid * 512), lane16 & 511u, lds0 + (unsigned)K1_CS_OFF);
    if (P1PR & 1) __builtin_amdgcn_s_setprio(0);
    if (P1PR & 2) wait_vm<0>();
    const bool lo = wl < 2;
    const int x0 = lo ? wl * 5 : wl * 4 + 2, w0 = lo ? wl * 3 : wl * 4 - 2;
    unsigned xo[5];
#pragma unroll
    for (int i = 0; i < 5; ++i) {
      const int c = (x0 + i) * 64 + lane;
      const int ch = c / 144;
      int gr = rt * 128 - 16 + (c - ch * 144);
      if (gr < 0) gr = 0;
      xo[i] = (unsigned)(((gr >> 6) * 72 + ch) * 1024 + (gr & 63) * 16);
      asm volatile("" : "+v"(xo[i]));
    }
    const char* xp = uni_ptr(ws + OFF_X1 + SZ_X);
    wp += 2 * K1C::WB + w0 * 1024;
    auto dma_x = [&](unsigned so) {
      const unsigned m = lds0 + so + (unsigned)(x0 * 1024);
#pragma unroll
      for (int i = 0; i < 4; ++i) dma16_sc1(xp, xo[i], m + (unsigned)(i * 1024));
      if (lo) dma16_sc1(xp, xo[4], m + 4096u);
      xp += 8192;
    };
    auto dma_w = [&](unsigned so) {
      const unsigned m = lds0 + so + (unsigned)(K1C::XB + w0 * 1024);
#pragma unroll
      for (int i = 0; i < 3; ++i) dma16_sc1(wp + i * 1024, lane16, m + (unsigned)(i * 1024));
      if (!lo) dma16_sc1(wp + 3072, lane16, m + 3072u);
      wp += K1C::WB;
    };
    const char* xs = uni_ptr(ws + OFF_XS + SZ_XS);
    const int g = lane >> 4, p2 = 64 + (lane & 7);
    int ra = rt * 128 - 16 + wl * 32 + 2 * (lane & 15);
    if (ra < 0) ra = 0;
    unsigned sa = (unsigned)(((ra >> 6) * 8 + g * 2) * 512 + (ra & 63) * 8);
    unsigned sb = (unsigned)(((rt * 2 + 1) * 8 + (lane >> 3)) * 512 + (2 * p2 - 80) * 8);
    unsigned sw = lds0 + (unsigned)(K1_ST_OFF + (2 * (lane < 32 ? wl * 16 + (lane & 15) : p2) + (g & 1)) * 8);
    asm volatile("" : "+v"(sa), "+v"(sb), "+v"(sw));
    MK(22);
    __builtin_amdgcn_s_setprio(3);
    bar();
    dma_run<14>(wq, (unsigned)K1C::XB);
    dma_run<14>(wq + K1C::WB, (unsigned)(SB + K1C::XB));
    dma_run<14>(wq + 2 * K1C::WB, (unsigned)(2 * SB + K1C::XB));
    MK(1);
    bar();
    dma_x(0u);
    dma_x((unsigned)SB);
    wbar<4>();
    MK(29);
    dma_x((unsigned)(2 * SB));
    dma_x((unsigned)(3 * SB));
    wbar<8>();
    MX(1, 3);
    dma_x(0u);
    wbar<8>();
    MX(1, 4);
    dma_x((unsigned)SB);
    f32x4 a, b, e;
    asm volatile("global_load_dwordx4 %0, %3, %5 sc1\n\tglobal_load_dwordx4 %1, %3, %5 offset:512 sc1\n\tglobal_load_dwordx4 %2, %4, %5 sc1"
                 : "=&v"(a), "=&v"(b), "=&v"(e)
                 : "v"(sa), "v"(sb), "s"(xs)
                 : "memory");
    wbar<11>();
    MX(1, 5);
    dma_w((unsigned)SB);
    dma_x((unsigned)(2 * SB));
    wbar<15>();
    MX(1, 6);
    dma_w((unsigned)(2 * SB));
    dma_x((unsigned)(3 * SB));
    wbar<19>();
    MX(1, 7);
    dma_w((unsigned)(3 * SB));
    dma_x(0u);
    wbar<16>();
    MX(1, 8);
    asm volatile("" : "+v"(a), "+v"(b), "+v"(e));
    float s, q;
    {
      float t[4] = {e[0], e[1], e[2], e[3]}, s2, q2;
      asm volatile(
          "v_add_f32_dpp %0, %0, %0 row_ror:8 row_mask:0xf bank_mask:0xf\n\tv_add_f32_dpp %1, %1, %1 row_ror:8 row_mask:0xf bank_mask:0xf\n\t"
          "v_add_f32_dpp %2, %2, %2 row_ror:8 row_mask:0xf bank_mask:0xf\n\tv_add_f32_dpp %3, %3, %3 row_ror:8 row_mask:0xf bank_mask:0xf\n\ts_nop 1"
          : "+v"(t[0]), "+v"(t[1]), "+v"(t[2]), "+v"(t[3]));
      red4(t, s2, q2);
      red8(a, b, s, q);
      s = lane < 32 ? s : s2;
      q = lane < 32 ? q : q2;
      asm volatile("" : "+v"(s), "+v"(q));
    }
    wbar<8>();
    MX(1, 9);
    {
      asm volatile("" : "+v"(s), "+v"(q));
      float mb, rinv;
      ln_finish(s, q, 1.0f / (float)D_, mb, rinv);
      asm volatile("ds_write_b64 %0, %1" ::"v"(sw), "v"(f32x2{-mb, rinv}) : "memory");
    }
    wbar<0>();
    MK(30);
  }

  template <int I>
  DEVI void p5_step(const char* wp) {
    constexpr int T = 2 * I + 1;
    wbar<(T == 1 ? 63 : (T <= 11 ? 3 : 0))>();
    if constexpr (T <= 9) {
      auto dm = [&]() {
      if (!G1W) {
        const unsigned wo = (unsigned)(12288 + (wl & 1) * 3072), so = (unsigned)(P5_W + ((T + 2 + (wl >> 1)) & 7) * 18432);
#pragma unroll
        for (int k = 0; k < 3; ++k) dma16_sc1(wp + (T + 7) * 18432 + k * 1024, lane16 + wo + (unsigned)((wl >> 1) * 18432), lds0 + so + wo + (unsigned)(k * 1024));
      }
      };
      if (!P5ORD) dm();
      pf_put<0, 12 * (T - 1), 12, P5PACE>((unsigned)(P5_W + ((T + 2) % 8) * 18432));
      pf_put<0, 12 * T, 12, P5PACE>((unsigned)(P5_W + ((T + 3) % 8) * 18432));
      if (P5ORD == 2) asm volatile("s_waitcnt lgkmcnt(0)" ::: "memory");
      if (P5ORD) dm();
    }
  }
  template <int... S>
  DEVI void p5_steps(const char* wp, std::integer_sequence<int, S...>) {
    (p5_step<S>(wp), ...);
  }

  template <int T0, int T1>
  DEVI void w5_load(const char* w5p) {
    const unsigned o = lane16 + (unsigned)(wl * 1024);
#pragma unroll
    for (int t = T0; t < T1; ++t)
      asm volatile("global_load_dwordx4 %0, %1, %2 sc1" : "=v"(pf[t]) : "v"(o + (unsigned)((t / 3 + 8) * 18432 + (t % 3) * 4096)), "s"(w5p) : "memory");
  }
  DEVI void p35(char* smem, char* ws, const u16* w_sp, int layer, int bid, int mi, int ni, int rb, int ct, int hp MK_ARG) {
    using C = K3C;
    constexpr int NS = C::NS, SB = C::SB, NDX = 4, NDW = 5, VPS = NDX + NDW;
    const char* wip = uni_ptr(ws + OFF_PI + (size_t)ni * (18 * C::WB));
    if (layer || L0REG) {
      pf_put<-48, 36, 18>((unsigned)(2 * SB + C::XB));
      pf_put<-48, 54, 18>((unsigned)(3 * SB + C::XB));
    } else {
      dma_run<18>(wip + 2 * C::WB, (unsigned)(2 * SB + C::XB));
      dma_run<18>(wip + 3 * C::WB, (unsigned)(3 * SB + C::XB));
    }
    const char* w5p = uni_ptr(ws + OFF_PW + (size_t)ct * (36 * 9216));
    const int w0 = wl < 2 ? wl * 5120 : wl * 4096 + 2048;
    const unsigned m0x = lds0 + (unsigned)(wl * 4096), m0w = lds0 + (unsigned)(C::XB + w0);
    const unsigned wo = lane16 + (unsigned)w0;
    const unsigned lw = lds0 + (unsigned)(LW_OFF + layer * 4), me = 1u << (8 * wl);
    auto dma_w = [&](const char* wp, unsigned so) {
#pragma unroll
      for (int i = 0; i < NDW; ++i) {
        if (i < NDW - 1 || wl < 2) dma16_sc1(wp, wo + (unsigned)(i * 1024), m0w + so + (unsigned)(i * 1024));
      }
    };
    {
      const char* xp = uni_ptr(ws + OFF_X1 + (size_t)layer * 2 * SZ_X + (size_t)(mi * 2 * 72 + wl * 2) * 1024);
      const unsigned xo1 = lane16 + 72u * 1024u;
      auto dma_x = [&](int k, unsigned so) {
        const char* p = xp + k * 8192;
        dma16_sc1(p, lane16, m0x + so);
        dma16_sc1(p, xo1, m0x + so + 1024u);
        dma16_sc1(p + 1024, lane16, m0x + so + 2048u);
        dma16_sc1(p + 1024, xo1, m0x + so + 3072u);
      };
      if (!MPOLL && wl == 0) cnt_wait(ws, CN_X1 + layer, mi * 16 + ni, 16u);
      MK(10);
      bar();
      dma_x(0, 0u);
      dma_x(1, (unsigned)SB);
      wait_vm<4>(); MX(3, 2); wbar<4>(); MX(2, 2);
      MK(25);
      dma_x(2, (unsigned)(2 * SB));
      dma_x(3, (unsigned)(3 * SB));
      dma_x(4, (unsigned)(4 * SB));
      wbar<12>(); MX(2, 3);
      dma_x(5, 0u);
      wait_vm<12>();
      bar(); MX(2, 4);
      dma_w(wip + 4 * C::WB, 0u);
      dma_x(6, (unsigned)SB);
      wait_w<8 + VPS>();
#pragma unroll 1
      for (int t = 1; t < 3; ++t) {
        bar(); MX(2, 4 + t);
        dma_w(wip + (t + 4) * C::WB, (unsigned)(t * SB));
        dma_x(t + 6, (unsigned)((t + 1) * SB));
        wait_w<NDX + VPS>();
      }
      bar(); MX(2, 7);
      dma_w(wip + 7 * C::WB, (unsigned)(3 * SB));
      wait_w<VPS>();
      bar(); MX(2, 8);
      dma_w(wip + 8 * C::WB, 0u);
      wait_w<NDW>();
      bar(); MX(2, 9);
      const char* wp = wip + 9 * C::WB;
      dma_w(wp + b_k(0) * C::WB, (unsigned)SB);
      if (layer == 0) {
        const char* sp = uni_ptr((const char*)w_sp + (size_t)(bid * 4 + wl) * 8192);
        const unsigned so = (unsigned)((lane >> 5) * 4096 + (lane & 31) * 16);
#pragma unroll
        for (int i = 0; i < WSP_V; ++i) dma16(sp + i * 512, so, lds0 + (unsigned)K3_DM_OFF);
        dma16(sp + (bid >> 4) * 256, so & ~0x100u, lds0 + (unsigned)K3_DM_OFF);
      }
      w5_load<0, 4>(w5p);
      wait_w<NDW + 4>();
      MX(3, 10); bar(); MX(2, 10);
      dma_w(wp + b_k(1) * C::WB, (unsigned)(2 * SB));
      w5_load<4, 8>(w5p);
      MK(26);
      lds_spin4(lw + LW_AD * 4, 1u);
      dma_w(wp + b_k(2) * C::WB, (unsigned)(3 * SB));
      dma_w(wp + b_k(3) * C::WB, 0u);
#define TCH 0
      if (TCH & 1) {
        const int li = wl * 54 + (lane < 54 ? lane : 53);
        touch64(wp + C::WB, (unsigned)((mi & 7) * 13824 + li * 64), lds0 + (unsigned)K3_DM_OFF);
      }
      w5_load<8, W5A>(w5p);
      wait_vm<W5A - 8>();
      lds_inc(lw + LW_L * 4, 4u * me);
      w5_load<W5A, 30>(w5p);
      MK(11);
#define LSL 2
      static_for<4, 7>([&](auto T_) {
        constexpr int t = T_;
        lds_spin4<LSL>(lw + LW_S * 4, (unsigned)(t - 3));
        MX(2, 12 + t);
        dma_w(wp + b_k(t) * C::WB, (unsigned)(((t + 1) % 4) * SB));
        if constexpr (t > 4) {
          wait_w<NDW>();
          lds_inc(lw + LW_L * 4, me);
        }
      });
      MX(2, 19);
      wait_vm<0>();
      lds_inc(lw + LW_L * 4, me);
      pf_pin<0, 30>();
      MK(13);
    }
    {
      unsigned xoff[NDX];
#pragma unroll
      for (int i = 0; i < NDX; ++i) {
        const int c = wl * 256 + i * 64 + lane;
        const int row = c >> 3, cc = c & 7;
        xoff[i] = (unsigned)((mi * 128 + row) * S_ * 2) + (unsigned)((cc ^ ((row >> 1) & 7)) * 16);
      }
      auto dma_x = [&](const char* xp, unsigned so) {
#pragma unroll
        for (int i = 0; i < NDX; ++i) dma16(xp, xoff[i], m0x + so + (unsigned)(i * 1024));
      };
      const char* xp = uni_ptr((const char*)w_sp);
      const char* wp = uni_ptr(ws + OFF_Z2 + (size_t)layer * SZ_Z + (size_t)ni * S_ * 288);
#define XEARLY 0
      if (!XEARLY) lds_spin4<LSL>(lw + LW_AD * 4, 5u);
      dma_x(xp + mi * 256, (unsigned)(2 * SB));
      dma_x(xp + mi * 256 + 128, (unsigned)(3 * SB));
      if (XEARLY) lds_spin4<LSL>(lw + LW_AD * 4, 5u);
      MK(24);
      int left = 2 * mi;
      auto next = [&]() {
        const int d = --left == 0 ? 3 : 1;
        xp += d * 128;
        wp += d * C::WB;
      };
      if (mi == 0) {
        xp += 256;
        wp += 2 * C::WB;
      }
      dma_x(xp, 0u);
      lds_spin4(lw + LW_S * 4, 6u);
      dma_x(xp + 128, (unsigned)SB);
      wait_vm<2 * NDX>();
      lds_inc(lw + LW_L * 4, me);
      MK(27);
      if (wl == 0) {
        cnt_wait(ws, CN_Z + layer, mi * 16 + ni, 16u);
        lds_wr32(lw + LW_GO * 4, 1u);
      } else {
        lds_spin(lw + LW_GO * 4, 1u);
      }
      MK(14);
      lds_spin4(lw + LW_S * 4, 8u);
      dma_w(wp, 0u);
      lds_spin4(lw + LW_S * 4, 9u);
      dma_w(wp + C::WB, (unsigned)SB);
      next();
      next();
      unsigned slot = (unsigned)(2 * SB);
      auto cissue = [&]() {
        dma_x(xp, slot);
        dma_w(wp, slot);
        next();
        slot = (slot == (unsigned)((NS - 1) * SB)) ? 0u : slot + (unsigned)SB;
      };
      auto cstep = [&]() {
        bar();
        cissue();
      };
      wait_w<NDW>();
      MK(15);
      int j = 2;
      if (layer == 0) {
        const char* sp = uni_ptr((const char*)w_sp + (size_t)(bid * 4 + wl) * 8192 + WSP_V * 512);
        unsigned d = 0u;
#pragma unroll 1
        for (; j < 2 + 32 - 4 * WSP_V; ++j) {
          cstep();
          asm volatile("s_load_dword %0, %1, 0\n\ts_load_dword %0, %1, 64\n\ts_load_dword %0, %1, 0x1000\n\ts_load_dword %0, %1, 0x1040"
                       : "+s"(d)
                       : "s"(sp));
          sp += 128;
          wait_w<VPS>();
        }
#pragma unroll 1
        for (int e = j + 4; j < e; ++j) {
          cstep();
          asm volatile("" : "+s"(d));
          wait_w<VPS>();
        }
        asm volatile("s_waitcnt lgkmcnt(0)" : "+s"(d));
      }
      place<AL_L>();
#pragma unroll 1
      for (; j <= 29; ++j) {
        cstep();
        wait_w<VPS>();
      }
      bar();
    }
    auto w5piece = [&](unsigned off) {
      dma16_sc1(w5p, lane16 + (off >= 54u * 1024u ? off - 54u * 1024u : off + 90u * 1024u), lds0 + (unsigned)P5_W + off);
    };
#pragma unroll
    for (int i = 0; i < 15; ++i) {
      const int p = 4 * i + wl;
      if (i < 14 || wl == 0) w5piece((unsigned)((p < 34 ? 53 + p : 121 - 34 + p) * 1024));
    }
    wait_vm<14>();
    bar();
#pragma unroll
    for (int i = 0; i < 9; ++i)
      if (i < 8 || wl < 2) w5piece((unsigned)((87 + 4 * i + wl) * 1024));
    wait_vm<0>();
    bar();
    if (wl) {
#pragma unroll
      for (int i = 0; i < 18; ++i)
        if (i < 17 || wl < 3) w5piece((unsigned)((3 * i + wl - 1) * 1024));
    } else {
      cnt_wait(ws, CN_H + layer, rb * 8 + ct, HSPLIT ? 8u : 16u);
    }
    MK(20);
    bar();
    p5_steps(w5p, std::make_integer_sequence<int, 7>{});
    if (layer == 0) {
      if (bid < 24 && wl == 0 && lane < 8) pk_st16(ws + OFF_HELLO + bid * 128 + lane16, u32x4{0u, 0u, 0u, 0u});
    }
  }
};

__global__ __launch_bounds__(768, 3) void k_main(const u16* __restrict__ x0, const u16* __restrict__ wqkv, const u16* __restrict__ wo,
                                                 const u16* __restrict__ w_in, const u16* __restrict__ w_sp,
                                                 const u16* __restrict__ b_sp, const u16* __restrict__ w_out,
                                                 u16* __restrict__ out, char* ws) {
  extern __shared__ __attribute__((aligned(16))) char smem[];
  const char* code0 = code_here();
  (void)code0;
  const unsigned lds0 = (unsigned)(size_t)smem;
  int wave = __builtin_amdgcn_readfirstlane((int)threadIdx.x >> 6);
  int bid = blockIdx.x;
#define FRESH() asm volatile("" : "+s"(bid), "+s"(ws), "+s"(wave))
#define XCD (bid & 7)
#define IDX (bid >> 3)
#define MI ((XCD >> 2) * 8 + (IDX >> 2))
#define NI ((XCD & 1) * 8 + (XCD & 2) * 2 + (IDX & 3))
#define RB (XCD * 4 + (IDX >> 3))
#define CT (IDX & 7)
#define P1MAP 0
#define RT ((XCD >> 1) * 4 + (IDX >> 3))
#define HP ((XCD & 1) * 8 + (IDX & 7))
  MK(0);
  if (CT0 && wave == 2) {
    constexpr int PER = (2 * NL_MAIN + 31) / 32;
    const int lane = lane_id();
    int li = (CT0 == 2 ? lane * 32 + IDX : IDX * PER + (lane < PER ? lane : PER - 1));
    if (li > 2 * NL_MAIN - 1) li = 2 * NL_MAIN - 1;
    touch64(code0, (unsigned)li * 64u, lds0 + (unsigned)(K1R_DUMMY + 768));
  }
  if (wave >= 3 && wave < 8) {
    constexpr int ST = 64;
    const int k = (7 - wave) * 64 + lane_id();
    const bool isx = k < 9216 / ST;
    int o = isx ? bid * 9216 + k * ST : bid * 7776 + (k - 9216 / ST) * ST;
    if (!isx && k - 9216 / ST > 7775 / ST) o = bid * 7776;
    const char* p = (isx ? (const char*)x0 : (const char*)wqkv) + o;
    asm volatile("s_mov_b32 m0, %1\n\ts_nop 0\n\tglobal_load_lds_dword %0, off" ::"v"(p), "s"(lds0 + (unsigned)K1R_DUMMY) : "memory");
  }

  if (__builtin_expect(wave >= 8, 1)) {
    __builtin_amdgcn_s_setprio(3);
    {
      const int lane = lane_id();
      RawLd rl;
      const bool cross = (((HP * 72) & ~15) & 127) > 48;
      const int c16 = IDX >> 1;
      const int job = cross ? (c16 < 11 ? 128 + XCD * 11 + c16 : 999) : XCD * 16 + c16;
      const int pj = cross ? (c16 >= 11 ? XCD * 5 + c16 - 11 : -1) : (XCD < 4 && (c16 & 7) == 0 ? 40 + XCD * 2 + (c16 >> 3) : -1);
      rl.front(x0, wqkv, wo, ws, bid, lds0, wave - 8, lane, RT, HP, pj MF_PASS);
      if (job < 216) k1_raw_loader<true>(rl, w_in, w_out, ws, rl.tag, bid, lds0, wave - 8, lane, RT, HP, CT, job, pj MF_PASS);
      else k1_raw_loader<false>(rl, w_in, w_out, ws, rl.tag, bid, lds0, wave - 8, lane, RT, HP, CT, job, pj MF_PASS);
    }
    Loader ld;
    ld.lds0 = lds0;
#pragma unroll 1
    for (int layer = 0; layer < 2; ++layer) {
      FRESH();
      ld.wl = wave - 8;
      ld.lane = lane_id();
      ld.lane16 = (unsigned)ld.lane * 16u;
      const char* wip = uni_ptr(ws + OFF_PI + (size_t)NI * (18 * K3C::WB));
      if (layer) {
        ld.p1(smem, ws, bid, RT, HP MK_PASS);
        ld.pf_load<0, 21, 0, 81>(uni_ptr(ws + OFF_PO + (size_t)CT * (18 * 4608)));
        ld.pf_load<21, 9, 36, 72>(wip);
        wait_vm<0>();
        ld.pf_pin<0, 30>();
        bar();
        ld.pf_put<0, 0, 40>((unsigned)P2_W);
        ld.pf_put<0, 40, 41>((unsigned)(P2_W + 40 * 1024));
      } else {
        ld.pf_undef<21, 9>();
        MK(3);
        MK(4);
        bar();
        const char* wp = uni_ptr(ws + OFF_PO + (size_t)CT * (18 * 4608) + ld.wl * 1024);
#pragma unroll
        for (int i = 0; i < 21; ++i)
          if (i < 20 || ld.wl == 0) dma16_sc1(wp + i * 4096, ld.lane16, lds0 + (unsigned)(P2_W + i * 4096 + ld.wl * 1024));
      }
      if (layer == 0 && ld.wl == 0) cnt_wait(ws, CN_O + layer, RB * 8 + CT, 16u);
      MK(6);
      asm volatile("s_waitcnt vmcnt(0) lgkmcnt(0)" ::: "memory");
      bar();
      lds_spin(lds0 + (unsigned)(LW_OFF + (LW_T + layer) * 4), 12u);
      if (layer == 0) {
        if (ld.wl == 0) {
          cnt_wait(ws, CN_PK, 8 + NI, 36u);
          lds_wr32(lds0 + (unsigned)(LW_OFF + LW_HELLO * 4), 2u);
        } else {
          lds_spin(lds0 + (unsigned)(LW_OFF + LW_HELLO * 4), 2u);
        }
        MK(7);
      }
      static_assert(2 * K3C::SB <= P2_W, "free during the first residual GEMM");
      ld.dma_run<18>(wip, (unsigned)K3C::XB);
      ld.dma_run<18>(wip + K3C::WB, (unsigned)(K3C::SB + K3C::XB));
      if (L0REG && layer == 0) ld.pf_load<21, 9, 36, 72>(wip);
      if (layer == 0 && ld.wl == 0) cnt_wait(ws, CN_PK, 24 + CT, 72u);
      wait_vm<0>();
      ld.pf_pin<21, 9>();
      bar();
      FRESH();
      ld.wl = wave - 8;
      ld.p35(smem, ws, w_sp, layer, bid, MI, NI, RB, CT, HP MK_PASS);
    }
  } else {
    MF(12);
    math_start(smem, code0, b_sp, wqkv, ws, bid, wave, lane_id());
    MF(13);
#pragma unroll 1
    for (int layer = 0; layer < 2; ++layer) {
      FRESH();
      {
        const int lane = lane_id(), r0 = RT * 128;
        float* csq = reinterpret_cast<float*>(ws + OFF_CSQ + bid * 512);
        Attn at;
        at.setup(lds0 + (unsigned)(layer ? K1_T_OFF : K1R_T_OFF), ws + OFF_O + (size_t)layer * SZ_X, r0, HP, wave, lane);
        if (layer) {
          if ((wave & 2) == 0) k1_gemm<2, 4>(smem, ws, csq, RT, HP, (wave & 1) * 2 + (wave & 4), 0, wave >= 4, lane MK_PASS);
          else if (wave < 4) k1_gemm<3, 3>(smem, ws, csq, RT, HP, (wave & 1) * 3, 4, false, lane MK_PASS);
          else if (wave == 6) k1_gemm<2, 3>(smem, ws, csq, RT, HP, 6, 4, true, lane MK_PASS);
          else k1_gemm<1, 7>(smem, ws, csq, RT, HP, 8, 0, true, lane MK_PASS);
        } else {
          if (wave == 0) k1_gemm_raw<3, false>(smem, HP, 0, lane MF_PASS);
          else if (wave < 4) k1_gemm_raw<2, false>(smem, HP, 1 + 2 * wave, lane MF_PASS);
          else if (wave < 7) k1_gemm_raw<2, true>(smem, HP, 2 * (wave - 4), lane MF_PASS);
          else k1_gemm_raw<3, true>(smem, HP, 6, lane MF_PASS);
        }
        msync(lds0 + (unsigned)(LW_OFF + (LW_T + layer) * 4));
        MK(3);
        at.run(r0, HP, wave, lane);
        if (layer == 0) {
          if (wave == 0 && lane < 32)
            asm volatile("global_store_dwordx4 %0, %1, %2 sc1\n\ts_nop 1" ::"v"((unsigned)lane * 16u),
                         "v"(*reinterpret_cast<const u32x4*>(smem + K1R_CS_OFF + lane * 16)), "s"(uni_ptr((const char*)csq))
                         : "memory");
          lds_spin(lds0 + (unsigned)(LW_OFF + LW_HELLO * 4), 1u);
        }
        MK(4);
        announce(lds0 + (unsigned)(LW_OFF + (LW_O + layer * 22 + (wave >> 2)) * 4), 4u, ws, CN_O + layer, (RT * 2 + (wave >> 2)) * 8 + lane,
                 lane < 8);
        MK(5);
        bar();
      }
      FRESH();
      {
        if (wave < 4) {
          const int lane = lane_id();
          u32x2 xv[5];
          ResIn ri;
          ri.setup(layer ? ws + OFF_X1 + SZ_X : (const char*)x0, layer == 0, RB, CT, wave, lane);
          ri.load(xv);
          ResOut ro;
          ro.setup(ws + OFF_X1 + (size_t)layer * 2 * SZ_X, ws + OFF_XS + (size_t)layer * 2 * SZ_XS, out, false, RB, CT, wave, lane);
          f32x4 acc[2][5];
          p2_gemm(acc, xv, lds0, uni_ptr(ws + OFF_O + (size_t)layer * SZ_X + (size_t)RB * (72 * 1024)), layer, wave, lane MK_PASS);
          MK(7);
          ro.run(acc[0], false);
          MK(8);
          announce(lds0 + (unsigned)(LW_OFF + (LW_X1 + layer) * 4), 4u, ws, CN_X1 + layer, (RB >> 1) * 16 + lane, lane < 16);
          MK(9);
          bar();
          FRESH();
          if (wave == 3) cnt_wait(ws, CN_X1 + layer, MI * 16 + NI, 16u);
          bar();
          MK(10);
          p3_g0(smem, ws, layer, MI, NI, wave, lane_id() MK_PASS);
        } else {
          if (layer && wave == 5) cnt_wait(ws, CN_O + layer, RB * 8 + CT, 16u);
          bar();
          bar();
          k3_const(smem, ws, b_sp, layer, bid, MI, NI, wave & 3);
          bar();
          MK(10);
          FRESH();
          p3_g1(smem, ws, code0, bid, layer, MI, wave & 3, lane_id() MK_PASS);
        }
      }
      FRESH();
      {
        const int lane = lane_id();
        if (wave < 4) {
          u32x2 xv[5];
          ResIn ri;
          ri.setup(ws + OFF_X1 + (size_t)layer * 2 * SZ_X, false, RB, CT, wave, lane);
          f32x4 acc[2][5];
          ResOut ro;
          ro.setup(ws + OFF_X1 + SZ_X, ws + OFF_XS + SZ_XS, out, layer != 0, RB, CT, wave, lane);
          ResX<36, 33, 8, P5_W, P5_XW, P5_X0, 3, P5_BAR> gm(acc);
          gm.setup(lds0, uni_ptr(ws + OFF_H + (size_t)layer * SZ_Z + (size_t)RB * (288 * 1024)), wave, lane, (unsigned)P5_RED, layer + 1u);
          announce(lds0 + (unsigned)(LW_OFF + (LW_H + layer * 2 + (wave >> 1)) * 4), 2u, ws, ((HSPLIT && NI >= 8) ? CN_H2 : CN_H) + layer,
                   (MI * 2 + (wave >> 1)) * 8 + lane, lane < 8);
          MK(19);
          if (XVE) ri.load(xv);
          bar();
          if (!XVE) ri.load(xv);
          gm.prow();
          place<AL_P5>();
          bar();
          MK(20);
          gm.prox<0>(std::make_integer_sequence<int, P5_X0>{});
          gm.init<2 * P5_X0>(xv);
          __builtin_amdgcn_sched_barrier(0);
          gm.run();
          MK(21);
          ro.run(acc[0], layer != 0);
          MK(22);
          if (layer == 0) {
            announce(lds0 + (unsigned)(LW_OFF + LW_X2 * 4), 4u, ws, CN_X2, (RB >> 1) * 16 + lane,
                     lane < 16 || (lane < 32 && (RB & 1) && RB < 31));
            MK(23);
          }
        } else {
          bar();
          u32x4 wq[15];
          const int wm = wave & 3;
          const unsigned o = (unsigned)((wm >> 1) * 18432 + 12288 + (wm & 1) * 3072 + lane * 16);
          if (G1W) {
            const char* wp = uni_ptr(ws + OFF_PW + (size_t)CT * (36 * 9216) + 8 * 18432);
#pragma unroll
            for (int i = 0; i < 15; ++i)
              asm volatile("global_load_dwordx4 %0, %1, %2 offset:%3 sc1" : "=v"(wq[i]) : "v"(o), "s"(wp + (i / 3) * 36864), "n"((i % 3) * 1024) : "memory");
          }
          bar();
#pragma unroll
          for (int i = 0; i < 7; ++i) {
            if (G1W && i == 0) asm volatile("s_waitcnt vmcnt(0)" ::: "memory");
            if (HSPLIT && i == HPOLL && wave == 5) {
              if (HPOLL < 2) {
                unsigned f;
                const char* cp = uni_ptr(ws + OFF_CNT + (size_t)((CN_H2 + layer) * 256 + RB * 8 + CT) * 128);
                asm volatile("global_load_dword %0, %1, %2 sc1" : "=v"(f) : "v"(vconst(0u)), "s"(cp) : "memory");
                bar();
                asm volatile("s_waitcnt vmcnt(0)" : "+v"(f) : : "memory");
                if (__builtin_amdgcn_readfirstlane(f) != 7u) cnt_wait(ws, CN_H2 + layer, RB * 8 + CT, 8u);
                continue;
              }
              cnt_wait(ws, CN_H2 + layer, RB * 8 + CT, 8u);
            }
            asm volatile("s_waitcnt lgkmcnt(0)" ::: "memory");
            bar();
            if (G1W && i < 5) {
              const unsigned a = lds0 + o + (unsigned)(P5_W + ((2 * i + 3) % 8) * 18432) - ((i == 2 && wm >= 2) ? 8u * 18432u : 0u);
#pragma unroll
              for (int k = 0; k < 3; ++k) {
                if (P5PACE) __builtin_amdgcn_s_sleep(2 * P5PACE);
                asm volatile("ds_write_b128 %0, %1 offset:%2" ::"v"(a), "v"(wq[3 * i + k]), "n"(k * 1024) : "memory");
              }
            }
          }
          MK(22);
        }
        if (layer == 0 && (!G1E || wave < 4)) bar();
      }
    }
  }
}

}

extern "C" void kernel_launch(void* const* d_in, const int* in_sizes, int n_in, void* d_out, int out_size,
                              void* d_ws, size_t ws_size, hipStream_t stream) {
  const u16* x0 = (const u16*)d_in[0];
  const u16* wqkv = (const u16*)d_in[1];
  const u16* wo = (const u16*)d_in[2];
  const u16* w_in = (const u16*)d_in[4];
  const u16* w_sp = (const u16*)d_in[5];
  const u16* b_sp = (const u16*)d_in[6];
  const u16* w_out = (const u16*)d_in[7];
  char* ws = (char*)d_ws;
  (void)hipFuncSetAttribute((const void*)k_main, hipFuncAttributeMaxDynamicSharedMemorySize, LDS_ALL);
  k_main<<<256, 768, LDS_ALL, stream>>>(x0, wqkv, wo, w_in, w_sp, b_sp, w_out, (u16*)d_out, ws);
}
```

```cpp
#include <hip/hip_runtime.h>
#include <hip/hip_bf16.h>
#include <stdint.h>

#define DEVI __device__ __forceinline__

typedef unsigned short u16;
typedef __attribute__((ext_vector_type(8))) short s16x8;
typedef __attribute__((ext_vector_type(4))) short s16x4;
typedef __attribute__((ext_vector_type(4))) float f32x4;
typedef __attribute__((ext_vector_type(2))) float f32x2;
typedef __attribute__((ext_vector_type(2))) __bf16 bf16x2;
typedef __attribute__((ext_vector_type(2))) unsigned u32x2;
typedef __attribute__((ext_vector_type(4))) unsigned u32x4;

#define MK(I) ((void)0)
#define MX(S, I) ((void)0)
#define MF(I) ((void)0)
#define MF_ARG
#define MF_PASS
#define MK_ARG
#define MK_PASS

#define SPIN_LDS for (;;)
#define SPIN_MEM (1u << 22)

#define G1W 0
#define P5PACE 0
#define L0REG 0
#define RTC 0
#define P1PR 3
#define HSPLIT 1
#define HPOLL 1
#define XA4 1
#define P5ORD 0
#define CT0 1
#define AL_A 0
#define AL_B 0
#define AL_C 0
#define AL_P5 0
#define AL_P2 0
#define AL_G -1
#define AL_K 0
#define AL_L 0
#define CSL 1
#define NOPL 0
#define G1E 1
#define XVE 0
#define ZSL 11
#define NR1 3
#define P2_X0 4
#define W01M 0
#define BEARLY 0
#define G1SL 40
#define SAFE_OUT(L)
#define NOP_(n) "=s"(n)
#define MPOLL 1
namespace {

constexpr int S_ = 2048, D_ = 576, F_ = 4608, FH_ = 2304;
constexpr int NQKV = 3 * D_;

constexpr size_t SZ_X = (size_t)S_ * D_ * 2;
constexpr size_t SZ_Z = (size_t)S_ * FH_ * 2;
constexpr size_t OFF_X1 = 0;
constexpr size_t OFF_O = OFF_X1 + 3 * SZ_X;
constexpr size_t OFF_Z2 = OFF_O + 2 * SZ_X;
constexpr size_t OFF_H = OFF_Z2 + 2 * SZ_Z;
constexpr size_t SZ_XS = (size_t)S_ * 8 * 8;
constexpr size_t OFF_XS = OFF_H + 2 * SZ_Z;
constexpr size_t OFF_CSQ = OFF_XS + 3 * SZ_XS;
constexpr size_t OFF_ZP = OFF_CSQ + 256 * 512;
constexpr size_t SZ_ZP = (size_t)16 * 16 * 1024;
constexpr size_t OFF_PQ = OFF_ZP + 2 * SZ_ZP;
constexpr size_t OFF_PO = OFF_PQ + (size_t)16 * 9 * 64 * 112 * 2;
constexpr size_t OFF_PI = OFF_PO + (size_t)D_ * D_ * 2;
constexpr size_t OFF_PW = OFF_PI + (size_t)32 * 9 * 64 * 144 * 2;
constexpr size_t OFF_CSP = OFF_PW + (size_t)FH_ * D_ * 2;
constexpr size_t OFF_CNT = OFF_CSP + (size_t)18 * F_ * 4;
constexpr int CN_X1 = 0, CN_H = 2, CN_X2 = 4, CN_O = 5, CN_Z = 7, CN_PK = 9, CN_H2 = 10, NCNT = 12;
constexpr size_t OFF_TAG = OFF_CNT + (size_t)NCNT * 256 * 128;
constexpr size_t OFF_HELLO = OFF_TAG + 128;
constexpr size_t OFF_POT = OFF_HELLO + 2048;
constexpr size_t OFF_CSK = OFF_POT + 1024;
constexpr size_t WS_END = OFF_CSK + 256 * 2048;

DEVI float bf_lo(unsigned u) { return __uint_as_float(u << 16); }
DEVI float bf_hi(unsigned u) { return __uint_as_float(u & 0xffff0000u); }
DEVI unsigned pack2(float a, float b) {
  f32x2 v = {a, b};
  bf16x2 r = __builtin_convertvector(v, bf16x2);
  return __builtin_bit_cast(unsigned, r);
}
DEVI float rbf(float a) {
  return bf_lo(pack2(a, 0.f));
}

DEVI float sum_g(float x) {
  const unsigned u = __float_as_uint(x);
  const auto p = __builtin_amdgcn_permlane32_swap(u, u, false, false);
  const unsigned v = __float_as_uint(__uint_as_float(p[0]) + __uint_as_float(p[1]));
  const auto q = __builtin_amdgcn_permlane16_swap(v, v, false, false);
  return __uint_as_float(q[0]) + __uint_as_float(q[1]);
}

DEVI unsigned vconst(unsigned c) {
  unsigned r;
  asm volatile("v_mov_b32 %0, %1" : "=v"(r) : "s"(c));
  return r;
}
template <int K>
DEVI void place() {
  if constexpr (K >= 0) {
    __builtin_amdgcn_sched_barrier(0);
    asm volatile(".p2align 6\n\t.fill %0, 4, 0xbf800000" ::"n"(K));
    __builtin_amdgcn_sched_barrier(0);
  }
}
DEVI const char* uni_ptr(const char* p) {
  unsigned long long v = (unsigned long long)p;
  unsigned lo = __builtin_amdgcn_readfirstlane((unsigned)v), hi = __builtin_amdgcn_readfirstlane((unsigned)(v >> 32));
  asm("s_nop 4" : "+s"(lo), "+s"(hi));
  return (const char*)(((unsigned long long)hi << 32) | lo);
}

DEVI void dma16(const char* sbase, unsigned voff, unsigned m0val) {
  asm volatile("s_mov_b32 m0, %2\n\ts_nop 0\n\tglobal_load_lds_dwordx4 %0, %1"
               :
               : "v"(voff), "s"(sbase), "s"(m0val)
               : "memory");
}

DEVI void ln_finish(float s, float q, float invn, float& mb, float& rinv) {
  float mean = s * invn;
  asm("" : "+v"(mean));
  float var = fmaxf(__builtin_fmaf(-mean, mean, q * invn), 0.f);
  mb = rbf(mean);
  float v = rbf(var);
  float t = rbf(v + 1e-5f);
  float sd = rbf(__builtin_amdgcn_sqrtf(t));
  rinv = __builtin_amdgcn_rcpf(sd);
}

DEVI unsigned pack2v(f32x2 v) { return pack2(v[0], v[1]); }

template <int BM_, int BN_>
struct GemmCfg {
  static constexpr int BM = BM_, BN = BN_, NS = 4;
  static constexpr int P = BN * 2;
  static constexpr int XB = BM * 128, WB = 64 * P, SB = XB + WB;
  static constexpr int LDS_BYTES = NS * SB;
};

DEVI int swap23(int r) { return (r & ~12) | ((r & 4) << 1) | ((r & 8) >> 1); }

DEVI unsigned lds_rd32(unsigned addr) {
  unsigned v;
  asm volatile("ds_read_b32 %0, %1\n\ts_waitcnt lgkmcnt(0)" : "=v"(v) : "v"(addr) : "memory");
  return v;
}
DEVI void lds_wr32(unsigned addr, unsigned v) { asm volatile("ds_write_b32 %0, %1" ::"v"(addr), "v"(v) : "memory"); }

template <class C, int MT, int NT, int D, int R, bool XCM, int WT, bool SQ = false, unsigned long long WS = 0>
struct GemmMath {
  static constexpr int WO = WT ? 1 : 2;
  static constexpr int NS = C::NS, SB = C::SB, P = C::P, NU = 2 * NT;
  static constexpr int XPU = MT <= 2 ? MT : MT - 2;
  static constexpr int XU = (MT + XPU - 1) / XPU;
  static_assert(NU % R == 0 && R > D && D <= NT && XU <= D, "shape");
  s16x4 wlo[R], whi[R];
  s16x8 wf[R];
  s16x8 xf[2][MT];
  unsigned xrd[2], wrd, xa[2], wa, so;
  f32x4 (&acc)[MT][NT];
  f32x4 accq[SQ ? MT : 1];
  unsigned long long wtab = WS;
  DEVI GemmMath(f32x4 (&a)[MT][NT]) : acc(a) {}

  template <int PU>
  DEVI void wread_lo() {
    constexpr int off = (PU / NT) * 32 * P + (PU % NT) * (WT == 2 ? 1024 : 32);
    if constexpr (WT) asm volatile("ds_read_b128 %0, %1 offset:%2" : "=v"(wf[PU % R]) : "v"(wa), "n"(off));
    else asm volatile("ds_read_b64_tr_b16 %0, %1 offset:%2" : "=v"(wlo[PU % R]) : "v"(wa), "n"(off));
  }
  template <int PU>
  DEVI void wread_hi() {
    if constexpr (!WT)
      asm volatile("ds_read_b64_tr_b16 %0, %1 offset:%2" : "=v"(whi[PU % R]) : "v"(wa), "n"((PU / NT) * 32 * P + (PU % NT) * 32 + 8 * P));
  }
  template <int H, int M>
  DEVI void xread1() {
    asm volatile("ds_read_b128 %0, %1 offset:%2" : "=v"(xf[H][M]) : "v"(xa[H]), "n"(M * (XCM ? 256 : 2048)));
  }
  static constexpr int xops(int v) {
    const int k = (v + D) % NT;
    if (k >= XU) return 0;
    const int n = MT - k * XPU;
    return n < XPU ? n : XPU;
  }
  static constexpr int ops(int v, bool last) { return (last && v + D >= NU) ? 0 : WO + xops(v); }
  static constexpr int newer(int U, bool last) {
    int n = 0;
    int v0 = U - D;
    if (U % NT == 0) v0 += XU - 1;
    else n += v0 >= 0 ? xops(v0) : xops(v0 + NU);
    for (int v = v0 + 1; v < U; ++v) n += v >= 0 ? ops(v, last) : ops(v + NU, false);
    return n;
  }
  template <int U, int M_>
  DEVI void mf() {
    constexpr int M = ((U % NT) & 1) ? MT - 1 - M_ : M_;
    s16x8 w;
    if constexpr (WT) w = wf[U % R];
    else w = __builtin_shufflevector(wlo[U % R], whi[U % R], 0, 1, 2, 3, 4, 5, 6, 7);
    acc[M][U % NT] = __builtin_amdgcn_mfma_f32_16x16x32_bf16(w, xf[U / NT][M], acc[M][U % NT], 0, 0, 0);
    __builtin_amdgcn_sched_barrier(0);
  }
  DEVI void pin() {
#pragma unroll
    for (int m = 0; m < MT; ++m)
#pragma unroll
      for (int n = 0; n < NT; ++n) asm volatile("" : "+v"(acc[m][n]));
  }
  template <int U, int I>
  DEVI void dsop() {
    constexpr int PU = (U + D) % NU;
    if constexpr (I == 0) wread_lo<PU>();
    else if constexpr (I == 1) wread_hi<PU>();
    else if constexpr (I - 2 < xops(U)) {
      constexpr int k = (U + D) % NT;
      xread1<((U + D - k) % NU) / NT, k * XPU + I - 2>();
    }
  }
  template <int U, bool LAST, bool BAR, int M>
  DEVI void slot() {
    constexpr bool PF = !(LAST && U + D >= NU);
    mf<U, M>();
    if constexpr (M == 0 && PF && U + D == NU) {
      if constexpr (BAR) __builtin_amdgcn_s_barrier();
      so = (so == (unsigned)((NS - 1) * SB)) ? 0u : so + (unsigned)SB;
      wa = wrd + (WS ? ((unsigned)wtab & 63u) << 11 : so);
      wtab >>= 6;
      xa[0] = xrd[0] + so;
      xa[1] = xrd[1] + so;
      __builtin_amdgcn_sched_barrier(0);
    }
    if constexpr (PF) {
      dsop<U, M>();
      if constexpr (M == MT - 1) {
        if constexpr (MT <= 1) dsop<U, 1>();
        if constexpr (MT <= 2) {
          dsop<U, 2>();
          dsop<U, 3>();
        }
      }
      __builtin_amdgcn_sched_barrier(0);
    }
  }
  template <int U, bool LAST, bool BAR, int... M>
  DEVI void unit(std::integer_sequence<int, M...>) {
    static_assert(newer(U, LAST) <= 15, "lgkmcnt range");
    asm volatile("s_waitcnt lgkmcnt(%0)" ::"n"(newer(U, LAST)));
    __builtin_amdgcn_sched_barrier(0);
    (slot<U, LAST, BAR, M>(), ...);
    if constexpr (SQ && (U % NT) < MT) {
      constexpr int m = U % NT;
      accq[m] = __builtin_amdgcn_mfma_f32_16x16x32_bf16(xf[U / NT][m], xf[U / NT][m], accq[m], 0, 0, 0);
      __builtin_amdgcn_sched_barrier(0);
    }
    __builtin_amdgcn_sched_barrier(0);
  }
  template <bool LAST, bool BAR, int... U>
  DEVI void stage(std::integer_sequence<int, U...>) {
    (unit<U, LAST, BAR>(std::make_integer_sequence<int, MT>{}), ...);
  }
  template <int... U>
  DEVI void prow(std::integer_sequence<int, U...>) {
    ((wread_lo<U>(), wread_hi<U>()), ...);
  }
  template <int... M>
  DEVI void prox(std::integer_sequence<int, M...>) {
    (xread1<0, M>(), ...);
  }
  DEVI void setup(unsigned lds0, int mt0, int nt0, int lane, unsigned so0) {
    const int g = lane >> 4, r = lane & 15, q = (lane >> 2) & 3, p = lane & 3;
#pragma unroll
    for (int h = 0; h < 2; ++h)
      xrd[h] = XCM ? lds0 + (unsigned)((4 * h + g) * (C::BM * 16) + (mt0 * 16 + r) * 16)
                   : lds0 + (unsigned)((mt0 * 16 + r) * 128 + (((4 * h + g) ^ ((r >> 1) & 7)) * 16));
    wrd = WT == 2   ? lds0 + (unsigned)(C::XB + nt0 * 1024 + lane * 16)
          : WT == 1 ? lds0 + (unsigned)(C::XB + (g * 8 + (r & 7)) * P + (r >> 3) * 16 + nt0 * 32)
                    : lds0 + (unsigned)(C::XB + (16 * (g >> 1) + 4 * (g & 1) + q) * P + 8 * p + nt0 * 32);
    so = so0;
    xa[0] = xrd[0] + so;
    xa[1] = xrd[1] + so;
    wa = wrd + so;
  }
  DEVI void first() {
    prox(std::make_integer_sequence<int, MT>{});
    prow(std::make_integer_sequence<int, D>{});
    asm volatile("s_waitcnt lgkmcnt(0)");
  }
  template <int N, bool BAR = true, int AL = -1>
  DEVI void mid() {
    place<AL>();
#pragma unroll 1
    for (int s = 0; s < N; ++s) stage<false, BAR>(std::make_integer_sequence<int, NU>{});
  }
  DEVI void last() {
    stage<true, true>(std::make_integer_sequence<int, NU>{});
    pin();
  }
  template <int KS>
  DEVI void run(unsigned lds0, int mt0, int nt0, int lane, unsigned so0) {
    setup(lds0, mt0, nt0, lane, so0);
    __builtin_amdgcn_s_barrier();
    first();
    mid<KS - 1, true, AL_K>();
    last();
  }
};

DEVI const char* code_here() {
  unsigned long long pc;
  asm volatile("s_getpc_b64 %0" : "=s"(pc));
  return (const char*)pc;
}
DEVI int okmap(int p) {
  const int ch = p >> 3, e = p & 7, c = (ch - 8) & 3;
  return ch < 8 ? (2 * ch + (e >> 2)) * 36 + 16 + (e & 3) : ((ch - 8) >> 2) * 36 + c * 8 + (c >> 1) * 4 + e;
}

DEVI void pk_st16(void* p, u32x4 v) {
  asm volatile("global_store_dwordx4 %0, %1, off sc1\n\ts_nop 1" ::"v"(p), "v"(v) : "memory");
}

using K1C = GemmCfg<144, 112>;
constexpr int K1_TP = 240;
constexpr int K1_T_OFF = K1C::SB;
constexpr int K1_ST_OFF = 163840 - 128 - 1152;
constexpr int K1_CS_OFF = 4 * K1C::SB;
constexpr int K1_WX = 4 * K1C::SB + 2048;
constexpr unsigned long long K1_WS = 16ull | 32ull << 6 | 48ull << 12 | 56ull << 18 | 63ull << 24 | 16ull << 30 | 32ull << 36 | 48ull << 42;
static_assert(K1C::SB == 16 * 2048 && K1_WX - K1C::XB == 56 * 2048 && K1C::WB == 7 * 2048 && K1_WX + 2 * K1C::WB <= K1_ST_OFF, "K1 w places");
static_assert(K1_T_OFF + 144 * K1_TP + 64 <= 2 * K1C::SB + K1C::XB, "K1 LDS alias");
using K1R = GemmCfg<144, 144>;
constexpr int K1R_T_OFF = K1R::SB;
constexpr int K1R_CS_OFF = K1R_T_OFF + 144 * K1_TP;
constexpr int K1R_ST_OFF = K1R_CS_OFF + 512;
constexpr int K1R_DUMMY = K1R::LDS_BYTES;
constexpr int K1R_PO = K1R_DUMMY + 1024;
constexpr int K1R_CNT = 163840 - 128 + 28 * 4;
#define SAFE_AND
#define SPIN_ASM(TEST)                                                                                                   \
  if constexpr (SL <= 1)                                                                                                 \
    asm volatile(".La%=:\n\tds_read_b32 %0, %3\n\ts_waitcnt lgkmcnt(0)\n\tv_readfirstlane_b32 %2, %0\n\t" TEST SAFE_AND \
                 "s_cbranch_scc1 .La%="                                                                                  \
                 : "=&v"(v), NOP_(c), "=&s"(t)                                                                           \
                 : "v"(addr), "s"(k)                                                                                     \
                 : "memory", "scc");                                                                                     \
  else                                                                                                                   \
    asm volatile("s_branch .Lb%=\n.La%=:\n\ts_sleep %5\n.Lb%=:\n\tds_read_b32 %0, %3\n\ts_waitcnt lgkmcnt(0)\n\t"     \
                 "v_readfirstlane_b32 %2, %0\n\t" TEST SAFE_AND "s_cbranch_scc1 .La%="                                  \
                 : "=&v"(v), NOP_(c), "=&s"(t)                                                                           \
                 : "v"(addr), "s"(k), "n"(SL)                                                                            \
                 : "memory", "scc")
template <int SL>
DEVI void spin_ge(unsigned addr, unsigned k) {
  unsigned v, c = 1u << 18, t;
  SPIN_ASM("s_cmp_lt_u32 %2, %4\n\t");
}
template <int SL>
DEVI void spin_b4(unsigned addr, unsigned n) {
  unsigned v, c = 1u << 18, t;
  const unsigned k = (0x80u - n) * 0x01010101u;
  SPIN_ASM("s_add_u32 %2, %2, %4\n\ts_andn2_b32 %2, 0x80808080, %2\n\t");
}
DEVI void msync(unsigned addr) {
  asm volatile("s_waitcnt lgkmcnt(0)" ::: "memory");
  asm volatile("s_mov_b64 exec, 1\n\tds_add_u32 %0, %1\n\ts_mov_b64 exec, -1" ::"v"(addr), "v"(vconst(1u)) : "memory");
  spin_ge<0>(addr, 8u);
}
static_assert(K1R_PO + 13824 + 256 <= 163840 - 128 && K1R_ST_OFF + 144 * 8 <= 3 * K1R::SB, "K1R LDS");

DEVI void touch64(const char* sbase, unsigned voff, unsigned m0val) {
  asm volatile("s_mov_b32 m0, %2\n\ts_nop 0\n\tglobal_load_lds_dword %0, %1" : : "v"(voff), "s"(sbase), "s"(m0val) : "memory");
}

template <int MT, bool G1>
DEVI void k1_gemm_raw(char* smem, int hp, int mt0, int lane MF_ARG) {
  constexpr int NT = 4, nt0 = G1 ? 4 : 0;
  const unsigned lds0 = (unsigned)(size_t)smem;
  const int g = lane >> 4, r = lane & 15;
  unsigned ta[NT];
#pragma unroll
  for (int nt = 0; nt < NT; ++nt) {
    const int j = (nt0 + nt) * 16 + g * 4, seg = j / 40, cj = j - seg * 40 - (hp & 1) * 4;
    const int to = (seg < 3 && cj >= 0 && cj < 36) ? seg * 80 + cj * 2 + (cj >= 20 ? 8 : 0) : (seg < 3 ? 40 : 120);
    ta[nt] = lds0 + (unsigned)(K1R_T_OFF + (mt0 * 16 + r) * K1_TP + to);
    asm volatile("" : "+v"(ta[nt]));
  }
  unsigned sa = lds0 + (unsigned)(K1R_ST_OFF + (mt0 * 16 + r) * 8), ca = lds0 + (unsigned)(K1R_CS_OFF + (nt0 * 16 + g * 4) * 4);
  asm volatile("" : "+v"(sa), "+v"(ca));
  f32x4 acc[MT][NT];
#pragma unroll
  for (int mt = 0; mt < MT; ++mt)
#pragma unroll
    for (int nt = 0; nt < NT; ++nt) acc[mt][nt] = f32x4{0.f, 0.f, 0.f, 0.f};
  {
    GemmMath<K1R, MT, 4, 3, 4, false, 0, G1> gm(acc);
    if constexpr (G1) {
#pragma unroll
      for (int mt = 0; mt < MT; ++mt) gm.accq[mt] = f32x4{0.f, 0.f, 0.f, 0.f};
    }
    MF(14);
    if constexpr (!G1) {
      if (mt0 < 7) asm volatile("s_waitcnt vmcnt(0)" ::: "memory");
    }
    MF(15);
    gm.template run<9>(lds0, mt0, nt0, lane, 0u);
    MF(2);
    if constexpr (G1) {
#pragma unroll
      for (int mt = 0; mt < MT; ++mt) {
        const f32x4 q = gm.accq[mt];
        const float d = (r & 2) ? ((r & 1) ? q[3] : q[2]) : ((r & 1) ? q[1] : q[0]);
        const float qq = sum_g(g == (r >> 2) ? d : 0.f);
        float mb, rinv;
        ln_finish(acc[mt][3][0], qq, 1.0f / (float)D_, mb, rinv);
        const f32x2 st = {-mb, rinv};
        if (g == 2) asm volatile("ds_write_b64 %0, %1 offset:%2" ::"v"(sa), "v"(st), "n"(mt * 128) : "memory");
      }
    }
  }
  if (mt0 == 0 && r == 0) {
#pragma unroll
    for (int nt = 0; nt < NT; ++nt) *reinterpret_cast<f32x4*>(smem + K1R_CS_OFF + ((nt0 + nt) * 16 + g * 4) * 4) = acc[0][nt];
  }
  msync(lds0 + (unsigned)K1R_CNT);
  MF(3);
  f32x2 st[MT];
  f32x4 cs[NT];
#pragma unroll
  for (int nt = 0; nt < NT; ++nt) asm volatile("ds_read_b128 %0, %1 offset:%2" : "=v"(cs[nt]) : "v"(ca), "n"(nt * 64) : "memory");
#pragma unroll
  for (int mt = 0; mt < MT; ++mt) asm volatile("ds_read_b64 %0, %1 offset:%2" : "=v"(st[mt]) : "v"(sa), "n"(mt * 128) : "memory");
#pragma unroll
  for (int i = 0; i < MT; ++i) asm volatile("s_waitcnt lgkmcnt(0)" : "+v"(st[i]));
#pragma unroll
  for (int i = 0; i < NT; ++i) asm volatile("" : "+v"(cs[i]));
#pragma unroll
  for (int mt = 0; mt < MT; ++mt) {
    f32x2 M = {st[mt][0], st[mt][0]}, R = {st[mt][1], st[mt][1]};
    asm volatile("" : "+v"(M), "+v"(R));
#pragma unroll
    for (int nt = 0; nt < NT; ++nt) {
      const f32x2 v0 = R * __builtin_elementwise_fma(M, f32x2{cs[nt][0], cs[nt][1]}, f32x2{acc[mt][nt][0], acc[mt][nt][1]});
      const f32x2 v1 = R * __builtin_elementwise_fma(M, f32x2{cs[nt][2], cs[nt][3]}, f32x2{acc[mt][nt][2], acc[mt][nt][3]});
      acc[mt][nt][0] = __uint_as_float(pack2v(v0));
      acc[mt][nt][1] = __uint_as_float(pack2v(v1));
      const f32x2 pk = {acc[mt][nt][0], acc[mt][nt][1]};
      asm volatile("ds_write_b64 %0, %1 offset:%2" ::"v"(ta[nt]), "v"(pk), "n"(mt * 16 * K1_TP) : "memory");
    }
  }
}

DEVI void dma16m(const char* sbase, unsigned voff, unsigned m0val, unsigned long long mask) {
  asm volatile("s_mov_b32 m0, %2\n\ts_mov_b64 exec, %3\n\tglobal_load_lds_dwordx4 %0, %1\n\ts_mov_b64 exec, -1"
               :
               : "v"(voff), "s"(sbase), "s"(m0val), "s"(mask)
               : "memory");
}
template <int N>
DEVI void wait_vm() {
  asm volatile("s_waitcnt vmcnt(%0)" ::"n"(N) : "memory");
}

constexpr int LDS_ALL = 163840;
constexpr int LW_OFF = LDS_ALL - 128;
constexpr int LW_GO = 0, LW_T = 2, LW_O = 4  , LW_X1 = 6, LW_H = 8, LW_X2 = 12, LW_PK = 13, LW_Z = 14, LW_PAIR = 16, LW_AD = 20,
              LW_L = 22, LW_S = 24, LW_ST = 29, LW_HELLO = 30, LW_Q = 31;
DEVI void announce(unsigned addr, unsigned n, char* ws, int kind, int who, bool act) {
  const char* cb = uni_ptr(ws + OFF_CNT + (size_t)kind * 256 * 128);
  const unsigned long long mask = __ballot(act);
  unsigned t, u;
  asm volatile(
      "s_waitcnt vmcnt(0)\n\t"
      "s_mov_b64 exec, 1\n\t"
      "ds_add_rtn_u32 %0, %2, %3\n\t"
      "s_waitcnt lgkmcnt(0)\n\t"
      "v_readfirstlane_b32 %1, %0\n\t"
      "s_cmp_eq_u32 %1, %4\n\t"
      "s_cselect_b64 exec, %5, 0\n\t"
      "global_atomic_add %6, %3, %7 sc1\n\t"
      "s_mov_b64 exec, -1\n\t"
      "s_nop 0"
      : "=&v"(t), "=&s"(u)
      : "v"(addr), "v"(vconst(1u)), "s"(n - 1u), "s"(mask), "v"((unsigned)(who & 255) * 128u), "s"(cb)
      : "memory", "scc");
}
DEVI void cnt_wait(const char* ws, int kind, int who, unsigned n) {
  const char* cp = uni_ptr(ws + OFF_CNT + (size_t)(kind * 256 + who) * 128);
  unsigned v, c = SPIN_MEM, t;
  asm volatile(
      ".La%=:\n\tglobal_load_dword %0, %3, %4 sc1\n\ts_sub_u32 %1, %1, 1\n\ts_waitcnt vmcnt(0)\n\tv_readfirstlane_b32 %2, %0\n\ts_cmp_lg_u32 %2, %5\n\t"
      "s_cselect_b32 %2, %1, 0\n\ts_cmp_lg_u32 %2, 0\n\ts_cbranch_scc1 .La%="
      : "=&v"(v), "+s"(c), "=&s"(t)
      : "v"(vconst(0u)), "s"(cp), "s"(n - 1u)
      : "memory", "scc");
}
DEVI void dma16_sc1(const char* sbase, unsigned voff, unsigned m0val) {
  asm volatile("s_mov_b32 m0, %2\n\ts_nop 0\n\tglobal_load_lds_dwordx4 %0, %1 sc1"
               :
               : "v"(voff), "s"(sbase), "s"(m0val)
               : "memory");
}
DEVI void bar() { __builtin_amdgcn_s_barrier(); }
DEVI void lds_inc(unsigned addr, unsigned by = 1u) {
  asm volatile("s_mov_b64 exec, 1\n\tds_add_u32 %0, %1\n\ts_mov_b64 exec, -1" ::"v"(addr), "v"(vconst(by)) : "memory");
}
DEVI void lds_spin(unsigned addr, unsigned n) {
  spin_ge<1>(addr, n);
}
template <int SL = 1>
DEVI void lds_spin4(unsigned addr, unsigned n) {
  spin_b4<SL>(addr, n);
}
DEVI int lane_id() {
  int l;
  asm volatile("v_mbcnt_lo_u32_b32 %0, -1, 0\n\tv_mbcnt_hi_u32_b32 %0, -1, %0" : "=v"(l));
  __builtin_assume((unsigned)l < 64u);
  return l;
}

template <int I>
DEVI u32x4 tr8(const u32x4 (&v)[8]) {
  constexpr unsigned sel = (I & 1) ? 0x07060302u : 0x05040100u;
  return u32x4{__builtin_amdgcn_perm(v[1][I / 2], v[0][I / 2], sel), __builtin_amdgcn_perm(v[3][I / 2], v[2][I / 2], sel),
               __builtin_amdgcn_perm(v[5][I / 2], v[4][I / 2], sel), __builtin_amdgcn_perm(v[7][I / 2], v[6][I / 2], sel)};
}
template <int PITCH, bool SUM, int... I>
DEVI void tr8_store(const u32x4 (&v)[8], unsigned dst, float (&cs)[8], std::integer_sequence<int, I...>) {
  auto one = [&](auto I_) {
    const u32x4 o = tr8<I_>(v);
    const unsigned d_ = dst;
    asm volatile("ds_write_b128 %0, %1 offset:%2" ::"v"(d_), "v"(o), "n"(I_ * PITCH) : "memory");
    if constexpr (SUM) {
#pragma unroll
      for (int d = 0; d < 4; ++d) {
        const unsigned od = o[d];
        cs[I_] = __builtin_amdgcn_fdot2_f32_bf16(__builtin_bit_cast(bf16x2, od), __builtin_bit_cast(bf16x2, 0x3f803f80u), cs[I_], false);
      }
    }
  };
  (one(std::integral_constant<int, I>{}), ...);
}
template <int NB>
DEVI void lds_out(unsigned src, char* dst, int lane) {
  constexpr int N = (NB + 1023) / 1024;
  u32x4 t[N];
  const unsigned a = src + (unsigned)lane * 16u;
#pragma unroll
  for (int i = 0; i < N; ++i) asm volatile("ds_read_b128 %0, %1 offset:%2" : "=v"(t[i]) : "v"(a), "n"(i * 1024) : "memory");
  const char* d = uni_ptr(dst);
#pragma unroll
  for (int i = 0; i < N; ++i) {
    asm volatile("s_waitcnt lgkmcnt(%1)" : "+v"(t[i]) : "n"(N - 1 - i) : "memory");
    if (i < N - 1 || lane < (NB - (N - 1) * 1024) / 16)
      asm volatile("global_store_dwordx4 %0, %1, %2 sc1\n\ts_nop 1" ::"v"((unsigned)lane * 16u), "v"(t[i]), "s"(d + i * 1024) : "memory");
  }
}
template <int A, int B, class F>
DEVI void static_for(F f) {
  if constexpr (A < B) {
    f(std::integral_constant<int, A>{});
    static_for<A + 1, B>(f);
  }
}
constexpr int WSP_V = 3;
#define RS0 4
#define RS1 7
#define RX0 RS0
constexpr int RAW_S1 = RS1;
constexpr int RAW_S0 = RS0;
template <int E>
struct RawX {
  static constexpr int first(int s) {
    return s <= RX0 ? 0 : (s > RAW_S1 ? E : (E * (s - RX0) + RAW_S1 - RX0) / (RAW_S1 + 1 - RX0));
  }
  static constexpr int n(int s) { return first(s + 1) - first(s); }
};
struct RawLd {
  static constexpr int VPS = 10, SB = K1R::SB;
  unsigned xoff[5], woff[5], m0x;
  unsigned long long xact0, wact[5], tag;
  const char *xp, *wp;
  int qb;
  DEVI void q_hook(int st, char* ws, unsigned lds0, int hp) {
    if ((qb >> 1) == st) {
      const int lane = lane_id(), g = lane >> 4;
      const unsigned a = lds0 + (unsigned)((st & 3) * SB + K1R::XB + ((qb & 1) * 32 + 16 * (g >> 1) + 4 * (g & 1) + ((lane >> 2) & 3)) * 288 +
                                           (lane & 3) * 8 + (hp & 1) * 8);
      const unsigned a2 = a + ((lane & 3) >= 1 ? 8u : 0u), a4 = a + ((lane & 3) >= 2 ? 8u : 0u);
      u32x2 qf[7][2];
#pragma unroll
      for (int nt = 0; nt < 7; ++nt)
        asm volatile("ds_read_b64_tr_b16 %0, %2 offset:%3\n\tds_read_b64_tr_b16 %1, %2 offset:%4"
                     : "=&v"(qf[nt][0]), "=&v"(qf[nt][1])
                     : "v"(nt == 2 ? a2 : (nt == 4 ? a4 : a)), "n"(nt * 32 + (nt > 4 ? 16 : (nt > 2 ? 8 : 0))),
                       "n"(2304 + nt * 32 + (nt > 4 ? 16 : (nt > 2 ? 8 : 0)))
                     : "memory");
      const char* d = uni_ptr(ws + OFF_PQ + (size_t)(hp * 18 + qb) * 7168 + 3072);
#pragma unroll
      for (int nt = 0; nt < 7; ++nt) {
        asm volatile("s_waitcnt lgkmcnt(%2)" : "+v"(qf[nt][0]), "+v"(qf[nt][1]) : "n"(12 - 2 * nt) : "memory");
        const u32x4 v = {qf[nt][0][0], qf[nt][0][1], qf[nt][1][0], qf[nt][1][1]};
        asm volatile("global_store_dwordx4 %0, %1, %2 offset:%3 sc1\n\ts_nop 1" ::"v"((unsigned)lane * 16u), "v"(v), "s"(d), "n"(nt * 1024 - 3072) : "memory");
      }
      if (st == 8) lds_wr32(lds0 + (unsigned)(LW_OFF + LW_Q * 4), 1u);
    }
  }
  DEVI void stage_x(unsigned so) {
    dma16m(xp, xoff[0], m0x + so, xact0);
#pragma unroll
    for (int i = 1; i < 4; ++i) dma16(xp, xoff[i], m0x + so + (unsigned)(i * 1024));
    dma16m(xp, xoff[4], m0x + so + 4096u, 0xffffffffull);
    xp += 128;
  }
  DEVI void stage_w(unsigned so) {
#pragma unroll
    for (int i = 0; i < 5; ++i) dma16m(wp, woff[i], m0x + so + (unsigned)(K1R::XB + i * 1024), wact[i]);
    wp += 64 * NQKV * 2;
  }
  DEVI void stage(unsigned so) {
    stage_x(so);
    stage_w(so);
  }
  DEVI void front(const u16* x, const u16* wqkv, const u16* wo, char* ws, int bid, unsigned lds0, int wl, int lane, int rt, int hp,
                  int pj MF_ARG) {
    xp = uni_ptr((const char*)x);
    wp = uni_ptr((const char*)wqkv + ((hp * 72) & ~15));
    m0x = lds0 + (unsigned)(wl * 288 * 16);
    xact0 = wl ? ~0ull : ~0xffull;
    const int row0 = wl * 36 + (lane >> 3);
#pragma unroll
    for (int i = 0; i < 5; ++i) {
      const int row = row0 + i * 8;
      int gr = rt * 128 - 16 + row;
      if (gr < 0) gr = 0;
      xoff[i] = (unsigned)(gr * (D_ * 2)) + (unsigned)(((lane ^ (row >> 1)) & 7) * 16);
    }
    stage_x(0u);
    u32x2 tg;
    asm volatile("global_load_dwordx2 %0, %1, %2 sc1" : "=v"(tg) : "v"(0u), "s"(uni_ptr(ws + OFF_TAG)) : "memory");
    asm volatile("" : "+v"(lane));
#pragma unroll
    for (int i = 0; i < 5; ++i) {
      const unsigned c = (unsigned)(i * 64 + (lane & 63));
      const unsigned wr = (c * 3641u) >> 16, wc = c - wr * 18u, seg = (wc * 13u) >> 6;
      wact[i] = __ballot(wc < 15u) & (i < 4 ? ~0ull : 0xffffffffull);
      woff[i] = (unsigned)(wl * 16 + swap23((int)wr)) * (unsigned)(NQKV * 2) + seg * (unsigned)(D_ * 2 - 80) + wc * 16u;
    }
    stage_w(0u);
    stage((unsigned)SB);
    MF(10);
    asm volatile("s_waitcnt vmcnt(%1)" : "+v"(tg) : "n"(VPS) : "memory");
    tag = (((unsigned long long)(unsigned)__builtin_amdgcn_readfirstlane(tg[1]) << 32) | (unsigned)__builtin_amdgcn_readfirstlane(tg[0])) +
          0x9e3779b97f4a7c15ull;
    MF(1);
    __builtin_amdgcn_s_barrier();
    MF(15);
    if (wl == 0 && lane == 0)
      asm volatile("global_store_dwordx2 %0, %1, off sc1\n\ts_nop 1" ::"v"(ws + OFF_HELLO + bid * 8), "v"(tag) : "memory");
    stage((unsigned)(2 * SB));
    qb = wl == 3 ? rt : ((wl == 2 && rt < 2) ? 16 + rt : 99);
    q_hook(0, ws, lds0, hp);
    const int nb = pj;
    const bool po = pj >= 0 && wl < 2;
    u32x4 v[8];
    unsigned da = 0u;
    if (po) {
      const int l54 = lane < 54 ? lane : 53, gl = l54 / 9, c = l54 - gl * 9, kl = wl * 6 + gl, t = nb / 6, kg = (nb - t * 6) * 12 + kl;
      da = lds0 + (unsigned)(K1R_PO + (kl >> 2) * 4608 + (16 * (kl & 1) + ((kl >> 1) & 1)) * 144 + c * 16);
      const unsigned sa[2] = {(unsigned)(okmap(kg * 8) * D_ + t * 72 + c * 8) * 2u, (unsigned)(okmap(kg * 8 + 4) * D_ + t * 72 + c * 8) * 2u};
      const char* ps = uni_ptr((const char*)wo);
#pragma unroll
      for (int j = 0; j < 8; ++j)
        asm volatile("global_load_dwordx4 %0, %1, %2 offset:%3" : "=v"(v[j]) : "v"(sa[j / 4]), "s"(ps), "n"((j % 4) * D_ * 2) : "memory");
    } else {
#pragma unroll
      for (int j = 0; j < 8; ++j) asm volatile("" : "=v"(v[j]));
    }
    unsigned slot = (unsigned)(3 * SB);
#pragma unroll
    for (int S = 0; S < RAW_S0; ++S) {
      wait_vm<VPS>();
      __builtin_amdgcn_s_barrier();
      if (S == 0) MF(16);
      if (S == 1) MF(17);
      if (S == 2) MF(18);
      if (S == 3) MF(19);
      stage(slot);
      slot = (slot == (unsigned)(3 * SB)) ? 0u : slot + (unsigned)SB;
      q_hook(S + 1, ws, lds0, hp);
      if (po && S == 1) {
        float cs[8];
        asm volatile("" : "+v"(v[0]), "+v"(v[1]), "+v"(v[2]), "+v"(v[3]), "+v"(v[4]), "+v"(v[5]), "+v"(v[6]), "+v"(v[7]));
        tr8_store<288, false>(v, da, cs, std::make_integer_sequence<int, 8>{});
        asm volatile("s_waitcnt lgkmcnt(0)" ::: "memory");
      }
      if (po && S == 2) lds_out<6912>(lds0 + (unsigned)(K1R_PO + wl * 6912), ws + OFF_PO + (size_t)nb * 13824 + wl * 6912, lane);
    }
  }
};
template <bool JOB>
DEVI void k1_raw_loader(RawLd& ld, const u16* __restrict__ w_in, const u16* __restrict__ w_out, char* __restrict__ ws,
                        unsigned long long tag, int bid, unsigned lds0, int wl, int lane, int rt, int hp, int ct, int job, int pj MF_ARG) {
  constexpr int VPS = RawLd::VPS, SB = RawLd::SB, E = JOB ? 16 : 0;
  const bool isi = job < 144;
  const int kb = isi ? job >> 3 : job - 144;
  const int pitch = isi ? F_ * 2 : D_ * 2;
  const int l36 = lane < 36 ? lane : 35;
  unsigned sa = (unsigned)l36 * 16u, da;
  if (isi) da = lds0 + (unsigned)((l36 >= 18 ? 9216 - 288 : 0) + wl * 8 * 288 + l36 * 16);
  else da = lds0 + (unsigned)((l36 / 9) * (4608 - 144) + (16 * (wl & 1) + (wl >> 1)) * 144 + l36 * 16);
  asm volatile("" : "+v"(sa), "+v"(da));
  u32x4 v[2][8];
  const char* ps = uni_ptr((const char*)(isi ? w_in + (job & 7) * 576 : w_out) + (size_t)(kb * 32 + wl * 8) * pitch);
  auto extra = [&](auto J_) {
    constexpr int j = J_;
    const char* q = ps + (j / 2) * pitch;
    u32x4 t;
    asm volatile("global_load_dwordx4 %0, %1, %2 offset:%3" : "=v"(t) : "v"(sa), "s"(q), "n"((j % 2) * 576) : "memory");
    v[j % 2][j / 2] = t;
  };
  auto step = [&](auto S_) {
    constexpr int S = S_;
    wait_vm<RawX<E>::n(S - 2) + (S <= 6 ? VPS : 0) + RawX<E>::n(S - 1)>();
    __builtin_amdgcn_s_barrier();
    MF(16 + S);
    if constexpr (S == RAW_S0) {
      static_assert(RAW_S0 >= 4, "wo blocks");
      if (pj >= 0 && wl == 0 && lane == 0)
        asm volatile("global_store_dwordx2 %0, %1, off sc1\n\ts_nop 1" ::"v"(ws + OFF_POT + (pj / 6) * 128 + (pj % 6) * 8), "v"(tag) : "memory");
    }
    if constexpr (S <= 5) ld.stage((unsigned)(((S + 3) % 4) * SB));
    ld.q_hook(S + 1, ws, lds0, hp);
    static_for<RawX<E>::first(S), RawX<E>::first(S + 1)>(extra);
  };
  const char* hp_ = ws + OFF_HELLO + lane * 32;
  u32x4 a, b;
  if (wl == 1) asm volatile("global_load_dwordx4 %0, %2, off sc1\n\tglobal_load_dwordx4 %1, %2, off offset:16 sc1" : "=&v"(a), "=&v"(b) : "v"(hp_) : "memory");
  else asm volatile("" : "=v"(a), "=v"(b));
  static_for<RAW_S0, 8>(step);
  MF(2);
  if (wl == 1) {
    asm volatile("" : "+v"(a), "+v"(b));
    const unsigned t0 = (unsigned)tag, t1 = (unsigned)(tag >> 32);
#pragma unroll 1
    for (unsigned spin = 0; spin < SPIN_MEM; ++spin) {
      const unsigned bad = ((a[0] ^ t0) | (a[2] ^ t0) | (b[0] ^ t0) | (b[2] ^ t0)) | ((a[1] ^ t1) | (a[3] ^ t1) | (b[1] ^ t1) | (b[3] ^ t1));
      if (!__any(bad != 0u)) break;
      __builtin_amdgcn_s_sleep(4);
      asm volatile("global_load_dwordx4 %0, %2, off sc1\n\tglobal_load_dwordx4 %1, %2, off offset:16 sc1\n\ts_waitcnt vmcnt(0)"
                   : "=&v"(a), "=&v"(b)
                   : "v"(hp_)
                   : "memory");
    }
    lds_wr32(lds0 + (unsigned)(LW_OFF + LW_HELLO * 4), 1u);
    if (bid == 0 && lane == 0) asm volatile("global_store_dwordx2 %0, %1, off sc1\n\ts_nop 1" ::"v"(ws + OFF_TAG), "v"(tag) : "memory");
  }
  MF(11);
  if (rt < 2) lds_spin(lds0 + (unsigned)(LW_OFF + LW_Q * 4), 1u);
  lds_spin(lds0 + (unsigned)K1R_CNT, 8u);
  if constexpr (JOB) {
#pragma unroll
    for (int b = 0; b < 2; ++b)
      asm volatile("s_waitcnt vmcnt(0)"
                   : "+v"(v[b][0]), "+v"(v[b][1]), "+v"(v[b][2]), "+v"(v[b][3]), "+v"(v[b][4]), "+v"(v[b][5]), "+v"(v[b][6]), "+v"(v[b][7])
                   :
                   : "memory");
  }
  MF(12);
  const char* tp = ws + OFF_POT + ct * 128 + (lane < 6 ? lane : 5) * 8;
  u32x2 f;
  asm volatile("global_load_dwordx2 %0, %1, off sc1" : "=v"(f) : "v"(tp) : "memory");
  if constexpr (JOB) {
    static_assert(SB == 4 * 9216, "staging");
#pragma unroll
    for (int b = 0; b < 2; ++b) {
      float cs[8] = {0.f, 0.f, 0.f, 0.f, 0.f, 0.f, 0.f, 0.f};
      tr8_store<288, true>(v[b], da + (unsigned)(b * 18432), cs, std::make_integer_sequence<int, 8>{});
      const f32x4 c0 = {cs[0], cs[1], cs[2], cs[3]}, c1 = {cs[4], cs[5], cs[6], cs[7]};
      const unsigned ca = lds0 + (unsigned)(3 * SB + wl * 2304) + sa * 2u;
      asm volatile("ds_write_b128 %0, %1 offset:%2" ::"v"(ca), "v"(c0), "n"(b * 1152) : "memory");
      asm volatile("ds_write_b128 %0, %1 offset:%2" ::"v"(ca), "v"(c1), "n"(b * 1152 + 16) : "memory");
    }
    lds_inc(lds0 + (unsigned)(LW_OFF + LW_ST * 4));
    lds_spin(lds0 + (unsigned)(LW_OFF + LW_ST * 4), 4u);
    if (isi) {
      const int j = (job & 7) * 4 + wl, ti = j < 16 ? 2 * j + 1 : 2 * (j - 16);
      lds_out<9216>(lds0 + (unsigned)(wl * 9216), ws + OFF_PI + (size_t)(ti * 18 + kb) * 9216, lane);
      f32x4 c[4];
      const unsigned ca = lds0 + (unsigned)(3 * SB + wl * 576) + sa;
#pragma unroll
      for (int i = 0; i < 4; ++i) asm volatile("ds_read_b128 %0, %1 offset:%2" : "=v"(c[i]) : "v"(ca), "n"(i * 2304) : "memory");
      asm volatile("s_waitcnt lgkmcnt(0)" : "+v"(c[0]), "+v"(c[1]), "+v"(c[2]), "+v"(c[3]));
      const f32x4 t = (c[0] + c[1]) + (c[2] + c[3]);
      if (lane < 36) pk_st16(ws + OFF_CSP + ((size_t)kb * F_ + ti * 144) * 4 + sa, __builtin_bit_cast(u32x4, t));
    } else {
      lds_out<4608>(lds0 + (unsigned)(wl * 9216), ws + OFF_PW + (size_t)(wl * 144 + kb) * 4608, lane);
      lds_out<4608>(lds0 + (unsigned)(wl * 9216 + 4608), ws + OFF_PW + (size_t)(wl * 144 + 72 + kb) * 4608, lane);
    }
  }
  MF(8);
  announce(lds0 + (unsigned)(LW_OFF + LW_PK * 4), 4u, ws, CN_PK, isi ? 8 + (job & 3) * 4 + lane : 24 + lane, JOB && lane < (isi ? 4 : 8));
  MF(9);
  if (wl == 0) {
    asm volatile("" : "+v"(f));
#pragma unroll 1
    for (unsigned spin = 0; spin < SPIN_MEM; ++spin) {
      if (!__any(f[0] != (unsigned)tag || f[1] != (unsigned)(tag >> 32))) break;
      __builtin_amdgcn_s_sleep(2);
      asm volatile("global_load_dwordx2 %0, %1, off sc1\n\ts_waitcnt vmcnt(0)" : "=&v"(f) : "v"(tp) : "memory");
    }
  }
}

template <int MT, int NT>
DEVI void k1_gemm(char* smem, const char* ws, const float* __restrict__ csq, int rt, int hp, int mt0, int nt0,
                  bool late, int lane MK_ARG) {
  const unsigned lds0 = (unsigned)(size_t)smem;
  const int g = lane >> 4;
  f32x4 cs[NT];
  unsigned ta[NT];
  const char* csp = uni_ptr((const char*)(csq + (hp & 1) * 4));
  unsigned cad[NT];
#pragma unroll
  for (int nt = 0; nt < NT; ++nt) {
    int c = (nt0 + nt) * 16 + g * 4;
    if (c > 104) c = 104;
    const int part = c / 36, ci = c - part * 36;
    if (CSL) {
      cad[nt] = lds0 + (unsigned)(K1_CS_OFF + (part * 40 + (hp & 1) * 4 + ci) * 4);
      asm volatile("" : "+v"(cad[nt]));
    } else
    asm volatile("global_load_dwordx4 %0, %1, %2 sc1" : "=v"(cs[nt]) : "v"((unsigned)(part * 40 + ci) * 4u), "s"(csp));
    const int to = (nt0 + nt) * 16 + g * 4 > 104 ? 120 : part * 80 + ci * 2 + (ci >= 20 ? 8 : 0);
    ta[nt] = lds0 + (unsigned)(K1_T_OFF + (mt0 * 16 + (lane & 15)) * K1_TP + to);
    asm volatile("" : "+v"(ta[nt]));
  }
  unsigned sa = lds0 + (unsigned)(K1_ST_OFF + (mt0 * 16 + (lane & 15)) * 8);
  asm volatile("" : "+v"(sa));
  f32x4 acc[MT][NT];
#pragma unroll
  for (int mt = 0; mt < MT; ++mt)
#pragma unroll
    for (int nt = 0; nt < NT; ++nt) acc[mt][nt] = f32x4{0.f, 0.f, 0.f, 0.f};
  f32x2 st[MT];
  if (G1E && late) bar();
  if constexpr (NT == 7) cnt_wait(ws, CN_X2, rt * 16 + hp, rt ? 24u : 16u);
  bar();
  MK(1);
  {
    GemmMath<K1C, MT, NT, (NT == 7 ? 5 : 3), (NT == 3 ? 6 : NT), true, 2, false, K1_WS> gm(acc);
    gm.setup(lds0, mt0, nt0, lane, 0u);
    bar();
    if (CSL) {
#pragma unroll
      for (int nt = 0; nt < NT; ++nt) asm volatile("ds_read_b128 %0, %1" : "=v"(cs[nt]) : "v"(cad[nt]));
    }
    gm.first();
    gm.template mid<8, true, AL_K>();
#pragma unroll
    for (int mt = 0; mt < MT; ++mt) asm volatile("ds_read_b64 %0, %1 offset:%2" : "=v"(st[mt]) : "v"(sa), "n"(mt * 128));
    asm volatile("s_waitcnt vmcnt(0)" ::: "memory");
    gm.last();
  }
#pragma unroll
  for (int nt = 0; nt < NT; ++nt) asm volatile("" : "+v"(cs[nt]));
#pragma unroll
  for (int mt = 0; mt < MT; ++mt) asm volatile("" : "+v"(st[mt]));
  MK(2);
#pragma unroll
  for (int mt = 0; mt < MT; ++mt) {
    f32x2 M = {st[mt][0], st[mt][0]}, R = {st[mt][1], st[mt][1]};
    asm volatile("" : "+v"(M), "+v"(R));
#pragma unroll
    for (int nt = 0; nt < NT; ++nt) {
      const f32x2 v0 = R * __builtin_elementwise_fma(M, f32x2{cs[nt][0], cs[nt][1]}, f32x2{acc[mt][nt][0], acc[mt][nt][1]});
      const f32x2 v1 = R * __builtin_elementwise_fma(M, f32x2{cs[nt][2], cs[nt][3]}, f32x2{acc[mt][nt][2], acc[mt][nt][3]});
      acc[mt][nt][0] = __uint_as_float(pack2v(v0));
      acc[mt][nt][1] = __uint_as_float(pack2v(v1));
      const f32x2 pk = {acc[mt][nt][0], acc[mt][nt][1]};
      asm volatile("ds_write_b64 %0, %1 offset:%2" ::"v"(ta[nt]), "v"(pk), "n"(mt * 16 * K1_TP) : "memory");
    }
  }
}

DEVI float vmax(float a, float b) {
  float r;
  asm("v_max_f32 %0, %1, %2" : "=v"(r) : "v"(a), "v"(b));
  return r;
}
struct Attn {
  unsigned ka, kb, va, vb, m0, m1, m2, pm[2], oa, pa;
  f32x4 mk;
  unsigned long long sel[4];
  const char* ob;
  DEVI void setup(unsigned T, char* o, int r0, int hp, int wave, int lane) {
    const int g = lane >> 4, r = lane & 15, blk = wave >> 1, hd = wave & 1;
    const unsigned co = (unsigned)(g < 2 ? hd * 48 + g * 16 : 32);
    ka = T + (unsigned)((blk * 32 + 2 * r) * K1_TP) + co;
    kb = T + (unsigned)((blk * 32 + 2 * (r & 7)) * K1_TP) + co;
    const int vr = 4 * g + ((lane >> 2) & 3);
    va = T + (unsigned)((blk * 32 + 2 * vr) * K1_TP + (lane & 3) * 8);
    vb = T + (unsigned)((blk * 32 + 2 * (vr & 7)) * K1_TP + (lane & 3) * 8);
    m0 = (g < 2 || (g == 2 && hd == 0)) ? ~0u : 0u;
    m1 = (g < 2 || (g == 2 && hd == 1)) ? ~0u : 0u;
    m2 = g < 2 ? ~0u : 0u;
    pm[0] = pm[1] = 0u;
    const float ci = __uint_as_float(vconst(0xff800000u));
    const unsigned ch = vconst(0xffffu);
#pragma unroll
    for (int e = 0; e < 4; ++e) {
      const int w1 = r + 8 - 4 * g - e;
      mk[e] = (w1 & 15) <= 7 ? 0.f : ci;
      sel[e] = __ballot(w1 >= 8);
      if (w1 >= 8) pm[e >> 1] |= ch << (16 * (e & 1));
    }
    oa = (unsigned)((8 + hp * 4 + hd * 2 + (g >> 1)) * 1024 + ((blk & 1) * 32 + 2 * r + (g & 1)) * 16);
    pa = ((oa & 1023u) >> 2) + (unsigned)((hp * 2 + hd) * 256);
    ob = uni_ptr(o + (size_t)((r0 >> 6) + (wave >> 2)) * (72 * 1024));
    asm volatile("" : "+v"(ka), "+v"(kb), "+v"(va), "+v"(vb), "+v"(m0), "+v"(m1), "+v"(m2), "+v"(pm[0]), "+v"(pm[1]), "+v"(oa));
    asm volatile("" : "+v"(mk), "+v"(pa), "+s"(ob), "+s"(sel[0]), "+s"(sel[1]), "+s"(sel[2]), "+s"(sel[3]));
  }
  DEVI void run(int r0, int hp, int wave, int lane) {
    const int hd = wave & 1;
    u32x4 q[2], k0[2], k1[2];
    u32x2 v[2][2][2];
#pragma unroll
    for (int p = 0; p < 2; ++p) {
      asm volatile("ds_read_b128 %0, %1 offset:%2" : "=v"(q[p]) : "v"(ka), "n"((16 + p) * K1_TP) : "memory");
      asm volatile("ds_read_b128 %0, %1 offset:%2" : "=v"(k0[p]) : "v"(ka), "n"(p * K1_TP + 80) : "memory");
      asm volatile("ds_read_b128 %0, %1 offset:%2" : "=v"(k1[p]) : "v"(kb), "n"((32 + p) * K1_TP + 80) : "memory");
    }
    const unsigned vh = (unsigned)hd * 48u;
#pragma unroll
    for (int p = 0; p < 2; ++p) {
      asm volatile("ds_read_b64_tr_b16 %0, %1 offset:%2" : "=v"(v[p][0][0]) : "v"(va + vh), "n"(p * K1_TP + 160) : "memory");
      asm volatile("ds_read_b64_tr_b16 %0, %1 offset:%2" : "=v"(v[p][0][1]) : "v"(vb + vh), "n"((32 + p) * K1_TP + 160) : "memory");
      asm volatile("ds_read_b64_tr_b16 %0, %1 offset:%2" : "=v"(v[p][1][0]) : "v"(va), "n"(p * K1_TP + 192) : "memory");
      asm volatile("ds_read_b64_tr_b16 %0, %1 offset:%2" : "=v"(v[p][1][1]) : "v"(vb), "n"((32 + p) * K1_TP + 192) : "memory");
    }
    const u32x4 one4 = {vconst(0x3f803f80u), vconst(0x3f803f80u), vconst(0x3f803f80u), vconst(0x3f803f80u)};
    const s16x8 ones = __builtin_bit_cast(s16x8, one4);
    const float c = 0.23570226039551584f * 1.4426950408889634f;
    const f32x4 z = {0.f, 0.f, 0.f, 0.f};
    f32x4 sc[2][2];
#pragma unroll
    for (int p = 0; p < 2; ++p) {
      asm volatile("s_waitcnt lgkmcnt(%3)" : "+v"(q[p]), "+v"(k0[p]), "+v"(k1[p]) : "n"(11 - 3 * p));
      const s16x8 qf = __builtin_bit_cast(s16x8, q[p] & u32x4{m0, m1, m2, m2});
      sc[p][0] = __builtin_amdgcn_mfma_f32_16x16x32_bf16(__builtin_bit_cast(s16x8, k0[p]), qf, mk, 0, 0, 0);
      sc[p][1] = __builtin_amdgcn_mfma_f32_16x16x32_bf16(__builtin_bit_cast(s16x8, k1[p]), qf, mk, 0, 0, 0);
      __builtin_amdgcn_sched_barrier(0);
    }
    if (__builtin_expect(r0 == 0 && wave < 2, 0)) {
#pragma unroll
      for (int p = 0; p < 2; ++p)
#pragma unroll
        for (int e = 0; e < 4; ++e)
          if (lane < 32) sc[p][0][e] = mk[e] - 4.242640687e9f;
    }
    float lg[2][4], mx[2];
#pragma unroll
    for (int p = 0; p < 2; ++p) {
#pragma unroll
      for (int e = 0; e < 4; ++e) lg[p][e] = __builtin_amdgcn_inverse_ballot_w64(sel[e]) ? sc[p][1][e] : sc[p][0][e];
      asm("v_max3_f32 %0, %1, %2, %3\n\tv_max_f32 %0, %0, %4" : "=&v"(mx[p]) : "v"(lg[p][0]), "v"(lg[p][1]), "v"(lg[p][2]), "v"(lg[p][3]));
    }
    {
      const auto a = __builtin_amdgcn_permlane32_swap(__float_as_uint(mx[0]), __float_as_uint(mx[1]), false, false);
      const unsigned b = __float_as_uint(vmax(__uint_as_float(a[0]), __uint_as_float(a[1])));
      const auto d = __builtin_amdgcn_permlane16_swap(b, b, false, false);
      const unsigned f = __float_as_uint(vmax(__uint_as_float(d[0]), __uint_as_float(d[1])) * -c);
      const auto h = __builtin_amdgcn_permlane32_swap(f, f, false, false);
      mx[0] = __uint_as_float(h[0]);
      mx[1] = __uint_as_float(h[1]);
    }
#pragma unroll
    for (int p = 0; p < 2; ++p)
#pragma unroll
      for (int e = 0; e < 4; ++e) lg[p][e] = __builtin_amdgcn_exp2f(__builtin_fmaf(lg[p][e], c, mx[p]));
    asm volatile("s_waitcnt lgkmcnt(0)"
                 : "+v"(v[0][0][0]), "+v"(v[0][0][1]), "+v"(v[0][1][0]), "+v"(v[0][1][1]), "+v"(v[1][0][0]), "+v"(v[1][0][1]),
                   "+v"(v[1][1][0]), "+v"(v[1][1][1]));
    __builtin_amdgcn_sched_barrier(0);
    f32x4 su[2], o0[2], o1[2];
    s16x8 pp[2];
#pragma unroll
    for (int p = 0; p < 2; ++p) {
      const unsigned p0 = pack2(lg[p][0], lg[p][1]), p1 = pack2(lg[p][2], lg[p][3]);
      pp[p] = __builtin_bit_cast(s16x8, u32x4{p0 & ~pm[0], p1 & ~pm[1], p0 & pm[0], p1 & pm[1]});
    }
    __builtin_amdgcn_sched_barrier(0);
#pragma unroll
    for (int p = 0; p < 2; ++p) {
      const s16x8 pf = pp[p];
      const u32x4 v0 = {v[p][0][0][0], v[p][0][0][1], v[p][0][1][0], v[p][0][1][1]};
      const u32x4 v1 = {v[p][1][0][0], v[p][1][0][1], v[p][1][1][0], v[p][1][1][1]};
      su[p] = __builtin_amdgcn_mfma_f32_16x16x32_bf16(ones, pf, z, 0, 0, 0);
      o0[p] = __builtin_amdgcn_mfma_f32_16x16x32_bf16(__builtin_bit_cast(s16x8, v0), pf, z, 0, 0, 0);
      o1[p] = __builtin_amdgcn_mfma_f32_16x16x32_bf16(__builtin_bit_cast(s16x8, v1), pf, z, 0, 0, 0);
    }
    __builtin_amdgcn_sched_barrier(0);
    u32x2 y[2], yl;
    unsigned hm = (unsigned)hd - 1u;
    asm volatile("" : "+s"(hm));
#pragma unroll
    for (int p = 0; p < 2; ++p) {
      const float inv = __builtin_amdgcn_rcpf(su[p][0]);
      y[p] = u32x2{pack2(o0[p][0] * inv, o0[p][1] * inv), pack2(o0[p][2] * inv, o0[p][3] * inv)};
      const unsigned la = pack2(o1[p][0] * inv, o1[p][1] * inv), lb = pack2(o1[p][2] * inv, o1[p][3] * inv);
      yl[p] = (la & hm) | (lb & ~hm);
    }
    const auto s0 = __builtin_amdgcn_permlane16_swap(y[0][0], y[1][0], false, false);
    const auto s1 = __builtin_amdgcn_permlane16_swap(y[0][1], y[1][1], false, false);
    const u32x4 w = {s0[0], s1[0], s0[1], s1[1]};
    asm volatile("global_store_dwordx4 %0, %1, %4 sc1\n\ts_mov_b64 exec, 0xffff\n\tglobal_store_dwordx2 %2, %3, %4 sc1\n\ts_mov_b64 exec, -1\n\ts_nop 0" ::"v"(oa),
                 "v"(w), "v"(pa), "v"(yl), "s"(ob)
                 : "memory");
  }
};

#define NL_MAIN 702
DEVI void code_again(const char* code0, int bid) {
  constexpr int PER = (2 * NL_MAIN + 31) / 32;
  const int lane = lane_id();
  int li = (bid >> 3) * PER + (lane < PER ? lane : PER - 1);
  if (li > 2 * NL_MAIN - 1) li = 2 * NL_MAIN - 1;
  unsigned t;
  asm volatile("global_load_dword %0, %1, %2\n\ts_waitcnt vmcnt(0)" : "=&v"(t) : "v"((unsigned)li * 64u), "s"(code0) : "memory");
}
DEVI void math_start(char* smem, const char* code0, const u16* b_sp, const u16* wqkv, char* ws, int bid, int wave, int lane) {
  const unsigned lds0 = (unsigned)(size_t)smem;
  const int tid = wave * 64 + lane, idx = bid >> 3;
  if (wave < 3) {
    static_assert(2 * SZ_ZP == (size_t)256 * 2048 && NCNT <= 16, "mark split");
    const u32x4 mk = {0xffffffffu, 0xffffffffu, 0xffffffffu, 0xffffffffu};
    char* c = ws + OFF_CNT + (size_t)((lane >> 3) * 256 + bid) * 128 + (lane & 7) * 16;
    pk_st16(wave < 2 ? ws + OFF_ZP + (size_t)((bid * 2 + wave) * 64 + lane) * 16 : c, mk);
    if (wave == 2 && lane < (NCNT - 8) * 8) pk_st16(c + 8 * 256 * 128, mk);
  }
  if (wave == 3) {
    const int li = idx * 54 + (lane < 54 ? lane : 53);
    touch64(uni_ptr((const char*)wqkv), (unsigned)(li * 1152 + 512), lds0 + (unsigned)(K1R_DUMMY + 768));
  }
  if (!CT0 && wave == 4) {
    constexpr int PER = (2 * NL_MAIN + 31) / 32;
    static_assert(PER <= 64, "one load");
    int li = idx * PER + (lane < PER ? lane : PER - 1);
    if (li > 2 * NL_MAIN - 1) li = 2 * NL_MAIN - 1;
    touch64(code0, (unsigned)li * 64u, lds0 + (unsigned)(K1R_DUMMY + 768));
  }
  if (wave == 5 && idx == 31) touch64(uni_ptr((const char*)b_sp), (unsigned)lane * 64u, lds0 + (unsigned)(K1R_DUMMY + 768));
  const u32x4 ones = {0x3f803f80u, 0x3f803f80u, 0x3f803f80u, 0x3f803f80u};
  if (tid < 256) *reinterpret_cast<u32x4*>(smem + (tid >> 6) * K1R::SB + K1R::XB + (tid & 63) * 288 + 240) = ones;
  else if (tid < 288) *reinterpret_cast<u32x4*>(smem + ((tid - 256) >> 3) * K1R::SB + (tid & 7) * 16) = ones;
  else if (tid < 296) *reinterpret_cast<u32x4*>(smem + LW_OFF + (tid - 288) * 16) = u32x4{0u, 0u, 0u, 0u};
  asm volatile("s_waitcnt lgkmcnt(0)" ::: "memory");
}

template <int NST, int NS, int NSW, int WOFF, int XW, int X0, int S0, unsigned BAR>
struct ResX {
  static constexpr int WS = 4 * 4608, R = 10, V1 = 5 * NS, NV = V1 + 10 * (NST - NS), NG = (NSW + 2) / 3;
  s16x8 xf[XW][2];
  s16x8 wf[R];
  unsigned wrd[2][NG], xo, ea, fa, fv, tag;
  const char* xp[3];
  f32x4 (&acc)[2][5];
  DEVI ResX(f32x4 (&a)[2][5]) : acc(a) {}
  static constexpr int st(int V) { return V < V1 ? V / 5 : NS + (V - V1) / 10; }
  static constexpr int hf(int V) { return V < V1 ? 0 : ((V - V1) / 5) & 1; }
  static constexpr bool first(int V) { return V < V1 ? V % 5 == 0 : (V - V1) % 10 == 0; }
  static constexpr int wreq(int V) {
    int n = V + 5;
    if (V >= V1 - 4) n += V < V1 ? V - (V1 - 5) : 4;
    return n < NV ? n : NV;
  }
  static constexpr int XU = V1 + 1, XF = V1 + 11, XR = V1 + 15, XC = V1 + 21;
  static constexpr int oth(int V) { return NS && ((V >= XU && V <= XF && (V - XU) % 2 == 0) || V == XR || (V > XC && V <= XC + 5)) ? 1 : 0; }
  static constexpr int newer(int V) {
    const int F = V + 1 < NV ? V + 1 : V;
    int v = V - 1, n = 0;
    for (; v >= 0 && wreq(v - 1) > F; --v) n += wreq(v) - wreq(v - 1) + oth(v);
    return n + wreq(v) - 1 - F + (v >= 0 ? oth(v) : 0);
  }
  static constexpr int xreq(int T) {
    int n = X0 + 2 * (T + 1);
    if (n > T + XW) n = T + XW;
    if (NS && T <= 11 && n > 18) n = 18;
    if (T < 0) n = X0;
    return n < NST ? n : NST;
  }
  static_assert(NS == 0 || (xreq(NS - 2) == NST && XC + 5 < NV), "the last steps only compute and swap");
  template <int T, int M>
  DEVI void xld() {
    asm volatile("global_load_dwordx4 %0, %1, %2 sc1" : "=v"(xf[T % XW][M]) : "v"(xo), "s"(xp[M == 0 ? 0 : (T < NS ? 1 : 2)]));
    if constexpr (M == 1 && T + 1 < NST) asm volatile("v_add_u32 %0, 0x2000, %0" : "+v"(xo));
  }
  template <int V>
  DEVI void rd() {
    constexpr int T = st(V), sl = (T / 2 + S0) % NSW;
    asm volatile("ds_read_b128 %0, %1 offset:%2" : "=v"(wf[V % R]) : "v"(wrd[hf(V)][sl / 3]), "n"((sl % 3) * WS + (T & 1) * 9216 + (V % 5) * 32));
  }
  template <int V, int M, int H>
  DEVI void mf() {
    acc[M][V % 5] = __builtin_amdgcn_mfma_f32_16x16x32_bf16(wf[V % R], xf[st(V) % XW][H], acc[M][V % 5], 0, 0, 0);
    __builtin_amdgcn_sched_barrier(0);
  }
  template <int V>
  DEVI void unit() {
    constexpr int T = st(V), n = V % 5, nx = xreq(T) - xreq(T - 1), nw = wreq(V) - wreq(V - 1);
    constexpr int vm = 2 * (xreq(T - 1) - 1 - T);
    static_assert(newer(V) <= 15, "lgkmcnt range");
    if constexpr (first(V) && V % 2 == 0) asm volatile("s_waitcnt vmcnt(%0) lgkmcnt(%1)" ::"n"(vm), "n"(newer(V)));
    else if constexpr (first(V)) asm volatile("s_waitcnt vmcnt(%0)" ::"n"(vm));
    else if constexpr (V % 2 == 0) asm volatile("s_waitcnt lgkmcnt(%0)" ::"n"(newer(V)));
    __builtin_amdgcn_sched_barrier(0);
    if constexpr (V < V1) mf<V, V & 1, V & 1>();
    else mf<V, 0, hf(V)>();
    if constexpr (nw > 0) rd<wreq(V - 1)>();
    __builtin_amdgcn_sched_barrier(0);
    if constexpr (V < V1) {
      mf<V, 1 - (V & 1), 1 - (V & 1)>();
      if constexpr (nw > 1) rd<wreq(V - 1) + 1>();
      if constexpr (nx == 2 && n < 4) xld<xreq(T - 1) + n / 2, n % 2>();
      if constexpr (nx == 1 && n == (T & 1)) xld<xreq(T - 1), 0>();
      if constexpr (nx == 1 && n == (T & 1) + 2) xld<xreq(T - 1), 1>();
      if constexpr (n == 4 && (T & 1) && ((BAR >> (T / 2)) & 1u)) bar();
    } else if constexpr (NS == 0) {
      constexpr int k = (V % 10) / 2;
      if constexpr (V % 2 == 0 && k < 2 * nx) xld<xreq(T - 1) + k / 2, k % 2>();
      if constexpr (V % 10 == 8 && nx > 0 && xreq(T) == NST)
        asm volatile("s_mov_b64 exec, 1\n\tds_add_u32 %0, %1\n\ts_mov_b64 exec, -1" ::"v"(fa), "v"(fv));
    } else {
      if constexpr (oth(V) && V < XF) asm volatile("ds_write_b128 %0, %1 offset:%2" ::"v"(ea), "v"(acc[1][(V - XU) / 2]), "n"((V - XU) * 512));
      if constexpr (V == XF) asm volatile("ds_write_b32 %0, %1" ::"v"(fa), "v"(fv));
      if constexpr (V == XF + 2) asm volatile("v_xor_b32 %0, 4, %0" : "+v"(fa));
      if constexpr (V == XR) asm volatile("ds_read_b32 %0, %1" : "+v"(fv) : "v"(fa));
      if constexpr (V == XR + 2) asm volatile("v_xor_b32 %0, 0x2000, %0" : "+v"(ea));
      if constexpr (V == XC) {
        static_assert(wreq(XR) <= XC + 1 || true, "");
        unsigned c = 1u << 18;
        asm volatile(
            "s_waitcnt lgkmcnt(%4)\n\tv_cmp_ne_u32 vcc, %3, %0\n\ts_cbranch_vccnz .Lw%=\n\t.subsection 1\n.Lw%=:\n\tds_read_b32 %0, %2\n\t"
            "s_waitcnt lgkmcnt(0)\n\tv_cmp_ne_u32 vcc, %3, %0\n\t" SAFE_OUT(".Lr%=") "s_cbranch_vccnz .Lw%=\n\ts_branch .Lr%=\n\t.subsection 0\n.Lr%=:"
            : "+v"(fv), NOP_(c)
            : "v"(fa), "s"(tag), "n"(wreq(XC) - wreq(XR))
            : "vcc", "scc");
      }
      if constexpr (oth(V) && V > XC) asm volatile("ds_read_b128 %0, %1 offset:%2" : "=v"(acc[1][V - XC - 1]) : "v"(ea), "n"((V - XC - 1) * 1024));
    }
    __builtin_amdgcn_sched_barrier(0);
  }
  template <int... V>
  DEVI void units(std::integer_sequence<int, V...>) {
    (unit<V>(), ...);
  }
  DEVI void run() {
    units(std::make_integer_sequence<int, NV>{});
    if constexpr (NS > 0) {
      asm volatile("s_waitcnt lgkmcnt(0)" : "+v"(acc[1][0]), "+v"(acc[1][1]), "+v"(acc[1][2]), "+v"(acc[1][3]), "+v"(acc[1][4]));
#pragma unroll
      for (int i = 0; i < 5; ++i) acc[0][i] += acc[1][i];
    }
  }
  template <int... V>
  DEVI void pro(std::integer_sequence<int, V...>) {
    (rd<V>(), ...);
  }
  DEVI void prow() { pro(std::make_integer_sequence<int, wreq(-1)>{}); }
  template <int T0, int... T>
  DEVI void prox(std::integer_sequence<int, T...>) {
    ((xld<T0 + T, 0>(), xld<T0 + T, 1>()), ...);
  }
  DEVI void setup(unsigned lds0, const char* xbase, int wave, int lane, unsigned red, unsigned nth) {
    const int g = lane >> 4, r = lane & 15, kq = wave & 1;
    xp[0] = xbase + wave * 256;
    xp[1] = xbase + (wave ^ 1) * 256;
    xp[2] = xp[0] + (kq ? -4096 : 4096);
    xo = (unsigned)((4 * kq + g) * 1024 + r * 16);
#pragma unroll
    for (int i = 0; i < 2 * NG; ++i) {
      wrd[i / NG][i % NG] = lds0 + (unsigned)(WOFF + (i % NG) * 3 * WS + (kq ^ (i / NG)) * 4608 + (2 * (r & 7) + 16 * (g & 1) + (g >> 1)) * 144 + (r >> 3) * 16);
      if (i < NG || NS == 0 || i % NG == ((NS / 2 + S0) % NSW) / 3) asm volatile("" : "+v"(wrd[i / NG][i % NG]));
    }
    asm volatile("" : "+v"(xo));
    if constexpr (NS > 0) {
      ea = lds0 + red + (unsigned)((wave ^ 1) * 8192 + lane * 16);
      fa = lds0 + (unsigned)(LW_OFF + (LW_PAIR + wave) * 4);
      fv = tag = nth;
      asm volatile("" : "+v"(ea), "+v"(fa), "+v"(fv));
    }
  }
  template <int N>
  DEVI void init(u32x2 (&xv)[5]) {
    asm volatile("s_waitcnt vmcnt(%5)" : "+v"(xv[0]), "+v"(xv[1]), "+v"(xv[2]), "+v"(xv[3]), "+v"(xv[4]) : "n"(N));
#pragma unroll
    for (int i = 0; i < 5; ++i) {
      acc[0][i] = f32x4{bf_lo(xv[i][0]), bf_hi(xv[i][0]), bf_lo(xv[i][1]), bf_hi(xv[i][1])};
      asm volatile("" : "+v"(acc[0][i]));
      acc[1][i] = f32x4{0.f, 0.f, 0.f, 0.f};
    }
  }
};

#define P5_XW 8
#define P5_X0 5
constexpr unsigned P5_BAR = 0x2aaau;
constexpr int P5_W = 15 * 1024;
constexpr int P2_W = K1_ST_OFF - 18 * 4608;
constexpr int P5_RED = 0;
static_assert(P5_RED % 16384 == 0 && P5_RED + 3 * 8192 + 5120 <= P5_W + 18432, "exchange area");
static_assert(P2_W >= K1_T_OFF + 144 * K1_TP + 64 && P2_W % 16 == 0 && P5_W + 8 * 18432 <= LW_OFF, "LDS placement");

struct ResIn {
  const char* xp;
  unsigned o, o2, st;
  DEVI void setup(const char* src, bool rm, int rb, int ct, int wave, int lane) {
    const int g = lane >> 4, g2 = g & 1, rl = wave * 16 + (lane & 15);
    xp = uni_ptr(rm ? src + ((size_t)rb * 64 * D_ + ct * 72) * 2 : src + (size_t)(rb * 72 + ct * 9) * 1024);
    o = rm ? (unsigned)(rl * (D_ * 2) + g * 8) : (unsigned)((g >> 1) * 1024 + rl * 16 + g2 * 8);
    o2 = rm ? (unsigned)(rl * (D_ * 2) + g2 * 8 + 128) : (unsigned)(8192 + rl * 16 + g2 * 8);
    st = rm ? 32u : 2048u;
    asm volatile("" : "+v"(o), "+v"(o2));
    asm volatile("" : "+s"(xp));
  }
  DEVI void load(u32x2 (&xv)[5]) {
#pragma unroll
    for (int i = 0; i < 4; ++i) asm volatile("global_load_dwordx2 %0, %1, %2 sc1" : "=v"(xv[i]) : "v"(o), "s"(xp + i * st));
    asm volatile("global_load_dwordx2 %0, %1, %2 sc1" : "=v"(xv[4]) : "v"(o2), "s"(xp));
  }
};

DEVI void p2_gemm(f32x4 (&acc)[2][5], u32x2 (&xv)[5], unsigned lds0, const char* ob, int layer, int wave, int lane MK_ARG) {
  ResX<9, 0, 5, P2_W, 9, P2_X0, 0, 0u> gm(acc);
  gm.setup(lds0, ob, wave, lane, 0u, 0u);
  unsigned a = (gm.xo & ~1023u) | ((gm.xo & 1023u) >> 2);
  gm.xo += 8192u;
  gm.fa = lds0 + (unsigned)(LW_OFF + (LW_T + layer) * 4);
  gm.fv = vconst(1u);
  asm volatile("" : "+v"(a), "+v"(gm.xo), "+v"(gm.fa));
  const char* pp[2] = {gm.xp[0] - wave * 192, gm.xp[2] - wave * 192};
  place<AL_P2>();
  bar();
  MK(6);
  unsigned e[2][4];
#pragma unroll
  for (int d = 0; d < 8; ++d) {
    if (NOPL && (d & 3)) e[d / 4][d % 4] = 0x3c003c00u;
    else asm volatile("global_load_dword %0, %1, %2 offset:%3 sc1" : "=v"(e[d / 4][d % 4]) : "v"(a), "s"(pp[d / 4]), "n"((d % 4) * 256));
  }
  gm.template prox<1>(std::make_integer_sequence<int, P2_X0 - 1>{});
  if (P2_X0 == 9) lds_inc(lds0 + (unsigned)(LW_OFF + (LW_T + layer) * 4));
  gm.prow();
  gm.template init<6 + 2 * P2_X0>(xv);
  asm volatile("s_waitcnt vmcnt(%8)"
               : "+v"(e[0][0]), "+v"(e[0][1]), "+v"(e[0][2]), "+v"(e[0][3]), "+v"(e[1][0]), "+v"(e[1][1]), "+v"(e[1][2]), "+v"(e[1][3])
               : "n"(2 * P2_X0 - 2));
  gm.xf[0][0] = __builtin_bit_cast(s16x8, u32x4{e[0][0], e[0][1], e[0][2], e[0][3]});
  gm.xf[0][1] = __builtin_bit_cast(s16x8, u32x4{e[1][0], e[1][1], e[1][2], e[1][3]});
  __builtin_amdgcn_sched_barrier(0);
  gm.run();
}

struct ResOut {
  const char *xp[3], *sp;
  unsigned xo, so;
  DEVI void setup(char* xim, char* xs, u16* out, bool fin, int rb, int ct, int wave, int lane) {
    const int g = lane >> 4, rl = wave * 16 + (lane & 15);
    xp[0] = uni_ptr(fin ? (const char*)(out + (size_t)rb * 64 * D_ + ct * 72) : xim + (size_t)(rb * 72 + ct * 9) * 1024);
    xp[1] = xp[0] + (fin ? 64 : 4096);
    xp[2] = xp[0] + (fin ? 128 : 8192);
    sp = uni_ptr(xs + (size_t)(rb * 8 + ct) * 512);
    xo = (unsigned)(((g & 1) * 2 + (g >> 1)) * (fin ? 16 : 1024) + rl * (fin ? D_ * 2 : 16));
    so = (unsigned)(rl * 8);
    asm volatile("" : "+v"(xo), "+v"(so), "+s"(xp[0]), "+s"(xp[1]), "+s"(xp[2]), "+s"(sp));
  }
  DEVI void run(const f32x4 (&a)[5], bool fin) {
    u32x2 y[5];
    u32x4 v[3];
#pragma unroll
    for (int nt = 0; nt < 5; ++nt) y[nt] = u32x2{pack2(a[nt][0], a[nt][1]), pack2(a[nt][2], a[nt][3])};
#pragma unroll
    for (int i = 0; i < 3; ++i) {
      const auto s0 = __builtin_amdgcn_permlane16_swap(y[2 * i][0], y[i < 2 ? 2 * i + 1 : 4][0], false, false);
      const auto s1 = __builtin_amdgcn_permlane16_swap(y[2 * i][1], y[i < 2 ? 2 * i + 1 : 4][1], false, false);
      v[i] = u32x4{s0[0], s1[0], s0[1], s1[1]};
    }
    if (fin) {
#define OUTM 0
#define OUTST ""
      asm volatile("global_store_dwordx4 %0, %1, %4" OUTST "\n\tglobal_store_dwordx4 %0, %2, %5" OUTST "\n\ts_mov_b64 exec, 0xffff\n\tglobal_store_dwordx4 %0, %3, %6" OUTST "\n\t"
                   "s_mov_b64 exec, -1\n\ts_nop 0" ::"v"(xo), "v"(v[0]), "v"(v[1]), "v"(v[2]), "s"(xp[0]), "s"(xp[1]), "s"(xp[2])
                   : "memory");
      return;
    }
    asm volatile("global_store_dwordx4 %0, %1, %4 sc1\n\tglobal_store_dwordx4 %0, %2, %5 sc1\n\ts_mov_b64 exec, 0xffff\n\t"
                 "global_store_dwordx4 %0, %3, %6 sc1\n\ts_mov_b64 exec, -1\n\ts_nop 0" ::"v"(xo), "v"(v[0]), "v"(v[1]), "v"(v[2]),
                 "s"(xp[0]), "s"(xp[1]), "s"(xp[2])
                 : "memory");
    const u32x4 one4 = {vconst(0x3f803f80u), vconst(0x3f803f80u), vconst(0x3f803f80u), vconst(0x3f803f80u)};
    const bool lo = __builtin_amdgcn_inverse_ballot_w64(0xffffull);
    v[2] = u32x4{lo ? v[2][0] : 0u, lo ? v[2][1] : 0u, lo ? v[2][2] : 0u, lo ? v[2][3] : 0u};
    f32x4 s = {0.f, 0.f, 0.f, 0.f}, q = s;
#pragma unroll
    for (int i = 0; i < 3; ++i) {
      const s16x8 u = __builtin_bit_cast(s16x8, v[i]);
      q = __builtin_amdgcn_mfma_f32_16x16x32_bf16(u, u, q, 0, 0, 0);
      s = __builtin_amdgcn_mfma_f32_16x16x32_bf16(__builtin_bit_cast(s16x8, one4), u, s, 0, 0, 0);
    }
    float q0 = q[0], q1 = q[1], q2 = q[2], q3 = q[3];
    const bool b0 = __builtin_amdgcn_inverse_ballot_w64(0xaaaaaaaaaaaaaaaaull), b1 = __builtin_amdgcn_inverse_ballot_w64(0xccccccccccccccccull);
    q0 = b0 ? q1 : q0;
    q2 = b0 ? q3 : q2;
    asm("" : "+v"(q0), "+v"(q2));
    const f32x2 sq = {s[0], b1 ? q2 : q0};
    asm volatile("s_mov_b64 exec, %3\n\tglobal_store_dwordx2 %0, %1, %2 sc1\n\ts_mov_b64 exec, -1" ::"v"(so), "v"(sq), "s"(sp), "s"(0xf0000f0000f0000full) : "memory");
  }
};

using K3C = GemmCfg<128, 144>;
constexpr int K3_Z1_OFF = K3C::LDS_BYTES;
constexpr int K3_C2_OFF = K3_Z1_OFF + K3C::XB;
constexpr int K3_ZC_OFF = K3_C2_OFF + 2048;
constexpr int K3_DM_OFF = K3_ZC_OFF + 1024;
constexpr int K3_ST_OFF = K3_Z1_OFF + 4 * 5120;
constexpr int K3_C1_OFF = K3_ST_OFF + 2048;
constexpr int K3_BS_OFF = K3_C1_OFF + 576;
static_assert(K3_BS_OFF + 512 <= LW_OFF, "K3 LDS");
static_assert(P5_W + 19 * 1024 == K3C::SB && K3C::SB == 34 * 1024, "w_out ring");
constexpr int K3_D = 4;

DEVI void red4(float (&t)[4], float& s, float& q) {
#pragma unroll
  for (int j = 0; j < 2; ++j) {
    const auto p = __builtin_amdgcn_permlane16_swap(__float_as_uint(t[j]), __float_as_uint(t[2 + j]), false, false);
    const unsigned v = __float_as_uint(__uint_as_float(p[0]) + __uint_as_float(p[1]));
    const auto w = __builtin_amdgcn_permlane32_swap(v, v, false, false);
    t[j] = __uint_as_float(w[0]) + __uint_as_float(w[1]);
    asm volatile("" : "+v"(t[j]));
  }
  s = t[0];
  q = t[1];
}
DEVI void red8(const f32x4& a, const f32x4& b, float& s, float& q) {
  float t[4];
#pragma unroll
  for (int j = 0; j < 4; ++j) {
    t[j] = a[j] + b[j];
    asm volatile("" : "+v"(t[j]));
  }
  red4(t, s, q);
}
DEVI void red16(const f32x4& a, const f32x4& b, const f32x4& c, const f32x4& d, float& s, float& q) {
  float t[4];
#pragma unroll
  for (int j = 0; j < 4; ++j) {
    float u = a[j] + b[j], v = c[j] + d[j];
    asm volatile("" : "+v"(u), "+v"(v));
    t[j] = u + v;
    asm volatile("" : "+v"(t[j]));
  }
  red4(t, s, q);
}

constexpr int b_k(int s) { return s < 2 ? s : (s < 4 ? 5 + s : (s < 6 ? 1 + s : (s == 6 ? 4 : s - 5))); }
template <bool B>
struct UMath {
  static constexpr int D = 4, R = 6, NV = 162, SB = K3C::SB, P = K3C::P;
  s16x8 wf[R], xf[2][2];
  s16x8 (&xc)[4][2][2];
  f32x4 (&acc)[2][9];
  f32x4 rc[4][2];
  f32x2 cs[9];
  unsigned ra, ca;
  template <int I>
  DEVI void cread() {
    if constexpr (I < 8) asm volatile("ds_read_b128 %0, %1 offset:%2" : "=v"(rc[I / 2][I % 2]) : "v"(ra), "n"((I / 2) * 512 + (I % 2) * 16));
    else if constexpr (I < 17) asm volatile("ds_read_b64 %0, %1 offset:%2" : "=v"(cs[I - 8]) : "v"(ca), "n"((I - 8) * 128));
  }
  unsigned xa, wa;
  unsigned fa, fw, fv;
  DEVI UMath(f32x4 (&a)[2][9], s16x8 (&x)[4][2][2]) : xc(x), acc(a) {}
  static constexpr int wslot(int s) { return B ? (s + 1) % 4 : s % 4; }
  static constexpr int xslot(int s) { return B ? b_k(s) % 5 : s % 5; }
  static constexpr bool res(int q) { return !B || b_k(q / 2) >= 4; }
#define BPS 9
  static constexpr bool chk(int s) { return s >= 3 && s < 7 && s != BPS; }
  static constexpr bool xop(int v) { return v + D < NV && (v + D) % 9 < 2 && res((v + D) / 9); }
  static constexpr int ops1(int v) { return (xop(v) || (B && ((v % 18 == 9 && chk(v / 18)) || (v % 18 == 0 && v > 0)))) ? 1 : 0; }
  static constexpr int newer(int V) {
    int v0 = (V % 9 == 8 ? V : V + 1) - D, n = 0;
    if (v0 >= 0 && !(V % 9 == 0 && res(V / 9))) n += ops1(v0);
    for (int v = v0 + 1; v < V; ++v)
      if (v >= 0) n += (v + D < NV ? 1 : (B ? 0 : 4)) + ops1(v);
    return n;
  }
  template <int V>
  DEVI void wread() {
    asm volatile("ds_read_b128 %0, %1 offset:%2" : "=v"(wf[V % R]) : "v"(wa), "n"((wslot(V / 18) & 1) * SB + ((V % 18) / 9) * 32 * P + (V % 9) * 32));
  }
  template <int Q, int M>
  DEVI void xread() {
    asm volatile("ds_read_b128 %0, %1 offset:%2" : "=v"(xf[Q & 1][M]) : "v"(xa), "n"((xslot(Q / 2) & 1) * SB + (Q & 1) * 8192 + M * (B ? 256 : 64)));
  }
  template <int V, int M_>
  DEVI void mf() {
    constexpr int M = ((V % 9) & 1) ? 1 - M_ : M_, q = V / 9;
    if constexpr (!B) acc[M][V % 9] = __builtin_amdgcn_mfma_f32_16x16x32_bf16(xf[q & 1][M], wf[V % R], acc[M][V % 9], 0, 0, 0);
    else if constexpr (res(q)) acc[M][V % 9] = __builtin_amdgcn_mfma_f32_16x16x32_bf16(wf[V % R], xf[q & 1][M], acc[M][V % 9], 0, 0, 0);
    else acc[M][V % 9] = __builtin_amdgcn_mfma_f32_16x16x32_bf16(wf[V % R], xc[b_k(q / 2)][q & 1][M], acc[M][V % 9], 0, 0, 0);
    __builtin_amdgcn_sched_barrier(0);
  }
  template <int V>
  DEVI void unit() {
    constexpr int U = V % 18, s = V / 18;
    static_assert(newer(V) <= 15, "lgkmcnt range");
    if constexpr ((V % 9) % 2 == 0 && V > 0) asm volatile("s_waitcnt lgkmcnt(%0)" ::"n"(newer(V)));
    __builtin_amdgcn_sched_barrier(0);
    mf<V, 0>();
    if constexpr (V + D < NV) wread<V + D>();
    if constexpr (!B && V + D >= NV) {
      cread<(V + D - NV) * 4>();
      cread<(V + D - NV) * 4 + 1>();
    }
    if constexpr (s < 8 && U == 13 && (wslot(s + 1) >> 1) != (wslot(s) >> 1))
      asm volatile("v_add_u32 %0, %1, %0" : "+v"(wa) : "n"(((wslot(s + 1) >> 1) - (wslot(s) >> 1)) * 2 * SB));
    __builtin_amdgcn_sched_barrier(0);
    mf<V, 1>();
    if constexpr (xop(V)) xread<(V + D) / 9, (V + D) % 9>();
    if constexpr (!B && V + D >= NV) {
      cread<(V + D - NV) * 4 + 2>();
      cread<(V + D - NV) * 4 + 3>();
      if constexpr (V == NV - 1) cread<16>();
    }
    if constexpr (s < 8) {
      if constexpr (U == 11 && res(2 * s + 2) && (xslot(s + 1) >> 1) != (xslot(s) >> 1))
        asm volatile("v_add_u32 %0, %1, %0" : "+v"(xa) : "n"(((xslot(s + 1) >> 1) - (xslot(s) >> 1)) * 2 * SB));
      if constexpr (!B && U == 13) __builtin_amdgcn_s_barrier();
      if constexpr (B && U == 9 && chk(s)) asm volatile("ds_read_u8 %0, %1 offset:%2" : "+v"(fv) : "v"(fa), "n"(s == 6 ? 0 : 8));
      unsigned n = 1u << 18;
      if constexpr (B && U == 13 && chk(s)) {
        asm volatile(
            "v_cmp_gt_u32 vcc, %3, %0\n\ts_cbranch_vccnz .Lw%=\n\t.subsection 1\n.Lw%=:\n\tds_read_u8 %0, %2 offset:%4\n\ts_waitcnt lgkmcnt(0)\n\t"
            "v_cmp_gt_u32 vcc, %3, %0\n\t" SAFE_OUT(".Lr%=") "s_cbranch_vccnz .Lw%=\n\ts_branch .Lr%=\n\t.subsection 0\n.Lr%=:"
            : "+v"(fv), NOP_(n)
            : "v"(fa), "n"(s == 6 ? 4 : s + 2), "n"(s == 6 ? 0 : 8)
            : "vcc", "scc");
      }
      if constexpr (B && U == 13 && s == BPS) {
        asm volatile(
            ".La%=:\n\ts_sleep 1\n\tds_read_u8 %0, %2\n\ts_waitcnt lgkmcnt(0)\n\tv_cmp_gt_u32 vcc, 3, %0\n\t" SAFE_OUT(".Lr%=") "s_cbranch_vccnz .La%=\n"
            ".Lb%=:\n\tds_read_u8 %0, %2 offset:8\n\ts_waitcnt lgkmcnt(0)\n\tv_cmp_gt_u32 vcc, %3, %0\n\t" SAFE_OUT(".Lr%=") "s_cbranch_vccnz .Lb%=\n.Lr%=:"
            : "+v"(fv), NOP_(n)
            : "v"(fa), "n"(s + 2)
            : "vcc", "scc");
      }
    }
    if constexpr (B && U == 15) asm volatile("v_mov_b32 %0, %1" : "+v"(fv) : "n"(s + 1));
    if constexpr (B && ((U == 0 && s > 0) || V == NV - 1)) asm volatile("ds_write_b8 %0, %1" ::"v"(fw), "v"(fv));
    __builtin_amdgcn_sched_barrier(0);
  }
  template <int... V>
  DEVI void units(std::integer_sequence<int, V...>) {
    (unit<V>(), ...);
  }
  DEVI void first() {
    if constexpr (!B) {
      xread<0, 0>();
      xread<0, 1>();
    }
    wread<0>();
    wread<1>();
    wread<2>();
    wread<3>();
    asm volatile("s_waitcnt lgkmcnt(0)");
  }
  DEVI void run() {
    units(std::make_integer_sequence<int, NV>{});
#pragma unroll
    for (int m = 0; m < 2; ++m)
#pragma unroll
      for (int n = 0; n < 9; ++n) asm volatile("" : "+v"(acc[m][n]));
  }
};

#define PACE 0
#define G0P 2
#define ZSLEEP 6
DEVI void p3_g0(char* smem, char* __restrict__ ws, int layer, int mi, int ni, int wm, int lane MK_ARG) {
  constexpr int SB = K3C::SB;
  const unsigned lds0 = (unsigned)(size_t)smem;
  const int g = lane >> 4, r = lane & 15;
  const unsigned lw = lds0 + (unsigned)(LW_OFF + layer * 4), me = 1u << (8 * wm);
  f32x4 ac[2][9];
#pragma unroll
  for (int mt = 0; mt < 2; ++mt)
#pragma unroll
    for (int j = 0; j < 9; ++j) ac[mt][j] = f32x4{0.f, 0.f, 0.f, 0.f};
  unsigned ra = lds0 + (unsigned)(K3_ST_OFF + (wm * 32 + g * 8) * 4), ca = lds0 + (unsigned)(K3_C2_OFF + r * 8);
  asm volatile("" : "+v"(ra), "+v"(ca));
  s16x8 xc[4][2][2];
  UMath<false> gm(ac, xc);
  gm.ra = ra;
  gm.ca = ca;
  gm.xa = lds0 + (unsigned)(g * 2048 + (wm * 32 + 8 * (r >> 2) + (r & 3)) * 16);
  gm.wa = lds0 + (unsigned)(K3C::XB + (g * 8 + (r & 7)) * K3C::P + (r >> 3) * 16);
  unsigned adw = lw + LW_AD * 4, mev = vconst(me), po = (unsigned)(ni * 1024 + lane * 4);
  const char* zp = uni_ptr(ws + OFF_ZP + (size_t)layer * SZ_ZP + (size_t)mi * 16384 + wm * 256);
  asm volatile("" : "+v"(gm.xa), "+v"(gm.wa), "+v"(adw), "+v"(po));
  asm volatile("" : "+s"(zp));
  place<AL_A>();
  bar();
  gm.first();
  gm.run();
  asm volatile("s_mov_b64 exec, 1\n\tds_add_u32 %0, %1\n\ts_mov_b64 exec, -1" ::"v"(adw), "v"(mev) : "memory");
  MK(11);
  f32x2 z[9][4];
  f32x2 sm[4], sq[4];
  {
    f32x4 (&rc)[4][2] = gm.rc;
    f32x2 (&cs)[9] = gm.cs;
    asm volatile("s_waitcnt lgkmcnt(1)"
                 : "+v"(rc[0][0]), "+v"(rc[0][1]), "+v"(rc[1][0]), "+v"(rc[1][1]), "+v"(rc[2][0]), "+v"(rc[2][1]), "+v"(rc[3][0]), "+v"(rc[3][1]));
#pragma unroll
    for (int j = 0; j < 9; ++j) asm volatile("" : "+v"(cs[j]));
    auto RC = [&](int k, int i) { return f32x2{rc[k][i / 2][(i % 2) * 2], rc[k][i / 2][(i % 2) * 2 + 1]}; };
#pragma unroll
    for (int j = 0; j < 9; ++j) {
      f32x2 u[4], w[4];
#pragma unroll
      for (int i = 0; i < 4; ++i) u[i] = __builtin_elementwise_fma(RC(0, i), cs[j], f32x2{ac[i / 2][j][(i % 2) * 2], ac[i / 2][j][(i % 2) * 2 + 1]});
#pragma unroll
      for (int i = 0; i < 4; ++i) w[i] = u[i] * u[i];
#pragma unroll
      for (int i = 0; i < 4; ++i) w[i] = __builtin_elementwise_fma(w[i], RC(3, i), RC(2, i));
#pragma unroll
      for (int i = 0; i < 4; ++i) w[i] = u[i] * w[i];
      __builtin_amdgcn_sched_barrier(0);
#pragma unroll
      for (int i = 0; i < 4; ++i) w[i] = f32x2{__builtin_amdgcn_exp2f(w[i][0]), __builtin_amdgcn_exp2f(w[i][1])};
      __builtin_amdgcn_sched_barrier(0);
#pragma unroll
      for (int i = 0; i < 4; ++i) w[i] = __builtin_elementwise_fma(w[i], RC(1, i), RC(1, i));
      __builtin_amdgcn_sched_barrier(0);
#pragma unroll
      for (int i = 0; i < 4; ++i) w[i] = f32x2{__builtin_amdgcn_rcpf(w[i][0]), __builtin_amdgcn_rcpf(w[i][1])};
      __builtin_amdgcn_sched_barrier(0);
#pragma unroll
      for (int i = 0; i < 4; ++i) z[j][i] = u[i] * w[i];
#pragma unroll
      for (int i = 0; i < 4; ++i) {
        if (j == 1) sm[i] = z[0][i] + z[1][i];
        if (j > 1) sm[i] += z[j][i];
      }
#pragma unroll
      for (int i = 0; i < 4; ++i) sq[i] = j ? __builtin_elementwise_fma(z[j][i], z[j][i], sq[i]) : z[j][i] * z[j][i];
      asm volatile("" : "+v"(z[j][0]), "+v"(z[j][1]), "+v"(z[j][2]), "+v"(z[j][3]));
      __builtin_amdgcn_sched_barrier(0);
    }
  }
  if (BEARLY) asm volatile("s_mov_b64 exec, 1\n\tds_add_u32 %0, %1\n\ts_mov_b64 exec, -1" ::"v"(adw), "v"(mev) : "memory");
  f32x4 e;
  {
    float v[4][4], d[2][4], f[4];
#pragma unroll
    for (int i = 0; i < 4; ++i) {
      v[i][0] = sm[i][0];
      v[i][1] = sq[i][0];
      v[i][2] = sm[i][1];
      v[i][3] = sq[i][1];
    }
    asm volatile("s_nop 1" : "+v"(v[3][1]), "+v"(v[3][3]), "+v"(v[2][1]), "+v"(v[2][3]));
#pragma unroll
    for (int i = 0; i < 2; ++i)
#pragma unroll
      for (int c = 0; c < 4; ++c)
        asm volatile("v_add_f32_dpp %0, %1, %1 row_ror:8 row_mask:0xf bank_mask:0x3\n\tv_add_f32_dpp %0, %2, %2 row_ror:8 row_mask:0xf bank_mask:0xc"
                     : "=&v"(d[i][c])
                     : "v"(v[i][c]), "v"(v[i + 2][c]));
#pragma unroll
    for (int c = 0; c < 4; ++c)
      asm volatile("v_add_f32_dpp %0, %1, %1 row_shl:4 row_mask:0xf bank_mask:0x5\n\tv_add_f32_dpp %0, %2, %2 row_shr:4 row_mask:0xf bank_mask:0xa"
                   : "=&v"(f[c])
                   : "v"(d[0][c]), "v"(d[1][c]));
    asm volatile(
        "v_add_f32_dpp %0, %0, %0 quad_perm:[1,0,3,2] row_mask:0xf bank_mask:0xf\n\t"
        "v_add_f32_dpp %1, %1, %1 quad_perm:[1,0,3,2] row_mask:0xf bank_mask:0xf\n\t"
        "v_add_f32_dpp %2, %2, %2 quad_perm:[1,0,3,2] row_mask:0xf bank_mask:0xf\n\t"
        "v_add_f32_dpp %3, %3, %3 quad_perm:[1,0,3,2] row_mask:0xf bank_mask:0xf\n\t"
        "v_add_f32_dpp %0, %0, %0 quad_perm:[2,3,0,1] row_mask:0xf bank_mask:0xf\n\t"
        "v_add_f32_dpp %1, %1, %1 quad_perm:[2,3,0,1] row_mask:0xf bank_mask:0xf\n\t"
        "v_add_f32_dpp %2, %2, %2 quad_perm:[2,3,0,1] row_mask:0xf bank_mask:0xf\n\t"
        "v_add_f32_dpp %3, %3, %3 quad_perm:[2,3,0,1] row_mask:0xf bank_mask:0xf"
        : "+v"(f[0]), "+v"(f[1]), "+v"(f[2]), "+v"(f[3]));
    e = f32x4{f[0], __uint_as_float(__float_as_uint(f[1]) & 0x7fffffffu), f[2], __uint_as_float(__float_as_uint(f[3]) & 0x7fffffffu)};
  }
  asm volatile("s_mov_b64 exec, %3\n\tglobal_store_dwordx4 %0, %1, %2 sc1\n\ts_mov_b64 exec, -1\n\ts_nop 0" ::"v"(po), "v"(e), "s"(zp),
               "s"(0x1111111111111111ull)
               : "memory");
  if (!BEARLY) asm volatile("s_mov_b64 exec, 1\n\tds_add_u32 %0, %1\n\ts_mov_b64 exec, -1" ::"v"(adw), "v"(mev) : "memory");
  __builtin_amdgcn_s_setprio(2);
  MK(12);
  const char* dst = uni_ptr(ws + OFF_Z2 + (size_t)layer * SZ_Z + (size_t)ni * S_ * 288 + (size_t)(mi * 4 + wm) * 9216 + 4096);
  const unsigned lane16 = (unsigned)lane * 16u;
  u32x4 wb[9];
  {
    static_assert(b_k(7) == 2 && b_k(8) == 3, "order");
    const char* wp = uni_ptr(ws + OFF_PI + (size_t)ni * (18 * K3C::WB) + 11 * K3C::WB + wm * 9216 + 4096);
#pragma unroll
    for (int j = 0; j < 8; ++j) asm volatile("global_load_dwordx4 %0, %1, %2 offset:%3 sc1" : "=v"(wb[j]) : "v"(lane16), "s"(wp), "n"(j * 1024 - 4096) : "memory");
    asm volatile("global_load_dwordx4 %0, %1, %2 sc1" : "=v"(wb[8]) : "v"(lane16 + 4096u), "s"(wp) : "memory");
  }
  u32x4 y[9];
  {
    const unsigned src = (unsigned)(g * 4096 + r * 16);
    unsigned zw = lds0 + (unsigned)(K3_ZC_OFF + (wm * 32 + 2 * r + (g & 1)) * 4), zr = lds0 + (unsigned)(K3_ZC_OFF + (wm * 32 + g * 8) * 4);
    asm volatile("" : "+v"(zw), "+v"(zr));
    f32x4 a, b, c, d;
    __builtin_amdgcn_s_sleep(ZSLEEP);
#pragma unroll 1
    for (unsigned spin = 0; spin < SPIN_MEM; ++spin) {
      asm volatile(
          "global_load_dwordx4 %0, %4, %5 sc1\n\t"
          "global_load_dwordx4 %1, %4, %5 offset:1024 sc1\n\t"
          "global_load_dwordx4 %2, %4, %5 offset:2048 sc1\n\t"
          "global_load_dwordx4 %3, %4, %5 offset:3072 sc1\n\t"
          "s_waitcnt vmcnt(0)"
          : "=&v"(a), "=&v"(b), "=&v"(c), "=&v"(d)
          : "v"(src), "s"(zp)
          : "memory");
      const unsigned bad = (__float_as_uint(a[1]) | __float_as_uint(a[3]) | __float_as_uint(b[1]) | __float_as_uint(b[3]) |
                            __float_as_uint(c[1]) | __float_as_uint(c[3]) | __float_as_uint(d[1]) | __float_as_uint(d[3])) >> 31;
      if (!__any(bad)) break;
    }
    MK(13);
    float s, q, zmb, zi;
    red16(a, b, c, d, s, q);
    ln_finish(s, q, 1.0f / (float)FH_, zmb, zi);
    f32x4 R[2], M[2];
    asm volatile(
        "ds_write2st64_b32 %4, %5, %6 offset1:2\n\t"
        "ds_read_b128 %0, %7\n\tds_read_b128 %2, %7 offset:512\n\tds_read_b128 %1, %7 offset:16\n\tds_read_b128 %3, %7 offset:528\n\t"
        "s_waitcnt lgkmcnt(0)"
        : "=&v"(R[0]), "=&v"(R[1]), "=&v"(M[0]), "=&v"(M[1])
        : "v"(zw), "v"(zi), "v"(-zmb * zi), "v"(zr)
        : "memory");
    MK(28);
#define NRM 0
    auto st = [&](int j) {
      if (j < 8) asm volatile("global_store_dwordx4 %0, %1, %2 offset:%3 sc1\n\ts_nop 1" ::"v"(lane16), "v"(y[j]), "s"(dst), "n"(j * 1024 - 4096) : "memory");
      else asm volatile("global_store_dwordx4 %0, %1, %2 sc1\n\ts_nop 1" ::"v"(lane16 + 4096u), "v"(y[j]), "s"(dst) : "memory");
    };
#pragma unroll
    for (int j = 0; j < 9; ++j) {
      if (NRM & 2) {
        f32x2 t[4];
#pragma unroll
        for (int k = 0; k < 4; ++k)
          t[k] = __builtin_elementwise_fma(z[j][k], f32x2{R[k / 2][(k % 2) * 2], R[k / 2][(k % 2) * 2 + 1]}, f32x2{M[k / 2][(k % 2) * 2], M[k / 2][(k % 2) * 2 + 1]});
        y[j] = u32x4{pack2v(t[0]), pack2v(t[1]), pack2v(t[2]), pack2v(t[3])};
      } else {
        float t[8];
#pragma unroll
        for (int k = 0; k < 8; ++k) {
          t[k] = __builtin_fmaf(z[j][k / 2][k % 2], R[k / 4][k % 4], M[k / 4][k % 4]);
          asm volatile("" : "+v"(t[k]));
        }
        y[j] = u32x4{pack2(t[0], t[1]), pack2(t[2], t[3]), pack2(t[4], t[5]), pack2(t[6], t[7])};
      }
      if (NRM & 1) asm volatile("" : "+v"(y[j]));
      else st(j);
      __builtin_amdgcn_sched_barrier(0);
    }
    MK(29);
    if (NRM & 1) {
#pragma unroll
      for (int j = 0; j < 9; ++j) st(j);
    }
  }
  lds_inc(lw + LW_AD * 4, me);
  MK(24);
  __builtin_amdgcn_s_setprio(G0P);
  lds_spin4(lw + LW_S * 4, 4u + (unsigned)(wm >> 1));
  {
    const unsigned fa = lds0 + (unsigned)((wm >> 1) * SB + K3C::XB + (wm & 1) * 9216) + lane16;
#pragma unroll
    for (int j = 0; j < 9; ++j) {
      asm volatile("ds_write_b128 %0, %1 offset:%2" ::"v"(fa), "v"(wb[j]), "n"(j * 1024) : "memory");
      if (PACE && j < 8) __builtin_amdgcn_s_sleep(PACE);
    }
    lds_inc(lw + LW_AD * 4, me);
  }
  announce(lw + LW_Z * 4, 4u, ws, CN_Z + layer, lane * 16 + ni, lane < 16);
  lds_inc(lw + LW_AD * 4, me);
  MK(27);
  f32x4 acc[2][9];
  {
    float bs[2];
    asm volatile("ds_read2_b32 %0, %1 offset1:16\n\ts_waitcnt lgkmcnt(0)"
                 : "=v"(*reinterpret_cast<f32x2*>(bs))
                 : "v"(lds0 + (unsigned)(K3_BS_OFF + (wm * 32 + r) * 4))
                 : "memory");
    lds_spin4(lw + LW_S * 4, 6u + (unsigned)(wm >> 1));
    const unsigned fa = lds0 + (unsigned)((2 + (wm >> 1)) * SB + K3C::XB + (wm & 1) * 9216) + lane16;
#pragma unroll
    for (int j = 0; j < 9; ++j) {
      asm volatile("ds_write_b128 %0, %1 offset:%2" ::"v"(fa), "v"(y[j]), "n"(j * 1024) : "memory");
      if (PACE && j < 8) __builtin_amdgcn_s_sleep(PACE);
    }
    lds_inc(lw + LW_AD * 4, me);
#pragma unroll
    for (int mt = 0; mt < 2; ++mt) {
      f32x4 b4 = {bs[mt], bs[mt], bs[mt], bs[mt]};
      asm volatile("" : "+v"(b4));
#pragma unroll
      for (int j = 0; j < 9; ++j) acc[mt][j] = b4;
    }
  }
  u32x4 z1[9];
  const char* hp = uni_ptr(ws + OFF_H + (size_t)layer * SZ_Z + (size_t)((mi * 2 + (wm >> 1)) * 288 + ni * 18) * 1024);
  unsigned ho = (unsigned)((lane >> 5) * 1024 + ((wm & 1) * 32 + (lane & 31)) * 16);
  unsigned zb = lds0 + (unsigned)(wm * 4096) + lane16, zf = lw + (unsigned)(LW_S * 4 + wm);
  asm volatile("" : "+v"(ho), "+v"(zb), "+v"(zf));
  asm volatile("" : "+s"(hp));
  {
    GemmMath<K3C, 2, 9, K3_D, 6, false, 2> gm(acc);
    gm.setup(lds0, wm * 2, 0, lane, (unsigned)(2 * SB));
    asm volatile("" : "+v"(gm.xrd[0]), "+v"(gm.xrd[1]), "+v"(gm.wrd), "+v"(gm.xa[0]), "+v"(gm.xa[1]), "+v"(gm.wa));
    MK(15);
    lds_spin4(lw + LW_L * 4, 8u);
    lds_spin4(lw + LW_AD * 4, 6u);
    MK(17);
    gm.first();
    __builtin_amdgcn_s_setprio(0);
    gm.template mid<1, false>();
    gm.template mid<11, true, AL_C>();
    const unsigned za = gm.wrd + (unsigned)(K3_Z1_OFF - K3C::XB + wm * 5120);
#pragma unroll
    for (int j = 0; j < NR1; ++j) asm volatile("ds_read_b128 %0, %1 offset:%2" : "=v"(z1[j]) : "v"(za), "n"(j * 1024));
    gm.template mid<19, true, AL_C>();
    gm.last();
  }
  MK(16);
  if (NR1 < 5) lds_inc(lw + LW_AD * 4, me);
  unsigned zv;
  u32x4 zl[4];
#pragma unroll
  for (int j = 0; j < 9; ++j) {
    const bool sec = j >= NR1 + 3;
    if (j == NR1 || j == NR1 + 4) {
      unsigned n = 1u << 18;
      asm volatile(
          "s_waitcnt lgkmcnt(0)\n\tv_cmp_gt_u32 vcc, %8, %0\n\ts_cbranch_vccnz .Lw%=\n\t.subsection 1\n.Lw%=:\n\tds_read_u8 %0, %6\n\tds_read_b128 %2, %7 offset:%9\n\t"
          "ds_read_b128 %3, %7 offset:%9+1024\n\tds_read_b128 %4, %7 offset:%9+2048\n\tds_read_b128 %5, %7 offset:%9+3072\n\ts_waitcnt lgkmcnt(0)\n\t"
          "v_cmp_gt_u32 vcc, %8, %0\n\t" SAFE_OUT(".Lr%=") "s_cbranch_vccnz .Lw%=\n\ts_branch .Lr%=\n\t.subsection 0\n.Lr%=:"
          : "+v"(zv), NOP_(n), "+v"(zl[0]), "+v"(zl[1]), "+v"(zl[2]), "+v"(zl[3])
          : "v"(zf), "v"(zb), "n"(sec ? 11 : 10), "n"(sec ? SB : 0)
          : "memory", "vcc", "scc");
#pragma unroll
      for (int k = 0; k < 4; ++k)
        if (j + k < 9) z1[j + k] = zl[k];
    }
    if (j == NR1 - 1 || (j == NR1 + 3 && j < 8))
      asm volatile("ds_read_u8 %0, %5\n\tds_read_b128 %1, %6 offset:%7\n\tds_read_b128 %2, %6 offset:%7+1024\n\tds_read_b128 %3, %6 offset:%7+2048\n\t"
                   "ds_read_b128 %4, %6 offset:%7+3072"
                   : "=&v"(zv), "=&v"(zl[0]), "=&v"(zl[1]), "=&v"(zl[2]), "=&v"(zl[3])
                   : "v"(zf), "v"(zb), "n"(sec ? SB : 0)
                   : "memory");
    asm volatile("" : "+v"(z1[j]));
    u32x2 h[2];
#pragma unroll
    for (int mt = 0; mt < 2; ++mt) {
      const f32x4 a = acc[mt][j];
      h[mt] = u32x2{pack2v(f32x2{bf_lo(z1[j][2 * mt]), bf_hi(z1[j][2 * mt])} * f32x2{a[0], a[1]}),
                    pack2v(f32x2{bf_lo(z1[j][2 * mt + 1]), bf_hi(z1[j][2 * mt + 1])} * f32x2{a[2], a[3]})};
    }
    const auto s0 = __builtin_amdgcn_permlane16_swap(h[0][0], h[1][0], false, false);
    const auto s1 = __builtin_amdgcn_permlane16_swap(h[0][1], h[1][1], false, false);
    const u32x4 v = {s0[0], s1[0], s0[1], s1[1]};
    asm volatile("global_store_dwordx4 %0, %1, %2 offset:%3 sc1\n\ts_nop 1" ::"v"(ho), "v"(v), "s"(hp + (j / 2) * 4096), "n"((j % 2) * 2048) : "memory");
    __builtin_amdgcn_sched_barrier(0);
  }
  MK(18);
}

DEVI void k3_const(char* smem, char* ws, const u16* b_sp, int layer, int bid, int mi, int ni, int wm) {
  const int lane = lane_id(), l = lane < 54 ? lane : 53, p = l / 18;
  const char* kp = uni_ptr(ws + OFF_CSK + bid * 2048 + wm * 512);
  const unsigned q16 = (unsigned)(l - p * 18) * 16u;
  unsigned bs;
  f32x4 t;
  asm volatile("global_load_ushort %0, %1, %2" : "=v"(bs) : "v"((unsigned)(lane & 31) * 2u), "s"(uni_ptr((const char*)(b_sp + mi * 128 + wm * 32))) : "memory");
  if (layer) {
    asm volatile("global_load_dwordx4 %0, %1, %2 sc1\n\ts_waitcnt vmcnt(0)" : "=&v"(t) : "v"(q16), "s"(kp) : "memory");
  } else {
    const char* cp = uni_ptr(ws + OFF_CSP + ni * 1152 + wm * 288);
    f32x4 a[6];
#pragma unroll
    for (int i = 0; i < 6; ++i)
      asm volatile("global_load_dwordx4 %0, %1, %2 sc1" : "=v"(a[i]) : "v"(q16 + (unsigned)(p * (6 * F_ * 4))), "s"(cp + i * (F_ * 4)) : "memory");
    asm volatile("s_waitcnt vmcnt(0)" : "+v"(a[0]), "+v"(a[1]), "+v"(a[2]), "+v"(a[3]), "+v"(a[4]), "+v"(a[5]));
    t = ((a[0] + a[1]) + (a[2] + a[3])) + (a[4] + a[5]);
#pragma unroll
    for (int j = 0; j < 4; ++j)
      t[j] += __int_as_float(__builtin_amdgcn_ds_bpermute((lane + 18) * 4, __float_as_int(t[j]))) +
              __int_as_float(__builtin_amdgcn_ds_bpermute((lane + 36) * 4, __float_as_int(t[j])));
    if (lane < 24) asm volatile("global_store_dwordx4 %0, %1, %2\n\ts_nop 1" ::"v"((unsigned)lane * 16u), "v"(t), "s"(kp) : "memory");
  }
  asm volatile("" : "+v"(bs));
  if (lane < 18) {
    if (wm < 2) {
      *reinterpret_cast<f32x4*>(smem + K3_C2_OFF + wm * 576 + lane * 32) = f32x4{t[0], t[0], t[1], t[1]};
      *reinterpret_cast<f32x4*>(smem + K3_C2_OFF + wm * 576 + lane * 32 + 16) = f32x4{t[2], t[2], t[3], t[3]};
    } else {
      *reinterpret_cast<f32x4*>(smem + K3_C1_OFF + (wm - 2) * 288 + lane * 16) = t;
    }
  }
  if (lane < 32) *reinterpret_cast<float*>(smem + K3_BS_OFF + (wm * 32 + lane) * 4) = bf_lo(bs);
}

DEVI void p3_g1(char* smem, char* __restrict__ ws, const char* code0, int bid, int layer, int mi, int wm, int lane MK_ARG) {
  const unsigned lds0 = (unsigned)(size_t)smem;
  const int g = lane >> 4, r = lane & 15;
  const unsigned lw = lds0 + (unsigned)(LW_OFF + layer * 4);
  const unsigned xrd = lds0 + (unsigned)(g * 2048 + (wm * 32 + r) * 16);
  s16x8 xc[4][2][2];
  {
    const char* xs = uni_ptr(ws + OFF_XS + (size_t)layer * 2 * SZ_XS + (size_t)(mi * 2 + (wm >> 1)) * 4096 + (wm & 1) * 256);
    f32x4 a, b;
    auto pick = [&](int t) {
      bar();
      asm volatile(
          "ds_read_b128 %0, %4\n\tds_read_b128 %1, %4 offset:256\n\tds_read_b128 %2, %4 offset:8192\n\tds_read_b128 %3, %4 offset:8448\n\t"
          "s_waitcnt lgkmcnt(0)"
          : "=&v"(xc[t][0][0]), "=&v"(xc[t][0][1]), "=&v"(xc[t][1][0]), "=&v"(xc[t][1][1])
          : "v"(xrd + (unsigned)(t * K3C::SB))
          : "memory");
    };
#define G1P 2
    __builtin_amdgcn_s_setprio(G1P);
    pick(0);
    pick(1);
    asm volatile("global_load_dwordx4 %0, %2, %3 sc1\n\tglobal_load_dwordx4 %1, %2, %3 offset:512 sc1"
                 : "=&v"(a), "=&v"(b)
                 : "v"((unsigned)(g * 1024 + r * 16)), "s"(xs)
                 : "memory");
    pick(2);
    pick(3);
    bar();
    asm volatile("s_waitcnt vmcnt(0)" : "+v"(a), "+v"(b) : : "memory");
    float s, q, mb, rinv;
    red8(a, b, s, q);
    bar();
    asm volatile("" : "+v"(s), "+v"(q));
    ln_finish(s, q, 1.0f / (float)D_, mb, rinv);
    asm volatile("" : "+v"(mb), "+v"(rinv));
    bar();
    asm volatile("" : "+v"(mb), "+v"(rinv));
    const float k0 = -2.0f * 0.7978845608028654f * 1.4426950408889634f, k1 = k0 * 0.044715f;
    float sd = __builtin_amdgcn_rcpf(rinv), c0 = k0 * rinv, c1 = k1 * rinv * rinv * rinv;
    asm volatile("" : "+v"(sd));
    asm volatile("" : "+v"(c0));
    asm volatile("" : "+v"(c1));
    const float e0 = lane < 32 ? -mb : c0, e1 = lane < 32 ? sd : c1;
    asm volatile("ds_write2st64_b32 %0, %1, %2 offset1:2" ::"v"(
                     lds0 + (unsigned)(K3_ST_OFF + (g >> 1) * 1024 + (wm * 32 + 2 * r + (g & 1)) * 4)),
                 "v"(e0), "v"(e1)
                 : "memory");
    bar();
    asm volatile("s_waitcnt lgkmcnt(0)" ::: "memory");
    bar();
    __builtin_amdgcn_s_setprio(0);
  }
  f32x4 ac[2][9];
#pragma unroll
  for (int mt = 0; mt < 2; ++mt)
#pragma unroll
    for (int j = 0; j < 9; ++j) ac[mt][j] = f32x4{0.f, 0.f, 0.f, 0.f};
  {
    UMath<true> gm(ac, xc);
    gm.xa = xrd;
    gm.wa = lds0 + (unsigned)(K3C::XB + (g * 8 + (r & 7)) * K3C::P + (r >> 3) * 16);
    gm.fa = lw + (unsigned)(LW_AD * 4 + (lane & 3));
    gm.fw = lw + (unsigned)(LW_S * 4 + wm);
    gm.fv = 0u;
    asm volatile("" : "+v"(gm.fa), "+v"(gm.fw), "+v"(gm.fv), "+v"(gm.xa), "+v"(gm.wa));
    __builtin_amdgcn_s_sleep(G1SL);
    lds_spin4<4>(lw + LW_L * 4, 4u);
    gm.first();
    place<AL_B>();
    lds_spin4(lw + LW_AD * 4, 2u);
#define BPR 1
    __builtin_amdgcn_s_setprio(BPR);
    gm.run();
    __builtin_amdgcn_s_setprio(0);
  }
  MK(14);
  f32x2 rc[4];
  f32x4 cs[9];
  const int l2 = lane_id();
  {
    const unsigned ra = lds0 + (unsigned)(K3_ST_OFF + (wm * 32 + (l2 & 15)) * 4), ca = lds0 + (unsigned)(K3_C1_OFF + (l2 & 48));
#pragma unroll
    for (int k = 0; k < 4; ++k) asm volatile("ds_read2_b32 %0, %1 offset1:16" : "=v"(rc[k]) : "v"(ra + (unsigned)(k * 512)) : "memory");
#pragma unroll
    for (int j = 0; j < 9; ++j) asm volatile("ds_read_b128 %0, %1 offset:%2" : "=v"(cs[j]) : "v"(ca), "n"(j * 64) : "memory");
    asm volatile("s_waitcnt lgkmcnt(0)" : "+v"(rc[0]), "+v"(rc[1]), "+v"(rc[2]), "+v"(rc[3]));
#pragma unroll
    for (int j = 0; j < 9; ++j) asm volatile("" : "+v"(cs[j]));
  }
  const unsigned za = lds0 + (unsigned)(K3_Z1_OFF + wm * 5120 + l2 * 16);
  u32x4 z1[9];
#pragma unroll
  for (int t = 0; t < 18; ++t) {
    const int j = t / 2, mt = t % 2;
    float u[4], w[4];
#pragma unroll
    for (int i = 0; i < 4; ++i) asm volatile("v_fma_f32 %0, %1, %2, %3" : "=v"(u[i]) : "v"(rc[0][mt]), "v"(cs[j][i]), "v"(ac[mt][j][i]));
#pragma unroll
    for (int i = 0; i < 4; ++i) asm volatile("v_mul_f32 %0, %1, %1" : "=v"(w[i]) : "v"(u[i]));
#pragma unroll
    for (int i = 0; i < 4; ++i) asm volatile("v_fma_f32 %0, %0, %1, %2" : "+v"(w[i]) : "v"(rc[3][mt]), "v"(rc[2][mt]));
#pragma unroll
    for (int i = 0; i < 4; ++i) asm volatile("v_mul_f32 %0, %0, %1" : "+v"(w[i]) : "v"(u[i]));
#pragma unroll
    for (int i = 0; i < 4; ++i) asm volatile("v_exp_f32 %0, %0" : "+v"(w[i]));
#pragma unroll
    for (int i = 0; i < 4; ++i) asm volatile("v_fma_f32 %0, %0, %1, %1" : "+v"(w[i]) : "v"(rc[1][mt]));
#pragma unroll
    for (int i = 0; i < 4; ++i) asm volatile("v_rcp_f32 %0, %0" : "+v"(w[i]));
#pragma unroll
    for (int i = 0; i < 4; ++i) asm volatile("v_mul_f32 %0, %0, %1" : "+v"(w[i]) : "v"(u[i]));
    z1[j][2 * mt] = pack2(w[0], w[1]);
    z1[j][2 * mt + 1] = pack2(w[2], w[3]);
    if (mt && j < NR1) asm volatile("ds_write_b128 %0, %1 offset:%2" ::"v"(za), "v"(z1[j]), "n"(j * 1024) : "memory");
    if (t == 9) asm volatile("s_waitcnt lgkmcnt(0)" ::: "memory");
    __builtin_amdgcn_sched_barrier(0);
    bar();
    __builtin_amdgcn_sched_barrier(0);
  }
  MK(15);
#pragma unroll 1
  for (int t = 20; t < 32; ++t) bar();
  __builtin_amdgcn_s_sleep(ZSL);
#pragma unroll
  for (int k = NR1; k < 9; ++k) {
    if (k == NR1 + 4) lds_spin4(lw + LW_AD * 4, 7u);
    asm volatile("ds_write_b128 %0, %1 offset:%2" ::"v"(lds0 + (unsigned)(wm * 4096 + l2 * 16)), "v"(z1[k]),
                 "n"(((k - NR1) / 4) * K3C::SB + ((k - NR1) % 4) * 1024)
                 : "memory");
    if (k == NR1 + 3 || k == 8) asm volatile("ds_write_b8 %0, %1" ::"v"(lw + (unsigned)(LW_S * 4 + wm)), "v"(vconst(k == NR1 + 3 ? 10u : 11u)) : "memory");
  }
  if ((RTC & 2) && wm == 2) code_again(code0, bid);
}

constexpr int W5A = 22, W5B = 26;
constexpr int NPF = 30;
struct Loader {
  u32x4 pf[NPF];
  unsigned lds0, lane16;
  int wl, lane;

  template <int R0, int N, int P0, int PE>
  DEVI void pf_load(const char* src) {
    const unsigned o = lane16 + (unsigned)(wl * 1024);
#pragma unroll
    for (int t = 0; t < N; ++t) {
      unsigned a = o + (unsigned)((P0 + 4 * t) * 1024);
      if (P0 + 4 * t + 3 >= PE && P0 + 4 * t + wl >= PE) a = lane16;
      asm volatile("global_load_dwordx4 %0, %1, %2 sc1" : "=v"(pf[R0 + t]) : "v"(a), "s"(src) : "memory");
    }
  }
  template <int R0, int N>
  DEVI void pf_pin() {
#pragma unroll
    for (int t = 0; t < N; ++t) asm volatile("" : "+v"(pf[R0 + t]));
  }
  template <int R0, int N>
  DEVI void pf_undef() {
#pragma unroll
    for (int t = 0; t < N; ++t) asm volatile("" : "=v"(pf[R0 + t]));
  }
  template <int PB, int A, int N, int SL = 0>
  DEVI void pf_put(unsigned dst) {
    constexpr int T0 = (A - PB) / 4, T1 = (A + N - 1 - PB) / 4;
    const unsigned a = lds0 + dst + lane16 + (unsigned)((wl + PB + 4 * T0 - A + 4) * 1024) - 4096u;
#pragma unroll
    for (int t = T0; t <= T1; ++t) {
      const int p0 = PB + 4 * t;
      if ((p0 >= A || p0 + wl >= A) && (p0 + 3 < A + N || p0 + wl < A + N))
        asm volatile("ds_write_b128 %0, %1 offset:%2" ::"v"(a), "v"(pf[t]), "n"((t - T0) * 4096) : "memory");
      if (SL) __builtin_amdgcn_s_sleep(SL);
    }
  }
  template <int N>
  DEVI void dma_run(const char* src, unsigned dst) {
    const unsigned o = lane16 + (unsigned)(wl * 1024), m = lds0 + dst + (unsigned)(wl * 1024);
#pragma unroll
    for (int i = 0; i < (N + 3) / 4; ++i)
      if (4 * i + 3 < N || 4 * i + wl < N) dma16_sc1(src + i * 4096, o, m + (unsigned)(i * 4096));
  }
  template <int V>
  DEVI void wait_w() {
    asm volatile("s_cmp_lt_u32 %0, 2\n\ts_cbranch_scc1 1f\n\ts_waitcnt vmcnt(%1)\n1:\n\ts_waitcnt vmcnt(%2)" ::"s"(wl), "n"(V - 1), "n"(V) : "memory", "scc");
  }
  template <int V>
  DEVI void wbar() {
    asm volatile("s_waitcnt vmcnt(%0) lgkmcnt(0)" ::"n"(V) : "memory");
    bar();
  }

  DEVI void p1(char* smem, char* ws, int bid, int rt, int hp MK_ARG) {
    constexpr int SB = K1C::SB;
    static_assert(K1C::XB == 18 * 1024 && K1C::WB == 14 * 1024, "K1 shape");
    const char* wp = uni_ptr(ws + OFF_PQ + (size_t)hp * (9 * K1C::WB) + 4 * K1C::WB);
    static_assert(3 * SB + K1C::XB >= P5_W + 5 * 18432, "free while the GEMM before ends");
    const char* wq = wp - 4 * K1C::WB;
    dma_run<14>(wq + 3 * K1C::WB, (unsigned)(3 * SB + K1C::XB));
    dma_run<28>(wp, (unsigned)K1_WX);
    if (CSL && wl == 3) dma16_sc1(uni_ptr(ws + OFF_CSQ + bid * 512), lane16 & 511u, lds0 + (unsigned)K1_CS_OFF);
    if (P1PR & 1) __builtin_amdgcn_s_setprio(0);
    if (P1PR & 2) wait_vm<0>();
    const bool lo = wl < 2;
    const int x0 = lo ? wl * 5 : wl * 4 + 2, w0 = lo ? wl * 3 : wl * 4 - 2;
    unsigned xo[5];
#pragma unroll
    for (int i = 0; i < 5; ++i) {
      const int c = (x0 + i) * 64 + lane;
      const int ch = c / 144;
      int gr = rt * 128 - 16 + (c - ch * 144);
      if (gr < 0) gr = 0;
      xo[i] = (unsigned)(((gr >> 6) * 72 + ch) * 1024 + (gr & 63) * 16);
      asm volatile("" : "+v"(xo[i]));
    }
    const char* xp = uni_ptr(ws + OFF_X1 + SZ_X);
    wp += 2 * K1C::WB + w0 * 1024;
    auto dma_x = [&](unsigned so) {
      const unsigned m = lds0 + so + (unsigned)(x0 * 1024);
#pragma unroll
      for (int i = 0; i < 4; ++i) dma16_sc1(xp, xo[i], m + (unsigned)(i * 1024));
      if (lo) dma16_sc1(xp, xo[4], m + 4096u);
      xp += 8192;
    };
    auto dma_w = [&](unsigned so) {
      const unsigned m = lds0 + so + (unsigned)(K1C::XB + w0 * 1024);
#pragma unroll
      for (int i = 0; i < 3; ++i) dma16_sc1(wp + i * 1024, lane16, m + (unsigned)(i * 1024));
      if (!lo) dma16_sc1(wp + 3072, lane16, m + 3072u);
      wp += K1C::WB;
    };
    const char* xs = uni_ptr(ws + OFF_XS + SZ_XS);
    const int g = lane >> 4, p2 = 64 + (lane & 7);
    int ra = rt * 128 - 16 + wl * 32 + 2 * (lane & 15);
    if (ra < 0) ra = 0;
    unsigned sa = (unsigned)(((ra >> 6) * 8 + g * 2) * 512 + (ra & 63) * 8);
    unsigned sb = (unsigned)(((rt * 2 + 1) * 8 + (lane >> 3)) * 512 + (2 * p2 - 80) * 8);
    unsigned sw = lds0 + (unsigned)(K1_ST_OFF + (2 * (lane < 32 ? wl * 16 + (lane & 15) : p2) + (g & 1)) * 8);
    asm volatile("" : "+v"(sa), "+v"(sb), "+v"(sw));
    MK(22);
    __builtin_amdgcn_s_setprio(3);
    bar();
    dma_run<14>(wq, (unsigned)K1C::XB);
    dma_run<14>(wq + K1C::WB, (unsigned)(SB + K1C::XB));
    dma_run<14>(wq + 2 * K1C::WB, (unsigned)(2 * SB + K1C::XB));
    MK(1);
    bar();
    dma_x(0u);
    dma_x((unsigned)SB);
    wbar<4>();
    MK(29);
    dma_x((unsigned)(2 * SB));
    dma_x((unsigned)(3 * SB));
    wbar<8>();
    MX(1, 3);
    dma_x(0u);
    wbar<8>();
    MX(1, 4);
    dma_x((unsigned)SB);
    f32x4 a, b, e;
    asm volatile("global_load_dwordx4 %0, %3, %5 sc1\n\tglobal_load_dwordx4 %1, %3, %5 offset:512 sc1\n\tglobal_load_dwordx4 %2, %4, %5 sc1"
                 : "=&v"(a), "=&v"(b), "=&v"(e)
                 : "v"(sa), "v"(sb), "s"(xs)
                 : "memory");
    wbar<11>();
    MX(1, 5);
    dma_w((unsigned)SB);
    dma_x((unsigned)(2 * SB));
    wbar<15>();
    MX(1, 6);
    dma_w((unsigned)(2 * SB));
    dma_x((unsigned)(3 * SB));
    wbar<19>();
    MX(1, 7);
    dma_w((unsigned)(3 * SB));
    dma_x(0u);
    wbar<16>();
    MX(1, 8);
    asm volatile("" : "+v"(a), "+v"(b), "+v"(e));
    float s, q;
    {
      float t[4] = {e[0], e[1], e[2], e[3]}, s2, q2;
      asm volatile(
          "v_add_f32_dpp %0, %0, %0 row_ror:8 row_mask:0xf bank_mask:0xf\n\tv_add_f32_dpp %1, %1, %1 row_ror:8 row_mask:0xf bank_mask:0xf\n\t"
          "v_add_f32_dpp %2, %2, %2 row_ror:8 row_mask:0xf bank_mask:0xf\n\tv_add_f32_dpp %3, %3, %3 row_ror:8 row_mask:0xf bank_mask:0xf\n\ts_nop 1"
          : "+v"(t[0]), "+v"(t[1]), "+v"(t[2]), "+v"(t[3]));
      red4(t, s2, q2);
      red8(a, b, s, q);
      s = lane < 32 ? s : s2;
      q = lane < 32 ? q : q2;
      asm volatile("" : "+v"(s), "+v"(q));
    }
    wbar<8>();
    MX(1, 9);
    {
      asm volatile("" : "+v"(s), "+v"(q));
      float mb, rinv;
      ln_finish(s, q, 1.0f / (float)D_, mb, rinv);
      asm volatile("ds_write_b64 %0, %1" ::"v"(sw), "v"(f32x2{-mb, rinv}) : "memory");
    }
    wbar<0>();
    MK(30);
  }

  template <int I>
  DEVI void p5_step(const char* wp) {
    constexpr int T = 2 * I + 1;
    wbar<(T == 1 ? 63 : (T <= 11 ? 3 : 0))>();
    if constexpr (T <= 9) {
      auto dm = [&]() {
      if (!G1W) {
        const unsigned wo = (unsigned)(12288 + (wl & 1) * 3072), so = (unsigned)(P5_W + ((T + 2 + (wl >> 1)) & 7) * 18432);
#pragma unroll
        for (int k = 0; k < 3; ++k) dma16_sc1(wp + (T + 7) * 18432 + k * 1024, lane16 + wo + (unsigned)((wl >> 1) * 18432), lds0 + so + wo + (unsigned)(k * 1024));
      }
      };
      if (!P5ORD) dm();
      pf_put<0, 12 * (T - 1), 12, P5PACE>((unsigned)(P5_W + ((T + 2) % 8) * 18432));
      pf_put<0, 12 * T, 12, P5PACE>((unsigned)(P5_W + ((T + 3) % 8) * 18432));
      if (P5ORD == 2) asm volatile("s_waitcnt lgkmcnt(0)" ::: "memory");
      if (P5ORD) dm();
    }
  }
  template <int... S>
  DEVI void p5_steps(const char* wp, std::integer_sequence<int, S...>) {
    (p5_step<S>(wp), ...);
  }

  template <int T0, int T1>
  DEVI void w5_load(const char* w5p) {
    const unsigned o = lane16 + (unsigned)(wl * 1024);
#pragma unroll
    for (int t = T0; t < T1; ++t)
      asm volatile("global_load_dwordx4 %0, %1, %2 sc1" : "=v"(pf[t]) : "v"(o + (unsigned)((t / 3 + 8) * 18432 + (t % 3) * 4096)), "s"(w5p) : "memory");
  }
  DEVI void p35(char* smem, char* ws, const u16* w_sp, int layer, int bid, int mi, int ni, int rb, int ct, int hp MK_ARG) {
    using C = K3C;
    constexpr int NS = C::NS, SB = C::SB, NDX = 4, NDW = 5, VPS = NDX + NDW;
    const char* wip = uni_ptr(ws + OFF_PI + (size_t)ni * (18 * C::WB));
    if (layer || L0REG) {
      pf_put<-48, 36, 18>((unsigned)(2 * SB + C::XB));
      pf_put<-48, 54, 18>((unsigned)(3 * SB + C::XB));
    } else {
      dma_run<18>(wip + 2 * C::WB, (unsigned)(2 * SB + C::XB));
      dma_run<18>(wip + 3 * C::WB, (unsigned)(3 * SB + C::XB));
    }
    if (W01M == 2) dma_run<18>(wip, (unsigned)C::XB);
    if (W01M == 1 || W01M == 2) dma_run<18>(wip + C::WB, (unsigned)(SB + C::XB));
    const char* w5p = uni_ptr(ws + OFF_PW + (size_t)ct * (36 * 9216));
    const int w0 = wl < 2 ? wl * 5120 : wl * 4096 + 2048;
    const unsigned m0x = lds0 + (unsigned)(wl * 4096), m0w = lds0 + (unsigned)(C::XB + w0);
    const unsigned wo = lane16 + (unsigned)w0;
    const unsigned lw = lds0 + (unsigned)(LW_OFF + layer * 4), me = 1u << (8 * wl);
    auto dma_w = [&](const char* wp, unsigned so) {
#pragma unroll
      for (int i = 0; i < NDW; ++i) {
        if (i < NDW - 1 || wl < 2) dma16_sc1(wp, wo + (unsigned)(i * 1024), m0w + so + (unsigned)(i * 1024));
      }
    };
    {
      const char* xp = uni_ptr(ws + OFF_X1 + (size_t)layer * 2 * SZ_X + (size_t)(mi * 2 * 72 + wl * 2) * 1024);
      const unsigned xo1 = lane16 + 72u * 1024u;
      auto dma_x = [&](int k, unsigned so) {
        const char* p = xp + k * 8192;
        dma16_sc1(p, lane16, m0x + so);
        dma16_sc1(p, xo1, m0x + so + 1024u);
        dma16_sc1(p + 1024, lane16, m0x + so + 2048u);
        dma16_sc1(p + 1024, xo1, m0x + so + 3072u);
      };
      if (!MPOLL && wl == 0) cnt_wait(ws, CN_X1 + layer, mi * 16 + ni, 16u);
      MK(10);
      bar();
      dma_x(0, 0u);
      dma_x(1, (unsigned)SB);
      wait_vm<4>(); MX(3, 2); wbar<4>(); MX(2, 2);
      MK(25);
      dma_x(2, (unsigned)(2 * SB));
      dma_x(3, (unsigned)(3 * SB));
      dma_x(4, (unsigned)(4 * SB));
      wbar<12>(); MX(2, 3);
      dma_x(5, 0u);
      wait_vm<12>();
      bar(); MX(2, 4);
      dma_w(wip + 4 * C::WB, 0u);
      dma_x(6, (unsigned)SB);
      wait_w<8 + VPS>();
#pragma unroll 1
      for (int t = 1; t < 3; ++t) {
        bar(); MX(2, 4 + t);
        dma_w(wip + (t + 4) * C::WB, (unsigned)(t * SB));
        dma_x(t + 6, (unsigned)((t + 1) * SB));
        wait_w<NDX + VPS>();
      }
      bar(); MX(2, 7);
      dma_w(wip + 7 * C::WB, (unsigned)(3 * SB));
      wait_w<VPS>();
      bar(); MX(2, 8);
      dma_w(wip + 8 * C::WB, 0u);
      wait_w<NDW>();
      bar(); MX(2, 9);
      const char* wp = wip + 9 * C::WB;
      dma_w(wp + b_k(0) * C::WB, (unsigned)SB);
      if (layer == 0) {
        const char* sp = uni_ptr((const char*)w_sp + (size_t)(bid * 4 + wl) * 8192);
        const unsigned so = (unsigned)((lane >> 5) * 4096 + (lane & 31) * 16);
#pragma unroll
        for (int i = 0; i < WSP_V; ++i) dma16(sp + i * 512, so, lds0 + (unsigned)K3_DM_OFF);
        dma16(sp + (bid >> 4) * 256, so & ~0x100u, lds0 + (unsigned)K3_DM_OFF);
      }
      w5_load<0, 4>(w5p);
      wait_w<NDW + 4>();
      MX(3, 10); bar(); MX(2, 10);
      dma_w(wp + b_k(1) * C::WB, (unsigned)(2 * SB));
      w5_load<4, 8>(w5p);
      MK(26);
      lds_spin4(lw + LW_AD * 4, 1u);
      dma_w(wp + b_k(2) * C::WB, (unsigned)(3 * SB));
      dma_w(wp + b_k(3) * C::WB, 0u);
#define TCH 0
      if (TCH & 1) {
        const int li = wl * 54 + (lane < 54 ? lane : 53);
        touch64(wp + C::WB, (unsigned)((mi & 7) * 13824 + li * 64), lds0 + (unsigned)K3_DM_OFF);
      }
      w5_load<8, W5A>(w5p);
      wait_vm<W5A - 8>();
      lds_inc(lw + LW_L * 4, 4u * me);
      w5_load<W5A, 30>(w5p);
      MK(11);
#define LSL 2
      static_for<4, 7>([&](auto T_) {
        constexpr int t = T_;
        lds_spin4<LSL>(lw + LW_S * 4, (unsigned)(t - 3));
        MX(2, 12 + t);
        dma_w(wp + b_k(t) * C::WB, (unsigned)(((t + 1) % 4) * SB));
        if constexpr (t > 4) {
          wait_w<NDW>();
          lds_inc(lw + LW_L * 4, me);
        }
      });
      MX(2, 19);
      wait_vm<0>();
      lds_inc(lw + LW_L * 4, me);
      pf_pin<0, 30>();
      MK(13);
    }
    {
      unsigned xoff[NDX];
#pragma unroll
      for (int i = 0; i < NDX; ++i) {
        const int c = wl * 256 + i * 64 + lane;
        const int row = c >> 3, cc = c & 7;
        xoff[i] = (unsigned)((mi * 128 + row) * S_ * 2) + (unsigned)((cc ^ ((row >> 1) & 7)) * 16);
      }
      auto dma_x = [&](const char* xp, unsigned so) {
#pragma unroll
        for (int i = 0; i < NDX; ++i) dma16(xp, xoff[i], m0x + so + (unsigned)(i * 1024));
      };
      const char* xp = uni_ptr((const char*)w_sp);
      const char* wp = uni_ptr(ws + OFF_Z2 + (size_t)layer * SZ_Z + (size_t)ni * S_ * 288);
#define XEARLY 0
      if (!XEARLY) lds_spin4<LSL>(lw + LW_AD * 4, 5u);
      dma_x(xp + mi * 256, (unsigned)(2 * SB));
      dma_x(xp + mi * 256 + 128, (unsigned)(3 * SB));
      if (XEARLY) lds_spin4<LSL>(lw + LW_AD * 4, 5u);
      MK(24);
      int left = 2 * mi;
      auto next = [&]() {
        const int d = --left == 0 ? 3 : 1;
        xp += d * 128;
        wp += d * C::WB;
      };
      if (mi == 0) {
        xp += 256;
        wp += 2 * C::WB;
      }
      dma_x(xp, 0u);
      lds_spin4(lw + LW_S * 4, 6u);
      dma_x(xp + 128, (unsigned)SB);
      wait_vm<2 * NDX>();
      lds_inc(lw + LW_L * 4, me);
      MK(27);
      if (wl == 0) {
        cnt_wait(ws, CN_Z + layer, mi * 16 + ni, 16u);
        lds_wr32(lw + LW_GO * 4, 1u);
      } else {
        lds_spin(lw + LW_GO * 4, 1u);
      }
      MK(14);
      lds_spin4(lw + LW_S * 4, 8u);
      dma_w(wp, 0u);
      lds_spin4(lw + LW_S * 4, 9u);
      dma_w(wp + C::WB, (unsigned)SB);
      next();
      next();
      unsigned slot = (unsigned)(2 * SB);
      auto cissue = [&]() {
        dma_x(xp, slot);
        dma_w(wp, slot);
        next();
        slot = (slot == (unsigned)((NS - 1) * SB)) ? 0u : slot + (unsigned)SB;
      };
      auto cstep = [&]() {
        bar();
        cissue();
      };
      wait_w<NDW>();
      MK(15);
      int j = 2;
      if (layer == 0) {
        const char* sp = uni_ptr((const char*)w_sp + (size_t)(bid * 4 + wl) * 8192 + WSP_V * 512);
        unsigned d = 0u;
#pragma unroll 1
        for (; j < 2 + 32 - 4 * WSP_V; ++j) {
          cstep();
          asm volatile("s_load_dword %0, %1, 0\n\ts_load_dword %0, %1, 64\n\ts_load_dword %0, %1, 0x1000\n\ts_load_dword %0, %1, 0x1040"
                       : "+s"(d)
                       : "s"(sp));
          sp += 128;
          wait_w<VPS>();
        }
#pragma unroll 1
        for (int e = j + 4; j < e; ++j) {
          cstep();
          asm volatile("" : "+s"(d));
          wait_w<VPS>();
        }
        asm volatile("s_waitcnt lgkmcnt(0)" : "+s"(d));
      }
      place<AL_L>();
#pragma unroll 1
      for (; j <= 29; ++j) {
        cstep();
        wait_w<VPS>();
      }
      bar();
    }
    auto w5piece = [&](unsigned off) {
      dma16_sc1(w5p, lane16 + (off >= 54u * 1024u ? off - 54u * 1024u : off + 90u * 1024u), lds0 + (unsigned)P5_W + off);
    };
#pragma unroll
    for (int i = 0; i < 15; ++i) {
      const int p = 4 * i + wl;
      if (i < 14 || wl == 0) w5piece((unsigned)((p < 34 ? 53 + p : 121 - 34 + p) * 1024));
    }
    wait_vm<14>();
    bar();
#pragma unroll
    for (int i = 0; i < 9; ++i)
      if (i < 8 || wl < 2) w5piece((unsigned)((87 + 4 * i + wl) * 1024));
    wait_vm<0>();
    bar();
    if (wl) {
#pragma unroll
      for (int i = 0; i < 18; ++i)
        if (i < 17 || wl < 3) w5piece((unsigned)((3 * i + wl - 1) * 1024));
    } else {
      cnt_wait(ws, CN_H + layer, rb * 8 + ct, HSPLIT ? 8u : 16u);
    }
    MK(20);
    bar();
    p5_steps(w5p, std::make_integer_sequence<int, 7>{});
    if (layer == 0) {
      if (bid < 24 && wl == 0 && lane < 8) pk_st16(ws + OFF_HELLO + bid * 128 + lane16, u32x4{0u, 0u, 0u, 0u});
    }
  }
};

__global__ __launch_bounds__(768, 3) void k_main(const u16* __restrict__ x0, const u16* __restrict__ wqkv, const u16* __restrict__ wo,
                                                 const u16* __restrict__ w_in, const u16* __restrict__ w_sp,
                                                 const u16* __restrict__ b_sp, const u16* __restrict__ w_out,
                                                 u16* __restrict__ out, char* ws) {
  extern __shared__ __attribute__((aligned(16))) char smem[];
  const char* code0 = code_here();
  (void)code0;
  const unsigned lds0 = (unsigned)(size_t)smem;
  int wave = __builtin_amdgcn_readfirstlane((int)threadIdx.x >> 6);
  int bid = blockIdx.x;
#define FRESH() asm volatile("" : "+s"(bid), "+s"(ws), "+s"(wave))
#define XCD (bid & 7)
#define IDX (bid >> 3)
#define MI ((XCD >> 2) * 8 + (IDX >> 2))
#define NI ((XCD & 1) * 8 + (XCD & 2) * 2 + (IDX & 3))
#define RB (XCD * 4 + (IDX >> 3))
#define CT (IDX & 7)
#define P1MAP 0
#define RT ((XCD >> 1) * 4 + (IDX >> 3))
#define HP ((XCD & 1) * 8 + (IDX & 7))
  MK(0);
  if (CT0 && wave == 2) {
    constexpr int PER = (2 * NL_MAIN + 31) / 32;
    const int lane = lane_id();
    int li = (CT0 == 2 ? lane * 32 + IDX : IDX * PER + (lane < PER ? lane : PER - 1));
    if (li > 2 * NL_MAIN - 1) li = 2 * NL_MAIN - 1;
    touch64(code0, (unsigned)li * 64u, lds0 + (unsigned)(K1R_DUMMY + 768));
  }
  if (wave >= 3 && wave < 8) {
    constexpr int ST = 64;
    const int k = (7 - wave) * 64 + lane_id();
    const bool isx = k < 9216 / ST;
    int o = isx ? bid * 9216 + k * ST : bid * 7776 + (k - 9216 / ST) * ST;
    if (!isx && k - 9216 / ST > 7775 / ST) o = bid * 7776;
    const char* p = (isx ? (const char*)x0 : (const char*)wqkv) + o;
    asm volatile("s_mov_b32 m0, %1\n\ts_nop 0\n\tglobal_load_lds_dword %0, off" ::"v"(p), "s"(lds0 + (unsigned)K1R_DUMMY) : "memory");
  }

  if (__builtin_expect(wave >= 8, 1)) {
    __builtin_amdgcn_s_setprio(3);
    {
      const int lane = lane_id();
      RawLd rl;
      const bool cross = (((HP * 72) & ~15) & 127) > 48;
      const int c16 = IDX >> 1;
      const int job = cross ? (c16 < 11 ? 128 + XCD * 11 + c16 : 999) : XCD * 16 + c16;
      const int pj = cross ? (c16 >= 11 ? XCD * 5 + c16 - 11 : -1) : (XCD < 4 && (c16 & 7) == 0 ? 40 + XCD * 2 + (c16 >> 3) : -1);
      rl.front(x0, wqkv, wo, ws, bid, lds0, wave - 8, lane, RT, HP, pj MF_PASS);
      if (job < 216) k1_raw_loader<true>(rl, w_in, w_out, ws, rl.tag, bid, lds0, wave - 8, lane, RT, HP, CT, job, pj MF_PASS);
      else k1_raw_loader<false>(rl, w_in, w_out, ws, rl.tag, bid, lds0, wave - 8, lane, RT, HP, CT, job, pj MF_PASS);
    }
    Loader ld;
    ld.lds0 = lds0;
#pragma unroll 1
    for (int layer = 0; layer < 2; ++layer) {
      FRESH();
      ld.wl = wave - 8;
      ld.lane = lane_id();
      ld.lane16 = (unsigned)ld.lane * 16u;
      const char* wip = uni_ptr(ws + OFF_PI + (size_t)NI * (18 * K3C::WB));
      if (layer) {
        ld.p1(smem, ws, bid, RT, HP MK_PASS);
        ld.pf_load<0, 21, 0, 81>(uni_ptr(ws + OFF_PO + (size_t)CT * (18 * 4608)));
        ld.pf_load<21, 9, 36, 72>(wip);
        wait_vm<0>();
        ld.pf_pin<0, 30>();
        bar();
        ld.pf_put<0, 0, 40>((unsigned)P2_W);
        ld.pf_put<0, 40, 41>((unsigned)(P2_W + 40 * 1024));
      } else {
        ld.pf_undef<21, 9>();
        MK(3);
        MK(4);
        bar();
        const char* wp = uni_ptr(ws + OFF_PO + (size_t)CT * (18 * 4608) + ld.wl * 1024);
#pragma unroll
        for (int i = 0; i < 21; ++i)
          if (i < 20 || ld.wl == 0) dma16_sc1(wp + i * 4096, ld.lane16, lds0 + (unsigned)(P2_W + i * 4096 + ld.wl * 1024));
      }
      if (layer == 0 && ld.wl == 0) cnt_wait(ws, CN_O + layer, RB * 8 + CT, 16u);
      MK(6);
      asm volatile("s_waitcnt vmcnt(0) lgkmcnt(0)" ::: "memory");
      bar();
      lds_spin(lds0 + (unsigned)(LW_OFF + (LW_T + layer) * 4), 12u);
      if (layer == 0) {
        if (ld.wl == 0) {
          cnt_wait(ws, CN_PK, 8 + NI, 36u);
          lds_wr32(lds0 + (unsigned)(LW_OFF + LW_HELLO * 4), 2u);
        } else {
          lds_spin(lds0 + (unsigned)(LW_OFF + LW_HELLO * 4), 2u);
        }
        MK(7);
      }
      static_assert(2 * K3C::SB <= P2_W, "free during the first residual GEMM");
      if (W01M < 2) ld.dma_run<18>(wip, (unsigned)K3C::XB);
      if (W01M < 1) ld.dma_run<18>(wip + K3C::WB, (unsigned)(K3C::SB + K3C::XB));
      if (L0REG && layer == 0) ld.pf_load<21, 9, 36, 72>(wip);
      if (layer == 0 && ld.wl == 0) cnt_wait(ws, CN_PK, 24 + CT, 72u);
      wait_vm<0>();
      ld.pf_pin<21, 9>();
      bar();
      FRESH();
      ld.wl = wave - 8;
      ld.p35(smem, ws, w_sp, layer, bid, MI, NI, RB, CT, HP MK_PASS);
    }
  } else {
    MF(12);
    math_start(smem, code0, b_sp, wqkv, ws, bid, wave, lane_id());
    MF(13);
#pragma unroll 1
    for (int layer = 0; layer < 2; ++layer) {
      FRESH();
      {
        const int lane = lane_id(), r0 = RT * 128;
        float* csq = reinterpret_cast<float*>(ws + OFF_CSQ + bid * 512);
        Attn at;
        at.setup(lds0 + (unsigned)(layer ? K1_T_OFF : K1R_T_OFF), ws + OFF_O + (size_t)layer * SZ_X, r0, HP, wave, lane);
        if (layer) {
          if ((wave & 2) == 0) k1_gemm<2, 4>(smem, ws, csq, RT, HP, (wave & 1) * 2 + (wave & 4), 0, wave >= 4, lane MK_PASS);
          else if (wave < 4) k1_gemm<3, 3>(smem, ws, csq, RT, HP, (wave & 1) * 3, 4, false, lane MK_PASS);
          else if (wave == 6) k1_gemm<2, 3>(smem, ws, csq, RT, HP, 6, 4, true, lane MK_PASS);
          else k1_gemm<1, 7>(smem, ws, csq, RT, HP, 8, 0, true, lane MK_PASS);
        } else {
          if (wave == 0) k1_gemm_raw<3, false>(smem, HP, 0, lane MF_PASS);
          else if (wave < 4) k1_gemm_raw<2, false>(smem, HP, 1 + 2 * wave, lane MF_PASS);
          else if (wave < 7) k1_gemm_raw<2, true>(smem, HP, 2 * (wave - 4), lane MF_PASS);
          else k1_gemm_raw<3, true>(smem, HP, 6, lane MF_PASS);
        }
        msync(lds0 + (unsigned)(LW_OFF + (LW_T + layer) * 4));
        MK(3);
        at.run(r0, HP, wave, lane);
        if (layer == 0) {
          if (wave == 0 && lane < 32)
            asm volatile("global_store_dwordx4 %0, %1, %2 sc1\n\ts_nop 1" ::"v"((unsigned)lane * 16u),
                         "v"(*reinterpret_cast<const u32x4*>(smem + K1R_CS_OFF + lane * 16)), "s"(uni_ptr((const char*)csq))
                         : "memory");
          lds_spin(lds0 + (unsigned)(LW_OFF + LW_HELLO * 4), 1u);
        }
        MK(4);
        announce(lds0 + (unsigned)(LW_OFF + (LW_O + layer * 22 + (wave >> 2)) * 4), 4u, ws, CN_O + layer, (RT * 2 + (wave >> 2)) * 8 + lane,
                 lane < 8);
        MK(5);
        bar();
      }
      FRESH();
      {
        if (wave < 4) {
          const int lane = lane_id();
          u32x2 xv[5];
          ResIn ri;
          ri.setup(layer ? ws + OFF_X1 + SZ_X : (const char*)x0, layer == 0, RB, CT, wave, lane);
          ri.load(xv);
          ResOut ro;
          ro.setup(ws + OFF_X1 + (size_t)layer * 2 * SZ_X, ws + OFF_XS + (size_t)layer * 2 * SZ_XS, out, false, RB, CT, wave, lane);
          f32x4 acc[2][5];
          p2_gemm(acc, xv, lds0, uni_ptr(ws + OFF_O + (size_t)layer * SZ_X + (size_t)RB * (72 * 1024)), layer, wave, lane MK_PASS);
          MK(7);
          ro.run(acc[0], false);
          MK(8);
          announce(lds0 + (unsigned)(LW_OFF + (LW_X1 + layer) * 4), 4u, ws, CN_X1 + layer, (RB >> 1) * 16 + lane, lane < 16);
          MK(9);
          bar();
          FRESH();
          if (wave == 3) cnt_wait(ws, CN_X1 + layer, MI * 16 + NI, 16u);
          bar();
          MK(10);
          p3_g0(smem, ws, layer, MI, NI, wave, lane_id() MK_PASS);
        } else {
          if (layer && wave == 5) cnt_wait(ws, CN_O + layer, RB * 8 + CT, 16u);
          bar();
          bar();
          k3_const(smem, ws, b_sp, layer, bid, MI, NI, wave & 3);
          bar();
          MK(10);
          FRESH();
          p3_g1(smem, ws, code0, bid, layer, MI, wave & 3, lane_id() MK_PASS);
        }
      }
      FRESH();
      {
        const int lane = lane_id();
        if (wave < 4) {
          u32x2 xv[5];
          ResIn ri;
          ri.setup(ws + OFF_X1 + (size_t)layer * 2 * SZ_X, false, RB, CT, wave, lane);
          f32x4 acc[2][5];
          ResOut ro;
          ro.setup(ws + OFF_X1 + SZ_X, ws + OFF_XS + SZ_XS, out, layer != 0, RB, CT, wave, lane);
          ResX<36, 33, 8, P5_W, P5_XW, P5_X0, 3, P5_BAR> gm(acc);
          gm.setup(lds0, uni_ptr(ws + OFF_H + (size_t)layer * SZ_Z + (size_t)RB * (288 * 1024)), wave, lane, (unsigned)P5_RED, layer + 1u);
          announce(lds0 + (unsigned)(LW_OFF + (LW_H + layer * 2 + (wave >> 1)) * 4), 2u, ws, ((HSPLIT && NI >= 8) ? CN_H2 : CN_H) + layer,
                   (MI * 2 + (wave >> 1)) * 8 + lane, lane < 8);
          MK(19);
          if (XVE) ri.load(xv);
          bar();
          if (!XVE) ri.load(xv);
          gm.prow();
          place<AL_P5>();
          bar();
          MK(20);
          gm.prox<0>(std::make_integer_sequence<int, P5_X0>{});
          gm.init<2 * P5_X0>(xv);
          __builtin_amdgcn_sched_barrier(0);
          gm.run();
          MK(21);
          ro.run(acc[0], layer != 0);
          MK(22);
          if (layer == 0) {
            announce(lds0 + (unsigned)(LW_OFF + LW_X2 * 4), 4u, ws, CN_X2, (RB >> 1) * 16 + lane,
                     lane < 16 || (lane < 32 && (RB & 1) && RB < 31));
            MK(23);
          }
        } else {
          bar();
          u32x4 wq[15];
          const int wm = wave & 3;
          const unsigned o = (unsigned)((wm >> 1) * 18432 + 12288 + (wm & 1) * 3072 + lane * 16);
          if (G1W) {
            const char* wp = uni_ptr(ws + OFF_PW + (size_t)CT * (36 * 9216) + 8 * 18432);
#pragma unroll
            for (int i = 0; i < 15; ++i)
              asm volatile("global_load_dwordx4 %0, %1, %2 offset:%3 sc1" : "=v"(wq[i]) : "v"(o), "s"(wp + (i / 3) * 36864), "n"((i % 3) * 1024) : "memory");
          }
          bar();
#pragma unroll
          for (int i = 0; i < 7; ++i) {
            if (G1W && i == 0) asm volatile("s_waitcnt vmcnt(0)" ::: "memory");
            if (HSPLIT && i == HPOLL && wave == 5) {
              if (HPOLL < 2) {
                unsigned f;
                const char* cp = uni_ptr(ws + OFF_CNT + (size_t)((CN_H2 + layer) * 256 + RB * 8 + CT) * 128);
                asm volatile("global_load_dword %0, %1, %2 sc1" : "=v"(f) : "v"(vconst(0u)), "s"(cp) : "memory");
                bar();
                asm volatile("s_waitcnt vmcnt(0)" : "+v"(f) : : "memory");
                if (__builtin_amdgcn_readfirstlane(f) != 7u) cnt_wait(ws, CN_H2 + layer, RB * 8 + CT, 8u);
                continue;
              }
              cnt_wait(ws, CN_H2 + layer, RB * 8 + CT, 8u);
            }
            asm volatile("s_waitcnt lgkmcnt(0)" ::: "memory");
            bar();
            if (G1W && i < 5) {
              const unsigned a = lds0 + o + (unsigned)(P5_W + ((2 * i + 3) % 8) * 18432) - ((i == 2 && wm >= 2) ? 8u * 18432u : 0u);
#pragma unroll
              for (int k = 0; k < 3; ++k) {
                if (P5PACE) __builtin_amdgcn_s_sleep(2 * P5PACE);
                asm volatile("ds_write_b128 %0, %1 offset:%2" ::"v"(a), "v"(wq[3 * i + k]), "n"(k * 1024) : "memory");
              }
            }
          }
          MK(22);
        }
        if (layer == 0 && (!G1E || wave < 4)) bar();
      }
    }
  }
}

}

extern "C" void kernel_launch(void* const* d_in, const int* in_sizes, int n_in, void* d_out, int out_size,
                              void* d_ws, size_t ws_size, hipStream_t stream) {
  const u16* x0 = (const u16*)d_in[0];
  const u16* wqkv = (const u16*)d_in[1];
  const u16* wo = (const u16*)d_in[2];
  const u16* w_in = (const u16*)d_in[4];
  const u16* w_sp = (const u16*)d_in[5];
  const u16* b_sp = (const u16*)d_in[6];
  const u16* w_out = (const u16*)d_in[7];
  char* ws = (char*)d_ws;
  (void)hipFuncSetAttribute((const void*)k_main, hipFuncAttributeMaxDynamicSharedMemorySize, LDS_ALL);
  k_main<<<256, 768, LDS_ALL, stream>>>(x0, wqkv, wo, w_in, w_sp, b_sp, w_out, (u16*)d_out, ws);
}
```

```cpp
#include <hip/hip_runtime.h>
#include <hip/hip_bf16.h>
#include <stdint.h>

#define DEVI __device__ __forceinline__

typedef unsigned short u16;
typedef __attribute__((ext_vector_type(8))) short s16x8;
typedef __attribute__((ext_vector_type(4))) short s16x4;
typedef __attribute__((ext_vector_type(4))) float f32x4;
typedef __attribute__((ext_vector_type(2))) float f32x2;
typedef __attribute__((ext_vector_type(2))) __bf16 bf16x2;
typedef __attribute__((ext_vector_type(2))) unsigned u32x2;
typedef __attribute__((ext_vector_type(4))) unsigned u32x4;

#define MK(I) ((void)0)
#define MX(S, I) ((void)0)
#define MF(I) ((void)0)
#define MF_ARG
#define MF_PASS
#define MK_ARG
#define MK_PASS

#define SPIN_LDS for (;;)
#define SPIN_MEM (1u << 22)

#define G1W 0
#define P5PACE 0
#define L0REG 0
#define RTC 0
#define P1PR 3
#define HSPLIT 1
#define HPOLL 1
#define XA4 1
#define P5ORD 0
#define CT0 1
#define AL_A 0
#define AL_B 0
#define AL_C 0
#define AL_P5 0
#define AL_P2 0
#define AL_G -1
#define AL_K 0
#define AL_L 0
#define CSL 1
#define NOPL 0
#define G1E 1
#define XVE 0
#define ZSL 11
#define NR1 3
#define P2_X0 4
#define W01M 1
#define BEARLY 0
#define G1SL 40
#define SAFE_OUT(L)
#define NOP_(n) "=s"(n)
#define JMAP 1
#define JORD 0x64753210u
#define L0W 0
#define EPUT 1
#define EPSL 4
#define FLOOP 1
#define MPOLL 1
namespace {

constexpr int S_ = 2048, D_ = 576, F_ = 4608, FH_ = 2304;
constexpr int NQKV = 3 * D_;

constexpr size_t SZ_X = (size_t)S_ * D_ * 2;
constexpr size_t SZ_Z = (size_t)S_ * FH_ * 2;
constexpr size_t OFF_X1 = 0;
constexpr size_t OFF_O = OFF_X1 + 3 * SZ_X;
constexpr size_t OFF_Z2 = OFF_O + 2 * SZ_X;
constexpr size_t OFF_H = OFF_Z2 + 2 * SZ_Z;
constexpr size_t SZ_XS = (size_t)S_ * 8 * 8;
constexpr size_t OFF_XS = OFF_H + 2 * SZ_Z;
constexpr size_t OFF_CSQ = OFF_XS + 3 * SZ_XS;
constexpr size_t OFF_ZP = OFF_CSQ + 256 * 512;
constexpr size_t SZ_ZP = (size_t)16 * 16 * 1024;
constexpr size_t OFF_PQ = OFF_ZP + 2 * SZ_ZP;
constexpr size_t OFF_PO = OFF_PQ + (size_t)16 * 9 * 64 * 112 * 2;
constexpr size_t OFF_PI = OFF_PO + (size_t)D_ * D_ * 2;
constexpr size_t OFF_PW = OFF_PI + (size_t)32 * 9 * 64 * 144 * 2;
constexpr size_t OFF_CSP = OFF_PW + (size_t)FH_ * D_ * 2;
constexpr size_t OFF_CNT = OFF_CSP + (size_t)18 * F_ * 4;
constexpr int CN_X1 = 0, CN_H = 2, CN_X2 = 4, CN_O = 5, CN_Z = 7, CN_PK = 9, CN_H2 = 10, NCNT = 12;
constexpr size_t OFF_TAG = OFF_CNT + (size_t)NCNT * 256 * 128;
constexpr size_t OFF_HELLO = OFF_TAG + 128;
constexpr size_t OFF_POT = OFF_HELLO + 2048;
constexpr size_t OFF_CSK = OFF_POT + 1024;
constexpr size_t WS_END = OFF_CSK + 256 * 2048;

DEVI float bf_lo(unsigned u) { return __uint_as_float(u << 16); }
DEVI float bf_hi(unsigned u) { return __uint_as_float(u & 0xffff0000u); }
DEVI unsigned pack2(float a, float b) {
  f32x2 v = {a, b};
  bf16x2 r = __builtin_convertvector(v, bf16x2);
  return __builtin_bit_cast(unsigned, r);
}
DEVI float rbf(float a) {
  return bf_lo(pack2(a, 0.f));
}

DEVI float sum_g(float x) {
  const unsigned u = __float_as_uint(x);
  const auto p = __builtin_amdgcn_permlane32_swap(u, u, false, false);
  const unsigned v = __float_as_uint(__uint_as_float(p[0]) + __uint_as_float(p[1]));
  const auto q = __builtin_amdgcn_permlane16_swap(v, v, false, false);
  return __uint_as_float(q[0]) + __uint_as_float(q[1]);
}

DEVI unsigned vconst(unsigned c) {
  unsigned r;
  asm volatile("v_mov_b32 %0, %1" : "=v"(r) : "s"(c));
  return r;
}
template <int K>
DEVI void place() {
  if constexpr (K >= 0) {
    __builtin_amdgcn_sched_barrier(0);
    asm volatile(".p2align 6\n\t.fill %0, 4, 0xbf800000" ::"n"(K));
    __builtin_amdgcn_sched_barrier(0);
  }
}
DEVI const char* uni_ptr(const char* p) {
  unsigned long long v = (unsigned long long)p;
  unsigned lo = __builtin_amdgcn_readfirstlane((unsigned)v), hi = __builtin_amdgcn_readfirstlane((unsigned)(v >> 32));
  asm("s_nop 4" : "+s"(lo), "+s"(hi));
  return (const char*)(((unsigned long long)hi << 32) | lo);
}

DEVI void dma16(const char* sbase, unsigned voff, unsigned m0val) {
  asm volatile("s_mov_b32 m0, %2\n\ts_nop 0\n\tglobal_load_lds_dwordx4 %0, %1"
               :
               : "v"(voff), "s"(sbase), "s"(m0val)
               : "memory");
}

DEVI void ln_finish(float s, float q, float invn, float& mb, float& rinv) {
  float mean = s * invn;
  asm("" : "+v"(mean));
  float var = fmaxf(__builtin_fmaf(-mean, mean, q * invn), 0.f);
  mb = rbf(mean);
  float v = rbf(var);
  float t = rbf(v + 1e-5f);
  float sd = rbf(__builtin_amdgcn_sqrtf(t));
  rinv = __builtin_amdgcn_rcpf(sd);
}

DEVI unsigned pack2v(f32x2 v) { return pack2(v[0], v[1]); }

template <int BM_, int BN_>
struct GemmCfg {
  static constexpr int BM = BM_, BN = BN_, NS = 4;
  static constexpr int P = BN * 2;
  static constexpr int XB = BM * 128, WB = 64 * P, SB = XB + WB;
  static constexpr int LDS_BYTES = NS * SB;
};

DEVI int swap23(int r) { return (r & ~12) | ((r & 4) << 1) | ((r & 8) >> 1); }

DEVI unsigned lds_rd32(unsigned addr) {
  unsigned v;
  asm volatile("ds_read_b32 %0, %1\n\ts_waitcnt lgkmcnt(0)" : "=v"(v) : "v"(addr) : "memory");
  return v;
}
DEVI void lds_wr32(unsigned addr, unsigned v) { asm volatile("ds_write_b32 %0, %1" ::"v"(addr), "v"(v) : "memory"); }

template <class C, int MT, int NT, int D, int R, bool XCM, int WT, bool SQ = false, unsigned long long WS = 0>
struct GemmMath {
  static constexpr int WO = WT ? 1 : 2;
  static constexpr int NS = C::NS, SB = C::SB, P = C::P, NU = 2 * NT;
  static constexpr int XPU = MT <= 2 ? MT : MT - 2;
  static constexpr int XU = (MT + XPU - 1) / XPU;
  static_assert(NU % R == 0 && R > D && D <= NT && XU <= D, "shape");
  s16x4 wlo[R], whi[R];
  s16x8 wf[R];
  s16x8 xf[2][MT];
  unsigned xrd[2], wrd, xa[2], wa, so;
  f32x4 (&acc)[MT][NT];
  f32x4 accq[SQ ? MT : 1];
  unsigned long long wtab = WS;
  DEVI GemmMath(f32x4 (&a)[MT][NT]) : acc(a) {}

  template <int PU>
  DEVI void wread_lo() {
    constexpr int off = (PU / NT) * 32 * P + (PU % NT) * (WT == 2 ? 1024 : 32);
    if constexpr (WT) asm volatile("ds_read_b128 %0, %1 offset:%2" : "=v"(wf[PU % R]) : "v"(wa), "n"(off));
    else asm volatile("ds_read_b64_tr_b16 %0, %1 offset:%2" : "=v"(wlo[PU % R]) : "v"(wa), "n"(off));
  }
  template <int PU>
  DEVI void wread_hi() {
    if constexpr (!WT)
      asm volatile("ds_read_b64_tr_b16 %0, %1 offset:%2" : "=v"(whi[PU % R]) : "v"(wa), "n"((PU / NT) * 32 * P + (PU % NT) * 32 + 8 * P));
  }
  template <int H, int M>
  DEVI void xread1() {
    asm volatile("ds_read_b128 %0, %1 offset:%2" : "=v"(xf[H][M]) : "v"(xa[H]), "n"(M * (XCM ? 256 : 2048)));
  }
  static constexpr int xops(int v) {
    const int k = (v + D) % NT;
    if (k >= XU) return 0;
    const int n = MT - k * XPU;
    return n < XPU ? n : XPU;
  }
  static constexpr int ops(int v, bool last) { return (last && v + D >= NU) ? 0 : WO + xops(v); }
  static constexpr int newer(int U, bool last) {
    int n = 0;
    int v0 = U - D;
    if (U % NT == 0) v0 += XU - 1;
    else n += v0 >= 0 ? xops(v0) : xops(v0 + NU);
    for (int v = v0 + 1; v < U; ++v) n += v >= 0 ? ops(v, last) : ops(v + NU, false);
    return n;
  }
  template <int U, int M_>
  DEVI void mf() {
    constexpr int M = ((U % NT) & 1) ? MT - 1 - M_ : M_;
    s16x8 w;
    if constexpr (WT) w = wf[U % R];
    else w = __builtin_shufflevector(wlo[U % R], whi[U % R], 0, 1, 2, 3, 4, 5, 6, 7);
    acc[M][U % NT] = __builtin_amdgcn_mfma_f32_16x16x32_bf16(w, xf[U / NT][M], acc[M][U % NT], 0, 0, 0);
    __builtin_amdgcn_sched_barrier(0);
  }
  DEVI void pin() {
#pragma unroll
    for (int m = 0; m < MT; ++m)
#pragma unroll
      for (int n = 0; n < NT; ++n) asm volatile("" : "+v"(acc[m][n]));
  }
  template <int U, int I>
  DEVI void dsop() {
    constexpr int PU = (U + D) % NU;
    if constexpr (I == 0) wread_lo<PU>();
    else if constexpr (I == 1) wread_hi<PU>();
    else if constexpr (I - 2 < xops(U)) {
      constexpr int k = (U + D) % NT;
      xread1<((U + D - k) % NU) / NT, k * XPU + I - 2>();
    }
  }
  template <int U, bool LAST, bool BAR, int M>
  DEVI void slot() {
    constexpr bool PF = !(LAST && U + D >= NU);
    mf<U, M>();
    if constexpr (M == 0 && PF && U + D == NU) {
      if constexpr (BAR) __builtin_amdgcn_s_barrier();
      so = (so == (unsigned)((NS - 1) * SB)) ? 0u : so + (unsigned)SB;
      wa = wrd + (WS ? ((unsigned)wtab & 63u) << 11 : so);
      wtab >>= 6;
      xa[0] = xrd[0] + so;
      xa[1] = xrd[1] + so;
      __builtin_amdgcn_sched_barrier(0);
    }
    if constexpr (PF) {
      dsop<U, M>();
      if constexpr (M == MT - 1) {
        if constexpr (MT <= 1) dsop<U, 1>();
        if constexpr (MT <= 2) {
          dsop<U, 2>();
          dsop<U, 3>();
        }
      }
      __builtin_amdgcn_sched_barrier(0);
    }
  }
  template <int U, bool LAST, bool BAR, int... M>
  DEVI void unit(std::integer_sequence<int, M...>) {
    static_assert(newer(U, LAST) <= 15, "lgkmcnt range");
    asm volatile("s_waitcnt lgkmcnt(%0)" ::"n"(newer(U, LAST)));
    __builtin_amdgcn_sched_barrier(0);
    (slot<U, LAST, BAR, M>(), ...);
    if constexpr (SQ && (U % NT) < MT) {
      constexpr int m = U % NT;
      accq[m] = __builtin_amdgcn_mfma_f32_16x16x32_bf16(xf[U / NT][m], xf[U / NT][m], accq[m], 0, 0, 0);
      __builtin_amdgcn_sched_barrier(0);
    }
    __builtin_amdgcn_sched_barrier(0);
  }
  template <bool LAST, bool BAR, int... U>
  DEVI void stage(std::integer_sequence<int, U...>) {
    (unit<U, LAST, BAR>(std::make_integer_sequence<int, MT>{}), ...);
  }
  template <int... U>
  DEVI void prow(std::integer_sequence<int, U...>) {
    ((wread_lo<U>(), wread_hi<U>()), ...);
  }
  template <int... M>
  DEVI void prox(std::integer_sequence<int, M...>) {
    (xread1<0, M>(), ...);
  }
  DEVI void setup(unsigned lds0, int mt0, int nt0, int lane, unsigned so0) {
    const int g = lane >> 4, r = lane & 15, q = (lane >> 2) & 3, p = lane & 3;
#pragma unroll
    for (int h = 0; h < 2; ++h)
      xrd[h] = XCM ? lds0 + (unsigned)((4 * h + g) * (C::BM * 16) + (mt0 * 16 + r) * 16)
                   : lds0 + (unsigned)((mt0 * 16 + r) * 128 + (((4 * h + g) ^ ((r >> 1) & 7)) * 16));
    wrd = WT == 2   ? lds0 + (unsigned)(C::XB + nt0 * 1024 + lane * 16)
          : WT == 1 ? lds0 + (unsigned)(C::XB + (g * 8 + (r & 7)) * P + (r >> 3) * 16 + nt0 * 32)
                    : lds0 + (unsigned)(C::XB + (16 * (g >> 1) + 4 * (g & 1) + q) * P + 8 * p + nt0 * 32);
    so = so0;
    xa[0] = xrd[0] + so;
    xa[1] = xrd[1] + so;
    wa = wrd + so;
  }
  DEVI void first() {
    prox(std::make_integer_sequence<int, MT>{});
    prow(std::make_integer_sequence<int, D>{});
    asm volatile("s_waitcnt lgkmcnt(0)");
  }
  template <int N, bool BAR = true, int AL = -1>
  DEVI void mid() {
    place<AL>();
#pragma unroll 1
    for (int s = 0; s < N; ++s) stage<false, BAR>(std::make_integer_sequence<int, NU>{});
  }
  DEVI void last() {
    stage<true, true>(std::make_integer_sequence<int, NU>{});
    pin();
  }
  template <int KS>
  DEVI void run(unsigned lds0, int mt0, int nt0, int lane, unsigned so0) {
    setup(lds0, mt0, nt0, lane, so0);
    __builtin_amdgcn_s_barrier();
    first();
    mid<KS - 1, true, AL_K>();
    last();
  }
};

DEVI const char* code_here() {
  unsigned long long pc;
  asm volatile("s_getpc_b64 %0" : "=s"(pc));
  return (const char*)pc;
}
DEVI int okmap(int p) {
  const int ch = p >> 3, e = p & 7, c = (ch - 8) & 3;
  return ch < 8 ? (2 * ch + (e >> 2)) * 36 + 16 + (e & 3) : ((ch - 8) >> 2) * 36 + c * 8 + (c >> 1) * 4 + e;
}

DEVI void pk_st16(void* p, u32x4 v) {
  asm volatile("global_store_dwordx4 %0, %1, off sc1\n\ts_nop 1" ::"v"(p), "v"(v) : "memory");
}

using K1C = GemmCfg<144, 112>;
constexpr int K1_TP = 240;
constexpr int K1_T_OFF = K1C::SB;
constexpr int K1_ST_OFF = 163840 - 128 - 1152;
constexpr int K1_CS_OFF = 4 * K1C::SB;
constexpr int K1_WX = 4 * K1C::SB + 2048;
constexpr unsigned long long K1_WS = 16ull | 32ull << 6 | 48ull << 12 | 56ull << 18 | 63ull << 24 | 16ull << 30 | 32ull << 36 | 48ull << 42;
static_assert(K1C::SB == 16 * 2048 && K1_WX - K1C::XB == 56 * 2048 && K1C::WB == 7 * 2048 && K1_WX + 2 * K1C::WB <= K1_ST_OFF, "K1 w places");
static_assert(K1_T_OFF + 144 * K1_TP + 64 <= 2 * K1C::SB + K1C::XB, "K1 LDS alias");
using K1R = GemmCfg<144, 144>;
constexpr int K1R_T_OFF = K1R::SB;
constexpr int K1R_CS_OFF = K1R_T_OFF + 144 * K1_TP;
constexpr int K1R_ST_OFF = K1R_CS_OFF + 512;
constexpr int K1R_DUMMY = K1R::LDS_BYTES;
constexpr int K1R_PO = K1R_DUMMY + 1024;
constexpr int K1R_CNT = 163840 - 128 + 28 * 4;
#define SAFE_AND
#define SPIN_ASM(TEST)                                                                                                   \
  if constexpr (SL <= 1)                                                                                                 \
    asm volatile(".La%=:\n\tds_read_b32 %0, %3\n\ts_waitcnt lgkmcnt(0)\n\tv_readfirstlane_b32 %2, %0\n\t" TEST SAFE_AND \
                 "s_cbranch_scc1 .La%="                                                                                  \
                 : "=&v"(v), NOP_(c), "=&s"(t)                                                                           \
                 : "v"(addr), "s"(k)                                                                                     \
                 : "memory", "scc");                                                                                     \
  else                                                                                                                   \
    asm volatile("s_branch .Lb%=\n.La%=:\n\ts_sleep %5\n.Lb%=:\n\tds_read_b32 %0, %3\n\ts_waitcnt lgkmcnt(0)\n\t"     \
                 "v_readfirstlane_b32 %2, %0\n\t" TEST SAFE_AND "s_cbranch_scc1 .La%="                                  \
                 : "=&v"(v), NOP_(c), "=&s"(t)                                                                           \
                 : "v"(addr), "s"(k), "n"(SL)                                                                            \
                 : "memory", "scc")
template <int SL>
DEVI void spin_ge(unsigned addr, unsigned k) {
  unsigned v, c = 1u << 18, t;
  SPIN_ASM("s_cmp_lt_u32 %2, %4\n\t");
}
template <int SL>
DEVI void spin_b4(unsigned addr, unsigned n) {
  unsigned v, c = 1u << 18, t;
  const unsigned k = (0x80u - n) * 0x01010101u;
  SPIN_ASM("s_add_u32 %2, %2, %4\n\ts_andn2_b32 %2, 0x80808080, %2\n\t");
}
DEVI void msync(unsigned addr) {
  asm volatile("s_waitcnt lgkmcnt(0)" ::: "memory");
  asm volatile("s_mov_b64 exec, 1\n\tds_add_u32 %0, %1\n\ts_mov_b64 exec, -1" ::"v"(addr), "v"(vconst(1u)) : "memory");
  spin_ge<0>(addr, 8u);
}
static_assert(K1R_PO + 13824 + 256 <= 163840 - 128 && K1R_ST_OFF + 144 * 8 <= 3 * K1R::SB, "K1R LDS");

DEVI void touch64(const char* sbase, unsigned voff, unsigned m0val) {
  asm volatile("s_mov_b32 m0, %2\n\ts_nop 0\n\tglobal_load_lds_dword %0, %1" : : "v"(voff), "s"(sbase), "s"(m0val) : "memory");
}

template <int MT, bool G1>
DEVI void k1_gemm_raw(char* smem, int hp, int mt0, int lane MF_ARG) {
  constexpr int NT = 4, nt0 = G1 ? 4 : 0;
  const unsigned lds0 = (unsigned)(size_t)smem;
  const int g = lane >> 4, r = lane & 15;
  unsigned ta[NT];
#pragma unroll
  for (int nt = 0; nt < NT; ++nt) {
    const int j = (nt0 + nt) * 16 + g * 4, seg = j / 40, cj = j - seg * 40 - (hp & 1) * 4;
    const int to = (seg < 3 && cj >= 0 && cj < 36) ? seg * 80 + cj * 2 + (cj >= 20 ? 8 : 0) : (seg < 3 ? 40 : 120);
    ta[nt] = lds0 + (unsigned)(K1R_T_OFF + (mt0 * 16 + r) * K1_TP + to);
    asm volatile("" : "+v"(ta[nt]));
  }
  unsigned sa = lds0 + (unsigned)(K1R_ST_OFF + (mt0 * 16 + r) * 8), ca = lds0 + (unsigned)(K1R_CS_OFF + (nt0 * 16 + g * 4) * 4);
  asm volatile("" : "+v"(sa), "+v"(ca));
  f32x4 acc[MT][NT];
#pragma unroll
  for (int mt = 0; mt < MT; ++mt)
#pragma unroll
    for (int nt = 0; nt < NT; ++nt) acc[mt][nt] = f32x4{0.f, 0.f, 0.f, 0.f};
  {
    GemmMath<K1R, MT, 4, 3, 4, false, 0, G1> gm(acc);
    if constexpr (G1) {
#pragma unroll
      for (int mt = 0; mt < MT; ++mt) gm.accq[mt] = f32x4{0.f, 0.f, 0.f, 0.f};
    }
    MF(14);
    if constexpr (!G1) {
      if (mt0 < 7) asm volatile("s_waitcnt vmcnt(0)" ::: "memory");
    }
    MF(15);
    gm.template run<9>(lds0, mt0, nt0, lane, 0u);
    MF(2);
    if constexpr (G1) {
#pragma unroll
      for (int mt = 0; mt < MT; ++mt) {
        const f32x4 q = gm.accq[mt];
        const float d = (r & 2) ? ((r & 1) ? q[3] : q[2]) : ((r & 1) ? q[1] : q[0]);
        const float qq = sum_g(g == (r >> 2) ? d : 0.f);
        float mb, rinv;
        ln_finish(acc[mt][3][0], qq, 1.0f / (float)D_, mb, rinv);
        const f32x2 st = {-mb, rinv};
        if (g == 2) asm volatile("ds_write_b64 %0, %1 offset:%2" ::"v"(sa), "v"(st), "n"(mt * 128) : "memory");
      }
    }
  }
  if (mt0 == 0 && r == 0) {
#pragma unroll
    for (int nt = 0; nt < NT; ++nt) *reinterpret_cast<f32x4*>(smem + K1R_CS_OFF + ((nt0 + nt) * 16 + g * 4) * 4) = acc[0][nt];
  }
  msync(lds0 + (unsigned)K1R_CNT);
  MF(3);
  f32x2 st[MT];
  f32x4 cs[NT];
#pragma unroll
  for (int nt = 0; nt < NT; ++nt) asm volatile("ds_read_b128 %0, %1 offset:%2" : "=v"(cs[nt]) : "v"(ca), "n"(nt * 64) : "memory");
#pragma unroll
  for (int mt = 0; mt < MT; ++mt) asm volatile("ds_read_b64 %0, %1 offset:%2" : "=v"(st[mt]) : "v"(sa), "n"(mt * 128) : "memory");
#pragma unroll
  for (int i = 0; i < MT; ++i) asm volatile("s_waitcnt lgkmcnt(0)" : "+v"(st[i]));
#pragma unroll
  for (int i = 0; i < NT; ++i) asm volatile("" : "+v"(cs[i]));
#pragma unroll
  for (int mt = 0; mt < MT; ++mt) {
    f32x2 M = {st[mt][0], st[mt][0]}, R = {st[mt][1], st[mt][1]};
    asm volatile("" : "+v"(M), "+v"(R));
#pragma unroll
    for (int nt = 0; nt < NT; ++nt) {
      const f32x2 v0 = R * __builtin_elementwise_fma(M, f32x2{cs[nt][0], cs[nt][1]}, f32x2{acc[mt][nt][0], acc[mt][nt][1]});
      const f32x2 v1 = R * __builtin_elementwise_fma(M, f32x2{cs[nt][2], cs[nt][3]}, f32x2{acc[mt][nt][2], acc[mt][nt][3]});
      acc[mt][nt][0] = __uint_as_float(pack2v(v0));
      acc[mt][nt][1] = __uint_as_float(pack2v(v1));
      const f32x2 pk = {acc[mt][nt][0], acc[mt][nt][1]};
      asm volatile("ds_write_b64 %0, %1 offset:%2" ::"v"(ta[nt]), "v"(pk), "n"(mt * 16 * K1_TP) : "memory");
    }
  }
}

DEVI void dma16m(const char* sbase, unsigned voff, unsigned m0val, unsigned long long mask) {
  asm volatile("s_mov_b32 m0, %2\n\ts_mov_b64 exec, %3\n\tglobal_load_lds_dwordx4 %0, %1\n\ts_mov_b64 exec, -1"
               :
               : "v"(voff), "s"(sbase), "s"(m0val), "s"(mask)
               : "memory");
}
template <int N>
DEVI void wait_vm() {
  asm volatile("s_waitcnt vmcnt(%0)" ::"n"(N) : "memory");
}

constexpr int LDS_ALL = 163840;
constexpr int LW_OFF = LDS_ALL - 128;
constexpr int LW_GO = 0, LW_T = 2, LW_O = 4  , LW_X1 = 6, LW_H = 8, LW_X2 = 12, LW_PK = 13, LW_Z = 14, LW_PAIR = 16, LW_AD = 20,
              LW_L = 22, LW_S = 24, LW_ST = 29, LW_HELLO = 30, LW_Q = 31;
DEVI void announce(unsigned addr, unsigned n, char* ws, int kind, int who, bool act) {
  const char* cb = uni_ptr(ws + OFF_CNT + (size_t)kind * 256 * 128);
  const unsigned long long mask = __ballot(act);
  unsigned t, u;
  asm volatile(
      "s_waitcnt vmcnt(0)\n\t"
      "s_mov_b64 exec, 1\n\t"
      "ds_add_rtn_u32 %0, %2, %3\n\t"
      "s_waitcnt lgkmcnt(0)\n\t"
      "v_readfirstlane_b32 %1, %0\n\t"
      "s_cmp_eq_u32 %1, %4\n\t"
      "s_cselect_b64 exec, %5, 0\n\t"
      "global_atomic_add %6, %3, %7 sc1\n\t"
      "s_mov_b64 exec, -1\n\t"
      "s_nop 0"
      : "=&v"(t), "=&s"(u)
      : "v"(addr), "v"(vconst(1u)), "s"(n - 1u), "s"(mask), "v"((unsigned)(who & 255) * 128u), "s"(cb)
      : "memory", "scc");
}
DEVI void cnt_wait(const char* ws, int kind, int who, unsigned n) {
  const char* cp = uni_ptr(ws + OFF_CNT + (size_t)(kind * 256 + who) * 128);
  unsigned v, c = SPIN_MEM, t;
  asm volatile(
      ".La%=:\n\tglobal_load_dword %0, %3, %4 sc1\n\ts_sub_u32 %1, %1, 1\n\ts_waitcnt vmcnt(0)\n\tv_readfirstlane_b32 %2, %0\n\ts_cmp_lg_u32 %2, %5\n\t"
      "s_cselect_b32 %2, %1, 0\n\ts_cmp_lg_u32 %2, 0\n\ts_cbranch_scc1 .La%="
      : "=&v"(v), "+s"(c), "=&s"(t)
      : "v"(vconst(0u)), "s"(cp), "s"(n - 1u)
      : "memory", "scc");
}
DEVI void dma16_sc1(const char* sbase, unsigned voff, unsigned m0val) {
  asm volatile("s_mov_b32 m0, %2\n\ts_nop 0\n\tglobal_load_lds_dwordx4 %0, %1 sc1"
               :
               : "v"(voff), "s"(sbase), "s"(m0val)
               : "memory");
}
template <int N, bool SC1>
DEVI void dma4(const char* sbase, unsigned v0, unsigned v1, unsigned v2, unsigned v3, unsigned m0val) {
#define DMA4(S)                                                                                                               \
  asm volatile("s_mov_b32 m0, %5\n\ts_nop 0\n\tglobal_load_lds_dwordx4 %0, %4" S "\n\t.if %6 > 1\n\tglobal_load_lds_dwordx4 %1, %4 offset:1024" S \
               "\n\t.endif\n\t.if %6 > 2\n\tglobal_load_lds_dwordx4 %2, %4 offset:2048" S                                    \
               "\n\t.endif\n\t.if %6 > 3\n\tglobal_load_lds_dwordx4 %3, %4 offset:3072" S "\n\t.endif"                      \
               :                                                                                                              \
               : "v"(v0), "v"(v1), "v"(v2), "v"(v3), "s"(sbase), "s"(m0val), "n"(N)                                           \
               : "memory")
  if constexpr (SC1) DMA4(" sc1");
  else DMA4("");
}
template <int N>
DEVI void dma_seq(const char* src, unsigned voff, unsigned m0val) {
  if constexpr (N > 0) {
    dma4<(N < 4 ? N : 4), true>(src, voff, voff, voff, voff, m0val);
    dma_seq<N - 4>(src + 4096, voff, m0val + 4096u);
  }
}
DEVI void bar() { __builtin_amdgcn_s_barrier(); }
DEVI void lds_inc(unsigned addr, unsigned by = 1u) {
  asm volatile("s_mov_b64 exec, 1\n\tds_add_u32 %0, %1\n\ts_mov_b64 exec, -1" ::"v"(addr), "v"(vconst(by)) : "memory");
}
DEVI void lds_spin(unsigned addr, unsigned n) {
  spin_ge<1>(addr, n);
}
template <int SL = 1>
DEVI void lds_spin4(unsigned addr, unsigned n) {
  spin_b4<SL>(addr, n);
}
DEVI int lane_id() {
  int l;
  asm volatile("v_mbcnt_lo_u32_b32 %0, -1, 0\n\tv_mbcnt_hi_u32_b32 %0, -1, %0" : "=v"(l));
  __builtin_assume((unsigned)l < 64u);
  return l;
}

template <int I>
DEVI u32x4 tr8(const u32x4 (&v)[8]) {
  constexpr unsigned sel = (I & 1) ? 0x07060302u : 0x05040100u;
  return u32x4{__builtin_amdgcn_perm(v[1][I / 2], v[0][I / 2], sel), __builtin_amdgcn_perm(v[3][I / 2], v[2][I / 2], sel),
               __builtin_amdgcn_perm(v[5][I / 2], v[4][I / 2], sel), __builtin_amdgcn_perm(v[7][I / 2], v[6][I / 2], sel)};
}
template <int PITCH, bool SUM, int... I>
DEVI void tr8_store(const u32x4 (&v)[8], unsigned dst, float (&cs)[8], std::integer_sequence<int, I...>) {
  auto one = [&](auto I_) {
    const u32x4 o = tr8<I_>(v);
    const unsigned d_ = dst;
    asm volatile("ds_write_b128 %0, %1 offset:%2" ::"v"(d_), "v"(o), "n"(I_ * PITCH) : "memory");
    if constexpr (SUM) {
#pragma unroll
      for (int d = 0; d < 4; ++d) {
        const unsigned od = o[d];
        cs[I_] = __builtin_amdgcn_fdot2_f32_bf16(__builtin_bit_cast(bf16x2, od), __builtin_bit_cast(bf16x2, 0x3f803f80u), cs[I_], false);
      }
    }
  };
  (one(std::integral_constant<int, I>{}), ...);
}
template <int NB>
DEVI void lds_out(unsigned src, char* dst, int lane) {
  constexpr int N = (NB + 1023) / 1024;
  u32x4 t[N];
  const unsigned a = src + (unsigned)lane * 16u;
#pragma unroll
  for (int i = 0; i < N; ++i) asm volatile("ds_read_b128 %0, %1 offset:%2" : "=v"(t[i]) : "v"(a), "n"(i * 1024) : "memory");
  const char* d = uni_ptr(dst);
#pragma unroll
  for (int i = 0; i < N; ++i) {
    asm volatile("s_waitcnt lgkmcnt(%1)" : "+v"(t[i]) : "n"(N - 1 - i) : "memory");
    if (i < N - 1 || lane < (NB - (N - 1) * 1024) / 16)
      asm volatile("global_store_dwordx4 %0, %1, %2 sc1\n\ts_nop 1" ::"v"((unsigned)lane * 16u), "v"(t[i]), "s"(d + i * 1024) : "memory");
  }
}
template <int A, int B, class F>
DEVI void static_for(F f) {
  if constexpr (A < B) {
    f(std::integral_constant<int, A>{});
    static_for<A + 1, B>(f);
  }
}
constexpr int WSP_V = 3;
#define RS0 4
#define RS1 7
#define RX0 RS0
constexpr int RAW_S1 = RS1;
constexpr int RAW_S0 = RS0;
template <int E>
struct RawX {
  static constexpr int first(int s) {
    return s <= RX0 ? 0 : (s > RAW_S1 ? E : (E * (s - RX0) + RAW_S1 - RX0) / (RAW_S1 + 1 - RX0));
  }
  static constexpr int n(int s) { return first(s + 1) - first(s); }
};
struct RawLd {
  static constexpr int VPS = 10, SB = K1R::SB;
  unsigned xoff[5], woff[5], m0x;
  unsigned long long xact0, wact[5], tag;
  const char *xp, *wp;
  int qb;
  DEVI void q_hook(int st, char* ws, unsigned lds0, int hp) {
    if ((qb >> 1) == st) {
      const int lane = lane_id(), g = lane >> 4;
      const unsigned a = lds0 + (unsigned)((st & 3) * SB + K1R::XB + ((qb & 1) * 32 + 16 * (g >> 1) + 4 * (g & 1) + ((lane >> 2) & 3)) * 288 +
                                           (lane & 3) * 8 + (hp & 1) * 8);
      const unsigned a2 = a + ((lane & 3) >= 1 ? 8u : 0u), a4 = a + ((lane & 3) >= 2 ? 8u : 0u);
      u32x2 qf[7][2];
#pragma unroll
      for (int nt = 0; nt < 7; ++nt)
        asm volatile("ds_read_b64_tr_b16 %0, %2 offset:%3\n\tds_read_b64_tr_b16 %1, %2 offset:%4"
                     : "=&v"(qf[nt][0]), "=&v"(qf[nt][1])
                     : "v"(nt == 2 ? a2 : (nt == 4 ? a4 : a)), "n"(nt * 32 + (nt > 4 ? 16 : (nt > 2 ? 8 : 0))),
                       "n"(2304 + nt * 32 + (nt > 4 ? 16 : (nt > 2 ? 8 : 0)))
                     : "memory");
      const char* d = uni_ptr(ws + OFF_PQ + (size_t)(hp * 18 + qb) * 7168 + 3072);
#pragma unroll
      for (int nt = 0; nt < 7; ++nt) {
        asm volatile("s_waitcnt lgkmcnt(%2)" : "+v"(qf[nt][0]), "+v"(qf[nt][1]) : "n"(12 - 2 * nt) : "memory");
        const u32x4 v = {qf[nt][0][0], qf[nt][0][1], qf[nt][1][0], qf[nt][1][1]};
        asm volatile("global_store_dwordx4 %0, %1, %2 offset:%3 sc1\n\ts_nop 1" ::"v"((unsigned)lane * 16u), "v"(v), "s"(d), "n"(nt * 1024 - 3072) : "memory");
      }
      if (st == 8) lds_wr32(lds0 + (unsigned)(LW_OFF + LW_Q * 4), 1u);
    }
  }
  DEVI void stage_x(unsigned so) {
    asm volatile(
        "s_mov_b32 m0, %6\n\ts_mov_b64 exec, %7\n\tglobal_load_lds_dwordx4 %0, %5\n\ts_mov_b64 exec, -1\n\t"
        "global_load_lds_dwordx4 %1, %5 offset:1024\n\tglobal_load_lds_dwordx4 %2, %5 offset:2048\n\tglobal_load_lds_dwordx4 %3, %5 offset:3072\n\t"
        "s_add_u32 m0, m0, 0x1000\n\ts_mov_b32 exec_hi, 0\n\tglobal_load_lds_dwordx4 %4, %5\n\ts_mov_b32 exec_hi, -1" ::"v"(xoff[0]),
        "v"(xoff[1]), "v"(xoff[2]), "v"(xoff[3]), "v"(xoff[4]), "s"(xp), "s"(m0x + so), "s"(xact0)
        : "memory", "scc");
    xp += 128;
  }
  DEVI void stage_w(unsigned so) {
    asm volatile(
        "s_mov_b32 m0, %6\n\ts_mov_b64 exec, %7\n\tglobal_load_lds_dwordx4 %0, %5\n\ts_mov_b64 exec, %8\n\t"
        "global_load_lds_dwordx4 %1, %5 offset:1024\n\ts_mov_b64 exec, %9\n\tglobal_load_lds_dwordx4 %2, %5 offset:2048\n\t"
        "s_mov_b64 exec, %10\n\tglobal_load_lds_dwordx4 %3, %5 offset:3072\n\ts_add_u32 m0, m0, 0x1000\n\ts_mov_b64 exec, %11\n\t"
        "global_load_lds_dwordx4 %4, %5\n\ts_mov_b64 exec, -1" ::"v"(woff[0]),
        "v"(woff[1]), "v"(woff[2]), "v"(woff[3]), "v"(woff[4]), "s"(wp), "s"(m0x + so + (unsigned)K1R::XB), "s"(wact[0]), "s"(wact[1]),
        "s"(wact[2]), "s"(wact[3]), "s"(wact[4])
        : "memory", "scc");
    wp += 64 * NQKV * 2;
  }
  DEVI void stage(unsigned so) {
    stage_x(so);
    stage_w(so);
  }
  DEVI void front(const u16* x, const u16* wqkv, const u16* wo, char* ws, int bid, unsigned lds0, int wl, int lane, int rt, int hp,
                  int pj MF_ARG) {
    xp = uni_ptr((const char*)x - 3072);
    wp = uni_ptr((const char*)wqkv + ((hp * 72) & ~15) - 3072);
    m0x = lds0 + (unsigned)(wl * 288 * 16);
    xact0 = wl ? ~0ull : ~0xffull;
    const int row0 = wl * 36 + (lane >> 3);
#pragma unroll
    for (int i = 0; i < 5; ++i) {
      const int row = row0 + i * 8;
      int gr = rt * 128 - 16 + row;
      if (gr < 0) gr = 0;
      xoff[i] = (unsigned)(gr * (D_ * 2) + (3 - (i & 3)) * 1024) + (unsigned)(((lane ^ (row >> 1)) & 7) * 16);
    }
    stage_x(0u);
    u32x2 tg;
    asm volatile("global_load_dwordx2 %0, %1, %2 sc1" : "=v"(tg) : "v"(0u), "s"(uni_ptr(ws + OFF_TAG)) : "memory");
    asm volatile("" : "+v"(lane));
#pragma unroll
    for (int i = 0; i < 5; ++i) {
      const unsigned c = (unsigned)(i * 64 + (lane & 63));
      const unsigned wr = (c * 3641u) >> 16, wc = c - wr * 18u, seg = (wc * 13u) >> 6;
      wact[i] = __ballot(wc < 15u) & (i < 4 ? ~0ull : 0xffffffffull);
      woff[i] = (unsigned)(wl * 16 + swap23((int)wr)) * (unsigned)(NQKV * 2) + seg * (unsigned)(D_ * 2 - 80) + wc * 16u + (unsigned)((3 - (i & 3)) * 1024);
    }
    stage_w(0u);
    stage((unsigned)SB);
    MF(10);
    asm volatile("s_waitcnt vmcnt(%1)" : "+v"(tg) : "n"(VPS) : "memory");
    tag = (((unsigned long long)(unsigned)__builtin_amdgcn_readfirstlane(tg[1]) << 32) | (unsigned)__builtin_amdgcn_readfirstlane(tg[0])) +
          0x9e3779b97f4a7c15ull;
    MF(1);
    __builtin_amdgcn_s_barrier();
    MF(15);
    if (wl == 0 && lane == 0)
      asm volatile("global_store_dwordx2 %0, %1, off sc1\n\ts_nop 1" ::"v"(ws + OFF_HELLO + bid * 8), "v"(tag) : "memory");
    stage((unsigned)(2 * SB));
    qb = wl == 3 ? rt : ((wl == 2 && rt < 2) ? 16 + rt : 99);
    q_hook(0, ws, lds0, hp);
    const int nb = pj;
    const bool po = pj >= 0 && wl < 2;
    u32x4 v[8];
    unsigned da = 0u;
    if (po) {
      const int l54 = lane < 54 ? lane : 53, gl = l54 / 9, c = l54 - gl * 9, kl = wl * 6 + gl, t = nb / 6, kg = (nb - t * 6) * 12 + kl;
      da = lds0 + (unsigned)(K1R_PO + (kl >> 2) * 4608 + (16 * (kl & 1) + ((kl >> 1) & 1)) * 144 + c * 16);
      const unsigned sa[2] = {(unsigned)(okmap(kg * 8) * D_ + t * 72 + c * 8) * 2u, (unsigned)(okmap(kg * 8 + 4) * D_ + t * 72 + c * 8) * 2u};
      const char* ps = uni_ptr((const char*)wo);
#pragma unroll
      for (int j = 0; j < 8; ++j)
        asm volatile("global_load_dwordx4 %0, %1, %2 offset:%3" : "=v"(v[j]) : "v"(sa[j / 4]), "s"(ps), "n"((j % 4) * D_ * 2) : "memory");
    } else {
#pragma unroll
      for (int j = 0; j < 8; ++j) asm volatile("" : "=v"(v[j]));
    }
    unsigned slot = (unsigned)(3 * SB);
#pragma unroll 1
    for (int S = 0; S < RAW_S0; ++S) {
      wait_vm<VPS>();
      __builtin_amdgcn_s_barrier();
      if (S == 0) MF(16);
      if (S == 1) MF(17);
      if (S == 2) MF(18);
      if (S == 3) MF(19);
      stage(slot);
      slot = (slot == (unsigned)(3 * SB)) ? 0u : slot + (unsigned)SB;
      q_hook(S + 1, ws, lds0, hp);
      if (po && S == 1) {
        float cs[8];
        asm volatile("" : "+v"(v[0]), "+v"(v[1]), "+v"(v[2]), "+v"(v[3]), "+v"(v[4]), "+v"(v[5]), "+v"(v[6]), "+v"(v[7]));
        tr8_store<288, false>(v, da, cs, std::make_integer_sequence<int, 8>{});
        asm volatile("s_waitcnt lgkmcnt(0)" ::: "memory");
      }
      if (po && S == 2) lds_out<6912>(lds0 + (unsigned)(K1R_PO + wl * 6912), ws + OFF_PO + (size_t)nb * 13824 + wl * 6912, lane);
    }
  }
};
template <bool JOB>
DEVI void k1_raw_loader(RawLd& ld, const u16* __restrict__ w_in, const u16* __restrict__ w_out, char* __restrict__ ws,
                        unsigned long long tag, int bid, unsigned lds0, int wl, int lane, int rt, int hp, int ct, int job, int pj MF_ARG) {
  constexpr int VPS = RawLd::VPS, SB = RawLd::SB, E = JOB ? 16 : 0;
  const bool isi = job < 144;
  const int kb = isi ? job >> 3 : job - 144;
  const int pitch = isi ? F_ * 2 : D_ * 2;
  const int l36 = lane < 36 ? lane : 35;
  unsigned sa = (unsigned)l36 * 16u, da;
  if (isi) da = lds0 + (unsigned)((l36 >= 18 ? 9216 - 288 : 0) + wl * 8 * 288 + l36 * 16);
  else da = lds0 + (unsigned)((l36 / 9) * (4608 - 144) + (16 * (wl & 1) + (wl >> 1)) * 144 + l36 * 16);
  asm volatile("" : "+v"(sa), "+v"(da));
  u32x4 v[2][8];
  const char* ps = uni_ptr((const char*)(isi ? w_in + (job & 7) * 576 : w_out) + (size_t)(kb * 32 + wl * 8) * pitch);
  auto extra = [&](auto J_) {
    constexpr int j = J_;
    const char* q = ps + (j / 2) * pitch;
    u32x4 t;
    asm volatile("global_load_dwordx4 %0, %1, %2 offset:%3" : "=v"(t) : "v"(sa), "s"(q), "n"((j % 2) * 576) : "memory");
    v[j % 2][j / 2] = t;
  };
  auto step = [&](auto S_) {
    constexpr int S = S_;
    wait_vm<RawX<E>::n(S - 2) + (S <= 6 ? VPS : 0) + RawX<E>::n(S - 1)>();
    __builtin_amdgcn_s_barrier();
    MF(16 + S);
    if constexpr (S == RAW_S0) {
      static_assert(RAW_S0 >= 4, "wo blocks");
      if (pj >= 0 && wl == 0 && lane == 0)
        asm volatile("global_store_dwordx2 %0, %1, off sc1\n\ts_nop 1" ::"v"(ws + OFF_POT + (pj / 6) * 128 + (pj % 6) * 8), "v"(tag) : "memory");
    }
    if constexpr (S <= 5) ld.stage((unsigned)(((S + 3) % 4) * SB));
    ld.q_hook(S + 1, ws, lds0, hp);
    static_for<RawX<E>::first(S), RawX<E>::first(S + 1)>(extra);
  };
  const char* hp_ = ws + OFF_HELLO + lane * 32;
  u32x4 a, b;
  if (wl == 1) asm volatile("global_load_dwordx4 %0, %2, off sc1\n\tglobal_load_dwordx4 %1, %2, off offset:16 sc1" : "=&v"(a), "=&v"(b) : "v"(hp_) : "memory");
  else asm volatile("" : "=v"(a), "=v"(b));
  static_for<RAW_S0, 8>(step);
  MF(2);
  if (wl == 1) {
    asm volatile("" : "+v"(a), "+v"(b));
    const unsigned t0 = (unsigned)tag, t1 = (unsigned)(tag >> 32);
#pragma unroll 1
    for (unsigned spin = 0; spin < SPIN_MEM; ++spin) {
      const unsigned bad = ((a[0] ^ t0) | (a[2] ^ t0) | (b[0] ^ t0) | (b[2] ^ t0)) | ((a[1] ^ t1) | (a[3] ^ t1) | (b[1] ^ t1) | (b[3] ^ t1));
      if (!__any(bad != 0u)) break;
      __builtin_amdgcn_s_sleep(4);
      asm volatile("global_load_dwordx4 %0, %2, off sc1\n\tglobal_load_dwordx4 %1, %2, off offset:16 sc1\n\ts_waitcnt vmcnt(0)"
                   : "=&v"(a), "=&v"(b)
                   : "v"(hp_)
                   : "memory");
    }
    lds_wr32(lds0 + (unsigned)(LW_OFF + LW_HELLO * 4), 1u);
    if (bid == 0 && lane == 0) asm volatile("global_store_dwordx2 %0, %1, off sc1\n\ts_nop 1" ::"v"(ws + OFF_TAG), "v"(tag) : "memory");
  }
  MF(11);
  if (rt < 2) lds_spin(lds0 + (unsigned)(LW_OFF + LW_Q * 4), 1u);
  lds_spin(lds0 + (unsigned)K1R_CNT, 8u);
  if constexpr (JOB) {
#pragma unroll
    for (int b = 0; b < 2; ++b)
      asm volatile("s_waitcnt vmcnt(0)"
                   : "+v"(v[b][0]), "+v"(v[b][1]), "+v"(v[b][2]), "+v"(v[b][3]), "+v"(v[b][4]), "+v"(v[b][5]), "+v"(v[b][6]), "+v"(v[b][7])
                   :
                   : "memory");
  }
  MF(12);
  const char* tp = ws + OFF_POT + ct * 128 + (lane < 6 ? lane : 5) * 8;
  u32x2 f;
  asm volatile("global_load_dwordx2 %0, %1, off sc1" : "=v"(f) : "v"(tp) : "memory");
  if constexpr (JOB) {
    static_assert(SB == 4 * 9216, "staging");
#pragma unroll
    for (int b = 0; b < 2; ++b) {
      float cs[8] = {0.f, 0.f, 0.f, 0.f, 0.f, 0.f, 0.f, 0.f};
      tr8_store<288, true>(v[b], da + (unsigned)(b * 18432), cs, std::make_integer_sequence<int, 8>{});
      const f32x4 c0 = {cs[0], cs[1], cs[2], cs[3]}, c1 = {cs[4], cs[5], cs[6], cs[7]};
      const unsigned ca = lds0 + (unsigned)(3 * SB + wl * 2304) + sa * 2u;
      asm volatile("ds_write_b128 %0, %1 offset:%2" ::"v"(ca), "v"(c0), "n"(b * 1152) : "memory");
      asm volatile("ds_write_b128 %0, %1 offset:%2" ::"v"(ca), "v"(c1), "n"(b * 1152 + 16) : "memory");
    }
    lds_inc(lds0 + (unsigned)(LW_OFF + LW_ST * 4));
    lds_spin(lds0 + (unsigned)(LW_OFF + LW_ST * 4), 4u);
    if (isi) {
      const int j = (job & 7) * 4 + wl, ti = j < 16 ? 2 * j + 1 : 2 * (j - 16);
      lds_out<9216>(lds0 + (unsigned)(wl * 9216), ws + OFF_PI + (size_t)(ti * 18 + kb) * 9216, lane);
      f32x4 c[4];
      const unsigned ca = lds0 + (unsigned)(3 * SB + wl * 576) + sa;
#pragma unroll
      for (int i = 0; i < 4; ++i) asm volatile("ds_read_b128 %0, %1 offset:%2" : "=v"(c[i]) : "v"(ca), "n"(i * 2304) : "memory");
      asm volatile("s_waitcnt lgkmcnt(0)" : "+v"(c[0]), "+v"(c[1]), "+v"(c[2]), "+v"(c[3]));
      const f32x4 t = (c[0] + c[1]) + (c[2] + c[3]);
      if (lane < 36) pk_st16(ws + OFF_CSP + ((size_t)kb * F_ + ti * 144) * 4 + sa, __builtin_bit_cast(u32x4, t));
    } else {
      lds_out<4608>(lds0 + (unsigned)(wl * 9216), ws + OFF_PW + (size_t)(wl * 144 + kb) * 4608, lane);
      lds_out<4608>(lds0 + (unsigned)(wl * 9216 + 4608), ws + OFF_PW + (size_t)(wl * 144 + 72 + kb) * 4608, lane);
    }
  }
  MF(8);
  announce(lds0 + (unsigned)(LW_OFF + LW_PK * 4), 4u, ws, CN_PK, isi ? 8 + (job & 3) * 4 + lane : 24 + lane, JOB && lane < (isi ? 4 : 8));
  MF(9);
  if (wl == 0) {
    asm volatile("" : "+v"(f));
#pragma unroll 1
    for (unsigned spin = 0; spin < SPIN_MEM; ++spin) {
      if (!__any(f[0] != (unsigned)tag || f[1] != (unsigned)(tag >> 32))) break;
      __builtin_amdgcn_s_sleep(2);
      asm volatile("global_load_dwordx2 %0, %1, off sc1\n\ts_waitcnt vmcnt(0)" : "=&v"(f) : "v"(tp) : "memory");
    }
  }
}

template <int MT, int NT>
DEVI void k1_gemm(char* smem, const char* ws, const float* __restrict__ csq, int rt, int hp, int mt0, int nt0,
                  bool late, int lane MK_ARG) {
  const unsigned lds0 = (unsigned)(size_t)smem;
  const int g = lane >> 4;
  f32x4 cs[NT];
  unsigned ta[NT];
  const char* csp = uni_ptr((const char*)(csq + (hp & 1) * 4));
  unsigned cad[NT];
#pragma unroll
  for (int nt = 0; nt < NT; ++nt) {
    int c = (nt0 + nt) * 16 + g * 4;
    if (c > 104) c = 104;
    const int part = c / 36, ci = c - part * 36;
    if (CSL) {
      cad[nt] = lds0 + (unsigned)(K1_CS_OFF + (part * 40 + (hp & 1) * 4 + ci) * 4);
      asm volatile("" : "+v"(cad[nt]));
    } else
    asm volatile("global_load_dwordx4 %0, %1, %2 sc1" : "=v"(cs[nt]) : "v"((unsigned)(part * 40 + ci) * 4u), "s"(csp));
    const int to = (nt0 + nt) * 16 + g * 4 > 104 ? 120 : part * 80 + ci * 2 + (ci >= 20 ? 8 : 0);
    ta[nt] = lds0 + (unsigned)(K1_T_OFF + (mt0 * 16 + (lane & 15)) * K1_TP + to);
    asm volatile("" : "+v"(ta[nt]));
  }
  unsigned sa = lds0 + (unsigned)(K1_ST_OFF + (mt0 * 16 + (lane & 15)) * 8);
  asm volatile("" : "+v"(sa));
  f32x4 acc[MT][NT];
#pragma unroll
  for (int mt = 0; mt < MT; ++mt)
#pragma unroll
    for (int nt = 0; nt < NT; ++nt) acc[mt][nt] = f32x4{0.f, 0.f, 0.f, 0.f};
  f32x2 st[MT];
  if (G1E && late) bar();
  if constexpr (NT == 7) cnt_wait(ws, CN_X2, rt * 16 + hp, rt ? 24u : 16u);
  bar();
  MK(1);
  {
    GemmMath<K1C, MT, NT, (NT == 7 ? 5 : 3), (NT == 3 ? 6 : NT), true, 2, false, K1_WS> gm(acc);
    gm.setup(lds0, mt0, nt0, lane, 0u);
    bar();
    if (CSL) {
#pragma unroll
      for (int nt = 0; nt < NT; ++nt) asm volatile("ds_read_b128 %0, %1" : "=v"(cs[nt]) : "v"(cad[nt]));
    }
    gm.first();
    gm.template mid<8, true, AL_K>();
#pragma unroll
    for (int mt = 0; mt < MT; ++mt) asm volatile("ds_read_b64 %0, %1 offset:%2" : "=v"(st[mt]) : "v"(sa), "n"(mt * 128));
    asm volatile("s_waitcnt vmcnt(0)" ::: "memory");
    gm.last();
  }
#pragma unroll
  for (int nt = 0; nt < NT; ++nt) asm volatile("" : "+v"(cs[nt]));
#pragma unroll
  for (int mt = 0; mt < MT; ++mt) asm volatile("" : "+v"(st[mt]));
  MK(2);
#pragma unroll
  for (int mt = 0; mt < MT; ++mt) {
    f32x2 M = {st[mt][0], st[mt][0]}, R = {st[mt][1], st[mt][1]};
    asm volatile("" : "+v"(M), "+v"(R));
#pragma unroll
    for (int nt = 0; nt < NT; ++nt) {
      const f32x2 v0 = R * __builtin_elementwise_fma(M, f32x2{cs[nt][0], cs[nt][1]}, f32x2{acc[mt][nt][0], acc[mt][nt][1]});
      const f32x2 v1 = R * __builtin_elementwise_fma(M, f32x2{cs[nt][2], cs[nt][3]}, f32x2{acc[mt][nt][2], acc[mt][nt][3]});
      acc[mt][nt][0] = __uint_as_float(pack2v(v0));
      acc[mt][nt][1] = __uint_as_float(pack2v(v1));
      const f32x2 pk = {acc[mt][nt][0], acc[mt][nt][1]};
      asm volatile("ds_write_b64 %0, %1 offset:%2" ::"v"(ta[nt]), "v"(pk), "n"(mt * 16 * K1_TP) : "memory");
    }
  }
}

DEVI float vmax(float a, float b) {
  float r;
  asm("v_max_f32 %0, %1, %2" : "=v"(r) : "v"(a), "v"(b));
  return r;
}
struct Attn {
  unsigned ka, kb, va, vb, m0, m1, m2, pm[2], oa, pa;
  f32x4 mk;
  unsigned long long sel[4];
  const char* ob;
  DEVI void setup(unsigned T, char* o, int r0, int hp, int wave, int lane) {
    const int g = lane >> 4, r = lane & 15, blk = wave >> 1, hd = wave & 1;
    const unsigned co = (unsigned)(g < 2 ? hd * 48 + g * 16 : 32);
    ka = T + (unsigned)((blk * 32 + 2 * r) * K1_TP) + co;
    kb = T + (unsigned)((blk * 32 + 2 * (r & 7)) * K1_TP) + co;
    const int vr = 4 * g + ((lane >> 2) & 3);
    va = T + (unsigned)((blk * 32 + 2 * vr) * K1_TP + (lane & 3) * 8);
    vb = T + (unsigned)((blk * 32 + 2 * (vr & 7)) * K1_TP + (lane & 3) * 8);
    m0 = (g < 2 || (g == 2 && hd == 0)) ? ~0u : 0u;
    m1 = (g < 2 || (g == 2 && hd == 1)) ? ~0u : 0u;
    m2 = g < 2 ? ~0u : 0u;
    pm[0] = pm[1] = 0u;
    const float ci = __uint_as_float(vconst(0xff800000u));
    const unsigned ch = vconst(0xffffu);
#pragma unroll
    for (int e = 0; e < 4; ++e) {
      const int w1 = r + 8 - 4 * g - e;
      mk[e] = (w1 & 15) <= 7 ? 0.f : ci;
      sel[e] = __ballot(w1 >= 8);
      if (w1 >= 8) pm[e >> 1] |= ch << (16 * (e & 1));
    }
    oa = (unsigned)((8 + hp * 4 + hd * 2 + (g >> 1)) * 1024 + ((blk & 1) * 32 + 2 * r + (g & 1)) * 16);
    pa = ((oa & 1023u) >> 2) + (unsigned)((hp * 2 + hd) * 256);
    ob = uni_ptr(o + (size_t)((r0 >> 6) + (wave >> 2)) * (72 * 1024));
    asm volatile("" : "+v"(ka), "+v"(kb), "+v"(va), "+v"(vb), "+v"(m0), "+v"(m1), "+v"(m2), "+v"(pm[0]), "+v"(pm[1]), "+v"(oa));
    asm volatile("" : "+v"(mk), "+v"(pa), "+s"(ob), "+s"(sel[0]), "+s"(sel[1]), "+s"(sel[2]), "+s"(sel[3]));
  }
  DEVI void run(int r0, int hp, int wave, int lane) {
    const int hd = wave & 1;
    u32x4 q[2], k0[2], k1[2];
    u32x2 v[2][2][2];
#pragma unroll
    for (int p = 0; p < 2; ++p) {
      asm volatile("ds_read_b128 %0, %1 offset:%2" : "=v"(q[p]) : "v"(ka), "n"((16 + p) * K1_TP) : "memory");
      asm volatile("ds_read_b128 %0, %1 offset:%2" : "=v"(k0[p]) : "v"(ka), "n"(p * K1_TP + 80) : "memory");
      asm volatile("ds_read_b128 %0, %1 offset:%2" : "=v"(k1[p]) : "v"(kb), "n"((32 + p) * K1_TP + 80) : "memory");
    }
    const unsigned vh = (unsigned)hd * 48u;
#pragma unroll
    for (int p = 0; p < 2; ++p) {
      asm volatile("ds_read_b64_tr_b16 %0, %1 offset:%2" : "=v"(v[p][0][0]) : "v"(va + vh), "n"(p * K1_TP + 160) : "memory");
      asm volatile("ds_read_b64_tr_b16 %0, %1 offset:%2" : "=v"(v[p][0][1]) : "v"(vb + vh), "n"((32 + p) * K1_TP + 160) : "memory");
      asm volatile("ds_read_b64_tr_b16 %0, %1 offset:%2" : "=v"(v[p][1][0]) : "v"(va), "n"(p * K1_TP + 192) : "memory");
      asm volatile("ds_read_b64_tr_b16 %0, %1 offset:%2" : "=v"(v[p][1][1]) : "v"(vb), "n"((32 + p) * K1_TP + 192) : "memory");
    }
    const u32x4 one4 = {vconst(0x3f803f80u), vconst(0x3f803f80u), vconst(0x3f803f80u), vconst(0x3f803f80u)};
    const s16x8 ones = __builtin_bit_cast(s16x8, one4);
    const float c = 0.23570226039551584f * 1.4426950408889634f;
    const f32x4 z = {0.f, 0.f, 0.f, 0.f};
    f32x4 sc[2][2];
#pragma unroll
    for (int p = 0; p < 2; ++p) {
      asm volatile("s_waitcnt lgkmcnt(%3)" : "+v"(q[p]), "+v"(k0[p]), "+v"(k1[p]) : "n"(11 - 3 * p));
      const s16x8 qf = __builtin_bit_cast(s16x8, q[p] & u32x4{m0, m1, m2, m2});
      sc[p][0] = __builtin_amdgcn_mfma_f32_16x16x32_bf16(__builtin_bit_cast(s16x8, k0[p]), qf, mk, 0, 0, 0);
      sc[p][1] = __builtin_amdgcn_mfma_f32_16x16x32_bf16(__builtin_bit_cast(s16x8, k1[p]), qf, mk, 0, 0, 0);
      __builtin_amdgcn_sched_barrier(0);
    }
    if (__builtin_expect(r0 == 0 && wave < 2, 0)) {
#pragma unroll
      for (int p = 0; p < 2; ++p)
#pragma unroll
        for (int e = 0; e < 4; ++e)
          if (lane < 32) sc[p][0][e] = mk[e] - 4.242640687e9f;
    }
    float lg[2][4], mx[2];
#pragma unroll
    for (int p = 0; p < 2; ++p) {
#pragma unroll
      for (int e = 0; e < 4; ++e) lg[p][e] = __builtin_amdgcn_inverse_ballot_w64(sel[e]) ? sc[p][1][e] : sc[p][0][e];
      asm("v_max3_f32 %0, %1, %2, %3\n\tv_max_f32 %0, %0, %4" : "=&v"(mx[p]) : "v"(lg[p][0]), "v"(lg[p][1]), "v"(lg[p][2]), "v"(lg[p][3]));
    }
    {
      const auto a = __builtin_amdgcn_permlane32_swap(__float_as_uint(mx[0]), __float_as_uint(mx[1]), false, false);
      const unsigned b = __float_as_uint(vmax(__uint_as_float(a[0]), __uint_as_float(a[1])));
      const auto d = __builtin_amdgcn_permlane16_swap(b, b, false, false);
      const unsigned f = __float_as_uint(vmax(__uint_as_float(d[0]), __uint_as_float(d[1])) * -c);
      const auto h = __builtin_amdgcn_permlane32_swap(f, f, false, false);
      mx[0] = __uint_as_float(h[0]);
      mx[1] = __uint_as_float(h[1]);
    }
#pragma unroll
    for (int p = 0; p < 2; ++p)
#pragma unroll
      for (int e = 0; e < 4; ++e) lg[p][e] = __builtin_amdgcn_exp2f(__builtin_fmaf(lg[p][e], c, mx[p]));
    asm volatile("s_waitcnt lgkmcnt(0)"
                 : "+v"(v[0][0][0]), "+v"(v[0][0][1]), "+v"(v[0][1][0]), "+v"(v[0][1][1]), "+v"(v[1][0][0]), "+v"(v[1][0][1]),
                   "+v"(v[1][1][0]), "+v"(v[1][1][1]));
    __builtin_amdgcn_sched_barrier(0);
    f32x4 su[2], o0[2], o1[2];
    s16x8 pp[2];
#pragma unroll
    for (int p = 0; p < 2; ++p) {
      const unsigned p0 = pack2(lg[p][0], lg[p][1]), p1 = pack2(lg[p][2], lg[p][3]);
      pp[p] = __builtin_bit_cast(s16x8, u32x4{p0 & ~pm[0], p1 & ~pm[1], p0 & pm[0], p1 & pm[1]});
    }
    __builtin_amdgcn_sched_barrier(0);
#pragma unroll
    for (int p = 0; p < 2; ++p) {
      const s16x8 pf = pp[p];
      const u32x4 v0 = {v[p][0][0][0], v[p][0][0][1], v[p][0][1][0], v[p][0][1][1]};
      const u32x4 v1 = {v[p][1][0][0], v[p][1][0][1], v[p][1][1][0], v[p][1][1][1]};
      su[p] = __builtin_amdgcn_mfma_f32_16x16x32_bf16(ones, pf, z, 0, 0, 0);
      o0[p] = __builtin_amdgcn_mfma_f32_16x16x32_bf16(__builtin_bit_cast(s16x8, v0), pf, z, 0, 0, 0);
      o1[p] = __builtin_amdgcn_mfma_f32_16x16x32_bf16(__builtin_bit_cast(s16x8, v1), pf, z, 0, 0, 0);
    }
    __builtin_amdgcn_sched_barrier(0);
    u32x2 y[2], yl;
    unsigned hm = (unsigned)hd - 1u;
    asm volatile("" : "+s"(hm));
#pragma unroll
    for (int p = 0; p < 2; ++p) {
      const float inv = __builtin_amdgcn_rcpf(su[p][0]);
      y[p] = u32x2{pack2(o0[p][0] * inv, o0[p][1] * inv), pack2(o0[p][2] * inv, o0[p][3] * inv)};
      const unsigned la = pack2(o1[p][0] * inv, o1[p][1] * inv), lb = pack2(o1[p][2] * inv, o1[p][3] * inv);
      yl[p] = (la & hm) | (lb & ~hm);
    }
    const auto s0 = __builtin_amdgcn_permlane16_swap(y[0][0], y[1][0], false, false);
    const auto s1 = __builtin_amdgcn_permlane16_swap(y[0][1], y[1][1], false, false);
    const u32x4 w = {s0[0], s1[0], s0[1], s1[1]};
    asm volatile("global_store_dwordx4 %0, %1, %4 sc1\n\ts_mov_b64 exec, 0xffff\n\tglobal_store_dwordx2 %2, %3, %4 sc1\n\ts_mov_b64 exec, -1\n\ts_nop 0" ::"v"(oa),
                 "v"(w), "v"(pa), "v"(yl), "s"(ob)
                 : "memory");
  }
};

#define NL_MAIN 650
DEVI void code_again(const char* code0, int bid) {
  constexpr int PER = (2 * NL_MAIN + 31) / 32;
  const int lane = lane_id();
  int li = (bid >> 3) * PER + (lane < PER ? lane : PER - 1);
  if (li > 2 * NL_MAIN - 1) li = 2 * NL_MAIN - 1;
  unsigned t;
  asm volatile("global_load_dword %0, %1, %2\n\ts_waitcnt vmcnt(0)" : "=&v"(t) : "v"((unsigned)li * 64u), "s"(code0) : "memory");
}
DEVI void math_start(char* smem, const char* code0, const u16* b_sp, const u16* wqkv, char* ws, int bid, int wave, int lane) {
  const unsigned lds0 = (unsigned)(size_t)smem;
  const int tid = wave * 64 + lane, idx = bid >> 3;
  if (wave < 3) {
    static_assert(2 * SZ_ZP == (size_t)256 * 2048 && NCNT <= 16, "mark split");
    const u32x4 mk = {0xffffffffu, 0xffffffffu, 0xffffffffu, 0xffffffffu};
    char* c = ws + OFF_CNT + (size_t)((lane >> 3) * 256 + bid) * 128 + (lane & 7) * 16;
    pk_st16(wave < 2 ? ws + OFF_ZP + (size_t)((bid * 2 + wave) * 64 + lane) * 16 : c, mk);
    if (wave == 2 && lane < (NCNT - 8) * 8) pk_st16(c + 8 * 256 * 128, mk);
  }
  if (wave == 3) {
    const int li = idx * 54 + (lane < 54 ? lane : 53);
    touch64(uni_ptr((const char*)wqkv), (unsigned)(li * 1152 + 512), lds0 + (unsigned)(K1R_DUMMY + 768));
  }
  if (!CT0 && wave == 4) {
    constexpr int PER = (2 * NL_MAIN + 31) / 32;
    static_assert(PER <= 64, "one load");
    int li = idx * PER + (lane < PER ? lane : PER - 1);
    if (li > 2 * NL_MAIN - 1) li = 2 * NL_MAIN - 1;
    touch64(code0, (unsigned)li * 64u, lds0 + (unsigned)(K1R_DUMMY + 768));
  }
  if (wave == 5 && idx == 31) touch64(uni_ptr((const char*)b_sp), (unsigned)lane * 64u, lds0 + (unsigned)(K1R_DUMMY + 768));
  const u32x4 ones = {0x3f803f80u, 0x3f803f80u, 0x3f803f80u, 0x3f803f80u};
  if (tid < 256) *reinterpret_cast<u32x4*>(smem + (tid >> 6) * K1R::SB + K1R::XB + (tid & 63) * 288 + 240) = ones;
  else if (tid < 288) *reinterpret_cast<u32x4*>(smem + ((tid - 256) >> 3) * K1R::SB + (tid & 7) * 16) = ones;
  else if (tid < 296) *reinterpret_cast<u32x4*>(smem + LW_OFF + (tid - 288) * 16) = u32x4{0u, 0u, 0u, 0u};
  asm volatile("s_waitcnt lgkmcnt(0)" ::: "memory");
}

template <int NST, int NS, int NSW, int WOFF, int XW, int X0, int S0, unsigned BAR>
struct ResX {
  static constexpr int WS = 4 * 4608, R = 10, V1 = 5 * NS, NV = V1 + 10 * (NST - NS), NG = (NSW + 2) / 3;
  s16x8 xf[XW][2];
  s16x8 wf[R];
  unsigned wrd[2][NG], xo, ea, fa, fv, tag;
  const char* xp[3];
  f32x4 (&acc)[2][5];
  DEVI ResX(f32x4 (&a)[2][5]) : acc(a) {}
  static constexpr int st(int V) { return V < V1 ? V / 5 : NS + (V - V1) / 10; }
  static constexpr int hf(int V) { return V < V1 ? 0 : ((V - V1) / 5) & 1; }
  static constexpr bool first(int V) { return V < V1 ? V % 5 == 0 : (V - V1) % 10 == 0; }
  static constexpr int wreq(int V) {
    int n = V + 5;
    if (V >= V1 - 4) n += V < V1 ? V - (V1 - 5) : 4;
    return n < NV ? n : NV;
  }
  static constexpr int XU = V1 + 1, XF = V1 + 11, XR = V1 + 15, XC = V1 + 21;
  static constexpr int oth(int V) { return NS && ((V >= XU && V <= XF && (V - XU) % 2 == 0) || V == XR || (V > XC && V <= XC + 5)) ? 1 : 0; }
  static constexpr int newer(int V) {
    const int F = V + 1 < NV ? V + 1 : V;
    int v = V - 1, n = 0;
    for (; v >= 0 && wreq(v - 1) > F; --v) n += wreq(v) - wreq(v - 1) + oth(v);
    return n + wreq(v) - 1 - F + (v >= 0 ? oth(v) : 0);
  }
  static constexpr int xreq(int T) {
    int n = X0 + 2 * (T + 1);
    if (n > T + XW) n = T + XW;
    if (NS && T <= 11 && n > 18) n = 18;
    if (T < 0) n = X0;
    return n < NST ? n : NST;
  }
  static_assert(NS == 0 || (xreq(NS - 2) == NST && XC + 5 < NV), "the last steps only compute and swap");
  template <int T, int M>
  DEVI void xld() {
    asm volatile("global_load_dwordx4 %0, %1, %2 sc1" : "=v"(xf[T % XW][M]) : "v"(xo), "s"(xp[M == 0 ? 0 : (T < NS ? 1 : 2)]));
    if constexpr (M == 1 && T + 1 < NST) asm volatile("v_add_u32 %0, 0x2000, %0" : "+v"(xo));
  }
  template <int V>
  DEVI void rd() {
    constexpr int T = st(V), sl = (T / 2 + S0) % NSW;
    asm volatile("ds_read_b128 %0, %1 offset:%2" : "=v"(wf[V % R]) : "v"(wrd[hf(V)][sl / 3]), "n"((sl % 3) * WS + (T & 1) * 9216 + (V % 5) * 32));
  }
  template <int V, int M, int H>
  DEVI void mf() {
    acc[M][V % 5] = __builtin_amdgcn_mfma_f32_16x16x32_bf16(wf[V % R], xf[st(V) % XW][H], acc[M][V % 5], 0, 0, 0);
    __builtin_amdgcn_sched_barrier(0);
  }
  template <int V>
  DEVI void unit() {
    constexpr int T = st(V), n = V % 5, nx = xreq(T) - xreq(T - 1), nw = wreq(V) - wreq(V - 1);
    constexpr int vm = 2 * (xreq(T - 1) - 1 - T);
    static_assert(newer(V) <= 15, "lgkmcnt range");
    if constexpr (first(V) && V % 2 == 0) asm volatile("s_waitcnt vmcnt(%0) lgkmcnt(%1)" ::"n"(vm), "n"(newer(V)));
    else if constexpr (first(V)) asm volatile("s_waitcnt vmcnt(%0)" ::"n"(vm));
    else if constexpr (V % 2 == 0) asm volatile("s_waitcnt lgkmcnt(%0)" ::"n"(newer(V)));
    __builtin_amdgcn_sched_barrier(0);
    if constexpr (V < V1) mf<V, V & 1, V & 1>();
    else mf<V, 0, hf(V)>();
    if constexpr (nw > 0) rd<wreq(V - 1)>();
    __builtin_amdgcn_sched_barrier(0);
    if constexpr (V < V1) {
      mf<V, 1 - (V & 1), 1 - (V & 1)>();
      if constexpr (nw > 1) rd<wreq(V - 1) + 1>();
      if constexpr (nx == 2 && n < 4) xld<xreq(T - 1) + n / 2, n % 2>();
      if constexpr (nx == 1 && n == (T & 1)) xld<xreq(T - 1), 0>();
      if constexpr (nx == 1 && n == (T & 1) + 2) xld<xreq(T - 1), 1>();
      if constexpr (n == 4 && (T & 1) && ((BAR >> (T / 2)) & 1u)) bar();
    } else if constexpr (NS == 0) {
      constexpr int k = (V % 10) / 2;
      if constexpr (V % 2 == 0 && k < 2 * nx) xld<xreq(T - 1) + k / 2, k % 2>();
      if constexpr (V % 10 == 8 && nx > 0 && xreq(T) == NST)
        asm volatile("s_mov_b64 exec, 1\n\tds_add_u32 %0, %1\n\ts_mov_b64 exec, -1" ::"v"(fa), "v"(fv));
    } else {
      if constexpr (oth(V) && V < XF) asm volatile("ds_write_b128 %0, %1 offset:%2" ::"v"(ea), "v"(acc[1][(V - XU) / 2]), "n"((V - XU) * 512));
      if constexpr (V == XF) asm volatile("ds_write_b32 %0, %1" ::"v"(fa), "v"(fv));
      if constexpr (V == XF + 2) asm volatile("v_xor_b32 %0, 4, %0" : "+v"(fa));
      if constexpr (V == XR) asm volatile("ds_read_b32 %0, %1" : "+v"(fv) : "v"(fa));
      if constexpr (V == XR + 2) asm volatile("v_xor_b32 %0, 0x2000, %0" : "+v"(ea));
      if constexpr (V == XC) {
        static_assert(wreq(XR) <= XC + 1 || true, "");
        unsigned c = 1u << 18;
        asm volatile(
            "s_waitcnt lgkmcnt(%4)\n\tv_cmp_ne_u32 vcc, %3, %0\n\ts_cbranch_vccnz .Lw%=\n\t.subsection 1\n.Lw%=:\n\tds_read_b32 %0, %2\n\t"
            "s_waitcnt lgkmcnt(0)\n\tv_cmp_ne_u32 vcc, %3, %0\n\t" SAFE_OUT(".Lr%=") "s_cbranch_vccnz .Lw%=\n\ts_branch .Lr%=\n\t.subsection 0\n.Lr%=:"
            : "+v"(fv), NOP_(c)
            : "v"(fa), "s"(tag), "n"(wreq(XC) - wreq(XR))
            : "vcc", "scc");
      }
      if constexpr (oth(V) && V > XC) asm volatile("ds_read_b128 %0, %1 offset:%2" : "=v"(acc[1][V - XC - 1]) : "v"(ea), "n"((V - XC - 1) * 1024));
    }
    __builtin_amdgcn_sched_barrier(0);
  }
  template <int... V>
  DEVI void units(std::integer_sequence<int, V...>) {
    (unit<V>(), ...);
  }
  DEVI void run() {
    units(std::make_integer_sequence<int, NV>{});
    if constexpr (NS > 0) {
      asm volatile("s_waitcnt lgkmcnt(0)" : "+v"(acc[1][0]), "+v"(acc[1][1]), "+v"(acc[1][2]), "+v"(acc[1][3]), "+v"(acc[1][4]));
#pragma unroll
      for (int i = 0; i < 5; ++i) acc[0][i] += acc[1][i];
    }
  }
  template <int... V>
  DEVI void pro(std::integer_sequence<int, V...>) {
    (rd<V>(), ...);
  }
  DEVI void prow() { pro(std::make_integer_sequence<int, wreq(-1)>{}); }
  template <int T0, int... T>
  DEVI void prox(std::integer_sequence<int, T...>) {
    ((xld<T0 + T, 0>(), xld<T0 + T, 1>()), ...);
  }
  DEVI void setup(unsigned lds0, const char* xbase, int wave, int lane, unsigned red, unsigned nth) {
    const int g = lane >> 4, r = lane & 15, kq = wave & 1;
    xp[0] = xbase + wave * 256;
    xp[1] = xbase + (wave ^ 1) * 256;
    xp[2] = xp[0] + (kq ? -4096 : 4096);
    xo = (unsigned)((4 * kq + g) * 1024 + r * 16);
#pragma unroll
    for (int i = 0; i < 2 * NG; ++i) {
      wrd[i / NG][i % NG] = lds0 + (unsigned)(WOFF + (i % NG) * 3 * WS + (kq ^ (i / NG)) * 4608 + (2 * (r & 7) + 16 * (g & 1) + (g >> 1)) * 144 + (r >> 3) * 16);
      if (i < NG || NS == 0 || i % NG == ((NS / 2 + S0) % NSW) / 3) asm volatile("" : "+v"(wrd[i / NG][i % NG]));
    }
    asm volatile("" : "+v"(xo));
    if constexpr (NS > 0) {
      ea = lds0 + red + (unsigned)((wave ^ 1) * 8192 + lane * 16);
      fa = lds0 + (unsigned)(LW_OFF + (LW_PAIR + wave) * 4);
      fv = tag = nth;
      asm volatile("" : "+v"(ea), "+v"(fa), "+v"(fv));
    }
  }
  template <int N>
  DEVI void init(u32x2 (&xv)[5]) {
    asm volatile("s_waitcnt vmcnt(%5)" : "+v"(xv[0]), "+v"(xv[1]), "+v"(xv[2]), "+v"(xv[3]), "+v"(xv[4]) : "n"(N));
#pragma unroll
    for (int i = 0; i < 5; ++i) {
      acc[0][i] = f32x4{bf_lo(xv[i][0]), bf_hi(xv[i][0]), bf_lo(xv[i][1]), bf_hi(xv[i][1])};
      asm volatile("" : "+v"(acc[0][i]));
      acc[1][i] = f32x4{0.f, 0.f, 0.f, 0.f};
    }
  }
};

#define P5_XW 8
#define P5_X0 5
constexpr unsigned P5_BAR = 0x2aaau;
constexpr int P5_W = 15 * 1024;
constexpr int P2_W = K1_ST_OFF - 18 * 4608;
constexpr int P5_RED = 0;
static_assert(P5_RED % 16384 == 0 && P5_RED + 3 * 8192 + 5120 <= P5_W + 18432, "exchange area");
static_assert(P2_W >= K1_T_OFF + 144 * K1_TP + 64 && P2_W % 16 == 0 && P5_W + 8 * 18432 <= LW_OFF, "LDS placement");

struct ResIn {
  const char* xp;
  unsigned o, o2, st;
  DEVI void setup(const char* src, bool rm, int rb, int ct, int wave, int lane) {
    const int g = lane >> 4, g2 = g & 1, rl = wave * 16 + (lane & 15);
    xp = uni_ptr(rm ? src + ((size_t)rb * 64 * D_ + ct * 72) * 2 : src + (size_t)(rb * 72 + ct * 9) * 1024);
    o = rm ? (unsigned)(rl * (D_ * 2) + g * 8) : (unsigned)((g >> 1) * 1024 + rl * 16 + g2 * 8);
    o2 = rm ? (unsigned)(rl * (D_ * 2) + g2 * 8 + 128) : (unsigned)(8192 + rl * 16 + g2 * 8);
    st = rm ? 32u : 2048u;
    asm volatile("" : "+v"(o), "+v"(o2));
    asm volatile("" : "+s"(xp));
  }
  DEVI void load(u32x2 (&xv)[5]) {
#pragma unroll
    for (int i = 0; i < 4; ++i) asm volatile("global_load_dwordx2 %0, %1, %2 sc1" : "=v"(xv[i]) : "v"(o), "s"(xp + i * st));
    asm volatile("global_load_dwordx2 %0, %1, %2 sc1" : "=v"(xv[4]) : "v"(o2), "s"(xp));
  }
};

DEVI void p2_gemm(f32x4 (&acc)[2][5], u32x2 (&xv)[5], unsigned lds0, const char* ob, int layer, int wave, int lane MK_ARG) {
  ResX<9, 0, 5, P2_W, 9, P2_X0, 0, 0u> gm(acc);
  gm.setup(lds0, ob, wave, lane, 0u, 0u);
  unsigned a = (gm.xo & ~1023u) | ((gm.xo & 1023u) >> 2);
  gm.xo += 8192u;
  gm.fa = lds0 + (unsigned)(LW_OFF + (LW_T + layer) * 4);
  gm.fv = vconst(1u);
  asm volatile("" : "+v"(a), "+v"(gm.xo), "+v"(gm.fa));
  const char* pp[2] = {gm.xp[0] - wave * 192, gm.xp[2] - wave * 192};
  place<AL_P2>();
  bar();
  MK(6);
  unsigned e[2][4];
#pragma unroll
  for (int d = 0; d < 8; ++d) {
    if (NOPL && (d & 3)) e[d / 4][d % 4] = 0x3c003c00u;
    else asm volatile("global_load_dword %0, %1, %2 offset:%3 sc1" : "=v"(e[d / 4][d % 4]) : "v"(a), "s"(pp[d / 4]), "n"((d % 4) * 256));
  }
  gm.template prox<1>(std::make_integer_sequence<int, P2_X0 - 1>{});
  if (P2_X0 == 9) lds_inc(lds0 + (unsigned)(LW_OFF + (LW_T + layer) * 4));
  gm.prow();
  gm.template init<6 + 2 * P2_X0>(xv);
  asm volatile("s_waitcnt vmcnt(%8)"
               : "+v"(e[0][0]), "+v"(e[0][1]), "+v"(e[0][2]), "+v"(e[0][3]), "+v"(e[1][0]), "+v"(e[1][1]), "+v"(e[1][2]), "+v"(e[1][3])
               : "n"(2 * P2_X0 - 2));
  gm.xf[0][0] = __builtin_bit_cast(s16x8, u32x4{e[0][0], e[0][1], e[0][2], e[0][3]});
  gm.xf[0][1] = __builtin_bit_cast(s16x8, u32x4{e[1][0], e[1][1], e[1][2], e[1][3]});
  __builtin_amdgcn_sched_barrier(0);
  gm.run();
}

struct ResOut {
  const char *xp[3], *sp;
  unsigned xo, so;
  DEVI void setup(char* xim, char* xs, u16* out, bool fin, int rb, int ct, int wave, int lane) {
    const int g = lane >> 4, rl = wave * 16 + (lane & 15);
    xp[0] = uni_ptr(fin ? (const char*)(out + (size_t)rb * 64 * D_ + ct * 72) : xim + (size_t)(rb * 72 + ct * 9) * 1024);
    xp[1] = xp[0] + (fin ? 64 : 4096);
    xp[2] = xp[0] + (fin ? 128 : 8192);
    sp = uni_ptr(xs + (size_t)(rb * 8 + ct) * 512);
    xo = (unsigned)(((g & 1) * 2 + (g >> 1)) * (fin ? 16 : 1024) + rl * (fin ? D_ * 2 : 16));
    so = (unsigned)(rl * 8);
    asm volatile("" : "+v"(xo), "+v"(so), "+s"(xp[0]), "+s"(xp[1]), "+s"(xp[2]), "+s"(sp));
  }
  DEVI void run(const f32x4 (&a)[5], bool fin) {
    u32x2 y[5];
    u32x4 v[3];
#pragma unroll
    for (int nt = 0; nt < 5; ++nt) y[nt] = u32x2{pack2(a[nt][0], a[nt][1]), pack2(a[nt][2], a[nt][3])};
#pragma unroll
    for (int i = 0; i < 3; ++i) {
      const auto s0 = __builtin_amdgcn_permlane16_swap(y[2 * i][0], y[i < 2 ? 2 * i + 1 : 4][0], false, false);
      const auto s1 = __builtin_amdgcn_permlane16_swap(y[2 * i][1], y[i < 2 ? 2 * i + 1 : 4][1], false, false);
      v[i] = u32x4{s0[0], s1[0], s0[1], s1[1]};
    }
    if (fin) {
#define OUTM 0
#define OUTST ""
      asm volatile("global_store_dwordx4 %0, %1, %4" OUTST "\n\tglobal_store_dwordx4 %0, %2, %5" OUTST "\n\ts_mov_b64 exec, 0xffff\n\tglobal_store_dwordx4 %0, %3, %6" OUTST "\n\t"
                   "s_mov_b64 exec, -1\n\ts_nop 0" ::"v"(xo), "v"(v[0]), "v"(v[1]), "v"(v[2]), "s"(xp[0]), "s"(xp[1]), "s"(xp[2])
                   : "memory");
      return;
    }
    asm volatile("global_store_dwordx4 %0, %1, %4 sc1\n\tglobal_store_dwordx4 %0, %2, %5 sc1\n\ts_mov_b64 exec, 0xffff\n\t"
                 "global_store_dwordx4 %0, %3, %6 sc1\n\ts_mov_b64 exec, -1\n\ts_nop 0" ::"v"(xo), "v"(v[0]), "v"(v[1]), "v"(v[2]),
                 "s"(xp[0]), "s"(xp[1]), "s"(xp[2])
                 : "memory");
    const u32x4 one4 = {vconst(0x3f803f80u), vconst(0x3f803f80u), vconst(0x3f803f80u), vconst(0x3f803f80u)};
    const bool lo = __builtin_amdgcn_inverse_ballot_w64(0xffffull);
    v[2] = u32x4{lo ? v[2][0] : 0u, lo ? v[2][1] : 0u, lo ? v[2][2] : 0u, lo ? v[2][3] : 0u};
    f32x4 s = {0.f, 0.f, 0.f, 0.f}, q = s;
#pragma unroll
    for (int i = 0; i < 3; ++i) {
      const s16x8 u = __builtin_bit_cast(s16x8, v[i]);
      q = __builtin_amdgcn_mfma_f32_16x16x32_bf16(u, u, q, 0, 0, 0);
      s = __builtin_amdgcn_mfma_f32_16x16x32_bf16(__builtin_bit_cast(s16x8, one4), u, s, 0, 0, 0);
    }
    float q0 = q[0], q1 = q[1], q2 = q[2], q3 = q[3];
    const bool b0 = __builtin_amdgcn_inverse_ballot_w64(0xaaaaaaaaaaaaaaaaull), b1 = __builtin_amdgcn_inverse_ballot_w64(0xccccccccccccccccull);
    q0 = b0 ? q1 : q0;
    q2 = b0 ? q3 : q2;
    asm("" : "+v"(q0), "+v"(q2));
    const f32x2 sq = {s[0], b1 ? q2 : q0};
    asm volatile("s_mov_b64 exec, %3\n\tglobal_store_dwordx2 %0, %1, %2 sc1\n\ts_mov_b64 exec, -1" ::"v"(so), "v"(sq), "s"(sp), "s"(0xf0000f0000f0000full) : "memory");
  }
};

using K3C = GemmCfg<128, 144>;
constexpr int K3_Z1_OFF = K3C::LDS_BYTES;
constexpr int K3_C2_OFF = K3_Z1_OFF + K3C::XB;
constexpr int K3_ZC_OFF = K3_C2_OFF + 2048;
constexpr int K3_DM_OFF = K3_ZC_OFF + 1024;
constexpr int K3_ST_OFF = K3_Z1_OFF + 4 * 5120;
constexpr int K3_C1_OFF = K3_ST_OFF + 2048;
constexpr int K3_BS_OFF = K3_C1_OFF + 576;
static_assert(K3_BS_OFF + 512 <= LW_OFF, "K3 LDS");
static_assert(P5_W + 19 * 1024 == K3C::SB && K3C::SB == 34 * 1024, "w_out ring");
constexpr int K3_D = 4;

DEVI void red4(float (&t)[4], float& s, float& q) {
#pragma unroll
  for (int j = 0; j < 2; ++j) {
    const auto p = __builtin_amdgcn_permlane16_swap(__float_as_uint(t[j]), __float_as_uint(t[2 + j]), false, false);
    const unsigned v = __float_as_uint(__uint_as_float(p[0]) + __uint_as_float(p[1]));
    const auto w = __builtin_amdgcn_permlane32_swap(v, v, false, false);
    t[j] = __uint_as_float(w[0]) + __uint_as_float(w[1]);
    asm volatile("" : "+v"(t[j]));
  }
  s = t[0];
  q = t[1];
}
DEVI void red8(const f32x4& a, const f32x4& b, float& s, float& q) {
  float t[4];
#pragma unroll
  for (int j = 0; j < 4; ++j) {
    t[j] = a[j] + b[j];
    asm volatile("" : "+v"(t[j]));
  }
  red4(t, s, q);
}
DEVI void red16(const f32x4& a, const f32x4& b, const f32x4& c, const f32x4& d, float& s, float& q) {
  float t[4];
#pragma unroll
  for (int j = 0; j < 4; ++j) {
    float u = a[j] + b[j], v = c[j] + d[j];
    asm volatile("" : "+v"(u), "+v"(v));
    t[j] = u + v;
    asm volatile("" : "+v"(t[j]));
  }
  red4(t, s, q);
}

constexpr int b_k(int s) { return s < 2 ? s : (s < 4 ? 5 + s : (s < 6 ? 1 + s : (s == 6 ? 4 : s - 5))); }
template <bool B>
struct UMath {
  static constexpr int D = 4, R = 6, NV = 162, SB = K3C::SB, P = K3C::P;
  s16x8 wf[R], xf[2][2];
  s16x8 (&xc)[4][2][2];
  f32x4 (&acc)[2][9];
  f32x4 rc[4][2];
  f32x2 cs[9];
  unsigned ra, ca;
  template <int I>
  DEVI void cread() {
    if constexpr (I < 8) asm volatile("ds_read_b128 %0, %1 offset:%2" : "=v"(rc[I / 2][I % 2]) : "v"(ra), "n"((I / 2) * 512 + (I % 2) * 16));
    else if constexpr (I < 17) asm volatile("ds_read_b64 %0, %1 offset:%2" : "=v"(cs[I - 8]) : "v"(ca), "n"((I - 8) * 128));
  }
  unsigned xa, wa;
  unsigned fa, fw, fv;
  DEVI UMath(f32x4 (&a)[2][9], s16x8 (&x)[4][2][2]) : xc(x), acc(a) {}
  static constexpr int wslot(int s) { return B ? (s + 1) % 4 : s % 4; }
  static constexpr int xslot(int s) { return B ? b_k(s) % 5 : s % 5; }
  static constexpr bool res(int q) { return !B || b_k(q / 2) >= 4; }
#define BPS 9
  static constexpr bool chk(int s) { return s >= 3 && s < 7 && s != BPS; }
  static constexpr bool xop(int v) { return v + D < NV && (v + D) % 9 < 2 && res((v + D) / 9); }
  static constexpr int ops1(int v) { return (xop(v) || (B && ((v % 18 == 9 && chk(v / 18)) || (v % 18 == 0 && v > 0)))) ? 1 : 0; }
  static constexpr int newer(int V) {
    int v0 = (V % 9 == 8 ? V : V + 1) - D, n = 0;
    if (v0 >= 0 && !(V % 9 == 0 && res(V / 9))) n += ops1(v0);
    for (int v = v0 + 1; v < V; ++v)
      if (v >= 0) n += (v + D < NV ? 1 : (B ? 0 : 4)) + ops1(v);
    return n;
  }
  template <int V>
  DEVI void wread() {
    asm volatile("ds_read_b128 %0, %1 offset:%2" : "=v"(wf[V % R]) : "v"(wa), "n"((wslot(V / 18) & 1) * SB + ((V % 18) / 9) * 32 * P + (V % 9) * 32));
  }
  template <int Q, int M>
  DEVI void xread() {
    asm volatile("ds_read_b128 %0, %1 offset:%2" : "=v"(xf[Q & 1][M]) : "v"(xa), "n"((xslot(Q / 2) & 1) * SB + (Q & 1) * 8192 + M * (B ? 256 : 64)));
  }
  template <int V, int M_>
  DEVI void mf() {
    constexpr int M = ((V % 9) & 1) ? 1 - M_ : M_, q = V / 9;
    if constexpr (!B) acc[M][V % 9] = __builtin_amdgcn_mfma_f32_16x16x32_bf16(xf[q & 1][M], wf[V % R], acc[M][V % 9], 0, 0, 0);
    else if constexpr (res(q)) acc[M][V % 9] = __builtin_amdgcn_mfma_f32_16x16x32_bf16(wf[V % R], xf[q & 1][M], acc[M][V % 9], 0, 0, 0);
    else acc[M][V % 9] = __builtin_amdgcn_mfma_f32_16x16x32_bf16(wf[V % R], xc[b_k(q / 2)][q & 1][M], acc[M][V % 9], 0, 0, 0);
    __builtin_amdgcn_sched_barrier(0);
  }
  template <int V>
  DEVI void unit() {
    constexpr int U = V % 18, s = V / 18;
    static_assert(newer(V) <= 15, "lgkmcnt range");
    if constexpr ((V % 9) % 2 == 0 && V > 0) asm volatile("s_waitcnt lgkmcnt(%0)" ::"n"(newer(V)));
    __builtin_amdgcn_sched_barrier(0);
    mf<V, 0>();
    if constexpr (V + D < NV) wread<V + D>();
    if constexpr (!B && V + D >= NV) {
      cread<(V + D - NV) * 4>();
      cread<(V + D - NV) * 4 + 1>();
    }
    if constexpr (s < 8 && U == 13 && (wslot(s + 1) >> 1) != (wslot(s) >> 1))
      asm volatile("v_add_u32 %0, %1, %0" : "+v"(wa) : "n"(((wslot(s + 1) >> 1) - (wslot(s) >> 1)) * 2 * SB));
    __builtin_amdgcn_sched_barrier(0);
    mf<V, 1>();
    if constexpr (xop(V)) xread<(V + D) / 9, (V + D) % 9>();
    if constexpr (!B && V + D >= NV) {
      cread<(V + D - NV) * 4 + 2>();
      cread<(V + D - NV) * 4 + 3>();
      if constexpr (V == NV - 1) cread<16>();
    }
    if constexpr (s < 8) {
      if constexpr (U == 11 && res(2 * s + 2) && (xslot(s + 1) >> 1) != (xslot(s) >> 1))
        asm volatile("v_add_u32 %0, %1, %0" : "+v"(xa) : "n"(((xslot(s + 1) >> 1) - (xslot(s) >> 1)) * 2 * SB));
      if constexpr (!B && U == 13) __builtin_amdgcn_s_barrier();
      if constexpr (B && U == 9 && chk(s)) asm volatile("ds_read_u8 %0, %1 offset:%2" : "+v"(fv) : "v"(fa), "n"(s == 6 ? 0 : 8));
      unsigned n = 1u << 18;
      if constexpr (B && U == 13 && chk(s)) {
        asm volatile(
            "v_cmp_gt_u32 vcc, %3, %0\n\ts_cbranch_vccnz .Lw%=\n\t.subsection 1\n.Lw%=:\n\tds_read_u8 %0, %2 offset:%4\n\ts_waitcnt lgkmcnt(0)\n\t"
            "v_cmp_gt_u32 vcc, %3, %0\n\t" SAFE_OUT(".Lr%=") "s_cbranch_vccnz .Lw%=\n\ts_branch .Lr%=\n\t.subsection 0\n.Lr%=:"
            : "+v"(fv), NOP_(n)
            : "v"(fa), "n"(s == 6 ? 4 : s + 2), "n"(s == 6 ? 0 : 8)
            : "vcc", "scc");
      }
      if constexpr (B && U == 13 && s == BPS) {
        asm volatile(
            ".La%=:\n\ts_sleep 1\n\tds_read_u8 %0, %2\n\ts_waitcnt lgkmcnt(0)\n\tv_cmp_gt_u32 vcc, 3, %0\n\t" SAFE_OUT(".Lr%=") "s_cbranch_vccnz .La%=\n"
            ".Lb%=:\n\tds_read_u8 %0, %2 offset:8\n\ts_waitcnt lgkmcnt(0)\n\tv_cmp_gt_u32 vcc, %3, %0\n\t" SAFE_OUT(".Lr%=") "s_cbranch_vccnz .Lb%=\n.Lr%=:"
            : "+v"(fv), NOP_(n)
            : "v"(fa), "n"(s + 2)
            : "vcc", "scc");
      }
    }
    if constexpr (B && U == 15) asm volatile("v_mov_b32 %0, %1" : "+v"(fv) : "n"(s + 1));
    if constexpr (B && ((U == 0 && s > 0) || V == NV - 1)) asm volatile("ds_write_b8 %0, %1" ::"v"(fw), "v"(fv));
    __builtin_amdgcn_sched_barrier(0);
  }
  template <int... V>
  DEVI void units(std::integer_sequence<int, V...>) {
    (unit<V>(), ...);
  }
  DEVI void first() {
    if constexpr (!B) {
      xread<0, 0>();
      xread<0, 1>();
    }
    wread<0>();
    wread<1>();
    wread<2>();
    wread<3>();
    asm volatile("s_waitcnt lgkmcnt(0)");
  }
  DEVI void run() {
    units(std::make_integer_sequence<int, NV>{});
#pragma unroll
    for (int m = 0; m < 2; ++m)
#pragma unroll
      for (int n = 0; n < 9; ++n) asm volatile("" : "+v"(acc[m][n]));
  }
};

#define PACE 0
#define G0P 2
#define ZSLEEP 6
DEVI void p3_g0(char* smem, char* __restrict__ ws, int layer, int mi, int ni, int wm, int lane MK_ARG) {
  constexpr int SB = K3C::SB;
  const unsigned lds0 = (unsigned)(size_t)smem;
  const int g = lane >> 4, r = lane & 15;
  const unsigned lw = lds0 + (unsigned)(LW_OFF + layer * 4), me = 1u << (8 * wm);
  f32x4 ac[2][9];
#pragma unroll
  for (int mt = 0; mt < 2; ++mt)
#pragma unroll
    for (int j = 0; j < 9; ++j) ac[mt][j] = f32x4{0.f, 0.f, 0.f, 0.f};
  unsigned ra = lds0 + (unsigned)(K3_ST_OFF + (wm * 32 + g * 8) * 4), ca = lds0 + (unsigned)(K3_C2_OFF + r * 8);
  asm volatile("" : "+v"(ra), "+v"(ca));
  s16x8 xc[4][2][2];
  UMath<false> gm(ac, xc);
  gm.ra = ra;
  gm.ca = ca;
  gm.xa = lds0 + (unsigned)(g * 2048 + (wm * 32 + 8 * (r >> 2) + (r & 3)) * 16);
  gm.wa = lds0 + (unsigned)(K3C::XB + (g * 8 + (r & 7)) * K3C::P + (r >> 3) * 16);
  unsigned adw = lw + LW_AD * 4, mev = vconst(me), po = (unsigned)(ni * 1024 + lane * 4);
  const char* zp = uni_ptr(ws + OFF_ZP + (size_t)layer * SZ_ZP + (size_t)mi * 16384 + wm * 256);
  asm volatile("" : "+v"(gm.xa), "+v"(gm.wa), "+v"(adw), "+v"(po));
  asm volatile("" : "+s"(zp));
  place<AL_A>();
  bar();
  gm.first();
  gm.run();
  asm volatile("s_mov_b64 exec, 1\n\tds_add_u32 %0, %1\n\ts_mov_b64 exec, -1" ::"v"(adw), "v"(mev) : "memory");
  MK(11);
  f32x2 z[9][4];
  f32x2 sm[4], sq[4];
  {
    f32x4 (&rc)[4][2] = gm.rc;
    f32x2 (&cs)[9] = gm.cs;
    asm volatile("s_waitcnt lgkmcnt(1)"
                 : "+v"(rc[0][0]), "+v"(rc[0][1]), "+v"(rc[1][0]), "+v"(rc[1][1]), "+v"(rc[2][0]), "+v"(rc[2][1]), "+v"(rc[3][0]), "+v"(rc[3][1]));
#pragma unroll
    for (int j = 0; j < 9; ++j) asm volatile("" : "+v"(cs[j]));
    auto RC = [&](int k, int i) { return f32x2{rc[k][i / 2][(i % 2) * 2], rc[k][i / 2][(i % 2) * 2 + 1]}; };
#pragma unroll
    for (int j = 0; j < 9; ++j) {
      f32x2 u[4], w[4];
#pragma unroll
      for (int i = 0; i < 4; ++i) u[i] = __builtin_elementwise_fma(RC(0, i), cs[j], f32x2{ac[i / 2][j][(i % 2) * 2], ac[i / 2][j][(i % 2) * 2 + 1]});
#pragma unroll
      for (int i = 0; i < 4; ++i) w[i] = u[i] * u[i];
#pragma unroll
      for (int i = 0; i < 4; ++i) w[i] = __builtin_elementwise_fma(w[i], RC(3, i), RC(2, i));
#pragma unroll
      for (int i = 0; i < 4; ++i) w[i] = u[i] * w[i];
      __builtin_amdgcn_sched_barrier(0);
#pragma unroll
      for (int i = 0; i < 4; ++i) w[i] = f32x2{__builtin_amdgcn_exp2f(w[i][0]), __builtin_amdgcn_exp2f(w[i][1])};
      __builtin_amdgcn_sched_barrier(0);
#pragma unroll
      for (int i = 0; i < 4; ++i) w[i] = __builtin_elementwise_fma(w[i], RC(1, i), RC(1, i));
      __builtin_amdgcn_sched_barrier(0);
#pragma unroll
      for (int i = 0; i < 4; ++i) w[i] = f32x2{__builtin_amdgcn_rcpf(w[i][0]), __builtin_amdgcn_rcpf(w[i][1])};
      __builtin_amdgcn_sched_barrier(0);
#pragma unroll
      for (int i = 0; i < 4; ++i) z[j][i] = u[i] * w[i];
#pragma unroll
      for (int i = 0; i < 4; ++i) {
        if (j == 1) sm[i] = z[0][i] + z[1][i];
        if (j > 1) sm[i] += z[j][i];
      }
#pragma unroll
      for (int i = 0; i < 4; ++i) sq[i] = j ? __builtin_elementwise_fma(z[j][i], z[j][i], sq[i]) : z[j][i] * z[j][i];
      asm volatile("" : "+v"(z[j][0]), "+v"(z[j][1]), "+v"(z[j][2]), "+v"(z[j][3]));
      __builtin_amdgcn_sched_barrier(0);
    }
  }
  if (BEARLY) asm volatile("s_mov_b64 exec, 1\n\tds_add_u32 %0, %1\n\ts_mov_b64 exec, -1" ::"v"(adw), "v"(mev) : "memory");
  f32x4 e;
  {
    float v[4][4], d[2][4], f[4];
#pragma unroll
    for (int i = 0; i < 4; ++i) {
      v[i][0] = sm[i][0];
      v[i][1] = sq[i][0];
      v[i][2] = sm[i][1];
      v[i][3] = sq[i][1];
    }
    asm volatile("s_nop 1" : "+v"(v[3][1]), "+v"(v[3][3]), "+v"(v[2][1]), "+v"(v[2][3]));
#pragma unroll
    for (int i = 0; i < 2; ++i)
#pragma unroll
      for (int c = 0; c < 4; ++c)
        asm volatile("v_add_f32_dpp %0, %1, %1 row_ror:8 row_mask:0xf bank_mask:0x3\n\tv_add_f32_dpp %0, %2, %2 row_ror:8 row_mask:0xf bank_mask:0xc"
                     : "=&v"(d[i][c])
                     : "v"(v[i][c]), "v"(v[i + 2][c]));
#pragma unroll
    for (int c = 0; c < 4; ++c)
      asm volatile("v_add_f32_dpp %0, %1, %1 row_shl:4 row_mask:0xf bank_mask:0x5\n\tv_add_f32_dpp %0, %2, %2 row_shr:4 row_mask:0xf bank_mask:0xa"
                   : "=&v"(f[c])
                   : "v"(d[0][c]), "v"(d[1][c]));
    asm volatile(
        "v_add_f32_dpp %0, %0, %0 quad_perm:[1,0,3,2] row_mask:0xf bank_mask:0xf\n\t"
        "v_add_f32_dpp %1, %1, %1 quad_perm:[1,0,3,2] row_mask:0xf bank_mask:0xf\n\t"
        "v_add_f32_dpp %2, %2, %2 quad_perm:[1,0,3,2] row_mask:0xf bank_mask:0xf\n\t"
        "v_add_f32_dpp %3, %3, %3 quad_perm:[1,0,3,2] row_mask:0xf bank_mask:0xf\n\t"
        "v_add_f32_dpp %0, %0, %0 quad_perm:[2,3,0,1] row_mask:0xf bank_mask:0xf\n\t"
        "v_add_f32_dpp %1, %1, %1 quad_perm:[2,3,0,1] row_mask:0xf bank_mask:0xf\n\t"
        "v_add_f32_dpp %2, %2, %2 quad_perm:[2,3,0,1] row_mask:0xf bank_mask:0xf\n\t"
        "v_add_f32_dpp %3, %3, %3 quad_perm:[2,3,0,1] row_mask:0xf bank_mask:0xf"
        : "+v"(f[0]), "+v"(f[1]), "+v"(f[2]), "+v"(f[3]));
    e = f32x4{f[0], __uint_as_float(__float_as_uint(f[1]) & 0x7fffffffu), f[2], __uint_as_float(__float_as_uint(f[3]) & 0x7fffffffu)};
  }
  asm volatile("s_mov_b64 exec, %3\n\tglobal_store_dwordx4 %0, %1, %2 sc1\n\ts_mov_b64 exec, -1\n\ts_nop 0" ::"v"(po), "v"(e), "s"(zp),
               "s"(0x1111111111111111ull)
               : "memory");
  if (!BEARLY) asm volatile("s_mov_b64 exec, 1\n\tds_add_u32 %0, %1\n\ts_mov_b64 exec, -1" ::"v"(adw), "v"(mev) : "memory");
  __builtin_amdgcn_s_setprio(2);
  MK(12);
  const char* dst = uni_ptr(ws + OFF_Z2 + (size_t)layer * SZ_Z + (size_t)ni * S_ * 288 + (size_t)(mi * 4 + wm) * 9216 + 4096);
  const unsigned lane16 = (unsigned)lane * 16u;
  u32x4 wb[9];
  {
    static_assert(b_k(7) == 2 && b_k(8) == 3, "order");
    const char* wp = uni_ptr(ws + OFF_PI + (size_t)ni * (18 * K3C::WB) + 11 * K3C::WB + wm * 9216 + 4096);
#pragma unroll
    for (int j = 0; j < 8; ++j) asm volatile("global_load_dwordx4 %0, %1, %2 offset:%3 sc1" : "=v"(wb[j]) : "v"(lane16), "s"(wp), "n"(j * 1024 - 4096) : "memory");
    asm volatile("global_load_dwordx4 %0, %1, %2 sc1" : "=v"(wb[8]) : "v"(lane16 + 4096u), "s"(wp) : "memory");
  }
  u32x4 y[9];
  {
    const unsigned src = (unsigned)(g * 4096 + r * 16);
    unsigned zw = lds0 + (unsigned)(K3_ZC_OFF + (wm * 32 + 2 * r + (g & 1)) * 4), zr = lds0 + (unsigned)(K3_ZC_OFF + (wm * 32 + g * 8) * 4);
    asm volatile("" : "+v"(zw), "+v"(zr));
    f32x4 a, b, c, d;
    __builtin_amdgcn_s_sleep(ZSLEEP);
#pragma unroll 1
    for (unsigned spin = 0; spin < SPIN_MEM; ++spin) {
      asm volatile(
          "global_load_dwordx4 %0, %4, %5 sc1\n\t"
          "global_load_dwordx4 %1, %4, %5 offset:1024 sc1\n\t"
          "global_load_dwordx4 %2, %4, %5 offset:2048 sc1\n\t"
          "global_load_dwordx4 %3, %4, %5 offset:3072 sc1\n\t"
          "s_waitcnt vmcnt(0)"
          : "=&v"(a), "=&v"(b), "=&v"(c), "=&v"(d)
          : "v"(src), "s"(zp)
          : "memory");
      const unsigned bad = (__float_as_uint(a[1]) | __float_as_uint(a[3]) | __float_as_uint(b[1]) | __float_as_uint(b[3]) |
                            __float_as_uint(c[1]) | __float_as_uint(c[3]) | __float_as_uint(d[1]) | __float_as_uint(d[3])) >> 31;
      if (!__any(bad)) break;
    }
    MK(13);
    float s, q, zmb, zi;
    red16(a, b, c, d, s, q);
    ln_finish(s, q, 1.0f / (float)FH_, zmb, zi);
    f32x4 R[2], M[2];
    asm volatile(
        "ds_write2st64_b32 %4, %5, %6 offset1:2\n\t"
        "ds_read_b128 %0, %7\n\tds_read_b128 %2, %7 offset:512\n\tds_read_b128 %1, %7 offset:16\n\tds_read_b128 %3, %7 offset:528\n\t"
        "s_waitcnt lgkmcnt(0)"
        : "=&v"(R[0]), "=&v"(R[1]), "=&v"(M[0]), "=&v"(M[1])
        : "v"(zw), "v"(zi), "v"(-zmb * zi), "v"(zr)
        : "memory");
    MK(28);
#define NRM 0
    auto st = [&](int j) {
      if (j < 8) asm volatile("global_store_dwordx4 %0, %1, %2 offset:%3 sc1\n\ts_nop 1" ::"v"(lane16), "v"(y[j]), "s"(dst), "n"(j * 1024 - 4096) : "memory");
      else asm volatile("global_store_dwordx4 %0, %1, %2 sc1\n\ts_nop 1" ::"v"(lane16 + 4096u), "v"(y[j]), "s"(dst) : "memory");
    };
#pragma unroll
    for (int j = 0; j < 9; ++j) {
      if (NRM & 2) {
        f32x2 t[4];
#pragma unroll
        for (int k = 0; k < 4; ++k)
          t[k] = __builtin_elementwise_fma(z[j][k], f32x2{R[k / 2][(k % 2) * 2], R[k / 2][(k % 2) * 2 + 1]}, f32x2{M[k / 2][(k % 2) * 2], M[k / 2][(k % 2) * 2 + 1]});
        y[j] = u32x4{pack2v(t[0]), pack2v(t[1]), pack2v(t[2]), pack2v(t[3])};
      } else {
        float t[8];
#pragma unroll
        for (int k = 0; k < 8; ++k) {
          t[k] = __builtin_fmaf(z[j][k / 2][k % 2], R[k / 4][k % 4], M[k / 4][k % 4]);
          asm volatile("" : "+v"(t[k]));
        }
        y[j] = u32x4{pack2(t[0], t[1]), pack2(t[2], t[3]), pack2(t[4], t[5]), pack2(t[6], t[7])};
      }
      if (NRM & 1) asm volatile("" : "+v"(y[j]));
      else st(j);
      __builtin_amdgcn_sched_barrier(0);
    }
    MK(29);
    if (NRM & 1) {
#pragma unroll
      for (int j = 0; j < 9; ++j) st(j);
    }
  }
  lds_inc(lw + LW_AD * 4, me);
  MK(24);
  __builtin_amdgcn_s_setprio(G0P);
  lds_spin4(lw + LW_S * 4, 4u + (unsigned)(wm >> 1));
  {
    const unsigned fa = lds0 + (unsigned)((wm >> 1) * SB + K3C::XB + (wm & 1) * 9216) + lane16;
#pragma unroll
    for (int j = 0; j < 9; ++j) {
      asm volatile("ds_write_b128 %0, %1 offset:%2" ::"v"(fa), "v"(wb[j]), "n"(j * 1024) : "memory");
      if (PACE && j < 8) __builtin_amdgcn_s_sleep(PACE);
    }
    lds_inc(lw + LW_AD * 4, me);
  }
  announce(lw + LW_Z * 4, 4u, ws, CN_Z + layer, lane * 16 + ni, lane < 16);
  lds_inc(lw + LW_AD * 4, me);
  MK(27);
  f32x4 acc[2][9];
  {
    float bs[2];
    asm volatile("ds_read2_b32 %0, %1 offset1:16\n\ts_waitcnt lgkmcnt(0)"
                 : "=v"(*reinterpret_cast<f32x2*>(bs))
                 : "v"(lds0 + (unsigned)(K3_BS_OFF + (wm * 32 + r) * 4))
                 : "memory");
    lds_spin4(lw + LW_S * 4, 6u + (unsigned)(wm >> 1));
    const unsigned fa = lds0 + (unsigned)((2 + (wm >> 1)) * SB + K3C::XB + (wm & 1) * 9216) + lane16;
#pragma unroll
    for (int j = 0; j < 9; ++j) {
      asm volatile("ds_write_b128 %0, %1 offset:%2" ::"v"(fa), "v"(y[j]), "n"(j * 1024) : "memory");
      if (PACE && j < 8) __builtin_amdgcn_s_sleep(PACE);
    }
    lds_inc(lw + LW_AD * 4, me);
#pragma unroll
    for (int mt = 0; mt < 2; ++mt) {
      f32x4 b4 = {bs[mt], bs[mt], bs[mt], bs[mt]};
      asm volatile("" : "+v"(b4));
#pragma unroll
      for (int j = 0; j < 9; ++j) acc[mt][j] = b4;
    }
  }
  u32x4 z1[9];
  const char* hp = uni_ptr(ws + OFF_H + (size_t)layer * SZ_Z + (size_t)((mi * 2 + (wm >> 1)) * 288 + ni * 18) * 1024);
  unsigned ho = (unsigned)((lane >> 5) * 1024 + ((wm & 1) * 32 + (lane & 31)) * 16);
  unsigned zb = lds0 + (unsigned)(wm * 4096) + lane16, zf = lw + (unsigned)(LW_S * 4 + wm);
  asm volatile("" : "+v"(ho), "+v"(zb), "+v"(zf));
  asm volatile("" : "+s"(hp));
  {
    GemmMath<K3C, 2, 9, K3_D, 6, false, 2> gm(acc);
    gm.setup(lds0, wm * 2, 0, lane, (unsigned)(2 * SB));
    asm volatile("" : "+v"(gm.xrd[0]), "+v"(gm.xrd[1]), "+v"(gm.wrd), "+v"(gm.xa[0]), "+v"(gm.xa[1]), "+v"(gm.wa));
    MK(15);
    lds_spin4(lw + LW_L * 4, 8u);
    lds_spin4(lw + LW_AD * 4, 6u);
    MK(17);
    gm.first();
    __builtin_amdgcn_s_setprio(0);
    gm.template mid<1, false>();
    gm.template mid<11, true, AL_C>();
    const unsigned za = gm.wrd + (unsigned)(K3_Z1_OFF - K3C::XB + wm * 5120);
#pragma unroll
    for (int j = 0; j < NR1; ++j) asm volatile("ds_read_b128 %0, %1 offset:%2" : "=v"(z1[j]) : "v"(za), "n"(j * 1024));
    gm.template mid<19, true, AL_C>();
    gm.last();
  }
  MK(16);
  if (NR1 < 5) lds_inc(lw + LW_AD * 4, me);
  unsigned zv;
  u32x4 zl[4];
#pragma unroll
  for (int j = 0; j < 9; ++j) {
    const bool sec = j >= NR1 + 3;
    if (j == NR1 || j == NR1 + 4) {
      unsigned n = 1u << 18;
      asm volatile(
          "s_waitcnt lgkmcnt(0)\n\tv_cmp_gt_u32 vcc, %8, %0\n\ts_cbranch_vccnz .Lw%=\n\t.subsection 1\n.Lw%=:\n\tds_read_u8 %0, %6\n\tds_read_b128 %2, %7 offset:%9\n\t"
          "ds_read_b128 %3, %7 offset:%9+1024\n\tds_read_b128 %4, %7 offset:%9+2048\n\tds_read_b128 %5, %7 offset:%9+3072\n\ts_waitcnt lgkmcnt(0)\n\t"
          "v_cmp_gt_u32 vcc, %8, %0\n\t" SAFE_OUT(".Lr%=") "s_cbranch_vccnz .Lw%=\n\ts_branch .Lr%=\n\t.subsection 0\n.Lr%=:"
          : "+v"(zv), NOP_(n), "+v"(zl[0]), "+v"(zl[1]), "+v"(zl[2]), "+v"(zl[3])
          : "v"(zf), "v"(zb), "n"(sec ? 11 : 10), "n"(sec ? SB : 0)
          : "memory", "vcc", "scc");
#pragma unroll
      for (int k = 0; k < 4; ++k)
        if (j + k < 9) z1[j + k] = zl[k];
    }
    if (j == NR1 - 1 || (j == NR1 + 3 && j < 8))
      asm volatile("ds_read_u8 %0, %5\n\tds_read_b128 %1, %6 offset:%7\n\tds_read_b128 %2, %6 offset:%7+1024\n\tds_read_b128 %3, %6 offset:%7+2048\n\t"
                   "ds_read_b128 %4, %6 offset:%7+3072"
                   : "=&v"(zv), "=&v"(zl[0]), "=&v"(zl[1]), "=&v"(zl[2]), "=&v"(zl[3])
                   : "v"(zf), "v"(zb), "n"(sec ? SB : 0)
                   : "memory");
    asm volatile("" : "+v"(z1[j]));
    u32x2 h[2];
#pragma unroll
    for (int mt = 0; mt < 2; ++mt) {
      const f32x4 a = acc[mt][j];
      h[mt] = u32x2{pack2v(f32x2{bf_lo(z1[j][2 * mt]), bf_hi(z1[j][2 * mt])} * f32x2{a[0], a[1]}),
                    pack2v(f32x2{bf_lo(z1[j][2 * mt + 1]), bf_hi(z1[j][2 * mt + 1])} * f32x2{a[2], a[3]})};
    }
    const auto s0 = __builtin_amdgcn_permlane16_swap(h[0][0], h[1][0], false, false);
    const auto s1 = __builtin_amdgcn_permlane16_swap(h[0][1], h[1][1], false, false);
    const u32x4 v = {s0[0], s1[0], s0[1], s1[1]};
    asm volatile("global_store_dwordx4 %0, %1, %2 offset:%3 sc1\n\ts_nop 1" ::"v"(ho), "v"(v), "s"(hp + (j / 2) * 4096), "n"((j % 2) * 2048) : "memory");
    __builtin_amdgcn_sched_barrier(0);
  }
  MK(18);
}

DEVI void k3_const(char* smem, char* ws, const u16* b_sp, int layer, int bid, int mi, int ni, int wm) {
  const int lane = lane_id(), l = lane < 54 ? lane : 53, p = l / 18;
  const char* kp = uni_ptr(ws + OFF_CSK + bid * 2048 + wm * 512);
  const unsigned q16 = (unsigned)(l - p * 18) * 16u;
  unsigned bs;
  f32x4 t;
  lds_spin((unsigned)(size_t)smem + (unsigned)(LW_OFF + (layer ? LW_T + 1 : LW_HELLO) * 4), layer ? 12u : 2u);
  asm volatile("global_load_ushort %0, %1, %2" : "=v"(bs) : "v"((unsigned)(lane & 31) * 2u), "s"(uni_ptr((const char*)(b_sp + mi * 128 + wm * 32))) : "memory");
  if (layer) {
    asm volatile("global_load_dwordx4 %0, %1, %2 sc1\n\ts_waitcnt vmcnt(0)" : "=&v"(t) : "v"(q16), "s"(kp) : "memory");
  } else {
    const char* cp = uni_ptr(ws + OFF_CSP + ni * 1152 + wm * 288);
    f32x4 a[6];
#pragma unroll
    for (int i = 0; i < 6; ++i)
      asm volatile("global_load_dwordx4 %0, %1, %2 sc1" : "=v"(a[i]) : "v"(q16 + (unsigned)(p * (6 * F_ * 4))), "s"(cp + i * (F_ * 4)) : "memory");
    asm volatile("s_waitcnt vmcnt(0)" : "+v"(a[0]), "+v"(a[1]), "+v"(a[2]), "+v"(a[3]), "+v"(a[4]), "+v"(a[5]));
    t = ((a[0] + a[1]) + (a[2] + a[3])) + (a[4] + a[5]);
#pragma unroll
    for (int j = 0; j < 4; ++j)
      t[j] += __int_as_float(__builtin_amdgcn_ds_bpermute((lane + 18) * 4, __float_as_int(t[j]))) +
              __int_as_float(__builtin_amdgcn_ds_bpermute((lane + 36) * 4, __float_as_int(t[j])));
  }
  asm volatile("" : "+v"(bs));
  const unsigned lds0 = (unsigned)(size_t)smem;
  f32x4 t0 = wm < 2 ? f32x4{t[0], t[0], t[1], t[1]} : t, t1 = wm < 2 ? f32x4{t[2], t[2], t[3], t[3]} : t;
  unsigned a0 = lds0 + (unsigned)(wm < 2 ? K3_C2_OFF + wm * 576 + lane * 32 : K3_C1_OFF + (wm - 2) * 288 + lane * 16), a1 = a0 + (wm < 2 ? 16u : 0u);
  unsigned ab = lds0 + (unsigned)(K3_BS_OFF + (wm * 32 + (lane & 31)) * 4);
  bs <<= 16;
  asm volatile("" : "+v"(t0), "+v"(t1), "+v"(a0), "+v"(a1), "+v"(ab), "+v"(bs));
  bar();
  asm volatile("ds_write_b32 %4, %5\n\ts_mov_b64 exec, 0x3ffff\n\tds_write_b128 %0, %2\n\tds_write_b128 %1, %3\n\ts_mov_b64 exec, -1" ::"v"(a0), "v"(a1),
               "v"(t0), "v"(t1), "v"(ab), "v"(bs)
               : "memory");
  if (layer == 0 && lane < 24) asm volatile("global_store_dwordx4 %0, %1, %2\n\ts_nop 1" ::"v"((unsigned)lane * 16u), "v"(t), "s"(kp) : "memory");
}

DEVI void p3_g1(char* smem, char* __restrict__ ws, const char* code0, int bid, int layer, int mi, int wm, int lane MK_ARG) {
  const unsigned lds0 = (unsigned)(size_t)smem;
  const int g = lane >> 4, r = lane & 15;
  const unsigned lw = lds0 + (unsigned)(LW_OFF + layer * 4);
  const unsigned xrd = lds0 + (unsigned)(g * 2048 + (wm * 32 + r) * 16);
  s16x8 xc[4][2][2];
  {
    const char* xs = uni_ptr(ws + OFF_XS + (size_t)layer * 2 * SZ_XS + (size_t)(mi * 2 + (wm >> 1)) * 4096 + (wm & 1) * 256);
    f32x4 a, b;
    auto pick = [&](int t) {
      bar();
      asm volatile(
          "ds_read_b128 %0, %4\n\tds_read_b128 %1, %4 offset:256\n\tds_read_b128 %2, %4 offset:8192\n\tds_read_b128 %3, %4 offset:8448\n\t"
          "s_waitcnt lgkmcnt(0)"
          : "=&v"(xc[t][0][0]), "=&v"(xc[t][0][1]), "=&v"(xc[t][1][0]), "=&v"(xc[t][1][1])
          : "v"(xrd + (unsigned)(t * K3C::SB))
          : "memory");
    };
#define G1P 2
    __builtin_amdgcn_s_setprio(G1P);
    pick(0);
    pick(1);
    asm volatile("global_load_dwordx4 %0, %2, %3 sc1\n\tglobal_load_dwordx4 %1, %2, %3 offset:512 sc1"
                 : "=&v"(a), "=&v"(b)
                 : "v"((unsigned)(g * 1024 + r * 16)), "s"(xs)
                 : "memory");
    pick(2);
    pick(3);
    bar();
    asm volatile("s_waitcnt vmcnt(0)" : "+v"(a), "+v"(b) : : "memory");
    float s, q, mb, rinv;
    red8(a, b, s, q);
    bar();
    asm volatile("" : "+v"(s), "+v"(q));
    ln_finish(s, q, 1.0f / (float)D_, mb, rinv);
    asm volatile("" : "+v"(mb), "+v"(rinv));
    bar();
    asm volatile("" : "+v"(mb), "+v"(rinv));
    const float k0 = -2.0f * 0.7978845608028654f * 1.4426950408889634f, k1 = k0 * 0.044715f;
    float sd = __builtin_amdgcn_rcpf(rinv), c0 = k0 * rinv, c1 = k1 * rinv * rinv * rinv;
    asm volatile("" : "+v"(sd));
    asm volatile("" : "+v"(c0));
    asm volatile("" : "+v"(c1));
    const float e0 = lane < 32 ? -mb : c0, e1 = lane < 32 ? sd : c1;
    asm volatile("ds_write2st64_b32 %0, %1, %2 offset1:2" ::"v"(
                     lds0 + (unsigned)(K3_ST_OFF + (g >> 1) * 1024 + (wm * 32 + 2 * r + (g & 1)) * 4)),
                 "v"(e0), "v"(e1)
                 : "memory");
    bar();
    asm volatile("s_waitcnt lgkmcnt(0)" ::: "memory");
    bar();
    __builtin_amdgcn_s_setprio(0);
  }
  f32x4 ac[2][9];
#pragma unroll
  for (int mt = 0; mt < 2; ++mt)
#pragma unroll
    for (int j = 0; j < 9; ++j) ac[mt][j] = f32x4{0.f, 0.f, 0.f, 0.f};
  {
    UMath<true> gm(ac, xc);
    gm.xa = xrd;
    gm.wa = lds0 + (unsigned)(K3C::XB + (g * 8 + (r & 7)) * K3C::P + (r >> 3) * 16);
    gm.fa = lw + (unsigned)(LW_AD * 4 + (lane & 3));
    gm.fw = lw + (unsigned)(LW_S * 4 + wm);
    gm.fv = 0u;
    asm volatile("" : "+v"(gm.fa), "+v"(gm.fw), "+v"(gm.fv), "+v"(gm.xa), "+v"(gm.wa));
    __builtin_amdgcn_s_sleep(G1SL);
    lds_spin4<4>(lw + LW_L * 4, 4u);
    gm.first();
    place<AL_B>();
    lds_spin4(lw + LW_AD * 4, 2u);
#define BPR 1
    __builtin_amdgcn_s_setprio(BPR);
    gm.run();
    __builtin_amdgcn_s_setprio(0);
  }
  MK(14);
  f32x2 rc[4];
  f32x4 cs[9];
  const int l2 = lane_id();
  {
    const unsigned ra = lds0 + (unsigned)(K3_ST_OFF + (wm * 32 + (l2 & 15)) * 4), ca = lds0 + (unsigned)(K3_C1_OFF + (l2 & 48));
#pragma unroll
    for (int k = 0; k < 4; ++k) asm volatile("ds_read2_b32 %0, %1 offset1:16" : "=v"(rc[k]) : "v"(ra + (unsigned)(k * 512)) : "memory");
#pragma unroll
    for (int j = 0; j < 9; ++j) asm volatile("ds_read_b128 %0, %1 offset:%2" : "=v"(cs[j]) : "v"(ca), "n"(j * 64) : "memory");
    asm volatile("s_waitcnt lgkmcnt(0)" : "+v"(rc[0]), "+v"(rc[1]), "+v"(rc[2]), "+v"(rc[3]));
#pragma unroll
    for (int j = 0; j < 9; ++j) asm volatile("" : "+v"(cs[j]));
  }
  const unsigned za = lds0 + (unsigned)(K3_Z1_OFF + wm * 5120 + l2 * 16);
  u32x4 z1[9];
#pragma unroll
  for (int t = 0; t < 18; ++t) {
    const int j = t / 2, mt = t % 2;
    float u[4], w[4];
#pragma unroll
    for (int i = 0; i < 4; ++i) asm volatile("v_fma_f32 %0, %1, %2, %3" : "=v"(u[i]) : "v"(rc[0][mt]), "v"(cs[j][i]), "v"(ac[mt][j][i]));
#pragma unroll
    for (int i = 0; i < 4; ++i) asm volatile("v_mul_f32 %0, %1, %1" : "=v"(w[i]) : "v"(u[i]));
#pragma unroll
    for (int i = 0; i < 4; ++i) asm volatile("v_fma_f32 %0, %0, %1, %2" : "+v"(w[i]) : "v"(rc[3][mt]), "v"(rc[2][mt]));
#pragma unroll
    for (int i = 0; i < 4; ++i) asm volatile("v_mul_f32 %0, %0, %1" : "+v"(w[i]) : "v"(u[i]));
#pragma unroll
    for (int i = 0; i < 4; ++i) asm volatile("v_exp_f32 %0, %0" : "+v"(w[i]));
#pragma unroll
    for (int i = 0; i < 4; ++i) asm volatile("v_fma_f32 %0, %0, %1, %1" : "+v"(w[i]) : "v"(rc[1][mt]));
#pragma unroll
    for (int i = 0; i < 4; ++i) asm volatile("v_rcp_f32 %0, %0" : "+v"(w[i]));
#pragma unroll
    for (int i = 0; i < 4; ++i) asm volatile("v_mul_f32 %0, %0, %1" : "+v"(w[i]) : "v"(u[i]));
    z1[j][2 * mt] = pack2(w[0], w[1]);
    z1[j][2 * mt + 1] = pack2(w[2], w[3]);
    if (mt && j < NR1) asm volatile("ds_write_b128 %0, %1 offset:%2" ::"v"(za), "v"(z1[j]), "n"(j * 1024) : "memory");
    if (t == 9) asm volatile("s_waitcnt lgkmcnt(0)" ::: "memory");
    __builtin_amdgcn_sched_barrier(0);
    bar();
    __builtin_amdgcn_sched_barrier(0);
  }
  MK(15);
#pragma unroll 1
  for (int t = 20; t < 32; ++t) bar();
  __builtin_amdgcn_s_sleep(ZSL);
#pragma unroll
  for (int k = NR1; k < 9; ++k) {
    if (k == NR1 + 4) lds_spin4(lw + LW_AD * 4, 7u);
    asm volatile("ds_write_b128 %0, %1 offset:%2" ::"v"(lds0 + (unsigned)(wm * 4096 + l2 * 16)), "v"(z1[k]),
                 "n"(((k - NR1) / 4) * K3C::SB + ((k - NR1) % 4) * 1024)
                 : "memory");
    if (k == NR1 + 3 || k == 8) asm volatile("ds_write_b8 %0, %1" ::"v"(lw + (unsigned)(LW_S * 4 + wm)), "v"(vconst(k == NR1 + 3 ? 10u : 11u)) : "memory");
  }
  if ((RTC & 2) && wm == 2) code_again(code0, bid);
}

constexpr int W5A = 22, W5B = 26;
constexpr int NPF = 30;
struct Loader {
  u32x4 pf[NPF];
  unsigned lds0, lane16;
  int wl, lane;

  template <int R0, int N, int P0, int PE>
  DEVI void pf_load(const char* src) {
    const unsigned o = lane16 + (unsigned)(wl * 1024);
#pragma unroll
    for (int t = 0; t < N; ++t) {
      unsigned a = o + (unsigned)((P0 + 4 * t) * 1024);
      if (P0 + 4 * t + 3 >= PE && P0 + 4 * t + wl >= PE) a = lane16;
      asm volatile("global_load_dwordx4 %0, %1, %2 sc1" : "=v"(pf[R0 + t]) : "v"(a), "s"(src) : "memory");
    }
  }
  template <int R0, int N>
  DEVI void pf_pin() {
#pragma unroll
    for (int t = 0; t < N; ++t) asm volatile("" : "+v"(pf[R0 + t]));
  }
  template <int R0, int N>
  DEVI void pf_undef() {
#pragma unroll
    for (int t = 0; t < N; ++t) asm volatile("" : "=v"(pf[R0 + t]));
  }
  template <int PB, int A, int N, int SL = 0>
  DEVI void pf_put(unsigned dst) {
    constexpr int T0 = (A - PB) / 4, T1 = (A + N - 1 - PB) / 4;
    const unsigned a = lds0 + dst + lane16 + (unsigned)((wl + PB + 4 * T0 - A + 4) * 1024) - 4096u;
#pragma unroll
    for (int t = T0; t <= T1; ++t) {
      const int p0 = PB + 4 * t;
      if ((p0 >= A || p0 + wl >= A) && (p0 + 3 < A + N || p0 + wl < A + N))
        asm volatile("ds_write_b128 %0, %1 offset:%2" ::"v"(a), "v"(pf[t]), "n"((t - T0) * 4096) : "memory");
      if (SL) __builtin_amdgcn_s_sleep(SL);
    }
  }
  template <int N>
  DEVI void dma_run(const char* src, unsigned dst) {
    constexpr int B = N / 4, R = N % 4;
    const unsigned f = (unsigned)(wl * B + (wl < R ? wl : R)) * 1024u, o = lane16 + f, m = lds0 + dst + f;
    dma_seq<B>(src, o, m);
    if (wl < R) dma16_sc1(src + B * 1024, o, m + (unsigned)(B * 1024));
  }
  template <int V>
  DEVI void wait_w() {
    asm volatile("s_cmp_lt_u32 %0, 2\n\ts_cbranch_scc1 1f\n\ts_waitcnt vmcnt(%1)\n1:\n\ts_waitcnt vmcnt(%2)" ::"s"(wl), "n"(V - 1), "n"(V) : "memory", "scc");
  }
  template <int V>
  DEVI void wbar() {
    asm volatile("s_waitcnt vmcnt(%0) lgkmcnt(0)" ::"n"(V) : "memory");
    bar();
  }

  DEVI void p1(char* smem, char* ws, int bid, int rt, int hp MK_ARG) {
    constexpr int SB = K1C::SB;
    static_assert(K1C::XB == 18 * 1024 && K1C::WB == 14 * 1024, "K1 shape");
    const char* wp = uni_ptr(ws + OFF_PQ + (size_t)hp * (9 * K1C::WB) + 4 * K1C::WB);
    static_assert(3 * SB + K1C::XB >= P5_W + 5 * 18432, "free while the GEMM before ends");
    const char* wq = wp - 4 * K1C::WB;
    dma_run<14>(wq + 3 * K1C::WB, (unsigned)(3 * SB + K1C::XB));
    dma_run<28>(wp, (unsigned)K1_WX);
    if (CSL && wl == 3) dma16_sc1(uni_ptr(ws + OFF_CSQ + bid * 512), lane16 & 511u, lds0 + (unsigned)K1_CS_OFF);
    if (P1PR & 1) __builtin_amdgcn_s_setprio(0);
    if (P1PR & 2) wait_vm<0>();
    const bool lo = wl < 2;
    const int x0 = lo ? wl * 5 : wl * 4 + 2, w0 = lo ? wl * 3 : wl * 4 - 2;
    unsigned xo[5];
#pragma unroll
    for (int i = 0; i < 5; ++i) {
      const int c = (x0 + i) * 64 + lane;
      const int ch = c / 144;
      int gr = rt * 128 - 16 + (c - ch * 144);
      if (gr < 0) gr = 0;
      xo[i] = (unsigned)(((gr >> 6) * 72 + ch + 3 - (i & 3)) * 1024 + (gr & 63) * 16);
      asm volatile("" : "+v"(xo[i]));
    }
    const char* xp = uni_ptr(ws + OFF_X1 + SZ_X - 3072);
    wp += 2 * K1C::WB + w0 * 1024;
    auto dma_x = [&](unsigned so) {
      const unsigned m = lds0 + so + (unsigned)(x0 * 1024);
      dma4<4, true>(xp, xo[0], xo[1], xo[2], xo[3], m);
      if (lo) dma16_sc1(xp, xo[4], m + 4096u);
      xp += 8192;
    };
    auto dma_w = [&](unsigned so) {
      const unsigned m = lds0 + so + (unsigned)(K1C::XB + w0 * 1024);
      dma_seq<3>(wp, lane16, m);
      if (!lo) dma16_sc1(wp + 3072, lane16, m + 3072u);
      wp += K1C::WB;
    };
    const char* xs = uni_ptr(ws + OFF_XS + SZ_XS);
    const int g = lane >> 4, p2 = 64 + (lane & 7);
    int ra = rt * 128 - 16 + wl * 32 + 2 * (lane & 15);
    if (ra < 0) ra = 0;
    unsigned sa = (unsigned)(((ra >> 6) * 8 + g * 2) * 512 + (ra & 63) * 8);
    unsigned sb = (unsigned)(((rt * 2 + 1) * 8 + (lane >> 3)) * 512 + (2 * p2 - 80) * 8);
    unsigned sw = lds0 + (unsigned)(K1_ST_OFF + (2 * (lane < 32 ? wl * 16 + (lane & 15) : p2) + (g & 1)) * 8);
    asm volatile("" : "+v"(sa), "+v"(sb), "+v"(sw));
    MK(22);
    __builtin_amdgcn_s_setprio(3);
    bar();
    dma_run<14>(wq, (unsigned)K1C::XB);
    dma_run<14>(wq + K1C::WB, (unsigned)(SB + K1C::XB));
    dma_run<14>(wq + 2 * K1C::WB, (unsigned)(2 * SB + K1C::XB));
    MK(1);
    bar();
    dma_x(0u);
    dma_x((unsigned)SB);
    wbar<4>();
    MK(29);
    dma_x((unsigned)(2 * SB));
    dma_x((unsigned)(3 * SB));
    wbar<8>();
    MX(1, 3);
    dma_x(0u);
    wbar<8>();
    MX(1, 4);
    dma_x((unsigned)SB);
    f32x4 a, b, e;
    asm volatile("global_load_dwordx4 %0, %3, %5 sc1\n\tglobal_load_dwordx4 %1, %3, %5 offset:512 sc1\n\tglobal_load_dwordx4 %2, %4, %5 sc1"
                 : "=&v"(a), "=&v"(b), "=&v"(e)
                 : "v"(sa), "v"(sb), "s"(xs)
                 : "memory");
    wbar<11>();
    MX(1, 5);
    dma_w((unsigned)SB);
    dma_x((unsigned)(2 * SB));
    wbar<15>();
    MX(1, 6);
    dma_w((unsigned)(2 * SB));
    dma_x((unsigned)(3 * SB));
    wbar<19>();
    MX(1, 7);
    dma_w((unsigned)(3 * SB));
    dma_x(0u);
    wbar<16>();
    MX(1, 8);
    asm volatile("" : "+v"(a), "+v"(b), "+v"(e));
    float s, q;
    {
      float t[4] = {e[0], e[1], e[2], e[3]}, s2, q2;
      asm volatile(
          "v_add_f32_dpp %0, %0, %0 row_ror:8 row_mask:0xf bank_mask:0xf\n\tv_add_f32_dpp %1, %1, %1 row_ror:8 row_mask:0xf bank_mask:0xf\n\t"
          "v_add_f32_dpp %2, %2, %2 row_ror:8 row_mask:0xf bank_mask:0xf\n\tv_add_f32_dpp %3, %3, %3 row_ror:8 row_mask:0xf bank_mask:0xf\n\ts_nop 1"
          : "+v"(t[0]), "+v"(t[1]), "+v"(t[2]), "+v"(t[3]));
      red4(t, s2, q2);
      red8(a, b, s, q);
      s = lane < 32 ? s : s2;
      q = lane < 32 ? q : q2;
      asm volatile("" : "+v"(s), "+v"(q));
    }
    wbar<8>();
    MX(1, 9);
    {
      asm volatile("" : "+v"(s), "+v"(q));
      float mb, rinv;
      ln_finish(s, q, 1.0f / (float)D_, mb, rinv);
      asm volatile("ds_write_b64 %0, %1" ::"v"(sw), "v"(f32x2{-mb, rinv}) : "memory");
    }
    wbar<0>();
    MK(30);
  }

  template <int I>
  DEVI void p5_step(const char* wp) {
    constexpr int T = 2 * I + 1;
    wbar<(T == 1 ? 63 : (T <= 11 ? 3 : 0))>();
    if constexpr (T <= 9) {
      auto dm = [&]() {
      if (!G1W) {
        const unsigned wo = (unsigned)(12288 + (wl & 1) * 3072), so = (unsigned)(P5_W + ((T + 2 + (wl >> 1)) & 7) * 18432);
        dma_seq<3>(wp + (T + 7) * 18432, lane16 + wo + (unsigned)((wl >> 1) * 18432), lds0 + so + wo);
      }
      };
      if (!P5ORD) dm();
      pf_put<0, 12 * (T - 1), 12, P5PACE>((unsigned)(P5_W + ((T + 2) % 8) * 18432));
      pf_put<0, 12 * T, 12, P5PACE>((unsigned)(P5_W + ((T + 3) % 8) * 18432));
      if (P5ORD == 2) asm volatile("s_waitcnt lgkmcnt(0)" ::: "memory");
      if (P5ORD) dm();
    }
  }
  template <int... S>
  DEVI void p5_steps(const char* wp, std::integer_sequence<int, S...>) {
    (p5_step<S>(wp), ...);
  }

  template <int T0, int T1>
  DEVI void w5_load(const char* w5p) {
    const unsigned o = lane16 + (unsigned)(wl * 1024);
#pragma unroll
    for (int t = T0; t < T1; ++t)
      asm volatile("global_load_dwordx4 %0, %1, %2 sc1" : "=v"(pf[t]) : "v"(o + (unsigned)((t / 3 + 8) * 18432 + (t % 3) * 4096)), "s"(w5p) : "memory");
  }
  DEVI void p35(char* smem, char* ws, const u16* w_sp, int layer, int bid, int mi, int ni, int rb, int ct, int hp MK_ARG) {
    using C = K3C;
    constexpr int NS = C::NS, SB = C::SB, NDX = 4, NDW = 5, VPS = NDX + NDW;
    const char* wip = uni_ptr(ws + OFF_PI + (size_t)ni * (18 * C::WB));
    if (layer || L0REG) {
      pf_put<-48, 36, 18>((unsigned)(2 * SB + C::XB));
      pf_put<-48, 54, 18>((unsigned)(3 * SB + C::XB));
    } else if (!L0W) {
      dma_run<18>(wip + 2 * C::WB, (unsigned)(2 * SB + C::XB));
      dma_run<18>(wip + 3 * C::WB, (unsigned)(3 * SB + C::XB));
    }
    if (W01M == 2) dma_run<18>(wip, (unsigned)C::XB);
    if (W01M == 1 || W01M == 2) dma_run<18>(wip + C::WB, (unsigned)(SB + C::XB));
    const char* w5p = uni_ptr(ws + OFF_PW + (size_t)ct * (36 * 9216));
    const int w0 = wl < 2 ? wl * 5120 : wl * 4096 + 2048;
    const unsigned m0x = lds0 + (unsigned)(wl * 4096), m0w = lds0 + (unsigned)(C::XB + w0);
    const unsigned wo = lane16 + (unsigned)w0;
    const unsigned lw = lds0 + (unsigned)(LW_OFF + layer * 4), me = 1u << (8 * wl);
    const unsigned wo4 = wo + 4096u;
    auto dma_w = [&](const char* wp, unsigned so) {
      dma_seq<4>(wp, wo, m0w + so);
      if (wl < 2) dma16_sc1(wp, wo4, m0w + so + 4096u);
    };
    {
      const char* xp = uni_ptr(ws + OFF_X1 + (size_t)layer * 2 * SZ_X + (size_t)(mi * 2 * 72 + wl * 2) * 1024 - 1024);
      const unsigned xo0 = lane16 + 1024u, xo1 = lane16 + 72u * 1024u, xo3 = lane16 + 71u * 1024u;
      auto dma_x = [&](int k, unsigned so) { dma4<4, true>(xp + k * 8192, xo0, xo1, lane16, xo3, m0x + so); };
      if (!MPOLL && wl == 0) cnt_wait(ws, CN_X1 + layer, mi * 16 + ni, 16u);
      MK(10);
      bar();
      dma_x(0, 0u);
      dma_x(1, (unsigned)SB);
      wait_vm<4>(); MX(3, 2); wbar<4>(); MX(2, 2);
      MK(25);
      if (L0W && layer == 0) {
        dma_w(wip + 2 * C::WB, (unsigned)(2 * SB));
        dma_x(2, (unsigned)(2 * SB));
        wbar<8>();
        dma_w(wip + 3 * C::WB, (unsigned)(3 * SB));
        dma_x(3, (unsigned)(3 * SB));
        dma_x(4, (unsigned)(4 * SB));
        dma_x(5, 0u);
        wait_vm<16>();
      } else {
        dma_x(2, (unsigned)(2 * SB));
        dma_x(3, (unsigned)(3 * SB));
        dma_x(4, (unsigned)(4 * SB));
        wbar<12>(); MX(2, 3);
        dma_x(5, 0u);
        wait_vm<12>();
      }
      bar(); MX(2, 4);
      dma_w(wip + 4 * C::WB, 0u);
      dma_x(6, (unsigned)SB);
      wait_w<8 + VPS>();
#pragma unroll 1
      for (int t = 1; t < 3; ++t) {
        bar(); MX(2, 4 + t);
        dma_w(wip + (t + 4) * C::WB, (unsigned)(t * SB));
        dma_x(t + 6, (unsigned)((t + 1) * SB));
        wait_w<NDX + VPS>();
      }
      bar(); MX(2, 7);
      dma_w(wip + 7 * C::WB, (unsigned)(3 * SB));
      wait_w<VPS>();
      bar(); MX(2, 8);
      dma_w(wip + 8 * C::WB, 0u);
      wait_w<NDW>();
      bar(); MX(2, 9);
      const char* wp = wip + 9 * C::WB;
      dma_w(wp + b_k(0) * C::WB, (unsigned)SB);
      if (layer == 0) {
        const char* sp = uni_ptr((const char*)w_sp + (size_t)(bid * 4 + wl) * 8192);
        const unsigned so = (unsigned)((lane >> 5) * 4096 + (lane & 31) * 16);
#pragma unroll
        for (int i = 0; i < WSP_V; ++i) dma16(sp + i * 512, so, lds0 + (unsigned)K3_DM_OFF);
        dma16(sp + (bid >> 4) * 256, so & ~0x100u, lds0 + (unsigned)K3_DM_OFF);
      }
      w5_load<0, 4>(w5p);
      wait_w<NDW + 4>();
      MX(3, 10); bar(); MX(2, 10);
      dma_w(wp + b_k(1) * C::WB, (unsigned)(2 * SB));
      w5_load<4, 8>(w5p);
      MK(26);
      lds_spin4(lw + LW_AD * 4, 1u);
      dma_w(wp + b_k(2) * C::WB, (unsigned)(3 * SB));
      dma_w(wp + b_k(3) * C::WB, 0u);
#define TCH 0
      if (TCH & 1) {
        const int li = wl * 54 + (lane < 54 ? lane : 53);
        touch64(wp + C::WB, (unsigned)((mi & 7) * 13824 + li * 64), lds0 + (unsigned)K3_DM_OFF);
      }
      w5_load<8, W5A>(w5p);
      wait_vm<W5A - 8>();
      lds_inc(lw + LW_L * 4, 4u * me);
      w5_load<W5A, 30>(w5p);
      MK(11);
#define LSL 2
      static_for<4, 7>([&](auto T_) {
        constexpr int t = T_;
        lds_spin4<LSL>(lw + LW_S * 4, (unsigned)(t - 3));
        MX(2, 12 + t);
        dma_w(wp + b_k(t) * C::WB, (unsigned)(((t + 1) % 4) * SB));
        if constexpr (t > 4) {
          wait_w<NDW>();
          lds_inc(lw + LW_L * 4, me);
        }
      });
      MX(2, 19);
      wait_vm<0>();
      lds_inc(lw + LW_L * 4, me);
      pf_pin<0, 30>();
      MK(13);
    }
    {
      unsigned xoff[NDX];
#pragma unroll
      for (int i = 0; i < NDX; ++i) {
        const int c = wl * 256 + i * 64 + lane;
        const int row = c >> 3, cc = c & 7;
        xoff[i] = (unsigned)((mi * 128 + row) * S_ * 2 - i * 1024) + (unsigned)((cc ^ ((row >> 1) & 7)) * 16);
      }
      auto dma_x = [&](const char* xp, unsigned so) { dma4<4, false>(xp, xoff[0], xoff[1], xoff[2], xoff[3], m0x + so); };
      const char* xp = uni_ptr((const char*)w_sp);
      const char* wp = uni_ptr(ws + OFF_Z2 + (size_t)layer * SZ_Z + (size_t)ni * S_ * 288);
#define XEARLY 0
      if (!XEARLY) lds_spin4<LSL>(lw + LW_AD * 4, 5u);
      dma_x(xp + mi * 256, (unsigned)(2 * SB));
      dma_x(xp + mi * 256 + 128, (unsigned)(3 * SB));
      if (XEARLY) lds_spin4<LSL>(lw + LW_AD * 4, 5u);
      MK(24);
      int left = 2 * mi;
      auto next = [&]() {
        const int d = --left == 0 ? 3 : 1;
        xp += d * 128;
        wp += d * C::WB;
      };
      if (mi == 0) {
        xp += 256;
        wp += 2 * C::WB;
      }
      dma_x(xp, 0u);
      lds_spin4(lw + LW_S * 4, 6u);
      dma_x(xp + 128, (unsigned)SB);
      wait_vm<2 * NDX>();
      lds_inc(lw + LW_L * 4, me);
      MK(27);
      if (wl == 0) {
        cnt_wait(ws, CN_Z + layer, mi * 16 + ni, 16u);
        lds_wr32(lw + LW_GO * 4, 1u);
      } else {
        lds_spin(lw + LW_GO * 4, 1u);
      }
      MK(14);
      lds_spin4(lw + LW_S * 4, 8u);
      dma_w(wp, 0u);
      lds_spin4(lw + LW_S * 4, 9u);
      dma_w(wp + C::WB, (unsigned)SB);
      next();
      next();
      unsigned slot = (unsigned)(2 * SB);
      auto cissue = [&]() {
        dma_x(xp, slot);
        dma_w(wp, slot);
        next();
        slot = (slot == (unsigned)((NS - 1) * SB)) ? 0u : slot + (unsigned)SB;
      };
      auto cstep = [&]() {
        bar();
        cissue();
      };
      wait_w<NDW>();
      MK(15);
      int j = 2;
      if (layer == 0) {
        const char* sp = uni_ptr((const char*)w_sp + (size_t)(bid * 4 + wl) * 8192 + WSP_V * 512);
        unsigned d = 0u;
#pragma unroll 1
        for (; j < 2 + 32 - 4 * WSP_V; ++j) {
          cstep();
          asm volatile("s_load_dword %0, %1, 0\n\ts_load_dword %0, %1, 64\n\ts_load_dword %0, %1, 0x1000\n\ts_load_dword %0, %1, 0x1040"
                       : "+s"(d)
                       : "s"(sp));
          sp += 128;
          wait_w<VPS>();
        }
#pragma unroll 1
        for (int e = j + 4; j < e; ++j) {
          cstep();
          asm volatile("" : "+s"(d));
          wait_w<VPS>();
        }
        asm volatile("s_waitcnt lgkmcnt(0)" : "+s"(d));
      }
      place<AL_L>();
#pragma unroll 1
      for (; j <= 29; ++j) {
        cstep();
        wait_w<VPS>();
      }
      bar();
    }
    dma_run<33>(w5p, (unsigned)(P5_W + 54 * 1024));
    dma_run<23>(w5p + 67 * 1024, (unsigned)(P5_W + 121 * 1024));
    if (wl == 3) dma16_sc1(w5p + 143 * 1024, lane16, lds0 + (unsigned)(P5_W + 53 * 1024));
    wait_vm<14>();
    bar();
    dma_run<34>(w5p + 33 * 1024, (unsigned)(P5_W + 87 * 1024));
    wait_vm<0>();
    bar();
    if (wl) {
      const unsigned f = (unsigned)(wl - 1) * 18432u;
      dma_seq<17>(w5p + 90 * 1024, lane16 + f, lds0 + (unsigned)P5_W + f);
      if (wl < 3) dma16_sc1(w5p + 107 * 1024, lane16 + f, lds0 + (unsigned)(P5_W + 17 * 1024) + f);
    } else {
      cnt_wait(ws, CN_H + layer, rb * 8 + ct, HSPLIT ? 8u : 16u);
    }
    MK(20);
    bar();
    p5_steps(w5p, std::make_integer_sequence<int, 7>{});
    if (layer == 0) {
      if (bid < 24 && wl == 0 && lane < 8) pk_st16(ws + OFF_HELLO + bid * 128 + lane16, u32x4{0u, 0u, 0u, 0u});
    }
  }
};

__global__ __launch_bounds__(768, 3) void k_main(const u16* __restrict__ x0, const u16* __restrict__ wqkv, const u16* __restrict__ wo,
                                                 const u16* __restrict__ w_in, const u16* __restrict__ w_sp,
                                                 const u16* __restrict__ b_sp, const u16* __restrict__ w_out,
                                                 u16* __restrict__ out, char* ws) {
  extern __shared__ __attribute__((aligned(16))) char smem[];
  const char* code0 = code_here();
  (void)code0;
  const unsigned lds0 = (unsigned)(size_t)smem;
  int wave = __builtin_amdgcn_readfirstlane((int)threadIdx.x >> 6);
  int bid = blockIdx.x;
#define FRESH() asm volatile("" : "+s"(bid), "+s"(ws), "+s"(wave))
#define XCD (bid & 7)
#define IDX (bid >> 3)
#define MI ((XCD >> 2) * 8 + (IDX >> 2))
#define NI ((XCD & 1) * 8 + (XCD & 2) * 2 + (IDX & 3))
#define RB (XCD * 4 + (IDX >> 3))
#define CT (IDX & 7)
#define P1MAP 0
#define RT ((XCD >> 1) * 4 + (IDX >> 3))
#define HP ((XCD & 1) * 8 + (IDX & 7))
  MK(0);
  if (CT0 && wave == 2) {
    constexpr int PER = (2 * NL_MAIN + 31) / 32;
    const int lane = lane_id();
    int li = (CT0 == 2 ? lane * 32 + IDX : IDX * PER + (lane < PER ? lane : PER - 1));
    if (li > 2 * NL_MAIN - 1) li = 2 * NL_MAIN - 1;
    touch64(code0, (unsigned)li * 64u, lds0 + (unsigned)(K1R_DUMMY + 768));
  }
  if (wave >= 3 && wave < 8) {
    constexpr int ST = 64;
    const int k = (7 - wave) * 64 + lane_id();
    const bool isx = k < 9216 / ST;
    int o = isx ? bid * 9216 + k * ST : bid * 7776 + (k - 9216 / ST) * ST;
    if (!isx && k - 9216 / ST > 7775 / ST) o = bid * 7776;
    const char* p = (isx ? (const char*)x0 : (const char*)wqkv) + o;
    asm volatile("s_mov_b32 m0, %1\n\ts_nop 0\n\tglobal_load_lds_dword %0, off" ::"v"(p), "s"(lds0 + (unsigned)K1R_DUMMY) : "memory");
  }

  if (__builtin_expect(wave >= 8, 1)) {
    __builtin_amdgcn_s_setprio(3);
    {
      const int lane = lane_id();
      RawLd rl;
      const bool cross = (((HP * 72) & ~15) & 127) > 48;
      const int c16 = IDX >> 1;
      const int cj = (int)((JORD >> (4 * XCD)) & 7u) * 16 + c16;
      const int job = cross ? (cj < 88 ? 128 + cj : 999) : XCD * 16 + c16;
      const int pj = cross ? cj - 88 : ((XCD & 5) == 0 && (c16 & 3) == 0 ? 40 + XCD * 2 + (c16 >> 2) : -1);
      rl.front(x0, wqkv, wo, ws, bid, lds0, wave - 8, lane, RT, HP, pj MF_PASS);
      if (job < 216) k1_raw_loader<true>(rl, w_in, w_out, ws, rl.tag, bid, lds0, wave - 8, lane, RT, HP, CT, job, pj MF_PASS);
      else k1_raw_loader<false>(rl, w_in, w_out, ws, rl.tag, bid, lds0, wave - 8, lane, RT, HP, CT, job, pj MF_PASS);
    }
    Loader ld;
    ld.lds0 = lds0;
#pragma unroll 1
    for (int layer = 0; layer < 2; ++layer) {
      FRESH();
      ld.wl = wave - 8;
      ld.lane = lane_id();
      ld.lane16 = (unsigned)ld.lane * 16u;
      const char* wip = uni_ptr(ws + OFF_PI + (size_t)NI * (18 * K3C::WB));
      if (layer) {
        ld.p1(smem, ws, bid, RT, HP MK_PASS);
        ld.pf_load<0, 21, 0, 81>(uni_ptr(ws + OFF_PO + (size_t)CT * (18 * 4608)));
        ld.pf_load<21, 9, 36, 72>(wip);
        if (EPUT) {
          wait_vm<9>();
          ld.pf_pin<0, 21>();
          lds_spin(lds0 + (unsigned)(LW_OFF + (LW_T + layer) * 4), 8u);
          __builtin_amdgcn_s_sleep(EPSL);
          ld.pf_put<0, 0, 40>((unsigned)P2_W);
          if (EPUT == 2) ld.pf_put<0, 40, 41>((unsigned)(P2_W + 40 * 1024));
          wait_vm<0>();
          ld.pf_pin<21, 9>();
          bar();
          if (EPUT == 1) ld.pf_put<0, 40, 41>((unsigned)(P2_W + 40 * 1024));
        } else {
          wait_vm<0>();
          ld.pf_pin<0, 30>();
          bar();
          ld.pf_put<0, 0, 40>((unsigned)P2_W);
          ld.pf_put<0, 40, 41>((unsigned)(P2_W + 40 * 1024));
        }
      } else {
        ld.pf_undef<21, 9>();
        MK(3);
        MK(4);
        bar();
        ld.dma_run<81>(uni_ptr(ws + OFF_PO + (size_t)CT * (18 * 4608)), (unsigned)P2_W);
      }
      if (layer == 0 && ld.wl == 0) cnt_wait(ws, CN_O + layer, RB * 8 + CT, 16u);
      MK(6);
      asm volatile("s_waitcnt vmcnt(0) lgkmcnt(0)" ::: "memory");
      bar();
      lds_spin(lds0 + (unsigned)(LW_OFF + (LW_T + layer) * 4), 12u);
      if (layer == 0) {
        if (ld.wl == 0) {
          cnt_wait(ws, CN_PK, 8 + NI, 36u);
          lds_wr32(lds0 + (unsigned)(LW_OFF + LW_HELLO * 4), 2u);
        } else {
          lds_spin(lds0 + (unsigned)(LW_OFF + LW_HELLO * 4), 2u);
        }
        MK(7);
      }
      static_assert(2 * K3C::SB <= P2_W, "free during the first residual GEMM");
      if (W01M < 2) ld.dma_run<18>(wip, (unsigned)K3C::XB);
      if (W01M < 1) ld.dma_run<18>(wip + K3C::WB, (unsigned)(K3C::SB + K3C::XB));
      MX(6, 24);
      if (L0REG && layer == 0) ld.pf_load<21, 9, 36, 72>(wip);
      if (layer == 0 && ld.wl == 0) cnt_wait(ws, CN_PK, 24 + CT, 72u);
      wait_vm<0>();
      ld.pf_pin<21, 9>();
      MX(6, 25);
      bar();
      FRESH();
      ld.wl = wave - 8;
      ld.p35(smem, ws, w_sp, layer, bid, MI, NI, RB, CT, HP MK_PASS);
    }
  } else {
    MF(12);
    math_start(smem, code0, b_sp, wqkv, ws, bid, wave, lane_id());
    MF(13);
#pragma unroll 1
    for (int layer = 0; layer < 2; ++layer) {
      FRESH();
      {
        const int lane = lane_id(), r0 = RT * 128;
        float* csq = reinterpret_cast<float*>(ws + OFF_CSQ + bid * 512);
        Attn at;
        at.setup(lds0 + (unsigned)(layer ? K1_T_OFF : K1R_T_OFF), ws + OFF_O + (size_t)layer * SZ_X, r0, HP, wave, lane);
        if (layer) {
          if ((wave & 2) == 0) k1_gemm<2, 4>(smem, ws, csq, RT, HP, (wave & 1) * 2 + (wave & 4), 0, wave >= 4, lane MK_PASS);
          else if (wave < 4) k1_gemm<3, 3>(smem, ws, csq, RT, HP, (wave & 1) * 3, 4, false, lane MK_PASS);
          else if (wave == 6) k1_gemm<2, 3>(smem, ws, csq, RT, HP, 6, 4, true, lane MK_PASS);
          else k1_gemm<1, 7>(smem, ws, csq, RT, HP, 8, 0, true, lane MK_PASS);
        } else {
          if (wave == 0) k1_gemm_raw<3, false>(smem, HP, 0, lane MF_PASS);
          else if (wave < 4) k1_gemm_raw<2, false>(smem, HP, 1 + 2 * wave, lane MF_PASS);
          else if (wave < 7) k1_gemm_raw<2, true>(smem, HP, 2 * (wave - 4), lane MF_PASS);
          else k1_gemm_raw<3, true>(smem, HP, 6, lane MF_PASS);
        }
        msync(lds0 + (unsigned)(LW_OFF + (LW_T + layer) * 4));
        MK(3);
        at.run(r0, HP, wave, lane);
        if (layer == 0) {
          if (wave == 0 && lane < 32)
            asm volatile("global_store_dwordx4 %0, %1, %2 sc1\n\ts_nop 1" ::"v"((unsigned)lane * 16u),
                         "v"(*reinterpret_cast<const u32x4*>(smem + K1R_CS_OFF + lane * 16)), "s"(uni_ptr((const char*)csq))
                         : "memory");
          lds_spin(lds0 + (unsigned)(LW_OFF + LW_HELLO * 4), 1u);
        }
        MK(4);
        announce(lds0 + (unsigned)(LW_OFF + (LW_O + layer * 22 + (wave >> 2)) * 4), 4u, ws, CN_O + layer, (RT * 2 + (wave >> 2)) * 8 + lane,
                 lane < 8);
        MK(5);
        bar();
      }
      FRESH();
      {
        if (wave < 4) {
          const int lane = lane_id();
          u32x2 xv[5];
          ResIn ri;
          ri.setup(layer ? ws + OFF_X1 + SZ_X : (const char*)x0, layer == 0, RB, CT, wave, lane);
          ri.load(xv);
          ResOut ro;
          ro.setup(ws + OFF_X1 + (size_t)layer * 2 * SZ_X, ws + OFF_XS + (size_t)layer * 2 * SZ_XS, out, false, RB, CT, wave, lane);
          f32x4 acc[2][5];
          p2_gemm(acc, xv, lds0, uni_ptr(ws + OFF_O + (size_t)layer * SZ_X + (size_t)RB * (72 * 1024)), layer, wave, lane MK_PASS);
          MK(7);
          ro.run(acc[0], false);
          MK(8);
          announce(lds0 + (unsigned)(LW_OFF + (LW_X1 + layer) * 4), 4u, ws, CN_X1 + layer, (RB >> 1) * 16 + lane, lane < 16);
          MK(9);
          bar();
          FRESH();
          if (wave == 3) cnt_wait(ws, CN_X1 + layer, MI * 16 + NI, 16u);
          bar();
          MK(10);
          p3_g0(smem, ws, layer, MI, NI, wave, lane_id() MK_PASS);
        } else {
          if (layer && wave == 5) cnt_wait(ws, CN_O + layer, RB * 8 + CT, 16u);
          bar();
          k3_const(smem, ws, b_sp, layer, bid, MI, NI, wave & 3);
          MX(6, 26);
          bar();
          MK(10);
          FRESH();
          p3_g1(smem, ws, code0, bid, layer, MI, wave & 3, lane_id() MK_PASS);
        }
      }
      FRESH();
      {
        const int lane = lane_id();
        if (wave < 4) {
          u32x2 xv[5];
          ResIn ri;
          ri.setup(ws + OFF_X1 + (size_t)layer * 2 * SZ_X, false, RB, CT, wave, lane);
          f32x4 acc[2][5];
          ResOut ro;
          ro.setup(ws + OFF_X1 + SZ_X, ws + OFF_XS + SZ_XS, out, layer != 0, RB, CT, wave, lane);
          ResX<36, 33, 8, P5_W, P5_XW, P5_X0, 3, P5_BAR> gm(acc);
          gm.setup(lds0, uni_ptr(ws + OFF_H + (size_t)layer * SZ_Z + (size_t)RB * (288 * 1024)), wave, lane, (unsigned)P5_RED, layer + 1u);
          announce(lds0 + (unsigned)(LW_OFF + (LW_H + layer * 2 + (wave >> 1)) * 4), 2u, ws, ((HSPLIT && NI >= 8) ? CN_H2 : CN_H) + layer,
                   (MI * 2 + (wave >> 1)) * 8 + lane, lane < 8);
          MK(19);
          if (XVE) ri.load(xv);
          bar();
          if (!XVE) ri.load(xv);
          gm.prow();
          place<AL_P5>();
          bar();
          MK(20);
          gm.prox<0>(std::make_integer_sequence<int, P5_X0>{});
          gm.init<2 * P5_X0>(xv);
          __builtin_amdgcn_sched_barrier(0);
          gm.run();
          MK(21);
          ro.run(acc[0], layer != 0);
          MK(22);
          if (layer == 0) {
            announce(lds0 + (unsigned)(LW_OFF + LW_X2 * 4), 4u, ws, CN_X2, (RB >> 1) * 16 + lane,
                     lane < 16 || (lane < 32 && (RB & 1) && RB < 31));
            MK(23);
          }
        } else {
          bar();
          u32x4 wq[15];
          const int wm = wave & 3;
          const unsigned o = (unsigned)((wm >> 1) * 18432 + 12288 + (wm & 1) * 3072 + lane * 16);
          if (G1W) {
            const char* wp = uni_ptr(ws + OFF_PW + (size_t)CT * (36 * 9216) + 8 * 18432);
#pragma unroll
            for (int i = 0; i < 15; ++i)
              asm volatile("global_load_dwordx4 %0, %1, %2 offset:%3 sc1" : "=v"(wq[i]) : "v"(o), "s"(wp + (i / 3) * 36864), "n"((i % 3) * 1024) : "memory");
          }
          bar();
#pragma unroll
          for (int i = 0; i < 7; ++i) {
            if (G1W && i == 0) asm volatile("s_waitcnt vmcnt(0)" ::: "memory");
            if (HSPLIT && i == HPOLL && wave == 5) {
              if (HPOLL < 2) {
                unsigned f;
                const char* cp = uni_ptr(ws + OFF_CNT + (size_t)((CN_H2 + layer) * 256 + RB * 8 + CT) * 128);
                asm volatile("global_load_dword %0, %1, %2 sc1" : "=v"(f) : "v"(vconst(0u)), "s"(cp) : "memory");
                bar();
                asm volatile("s_waitcnt vmcnt(0)" : "+v"(f) : : "memory");
                if (__builtin_amdgcn_readfirstlane(f) != 7u) cnt_wait(ws, CN_H2 + layer, RB * 8 + CT, 8u);
                continue;
              }
              cnt_wait(ws, CN_H2 + layer, RB * 8 + CT, 8u);
            }
            asm volatile("s_waitcnt lgkmcnt(0)" ::: "memory");
            bar();
            if (G1W && i < 5) {
              const unsigned a = lds0 + o + (unsigned)(P5_W + ((2 * i + 3) % 8) * 18432) - ((i == 2 && wm >= 2) ? 8u * 18432u : 0u);
#pragma unroll
              for (int k = 0; k < 3; ++k) {
                if (P5PACE) __builtin_amdgcn_s_sleep(2 * P5PACE);
                asm volatile("ds_write_b128 %0, %1 offset:%2" ::"v"(a), "v"(wq[3 * i + k]), "n"(k * 1024) : "memory");
              }
            }
          }
          MK(22);
        }
        if (layer == 0 && (!G1E || wave < 4)) bar();
      }
    }
  }
}

}

extern "C" void kernel_launch(void* const* d_in, const int* in_sizes, int n_in, void* d_out, int out_size,
                              void* d_ws, size_t ws_size, hipStream_t stream) {
  const u16* x0 = (const u16*)d_in[0];
  const u16* wqkv = (const u16*)d_in[1];
  const u16* wo = (const u16*)d_in[2];
  const u16* w_in = (const u16*)d_in[4];
  const u16* w_sp = (const u16*)d_in[5];
  const u16* b_sp = (const u16*)d_in[6];
  const u16* w_out = (const u16*)d_in[7];
  char* ws = (char*)d_ws;
  (void)hipFuncSetAttribute((const void*)k_main, hipFuncAttributeMaxDynamicSharedMemorySize, LDS_ALL);
  k_main<<<256, 768, LDS_ALL, stream>>>(x0, wqkv, wo, w_in, w_sp, b_sp, w_out, (u16*)d_out, ws);
}
```

```cpp
#include <hip/hip_runtime.h>
#include <hip/hip_bf16.h>
#include <stdint.h>

#define DEVI __device__ __forceinline__

typedef unsigned short u16;
typedef __attribute__((ext_vector_type(8))) short s16x8;
typedef __attribute__((ext_vector_type(4))) short s16x4;
typedef __attribute__((ext_vector_type(4))) float f32x4;
typedef __attribute__((ext_vector_type(2))) float f32x2;
typedef __attribute__((ext_vector_type(2))) __bf16 bf16x2;
typedef __attribute__((ext_vector_type(2))) unsigned u32x2;
typedef __attribute__((ext_vector_type(4))) unsigned u32x4;

#define MK(I) ((void)0)
#define MX(S, I) ((void)0)
#define MF(I) ((void)0)
#define MF_ARG
#define MF_PASS
#define MK_ARG
#define MK_PASS

#define SPIN_MEM (1u << 22)
#define SAFE_OUT(L)
#define SAFE_AND
#define NOP_(n) "=s"(n)

namespace {

constexpr int S_ = 2048, D_ = 576, F_ = 4608, FH_ = 2304;
constexpr int NQKV = 3 * D_;

constexpr size_t SZ_X = (size_t)S_ * D_ * 2;
constexpr size_t SZ_Z = (size_t)S_ * FH_ * 2;
constexpr size_t OFF_X1 = 0;
constexpr size_t OFF_O = OFF_X1 + 3 * SZ_X;
constexpr size_t OFF_Z2 = OFF_O + 2 * SZ_X;
constexpr size_t OFF_H = OFF_Z2 + 2 * SZ_Z;
constexpr size_t SZ_XS = (size_t)S_ * 8 * 8;
constexpr size_t OFF_XS = OFF_H + 2 * SZ_Z;
constexpr size_t OFF_CSQ = OFF_XS + 3 * SZ_XS;
constexpr size_t OFF_ZP = OFF_CSQ + 256 * 512;
constexpr size_t SZ_ZP = (size_t)16 * 16 * 1024;
constexpr size_t OFF_PQ = OFF_ZP + 2 * SZ_ZP;
constexpr size_t OFF_PO = OFF_PQ + (size_t)16 * 9 * 64 * 112 * 2;
constexpr size_t OFF_PI = OFF_PO + (size_t)D_ * D_ * 2;
constexpr size_t OFF_PW = OFF_PI + (size_t)32 * 9 * 64 * 144 * 2;
constexpr size_t OFF_CSP = OFF_PW + (size_t)FH_ * D_ * 2;
constexpr size_t OFF_CNT = OFF_CSP + (size_t)18 * F_ * 4;
constexpr int CN_X1 = 0, CN_H = 2, CN_X2 = 4, CN_O = 5, CN_Z = 7, CN_PK = 9, CN_H2 = 10, NCNT = 12;
constexpr size_t OFF_TAG = OFF_CNT + (size_t)NCNT * 256 * 128;
constexpr size_t OFF_HELLO = OFF_TAG + 128;
constexpr size_t OFF_POT = OFF_HELLO + 2048;
constexpr size_t OFF_CSK = OFF_POT + 1024;
constexpr size_t WS_END = OFF_CSK + 256 * 2048;

DEVI float bf_lo(unsigned u) { return __uint_as_float(u << 16); }
DEVI float bf_hi(unsigned u) { return __uint_as_float(u & 0xffff0000u); }
DEVI unsigned pack2(float a, float b) {
  f32x2 v = {a, b};
  bf16x2 r = __builtin_convertvector(v, bf16x2);
  return __builtin_bit_cast(unsigned, r);
}
DEVI float rbf(float a) {
  return bf_lo(pack2(a, 0.f));
}

DEVI float sum_g(float x) {
  const unsigned u = __float_as_uint(x);
  const auto p = __builtin_amdgcn_permlane32_swap(u, u, false, false);
  const unsigned v = __float_as_uint(__uint_as_float(p[0]) + __uint_as_float(p[1]));
  const auto q = __builtin_amdgcn_permlane16_swap(v, v, false, false);
  return __uint_as_float(q[0]) + __uint_as_float(q[1]);
}

DEVI unsigned vconst(unsigned c) {
  unsigned r;
  asm volatile("v_mov_b32 %0, %1" : "=v"(r) : "s"(c));
  return r;
}
DEVI void place() {
  __builtin_amdgcn_sched_barrier(0);
  asm volatile(".p2align 6");
  __builtin_amdgcn_sched_barrier(0);
}
DEVI const char* uni_ptr(const char* p) {
  unsigned long long v = (unsigned long long)p;
  unsigned lo = __builtin_amdgcn_readfirstlane((unsigned)v), hi = __builtin_amdgcn_readfirstlane((unsigned)(v >> 32));
  asm("s_nop 4" : "+s"(lo), "+s"(hi));
  return (const char*)(((unsigned long long)hi << 32) | lo);
}

DEVI void dma16(const char* sbase, unsigned voff, unsigned m0val) {
  asm volatile("s_mov_b32 m0, %2\n\ts_nop 0\n\tglobal_load_lds_dwordx4 %0, %1"
               :
               : "v"(voff), "s"(sbase), "s"(m0val)
               : "memory");
}

DEVI void ln_finish(float s, float q, float invn, float& mb, float& rinv) {
  float mean = s * invn;
  asm("" : "+v"(mean));
  float var = fmaxf(__builtin_fmaf(-mean, mean, q * invn), 0.f);
  mb = rbf(mean);
  float v = rbf(var);
  float t = rbf(v + 1e-5f);
  float sd = rbf(__builtin_amdgcn_sqrtf(t));
  rinv = __builtin_amdgcn_rcpf(sd);
}

DEVI unsigned pack2v(f32x2 v) { return pack2(v[0], v[1]); }

template <int BM_, int BN_>
struct GemmCfg {
  static constexpr int BM = BM_, BN = BN_, NS = 4;
  static constexpr int P = BN * 2;
  static constexpr int XB = BM * 128, WB = 64 * P, SB = XB + WB;
  static constexpr int LDS_BYTES = NS * SB;
};

DEVI int swap23(int r) { return (r & ~12) | ((r & 4) << 1) | ((r & 8) >> 1); }

DEVI void lds_wr32(unsigned addr, unsigned v) { asm volatile("ds_write_b32 %0, %1" ::"v"(addr), "v"(v) : "memory"); }

template <class C, int MT, int NT, int D, int R, bool XCM, int WT, bool SQ = false, unsigned long long WS = 0>
struct GemmMath {
  static constexpr int WO = WT ? 1 : 2;
  static constexpr int NS = C::NS, SB = C::SB, P = C::P, NU = 2 * NT;
  static constexpr int XPU = MT <= 2 ? MT : MT - 2;
  static constexpr int XU = (MT + XPU - 1) / XPU;
  static_assert(NU % R == 0 && R > D && D <= NT && XU <= D, "shape");
  s16x4 wlo[R], whi[R];
  s16x8 wf[R];
  s16x8 xf[2][MT];
  unsigned xrd[2], wrd, xa[2], wa, so;
  f32x4 (&acc)[MT][NT];
  f32x4 accq[SQ ? MT : 1];
  unsigned long long wtab = WS;
  DEVI GemmMath(f32x4 (&a)[MT][NT]) : acc(a) {}

  template <int PU>
  DEVI void wread_lo() {
    constexpr int off = (PU / NT) * 32 * P + (PU % NT) * (WT == 2 ? 1024 : 32);
    if constexpr (WT) asm volatile("ds_read_b128 %0, %1 offset:%2" : "=v"(wf[PU % R]) : "v"(wa), "n"(off));
    else asm volatile("ds_read_b64_tr_b16 %0, %1 offset:%2" : "=v"(wlo[PU % R]) : "v"(wa), "n"(off));
  }
  template <int PU>
  DEVI void wread_hi() {
    if constexpr (!WT)
      asm volatile("ds_read_b64_tr_b16 %0, %1 offset:%2" : "=v"(whi[PU % R]) : "v"(wa), "n"((PU / NT) * 32 * P + (PU % NT) * 32 + 8 * P));
  }
  template <int H, int M>
  DEVI void xread1() {
    asm volatile("ds_read_b128 %0, %1 offset:%2" : "=v"(xf[H][M]) : "v"(xa[H]), "n"(M * (XCM ? 256 : 2048)));
  }
  static constexpr int xops(int v) {
    const int k = (v + D) % NT;
    if (k >= XU) return 0;
    const int n = MT - k * XPU;
    return n < XPU ? n : XPU;
  }
  static constexpr int ops(int v, bool last) { return (last && v + D >= NU) ? 0 : WO + xops(v); }
  static constexpr int newer(int U, bool last) {
    int n = 0;
    int v0 = U - D;
    if (U % NT == 0) v0 += XU - 1;
    else n += v0 >= 0 ? xops(v0) : xops(v0 + NU);
    for (int v = v0 + 1; v < U; ++v) n += v >= 0 ? ops(v, last) : ops(v + NU, false);
    return n;
  }
  template <int U, int M_>
  DEVI void mf() {
    constexpr int M = ((U % NT) & 1) ? MT - 1 - M_ : M_;
    s16x8 w;
    if constexpr (WT) w = wf[U % R];
    else w = __builtin_shufflevector(wlo[U % R], whi[U % R], 0, 1, 2, 3, 4, 5, 6, 7);
    acc[M][U % NT] = __builtin_amdgcn_mfma_f32_16x16x32_bf16(w, xf[U / NT][M], acc[M][U % NT], 0, 0, 0);
    __builtin_amdgcn_sched_barrier(0);
  }
  DEVI void pin() {
#pragma unroll
    for (int m = 0; m < MT; ++m)
#pragma unroll
      for (int n = 0; n < NT; ++n) asm volatile("" : "+v"(acc[m][n]));
  }
  template <int U, int I>
  DEVI void dsop() {
    constexpr int PU = (U + D) % NU;
    if constexpr (I == 0) wread_lo<PU>();
    else if constexpr (I == 1) wread_hi<PU>();
    else if constexpr (I - 2 < xops(U)) {
      constexpr int k = (U + D) % NT;
      xread1<((U + D - k) % NU) / NT, k * XPU + I - 2>();
    }
  }
  template <int U, bool LAST, bool BAR, int M>
  DEVI void slot() {
    constexpr bool PF = !(LAST && U + D >= NU);
    mf<U, M>();
    if constexpr (M == 0 && PF && U + D == NU) {
      if constexpr (BAR) __builtin_amdgcn_s_barrier();
      so = (so == (unsigned)((NS - 1) * SB)) ? 0u : so + (unsigned)SB;
      wa = wrd + (WS ? ((unsigned)wtab & 63u) << 11 : so);
      wtab >>= 6;
      xa[0] = xrd[0] + so;
      xa[1] = xrd[1] + so;
      __builtin_amdgcn_sched_barrier(0);
    }
    if constexpr (PF) {
      dsop<U, M>();
      if constexpr (M == MT - 1) {
        if constexpr (MT <= 1) dsop<U, 1>();
        if constexpr (MT <= 2) {
          dsop<U, 2>();
          dsop<U, 3>();
        }
      }
      __builtin_amdgcn_sched_barrier(0);
    }
  }
  template <int U, bool LAST, bool BAR, int... M>
  DEVI void unit(std::integer_sequence<int, M...>) {
    static_assert(newer(U, LAST) <= 15, "lgkmcnt range");
    asm volatile("s_waitcnt lgkmcnt(%0)" ::"n"(newer(U, LAST)));
    __builtin_amdgcn_sched_barrier(0);
    (slot<U, LAST, BAR, M>(), ...);
    if constexpr (SQ && (U % NT) < MT) {
      constexpr int m = U % NT;
      accq[m] = __builtin_amdgcn_mfma_f32_16x16x32_bf16(xf[U / NT][m], xf[U / NT][m], accq[m], 0, 0, 0);
      __builtin_amdgcn_sched_barrier(0);
    }
    __builtin_amdgcn_sched_barrier(0);
  }
  template <bool LAST, bool BAR, int... U>
  DEVI void stage(std::integer_sequence<int, U...>) {
    (unit<U, LAST, BAR>(std::make_integer_sequence<int, MT>{}), ...);
  }
  template <int... U>
  DEVI void prow(std::integer_sequence<int, U...>) {
    ((wread_lo<U>(), wread_hi<U>()), ...);
  }
  template <int... M>
  DEVI void prox(std::integer_sequence<int, M...>) {
    (xread1<0, M>(), ...);
  }
  DEVI void setup(unsigned lds0, int mt0, int nt0, int lane, unsigned so0) {
    const int g = lane >> 4, r = lane & 15, q = (lane >> 2) & 3, p = lane & 3;
#pragma unroll
    for (int h = 0; h < 2; ++h)
      xrd[h] = XCM ? lds0 + (unsigned)((4 * h + g) * (C::BM * 16) + (mt0 * 16 + r) * 16)
                   : lds0 + (unsigned)((mt0 * 16 + r) * 128 + (((4 * h + g) ^ ((r >> 1) & 7)) * 16));
    wrd = WT == 2   ? lds0 + (unsigned)(C::XB + nt0 * 1024 + lane * 16)
          : WT == 1 ? lds0 + (unsigned)(C::XB + (g * 8 + (r & 7)) * P + (r >> 3) * 16 + nt0 * 32)
                    : lds0 + (unsigned)(C::XB + (16 * (g >> 1) + 4 * (g & 1) + q) * P + 8 * p + nt0 * 32);
    so = so0;
    xa[0] = xrd[0] + so;
    xa[1] = xrd[1] + so;
    wa = wrd + so;
  }
  DEVI void first() {
    prox(std::make_integer_sequence<int, MT>{});
    prow(std::make_integer_sequence<int, D>{});
    asm volatile("s_waitcnt lgkmcnt(0)");
  }
  template <int N, bool BAR = true>
  DEVI void mid() {
    if constexpr (N > 1) place();
#pragma unroll 1
    for (int s = 0; s < N; ++s) stage<false, BAR>(std::make_integer_sequence<int, NU>{});
  }
  DEVI void last() {
    stage<true, true>(std::make_integer_sequence<int, NU>{});
    pin();
  }
  template <int KS>
  DEVI void run(unsigned lds0, int mt0, int nt0, int lane, unsigned so0) {
    setup(lds0, mt0, nt0, lane, so0);
    __builtin_amdgcn_s_barrier();
    first();
    mid<KS - 1>();
    last();
  }
};

DEVI const char* code_here() {
  unsigned long long pc;
  asm volatile("s_getpc_b64 %0" : "=s"(pc));
  return (const char*)pc;
}
DEVI int okmap(int p) {
  const int ch = p >> 3, e = p & 7, c = (ch - 8) & 3;
  return ch < 8 ? (2 * ch + (e >> 2)) * 36 + 16 + (e & 3) : ((ch - 8) >> 2) * 36 + c * 8 + (c >> 1) * 4 + e;
}

DEVI void pk_st16(void* p, u32x4 v) {
  asm volatile("global_store_dwordx4 %0, %1, off sc1\n\ts_nop 1" ::"v"(p), "v"(v) : "memory");
}

using K1C = GemmCfg<144, 112>;
constexpr int K1_TP = 240;
constexpr int K1_T_OFF = K1C::SB;
constexpr int K1_ST_OFF = 163840 - 128 - 1152;
constexpr int K1_CS_OFF = 4 * K1C::SB;
constexpr int K1_WX = 4 * K1C::SB + 2048;
constexpr unsigned long long K1_WS = 16ull | 32ull << 6 | 48ull << 12 | 56ull << 18 | 63ull << 24 | 16ull << 30 | 32ull << 36 | 48ull << 42;
static_assert(K1C::SB == 16 * 2048 && K1_WX - K1C::XB == 56 * 2048 && K1C::WB == 7 * 2048 && K1_WX + 2 * K1C::WB <= K1_ST_OFF, "K1 w places");
static_assert(K1_T_OFF + 144 * K1_TP + 64 <= 2 * K1C::SB + K1C::XB, "K1 LDS alias");
using K1R = GemmCfg<144, 144>;
constexpr int K1R_T_OFF = K1R::SB;
constexpr int K1R_CS_OFF = K1R_T_OFF + 144 * K1_TP;
constexpr int K1R_ST_OFF = K1R_CS_OFF + 512;
constexpr int K1R_DUMMY = K1R::LDS_BYTES;
constexpr int K1R_PO = K1R_DUMMY + 1024;
constexpr int K1R_CNT = 163840 - 128 + 28 * 4;
#define SPIN_ASM(TEST)                                                                                                   \
  if constexpr (SL <= 1)                                                                                                 \
    asm volatile(".La%=:\n\tds_read_b32 %0, %3\n\ts_waitcnt lgkmcnt(0)\n\tv_readfirstlane_b32 %2, %0\n\t" TEST SAFE_AND \
                 "s_cbranch_scc1 .La%="                                                                                  \
                 : "=&v"(v), NOP_(c), "=&s"(t)                                                                           \
                 : "v"(addr), "s"(k)                                                                                     \
                 : "memory", "scc");                                                                                     \
  else                                                                                                                   \
    asm volatile("s_branch .Lb%=\n.La%=:\n\ts_sleep %5\n.Lb%=:\n\tds_read_b32 %0, %3\n\ts_waitcnt lgkmcnt(0)\n\t"     \
                 "v_readfirstlane_b32 %2, %0\n\t" TEST SAFE_AND "s_cbranch_scc1 .La%="                                  \
                 : "=&v"(v), NOP_(c), "=&s"(t)                                                                           \
                 : "v"(addr), "s"(k), "n"(SL)                                                                            \
                 : "memory", "scc")
template <int SL>
DEVI void spin_ge(unsigned addr, unsigned k) {
  unsigned v, c = 1u << 18, t;
  SPIN_ASM("s_cmp_lt_u32 %2, %4\n\t");
}
template <int SL>
DEVI void spin_b4(unsigned addr, unsigned n) {
  unsigned v, c = 1u << 18, t;
  const unsigned k = (0x80u - n) * 0x01010101u;
  SPIN_ASM("s_add_u32 %2, %2, %4\n\ts_andn2_b32 %2, 0x80808080, %2\n\t");
}
DEVI void msync(unsigned addr) {
  asm volatile("s_waitcnt lgkmcnt(0)" ::: "memory");
  asm volatile("s_mov_b64 exec, 1\n\tds_add_u32 %0, %1\n\ts_mov_b64 exec, -1" ::"v"(addr), "v"(vconst(1u)) : "memory");
  spin_ge<0>(addr, 8u);
}
static_assert(K1R_PO + 13824 + 256 <= 163840 - 128 && K1R_ST_OFF + 144 * 8 <= 3 * K1R::SB, "K1R LDS");

DEVI void touch64(const char* sbase, unsigned voff, unsigned m0val) {
  asm volatile("s_mov_b32 m0, %2\n\ts_nop 0\n\tglobal_load_lds_dword %0, %1" : : "v"(voff), "s"(sbase), "s"(m0val) : "memory");
}

template <int MT, bool G1>
DEVI void k1_gemm_raw(char* smem, int hp, int mt0, int lane MF_ARG) {
  constexpr int NT = 4, nt0 = G1 ? 4 : 0;
  const unsigned lds0 = (unsigned)(size_t)smem;
  const int g = lane >> 4, r = lane & 15;
  unsigned ta[NT];
#pragma unroll
  for (int nt = 0; nt < NT; ++nt) {
    const int j = (nt0 + nt) * 16 + g * 4, seg = j / 40, cj = j - seg * 40 - (hp & 1) * 4;
    const int to = (seg < 3 && cj >= 0 && cj < 36) ? seg * 80 + cj * 2 + (cj >= 20 ? 8 : 0) : (seg < 3 ? 40 : 120);
    ta[nt] = lds0 + (unsigned)(K1R_T_OFF + (mt0 * 16 + r) * K1_TP + to);
    asm volatile("" : "+v"(ta[nt]));
  }
  unsigned sa = lds0 + (unsigned)(K1R_ST_OFF + (mt0 * 16 + r) * 8), ca = lds0 + (unsigned)(K1R_CS_OFF + (nt0 * 16 + g * 4) * 4);
  asm volatile("" : "+v"(sa), "+v"(ca));
  f32x4 acc[MT][NT];
#pragma unroll
  for (int mt = 0; mt < MT; ++mt)
#pragma unroll
    for (int nt = 0; nt < NT; ++nt) acc[mt][nt] = f32x4{0.f, 0.f, 0.f, 0.f};
  {
    GemmMath<K1R, MT, 4, 3, 4, false, 0, G1> gm(acc);
    if constexpr (G1) {
#pragma unroll
      for (int mt = 0; mt < MT; ++mt) gm.accq[mt] = f32x4{0.f, 0.f, 0.f, 0.f};
    }
    MF(14);
    if constexpr (!G1) {
      if (mt0 < 7) asm volatile("s_waitcnt vmcnt(0)" ::: "memory");
    }
    MF(15);
    gm.template run<9>(lds0, mt0, nt0, lane, 0u);
    MF(2);
    if constexpr (G1) {
#pragma unroll
      for (int mt = 0; mt < MT; ++mt) {
        const f32x4 q = gm.accq[mt];
        const float d = (r & 2) ? ((r & 1) ? q[3] : q[2]) : ((r & 1) ? q[1] : q[0]);
        const float qq = sum_g(g == (r >> 2) ? d : 0.f);
        float mb, rinv;
        ln_finish(acc[mt][3][0], qq, 1.0f / (float)D_, mb, rinv);
        const f32x2 st = {-mb, rinv};
        if (g == 2) asm volatile("ds_write_b64 %0, %1 offset:%2" ::"v"(sa), "v"(st), "n"(mt * 128) : "memory");
      }
    }
  }
  if (mt0 == 0 && r == 0) {
#pragma unroll
    for (int nt = 0; nt < NT; ++nt) *reinterpret_cast<f32x4*>(smem + K1R_CS_OFF + ((nt0 + nt) * 16 + g * 4) * 4) = acc[0][nt];
  }
  msync(lds0 + (unsigned)K1R_CNT);
  MF(3);
  f32x2 st[MT];
  f32x4 cs[NT];
#pragma unroll
  for (int nt = 0; nt < NT; ++nt) asm volatile("ds_read_b128 %0, %1 offset:%2" : "=v"(cs[nt]) : "v"(ca), "n"(nt * 64) : "memory");
#pragma unroll
  for (int mt = 0; mt < MT; ++mt) asm volatile("ds_read_b64 %0, %1 offset:%2" : "=v"(st[mt]) : "v"(sa), "n"(mt * 128) : "memory");
#pragma unroll
  for (int i = 0; i < MT; ++i) asm volatile("s_waitcnt lgkmcnt(0)" : "+v"(st[i]));
#pragma unroll
  for (int i = 0; i < NT; ++i) asm volatile("" : "+v"(cs[i]));
#pragma unroll
  for (int mt = 0; mt < MT; ++mt) {
    f32x2 M = {st[mt][0], st[mt][0]}, R = {st[mt][1], st[mt][1]};
    asm volatile("" : "+v"(M), "+v"(R));
#pragma unroll
    for (int nt = 0; nt < NT; ++nt) {
      const f32x2 v0 = R * __builtin_elementwise_fma(M, f32x2{cs[nt][0], cs[nt][1]}, f32x2{acc[mt][nt][0], acc[mt][nt][1]});
      const f32x2 v1 = R * __builtin_elementwise_fma(M, f32x2{cs[nt][2], cs[nt][3]}, f32x2{acc[mt][nt][2], acc[mt][nt][3]});
      acc[mt][nt][0] = __uint_as_float(pack2v(v0));
      acc[mt][nt][1] = __uint_as_float(pack2v(v1));
      const f32x2 pk = {acc[mt][nt][0], acc[mt][nt][1]};
      asm volatile("ds_write_b64 %0, %1 offset:%2" ::"v"(ta[nt]), "v"(pk), "n"(mt * 16 * K1_TP) : "memory");
    }
  }
}

template <int N>
DEVI void wait_vm() {
  asm volatile("s_waitcnt vmcnt(%0)" ::"n"(N) : "memory");
}

constexpr int LDS_ALL = 163840;
constexpr int LW_OFF = LDS_ALL - 128;
constexpr int LW_GO = 0, LW_T = 2, LW_O = 4  , LW_X1 = 6, LW_H = 8, LW_X2 = 12, LW_PK = 13, LW_Z = 14, LW_PAIR = 16, LW_AD = 20,
              LW_L = 22, LW_S = 24, LW_ST = 29, LW_HELLO = 30, LW_Q = 31;
DEVI void announce(unsigned addr, unsigned n, char* ws, int kind, int who, bool act) {
  const char* cb = uni_ptr(ws + OFF_CNT + (size_t)kind * 256 * 128);
  const unsigned long long mask = __ballot(act);
  unsigned t, u;
  asm volatile(
      "s_waitcnt vmcnt(0)\n\t"
      "s_mov_b64 exec, 1\n\t"
      "ds_add_rtn_u32 %0, %2, %3\n\t"
      "s_waitcnt lgkmcnt(0)\n\t"
      "v_readfirstlane_b32 %1, %0\n\t"
      "s_cmp_eq_u32 %1, %4\n\t"
      "s_cselect_b64 exec, %5, 0\n\t"
      "global_atomic_add %6, %3, %7 sc1\n\t"
      "s_mov_b64 exec, -1\n\t"
      "s_nop 0"
      : "=&v"(t), "=&s"(u)
      : "v"(addr), "v"(vconst(1u)), "s"(n - 1u), "s"(mask), "v"((unsigned)(who & 255) * 128u), "s"(cb)
      : "memory", "scc");
}
DEVI void cnt_wait(const char* ws, int kind, int who, unsigned n) {
  const char* cp = uni_ptr(ws + OFF_CNT + (size_t)(kind * 256 + who) * 128);
  unsigned v, c = SPIN_MEM, t;
  asm volatile(
      ".La%=:\n\tglobal_load_dword %0, %3, %4 sc1\n\ts_sub_u32 %1, %1, 1\n\ts_waitcnt vmcnt(0)\n\tv_readfirstlane_b32 %2, %0\n\ts_cmp_lg_u32 %2, %5\n\t"
      "s_cselect_b32 %2, %1, 0\n\ts_cmp_lg_u32 %2, 0\n\ts_cbranch_scc1 .La%="
      : "=&v"(v), "+s"(c), "=&s"(t)
      : "v"(vconst(0u)), "s"(cp), "s"(n - 1u)
      : "memory", "scc");
}
DEVI void dma16_sc1(const char* sbase, unsigned voff, unsigned m0val) {
  asm volatile("s_mov_b32 m0, %2\n\ts_nop 0\n\tglobal_load_lds_dwordx4 %0, %1 sc1"
               :
               : "v"(voff), "s"(sbase), "s"(m0val)
               : "memory");
}
template <int N, bool SC1>
DEVI void dma4(const char* sbase, unsigned v0, unsigned v1, unsigned v2, unsigned v3, unsigned m0val) {
#define DMA4(S)                                                                                                               \
  asm volatile("s_mov_b32 m0, %5\n\ts_nop 0\n\tglobal_load_lds_dwordx4 %0, %4" S "\n\t.if %6 > 1\n\tglobal_load_lds_dwordx4 %1, %4 offset:1024" S \
               "\n\t.endif\n\t.if %6 > 2\n\tglobal_load_lds_dwordx4 %2, %4 offset:2048" S                                    \
               "\n\t.endif\n\t.if %6 > 3\n\tglobal_load_lds_dwordx4 %3, %4 offset:3072" S "\n\t.endif"                      \
               :                                                                                                              \
               : "v"(v0), "v"(v1), "v"(v2), "v"(v3), "s"(sbase), "s"(m0val), "n"(N)                                           \
               : "memory")
  if constexpr (SC1) DMA4(" sc1");
  else DMA4("");
}
template <int N>
DEVI void dma_seq(const char* src, unsigned voff, unsigned m0val) {
  if constexpr (N > 0) {
    dma4<(N < 4 ? N : 4), true>(src, voff, voff, voff, voff, m0val);
    dma_seq<N - 4>(src + 4096, voff, m0val + 4096u);
  }
}
DEVI void bar() { __builtin_amdgcn_s_barrier(); }
DEVI void lds_inc(unsigned addr, unsigned by = 1u) {
  asm volatile("s_mov_b64 exec, 1\n\tds_add_u32 %0, %1\n\ts_mov_b64 exec, -1" ::"v"(addr), "v"(vconst(by)) : "memory");
}
DEVI void lds_spin(unsigned addr, unsigned n) {
  spin_ge<1>(addr, n);
}
template <int SL = 1>
DEVI void lds_spin4(unsigned addr, unsigned n) {
  spin_b4<SL>(addr, n);
}
DEVI int lane_id() {
  int l;
  asm volatile("v_mbcnt_lo_u32_b32 %0, -1, 0\n\tv_mbcnt_hi_u32_b32 %0, -1, %0" : "=v"(l));
  __builtin_assume((unsigned)l < 64u);
  return l;
}

template <int I>
DEVI u32x4 tr8(const u32x4 (&v)[8]) {
  constexpr unsigned sel = (I & 1) ? 0x07060302u : 0x05040100u;
  return u32x4{__builtin_amdgcn_perm(v[1][I / 2], v[0][I / 2], sel), __builtin_amdgcn_perm(v[3][I / 2], v[2][I / 2], sel),
               __builtin_amdgcn_perm(v[5][I / 2], v[4][I / 2], sel), __builtin_amdgcn_perm(v[7][I / 2], v[6][I / 2], sel)};
}
template <int PITCH, bool SUM, int... I>
DEVI void tr8_store(const u32x4 (&v)[8], unsigned dst, float (&cs)[8], std::integer_sequence<int, I...>) {
  auto one = [&](auto I_) {
    const u32x4 o = tr8<I_>(v);
    const unsigned d_ = dst;
    asm volatile("ds_write_b128 %0, %1 offset:%2" ::"v"(d_), "v"(o), "n"(I_ * PITCH) : "memory");
    if constexpr (SUM) {
#pragma unroll
      for (int d = 0; d < 4; ++d) {
        const unsigned od = o[d];
        cs[I_] = __builtin_amdgcn_fdot2_f32_bf16(__builtin_bit_cast(bf16x2, od), __builtin_bit_cast(bf16x2, 0x3f803f80u), cs[I_], false);
      }
    }
  };
  (one(std::integral_constant<int, I>{}), ...);
}
template <int NB>
DEVI void lds_out(unsigned src, char* dst, int lane) {
  constexpr int N = (NB + 1023) / 1024;
  u32x4 t[N];
  const unsigned a = src + (unsigned)lane * 16u;
#pragma unroll
  for (int i = 0; i < N; ++i) asm volatile("ds_read_b128 %0, %1 offset:%2" : "=v"(t[i]) : "v"(a), "n"(i * 1024) : "memory");
  const char* d = uni_ptr(dst);
#pragma unroll
  for (int i = 0; i < N; ++i) {
    asm volatile("s_waitcnt lgkmcnt(%1)" : "+v"(t[i]) : "n"(N - 1 - i) : "memory");
    if (i < N - 1 || lane < (NB - (N - 1) * 1024) / 16)
      asm volatile("global_store_dwordx4 %0, %1, %2 sc1\n\ts_nop 1" ::"v"((unsigned)lane * 16u), "v"(t[i]), "s"(d + i * 1024) : "memory");
  }
}
template <int A, int B, class F>
DEVI void static_for(F f) {
  if constexpr (A < B) {
    f(std::integral_constant<int, A>{});
    static_for<A + 1, B>(f);
  }
}
constexpr int WSP_V = 3;
constexpr int RAW_S1 = 6;
constexpr int RAW_S0 = 4;
template <int E>
struct RawX {
  static constexpr int first(int s) {
    return s <= RAW_S0 ? 0 : (s > RAW_S1 ? E : (E * (s - RAW_S0) + RAW_S1 - RAW_S0) / (RAW_S1 + 1 - RAW_S0));
  }
  static constexpr int n(int s) { return first(s + 1) - first(s); }
};
struct RawLd {
  static constexpr int VPS = 10, SB = K1R::SB;
  unsigned xoff[5], woff[5], m0x;
  unsigned long long xact0, wact[5], tag;
  const char *xp, *wp;
  int qb;
  DEVI void q_hook(int st, char* ws, unsigned lds0, int hp) {
    if ((qb >> 1) == st) {
      const int lane = lane_id(), g = lane >> 4;
      const unsigned a = lds0 + (unsigned)((st & 3) * SB + K1R::XB + ((qb & 1) * 32 + 16 * (g >> 1) + 4 * (g & 1) + ((lane >> 2) & 3)) * 288 +
                                           (lane & 3) * 8 + (hp & 1) * 8);
      const unsigned a2 = a + ((lane & 3) >= 1 ? 8u : 0u), a4 = a + ((lane & 3) >= 2 ? 8u : 0u);
      u32x2 qf[7][2];
#pragma unroll
      for (int nt = 0; nt < 7; ++nt)
        asm volatile("ds_read_b64_tr_b16 %0, %2 offset:%3\n\tds_read_b64_tr_b16 %1, %2 offset:%4"
                     : "=&v"(qf[nt][0]), "=&v"(qf[nt][1])
                     : "v"(nt == 2 ? a2 : (nt == 4 ? a4 : a)), "n"(nt * 32 + (nt > 4 ? 16 : (nt > 2 ? 8 : 0))),
                       "n"(2304 + nt * 32 + (nt > 4 ? 16 : (nt > 2 ? 8 : 0)))
                     : "memory");
      const char* d = uni_ptr(ws + OFF_PQ + (size_t)(hp * 18 + qb) * 7168 + 3072);
#pragma unroll
      for (int nt = 0; nt < 7; ++nt) {
        asm volatile("s_waitcnt lgkmcnt(%2)" : "+v"(qf[nt][0]), "+v"(qf[nt][1]) : "n"(12 - 2 * nt) : "memory");
        const u32x4 v = {qf[nt][0][0], qf[nt][0][1], qf[nt][1][0], qf[nt][1][1]};
        asm volatile("global_store_dwordx4 %0, %1, %2 offset:%3 sc1\n\ts_nop 1" ::"v"((unsigned)lane * 16u), "v"(v), "s"(d), "n"(nt * 1024 - 3072) : "memory");
      }
      if (st == 8) lds_wr32(lds0 + (unsigned)(LW_OFF + LW_Q * 4), 1u);
    }
  }
  DEVI void stage_x(unsigned so) {
    asm volatile(
        "s_mov_b32 m0, %6\n\ts_mov_b64 exec, %7\n\tglobal_load_lds_dwordx4 %0, %5\n\ts_mov_b64 exec, -1\n\t"
        "global_load_lds_dwordx4 %1, %5 offset:1024\n\tglobal_load_lds_dwordx4 %2, %5 offset:2048\n\tglobal_load_lds_dwordx4 %3, %5 offset:3072\n\t"
        "s_add_u32 m0, m0, 0x1000\n\ts_mov_b32 exec_hi, 0\n\tglobal_load_lds_dwordx4 %4, %5\n\ts_mov_b32 exec_hi, -1" ::"v"(xoff[0]),
        "v"(xoff[1]), "v"(xoff[2]), "v"(xoff[3]), "v"(xoff[4]), "s"(xp), "s"(m0x + so), "s"(xact0)
        : "memory", "scc");
    xp += 128;
  }
  DEVI void stage_w(unsigned so) {
    asm volatile(
        "s_mov_b32 m0, %6\n\ts_mov_b64 exec, %7\n\tglobal_load_lds_dwordx4 %0, %5\n\ts_mov_b64 exec, %8\n\t"
        "global_load_lds_dwordx4 %1, %5 offset:1024\n\ts_mov_b64 exec, %9\n\tglobal_load_lds_dwordx4 %2, %5 offset:2048\n\t"
        "s_mov_b64 exec, %10\n\tglobal_load_lds_dwordx4 %3, %5 offset:3072\n\ts_add_u32 m0, m0, 0x1000\n\ts_mov_b64 exec, %11\n\t"
        "global_load_lds_dwordx4 %4, %5\n\ts_mov_b64 exec, -1" ::"v"(woff[0]),
        "v"(woff[1]), "v"(woff[2]), "v"(woff[3]), "v"(woff[4]), "s"(wp), "s"(m0x + so + (unsigned)K1R::XB), "s"(wact[0]), "s"(wact[1]),
        "s"(wact[2]), "s"(wact[3]), "s"(wact[4])
        : "memory", "scc");
    wp += 64 * NQKV * 2;
  }
  DEVI void stage(unsigned so) {
    stage_x(so);
    stage_w(so);
  }
  DEVI void front(const u16* x, const u16* wqkv, const u16* wo, char* ws, int bid, unsigned lds0, int wl, int lane, int rt, int hp,
                  int pj MF_ARG) {
    xp = uni_ptr((const char*)x - 3072);
    wp = uni_ptr((const char*)wqkv + ((hp * 72) & ~15) - 3072);
    m0x = lds0 + (unsigned)(wl * 288 * 16);
    xact0 = wl ? ~0ull : ~0xffull;
    const int row0 = wl * 36 + (lane >> 3);
#pragma unroll
    for (int i = 0; i < 5; ++i) {
      const int row = row0 + i * 8;
      int gr = rt * 128 - 16 + row;
      if (gr < 0) gr = 0;
      xoff[i] = (unsigned)(gr * (D_ * 2) + (3 - (i & 3)) * 1024) + (unsigned)(((lane ^ (row >> 1)) & 7) * 16);
    }
    stage_x(0u);
    u32x2 tg;
    asm volatile("global_load_dwordx2 %0, %1, %2 sc1" : "=v"(tg) : "v"(0u), "s"(uni_ptr(ws + OFF_TAG)) : "memory");
    asm volatile("" : "+v"(lane));
#pragma unroll
    for (int i = 0; i < 5; ++i) {
      const unsigned c = (unsigned)(i * 64 + (lane & 63));
      const unsigned wr = (c * 3641u) >> 16, wc = c - wr * 18u, seg = (wc * 13u) >> 6;
      wact[i] = __ballot(wc < 15u) & (i < 4 ? ~0ull : 0xffffffffull);
      woff[i] = (unsigned)(wl * 16 + swap23((int)wr)) * (unsigned)(NQKV * 2) + seg * (unsigned)(D_ * 2 - 80) + wc * 16u + (unsigned)((3 - (i & 3)) * 1024);
    }
    stage_w(0u);
    stage((unsigned)SB);
    MF(10);
    asm volatile("s_waitcnt vmcnt(%1)" : "+v"(tg) : "n"(VPS) : "memory");
    tag = (((unsigned long long)(unsigned)__builtin_amdgcn_readfirstlane(tg[1]) << 32) | (unsigned)__builtin_amdgcn_readfirstlane(tg[0])) +
          0x9e3779b97f4a7c15ull;
    MF(1);
    __builtin_amdgcn_s_barrier();
    MF(15);
    if (wl == 0 && lane == 0)
      asm volatile("global_store_dwordx2 %0, %1, off sc1\n\ts_nop 1" ::"v"(ws + OFF_HELLO + bid * 8), "v"(tag) : "memory");
    stage((unsigned)(2 * SB));
    qb = wl == 3 ? rt : ((wl == 2 && rt < 2) ? 16 + rt : 99);
    q_hook(0, ws, lds0, hp);
    const int nb = pj;
    const bool po = pj >= 0 && wl < 2;
    u32x4 v[8];
    unsigned da = 0u;
    if (po) {
      const int l54 = lane < 54 ? lane : 53, gl = l54 / 9, c = l54 - gl * 9, kl = wl * 6 + gl, t = nb / 6, kg = (nb - t * 6) * 12 + kl;
      da = lds0 + (unsigned)(K1R_PO + (kl >> 2) * 4608 + (16 * (kl & 1) + ((kl >> 1) & 1)) * 144 + c * 16);
      const unsigned sa[2] = {(unsigned)(okmap(kg * 8) * D_ + t * 72 + c * 8) * 2u, (unsigned)(okmap(kg * 8 + 4) * D_ + t * 72 + c * 8) * 2u};
      const char* ps = uni_ptr((const char*)wo);
#pragma unroll
      for (int j = 0; j < 8; ++j)
        asm volatile("global_load_dwordx4 %0, %1, %2 offset:%3" : "=v"(v[j]) : "v"(sa[j / 4]), "s"(ps), "n"((j % 4) * D_ * 2) : "memory");
    } else {
#pragma unroll
      for (int j = 0; j < 8; ++j) asm volatile("" : "=v"(v[j]));
    }
    unsigned slot = (unsigned)(3 * SB);
#pragma unroll 1
    for (int S = 0; S < RAW_S0; ++S) {
      wait_vm<VPS>();
      __builtin_amdgcn_s_barrier();
      if (S == 0) MF(16);
      if (S == 1) MF(17);
      if (S == 2) MF(18);
      if (S == 3) MF(19);
      stage(slot);
      slot = (slot == (unsigned)(3 * SB)) ? 0u : slot + (unsigned)SB;
      q_hook(S + 1, ws, lds0, hp);
      if (po && S == 1) {
        float cs[8];
        asm volatile("s_nop 0 ; LOOPWAIT" : "+v"(v[0]), "+v"(v[1]), "+v"(v[2]), "+v"(v[3]), "+v"(v[4]), "+v"(v[5]), "+v"(v[6]), "+v"(v[7]));
        tr8_store<288, false>(v, da, cs, std::make_integer_sequence<int, 8>{});
        asm volatile("s_waitcnt lgkmcnt(0)" ::: "memory");
      }
      if (po && S == 2) lds_out<6912>(lds0 + (unsigned)(K1R_PO + wl * 6912), ws + OFF_PO + (size_t)nb * 13824 + wl * 6912, lane);
    }
  }
};
template <bool JOB>
DEVI void k1_raw_loader(RawLd& ld, const u16* __restrict__ w_in, const u16* __restrict__ w_out, char* __restrict__ ws,
                        unsigned long long tag, int bid, unsigned lds0, int wl, int lane, int rt, int hp, int ct, int job, int pj MF_ARG) {
  constexpr int VPS = RawLd::VPS, SB = RawLd::SB, E = JOB ? 16 : 0;
  const bool isi = job < 144;
  const int kb = isi ? job >> 3 : job - 144;
  const int pitch = isi ? F_ * 2 : D_ * 2;
  const int l36 = lane < 36 ? lane : 35;
  unsigned sa = (unsigned)l36 * 16u, da;
  if (isi) da = lds0 + (unsigned)((l36 >= 18 ? 9216 - 288 : 0) + wl * 8 * 288 + l36 * 16);
  else da = lds0 + (unsigned)((l36 / 9) * (4608 - 144) + (16 * (wl & 1) + (wl >> 1)) * 144 + l36 * 16);
  asm volatile("" : "+v"(sa), "+v"(da));
  u32x4 v[2][8];
  const char* ps = uni_ptr((const char*)(isi ? w_in + (job & 7) * 576 : w_out) + (size_t)(kb * 32 + wl * 8) * pitch);
  auto extra = [&](auto J_) {
    constexpr int j = J_;
    const char* q = ps + (j / 2) * pitch;
    u32x4 t;
    asm volatile("global_load_dwordx4 %0, %1, %2 offset:%3" : "=v"(t) : "v"(sa), "s"(q), "n"((j % 2) * 576) : "memory");
    v[j % 2][j / 2] = t;
  };
  auto step = [&](auto S_) {
    constexpr int S = S_;
    wait_vm<RawX<E>::n(S - 2) + (S <= 6 ? VPS : 0) + RawX<E>::n(S - 1)>();
    __builtin_amdgcn_s_barrier();
    MF(16 + S);
    if constexpr (S == RAW_S0) {
      static_assert(RAW_S0 >= 4, "wo blocks");
      if (pj >= 0 && wl == 0 && lane == 0)
        asm volatile("global_store_dwordx2 %0, %1, off sc1\n\ts_nop 1" ::"v"(ws + OFF_POT + (pj / 6) * 128 + (pj % 6) * 8), "v"(tag) : "memory");
    }
    if constexpr (S <= 5) ld.stage((unsigned)(((S + 3) % 4) * SB));
    ld.q_hook(S + 1, ws, lds0, hp);
    static_for<RawX<E>::first(S), RawX<E>::first(S + 1)>(extra);
  };
  const char* hp_ = ws + OFF_HELLO + lane * 32;
  u32x4 a, b;
  if (wl == 1) asm volatile("global_load_dwordx4 %0, %2, off sc1\n\tglobal_load_dwordx4 %1, %2, off offset:16 sc1" : "=&v"(a), "=&v"(b) : "v"(hp_) : "memory");
  else asm volatile("" : "=v"(a), "=v"(b));
  static_for<RAW_S0, 8>(step);
  MF(2);
  if (wl == 1) {
    asm volatile("" : "+v"(a), "+v"(b));
    const unsigned t0 = (unsigned)tag, t1 = (unsigned)(tag >> 32);
#pragma unroll 1
    for (unsigned spin = 0; spin < SPIN_MEM; ++spin) {
      const unsigned bad = ((a[0] ^ t0) | (a[2] ^ t0) | (b[0] ^ t0) | (b[2] ^ t0)) | ((a[1] ^ t1) | (a[3] ^ t1) | (b[1] ^ t1) | (b[3] ^ t1));
      if (!__any(bad != 0u)) break;
      __builtin_amdgcn_s_sleep(4);
      asm volatile("global_load_dwordx4 %0, %2, off sc1\n\tglobal_load_dwordx4 %1, %2, off offset:16 sc1\n\ts_waitcnt vmcnt(0)"
                   : "=&v"(a), "=&v"(b)
                   : "v"(hp_)
                   : "memory");
    }
    lds_wr32(lds0 + (unsigned)(LW_OFF + LW_HELLO * 4), 1u);
    if (bid == 0 && lane == 0) asm volatile("global_store_dwordx2 %0, %1, off sc1\n\ts_nop 1" ::"v"(ws + OFF_TAG), "v"(tag) : "memory");
  }
  MF(11);
  if (rt < 2) lds_spin(lds0 + (unsigned)(LW_OFF + LW_Q * 4), 1u);
  lds_spin(lds0 + (unsigned)K1R_CNT, 8u);
  if constexpr (JOB) {
#pragma unroll
    for (int b = 0; b < 2; ++b)
      asm volatile("s_waitcnt vmcnt(0)"
                   : "+v"(v[b][0]), "+v"(v[b][1]), "+v"(v[b][2]), "+v"(v[b][3]), "+v"(v[b][4]), "+v"(v[b][5]), "+v"(v[b][6]), "+v"(v[b][7])
                   :
                   : "memory");
  }
  MF(12);
  const char* tp = ws + OFF_POT + ct * 128 + (lane < 6 ? lane : 5) * 8;
  u32x2 f;
  asm volatile("global_load_dwordx2 %0, %1, off sc1" : "=v"(f) : "v"(tp) : "memory");
  if constexpr (JOB) {
    static_assert(SB == 4 * 9216, "staging");
#pragma unroll
    for (int b = 0; b < 2; ++b) {
      float cs[8] = {0.f, 0.f, 0.f, 0.f, 0.f, 0.f, 0.f, 0.f};
      tr8_store<288, true>(v[b], da + (unsigned)(b * 18432), cs, std::make_integer_sequence<int, 8>{});
      const f32x4 c0 = {cs[0], cs[1], cs[2], cs[3]}, c1 = {cs[4], cs[5], cs[6], cs[7]};
      const unsigned ca = lds0 + (unsigned)(3 * SB + wl * 2304) + sa * 2u;
      asm volatile("ds_write_b128 %0, %1 offset:%2" ::"v"(ca), "v"(c0), "n"(b * 1152) : "memory");
      asm volatile("ds_write_b128 %0, %1 offset:%2" ::"v"(ca), "v"(c1), "n"(b * 1152 + 16) : "memory");
    }
    lds_inc(lds0 + (unsigned)(LW_OFF + LW_ST * 4));
    lds_spin(lds0 + (unsigned)(LW_OFF + LW_ST * 4), 4u);
    if (isi) {
      const int j = (job & 7) * 4 + wl, ti = j < 16 ? 2 * j + 1 : 2 * (j - 16);
      lds_out<9216>(lds0 + (unsigned)(wl * 9216), ws + OFF_PI + (size_t)(ti * 18 + kb) * 9216, lane);
      f32x4 c[4];
      const unsigned ca = lds0 + (unsigned)(3 * SB + wl * 576) + sa;
#pragma unroll
      for (int i = 0; i < 4; ++i) asm volatile("ds_read_b128 %0, %1 offset:%2" : "=v"(c[i]) : "v"(ca), "n"(i * 2304) : "memory");
      asm volatile("s_waitcnt lgkmcnt(0)" : "+v"(c[0]), "+v"(c[1]), "+v"(c[2]), "+v"(c[3]));
      const f32x4 t = (c[0] + c[1]) + (c[2] + c[3]);
      if (lane < 36) pk_st16(ws + OFF_CSP + ((size_t)kb * F_ + ti * 144) * 4 + sa, __builtin_bit_cast(u32x4, t));
    } else {
      lds_out<4608>(lds0 + (unsigned)(wl * 9216), ws + OFF_PW + (size_t)(wl * 144 + kb) * 4608, lane);
      lds_out<4608>(lds0 + (unsigned)(wl * 9216 + 4608), ws + OFF_PW + (size_t)(wl * 144 + 72 + kb) * 4608, lane);
    }
  }
  MF(8);
  announce(lds0 + (unsigned)(LW_OFF + LW_PK * 4), 4u, ws, CN_PK, isi ? 8 + (job & 3) * 4 + lane : 24 + lane, JOB && lane < (isi ? 4 : 8));
  MF(9);
  if (wl == 0) {
    asm volatile("" : "+v"(f));
#pragma unroll 1
    for (unsigned spin = 0; spin < SPIN_MEM; ++spin) {
      if (!__any(f[0] != (unsigned)tag || f[1] != (unsigned)(tag >> 32))) break;
      __builtin_amdgcn_s_sleep(2);
      asm volatile("global_load_dwordx2 %0, %1, off sc1\n\ts_waitcnt vmcnt(0)" : "=&v"(f) : "v"(tp) : "memory");
    }
  }
}

template <int MT, int NT>
DEVI void k1_gemm(char* smem, const char* ws, int rt, int hp, int mt0, int nt0,
                  bool late, int lane MK_ARG) {
  const unsigned lds0 = (unsigned)(size_t)smem;
  const int g = lane >> 4;
  f32x4 cs[NT];
  unsigned ta[NT];
  unsigned cad[NT];
#pragma unroll
  for (int nt = 0; nt < NT; ++nt) {
    int c = (nt0 + nt) * 16 + g * 4;
    if (c > 104) c = 104;
    const int part = c / 36, ci = c - part * 36;
    cad[nt] = lds0 + (unsigned)(K1_CS_OFF + (part * 40 + (hp & 1) * 4 + ci) * 4);
    asm volatile("" : "+v"(cad[nt]));
    const int to = (nt0 + nt) * 16 + g * 4 > 104 ? 120 : part * 80 + ci * 2 + (ci >= 20 ? 8 : 0);
    ta[nt] = lds0 + (unsigned)(K1_T_OFF + (mt0 * 16 + (lane & 15)) * K1_TP + to);
    asm volatile("" : "+v"(ta[nt]));
  }
  unsigned sa = lds0 + (unsigned)(K1_ST_OFF + (mt0 * 16 + (lane & 15)) * 8);
  asm volatile("" : "+v"(sa));
  f32x4 acc[MT][NT];
#pragma unroll
  for (int mt = 0; mt < MT; ++mt)
#pragma unroll
    for (int nt = 0; nt < NT; ++nt) acc[mt][nt] = f32x4{0.f, 0.f, 0.f, 0.f};
  f32x2 st[MT];
  if (late) bar();
  if constexpr (NT == 7) cnt_wait(ws, CN_X2, rt * 16 + hp, rt ? 24u : 16u);
  bar();
  MK(1);
  {
    GemmMath<K1C, MT, NT, (NT == 7 ? 5 : 3), (NT == 3 ? 6 : NT), true, 2, false, K1_WS> gm(acc);
    gm.setup(lds0, mt0, nt0, lane, 0u);
    bar();
#pragma unroll
    for (int nt = 0; nt < NT; ++nt) asm volatile("ds_read_b128 %0, %1" : "=v"(cs[nt]) : "v"(cad[nt]));
    gm.first();
    gm.template mid<8>();
#pragma unroll
    for (int mt = 0; mt < MT; ++mt) asm volatile("ds_read_b64 %0, %1 offset:%2" : "=v"(st[mt]) : "v"(sa), "n"(mt * 128));
    asm volatile("s_waitcnt vmcnt(0)" ::: "memory");
    gm.last();
  }
#pragma unroll
  for (int nt = 0; nt < NT; ++nt) asm volatile("" : "+v"(cs[nt]));
#pragma unroll
  for (int mt = 0; mt < MT; ++mt) asm volatile("" : "+v"(st[mt]));
  MK(2);
#pragma unroll
  for (int mt = 0; mt < MT; ++mt) {
    f32x2 M = {st[mt][0], st[mt][0]}, R = {st[mt][1], st[mt][1]};
    asm volatile("" : "+v"(M), "+v"(R));
#pragma unroll
    for (int nt = 0; nt < NT; ++nt) {
      const f32x2 v0 = R * __builtin_elementwise_fma(M, f32x2{cs[nt][0], cs[nt][1]}, f32x2{acc[mt][nt][0], acc[mt][nt][1]});
      const f32x2 v1 = R * __builtin_elementwise_fma(M, f32x2{cs[nt][2], cs[nt][3]}, f32x2{acc[mt][nt][2], acc[mt][nt][3]});
      acc[mt][nt][0] = __uint_as_float(pack2v(v0));
      acc[mt][nt][1] = __uint_as_float(pack2v(v1));
      const f32x2 pk = {acc[mt][nt][0], acc[mt][nt][1]};
      asm volatile("ds_write_b64 %0, %1 offset:%2" ::"v"(ta[nt]), "v"(pk), "n"(mt * 16 * K1_TP) : "memory");
    }
  }
}

DEVI float vmax(float a, float b) {
  float r;
  asm("v_max_f32 %0, %1, %2" : "=v"(r) : "v"(a), "v"(b));
  return r;
}
struct Attn {
  unsigned ka, kb, va, vb, m0, m1, m2, pm[2], oa, pa;
  f32x4 mk;
  unsigned long long sel[4];
  const char* ob;
  DEVI void setup(unsigned T, char* o, int r0, int hp, int wave, int lane) {
    const int g = lane >> 4, r = lane & 15, blk = wave >> 1, hd = wave & 1;
    const unsigned co = (unsigned)(g < 2 ? hd * 48 + g * 16 : 32);
    ka = T + (unsigned)((blk * 32 + 2 * r) * K1_TP) + co;
    kb = T + (unsigned)((blk * 32 + 2 * (r & 7)) * K1_TP) + co;
    const int vr = 4 * g + ((lane >> 2) & 3);
    va = T + (unsigned)((blk * 32 + 2 * vr) * K1_TP + (lane & 3) * 8);
    vb = T + (unsigned)((blk * 32 + 2 * (vr & 7)) * K1_TP + (lane & 3) * 8);
    m0 = (g < 2 || (g == 2 && hd == 0)) ? ~0u : 0u;
    m1 = (g < 2 || (g == 2 && hd == 1)) ? ~0u : 0u;
    m2 = g < 2 ? ~0u : 0u;
    pm[0] = pm[1] = 0u;
    const float ci = __uint_as_float(vconst(0xff800000u));
    const unsigned ch = vconst(0xffffu);
#pragma unroll
    for (int e = 0; e < 4; ++e) {
      const int w1 = r + 8 - 4 * g - e;
      mk[e] = (w1 & 15) <= 7 ? 0.f : ci;
      sel[e] = __ballot(w1 >= 8);
      if (w1 >= 8) pm[e >> 1] |= ch << (16 * (e & 1));
    }
    oa = (unsigned)((8 + hp * 4 + hd * 2 + (g >> 1)) * 1024 + ((blk & 1) * 32 + 2 * r + (g & 1)) * 16);
    pa = ((oa & 1023u) >> 2) + (unsigned)((hp * 2 + hd) * 256);
    ob = uni_ptr(o + (size_t)((r0 >> 6) + (wave >> 2)) * (72 * 1024));
    asm volatile("" : "+v"(ka), "+v"(kb), "+v"(va), "+v"(vb), "+v"(m0), "+v"(m1), "+v"(m2), "+v"(pm[0]), "+v"(pm[1]), "+v"(oa));
    asm volatile("" : "+v"(mk), "+v"(pa), "+s"(ob), "+s"(sel[0]), "+s"(sel[1]), "+s"(sel[2]), "+s"(sel[3]));
  }
  DEVI void run(int r0, int hp, int wave, int lane) {
    const int hd = wave & 1;
    u32x4 q[2], k0[2], k1[2];
    u32x2 v[2][2][2];
#pragma unroll
    for (int p = 0; p < 2; ++p) {
      asm volatile("ds_read_b128 %0, %1 offset:%2" : "=v"(q[p]) : "v"(ka), "n"((16 + p) * K1_TP) : "memory");
      asm volatile("ds_read_b128 %0, %1 offset:%2" : "=v"(k0[p]) : "v"(ka), "n"(p * K1_TP + 80) : "memory");
      asm volatile("ds_read_b128 %0, %1 offset:%2" : "=v"(k1[p]) : "v"(kb), "n"((32 + p) * K1_TP + 80) : "memory");
    }
    const unsigned vh = (unsigned)hd * 48u;
#pragma unroll
    for (int p = 0; p < 2; ++p) {
      asm volatile("ds_read_b64_tr_b16 %0, %1 offset:%2" : "=v"(v[p][0][0]) : "v"(va + vh), "n"(p * K1_TP + 160) : "memory");
      asm volatile("ds_read_b64_tr_b16 %0, %1 offset:%2" : "=v"(v[p][0][1]) : "v"(vb + vh), "n"((32 + p) * K1_TP + 160) : "memory");
      asm volatile("ds_read_b64_tr_b16 %0, %1 offset:%2" : "=v"(v[p][1][0]) : "v"(va), "n"(p * K1_TP + 192) : "memory");
      asm volatile("ds_read_b64_tr_b16 %0, %1 offset:%2" : "=v"(v[p][1][1]) : "v"(vb), "n"((32 + p) * K1_TP + 192) : "memory");
    }
    const u32x4 one4 = {vconst(0x3f803f80u), vconst(0x3f803f80u), vconst(0x3f803f80u), vconst(0x3f803f80u)};
    const s16x8 ones = __builtin_bit_cast(s16x8, one4);
    const float c = 0.23570226039551584f * 1.4426950408889634f;
    const f32x4 z = {0.f, 0.f, 0.f, 0.f};
    f32x4 sc[2][2];
#pragma unroll
    for (int p = 0; p < 2; ++p) {
      asm volatile("s_waitcnt lgkmcnt(%3)" : "+v"(q[p]), "+v"(k0[p]), "+v"(k1[p]) : "n"(11 - 3 * p));
      const s16x8 qf = __builtin_bit_cast(s16x8, q[p] & u32x4{m0, m1, m2, m2});
      sc[p][0] = __builtin_amdgcn_mfma_f32_16x16x32_bf16(__builtin_bit_cast(s16x8, k0[p]), qf, mk, 0, 0, 0);
      sc[p][1] = __builtin_amdgcn_mfma_f32_16x16x32_bf16(__builtin_bit_cast(s16x8, k1[p]), qf, mk, 0, 0, 0);
      __builtin_amdgcn_sched_barrier(0);
    }
    if (__builtin_expect(r0 == 0 && wave < 2, 0)) {
#pragma unroll
      for (int p = 0; p < 2; ++p)
#pragma unroll
        for (int e = 0; e < 4; ++e)
          if (lane < 32) sc[p][0][e] = mk[e] - 4.242640687e9f;
    }
    float lg[2][4], mx[2];
#pragma unroll
    for (int p = 0; p < 2; ++p) {
#pragma unroll
      for (int e = 0; e < 4; ++e) lg[p][e] = __builtin_amdgcn_inverse_ballot_w64(sel[e]) ? sc[p][1][e] : sc[p][0][e];
      asm("v_max3_f32 %0, %1, %2, %3\n\tv_max_f32 %0, %0, %4" : "=&v"(mx[p]) : "v"(lg[p][0]), "v"(lg[p][1]), "v"(lg[p][2]), "v"(lg[p][3]));
    }
    {
      const auto a = __builtin_amdgcn_permlane32_swap(__float_as_uint(mx[0]), __float_as_uint(mx[1]), false, false);
      const unsigned b = __float_as_uint(vmax(__uint_as_float(a[0]), __uint_as_float(a[1])));
      const auto d = __builtin_amdgcn_permlane16_swap(b, b, false, false);
      const unsigned f = __float_as_uint(vmax(__uint_as_float(d[0]), __uint_as_float(d[1])) * -c);
      const auto h = __builtin_amdgcn_permlane32_swap(f, f, false, false);
      mx[0] = __uint_as_float(h[0]);
      mx[1] = __uint_as_float(h[1]);
    }
#pragma unroll
    for (int p = 0; p < 2; ++p)
#pragma unroll
      for (int e = 0; e < 4; ++e) lg[p][e] = __builtin_amdgcn_exp2f(__builtin_fmaf(lg[p][e], c, mx[p]));
    asm volatile("s_waitcnt lgkmcnt(0)"
                 : "+v"(v[0][0][0]), "+v"(v[0][0][1]), "+v"(v[0][1][0]), "+v"(v[0][1][1]), "+v"(v[1][0][0]), "+v"(v[1][0][1]),
                   "+v"(v[1][1][0]), "+v"(v[1][1][1]));
    __builtin_amdgcn_sched_barrier(0);
    f32x4 su[2], o0[2], o1[2];
    s16x8 pp[2];
#pragma unroll
    for (int p = 0; p < 2; ++p) {
      const unsigned p0 = pack2(lg[p][0], lg[p][1]), p1 = pack2(lg[p][2], lg[p][3]);
      pp[p] = __builtin_bit_cast(s16x8, u32x4{p0 & ~pm[0], p1 & ~pm[1], p0 & pm[0], p1 & pm[1]});
    }
    __builtin_amdgcn_sched_barrier(0);
#pragma unroll
    for (int p = 0; p < 2; ++p) {
      const s16x8 pf = pp[p];
      const u32x4 v0 = {v[p][0][0][0], v[p][0][0][1], v[p][0][1][0], v[p][0][1][1]};
      const u32x4 v1 = {v[p][1][0][0], v[p][1][0][1], v[p][1][1][0], v[p][1][1][1]};
      su[p] = __builtin_amdgcn_mfma_f32_16x16x32_bf16(ones, pf, z, 0, 0, 0);
      o0[p] = __builtin_amdgcn_mfma_f32_16x16x32_bf16(__builtin_bit_cast(s16x8, v0), pf, z, 0, 0, 0);
      o1[p] = __builtin_amdgcn_mfma_f32_16x16x32_bf16(__builtin_bit_cast(s16x8, v1), pf, z, 0, 0, 0);
    }
    __builtin_amdgcn_sched_barrier(0);
    u32x2 y[2], yl;
    unsigned hm = (unsigned)hd - 1u;
    asm volatile("" : "+s"(hm));
#pragma unroll
    for (int p = 0; p < 2; ++p) {
      const float inv = __builtin_amdgcn_rcpf(su[p][0]);
      y[p] = u32x2{pack2(o0[p][0] * inv, o0[p][1] * inv), pack2(o0[p][2] * inv, o0[p][3] * inv)};
      const unsigned la = pack2(o1[p][0] * inv, o1[p][1] * inv), lb = pack2(o1[p][2] * inv, o1[p][3] * inv);
      yl[p] = (la & hm) | (lb & ~hm);
    }
    const auto s0 = __builtin_amdgcn_permlane16_swap(y[0][0], y[1][0], false, false);
    const auto s1 = __builtin_amdgcn_permlane16_swap(y[0][1], y[1][1], false, false);
    const u32x4 w = {s0[0], s1[0], s0[1], s1[1]};
    asm volatile("global_store_dwordx4 %0, %1, %4 sc1\n\ts_mov_b64 exec, 0xffff\n\tglobal_store_dwordx2 %2, %3, %4 sc1\n\ts_mov_b64 exec, -1\n\ts_nop 0" ::"v"(oa),
                 "v"(w), "v"(pa), "v"(yl), "s"(ob)
                 : "memory");
  }
};

#define NL_MAIN 650
DEVI void math_start(char* smem, const u16* b_sp, const u16* wqkv, char* ws, int bid, int wave, int lane) {
  const unsigned lds0 = (unsigned)(size_t)smem;
  const int tid = wave * 64 + lane, idx = bid >> 3;
  if (wave < 3) {
    static_assert(2 * SZ_ZP == (size_t)256 * 2048 && NCNT <= 16, "mark split");
    const u32x4 mk = {0xffffffffu, 0xffffffffu, 0xffffffffu, 0xffffffffu};
    char* c = ws + OFF_CNT + (size_t)((lane >> 3) * 256 + bid) * 128 + (lane & 7) * 16;
    pk_st16(wave < 2 ? ws + OFF_ZP + (size_t)((bid * 2 + wave) * 64 + lane) * 16 : c, mk);
    if (wave == 2 && lane < (NCNT - 8) * 8) pk_st16(c + 8 * 256 * 128, mk);
  }
  if (wave == 3) {
    const int li = idx * 54 + (lane < 54 ? lane : 53);
    touch64(uni_ptr((const char*)wqkv), (unsigned)(li * 1152 + 512), lds0 + (unsigned)(K1R_DUMMY + 768));
  }
  if (wave == 5 && idx == 31) touch64(uni_ptr((const char*)b_sp), (unsigned)lane * 64u, lds0 + (unsigned)(K1R_DUMMY + 768));
  const u32x4 ones = {0x3f803f80u, 0x3f803f80u, 0x3f803f80u, 0x3f803f80u};
  if (tid < 256) *reinterpret_cast<u32x4*>(smem + (tid >> 6) * K1R::SB + K1R::XB + (tid & 63) * 288 + 240) = ones;
  else if (tid < 288) *reinterpret_cast<u32x4*>(smem + ((tid - 256) >> 3) * K1R::SB + (tid & 7) * 16) = ones;
  else if (tid < 296) *reinterpret_cast<u32x4*>(smem + LW_OFF + (tid - 288) * 16) = u32x4{0u, 0u, 0u, 0u};
  asm volatile("s_waitcnt lgkmcnt(0)" ::: "memory");
}

template <int NST, int NS, int NSW, int WOFF, int XW, int X0, int S0, unsigned BAR>
struct ResX {
  static constexpr int WS = 4 * 4608, R = 10, V1 = 5 * NS, NV = V1 + 10 * (NST - NS), NG = (NSW + 2) / 3;
  s16x8 xf[XW][2];
  s16x8 wf[R];
  unsigned wrd[2][NG], xo, ea, fa, fv, tag;
  const char* xp[3];
  f32x4 (&acc)[2][5];
  DEVI ResX(f32x4 (&a)[2][5]) : acc(a) {}
  static constexpr int st(int V) { return V < V1 ? V / 5 : NS + (V - V1) / 10; }
  static constexpr int hf(int V) { return V < V1 ? 0 : ((V - V1) / 5) & 1; }
  static constexpr bool first(int V) { return V < V1 ? V % 5 == 0 : (V - V1) % 10 == 0; }
  static constexpr int wreq(int V) {
    int n = V + 5;
    if (V >= V1 - 4) n += V < V1 ? V - (V1 - 5) : 4;
    return n < NV ? n : NV;
  }
  static constexpr int XU = V1 + 1, XF = V1 + 11, XR = V1 + 15, XC = V1 + 21;
  static constexpr int oth(int V) { return NS && ((V >= XU && V <= XF && (V - XU) % 2 == 0) || V == XR || (V > XC && V <= XC + 5)) ? 1 : 0; }
  static constexpr int newer(int V) {
    const int F = V + 1 < NV ? V + 1 : V;
    int v = V - 1, n = 0;
    for (; v >= 0 && wreq(v - 1) > F; --v) n += wreq(v) - wreq(v - 1) + oth(v);
    return n + wreq(v) - 1 - F + (v >= 0 ? oth(v) : 0);
  }
  static constexpr int xreq(int T) {
    int n = X0 + 2 * (T + 1);
    if (n > T + XW) n = T + XW;
    if (NS && T <= 11 && n > 18) n = 18;
    if (T < 0) n = X0;
    return n < NST ? n : NST;
  }
  static_assert(NS == 0 || (xreq(NS - 2) == NST && XC + 5 < NV), "the last steps only compute and swap");
  template <int T, int M>
  DEVI void xld() {
    asm volatile("global_load_dwordx4 %0, %1, %2 sc1" : "=v"(xf[T % XW][M]) : "v"(xo), "s"(xp[M == 0 ? 0 : (T < NS ? 1 : 2)]));
    if constexpr (M == 1 && T + 1 < NST) asm volatile("v_add_u32 %0, 0x2000, %0" : "+v"(xo));
  }
  template <int V>
  DEVI void rd() {
    constexpr int T = st(V), sl = (T / 2 + S0) % NSW;
    asm volatile("ds_read_b128 %0, %1 offset:%2" : "=v"(wf[V % R]) : "v"(wrd[hf(V)][sl / 3]), "n"((sl % 3) * WS + (T & 1) * 9216 + (V % 5) * 32));
  }
  template <int V, int M, int H>
  DEVI void mf() {
    acc[M][V % 5] = __builtin_amdgcn_mfma_f32_16x16x32_bf16(wf[V % R], xf[st(V) % XW][H], acc[M][V % 5], 0, 0, 0);
    __builtin_amdgcn_sched_barrier(0);
  }
  template <int V>
  DEVI void unit() {
    constexpr int T = st(V), n = V % 5, nx = xreq(T) - xreq(T - 1), nw = wreq(V) - wreq(V - 1);
    constexpr int vm = 2 * (xreq(T - 1) - 1 - T);
    static_assert(newer(V) <= 15, "lgkmcnt range");
    if constexpr (first(V) && V % 2 == 0) asm volatile("s_waitcnt vmcnt(%0) lgkmcnt(%1)" ::"n"(vm), "n"(newer(V)));
    else if constexpr (first(V)) asm volatile("s_waitcnt vmcnt(%0)" ::"n"(vm));
    else if constexpr (V % 2 == 0) asm volatile("s_waitcnt lgkmcnt(%0)" ::"n"(newer(V)));
    __builtin_amdgcn_sched_barrier(0);
    if constexpr (V < V1) mf<V, V & 1, V & 1>();
    else mf<V, 0, hf(V)>();
    if constexpr (nw > 0) rd<wreq(V - 1)>();
    __builtin_amdgcn_sched_barrier(0);
    if constexpr (V < V1) {
      mf<V, 1 - (V & 1), 1 - (V & 1)>();
      if constexpr (nw > 1) rd<wreq(V - 1) + 1>();
      if constexpr (nx == 2 && n < 4) xld<xreq(T - 1) + n / 2, n % 2>();
      if constexpr (nx == 1 && n == (T & 1)) xld<xreq(T - 1), 0>();
      if constexpr (nx == 1 && n == (T & 1) + 2) xld<xreq(T - 1), 1>();
      if constexpr (n == 4 && (T & 1) && ((BAR >> (T / 2)) & 1u)) bar();
    } else if constexpr (NS == 0) {
      constexpr int k = (V % 10) / 2;
      if constexpr (V % 2 == 0 && k < 2 * nx) xld<xreq(T - 1) + k / 2, k % 2>();
      if constexpr (V % 10 == 8 && nx > 0 && xreq(T) == NST)
        asm volatile("s_mov_b64 exec, 1\n\tds_add_u32 %0, %1\n\ts_mov_b64 exec, -1" ::"v"(fa), "v"(fv));
    } else {
      if constexpr (oth(V) && V < XF) asm volatile("ds_write_b128 %0, %1 offset:%2" ::"v"(ea), "v"(acc[1][(V - XU) / 2]), "n"((V - XU) * 512));
      if constexpr (V == XF) asm volatile("ds_write_b32 %0, %1" ::"v"(fa), "v"(fv));
      if constexpr (V == XF + 2) asm volatile("v_xor_b32 %0, 4, %0" : "+v"(fa));
      if constexpr (V == XR) asm volatile("ds_read_b32 %0, %1" : "+v"(fv) : "v"(fa));
      if constexpr (V == XR + 2) asm volatile("v_xor_b32 %0, 0x2000, %0" : "+v"(ea));
      if constexpr (V == XC) {
        unsigned c = 1u << 18;
        asm volatile(
            "s_waitcnt lgkmcnt(%4)\n\tv_cmp_ne_u32 vcc, %3, %0\n\ts_cbranch_vccnz .Lw%=\n\t.subsection 1\n.Lw%=:\n\tds_read_b32 %0, %2\n\t"
            "s_waitcnt lgkmcnt(0)\n\tv_cmp_ne_u32 vcc, %3, %0\n\t" SAFE_OUT(".Lr%=") "s_cbranch_vccnz .Lw%=\n\ts_branch .Lr%=\n\t.subsection 0\n.Lr%=:"
            : "+v"(fv), NOP_(c)
            : "v"(fa), "s"(tag), "n"(wreq(XC) - wreq(XR))
            : "vcc", "scc");
      }
      if constexpr (oth(V) && V > XC) asm volatile("ds_read_b128 %0, %1 offset:%2" : "=v"(acc[1][V - XC - 1]) : "v"(ea), "n"((V - XC - 1) * 1024));
    }
    __builtin_amdgcn_sched_barrier(0);
  }
  template <int... V>
  DEVI void units(std::integer_sequence<int, V...>) {
    (unit<V>(), ...);
  }
  DEVI void run() {
    units(std::make_integer_sequence<int, NV>{});
    if constexpr (NS > 0) {
      asm volatile("s_waitcnt lgkmcnt(0)" : "+v"(acc[1][0]), "+v"(acc[1][1]), "+v"(acc[1][2]), "+v"(acc[1][3]), "+v"(acc[1][4]));
#pragma unroll
      for (int i = 0; i < 5; ++i) acc[0][i] += acc[1][i];
    }
  }
  template <int... V>
  DEVI void pro(std::integer_sequence<int, V...>) {
    (rd<V>(), ...);
  }
  DEVI void prow() { pro(std::make_integer_sequence<int, wreq(-1)>{}); }
  template <int T0, int... T>
  DEVI void prox(std::integer_sequence<int, T...>) {
    ((xld<T0 + T, 0>(), xld<T0 + T, 1>()), ...);
  }
  DEVI void setup(unsigned lds0, const char* xbase, int wave, int lane, unsigned red, unsigned nth) {
    const int g = lane >> 4, r = lane & 15, kq = wave & 1;
    xp[0] = xbase + wave * 256;
    xp[1] = xbase + (wave ^ 1) * 256;
    xp[2] = xp[0] + (kq ? -4096 : 4096);
    xo = (unsigned)((4 * kq + g) * 1024 + r * 16);
#pragma unroll
    for (int i = 0; i < 2 * NG; ++i) {
      wrd[i / NG][i % NG] = lds0 + (unsigned)(WOFF + (i % NG) * 3 * WS + (kq ^ (i / NG)) * 4608 + (2 * (r & 7) + 16 * (g & 1) + (g >> 1)) * 144 + (r >> 3) * 16);
      if (i < NG || NS == 0 || i % NG == ((NS / 2 + S0) % NSW) / 3) asm volatile("" : "+v"(wrd[i / NG][i % NG]));
    }
    asm volatile("" : "+v"(xo));
    if constexpr (NS > 0) {
      ea = lds0 + red + (unsigned)((wave ^ 1) * 8192 + lane * 16);
      fa = lds0 + (unsigned)(LW_OFF + (LW_PAIR + wave) * 4);
      fv = tag = nth;
      asm volatile("" : "+v"(ea), "+v"(fa), "+v"(fv));
    }
  }
  template <int N>
  DEVI void init(u32x2 (&xv)[5]) {
    asm volatile("s_waitcnt vmcnt(%5)" : "+v"(xv[0]), "+v"(xv[1]), "+v"(xv[2]), "+v"(xv[3]), "+v"(xv[4]) : "n"(N));
#pragma unroll
    for (int i = 0; i < 5; ++i) {
      acc[0][i] = f32x4{bf_lo(xv[i][0]), bf_hi(xv[i][0]), bf_lo(xv[i][1]), bf_hi(xv[i][1])};
      asm volatile("" : "+v"(acc[0][i]));
      acc[1][i] = f32x4{0.f, 0.f, 0.f, 0.f};
    }
  }
};

constexpr int P5_XW = 8, P5_X0 = 5, P2_X0 = 4;
constexpr unsigned P5_BAR = 0x2aaau;
constexpr int P5_W = 15 * 1024;
constexpr int P2_W = K1_ST_OFF - 18 * 4608;
constexpr int P5_RED = 0;
static_assert(P5_RED % 16384 == 0 && P5_RED + 3 * 8192 + 5120 <= P5_W + 18432, "exchange area");
static_assert(P2_W >= K1_T_OFF + 144 * K1_TP + 64 && P2_W % 16 == 0 && P5_W + 8 * 18432 <= LW_OFF, "LDS placement");

struct ResIn {
  const char* xp;
  unsigned o, o2, st;
  DEVI void setup(const char* src, bool rm, int rb, int ct, int wave, int lane) {
    const int g = lane >> 4, g2 = g & 1, rl = wave * 16 + (lane & 15);
    xp = uni_ptr(rm ? src + ((size_t)rb * 64 * D_ + ct * 72) * 2 : src + (size_t)(rb * 72 + ct * 9) * 1024);
    o = rm ? (unsigned)(rl * (D_ * 2) + g * 8) : (unsigned)((g >> 1) * 1024 + rl * 16 + g2 * 8);
    o2 = rm ? (unsigned)(rl * (D_ * 2) + g2 * 8 + 128) : (unsigned)(8192 + rl * 16 + g2 * 8);
    st = rm ? 32u : 2048u;
    asm volatile("" : "+v"(o), "+v"(o2));
    asm volatile("" : "+s"(xp));
  }
  DEVI void load(u32x2 (&xv)[5]) {
#pragma unroll
    for (int i = 0; i < 4; ++i) asm volatile("global_load_dwordx2 %0, %1, %2 sc1" : "=v"(xv[i]) : "v"(o), "s"(xp + i * st));
    asm volatile("global_load_dwordx2 %0, %1, %2 sc1" : "=v"(xv[4]) : "v"(o2), "s"(xp));
  }
};

DEVI void p2_gemm(f32x4 (&acc)[2][5], u32x2 (&xv)[5], unsigned lds0, const char* ob, int layer, int wave, int lane MK_ARG) {
  ResX<9, 0, 5, P2_W, 9, P2_X0, 0, 0u> gm(acc);
  gm.setup(lds0, ob, wave, lane, 0u, 0u);
  unsigned a = (gm.xo & ~1023u) | ((gm.xo & 1023u) >> 2);
  gm.xo += 8192u;
  gm.fa = lds0 + (unsigned)(LW_OFF + (LW_T + layer) * 4);
  gm.fv = vconst(1u);
  asm volatile("" : "+v"(a), "+v"(gm.xo), "+v"(gm.fa));
  const char* pp[2] = {gm.xp[0] - wave * 192, gm.xp[2] - wave * 192};
  place();
  bar();
  MK(6);
  unsigned e[2][4];
#pragma unroll
  for (int d = 0; d < 8; ++d)
    asm volatile("global_load_dword %0, %1, %2 offset:%3 sc1" : "=v"(e[d / 4][d % 4]) : "v"(a), "s"(pp[d / 4]), "n"((d % 4) * 256));
  gm.template prox<1>(std::make_integer_sequence<int, P2_X0 - 1>{});
  gm.prow();
  gm.template init<6 + 2 * P2_X0>(xv);
  asm volatile("s_waitcnt vmcnt(%8)"
               : "+v"(e[0][0]), "+v"(e[0][1]), "+v"(e[0][2]), "+v"(e[0][3]), "+v"(e[1][0]), "+v"(e[1][1]), "+v"(e[1][2]), "+v"(e[1][3])
               : "n"(2 * P2_X0 - 2));
  gm.xf[0][0] = __builtin_bit_cast(s16x8, u32x4{e[0][0], e[0][1], e[0][2], e[0][3]});
  gm.xf[0][1] = __builtin_bit_cast(s16x8, u32x4{e[1][0], e[1][1], e[1][2], e[1][3]});
  __builtin_amdgcn_sched_barrier(0);
  gm.run();
}

struct ResOut {
  const char *xp[3], *sp;
  unsigned xo, so;
  DEVI void setup(char* xim, char* xs, u16* out, bool fin, int rb, int ct, int wave, int lane) {
    const int g = lane >> 4, rl = wave * 16 + (lane & 15);
    xp[0] = uni_ptr(fin ? (const char*)(out + (size_t)rb * 64 * D_ + ct * 72) : xim + (size_t)(rb * 72 + ct * 9) * 1024);
    xp[1] = xp[0] + (fin ? 64 : 4096);
    xp[2] = xp[0] + (fin ? 128 : 8192);
    sp = uni_ptr(xs + (size_t)(rb * 8 + ct) * 512);
    xo = (unsigned)(((g & 1) * 2 + (g >> 1)) * (fin ? 16 : 1024) + rl * (fin ? D_ * 2 : 16));
    so = (unsigned)(rl * 8);
    asm volatile("" : "+v"(xo), "+v"(so), "+s"(xp[0]), "+s"(xp[1]), "+s"(xp[2]), "+s"(sp));
  }
  DEVI void run(const f32x4 (&a)[5], bool fin) {
    u32x2 y[5];
    u32x4 v[3];
#pragma unroll
    for (int nt = 0; nt < 5; ++nt) y[nt] = u32x2{pack2(a[nt][0], a[nt][1]), pack2(a[nt][2], a[nt][3])};
#pragma unroll
    for (int i = 0; i < 3; ++i) {
      const auto s0 = __builtin_amdgcn_permlane16_swap(y[2 * i][0], y[i < 2 ? 2 * i + 1 : 4][0], false, false);
      const auto s1 = __builtin_amdgcn_permlane16_swap(y[2 * i][1], y[i < 2 ? 2 * i + 1 : 4][1], false, false);
      v[i] = u32x4{s0[0], s1[0], s0[1], s1[1]};
    }
    if (fin) {
      asm volatile("global_store_dwordx4 %0, %1, %4\n\tglobal_store_dwordx4 %0, %2, %5\n\ts_mov_b64 exec, 0xffff\n\tglobal_store_dwordx4 %0, %3, %6\n\t"
                   "s_mov_b64 exec, -1\n\ts_nop 0" ::"v"(xo), "v"(v[0]), "v"(v[1]), "v"(v[2]), "s"(xp[0]), "s"(xp[1]), "s"(xp[2])
                   : "memory");
      return;
    }
    asm volatile("global_store_dwordx4 %0, %1, %4 sc1\n\tglobal_store_dwordx4 %0, %2, %5 sc1\n\ts_mov_b64 exec, 0xffff\n\t"
                 "global_store_dwordx4 %0, %3, %6 sc1\n\ts_mov_b64 exec, -1\n\ts_nop 0" ::"v"(xo), "v"(v[0]), "v"(v[1]), "v"(v[2]),
                 "s"(xp[0]), "s"(xp[1]), "s"(xp[2])
                 : "memory");
    const u32x4 one4 = {vconst(0x3f803f80u), vconst(0x3f803f80u), vconst(0x3f803f80u), vconst(0x3f803f80u)};
    const bool lo = __builtin_amdgcn_inverse_ballot_w64(0xffffull);
    v[2] = u32x4{lo ? v[2][0] : 0u, lo ? v[2][1] : 0u, lo ? v[2][2] : 0u, lo ? v[2][3] : 0u};
    f32x4 s = {0.f, 0.f, 0.f, 0.f}, q = s;
#pragma unroll
    for (int i = 0; i < 3; ++i) {
      const s16x8 u = __builtin_bit_cast(s16x8, v[i]);
      q = __builtin_amdgcn_mfma_f32_16x16x32_bf16(u, u, q, 0, 0, 0);
      s = __builtin_amdgcn_mfma_f32_16x16x32_bf16(__builtin_bit_cast(s16x8, one4), u, s, 0, 0, 0);
    }
    float q0 = q[0], q1 = q[1], q2 = q[2], q3 = q[3];
    const bool b0 = __builtin_amdgcn_inverse_ballot_w64(0xaaaaaaaaaaaaaaaaull), b1 = __builtin_amdgcn_inverse_ballot_w64(0xccccccccccccccccull);
    q0 = b0 ? q1 : q0;
    q2 = b0 ? q3 : q2;
    asm("" : "+v"(q0), "+v"(q2));
    const f32x2 sq = {s[0], b1 ? q2 : q0};
    asm volatile("s_mov_b64 exec, %3\n\tglobal_store_dwordx2 %0, %1, %2 sc1\n\ts_mov_b64 exec, -1" ::"v"(so), "v"(sq), "s"(sp), "s"(0xf0000f0000f0000full) : "memory");
  }
};

using K3C = GemmCfg<128, 144>;
constexpr int K3_Z1_OFF = K3C::LDS_BYTES;
constexpr int K3_C2_OFF = K3_Z1_OFF + K3C::XB;
constexpr int K3_ZC_OFF = K3_C2_OFF + 2048;
constexpr int K3_DM_OFF = K3_ZC_OFF + 1024;
constexpr int K3_ST_OFF = K3_Z1_OFF + 4 * 5120;
constexpr int K3_C1_OFF = K3_ST_OFF + 2048;
constexpr int K3_BS_OFF = K3_C1_OFF + 576;
static_assert(K3_BS_OFF + 512 <= LW_OFF, "K3 LDS");
static_assert(P5_W + 19 * 1024 == K3C::SB && K3C::SB == 34 * 1024, "w_out ring");
constexpr int K3_D = 4;

DEVI void red4(float (&t)[4], float& s, float& q) {
#pragma unroll
  for (int j = 0; j < 2; ++j) {
    const auto p = __builtin_amdgcn_permlane16_swap(__float_as_uint(t[j]), __float_as_uint(t[2 + j]), false, false);
    const unsigned v = __float_as_uint(__uint_as_float(p[0]) + __uint_as_float(p[1]));
    const auto w = __builtin_amdgcn_permlane32_swap(v, v, false, false);
    t[j] = __uint_as_float(w[0]) + __uint_as_float(w[1]);
    asm volatile("" : "+v"(t[j]));
  }
  s = t[0];
  q = t[1];
}
DEVI void red8(const f32x4& a, const f32x4& b, float& s, float& q) {
  float t[4];
#pragma unroll
  for (int j = 0; j < 4; ++j) {
    t[j] = a[j] + b[j];
    asm volatile("" : "+v"(t[j]));
  }
  red4(t, s, q);
}
DEVI void red16(const f32x4& a, const f32x4& b, const f32x4& c, const f32x4& d, float& s, float& q) {
  float t[4];
#pragma unroll
  for (int j = 0; j < 4; ++j) {
    float u = a[j] + b[j], v = c[j] + d[j];
    asm volatile("" : "+v"(u), "+v"(v));
    t[j] = u + v;
    asm volatile("" : "+v"(t[j]));
  }
  red4(t, s, q);
}

constexpr int b_k(int s) { return s < 2 ? s : (s < 4 ? 5 + s : (s < 6 ? 1 + s : (s == 6 ? 4 : s - 5))); }
template <bool B>
struct UMath {
  static constexpr int D = 4, R = 6, NV = 162, SB = K3C::SB, P = K3C::P;
  s16x8 wf[R], xf[2][2];
  s16x8 (&xc)[4][2][2];
  f32x4 (&acc)[2][9];
  f32x4 rc[4][2];
  f32x2 cs[9];
  unsigned ra, ca;
  template <int I>
  DEVI void cread() {
    if constexpr (I < 8) asm volatile("ds_read_b128 %0, %1 offset:%2" : "=v"(rc[I / 2][I % 2]) : "v"(ra), "n"((I / 2) * 512 + (I % 2) * 16));
    else if constexpr (I < 17) asm volatile("ds_read_b64 %0, %1 offset:%2" : "=v"(cs[I - 8]) : "v"(ca), "n"((I - 8) * 128));
  }
  unsigned xa, wa;
  unsigned fa, fw, fv;
  DEVI UMath(f32x4 (&a)[2][9], s16x8 (&x)[4][2][2]) : xc(x), acc(a) {}
  static constexpr int wslot(int s) { return B ? (s + 1) % 4 : s % 4; }
  static constexpr int xslot(int s) { return B ? b_k(s) % 5 : s % 5; }
  static constexpr bool res(int q) { return !B || b_k(q / 2) >= 4; }
  static constexpr bool chk(int s) { return s >= 3 && s < 7; }
  static constexpr bool xop(int v) { return v + D < NV && (v + D) % 9 < 2 && res((v + D) / 9); }
  static constexpr int ops1(int v) { return (xop(v) || (B && ((v % 18 == 9 && chk(v / 18)) || (v % 18 == 0 && v > 0)))) ? 1 : 0; }
  static constexpr int newer(int V) {
    int v0 = (V % 9 == 8 ? V : V + 1) - D, n = 0;
    if (v0 >= 0 && !(V % 9 == 0 && res(V / 9))) n += ops1(v0);
    for (int v = v0 + 1; v < V; ++v)
      if (v >= 0) n += (v + D < NV ? 1 : (B ? 0 : 4)) + ops1(v);
    return n;
  }
  template <int V>
  DEVI void wread() {
    asm volatile("ds_read_b128 %0, %1 offset:%2" : "=v"(wf[V % R]) : "v"(wa), "n"((wslot(V / 18) & 1) * SB + ((V % 18) / 9) * 32 * P + (V % 9) * 32));
  }
  template <int Q, int M>
  DEVI void xread() {
    asm volatile("ds_read_b128 %0, %1 offset:%2" : "=v"(xf[Q & 1][M]) : "v"(xa), "n"((xslot(Q / 2) & 1) * SB + (Q & 1) * 8192 + M * (B ? 256 : 64)));
  }
  template <int V, int M_>
  DEVI void mf() {
    constexpr int M = ((V % 9) & 1) ? 1 - M_ : M_, q = V / 9;
    if constexpr (!B) acc[M][V % 9] = __builtin_amdgcn_mfma_f32_16x16x32_bf16(xf[q & 1][M], wf[V % R], acc[M][V % 9], 0, 0, 0);
    else if constexpr (res(q)) acc[M][V % 9] = __builtin_amdgcn_mfma_f32_16x16x32_bf16(wf[V % R], xf[q & 1][M], acc[M][V % 9], 0, 0, 0);
    else acc[M][V % 9] = __builtin_amdgcn_mfma_f32_16x16x32_bf16(wf[V % R], xc[b_k(q / 2)][q & 1][M], acc[M][V % 9], 0, 0, 0);
    __builtin_amdgcn_sched_barrier(0);
  }
  template <int V>
  DEVI void unit() {
    constexpr int U = V % 18, s = V / 18;
    static_assert(newer(V) <= 15, "lgkmcnt range");
    if constexpr ((V % 9) % 2 == 0 && V > 0) asm volatile("s_waitcnt lgkmcnt(%0)" ::"n"(newer(V)));
    __builtin_amdgcn_sched_barrier(0);
    mf<V, 0>();
    if constexpr (V + D < NV) wread<V + D>();
    if constexpr (!B && V + D >= NV) {
      cread<(V + D - NV) * 4>();
      cread<(V + D - NV) * 4 + 1>();
    }
    if constexpr (s < 8 && U == 13 && (wslot(s + 1) >> 1) != (wslot(s) >> 1))
      asm volatile("v_add_u32 %0, %1, %0" : "+v"(wa) : "n"(((wslot(s + 1) >> 1) - (wslot(s) >> 1)) * 2 * SB));
    __builtin_amdgcn_sched_barrier(0);
    mf<V, 1>();
    if constexpr (xop(V)) xread<(V + D) / 9, (V + D) % 9>();
    if constexpr (!B && V + D >= NV) {
      cread<(V + D - NV) * 4 + 2>();
      cread<(V + D - NV) * 4 + 3>();
      if constexpr (V == NV - 1) cread<16>();
    }
    if constexpr (s < 8) {
      if constexpr (U == 11 && res(2 * s + 2) && (xslot(s + 1) >> 1) != (xslot(s) >> 1))
        asm volatile("v_add_u32 %0, %1, %0" : "+v"(xa) : "n"(((xslot(s + 1) >> 1) - (xslot(s) >> 1)) * 2 * SB));
      if constexpr (!B && U == 13) __builtin_amdgcn_s_barrier();
      if constexpr (B && U == 9 && chk(s)) asm volatile("ds_read_u8 %0, %1 offset:%2" : "+v"(fv) : "v"(fa), "n"(s == 6 ? 0 : 8));
      unsigned n = 1u << 18;
      if constexpr (B && U == 13 && chk(s)) {
        asm volatile(
            "v_cmp_gt_u32 vcc, %3, %0\n\ts_cbranch_vccnz .Lw%=\n\t.subsection 1\n.Lw%=:\n\tds_read_u8 %0, %2 offset:%4\n\ts_waitcnt lgkmcnt(0)\n\t"
            "v_cmp_gt_u32 vcc, %3, %0\n\t" SAFE_OUT(".Lr%=") "s_cbranch_vccnz .Lw%=\n\ts_branch .Lr%=\n\t.subsection 0\n.Lr%=:"
            : "+v"(fv), NOP_(n)
            : "v"(fa), "n"(s == 6 ? 4 : s + 2), "n"(s == 6 ? 0 : 8)
            : "vcc", "scc");
      }
    }
    if constexpr (B && U == 15) asm volatile("v_mov_b32 %0, %1" : "+v"(fv) : "n"(s + 1));
    if constexpr (B && ((U == 0 && s > 0) || V == NV - 1)) asm volatile("ds_write_b8 %0, %1" ::"v"(fw), "v"(fv));
    __builtin_amdgcn_sched_barrier(0);
  }
  template <int... V>
  DEVI void units(std::integer_sequence<int, V...>) {
    (unit<V>(), ...);
  }
  DEVI void first() {
    if constexpr (!B) {
      xread<0, 0>();
      xread<0, 1>();
    }
    wread<0>();
    wread<1>();
    wread<2>();
    wread<3>();
    asm volatile("s_waitcnt lgkmcnt(0)");
  }
  DEVI void run() {
    units(std::make_integer_sequence<int, NV>{});
#pragma unroll
    for (int m = 0; m < 2; ++m)
#pragma unroll
      for (int n = 0; n < 9; ++n) asm volatile("" : "+v"(acc[m][n]));
  }
};

constexpr int NR1 = 3;
DEVI void p3_g0(char* smem, char* __restrict__ ws, int layer, int mi, int ni, int wm, int lane MK_ARG) {
  constexpr int SB = K3C::SB;
  const unsigned lds0 = (unsigned)(size_t)smem;
  const int g = lane >> 4, r = lane & 15;
  const unsigned lw = lds0 + (unsigned)(LW_OFF + layer * 4), me = 1u << (8 * wm);
  f32x4 ac[2][9];
#pragma unroll
  for (int mt = 0; mt < 2; ++mt)
#pragma unroll
    for (int j = 0; j < 9; ++j) ac[mt][j] = f32x4{0.f, 0.f, 0.f, 0.f};
  unsigned ra = lds0 + (unsigned)(K3_ST_OFF + (wm * 32 + g * 8) * 4), ca = lds0 + (unsigned)(K3_C2_OFF + r * 8);
  asm volatile("" : "+v"(ra), "+v"(ca));
  s16x8 xc[4][2][2];
  UMath<false> gm(ac, xc);
  gm.ra = ra;
  gm.ca = ca;
  gm.xa = lds0 + (unsigned)(g * 2048 + (wm * 32 + 8 * (r >> 2) + (r & 3)) * 16);
  gm.wa = lds0 + (unsigned)(K3C::XB + (g * 8 + (r & 7)) * K3C::P + (r >> 3) * 16);
  unsigned adw = lw + LW_AD * 4, mev = vconst(me), po = (unsigned)(ni * 1024 + lane * 4);
  const char* zp = uni_ptr(ws + OFF_ZP + (size_t)layer * SZ_ZP + (size_t)mi * 16384 + wm * 256);
  asm volatile("" : "+v"(gm.xa), "+v"(gm.wa), "+v"(adw), "+v"(po));
  asm volatile("" : "+s"(zp));
  place();
  bar();
  gm.first();
  gm.run();
  asm volatile("s_mov_b64 exec, 1\n\tds_add_u32 %0, %1\n\ts_mov_b64 exec, -1" ::"v"(adw), "v"(mev) : "memory");
  MK(11);
  f32x2 z[9][4];
  f32x2 sm[4], sq[4];
  {
    f32x4 (&rc)[4][2] = gm.rc;
    f32x2 (&cs)[9] = gm.cs;
    asm volatile("s_waitcnt lgkmcnt(1)"
                 : "+v"(rc[0][0]), "+v"(rc[0][1]), "+v"(rc[1][0]), "+v"(rc[1][1]), "+v"(rc[2][0]), "+v"(rc[2][1]), "+v"(rc[3][0]), "+v"(rc[3][1]));
#pragma unroll
    for (int j = 0; j < 9; ++j) asm volatile("" : "+v"(cs[j]));
    auto RC = [&](int k, int i) { return f32x2{rc[k][i / 2][(i % 2) * 2], rc[k][i / 2][(i % 2) * 2 + 1]}; };
#pragma unroll
    for (int j = 0; j < 9; ++j) {
      f32x2 u[4], w[4];
#pragma unroll
      for (int i = 0; i < 4; ++i) u[i] = __builtin_elementwise_fma(RC(0, i), cs[j], f32x2{ac[i / 2][j][(i % 2) * 2], ac[i / 2][j][(i % 2) * 2 + 1]});
#pragma unroll
      for (int i = 0; i < 4; ++i) w[i] = u[i] * u[i];
#pragma unroll
      for (int i = 0; i < 4; ++i) w[i] = __builtin_elementwise_fma(w[i], RC(3, i), RC(2, i));
#pragma unroll
      for (int i = 0; i < 4; ++i) w[i] = u[i] * w[i];
      __builtin_amdgcn_sched_barrier(0);
#pragma unroll
      for (int i = 0; i < 4; ++i) w[i] = f32x2{__builtin_amdgcn_exp2f(w[i][0]), __builtin_amdgcn_exp2f(w[i][1])};
      __builtin_amdgcn_sched_barrier(0);
#pragma unroll
      for (int i = 0; i < 4; ++i) w[i] = __builtin_elementwise_fma(w[i], RC(1, i), RC(1, i));
      __builtin_amdgcn_sched_barrier(0);
#pragma unroll
      for (int i = 0; i < 4; ++i) w[i] = f32x2{__builtin_amdgcn_rcpf(w[i][0]), __builtin_amdgcn_rcpf(w[i][1])};
      __builtin_amdgcn_sched_barrier(0);
#pragma unroll
      for (int i = 0; i < 4; ++i) z[j][i] = u[i] * w[i];
#pragma unroll
      for (int i = 0; i < 4; ++i) {
        if (j == 1) sm[i] = z[0][i] + z[1][i];
        if (j > 1) sm[i] += z[j][i];
      }
#pragma unroll
      for (int i = 0; i < 4; ++i) sq[i] = j ? __builtin_elementwise_fma(z[j][i], z[j][i], sq[i]) : z[j][i] * z[j][i];
      asm volatile("" : "+v"(z[j][0]), "+v"(z[j][1]), "+v"(z[j][2]), "+v"(z[j][3]));
      __builtin_amdgcn_sched_barrier(0);
    }
  }
  f32x4 e;
  {
    float v[4][4], d[2][4], f[4];
#pragma unroll
    for (int i = 0; i < 4; ++i) {
      v[i][0] = sm[i][0];
      v[i][1] = sq[i][0];
      v[i][2] = sm[i][1];
      v[i][3] = sq[i][1];
    }
    asm volatile("s_nop 1" : "+v"(v[3][1]), "+v"(v[3][3]), "+v"(v[2][1]), "+v"(v[2][3]));
#pragma unroll
    for (int i = 0; i < 2; ++i)
#pragma unroll
      for (int c = 0; c < 4; ++c)
        asm volatile("v_add_f32_dpp %0, %1, %1 row_ror:8 row_mask:0xf bank_mask:0x3\n\tv_add_f32_dpp %0, %2, %2 row_ror:8 row_mask:0xf bank_mask:0xc"
                     : "=&v"(d[i][c])
                     : "v"(v[i][c]), "v"(v[i + 2][c]));
#pragma unroll
    for (int c = 0; c < 4; ++c)
      asm volatile("v_add_f32_dpp %0, %1, %1 row_shl:4 row_mask:0xf bank_mask:0x5\n\tv_add_f32_dpp %0, %2, %2 row_shr:4 row_mask:0xf bank_mask:0xa"
                   : "=&v"(f[c])
                   : "v"(d[0][c]), "v"(d[1][c]));
    asm volatile(
        "v_add_f32_dpp %0, %0, %0 quad_perm:[1,0,3,2] row_mask:0xf bank_mask:0xf\n\t"
        "v_add_f32_dpp %1, %1, %1 quad_perm:[1,0,3,2] row_mask:0xf bank_mask:0xf\n\t"
        "v_add_f32_dpp %2, %2, %2 quad_perm:[1,0,3,2] row_mask:0xf bank_mask:0xf\n\t"
        "v_add_f32_dpp %3, %3, %3 quad_perm:[1,0,3,2] row_mask:0xf bank_mask:0xf\n\t"
        "v_add_f32_dpp %0, %0, %0 quad_perm:[2,3,0,1] row_mask:0xf bank_mask:0xf\n\t"
        "v_add_f32_dpp %1, %1, %1 quad_perm:[2,3,0,1] row_mask:0xf bank_mask:0xf\n\t"
        "v_add_f32_dpp %2, %2, %2 quad_perm:[2,3,0,1] row_mask:0xf bank_mask:0xf\n\t"
        "v_add_f32_dpp %3, %3, %3 quad_perm:[2,3,0,1] row_mask:0xf bank_mask:0xf"
        : "+v"(f[0]), "+v"(f[1]), "+v"(f[2]), "+v"(f[3]));
    e = f32x4{f[0], __uint_as_float(__float_as_uint(f[1]) & 0x7fffffffu), f[2], __uint_as_float(__float_as_uint(f[3]) & 0x7fffffffu)};
  }
  asm volatile("s_mov_b64 exec, %3\n\tglobal_store_dwordx4 %0, %1, %2 sc1\n\ts_mov_b64 exec, -1\n\ts_nop 0" ::"v"(po), "v"(e), "s"(zp),
               "s"(0x1111111111111111ull)
               : "memory");
  asm volatile("s_mov_b64 exec, 1\n\tds_add_u32 %0, %1\n\ts_mov_b64 exec, -1" ::"v"(adw), "v"(mev) : "memory");
  __builtin_amdgcn_s_setprio(2);
  MK(12);
  const char* dst = uni_ptr(ws + OFF_Z2 + (size_t)layer * SZ_Z + (size_t)ni * S_ * 288 + (size_t)(mi * 4 + wm) * 9216 + 4096);
  const unsigned lane16 = (unsigned)lane * 16u;
  u32x4 wb[9];
  {
    static_assert(b_k(7) == 2 && b_k(8) == 3, "order");
    const char* wp = uni_ptr(ws + OFF_PI + (size_t)ni * (18 * K3C::WB) + 11 * K3C::WB + wm * 9216 + 4096);
#pragma unroll
    for (int j = 0; j < 8; ++j) asm volatile("global_load_dwordx4 %0, %1, %2 offset:%3 sc1" : "=v"(wb[j]) : "v"(lane16), "s"(wp), "n"(j * 1024 - 4096) : "memory");
    asm volatile("global_load_dwordx4 %0, %1, %2 sc1" : "=v"(wb[8]) : "v"(lane16 + 4096u), "s"(wp) : "memory");
  }
  u32x4 y[9];
  {
    const unsigned src = (unsigned)(g * 4096 + r * 16);
    unsigned zw = lds0 + (unsigned)(K3_ZC_OFF + (wm * 32 + 2 * r + (g & 1)) * 4), zr = lds0 + (unsigned)(K3_ZC_OFF + (wm * 32 + g * 8) * 4);
    asm volatile("" : "+v"(zw), "+v"(zr));
    f32x4 a, b, c, d;
    __builtin_amdgcn_s_sleep(6);
#pragma unroll 1
    for (unsigned spin = 0; spin < SPIN_MEM; ++spin) {
      asm volatile(
          "global_load_dwordx4 %0, %4, %5 sc1\n\t"
          "global_load_dwordx4 %1, %4, %5 offset:1024 sc1\n\t"
          "global_load_dwordx4 %2, %4, %5 offset:2048 sc1\n\t"
          "global_load_dwordx4 %3, %4, %5 offset:3072 sc1\n\t"
          "s_waitcnt vmcnt(0)"
          : "=&v"(a), "=&v"(b), "=&v"(c), "=&v"(d)
          : "v"(src), "s"(zp)
          : "memory");
      const unsigned bad = (__float_as_uint(a[1]) | __float_as_uint(a[3]) | __float_as_uint(b[1]) | __float_as_uint(b[3]) |
                            __float_as_uint(c[1]) | __float_as_uint(c[3]) | __float_as_uint(d[1]) | __float_as_uint(d[3])) >> 31;
      if (!__any(bad)) break;
    }
    MK(13);
    float s, q, zmb, zi;
    red16(a, b, c, d, s, q);
    ln_finish(s, q, 1.0f / (float)FH_, zmb, zi);
    f32x4 R[2], M[2];
    asm volatile(
        "ds_write2st64_b32 %4, %5, %6 offset1:2\n\t"
        "ds_read_b128 %0, %7\n\tds_read_b128 %2, %7 offset:512\n\tds_read_b128 %1, %7 offset:16\n\tds_read_b128 %3, %7 offset:528\n\t"
        "s_waitcnt lgkmcnt(0)"
        : "=&v"(R[0]), "=&v"(R[1]), "=&v"(M[0]), "=&v"(M[1])
        : "v"(zw), "v"(zi), "v"(-zmb * zi), "v"(zr)
        : "memory");
    MK(28);
    auto st = [&](int j) {
      if (j < 8) asm volatile("global_store_dwordx4 %0, %1, %2 offset:%3 sc1\n\ts_nop 1" ::"v"(lane16), "v"(y[j]), "s"(dst), "n"(j * 1024 - 4096) : "memory");
      else asm volatile("global_store_dwordx4 %0, %1, %2 sc1\n\ts_nop 1" ::"v"(lane16 + 4096u), "v"(y[j]), "s"(dst) : "memory");
    };
#pragma unroll
    for (int j = 0; j < 9; ++j) {
      {
        float t[8];
#pragma unroll
        for (int k = 0; k < 8; ++k) {
          t[k] = __builtin_fmaf(z[j][k / 2][k % 2], R[k / 4][k % 4], M[k / 4][k % 4]);
          asm volatile("" : "+v"(t[k]));
        }
        y[j] = u32x4{pack2(t[0], t[1]), pack2(t[2], t[3]), pack2(t[4], t[5]), pack2(t[6], t[7])};
      }
      st(j);
      __builtin_amdgcn_sched_barrier(0);
    }
    MK(29);
  }
  lds_inc(lw + LW_AD * 4, me);
  MK(24);
  __builtin_amdgcn_s_setprio(2);
  lds_spin4<2>(lw + LW_S * 4, 4u + (unsigned)(wm >> 1));
  {
    const unsigned fa = lds0 + (unsigned)((wm >> 1) * SB + K3C::XB + (wm & 1) * 9216) + lane16;
#pragma unroll
    for (int j = 0; j < 9; ++j) {
      asm volatile("ds_write_b128 %0, %1 offset:%2" ::"v"(fa), "v"(wb[j]), "n"(j * 1024) : "memory");
    }
    lds_inc(lw + LW_AD * 4, me);
  }
  announce(lw + LW_Z * 4, 4u, ws, CN_Z + layer, lane * 16 + ni, lane < 16);
  lds_inc(lw + LW_AD * 4, me);
  MK(27);
  f32x4 acc[2][9];
  {
    float bs[2];
    asm volatile("ds_read2_b32 %0, %1 offset1:16\n\ts_waitcnt lgkmcnt(0)"
                 : "=v"(*reinterpret_cast<f32x2*>(bs))
                 : "v"(lds0 + (unsigned)(K3_BS_OFF + (wm * 32 + r) * 4))
                 : "memory");
    lds_spin4<2>(lw + LW_S * 4, 6u + (unsigned)(wm >> 1));
    const unsigned fa = lds0 + (unsigned)((2 + (wm >> 1)) * SB + K3C::XB + (wm & 1) * 9216) + lane16;
#pragma unroll
    for (int j = 0; j < 9; ++j) {
      asm volatile("ds_write_b128 %0, %1 offset:%2" ::"v"(fa), "v"(y[j]), "n"(j * 1024) : "memory");
    }
    lds_inc(lw + LW_AD * 4, me);
#pragma unroll
    for (int mt = 0; mt < 2; ++mt) {
      f32x4 b4 = {bs[mt], bs[mt], bs[mt], bs[mt]};
      asm volatile("" : "+v"(b4));
#pragma unroll
      for (int j = 0; j < 9; ++j) acc[mt][j] = b4;
    }
  }
  u32x4 z1[9];
  const char* hp = uni_ptr(ws + OFF_H + (size_t)layer * SZ_Z + (size_t)((mi * 2 + (wm >> 1)) * 288 + ni * 18) * 1024);
  unsigned ho = (unsigned)((lane >> 5) * 1024 + ((wm & 1) * 32 + (lane & 31)) * 16);
  unsigned zb = lds0 + (unsigned)(wm * 4096) + lane16, zf = lw + (unsigned)(LW_S * 4 + wm);
  asm volatile("" : "+v"(ho), "+v"(zb), "+v"(zf));
  asm volatile("" : "+s"(hp));
  {
    GemmMath<K3C, 2, 9, K3_D, 6, false, 2> gm(acc);
    gm.setup(lds0, wm * 2, 0, lane, (unsigned)(2 * SB));
    asm volatile("" : "+v"(gm.xrd[0]), "+v"(gm.xrd[1]), "+v"(gm.wrd), "+v"(gm.xa[0]), "+v"(gm.xa[1]), "+v"(gm.wa));
    MK(15);
    lds_spin4<2>(lw + LW_L * 4, 8u);
    lds_spin4(lw + LW_AD * 4, 6u);
    MK(17);
    gm.first();
    __builtin_amdgcn_s_setprio(0);
    gm.template mid<1, false>();
    gm.template mid<11>();
    const unsigned za = gm.wrd + (unsigned)(K3_Z1_OFF - K3C::XB + wm * 5120);
#pragma unroll
    for (int j = 0; j < NR1; ++j) asm volatile("ds_read_b128 %0, %1 offset:%2" : "=v"(z1[j]) : "v"(za), "n"(j * 1024));
    gm.template mid<19>();
    gm.last();
  }
  MK(16);
  if (NR1 < 5) lds_inc(lw + LW_AD * 4, me);
  unsigned zv;
  u32x4 zl[4];
#pragma unroll
  for (int j = 0; j < 9; ++j) {
    const bool sec = j >= NR1 + 3;
    if (j == NR1 || j == NR1 + 4) {
      unsigned n = 1u << 18;
      asm volatile(
          "s_waitcnt lgkmcnt(0)\n\tv_cmp_gt_u32 vcc, %8, %0\n\ts_cbranch_vccnz .Lw%=\n\t.subsection 1\n.Lw%=:\n\tds_read_u8 %0, %6\n\tds_read_b128 %2, %7 offset:%9\n\t"
          "ds_read_b128 %3, %7 offset:%9+1024\n\tds_read_b128 %4, %7 offset:%9+2048\n\tds_read_b128 %5, %7 offset:%9+3072\n\ts_waitcnt lgkmcnt(0)\n\t"
          "v_cmp_gt_u32 vcc, %8, %0\n\t" SAFE_OUT(".Lr%=") "s_cbranch_vccnz .Lw%=\n\ts_branch .Lr%=\n\t.subsection 0\n.Lr%=:"
          : "+v"(zv), NOP_(n), "+v"(zl[0]), "+v"(zl[1]), "+v"(zl[2]), "+v"(zl[3])
          : "v"(zf), "v"(zb), "n"(sec ? 11 : 10), "n"(sec ? SB : 0)
          : "memory", "vcc", "scc");
#pragma unroll
      for (int k = 0; k < 4; ++k)
        if (j + k < 9) z1[j + k] = zl[k];
    }
    if (j == NR1 - 1 || (j == NR1 + 3 && j < 8))
      asm volatile("ds_read_u8 %0, %5\n\tds_read_b128 %1, %6 offset:%7\n\tds_read_b128 %2, %6 offset:%7+1024\n\tds_read_b128 %3, %6 offset:%7+2048\n\t"
                   "ds_read_b128 %4, %6 offset:%7+3072"
                   : "=&v"(zv), "=&v"(zl[0]), "=&v"(zl[1]), "=&v"(zl[2]), "=&v"(zl[3])
                   : "v"(zf), "v"(zb), "n"(sec ? SB : 0)
                   : "memory");
    asm volatile("" : "+v"(z1[j]));
    u32x2 h[2];
#pragma unroll
    for (int mt = 0; mt < 2; ++mt) {
      const f32x4 a = acc[mt][j];
      h[mt] = u32x2{pack2v(f32x2{bf_lo(z1[j][2 * mt]), bf_hi(z1[j][2 * mt])} * f32x2{a[0], a[1]}),
                    pack2v(f32x2{bf_lo(z1[j][2 * mt + 1]), bf_hi(z1[j][2 * mt + 1])} * f32x2{a[2], a[3]})};
    }
    const auto s0 = __builtin_amdgcn_permlane16_swap(h[0][0], h[1][0], false, false);
    const auto s1 = __builtin_amdgcn_permlane16_swap(h[0][1], h[1][1], false, false);
    const u32x4 v = {s0[0], s1[0], s0[1], s1[1]};
    asm volatile("global_store_dwordx4 %0, %1, %2 offset:%3 sc1\n\ts_nop 1" ::"v"(ho), "v"(v), "s"(hp + (j / 2) * 4096), "n"((j % 2) * 2048) : "memory");
    __builtin_amdgcn_sched_barrier(0);
  }
  MK(18);
}

DEVI void k3_const(char* smem, char* ws, const u16* b_sp, int layer, int bid, int mi, int ni, int wm) {
  const int lane = lane_id(), l = lane < 54 ? lane : 53, p = l / 18;
  const char* kp = uni_ptr(ws + OFF_CSK + bid * 2048 + wm * 512);
  const unsigned q16 = (unsigned)(l - p * 18) * 16u;
  unsigned bs;
  f32x4 t;
  lds_spin((unsigned)(size_t)smem + (unsigned)(LW_OFF + (layer ? LW_T + 1 : LW_HELLO) * 4), layer ? 12u : 2u);
  asm volatile("global_load_ushort %0, %1, %2" : "=v"(bs) : "v"((unsigned)(lane & 31) * 2u), "s"(uni_ptr((const char*)(b_sp + mi * 128 + wm * 32))) : "memory");
  if (layer) {
    asm volatile("global_load_dwordx4 %0, %1, %2 sc1\n\ts_waitcnt vmcnt(0)" : "=&v"(t) : "v"(q16), "s"(kp) : "memory");
  } else {
    const char* cp = uni_ptr(ws + OFF_CSP + ni * 1152 + wm * 288);
    f32x4 a[6];
#pragma unroll
    for (int i = 0; i < 6; ++i)
      asm volatile("global_load_dwordx4 %0, %1, %2 sc1" : "=v"(a[i]) : "v"(q16 + (unsigned)(p * (6 * F_ * 4))), "s"(cp + i * (F_ * 4)) : "memory");
    asm volatile("s_waitcnt vmcnt(0)" : "+v"(a[0]), "+v"(a[1]), "+v"(a[2]), "+v"(a[3]), "+v"(a[4]), "+v"(a[5]));
    t = ((a[0] + a[1]) + (a[2] + a[3])) + (a[4] + a[5]);
#pragma unroll
    for (int j = 0; j < 4; ++j)
      t[j] += __int_as_float(__builtin_amdgcn_ds_bpermute((lane + 18) * 4, __float_as_int(t[j]))) +
              __int_as_float(__builtin_amdgcn_ds_bpermute((lane + 36) * 4, __float_as_int(t[j])));
  }
  asm volatile("" : "+v"(bs));
  const unsigned lds0 = (unsigned)(size_t)smem;
  f32x4 t0 = wm < 2 ? f32x4{t[0], t[0], t[1], t[1]} : t, t1 = wm < 2 ? f32x4{t[2], t[2], t[3], t[3]} : t;
  unsigned a0 = lds0 + (unsigned)(wm < 2 ? K3_C2_OFF + wm * 576 + lane * 32 : K3_C1_OFF + (wm - 2) * 288 + lane * 16), a1 = a0 + (wm < 2 ? 16u : 0u);
  unsigned ab = lds0 + (unsigned)(K3_BS_OFF + (wm * 32 + (lane & 31)) * 4);
  bs <<= 16;
  asm volatile("" : "+v"(t0), "+v"(t1), "+v"(a0), "+v"(a1), "+v"(ab), "+v"(bs));
  bar();
  asm volatile("ds_write_b32 %4, %5\n\ts_mov_b64 exec, 0x3ffff\n\tds_write_b128 %0, %2\n\tds_write_b128 %1, %3\n\ts_mov_b64 exec, -1" ::"v"(a0), "v"(a1),
               "v"(t0), "v"(t1), "v"(ab), "v"(bs)
               : "memory");
  if (layer == 0 && lane < 24) asm volatile("global_store_dwordx4 %0, %1, %2\n\ts_nop 1" ::"v"((unsigned)lane * 16u), "v"(t), "s"(kp) : "memory");
}

DEVI void p3_g1(char* smem, char* __restrict__ ws, int layer, int mi, int wm, int lane MK_ARG) {
  const unsigned lds0 = (unsigned)(size_t)smem;
  const int g = lane >> 4, r = lane & 15;
  const unsigned lw = lds0 + (unsigned)(LW_OFF + layer * 4);
  const unsigned xrd = lds0 + (unsigned)(g * 2048 + (wm * 32 + r) * 16);
  s16x8 xc[4][2][2];
  {
    const char* xs = uni_ptr(ws + OFF_XS + (size_t)layer * 2 * SZ_XS + (size_t)(mi * 2 + (wm >> 1)) * 4096 + (wm & 1) * 256);
    f32x4 a, b;
    auto pick = [&](int t) {
      bar();
      asm volatile(
          "ds_read_b128 %0, %4\n\tds_read_b128 %1, %4 offset:256\n\tds_read_b128 %2, %4 offset:8192\n\tds_read_b128 %3, %4 offset:8448\n\t"
          "s_waitcnt lgkmcnt(0)"
          : "=&v"(xc[t][0][0]), "=&v"(xc[t][0][1]), "=&v"(xc[t][1][0]), "=&v"(xc[t][1][1])
          : "v"(xrd + (unsigned)(t * K3C::SB))
          : "memory");
    };
    __builtin_amdgcn_s_setprio(2);
    pick(0);
    pick(1);
    asm volatile("global_load_dwordx4 %0, %2, %3 sc1\n\tglobal_load_dwordx4 %1, %2, %3 offset:512 sc1"
                 : "=&v"(a), "=&v"(b)
                 : "v"((unsigned)(g * 1024 + r * 16)), "s"(xs)
                 : "memory");
    pick(2);
    pick(3);
    bar();
    asm volatile("s_waitcnt vmcnt(0)" : "+v"(a), "+v"(b) : : "memory");
    float s, q, mb, rinv;
    red8(a, b, s, q);
    bar();
    asm volatile("" : "+v"(s), "+v"(q));
    ln_finish(s, q, 1.0f / (float)D_, mb, rinv);
    asm volatile("" : "+v"(mb), "+v"(rinv));
    bar();
    asm volatile("" : "+v"(mb), "+v"(rinv));
    const float k0 = -2.0f * 0.7978845608028654f * 1.4426950408889634f, k1 = k0 * 0.044715f;
    float sd = __builtin_amdgcn_rcpf(rinv), c0 = k0 * rinv, c1 = k1 * rinv * rinv * rinv;
    asm volatile("" : "+v"(sd));
    asm volatile("" : "+v"(c0));
    asm volatile("" : "+v"(c1));
    const float e0 = lane < 32 ? -mb : c0, e1 = lane < 32 ? sd : c1;
    asm volatile("ds_write2st64_b32 %0, %1, %2 offset1:2" ::"v"(
                     lds0 + (unsigned)(K3_ST_OFF + (g >> 1) * 1024 + (wm * 32 + 2 * r + (g & 1)) * 4)),
                 "v"(e0), "v"(e1)
                 : "memory");
    bar();
    asm volatile("s_waitcnt lgkmcnt(0)" ::: "memory");
    bar();
    __builtin_amdgcn_s_setprio(0);
  }
  f32x4 ac[2][9];
#pragma unroll
  for (int mt = 0; mt < 2; ++mt)
#pragma unroll
    for (int j = 0; j < 9; ++j) ac[mt][j] = f32x4{0.f, 0.f, 0.f, 0.f};
  {
    UMath<true> gm(ac, xc);
    gm.xa = xrd;
    gm.wa = lds0 + (unsigned)(K3C::XB + (g * 8 + (r & 7)) * K3C::P + (r >> 3) * 16);
    gm.fa = lw + (unsigned)(LW_AD * 4 + (lane & 3));
    gm.fw = lw + (unsigned)(LW_S * 4 + wm);
    gm.fv = 0u;
    asm volatile("" : "+v"(gm.fa), "+v"(gm.fw), "+v"(gm.fv), "+v"(gm.xa), "+v"(gm.wa));
    __builtin_amdgcn_s_sleep(40);
    lds_spin4<4>(lw + LW_L * 4, 4u);
    gm.first();
    place();
    lds_spin4(lw + LW_AD * 4, 2u);
    __builtin_amdgcn_s_setprio(1);
    gm.run();
    __builtin_amdgcn_s_setprio(0);
  }
  MK(14);
  f32x2 rc[4];
  f32x4 cs[9];
  const int l2 = lane_id();
  {
    const unsigned ra = lds0 + (unsigned)(K3_ST_OFF + (wm * 32 + (l2 & 15)) * 4), ca = lds0 + (unsigned)(K3_C1_OFF + (l2 & 48));
#pragma unroll
    for (int k = 0; k < 4; ++k) asm volatile("ds_read2_b32 %0, %1 offset1:16" : "=v"(rc[k]) : "v"(ra + (unsigned)(k * 512)) : "memory");
#pragma unroll
    for (int j = 0; j < 9; ++j) asm volatile("ds_read_b128 %0, %1 offset:%2" : "=v"(cs[j]) : "v"(ca), "n"(j * 64) : "memory");
    asm volatile("s_waitcnt lgkmcnt(0)" : "+v"(rc[0]), "+v"(rc[1]), "+v"(rc[2]), "+v"(rc[3]));
#pragma unroll
    for (int j = 0; j < 9; ++j) asm volatile("" : "+v"(cs[j]));
  }
  const unsigned za = lds0 + (unsigned)(K3_Z1_OFF + wm * 5120 + l2 * 16);
  u32x4 z1[9];
#pragma unroll
  for (int t = 0; t < 18; ++t) {
    const int j = t / 2, mt = t % 2;
    float u[4], w[4];
#pragma unroll
    for (int i = 0; i < 4; ++i) asm volatile("v_fma_f32 %0, %1, %2, %3" : "=v"(u[i]) : "v"(rc[0][mt]), "v"(cs[j][i]), "v"(ac[mt][j][i]));
#pragma unroll
    for (int i = 0; i < 4; ++i) asm volatile("v_mul_f32 %0, %1, %1" : "=v"(w[i]) : "v"(u[i]));
#pragma unroll
    for (int i = 0; i < 4; ++i) asm volatile("v_fma_f32 %0, %0, %1, %2" : "+v"(w[i]) : "v"(rc[3][mt]), "v"(rc[2][mt]));
#pragma unroll
    for (int i = 0; i < 4; ++i) asm volatile("v_mul_f32 %0, %0, %1" : "+v"(w[i]) : "v"(u[i]));
#pragma unroll
    for (int i = 0; i < 4; ++i) asm volatile("v_exp_f32 %0, %0" : "+v"(w[i]));
#pragma unroll
    for (int i = 0; i < 4; ++i) asm volatile("v_fma_f32 %0, %0, %1, %1" : "+v"(w[i]) : "v"(rc[1][mt]));
#pragma unroll
    for (int i = 0; i < 4; ++i) asm volatile("v_rcp_f32 %0, %0" : "+v"(w[i]));
#pragma unroll
    for (int i = 0; i < 4; ++i) asm volatile("v_mul_f32 %0, %0, %1" : "+v"(w[i]) : "v"(u[i]));
    z1[j][2 * mt] = pack2(w[0], w[1]);
    z1[j][2 * mt + 1] = pack2(w[2], w[3]);
    if (mt && j < NR1) asm volatile("ds_write_b128 %0, %1 offset:%2" ::"v"(za), "v"(z1[j]), "n"(j * 1024) : "memory");
    if (t == 9) asm volatile("s_waitcnt lgkmcnt(0)" ::: "memory");
    __builtin_amdgcn_sched_barrier(0);
    bar();
    __builtin_amdgcn_sched_barrier(0);
  }
  MK(15);
#pragma unroll 1
  for (int t = 20; t < 32; ++t) bar();
  __builtin_amdgcn_s_sleep(11);
#pragma unroll
  for (int k = NR1; k < 9; ++k) {
    if (k == NR1 + 4) lds_spin4(lw + LW_AD * 4, 7u);
    asm volatile("ds_write_b128 %0, %1 offset:%2" ::"v"(lds0 + (unsigned)(wm * 4096 + l2 * 16)), "v"(z1[k]),
                 "n"(((k - NR1) / 4) * K3C::SB + ((k - NR1) % 4) * 1024)
                 : "memory");
    if (k == NR1 + 3 || k == 8) asm volatile("ds_write_b8 %0, %1" ::"v"(lw + (unsigned)(LW_S * 4 + wm)), "v"(vconst(k == NR1 + 3 ? 10u : 11u)) : "memory");
  }
}

constexpr int W5A = 22;
constexpr int NPF = 30;
struct Loader {
  u32x4 pf[NPF];
  unsigned lds0, lane16;
  int wl, lane;

  template <int R0, int N, int P0, int PE>
  DEVI void pf_load(const char* src) {
    const unsigned o = lane16 + (unsigned)(wl * 1024);
#pragma unroll
    for (int t = 0; t < N; ++t) {
      unsigned a = o + (unsigned)((P0 + 4 * t) * 1024);
      if (P0 + 4 * t + 3 >= PE && P0 + 4 * t + wl >= PE) a = lane16;
      asm volatile("global_load_dwordx4 %0, %1, %2 sc1" : "=v"(pf[R0 + t]) : "v"(a), "s"(src) : "memory");
    }
  }
  template <int R0, int N>
  DEVI void pf_pin() {
#pragma unroll
    for (int t = 0; t < N; ++t) asm volatile("" : "+v"(pf[R0 + t]));
  }
  template <int R0, int N>
  DEVI void pf_undef() {
#pragma unroll
    for (int t = 0; t < N; ++t) asm volatile("" : "=v"(pf[R0 + t]));
  }
  template <int PB, int A, int N>
  DEVI void pf_put(unsigned dst) {
    constexpr int T0 = (A - PB) / 4, T1 = (A + N - 1 - PB) / 4;
    const unsigned a = lds0 + dst + lane16 + (unsigned)((wl + PB + 4 * T0 - A + 4) * 1024) - 4096u;
#pragma unroll
    for (int t = T0; t <= T1; ++t) {
      const int p0 = PB + 4 * t;
      if ((p0 >= A || p0 + wl >= A) && (p0 + 3 < A + N || p0 + wl < A + N))
        asm volatile("ds_write_b128 %0, %1 offset:%2" ::"v"(a), "v"(pf[t]), "n"((t - T0) * 4096) : "memory");
    }
  }
  template <int N>
  DEVI void dma_run(const char* src, unsigned dst) {
    constexpr int B = N / 4, R = N % 4;
    const unsigned f = (unsigned)(wl * B + (wl < R ? wl : R)) * 1024u, o = lane16 + f, m = lds0 + dst + f;
    dma_seq<B>(src, o, m);
    if (wl < R) dma16_sc1(src + B * 1024, o, m + (unsigned)(B * 1024));
  }
  template <int V>
  DEVI void wait_w() {
    asm volatile("s_cmp_lt_u32 %0, 2\n\ts_cbranch_scc1 1f\n\ts_waitcnt vmcnt(%1)\n1:\n\ts_waitcnt vmcnt(%2)" ::"s"(wl), "n"(V - 1), "n"(V) : "memory", "scc");
  }
  template <int V>
  DEVI void wbar() {
    asm volatile("s_waitcnt vmcnt(%0) lgkmcnt(0)" ::"n"(V) : "memory");
    bar();
  }

  DEVI void p1(char* smem, char* ws, int bid, int rt, int hp MK_ARG) {
    constexpr int SB = K1C::SB;
    static_assert(K1C::XB == 18 * 1024 && K1C::WB == 14 * 1024, "K1 shape");
    const char* wp = uni_ptr(ws + OFF_PQ + (size_t)hp * (9 * K1C::WB) + 4 * K1C::WB);
    static_assert(3 * SB + K1C::XB >= P5_W + 5 * 18432, "free while the GEMM before ends");
    const char* wq = wp - 4 * K1C::WB;
    dma_run<14>(wq + 3 * K1C::WB, (unsigned)(3 * SB + K1C::XB));
    dma_run<28>(wp, (unsigned)K1_WX);
    if (wl == 3) dma16_sc1(uni_ptr(ws + OFF_CSQ + bid * 512), lane16 & 511u, lds0 + (unsigned)K1_CS_OFF);
    __builtin_amdgcn_s_setprio(0);
    wait_vm<0>();
    const bool lo = wl < 2;
    const int x0 = lo ? wl * 5 : wl * 4 + 2, w0 = lo ? wl * 3 : wl * 4 - 2;
    unsigned xo[5];
#pragma unroll
    for (int i = 0; i < 5; ++i) {
      const int c = (x0 + i) * 64 + lane;
      const int ch = c / 144;
      int gr = rt * 128 - 16 + (c - ch * 144);
      if (gr < 0) gr = 0;
      xo[i] = (unsigned)(((gr >> 6) * 72 + ch + 3 - (i & 3)) * 1024 + (gr & 63) * 16);
      asm volatile("" : "+v"(xo[i]));
    }
    const char* xp = uni_ptr(ws + OFF_X1 + SZ_X - 3072);
    wp += 2 * K1C::WB + w0 * 1024;
    auto dma_x = [&](unsigned so) {
      const unsigned m = lds0 + so + (unsigned)(x0 * 1024);
      dma4<4, true>(xp, xo[0], xo[1], xo[2], xo[3], m);
      if (lo) dma16_sc1(xp, xo[4], m + 4096u);
      xp += 8192;
    };
    auto dma_w = [&](unsigned so) {
      const unsigned m = lds0 + so + (unsigned)(K1C::XB + w0 * 1024);
      dma_seq<3>(wp, lane16, m);
      if (!lo) dma16_sc1(wp + 3072, lane16, m + 3072u);
      wp += K1C::WB;
    };
    const char* xs = uni_ptr(ws + OFF_XS + SZ_XS);
    const int g = lane >> 4, p2 = 64 + (lane & 7);
    int ra = rt * 128 - 16 + wl * 32 + 2 * (lane & 15);
    if (ra < 0) ra = 0;
    unsigned sa = (unsigned)(((ra >> 6) * 8 + g * 2) * 512 + (ra & 63) * 8);
    unsigned sb = (unsigned)(((rt * 2 + 1) * 8 + (lane >> 3)) * 512 + (2 * p2 - 80) * 8);
    unsigned sw = lds0 + (unsigned)(K1_ST_OFF + (2 * (lane < 32 ? wl * 16 + (lane & 15) : p2) + (g & 1)) * 8);
    asm volatile("" : "+v"(sa), "+v"(sb), "+v"(sw));
    MK(22);
    __builtin_amdgcn_s_setprio(3);
    bar();
    dma_run<14>(wq, (unsigned)K1C::XB);
    dma_run<14>(wq + K1C::WB, (unsigned)(SB + K1C::XB));
    dma_run<14>(wq + 2 * K1C::WB, (unsigned)(2 * SB + K1C::XB));
    MK(1);
    bar();
    dma_x(0u);
    dma_x((unsigned)SB);
    wbar<4>();
    MK(29);
    dma_x((unsigned)(2 * SB));
    dma_x((unsigned)(3 * SB));
    wbar<8>();
    MX(1, 3);
    dma_x(0u);
    wbar<8>();
    MX(1, 4);
    dma_x((unsigned)SB);
    f32x4 a, b, e;
    asm volatile("global_load_dwordx4 %0, %3, %5 sc1\n\tglobal_load_dwordx4 %1, %3, %5 offset:512 sc1\n\tglobal_load_dwordx4 %2, %4, %5 sc1"
                 : "=&v"(a), "=&v"(b), "=&v"(e)
                 : "v"(sa), "v"(sb), "s"(xs)
                 : "memory");
    wbar<11>();
    MX(1, 5);
    dma_w((unsigned)SB);
    dma_x((unsigned)(2 * SB));
    wbar<15>();
    MX(1, 6);
    dma_w((unsigned)(2 * SB));
    dma_x((unsigned)(3 * SB));
    wbar<19>();
    MX(1, 7);
    dma_w((unsigned)(3 * SB));
    dma_x(0u);
    wbar<16>();
    MX(1, 8);
    asm volatile("" : "+v"(a), "+v"(b), "+v"(e));
    float s, q;
    {
      float t[4] = {e[0], e[1], e[2], e[3]}, s2, q2;
      asm volatile(
          "v_add_f32_dpp %0, %0, %0 row_ror:8 row_mask:0xf bank_mask:0xf\n\tv_add_f32_dpp %1, %1, %1 row_ror:8 row_mask:0xf bank_mask:0xf\n\t"
          "v_add_f32_dpp %2, %2, %2 row_ror:8 row_mask:0xf bank_mask:0xf\n\tv_add_f32_dpp %3, %3, %3 row_ror:8 row_mask:0xf bank_mask:0xf\n\ts_nop 1"
          : "+v"(t[0]), "+v"(t[1]), "+v"(t[2]), "+v"(t[3]));
      red4(t, s2, q2);
      red8(a, b, s, q);
      s = lane < 32 ? s : s2;
      q = lane < 32 ? q : q2;
      asm volatile("" : "+v"(s), "+v"(q));
    }
    wbar<8>();
    MX(1, 9);
    {
      asm volatile("" : "+v"(s), "+v"(q));
      float mb, rinv;
      ln_finish(s, q, 1.0f / (float)D_, mb, rinv);
      asm volatile("ds_write_b64 %0, %1" ::"v"(sw), "v"(f32x2{-mb, rinv}) : "memory");
    }
    wbar<0>();
    MK(30);
  }

  template <int I>
  DEVI void p5_step(const char* wp) {
    constexpr int T = 2 * I + 1;
    wbar<(T == 1 ? 63 : (T <= 11 ? 3 : 0))>();
    if constexpr (T <= 9) {
      const unsigned wo = (unsigned)(12288 + (wl & 1) * 3072), so = (unsigned)(P5_W + ((T + 2 + (wl >> 1)) & 7) * 18432);
      dma_seq<3>(wp + (T + 7) * 18432, lane16 + wo + (unsigned)((wl >> 1) * 18432), lds0 + so + wo);
      pf_put<0, 12 * (T - 1), 12>((unsigned)(P5_W + ((T + 2) % 8) * 18432));
      pf_put<0, 12 * T, 12>((unsigned)(P5_W + ((T + 3) % 8) * 18432));
    }
  }
  template <int... S>
  DEVI void p5_steps(const char* wp, std::integer_sequence<int, S...>) {
    (p5_step<S>(wp), ...);
  }

  template <int T0, int T1>
  DEVI void w5_load(const char* w5p) {
    const unsigned o = lane16 + (unsigned)(wl * 1024);
#pragma unroll
    for (int t = T0; t < T1; ++t)
      asm volatile("global_load_dwordx4 %0, %1, %2 sc1" : "=v"(pf[t]) : "v"(o + (unsigned)((t / 3 + 8) * 18432 + (t % 3) * 4096)), "s"(w5p) : "memory");
  }
  DEVI void p35(char* smem, char* ws, const u16* w_sp, int layer, int bid, int mi, int ni, int rb, int ct, int hp MK_ARG) {
    using C = K3C;
    constexpr int NS = C::NS, SB = C::SB, NDX = 4, NDW = 5, VPS = NDX + NDW;
    const char* wip = uni_ptr(ws + OFF_PI + (size_t)ni * (18 * C::WB));
    if (layer) {
      pf_put<-48, 36, 18>((unsigned)(2 * SB + C::XB));
      pf_put<-48, 54, 18>((unsigned)(3 * SB + C::XB));
    } else {
      dma_run<18>(wip + 2 * C::WB, (unsigned)(2 * SB + C::XB));
      dma_run<18>(wip + 3 * C::WB, (unsigned)(3 * SB + C::XB));
    }
    dma_run<18>(wip + C::WB, (unsigned)(SB + C::XB));
    const char* w5p = uni_ptr(ws + OFF_PW + (size_t)ct * (36 * 9216));
    const int w0 = wl < 2 ? wl * 5120 : wl * 4096 + 2048;
    const unsigned m0x = lds0 + (unsigned)(wl * 4096), m0w = lds0 + (unsigned)(C::XB + w0);
    const unsigned wo = lane16 + (unsigned)w0;
    const unsigned lw = lds0 + (unsigned)(LW_OFF + layer * 4), me = 1u << (8 * wl);
    const unsigned wo4 = wo + 4096u;
    auto dma_w = [&](const char* wp, unsigned so) {
      dma_seq<4>(wp, wo, m0w + so);
      if (wl < 2) dma16_sc1(wp, wo4, m0w + so + 4096u);
    };
    {
      const char* xp = uni_ptr(ws + OFF_X1 + (size_t)layer * 2 * SZ_X + (size_t)(mi * 2 * 72 + wl * 2) * 1024 - 1024);
      const unsigned xo0 = lane16 + 1024u, xo1 = lane16 + 72u * 1024u, xo3 = lane16 + 71u * 1024u;
      auto dma_x = [&](int k, unsigned so) { dma4<4, true>(xp + k * 8192, xo0, xo1, lane16, xo3, m0x + so); };
      MK(10);
      bar();
      dma_x(0, 0u);
      dma_x(1, (unsigned)SB);
      wait_vm<4>(); MX(3, 2); wbar<4>(); MX(2, 2);
      MK(25);
      {
        dma_x(2, (unsigned)(2 * SB));
        dma_x(3, (unsigned)(3 * SB));
        dma_x(4, (unsigned)(4 * SB));
        wbar<12>(); MX(2, 3);
        dma_x(5, 0u);
        wait_vm<12>();
      }
      bar(); MX(2, 4);
      dma_w(wip + 4 * C::WB, 0u);
      dma_x(6, (unsigned)SB);
      wait_w<8 + VPS>();
#pragma unroll 1
      for (int t = 1; t < 3; ++t) {
        bar(); MX(2, 4 + t);
        dma_w(wip + (t + 4) * C::WB, (unsigned)(t * SB));
        dma_x(t + 6, (unsigned)((t + 1) * SB));
        wait_w<NDX + VPS>();
      }
      bar(); MX(2, 7);
      dma_w(wip + 7 * C::WB, (unsigned)(3 * SB));
      wait_w<VPS>();
      bar(); MX(2, 8);
      dma_w(wip + 8 * C::WB, 0u);
      wait_w<NDW>();
      bar(); MX(2, 9);
      const char* wp = wip + 9 * C::WB;
      dma_w(wp + b_k(0) * C::WB, (unsigned)SB);
      if (layer == 0) {
        const char* sp = uni_ptr((const char*)w_sp + (size_t)(bid * 4 + wl) * 8192);
        const unsigned so = (unsigned)((lane >> 5) * 4096 + (lane & 31) * 16);
#pragma unroll
        for (int i = 0; i < WSP_V; ++i) dma16(sp + i * 512, so, lds0 + (unsigned)K3_DM_OFF);
        dma16(sp + (bid >> 4) * 256, so & ~0x100u, lds0 + (unsigned)K3_DM_OFF);
      }
      w5_load<0, 4>(w5p);
      wait_w<NDW + 4>();
      MX(3, 10); bar(); MX(2, 10);
      dma_w(wp + b_k(1) * C::WB, (unsigned)(2 * SB));
      w5_load<4, 8>(w5p);
      MK(26);
      lds_spin4(lw + LW_AD * 4, 1u);
      dma_w(wp + b_k(2) * C::WB, (unsigned)(3 * SB));
      dma_w(wp + b_k(3) * C::WB, 0u);
      w5_load<8, W5A>(w5p);
      wait_vm<W5A - 8>();
      lds_inc(lw + LW_L * 4, 4u * me);
      w5_load<W5A, 30>(w5p);
      MK(11);
      static_for<4, 7>([&](auto T_) {
        constexpr int t = T_;
        lds_spin4<2>(lw + LW_S * 4, (unsigned)(t - 3));
        MX(2, 12 + t);
        dma_w(wp + b_k(t) * C::WB, (unsigned)(((t + 1) % 4) * SB));
        if constexpr (t > 4) {
          wait_w<NDW>();
          lds_inc(lw + LW_L * 4, me);
        }
      });
      MX(2, 19);
      wait_vm<0>();
      lds_inc(lw + LW_L * 4, me);
      pf_pin<0, 30>();
      MK(13);
    }
    {
      unsigned xoff[NDX];
#pragma unroll
      for (int i = 0; i < NDX; ++i) {
        const int c = wl * 256 + i * 64 + lane;
        const int row = c >> 3, cc = c & 7;
        xoff[i] = (unsigned)((mi * 128 + row) * S_ * 2 - i * 1024) + (unsigned)((cc ^ ((row >> 1) & 7)) * 16);
      }
      auto dma_x = [&](const char* xp, unsigned so) { dma4<4, false>(xp, xoff[0], xoff[1], xoff[2], xoff[3], m0x + so); };
      const char* xp = uni_ptr((const char*)w_sp);
      const char* wp = uni_ptr(ws + OFF_Z2 + (size_t)layer * SZ_Z + (size_t)ni * S_ * 288);
      lds_spin4<2>(lw + LW_AD * 4, 5u);
      dma_x(xp + mi * 256, (unsigned)(2 * SB));
      dma_x(xp + mi * 256 + 128, (unsigned)(3 * SB));
      MK(24);
      int left = 2 * mi;
      auto next = [&]() {
        const int d = --left == 0 ? 3 : 1;
        xp += d * 128;
        wp += d * C::WB;
      };
      if (mi == 0) {
        xp += 256;
        wp += 2 * C::WB;
      }
      dma_x(xp, 0u);
      lds_spin4(lw + LW_S * 4, 6u);
      dma_x(xp + 128, (unsigned)SB);
      wait_vm<2 * NDX>();
      lds_inc(lw + LW_L * 4, me);
      MK(27);
      if (wl == 0) {
        cnt_wait(ws, CN_Z + layer, mi * 16 + ni, 16u);
        lds_wr32(lw + LW_GO * 4, 1u);
      } else {
        lds_spin(lw + LW_GO * 4, 1u);
      }
      MK(14);
      lds_spin4(lw + LW_S * 4, 8u);
      dma_w(wp, 0u);
      lds_spin4(lw + LW_S * 4, 9u);
      dma_w(wp + C::WB, (unsigned)SB);
      next();
      next();
      unsigned slot = (unsigned)(2 * SB);
      auto cissue = [&]() {
        dma_x(xp, slot);
        dma_w(wp, slot);
        next();
        slot = (slot == (unsigned)((NS - 1) * SB)) ? 0u : slot + (unsigned)SB;
      };
      auto cstep = [&]() {
        bar();
        cissue();
      };
      wait_w<NDW>();
      MK(15);
      int j = 2;
      if (layer == 0) {
        const char* sp = uni_ptr((const char*)w_sp + (size_t)(bid * 4 + wl) * 8192 + WSP_V * 512);
        unsigned d = 0u;
#pragma unroll 1
        for (; j < 2 + 32 - 4 * WSP_V; ++j) {
          cstep();
          asm volatile("s_load_dword %0, %1, 0\n\ts_load_dword %0, %1, 64\n\ts_load_dword %0, %1, 0x1000\n\ts_load_dword %0, %1, 0x1040"
                       : "+s"(d)
                       : "s"(sp));
          sp += 128;
          wait_w<VPS>();
        }
#pragma unroll 1
        for (int e = j + 4; j < e; ++j) {
          cstep();
          asm volatile("" : "+s"(d));
          wait_w<VPS>();
        }
        asm volatile("s_waitcnt lgkmcnt(0)" : "+s"(d));
      }
      place();
#pragma unroll 1
      for (; j <= 29; ++j) {
        cstep();
        wait_w<VPS>();
      }
      bar();
    }
    dma_run<33>(w5p, (unsigned)(P5_W + 54 * 1024));
    dma_run<23>(w5p + 67 * 1024, (unsigned)(P5_W + 121 * 1024));
    if (wl == 3) dma16_sc1(w5p + 143 * 1024, lane16, lds0 + (unsigned)(P5_W + 53 * 1024));
    wait_vm<14>();
    bar();
    dma_run<34>(w5p + 33 * 1024, (unsigned)(P5_W + 87 * 1024));
    wait_vm<0>();
    bar();
    if (wl) {
      const unsigned f = (unsigned)(wl - 1) * 18432u;
      dma_seq<17>(w5p + 90 * 1024, lane16 + f, lds0 + (unsigned)P5_W + f);
      if (wl < 3) dma16_sc1(w5p + 107 * 1024, lane16 + f, lds0 + (unsigned)(P5_W + 17 * 1024) + f);
    } else {
      cnt_wait(ws, CN_H + layer, rb * 8 + ct, 8u);
    }
    MK(20);
    bar();
    p5_steps(w5p, std::make_integer_sequence<int, 7>{});
    if (layer == 0) {
      if (bid < 24 && wl == 0 && lane < 8) pk_st16(ws + OFF_HELLO + bid * 128 + lane16, u32x4{0u, 0u, 0u, 0u});
    }
  }
};

__global__ __launch_bounds__(768, 3) void k_main(const u16* __restrict__ x0, const u16* __restrict__ wqkv, const u16* __restrict__ wo,
                                                 const u16* __restrict__ w_in, const u16* __restrict__ w_sp,
                                                 const u16* __restrict__ b_sp, const u16* __restrict__ w_out,
                                                 u16* __restrict__ out, char* ws) {
  extern __shared__ __attribute__((aligned(16))) char smem[];
  const char* code0 = code_here();
  (void)code0;
  const unsigned lds0 = (unsigned)(size_t)smem;
  int wave = __builtin_amdgcn_readfirstlane((int)threadIdx.x >> 6);
  int bid = blockIdx.x;
#define FRESH() asm volatile("" : "+s"(bid), "+s"(ws), "+s"(wave))
#define XCD (bid & 7)
#define IDX (bid >> 3)
#define MI ((XCD >> 2) * 8 + (IDX >> 2))
#define NI ((XCD & 1) * 8 + (XCD & 2) * 2 + (IDX & 3))
#define RB (XCD * 4 + (IDX >> 3))
#define CT (IDX & 7)
#define RT ((XCD >> 1) * 4 + (IDX >> 3))
#define HP ((XCD & 1) * 8 + (IDX & 7))
  MK(0);
  if (wave == 2) {
    constexpr int PER = (2 * NL_MAIN + 31) / 32;
    const int lane = lane_id();
    static_assert(PER <= 64, "one load");
    int li = IDX * PER + (lane < PER ? lane : PER - 1);
    if (li > 2 * NL_MAIN - 1) li = 2 * NL_MAIN - 1;
    touch64(code0, (unsigned)li * 64u, lds0 + (unsigned)(K1R_DUMMY + 768));
  }
  if (wave >= 3 && wave < 8) {
    constexpr int ST = 64;
    const int k = (7 - wave) * 64 + lane_id();
    const bool isx = k < 9216 / ST;
    int o = isx ? bid * 9216 + k * ST : bid * 7776 + (k - 9216 / ST) * ST;
    if (!isx && k - 9216 / ST > 7775 / ST) o = bid * 7776;
    const char* p = (isx ? (const char*)x0 : (const char*)wqkv) + o;
    asm volatile("s_mov_b32 m0, %1\n\ts_nop 0\n\tglobal_load_lds_dword %0, off" ::"v"(p), "s"(lds0 + (unsigned)K1R_DUMMY) : "memory");
  }

  if (__builtin_expect(wave >= 8, 1)) {
    __builtin_amdgcn_s_setprio(3);
    {
      const int lane = lane_id();
      RawLd rl;
      const bool cross = (((HP * 72) & ~15) & 127) > 48;
      const int c16 = IDX >> 1;
      const int cj = (int)((0x65743210u >> (4 * XCD)) & 7u) * 16 + c16;
      const int job = cross ? (cj < 88 ? 128 + cj : 999) : XCD * 16 + c16;
      const int pj = cross ? cj - 88 : ((XCD & 5) == 0 && (c16 & 3) == (c16 >> 2) ? 40 + XCD * 2 + (c16 >> 2) : -1);
      rl.front(x0, wqkv, wo, ws, bid, lds0, wave - 8, lane, RT, HP, pj MF_PASS);
      if (job < 216) k1_raw_loader<true>(rl, w_in, w_out, ws, rl.tag, bid, lds0, wave - 8, lane, RT, HP, CT, job, pj MF_PASS);
      else k1_raw_loader<false>(rl, w_in, w_out, ws, rl.tag, bid, lds0, wave - 8, lane, RT, HP, CT, job, pj MF_PASS);
    }
    Loader ld;
    ld.lds0 = lds0;
#pragma unroll 1
    for (int layer = 0; layer < 2; ++layer) {
      FRESH();
      ld.wl = wave - 8;
      ld.lane = lane_id();
      ld.lane16 = (unsigned)ld.lane * 16u;
      const char* wip = uni_ptr(ws + OFF_PI + (size_t)NI * (18 * K3C::WB));
      if (layer) {
        ld.p1(smem, ws, bid, RT, HP MK_PASS);
        ld.pf_load<0, 21, 0, 81>(uni_ptr(ws + OFF_PO + (size_t)CT * (18 * 4608)));
        ld.pf_load<21, 9, 36, 72>(wip);
        wait_vm<9>();
        ld.pf_pin<0, 21>();
        lds_spin(lds0 + (unsigned)(LW_OFF + (LW_T + layer) * 4), 8u);
        __builtin_amdgcn_s_sleep(4);
        ld.pf_put<0, 0, 40>((unsigned)P2_W);
        wait_vm<0>();
        ld.pf_pin<21, 9>();
        bar();
        ld.pf_put<0, 40, 41>((unsigned)(P2_W + 40 * 1024));
      } else {
        ld.pf_undef<21, 9>();
        MK(3);
        MK(4);
        bar();
        ld.dma_run<81>(uni_ptr(ws + OFF_PO + (size_t)CT * (18 * 4608)), (unsigned)P2_W);
      }
      if (layer == 0 && ld.wl == 0) cnt_wait(ws, CN_O + layer, RB * 8 + CT, 16u);
      MK(6);
      asm volatile("s_waitcnt vmcnt(0) lgkmcnt(0)" ::: "memory");
      bar();
      lds_spin(lds0 + (unsigned)(LW_OFF + (LW_T + layer) * 4), 12u);
      if (layer == 0) {
        if (ld.wl == 0) {
          cnt_wait(ws, CN_PK, 8 + NI, 36u);
          lds_wr32(lds0 + (unsigned)(LW_OFF + LW_HELLO * 4), 2u);
        } else {
          lds_spin(lds0 + (unsigned)(LW_OFF + LW_HELLO * 4), 2u);
        }
        MK(7);
      }
      static_assert(2 * K3C::SB <= P2_W, "free during the first residual GEMM");
      ld.dma_run<18>(wip, (unsigned)K3C::XB);
      MX(6, 24);
      if (layer == 0 && ld.wl == 0) cnt_wait(ws, CN_PK, 24 + CT, 72u);
      wait_vm<0>();
      ld.pf_pin<21, 9>();
      MX(6, 25);
      bar();
      FRESH();
      ld.wl = wave - 8;
      ld.p35(smem, ws, w_sp, layer, bid, MI, NI, RB, CT, HP MK_PASS);
    }
  } else {
    MF(12);
    math_start(smem, b_sp, wqkv, ws, bid, wave, lane_id());
    MF(13);
#pragma unroll 1
    for (int layer = 0; layer < 2; ++layer) {
      FRESH();
      {
        const int lane = lane_id(), r0 = RT * 128;
        float* csq = reinterpret_cast<float*>(ws + OFF_CSQ + bid * 512);
        Attn at;
        at.setup(lds0 + (unsigned)(layer ? K1_T_OFF : K1R_T_OFF), ws + OFF_O + (size_t)layer * SZ_X, r0, HP, wave, lane);
        if (layer) {
          if ((wave & 2) == 0) k1_gemm<2, 4>(smem, ws, RT, HP, (wave & 1) * 2 + (wave & 4), 0, wave >= 4, lane MK_PASS);
          else if (wave < 4) k1_gemm<3, 3>(smem, ws, RT, HP, (wave & 1) * 3, 4, false, lane MK_PASS);
          else if (wave == 6) k1_gemm<2, 3>(smem, ws, RT, HP, 6, 4, true, lane MK_PASS);
          else k1_gemm<1, 7>(smem, ws, RT, HP, 8, 0, true, lane MK_PASS);
        } else {
          if (wave == 0) k1_gemm_raw<3, false>(smem, HP, 0, lane MF_PASS);
          else if (wave < 4) k1_gemm_raw<2, false>(smem, HP, 1 + 2 * wave, lane MF_PASS);
          else if (wave < 7) k1_gemm_raw<2, true>(smem, HP, 2 * (wave - 4), lane MF_PASS);
          else k1_gemm_raw<3, true>(smem, HP, 6, lane MF_PASS);
        }
        msync(lds0 + (unsigned)(LW_OFF + (LW_T + layer) * 4));
        MK(3);
        at.run(r0, HP, wave, lane);
        if (layer == 0) {
          if (wave == 0 && lane < 32)
            asm volatile("global_store_dwordx4 %0, %1, %2 sc1\n\ts_nop 1" ::"v"((unsigned)lane * 16u),
                         "v"(*reinterpret_cast<const u32x4*>(smem + K1R_CS_OFF + lane * 16)), "s"(uni_ptr((const char*)csq))
                         : "memory");
          lds_spin(lds0 + (unsigned)(LW_OFF + LW_HELLO * 4), 1u);
        }
        MK(4);
        announce(lds0 + (unsigned)(LW_OFF + (LW_O + layer * 22 + (wave >> 2)) * 4), 4u, ws, CN_O + layer, (RT * 2 + (wave >> 2)) * 8 + lane,
                 lane < 8);
        MK(5);
        bar();
      }
      FRESH();
      {
        if (wave < 4) {
          const int lane = lane_id();
          u32x2 xv[5];
          ResIn ri;
          ri.setup(layer ? ws + OFF_X1 + SZ_X : (const char*)x0, layer == 0, RB, CT, wave, lane);
          ri.load(xv);
          ResOut ro;
          ro.setup(ws + OFF_X1 + (size_t)layer * 2 * SZ_X, ws + OFF_XS + (size_t)layer * 2 * SZ_XS, out, false, RB, CT, wave, lane);
          f32x4 acc[2][5];
          p2_gemm(acc, xv, lds0, uni_ptr(ws + OFF_O + (size_t)layer * SZ_X + (size_t)RB * (72 * 1024)), layer, wave, lane MK_PASS);
          MK(7);
          ro.run(acc[0], false);
          MK(8);
          announce(lds0 + (unsigned)(LW_OFF + (LW_X1 + layer) * 4), 4u, ws, CN_X1 + layer, (RB >> 1) * 16 + lane, lane < 16);
          MK(9);
          bar();
          FRESH();
          if (wave == 3) cnt_wait(ws, CN_X1 + layer, MI * 16 + NI, 16u);
          bar();
          MK(10);
          p3_g0(smem, ws, layer, MI, NI, wave, lane_id() MK_PASS);
        } else {
          if (layer && wave == 5) cnt_wait(ws, CN_O + layer, RB * 8 + CT, 16u);
          bar();
          k3_const(smem, ws, b_sp, layer, bid, MI, NI, wave & 3);
          MX(6, 26);
          bar();
          MK(10);
          FRESH();
          p3_g1(smem, ws, layer, MI, wave & 3, lane_id() MK_PASS);
        }
      }
      FRESH();
      {
        const int lane = lane_id();
        if (wave < 4) {
          u32x2 xv[5];
          ResIn ri;
          ri.setup(ws + OFF_X1 + (size_t)layer * 2 * SZ_X, false, RB, CT, wave, lane);
          f32x4 acc[2][5];
          ResOut ro;
          ro.setup(ws + OFF_X1 + SZ_X, ws + OFF_XS + SZ_XS, out, layer != 0, RB, CT, wave, lane);
          ResX<36, 33, 8, P5_W, P5_XW, P5_X0, 3, P5_BAR> gm(acc);
          gm.setup(lds0, uni_ptr(ws + OFF_H + (size_t)layer * SZ_Z + (size_t)RB * (288 * 1024)), wave, lane, (unsigned)P5_RED, layer + 1u);
          announce(lds0 + (unsigned)(LW_OFF + (LW_H + layer * 2 + (wave >> 1)) * 4), 2u, ws, ((NI >= 8) ? CN_H2 : CN_H) + layer,
                   (MI * 2 + (wave >> 1)) * 8 + lane, lane < 8);
          MK(19);
          bar();
          ri.load(xv);
          gm.prow();
          place();
          bar();
          MK(20);
          gm.prox<0>(std::make_integer_sequence<int, P5_X0>{});
          gm.init<2 * P5_X0>(xv);
          __builtin_amdgcn_sched_barrier(0);
          gm.run();
          MK(21);
          ro.run(acc[0], layer != 0);
          MK(22);
          if (layer == 0) {
            announce(lds0 + (unsigned)(LW_OFF + LW_X2 * 4), 4u, ws, CN_X2, (RB >> 1) * 16 + lane,
                     lane < 16 || (lane < 32 && (RB & 1) && RB < 31));
            MK(23);
          }
        } else {
          bar();
          bar();
#pragma unroll
          for (int i = 0; i < 7; ++i) {
            if (i == 1 && wave == 5) {
              unsigned f;
              const char* cp = uni_ptr(ws + OFF_CNT + (size_t)((CN_H2 + layer) * 256 + RB * 8 + CT) * 128);
              asm volatile("global_load_dword %0, %1, %2 sc1" : "=v"(f) : "v"(vconst(0u)), "s"(cp) : "memory");
              bar();
              asm volatile("s_waitcnt vmcnt(0)" : "+v"(f) : : "memory");
              if (__builtin_amdgcn_readfirstlane(f) != 7u) cnt_wait(ws, CN_H2 + layer, RB * 8 + CT, 8u);
              continue;
            }
            asm volatile("s_waitcnt lgkmcnt(0)" ::: "memory");
            bar();
          }
          MK(22);
        }
        if (layer == 0 && wave < 4) bar();
      }
    }
  }
}

}

extern "C" void kernel_launch(void* const* d_in, const int* in_sizes, int n_in, void* d_out, int out_size,
                              void* d_ws, size_t ws_size, hipStream_t stream) {
  const u16* x0 = (const u16*)d_in[0];
  const u16* wqkv = (const u16*)d_in[1];
  const u16* wo = (const u16*)d_in[2];
  const u16* w_in = (const u16*)d_in[4];
  const u16* w_sp = (const u16*)d_in[5];
  const u16* b_sp = (const u16*)d_in[6];
  const u16* w_out = (const u16*)d_in[7];
  char* ws = (char*)d_ws;
  (void)hipFuncSetAttribute((const void*)k_main, hipFuncAttributeMaxDynamicSharedMemorySize, LDS_ALL);
  k_main<<<256, 768, LDS_ALL, stream>>>(x0, wqkv, wo, w_in, w_sp, b_sp, w_out, (u16*)d_out, ws);
}
```

```cpp
#include <hip/hip_runtime.h>
#include <hip/hip_bf16.h>
#include <stdint.h>

#define DEVI __device__ __forceinline__

typedef unsigned short u16;
typedef __attribute__((ext_vector_type(8))) short s16x8;
typedef __attribute__((ext_vector_type(4))) short s16x4;
typedef __attribute__((ext_vector_type(4))) float f32x4;
typedef __attribute__((ext_vector_type(2))) float f32x2;
typedef __attribute__((ext_vector_type(2))) __bf16 bf16x2;
typedef __attribute__((ext_vector_type(2))) unsigned u32x2;
typedef __attribute__((ext_vector_type(4))) unsigned u32x4;

#define MK(I) ((void)0)
#define MX(S, I) ((void)0)
#define MF(I) ((void)0)
#define MF_ARG
#define MF_PASS
#define MK_ARG
#define MK_PASS

#define SPIN_MEM (1u << 22)
#define SAFE_OUT(L)
#define SAFE_AND
#define NOP_(n) "=s"(n)

namespace {

constexpr int S_ = 2048, D_ = 576, F_ = 4608, FH_ = 2304;
constexpr int NQKV = 3 * D_;

constexpr size_t SZ_X = (size_t)S_ * D_ * 2;
constexpr size_t SZ_Z = (size_t)S_ * FH_ * 2;
constexpr size_t OFF_X1 = 0;
constexpr size_t OFF_O = OFF_X1 + 3 * SZ_X;
constexpr size_t OFF_Z2 = OFF_O + 2 * SZ_X;
constexpr size_t OFF_H = OFF_Z2 + 2 * SZ_Z;
constexpr size_t SZ_XS = (size_t)S_ * 8 * 8;
constexpr size_t OFF_XS = OFF_H + 2 * SZ_Z;
constexpr size_t OFF_CSQ = OFF_XS + 3 * SZ_XS;
constexpr size_t OFF_ZP = OFF_CSQ + 256 * 512;
constexpr size_t SZ_ZP = (size_t)16 * 16 * 1024;
constexpr size_t OFF_PQ = OFF_ZP + 2 * SZ_ZP;
constexpr size_t OFF_PO = OFF_PQ + (size_t)16 * 9 * 64 * 112 * 2;
constexpr size_t OFF_PI = OFF_PO + (size_t)D_ * D_ * 2;
constexpr size_t OFF_PW = OFF_PI + (size_t)32 * 9 * 64 * 144 * 2;
constexpr size_t OFF_CSP = OFF_PW + (size_t)FH_ * D_ * 2;
constexpr size_t OFF_CNT = OFF_CSP + (size_t)18 * F_ * 4;
constexpr int CN_X1 = 0, CN_H = 2, CN_X2 = 4, CN_O = 5, CN_Z = 7, CN_PK = 9, CN_H2 = 10, NCNT = 12;
constexpr size_t OFF_TAG = OFF_CNT + (size_t)NCNT * 256 * 128;
constexpr size_t OFF_HELLO = OFF_TAG + 128;
constexpr size_t OFF_POT = OFF_HELLO + 2048;
constexpr size_t OFF_CSK = OFF_POT + 1024;
constexpr size_t WS_END = OFF_CSK + 256 * 2048;

DEVI float bf_lo(unsigned u) { return __uint_as_float(u << 16); }
DEVI float bf_hi(unsigned u) { return __uint_as_float(u & 0xffff0000u); }
DEVI unsigned pack2(float a, float b) {
  f32x2 v = {a, b};
  bf16x2 r = __builtin_convertvector(v, bf16x2);
  return __builtin_bit_cast(unsigned, r);
}
DEVI float rbf(float a) {
  return bf_lo(pack2(a, 0.f));
}

DEVI float sum_g(float x) {
  const unsigned u = __float_as_uint(x);
  const auto p = __builtin_amdgcn_permlane32_swap(u, u, false, false);
  const unsigned v = __float_as_uint(__uint_as_float(p[0]) + __uint_as_float(p[1]));
  const auto q = __builtin_amdgcn_permlane16_swap(v, v, false, false);
  return __uint_as_float(q[0]) + __uint_as_float(q[1]);
}

DEVI unsigned vconst(unsigned c) {
  unsigned r;
  asm volatile("v_mov_b32 %0, %1" : "=v"(r) : "s"(c));
  return r;
}
DEVI void place() {
  __builtin_amdgcn_sched_barrier(0);
  asm volatile(".p2align 6");
  __builtin_amdgcn_sched_barrier(0);
}
DEVI const char* uni_ptr(const char* p) {
  unsigned long long v = (unsigned long long)p;
  unsigned lo = __builtin_amdgcn_readfirstlane((unsigned)v), hi = __builtin_amdgcn_readfirstlane((unsigned)(v >> 32));
  asm("s_nop 4" : "+s"(lo), "+s"(hi));
  return (const char*)(((unsigned long long)hi << 32) | lo);
}

DEVI void dma16(const char* sbase, unsigned voff, unsigned m0val) {
  asm volatile("s_mov_b32 m0, %2\n\ts_nop 0\n\tglobal_load_lds_dwordx4 %0, %1"
               :
               : "v"(voff), "s"(sbase), "s"(m0val)
               : "memory");
}

DEVI void ln_finish(float s, float q, float invn, float& mb, float& rinv) {
  float mean = s * invn;
  asm("" : "+v"(mean));
  float var = fmaxf(__builtin_fmaf(-mean, mean, q * invn), 0.f);
  mb = rbf(mean);
  float v = rbf(var);
  float t = rbf(v + 1e-5f);
  float sd = rbf(__builtin_amdgcn_sqrtf(t));
  rinv = __builtin_amdgcn_rcpf(sd);
}

DEVI unsigned pack2v(f32x2 v) { return pack2(v[0], v[1]); }

template <int BM_, int BN_>
struct GemmCfg {
  static constexpr int BM = BM_, BN = BN_, NS = 4;
  static constexpr int P = BN * 2;
  static constexpr int XB = BM * 128, WB = 64 * P, SB = XB + WB;
  static constexpr int LDS_BYTES = NS * SB;
};

DEVI int swap23(int r) { return (r & ~12) | ((r & 4) << 1) | ((r & 8) >> 1); }

DEVI void lds_wr32(unsigned addr, unsigned v) { asm volatile("ds_write_b32 %0, %1" ::"v"(addr), "v"(v) : "memory"); }

template <class C, int MT, int NT, int D, int R, bool XCM, int WT, bool SQ = false, unsigned long long WS = 0>
struct GemmMath {
  static constexpr int WO = WT ? 1 : 2;
  static constexpr int NS = C::NS, SB = C::SB, P = C::P, NU = 2 * NT;
  static constexpr int XPU = MT <= 2 ? MT : MT - 2;
  static constexpr int XU = (MT + XPU - 1) / XPU;
  static_assert(NU % R == 0 && R > D && D <= NT && XU <= D, "shape");
  s16x4 wlo[R], whi[R];
  s16x8 wf[R];
  s16x8 xf[2][MT];
  unsigned xrd[2], wrd, xa[2], wa, so;
  f32x4 (&acc)[MT][NT];
  f32x4 accq[SQ ? MT : 1];
  unsigned long long wtab = WS;
  DEVI GemmMath(f32x4 (&a)[MT][NT]) : acc(a) {}

  template <int PU>
  DEVI void wread_lo() {
    constexpr int off = (PU / NT) * 32 * P + (PU % NT) * (WT == 2 ? 1024 : 32);
    if constexpr (WT) asm volatile("ds_read_b128 %0, %1 offset:%2" : "=v"(wf[PU % R]) : "v"(wa), "n"(off));
    else asm volatile("ds_read_b64_tr_b16 %0, %1 offset:%2" : "=v"(wlo[PU % R]) : "v"(wa), "n"(off));
  }
  template <int PU>
  DEVI void wread_hi() {
    if constexpr (!WT)
      asm volatile("ds_read_b64_tr_b16 %0, %1 offset:%2" : "=v"(whi[PU % R]) : "v"(wa), "n"((PU / NT) * 32 * P + (PU % NT) * 32 + 8 * P));
  }
  template <int H, int M>
  DEVI void xread1() {
    asm volatile("ds_read_b128 %0, %1 offset:%2" : "=v"(xf[H][M]) : "v"(xa[H]), "n"(M * (XCM ? 256 : 2048)));
  }
  static constexpr int xops(int v) {
    const int k = (v + D) % NT;
    if (k >= XU) return 0;
    const int n = MT - k * XPU;
    return n < XPU ? n : XPU;
  }
  static constexpr int ops(int v, bool last) { return (last && v + D >= NU) ? 0 : WO + xops(v); }
  static constexpr int newer(int U, bool last) {
    int n = 0;
    int v0 = U - D;
    if (U % NT == 0) v0 += XU - 1;
    else n += v0 >= 0 ? xops(v0) : xops(v0 + NU);
    for (int v = v0 + 1; v < U; ++v) n += v >= 0 ? ops(v, last) : ops(v + NU, false);
    return n;
  }
  template <int U, int M_>
  DEVI void mf() {
    constexpr int M = ((U % NT) & 1) ? MT - 1 - M_ : M_;
    s16x8 w;
    if constexpr (WT) w = wf[U % R];
    else w = __builtin_shufflevector(wlo[U % R], whi[U % R], 0, 1, 2, 3, 4, 5, 6, 7);
    acc[M][U % NT] = __builtin_amdgcn_mfma_f32_16x16x32_bf16(w, xf[U / NT][M], acc[M][U % NT], 0, 0, 0);
    __builtin_amdgcn_sched_barrier(0);
  }
  DEVI void pin() {
#pragma unroll
    for (int m = 0; m < MT; ++m)
#pragma unroll
      for (int n = 0; n < NT; ++n) asm volatile("" : "+v"(acc[m][n]));
  }
  template <int U, int I>
  DEVI void dsop() {
    constexpr int PU = (U + D) % NU;
    if constexpr (I == 0) wread_lo<PU>();
    else if constexpr (I == 1) wread_hi<PU>();
    else if constexpr (I - 2 < xops(U)) {
      constexpr int k = (U + D) % NT;
      xread1<((U + D - k) % NU) / NT, k * XPU + I - 2>();
    }
  }
  template <int U, bool LAST, bool BAR, int M>
  DEVI void slot() {
    constexpr bool PF = !(LAST && U + D >= NU);
    mf<U, M>();
    if constexpr (M == 0 && PF && U + D == NU) {
      if constexpr (BAR) __builtin_amdgcn_s_barrier();
      so = (so == (unsigned)((NS - 1) * SB)) ? 0u : so + (unsigned)SB;
      wa = wrd + (WS ? ((unsigned)wtab & 63u) << 11 : so);
      wtab >>= 6;
      xa[0] = xrd[0] + so;
      xa[1] = xrd[1] + so;
      __builtin_amdgcn_sched_barrier(0);
    }
    if constexpr (PF) {
      dsop<U, M>();
      if constexpr (M == MT - 1) {
        if constexpr (MT <= 1) dsop<U, 1>();
        if constexpr (MT <= 2) {
          dsop<U, 2>();
          dsop<U, 3>();
        }
      }
      __builtin_amdgcn_sched_barrier(0);
    }
  }
  template <int U, bool LAST, bool BAR, int... M>
  DEVI void unit(std::integer_sequence<int, M...>) {
    static_assert(newer(U, LAST) <= 15, "lgkmcnt range");
    asm volatile("s_waitcnt lgkmcnt(%0)" ::"n"(newer(U, LAST)));
    __builtin_amdgcn_sched_barrier(0);
    (slot<U, LAST, BAR, M>(), ...);
    if constexpr (SQ && (U % NT) < MT) {
      constexpr int m = U % NT;
      accq[m] = __builtin_amdgcn_mfma_f32_16x16x32_bf16(xf[U / NT][m], xf[U / NT][m], accq[m], 0, 0, 0);
      __builtin_amdgcn_sched_barrier(0);
    }
    __builtin_amdgcn_sched_barrier(0);
  }
  template <bool LAST, bool BAR, int... U>
  DEVI void stage(std::integer_sequence<int, U...>) {
    (unit<U, LAST, BAR>(std::make_integer_sequence<int, MT>{}), ...);
  }
  template <int... U>
  DEVI void prow(std::integer_sequence<int, U...>) {
    ((wread_lo<U>(), wread_hi<U>()), ...);
  }
  template <int... M>
  DEVI void prox(std::integer_sequence<int, M...>) {
    (xread1<0, M>(), ...);
  }
  DEVI void setup(unsigned lds0, int mt0, int nt0, int lane, unsigned so0) {
    const int g = lane >> 4, r = lane & 15, q = (lane >> 2) & 3, p = lane & 3;
#pragma unroll
    for (int h = 0; h < 2; ++h)
      xrd[h] = XCM ? lds0 + (unsigned)((4 * h + g) * (C::BM * 16) + (mt0 * 16 + r) * 16)
                   : lds0 + (unsigned)((mt0 * 16 + r) * 128 + (((4 * h + g) ^ ((r >> 1) & 7)) * 16));
    wrd = WT == 2   ? lds0 + (unsigned)(C::XB + nt0 * 1024 + lane * 16)
          : WT == 1 ? lds0 + (unsigned)(C::XB + (g * 8 + (r & 7)) * P + (r >> 3) * 16 + nt0 * 32)
                    : lds0 + (unsigned)(C::XB + (16 * (g >> 1) + 4 * (g & 1) + q) * P + 8 * p + nt0 * 32);
    so = so0;
    xa[0] = xrd[0] + so;
    xa[1] = xrd[1] + so;
    wa = wrd + so;
  }
  DEVI void first() {
    prox(std::make_integer_sequence<int, MT>{});
    prow(std::make_integer_sequence<int, D>{});
    asm volatile("s_waitcnt lgkmcnt(0)");
  }
  template <int N, bool BAR = true>
  DEVI void mid() {
    if constexpr (N > 1) place();
#pragma unroll 1
    for (int s = 0; s < N; ++s) stage<false, BAR>(std::make_integer_sequence<int, NU>{});
  }
  DEVI void last() {
    stage<true, true>(std::make_integer_sequence<int, NU>{});
    pin();
  }
  template <int KS>
  DEVI void run(unsigned lds0, int mt0, int nt0, int lane, unsigned so0) {
    setup(lds0, mt0, nt0, lane, so0);
    __builtin_amdgcn_s_barrier();
    first();
    mid<KS - 1>();
    last();
  }
};

DEVI const char* code_here() {
  unsigned long long pc;
  asm volatile("s_getpc_b64 %0" : "=s"(pc));
  return (const char*)pc;
}
DEVI int okmap(int p) {
  const int ch = p >> 3, e = p & 7, c = (ch - 8) & 3;
  return ch < 8 ? (2 * ch + (e >> 2)) * 36 + 16 + (e & 3) : ((ch - 8) >> 2) * 36 + c * 8 + (c >> 1) * 4 + e;
}

DEVI void pk_st16(void* p, u32x4 v) {
  asm volatile("global_store_dwordx4 %0, %1, off sc1\n\ts_nop 1" ::"v"(p), "v"(v) : "memory");
}

using K1C = GemmCfg<144, 112>;
constexpr int K1_TP = 240;
constexpr int K1_T_OFF = K1C::SB;
constexpr int K1_ST_OFF = 163840 - 128 - 1152;
constexpr int K1_CS_OFF = 4 * K1C::SB;
constexpr int K1_WX = 4 * K1C::SB + 2048;
constexpr unsigned long long K1_WS = 16ull | 32ull << 6 | 48ull << 12 | 56ull << 18 | 63ull << 24 | 16ull << 30 | 32ull << 36 | 48ull << 42;
static_assert(K1C::SB == 16 * 2048 && K1_WX - K1C::XB == 56 * 2048 && K1C::WB == 7 * 2048 && K1_WX + 2 * K1C::WB <= K1_ST_OFF, "K1 w places");
static_assert(K1_T_OFF + 144 * K1_TP + 64 <= 2 * K1C::SB + K1C::XB, "K1 LDS alias");
using K1R = GemmCfg<144, 144>;
constexpr int K1R_T_OFF = K1R::SB;
constexpr int K1R_CS_OFF = K1R_T_OFF + 144 * K1_TP;
constexpr int K1R_ST_OFF = K1R_CS_OFF + 512;
constexpr int K1R_DUMMY = K1R::LDS_BYTES;
constexpr int K1R_PO = K1R_DUMMY + 1024;
constexpr int K1R_CNT = 163840 - 128 + 28 * 4;
#define SPIN_ASM(TEST)                                                                                                   \
  if constexpr (SL <= 1)                                                                                                 \
    asm volatile(".La%=:\n\tds_read_b32 %0, %3\n\ts_waitcnt lgkmcnt(0)\n\tv_readfirstlane_b32 %2, %0\n\t" TEST SAFE_AND \
                 "s_cbranch_scc1 .La%="                                                                                  \
                 : "=&v"(v), NOP_(c), "=&s"(t)                                                                           \
                 : "v"(addr), "s"(k)                                                                                     \
                 : "memory", "scc");                                                                                     \
  else                                                                                                                   \
    asm volatile("s_branch .Lb%=\n.La%=:\n\ts_sleep %5\n.Lb%=:\n\tds_read_b32 %0, %3\n\ts_waitcnt lgkmcnt(0)\n\t"     \
                 "v_readfirstlane_b32 %2, %0\n\t" TEST SAFE_AND "s_cbranch_scc1 .La%="                                  \
                 : "=&v"(v), NOP_(c), "=&s"(t)                                                                           \
                 : "v"(addr), "s"(k), "n"(SL)                                                                            \
                 : "memory", "scc")
template <int SL>
DEVI void spin_ge(unsigned addr, unsigned k) {
  unsigned v, c = 1u << 18, t;
  SPIN_ASM("s_cmp_lt_u32 %2, %4\n\t");
}
template <int SL>
DEVI void spin_b4(unsigned addr, unsigned n) {
  unsigned v, c = 1u << 18, t;
  const unsigned k = (0x80u - n) * 0x01010101u;
  SPIN_ASM("s_add_u32 %2, %2, %4\n\ts_andn2_b32 %2, 0x80808080, %2\n\t");
}
DEVI void msync(unsigned addr) {
  asm volatile("s_waitcnt lgkmcnt(0)" ::: "memory");
  asm volatile("s_mov_b64 exec, 1\n\tds_add_u32 %0, %1\n\ts_mov_b64 exec, -1" ::"v"(addr), "v"(vconst(1u)) : "memory");
  spin_ge<0>(addr, 8u);
}
static_assert(K1R_PO + 13824 + 256 <= 163840 - 128 && K1R_ST_OFF + 144 * 8 <= 3 * K1R::SB, "K1R LDS");

DEVI void touch64(const char* sbase, unsigned voff, unsigned m0val) {
  asm volatile("s_mov_b32 m0, %2\n\ts_nop 0\n\tglobal_load_lds_dword %0, %1" : : "v"(voff), "s"(sbase), "s"(m0val) : "memory");
}

template <int MT, bool G1>
DEVI void k1_gemm_raw(char* smem, int hp, int mt0, int lane MF_ARG) {
  constexpr int NT = 4, nt0 = G1 ? 4 : 0;
  const unsigned lds0 = (unsigned)(size_t)smem;
  const int g = lane >> 4, r = lane & 15;
  unsigned ta[NT];
#pragma unroll
  for (int nt = 0; nt < NT; ++nt) {
    const int j = (nt0 + nt) * 16 + g * 4, seg = j / 40, cj = j - seg * 40 - (hp & 1) * 4;
    const int to = (seg < 3 && cj >= 0 && cj < 36) ? seg * 80 + cj * 2 + (cj >= 20 ? 8 : 0) : (seg < 3 ? 40 : 120);
    ta[nt] = lds0 + (unsigned)(K1R_T_OFF + (mt0 * 16 + r) * K1_TP + to);
    asm volatile("" : "+v"(ta[nt]));
  }
  unsigned sa = lds0 + (unsigned)(K1R_ST_OFF + (mt0 * 16 + r) * 8), ca = lds0 + (unsigned)(K1R_CS_OFF + (nt0 * 16 + g * 4) * 4);
  asm volatile("" : "+v"(sa), "+v"(ca));
  f32x4 acc[MT][NT];
#pragma unroll
  for (int mt = 0; mt < MT; ++mt)
#pragma unroll
    for (int nt = 0; nt < NT; ++nt) acc[mt][nt] = f32x4{0.f, 0.f, 0.f, 0.f};
  {
    GemmMath<K1R, MT, 4, 3, 4, false, 0, G1> gm(acc);
    if constexpr (G1) {
#pragma unroll
      for (int mt = 0; mt < MT; ++mt) gm.accq[mt] = f32x4{0.f, 0.f, 0.f, 0.f};
    }
    MF(14);
    if constexpr (!G1) {
      if (mt0 < 7) asm volatile("s_waitcnt vmcnt(0)" ::: "memory");
    }
    MF(15);
    gm.template run<9>(lds0, mt0, nt0, lane, 0u);
    MF(2);
    if constexpr (G1) {
#pragma unroll
      for (int mt = 0; mt < MT; ++mt) {
        const f32x4 q = gm.accq[mt];
        const float d = (r & 2) ? ((r & 1) ? q[3] : q[2]) : ((r & 1) ? q[1] : q[0]);
        const float qq = sum_g(g == (r >> 2) ? d : 0.f);
        float mb, rinv;
        ln_finish(acc[mt][3][0], qq, 1.0f / (float)D_, mb, rinv);
        const f32x2 st = {-mb, rinv};
        if (g == 2) asm volatile("ds_write_b64 %0, %1 offset:%2" ::"v"(sa), "v"(st), "n"(mt * 128) : "memory");
      }
    }
  }
  if (mt0 == 0 && r == 0) {
#pragma unroll
    for (int nt = 0; nt < NT; ++nt) *reinterpret_cast<f32x4*>(smem + K1R_CS_OFF + ((nt0 + nt) * 16 + g * 4) * 4) = acc[0][nt];
  }
  msync(lds0 + (unsigned)K1R_CNT);
  MF(3);
  f32x2 st[MT];
  f32x4 cs[NT];
#pragma unroll
  for (int nt = 0; nt < NT; ++nt) asm volatile("ds_read_b128 %0, %1 offset:%2" : "=v"(cs[nt]) : "v"(ca), "n"(nt * 64) : "memory");
#pragma unroll
  for (int mt = 0; mt < MT; ++mt) asm volatile("ds_read_b64 %0, %1 offset:%2" : "=v"(st[mt]) : "v"(sa), "n"(mt * 128) : "memory");
#pragma unroll
  for (int i = 0; i < MT; ++i) asm volatile("s_waitcnt lgkmcnt(0)" : "+v"(st[i]));
#pragma unroll
  for (int i = 0; i < NT; ++i) asm volatile("" : "+v"(cs[i]));
#pragma unroll
  for (int mt = 0; mt < MT; ++mt) {
    f32x2 M = {st[mt][0], st[mt][0]}, R = {st[mt][1], st[mt][1]};
    asm volatile("" : "+v"(M), "+v"(R));
#pragma unroll
    for (int nt = 0; nt < NT; ++nt) {
      const f32x2 v0 = R * __builtin_elementwise_fma(M, f32x2{cs[nt][0], cs[nt][1]}, f32x2{acc[mt][nt][0], acc[mt][nt][1]});
      const f32x2 v1 = R * __builtin_elementwise_fma(M, f32x2{cs[nt][2], cs[nt][3]}, f32x2{acc[mt][nt][2], acc[mt][nt][3]});
      acc[mt][nt][0] = __uint_as_float(pack2v(v0));
      acc[mt][nt][1] = __uint_as_float(pack2v(v1));
      const f32x2 pk = {acc[mt][nt][0], acc[mt][nt][1]};
      asm volatile("ds_write_b64 %0, %1 offset:%2" ::"v"(ta[nt]), "v"(pk), "n"(mt * 16 * K1_TP) : "memory");
    }
  }
}

template <int N>
DEVI void wait_vm() {
  asm volatile("s_waitcnt vmcnt(%0)" ::"n"(N) : "memory");
}

constexpr int LDS_ALL = 163840;
constexpr int LW_OFF = LDS_ALL - 128;
constexpr int LW_GO = 0, LW_T = 2, LW_O = 4  , LW_X1 = 6, LW_H = 8, LW_X2 = 12, LW_PK = 13, LW_Z = 14, LW_PAIR = 16, LW_AD = 20,
              LW_L = 22, LW_S = 24, LW_ST = 29, LW_HELLO = 30, LW_Q = 31;
DEVI void announce(unsigned addr, unsigned n, char* ws, int kind, int who, bool act) {
  const char* cb = uni_ptr(ws + OFF_CNT + (size_t)kind * 256 * 128);
  const unsigned long long mask = __ballot(act);
  unsigned t, u;
  asm volatile(
      "s_waitcnt vmcnt(0)\n\t"
      "s_mov_b64 exec, 1\n\t"
      "ds_add_rtn_u32 %0, %2, %3\n\t"
      "s_waitcnt lgkmcnt(0)\n\t"
      "v_readfirstlane_b32 %1, %0\n\t"
      "s_cmp_eq_u32 %1, %4\n\t"
      "s_cselect_b64 exec, %5, 0\n\t"
      "global_atomic_add %6, %3, %7 sc1\n\t"
      "s_mov_b64 exec, -1\n\t"
      "s_nop 0"
      : "=&v"(t), "=&s"(u)
      : "v"(addr), "v"(vconst(1u)), "s"(n - 1u), "s"(mask), "v"((unsigned)(who & 255) * 128u), "s"(cb)
      : "memory", "scc");
}
DEVI void cnt_wait(const char* ws, int kind, int who, unsigned n) {
  const char* cp = uni_ptr(ws + OFF_CNT + (size_t)(kind * 256 + who) * 128);
  unsigned v, c = SPIN_MEM, t;
  asm volatile(
      ".La%=:\n\tglobal_load_dword %0, %3, %4 sc1\n\ts_sub_u32 %1, %1, 1\n\ts_waitcnt vmcnt(0)\n\tv_readfirstlane_b32 %2, %0\n\ts_cmp_lg_u32 %2, %5\n\t"
      "s_cselect_b32 %2, %1, 0\n\ts_cmp_lg_u32 %2, 0\n\ts_cbranch_scc1 .La%="
      : "=&v"(v), "+s"(c), "=&s"(t)
      : "v"(vconst(0u)), "s"(cp), "s"(n - 1u)
      : "memory", "scc");
}
DEVI void dma16_sc1(const char* sbase, unsigned voff, unsigned m0val) {
  asm volatile("s_mov_b32 m0, %2\n\ts_nop 0\n\tglobal_load_lds_dwordx4 %0, %1 sc1"
               :
               : "v"(voff), "s"(sbase), "s"(m0val)
               : "memory");
}
template <int N, bool SC1>
DEVI void dma4(const char* sbase, unsigned v0, unsigned v1, unsigned v2, unsigned v3, unsigned m0val) {
#define DMA4(S)                                                                                                               \
  asm volatile("s_mov_b32 m0, %5\n\ts_nop 0\n\tglobal_load_lds_dwordx4 %0, %4" S "\n\t.if %6 > 1\n\tglobal_load_lds_dwordx4 %1, %4 offset:1024" S \
               "\n\t.endif\n\t.if %6 > 2\n\tglobal_load_lds_dwordx4 %2, %4 offset:2048" S                                    \
               "\n\t.endif\n\t.if %6 > 3\n\tglobal_load_lds_dwordx4 %3, %4 offset:3072" S "\n\t.endif"                      \
               :                                                                                                              \
               : "v"(v0), "v"(v1), "v"(v2), "v"(v3), "s"(sbase), "s"(m0val), "n"(N)                                           \
               : "memory")
  if constexpr (SC1) DMA4(" sc1");
  else DMA4("");
}
template <int N>
DEVI void dma_seq(const char* src, unsigned voff, unsigned m0val) {
  if constexpr (N > 0) {
    dma4<(N < 4 ? N : 4), true>(src, voff, voff, voff, voff, m0val);
    dma_seq<N - 4>(src + 4096, voff, m0val + 4096u);
  }
}
DEVI void bar() { __builtin_amdgcn_s_barrier(); }
DEVI void lds_inc(unsigned addr, unsigned by = 1u) {
  asm volatile("s_mov_b64 exec, 1\n\tds_add_u32 %0, %1\n\ts_mov_b64 exec, -1" ::"v"(addr), "v"(vconst(by)) : "memory");
}
DEVI void lds_spin(unsigned addr, unsigned n) {
  spin_ge<1>(addr, n);
}
template <int SL = 1>
DEVI void lds_spin4(unsigned addr, unsigned n) {
  spin_b4<SL>(addr, n);
}
DEVI int lane_id() {
  int l;
  asm volatile("v_mbcnt_lo_u32_b32 %0, -1, 0\n\tv_mbcnt_hi_u32_b32 %0, -1, %0" : "=v"(l));
  __builtin_assume((unsigned)l < 64u);
  return l;
}

template <int I>
DEVI u32x4 tr8(const u32x4 (&v)[8]) {
  constexpr unsigned sel = (I & 1) ? 0x07060302u : 0x05040100u;
  return u32x4{__builtin_amdgcn_perm(v[1][I / 2], v[0][I / 2], sel), __builtin_amdgcn_perm(v[3][I / 2], v[2][I / 2], sel),
               __builtin_amdgcn_perm(v[5][I / 2], v[4][I / 2], sel), __builtin_amdgcn_perm(v[7][I / 2], v[6][I / 2], sel)};
}
template <int PITCH, bool SUM, int... I>
DEVI void tr8_store(const u32x4 (&v)[8], unsigned dst, float (&cs)[8], std::integer_sequence<int, I...>) {
  auto one = [&](auto I_) {
    const u32x4 o = tr8<I_>(v);
    const unsigned d_ = dst;
    asm volatile("ds_write_b128 %0, %1 offset:%2" ::"v"(d_), "v"(o), "n"(I_ * PITCH) : "memory");
    if constexpr (SUM) {
#pragma unroll
      for (int d = 0; d < 4; ++d) {
        const unsigned od = o[d];
        cs[I_] = __builtin_amdgcn_fdot2_f32_bf16(__builtin_bit_cast(bf16x2, od), __builtin_bit_cast(bf16x2, 0x3f803f80u), cs[I_], false);
      }
    }
  };
  (one(std::integral_constant<int, I>{}), ...);
}
template <int NB>
DEVI void lds_out(unsigned src, char* dst, int lane) {
  constexpr int N = (NB + 1023) / 1024;
  u32x4 t[N];
  const unsigned a = src + (unsigned)lane * 16u;
#pragma unroll
  for (int i = 0; i < N; ++i) asm volatile("ds_read_b128 %0, %1 offset:%2" : "=v"(t[i]) : "v"(a), "n"(i * 1024) : "memory");
  const char* d = uni_ptr(dst);
#pragma unroll
  for (int i = 0; i < N; ++i) {
    asm volatile("s_waitcnt lgkmcnt(%1)" : "+v"(t[i]) : "n"(N - 1 - i) : "memory");
    if (i < N - 1 || lane < (NB - (N - 1) * 1024) / 16)
      asm volatile("global_store_dwordx4 %0, %1, %2 sc1\n\ts_nop 1" ::"v"((unsigned)lane * 16u), "v"(t[i]), "s"(d + i * 1024) : "memory");
  }
}
template <int A, int B, class F>
DEVI void static_for(F f) {
  if constexpr (A < B) {
    f(std::integral_constant<int, A>{});
    static_for<A + 1, B>(f);
  }
}
constexpr int WSP_V = 3;
constexpr int RAW_S1 = 6;
constexpr int RAW_S0 = 4;
template <int E>
struct RawX {
  static constexpr int first(int s) {
    return s <= RAW_S0 ? 0 : (s > RAW_S1 ? E : (E * (s - RAW_S0) + RAW_S1 - RAW_S0) / (RAW_S1 + 1 - RAW_S0));
  }
  static constexpr int n(int s) { return first(s + 1) - first(s); }
};
struct RawLd {
  static constexpr int VPS = 10, SB = K1R::SB;
  unsigned xoff[5], woff[5], m0x;
  unsigned long long xact0, wact[5], tag;
  const char *xp, *wp;
  int qb;
  DEVI void q_hook(int st, char* ws, unsigned lds0, int hp) {
    if ((qb >> 1) == st) {
      const int lane = lane_id(), g = lane >> 4;
      const unsigned a = lds0 + (unsigned)((st & 3) * SB + K1R::XB + ((qb & 1) * 32 + 16 * (g >> 1) + 4 * (g & 1) + ((lane >> 2) & 3)) * 288 +
                                           (lane & 3) * 8 + (hp & 1) * 8);
      const unsigned a2 = a + ((lane & 3) >= 1 ? 8u : 0u), a4 = a + ((lane & 3) >= 2 ? 8u : 0u);
      u32x2 qf[7][2];
#pragma unroll
      for (int nt = 0; nt < 7; ++nt)
        asm volatile("ds_read_b64_tr_b16 %0, %2 offset:%3\n\tds_read_b64_tr_b16 %1, %2 offset:%4"
                     : "=&v"(qf[nt][0]), "=&v"(qf[nt][1])
                     : "v"(nt == 2 ? a2 : (nt == 4 ? a4 : a)), "n"(nt * 32 + (nt > 4 ? 16 : (nt > 2 ? 8 : 0))),
                       "n"(2304 + nt * 32 + (nt > 4 ? 16 : (nt > 2 ? 8 : 0)))
                     : "memory");
      const char* d = uni_ptr(ws + OFF_PQ + (size_t)(hp * 18 + qb) * 7168 + 3072);
#pragma unroll
      for (int nt = 0; nt < 7; ++nt) {
        asm volatile("s_waitcnt lgkmcnt(%2)" : "+v"(qf[nt][0]), "+v"(qf[nt][1]) : "n"(12 - 2 * nt) : "memory");
        const u32x4 v = {qf[nt][0][0], qf[nt][0][1], qf[nt][1][0], qf[nt][1][1]};
        asm volatile("global_store_dwordx4 %0, %1, %2 offset:%3 sc1\n\ts_nop 1" ::"v"((unsigned)lane * 16u), "v"(v), "s"(d), "n"(nt * 1024 - 3072) : "memory");
      }
      if (st == 8) lds_wr32(lds0 + (unsigned)(LW_OFF + LW_Q * 4), 1u);
    }
  }
  DEVI void stage_x(unsigned so) {
    asm volatile(
        "s_mov_b32 m0, %6\n\ts_mov_b64 exec, %7\n\tglobal_load_lds_dwordx4 %0, %5\n\ts_mov_b64 exec, -1\n\t"
        "global_load_lds_dwordx4 %1, %5 offset:1024\n\tglobal_load_lds_dwordx4 %2, %5 offset:2048\n\tglobal_load_lds_dwordx4 %3, %5 offset:3072\n\t"
        "s_add_u32 m0, m0, 0x1000\n\ts_mov_b32 exec_hi, 0\n\tglobal_load_lds_dwordx4 %4, %5\n\ts_mov_b32 exec_hi, -1" ::"v"(xoff[0]),
        "v"(xoff[1]), "v"(xoff[2]), "v"(xoff[3]), "v"(xoff[4]), "s"(xp), "s"(m0x + so), "s"(xact0)
        : "memory", "scc");
    xp += 128;
  }
  DEVI void stage_w(unsigned so) {
    asm volatile(
        "s_mov_b32 m0, %6\n\ts_mov_b64 exec, %7\n\tglobal_load_lds_dwordx4 %0, %5\n\ts_mov_b64 exec, %8\n\t"
        "global_load_lds_dwordx4 %1, %5 offset:1024\n\ts_mov_b64 exec, %9\n\tglobal_load_lds_dwordx4 %2, %5 offset:2048\n\t"
        "s_mov_b64 exec, %10\n\tglobal_load_lds_dwordx4 %3, %5 offset:3072\n\ts_add_u32 m0, m0, 0x1000\n\ts_mov_b64 exec, %11\n\t"
        "global_load_lds_dwordx4 %4, %5\n\ts_mov_b64 exec, -1" ::"v"(woff[0]),
        "v"(woff[1]), "v"(woff[2]), "v"(woff[3]), "v"(woff[4]), "s"(wp), "s"(m0x + so + (unsigned)K1R::XB), "s"(wact[0]), "s"(wact[1]),
        "s"(wact[2]), "s"(wact[3]), "s"(wact[4])
        : "memory", "scc");
    wp += 64 * NQKV * 2;
  }
  DEVI void stage(unsigned so) {
    stage_x(so);
    stage_w(so);
  }
  DEVI void front(const u16* x, const u16* wqkv, const u16* wo, char* ws, int bid, unsigned lds0, int wl, int lane, int rt, int hp,
                  int pj MF_ARG) {
    xp = uni_ptr((const char*)x - 3072);
    wp = uni_ptr((const char*)wqkv + ((hp * 72) & ~15) - 3072);
    m0x = lds0 + (unsigned)(wl * 288 * 16);
    xact0 = wl ? ~0ull : ~0xffull;
    const int row0 = wl * 36 + (lane >> 3);
#pragma unroll
    for (int i = 0; i < 5; ++i) {
      const int row = row0 + i * 8;
      int gr = rt * 128 - 16 + row;
      if (gr < 0) gr = 0;
      xoff[i] = (unsigned)(gr * (D_ * 2) + (3 - (i & 3)) * 1024) + (unsigned)(((lane ^ (row >> 1)) & 7) * 16);
    }
    stage_x(0u);
    u32x2 tg;
    asm volatile("global_load_dwordx2 %0, %1, %2 sc1" : "=v"(tg) : "v"(0u), "s"(uni_ptr(ws + OFF_TAG)) : "memory");
    asm volatile("" : "+v"(lane));
#pragma unroll
    for (int i = 0; i < 5; ++i) {
      const unsigned c = (unsigned)(i * 64 + (lane & 63));
      const unsigned wr = (c * 3641u) >> 16, wc = c - wr * 18u, seg = (wc * 13u) >> 6;
      wact[i] = __ballot(wc < 15u) & (i < 4 ? ~0ull : 0xffffffffull);
      woff[i] = (unsigned)(wl * 16 + swap23((int)wr)) * (unsigned)(NQKV * 2) + seg * (unsigned)(D_ * 2 - 80) + wc * 16u + (unsigned)((3 - (i & 3)) * 1024);
    }
    stage_w(0u);
    stage((unsigned)SB);
    MF(10);
    asm volatile("s_waitcnt vmcnt(%1)" : "+v"(tg) : "n"(VPS) : "memory");
    tag = (((unsigned long long)(unsigned)__builtin_amdgcn_readfirstlane(tg[1]) << 32) | (unsigned)__builtin_amdgcn_readfirstlane(tg[0])) +
          0x9e3779b97f4a7c15ull;
    MF(1);
    __builtin_amdgcn_s_barrier();
    MF(15);
    if (wl == 0 && lane == 0)
      asm volatile("global_store_dwordx2 %0, %1, off sc1\n\ts_nop 1" ::"v"(ws + OFF_HELLO + bid * 8), "v"(tag) : "memory");
    stage((unsigned)(2 * SB));
    qb = wl == 3 ? rt : ((wl == 2 && rt < 2) ? 16 + rt : 99);
    q_hook(0, ws, lds0, hp);
    const int nb = pj;
    const bool po = pj >= 0 && wl < 2;
    u32x4 v[8];
    unsigned da = 0u;
    if (po) {
      const int l54 = lane < 54 ? lane : 53, gl = l54 / 9, c = l54 - gl * 9, kl = wl * 6 + gl, t = nb / 6, kg = (nb - t * 6) * 12 + kl;
      da = lds0 + (unsigned)(K1R_PO + (kl >> 2) * 4608 + (16 * (kl & 1) + ((kl >> 1) & 1)) * 144 + c * 16);
      const unsigned sa[2] = {(unsigned)(okmap(kg * 8) * D_ + t * 72 + c * 8) * 2u, (unsigned)(okmap(kg * 8 + 4) * D_ + t * 72 + c * 8) * 2u};
      const char* ps = uni_ptr((const char*)wo);
#pragma unroll
      for (int j = 0; j < 8; ++j)
        asm volatile("global_load_dwordx4 %0, %1, %2 offset:%3" : "=v"(v[j]) : "v"(sa[j / 4]), "s"(ps), "n"((j % 4) * D_ * 2) : "memory");
    } else {
#pragma unroll
      for (int j = 0; j < 8; ++j) asm volatile("" : "=v"(v[j]));
    }
    unsigned slot = (unsigned)(3 * SB);
#pragma unroll 1
    for (int S = 0; S < RAW_S0; ++S) {
      wait_vm<VPS>();
      __builtin_amdgcn_s_barrier();
      if (S == 0) MF(16);
      if (S == 1) MF(17);
      if (S == 2) MF(18);
      if (S == 3) MF(19);
      stage(slot);
      slot = (slot == (unsigned)(3 * SB)) ? 0u : slot + (unsigned)SB;
      q_hook(S + 1, ws, lds0, hp);
      if (po && S == 1) {
        float cs[8];
        asm volatile("s_nop 0 ; LOOPWAIT" : "+v"(v[0]), "+v"(v[1]), "+v"(v[2]), "+v"(v[3]), "+v"(v[4]), "+v"(v[5]), "+v"(v[6]), "+v"(v[7]));
        tr8_store<288, false>(v, da, cs, std::make_integer_sequence<int, 8>{});
        asm volatile("s_waitcnt lgkmcnt(0)" ::: "memory");
      }
      if (po && S == 2) lds_out<6912>(lds0 + (unsigned)(K1R_PO + wl * 6912), ws + OFF_PO + (size_t)nb * 13824 + wl * 6912, lane);
    }
  }
};
template <bool JOB>
DEVI void k1_raw_loader(RawLd& ld, const u16* __restrict__ w_in, const u16* __restrict__ w_out, char* __restrict__ ws,
                        unsigned long long tag, int bid, unsigned lds0, int wl, int lane, int rt, int hp, int ct, int job, int pj MF_ARG) {
  constexpr int VPS = RawLd::VPS, SB = RawLd::SB, E = JOB ? 16 : 0;
  const bool isi = job < 144;
  const int kb = isi ? job >> 3 : job - 144;
  const int pitch = isi ? F_ * 2 : D_ * 2;
  const int l36 = lane < 36 ? lane : 35;
  unsigned sa = (unsigned)l36 * 16u, da;
  if (isi) da = lds0 + (unsigned)((l36 >= 18 ? 9216 - 288 : 0) + wl * 8 * 288 + l36 * 16);
  else da = lds0 + (unsigned)((l36 / 9) * (4608 - 144) + (16 * (wl & 1) + (wl >> 1)) * 144 + l36 * 16);
  asm volatile("" : "+v"(sa), "+v"(da));
  u32x4 v[2][8];
  const char* ps = uni_ptr((const char*)(isi ? w_in + (job & 7) * 576 : w_out) + (size_t)(kb * 32 + wl * 8) * pitch);
  auto extra = [&](auto J_) {
    constexpr int j = J_;
    const char* q = ps + (j / 2) * pitch;
    u32x4 t;
    asm volatile("global_load_dwordx4 %0, %1, %2 offset:%3" : "=v"(t) : "v"(sa), "s"(q), "n"((j % 2) * 576) : "memory");
    v[j % 2][j / 2] = t;
  };
  auto step = [&](auto S_) {
    constexpr int S = S_;
    wait_vm<RawX<E>::n(S - 2) + (S <= 6 ? VPS : 0) + RawX<E>::n(S - 1)>();
    __builtin_amdgcn_s_barrier();
    MF(16 + S);
    if constexpr (S == RAW_S0) {
      static_assert(RAW_S0 >= 4, "wo blocks");
      if (pj >= 0 && wl == 0 && lane == 0)
        asm volatile("global_store_dwordx2 %0, %1, off sc1\n\ts_nop 1" ::"v"(ws + OFF_POT + (pj / 6) * 128 + (pj % 6) * 8), "v"(tag) : "memory");
    }
    if constexpr (S <= 5) ld.stage((unsigned)(((S + 3) % 4) * SB));
    ld.q_hook(S + 1, ws, lds0, hp);
    static_for<RawX<E>::first(S), RawX<E>::first(S + 1)>(extra);
  };
  const char* hp_ = ws + OFF_HELLO + lane * 32;
  u32x4 a, b;
  if (wl == 1) asm volatile("global_load_dwordx4 %0, %2, off sc1\n\tglobal_load_dwordx4 %1, %2, off offset:16 sc1" : "=&v"(a), "=&v"(b) : "v"(hp_) : "memory");
  else asm volatile("" : "=v"(a), "=v"(b));
  static_for<RAW_S0, 8>(step);
  MF(2);
  if (wl == 1) {
    asm volatile("" : "+v"(a), "+v"(b));
    const unsigned t0 = (unsigned)tag, t1 = (unsigned)(tag >> 32);
#pragma unroll 1
    for (unsigned spin = 0; spin < SPIN_MEM; ++spin) {
      const unsigned bad = ((a[0] ^ t0) | (a[2] ^ t0) | (b[0] ^ t0) | (b[2] ^ t0)) | ((a[1] ^ t1) | (a[3] ^ t1) | (b[1] ^ t1) | (b[3] ^ t1));
      if (!__any(bad != 0u)) break;
      __builtin_amdgcn_s_sleep(4);
      asm volatile("global_load_dwordx4 %0, %2, off sc1\n\tglobal_load_dwordx4 %1, %2, off offset:16 sc1\n\ts_waitcnt vmcnt(0)"
                   : "=&v"(a), "=&v"(b)
                   : "v"(hp_)
                   : "memory");
    }
    lds_wr32(lds0 + (unsigned)(LW_OFF + LW_HELLO * 4), 1u);
    if (bid == 0 && lane == 0) asm volatile("global_store_dwordx2 %0, %1, off sc1\n\ts_nop 1" ::"v"(ws + OFF_TAG), "v"(tag) : "memory");
  }
  MF(11);
  if (rt < 2) lds_spin(lds0 + (unsigned)(LW_OFF + LW_Q * 4), 1u);
  lds_spin(lds0 + (unsigned)K1R_CNT, 8u);
  if constexpr (JOB) {
#pragma unroll
    for (int b = 0; b < 2; ++b)
      asm volatile("s_waitcnt vmcnt(0)"
                   : "+v"(v[b][0]), "+v"(v[b][1]), "+v"(v[b][2]), "+v"(v[b][3]), "+v"(v[b][4]), "+v"(v[b][5]), "+v"(v[b][6]), "+v"(v[b][7])
                   :
                   : "memory");
  }
  MF(12);
  const char* tp = ws + OFF_POT + ct * 128 + (lane < 6 ? lane : 5) * 8;
  u32x2 f;
  asm volatile("global_load_dwordx2 %0, %1, off sc1" : "=v"(f) : "v"(tp) : "memory");
  if constexpr (JOB) {
    static_assert(SB == 4 * 9216, "staging");
#pragma unroll
    for (int b = 0; b < 2; ++b) {
      float cs[8] = {0.f, 0.f, 0.f, 0.f, 0.f, 0.f, 0.f, 0.f};
      tr8_store<288, true>(v[b], da + (unsigned)(b * 18432), cs, std::make_integer_sequence<int, 8>{});
      const f32x4 c0 = {cs[0], cs[1], cs[2], cs[3]}, c1 = {cs[4], cs[5], cs[6], cs[7]};
      const unsigned ca = lds0 + (unsigned)(3 * SB + wl * 2304) + sa * 2u;
      asm volatile("ds_write_b128 %0, %1 offset:%2" ::"v"(ca), "v"(c0), "n"(b * 1152) : "memory");
      asm volatile("ds_write_b128 %0, %1 offset:%2" ::"v"(ca), "v"(c1), "n"(b * 1152 + 16) : "memory");
    }
    lds_inc(lds0 + (unsigned)(LW_OFF + LW_ST * 4));
    lds_spin(lds0 + (unsigned)(LW_OFF + LW_ST * 4), 4u);
    if (isi) {
      const int j = (job & 7) * 4 + wl, ti = j < 16 ? 2 * j + 1 : 2 * (j - 16);
      lds_out<9216>(lds0 + (unsigned)(wl * 9216), ws + OFF_PI + (size_t)(ti * 18 + kb) * 9216, lane);
      f32x4 c[4];
      const unsigned ca = lds0 + (unsigned)(3 * SB + wl * 576) + sa;
#pragma unroll
      for (int i = 0; i < 4; ++i) asm volatile("ds_read_b128 %0, %1 offset:%2" : "=v"(c[i]) : "v"(ca), "n"(i * 2304) : "memory");
      asm volatile("s_waitcnt lgkmcnt(0)" : "+v"(c[0]), "+v"(c[1]), "+v"(c[2]), "+v"(c[3]));
      const f32x4 t = (c[0] + c[1]) + (c[2] + c[3]);
      if (lane < 36) pk_st16(ws + OFF_CSP + ((size_t)kb * F_ + ti * 144) * 4 + sa, __builtin_bit_cast(u32x4, t));
    } else {
      lds_out<4608>(lds0 + (unsigned)(wl * 9216), ws + OFF_PW + (size_t)(wl * 144 + kb) * 4608, lane);
      lds_out<4608>(lds0 + (unsigned)(wl * 9216 + 4608), ws + OFF_PW + (size_t)(wl * 144 + 72 + kb) * 4608, lane);
    }
  }
  MF(8);
  announce(lds0 + (unsigned)(LW_OFF + LW_PK * 4), 4u, ws, CN_PK, isi ? 8 + (job & 3) * 4 + lane : 24 + lane, JOB && lane < (isi ? 4 : 8));
  MF(9);
  if (wl == 0) {
    asm volatile("" : "+v"(f));
#pragma unroll 1
    for (unsigned spin = 0; spin < SPIN_MEM; ++spin) {
      if (!__any(f[0] != (unsigned)tag || f[1] != (unsigned)(tag >> 32))) break;
      __builtin_amdgcn_s_sleep(2);
      asm volatile("global_load_dwordx2 %0, %1, off sc1\n\ts_waitcnt vmcnt(0)" : "=&v"(f) : "v"(tp) : "memory");
    }
  }
}

template <int MT, int NT>
DEVI void k1_gemm(char* smem, const char* ws, int rt, int hp, int mt0, int nt0,
                  bool late, int lane MK_ARG) {
  const unsigned lds0 = (unsigned)(size_t)smem;
  const int g = lane >> 4;
  f32x4 cs[NT];
  unsigned ta[NT];
  unsigned cad[NT];
#pragma unroll
  for (int nt = 0; nt < NT; ++nt) {
    int c = (nt0 + nt) * 16 + g * 4;
    if (c > 104) c = 104;
    const int part = c / 36, ci = c - part * 36;
    cad[nt] = lds0 + (unsigned)(K1_CS_OFF + (part * 40 + (hp & 1) * 4 + ci) * 4);
    asm volatile("" : "+v"(cad[nt]));
    const int to = (nt0 + nt) * 16 + g * 4 > 104 ? 120 : part * 80 + ci * 2 + (ci >= 20 ? 8 : 0);
    ta[nt] = lds0 + (unsigned)(K1_T_OFF + (mt0 * 16 + (lane & 15)) * K1_TP + to);
    asm volatile("" : "+v"(ta[nt]));
  }
  unsigned sa = lds0 + (unsigned)(K1_ST_OFF + (mt0 * 16 + (lane & 15)) * 8);
  asm volatile("" : "+v"(sa));
  f32x4 acc[MT][NT];
#pragma unroll
  for (int mt = 0; mt < MT; ++mt)
#pragma unroll
    for (int nt = 0; nt < NT; ++nt) acc[mt][nt] = f32x4{0.f, 0.f, 0.f, 0.f};
  f32x2 st[MT];
  if (late) bar();
  if constexpr (NT == 7) cnt_wait(ws, CN_X2, rt * 16 + hp, rt ? 24u : 16u);
  bar();
  MK(1);
  {
    GemmMath<K1C, MT, NT, (NT == 7 ? 5 : 3), (NT == 3 ? 6 : NT), true, 2, false, K1_WS> gm(acc);
    gm.setup(lds0, mt0, nt0, lane, 0u);
    bar();
#pragma unroll
    for (int nt = 0; nt < NT; ++nt) asm volatile("ds_read_b128 %0, %1" : "=v"(cs[nt]) : "v"(cad[nt]));
    gm.first();
    gm.template mid<8>();
#pragma unroll
    for (int mt = 0; mt < MT; ++mt) asm volatile("ds_read_b64 %0, %1 offset:%2" : "=v"(st[mt]) : "v"(sa), "n"(mt * 128));
    asm volatile("s_waitcnt vmcnt(0)" ::: "memory");
    gm.last();
  }
#pragma unroll
  for (int nt = 0; nt < NT; ++nt) asm volatile("" : "+v"(cs[nt]));
#pragma unroll
  for (int mt = 0; mt < MT; ++mt) asm volatile("" : "+v"(st[mt]));
  MK(2);
#pragma unroll
  for (int mt = 0; mt < MT; ++mt) {
    f32x2 M = {st[mt][0], st[mt][0]}, R = {st[mt][1], st[mt][1]};
    asm volatile("" : "+v"(M), "+v"(R));
#pragma unroll
    for (int nt = 0; nt < NT; ++nt) {
      const f32x2 v0 = R * __builtin_elementwise_fma(M, f32x2{cs[nt][0], cs[nt][1]}, f32x2{acc[mt][nt][0], acc[mt][nt][1]});
      const f32x2 v1 = R * __builtin_elementwise_fma(M, f32x2{cs[nt][2], cs[nt][3]}, f32x2{acc[mt][nt][2], acc[mt][nt][3]});
      acc[mt][nt][0] = __uint_as_float(pack2v(v0));
      acc[mt][nt][1] = __uint_as_float(pack2v(v1));
      const f32x2 pk = {acc[mt][nt][0], acc[mt][nt][1]};
      asm volatile("ds_write_b64 %0, %1 offset:%2" ::"v"(ta[nt]), "v"(pk), "n"(mt * 16 * K1_TP) : "memory");
    }
  }
}

DEVI float vmax(float a, float b) {
  float r;
  asm("v_max_f32 %0, %1, %2" : "=v"(r) : "v"(a), "v"(b));
  return r;
}
struct Attn {
  unsigned ka, kb, va, vb, m0, m1, m2, pm[2], oa, pa;
  f32x4 mk;
  unsigned long long sel[4];
  const char* ob;
  DEVI void setup(unsigned T, char* o, int r0, int hp, int wave, int lane) {
    const int g = lane >> 4, r = lane & 15, blk = wave >> 1, hd = wave & 1;
    const unsigned co = (unsigned)(g < 2 ? hd * 48 + g * 16 : 32);
    ka = T + (unsigned)((blk * 32 + 2 * r) * K1_TP) + co;
    kb = T + (unsigned)((blk * 32 + 2 * (r & 7)) * K1_TP) + co;
    const int vr = 4 * g + ((lane >> 2) & 3);
    va = T + (unsigned)((blk * 32 + 2 * vr) * K1_TP + (lane & 3) * 8);
    vb = T + (unsigned)((blk * 32 + 2 * (vr & 7)) * K1_TP + (lane & 3) * 8);
    m0 = (g < 2 || (g == 2 && hd == 0)) ? ~0u : 0u;
    m1 = (g < 2 || (g == 2 && hd == 1)) ? ~0u : 0u;
    m2 = g < 2 ? ~0u : 0u;
    pm[0] = pm[1] = 0u;
    const float ci = __uint_as_float(vconst(0xff800000u));
    const unsigned ch = vconst(0xffffu);
#pragma unroll
    for (int e = 0; e < 4; ++e) {
      const int w1 = r + 8 - 4 * g - e;
      mk[e] = (w1 & 15) <= 7 ? 0.f : ci;
      sel[e] = __ballot(w1 >= 8);
      if (w1 >= 8) pm[e >> 1] |= ch << (16 * (e & 1));
    }
    oa = (unsigned)((8 + hp * 4 + hd * 2 + (g >> 1)) * 1024 + ((blk & 1) * 32 + 2 * r + (g & 1)) * 16);
    pa = ((oa & 1023u) >> 2) + (unsigned)((hp * 2 + hd) * 256);
    ob = uni_ptr(o + (size_t)((r0 >> 6) + (wave >> 2)) * (72 * 1024));
    asm volatile("" : "+v"(ka), "+v"(kb), "+v"(va), "+v"(vb), "+v"(m0), "+v"(m1), "+v"(m2), "+v"(pm[0]), "+v"(pm[1]), "+v"(oa));
    asm volatile("" : "+v"(mk), "+v"(pa), "+s"(ob), "+s"(sel[0]), "+s"(sel[1]), "+s"(sel[2]), "+s"(sel[3]));
  }
  DEVI void run(int r0, int hp, int wave, int lane) {
    const int hd = wave & 1;
    u32x4 q[2], k0[2], k1[2];
    u32x2 v[2][2][2];
#pragma unroll
    for (int p = 0; p < 2; ++p) {
      asm volatile("ds_read_b128 %0, %1 offset:%2" : "=v"(q[p]) : "v"(ka), "n"((16 + p) * K1_TP) : "memory");
      asm volatile("ds_read_b128 %0, %1 offset:%2" : "=v"(k0[p]) : "v"(ka), "n"(p * K1_TP + 80) : "memory");
      asm volatile("ds_read_b128 %0, %1 offset:%2" : "=v"(k1[p]) : "v"(kb), "n"((32 + p) * K1_TP + 80) : "memory");
    }
    const unsigned vh = (unsigned)hd * 48u;
#pragma unroll
    for (int p = 0; p < 2; ++p) {
      asm volatile("ds_read_b64_tr_b16 %0, %1 offset:%2" : "=v"(v[p][0][0]) : "v"(va + vh), "n"(p * K1_TP + 160) : "memory");
      asm volatile("ds_read_b64_tr_b16 %0, %1 offset:%2" : "=v"(v[p][0][1]) : "v"(vb + vh), "n"((32 + p) * K1_TP + 160) : "memory");
      asm volatile("ds_read_b64_tr_b16 %0, %1 offset:%2" : "=v"(v[p][1][0]) : "v"(va), "n"(p * K1_TP + 192) : "memory");
      asm volatile("ds_read_b64_tr_b16 %0, %1 offset:%2" : "=v"(v[p][1][1]) : "v"(vb), "n"((32 + p) * K1_TP + 192) : "memory");
    }
    const u32x4 one4 = {vconst(0x3f803f80u), vconst(0x3f803f80u), vconst(0x3f803f80u), vconst(0x3f803f80u)};
    const s16x8 ones = __builtin_bit_cast(s16x8, one4);
    const float c = 0.23570226039551584f * 1.4426950408889634f;
    const f32x4 z = {0.f, 0.f, 0.f, 0.f};
    f32x4 sc[2][2];
#pragma unroll
    for (int p = 0; p < 2; ++p) {
      asm volatile("s_waitcnt lgkmcnt(%3)" : "+v"(q[p]), "+v"(k0[p]), "+v"(k1[p]) : "n"(11 - 3 * p));
      const s16x8 qf = __builtin_bit_cast(s16x8, q[p] & u32x4{m0, m1, m2, m2});
      sc[p][0] = __builtin_amdgcn_mfma_f32_16x16x32_bf16(__builtin_bit_cast(s16x8, k0[p]), qf, mk, 0, 0, 0);
      sc[p][1] = __builtin_amdgcn_mfma_f32_16x16x32_bf16(__builtin_bit_cast(s16x8, k1[p]), qf, mk, 0, 0, 0);
      __builtin_amdgcn_sched_barrier(0);
    }
    if (__builtin_expect(r0 == 0 && wave < 2, 0)) {
#pragma unroll
      for (int p = 0; p < 2; ++p)
#pragma unroll
        for (int e = 0; e < 4; ++e)
          if (lane < 32) sc[p][0][e] = mk[e] - 4.242640687e9f;
    }
    float lg[2][4], mx[2];
#pragma unroll
    for (int p = 0; p < 2; ++p) {
#pragma unroll
      for (int e = 0; e < 4; ++e) lg[p][e] = __builtin_amdgcn_inverse_ballot_w64(sel[e]) ? sc[p][1][e] : sc[p][0][e];
      asm("v_max3_f32 %0, %1, %2, %3\n\tv_max_f32 %0, %0, %4" : "=&v"(mx[p]) : "v"(lg[p][0]), "v"(lg[p][1]), "v"(lg[p][2]), "v"(lg[p][3]));
    }
    {
      const auto a = __builtin_amdgcn_permlane32_swap(__float_as_uint(mx[0]), __float_as_uint(mx[1]), false, false);
      const unsigned b = __float_as_uint(vmax(__uint_as_float(a[0]), __uint_as_float(a[1])));
      const auto d = __builtin_amdgcn_permlane16_swap(b, b, false, false);
      const unsigned f = __float_as_uint(vmax(__uint_as_float(d[0]), __uint_as_float(d[1])) * -c);
      const auto h = __builtin_amdgcn_permlane32_swap(f, f, false, false);
      mx[0] = __uint_as_float(h[0]);
      mx[1] = __uint_as_float(h[1]);
    }
#pragma unroll
    for (int p = 0; p < 2; ++p)
#pragma unroll
      for (int e = 0; e < 4; ++e) lg[p][e] = __builtin_amdgcn_exp2f(__builtin_fmaf(lg[p][e], c, mx[p]));
    asm volatile("s_waitcnt lgkmcnt(0)"
                 : "+v"(v[0][0][0]), "+v"(v[0][0][1]), "+v"(v[0][1][0]), "+v"(v[0][1][1]), "+v"(v[1][0][0]), "+v"(v[1][0][1]),
                   "+v"(v[1][1][0]), "+v"(v[1][1][1]));
    __builtin_amdgcn_sched_barrier(0);
    f32x4 su[2], o0[2], o1[2];
    s16x8 pp[2];
#pragma unroll
    for (int p = 0; p < 2; ++p) {
      const unsigned p0 = pack2(lg[p][0], lg[p][1]), p1 = pack2(lg[p][2], lg[p][3]);
      pp[p] = __builtin_bit_cast(s16x8, u32x4{p0 & ~pm[0], p1 & ~pm[1], p0 & pm[0], p1 & pm[1]});
    }
    __builtin_amdgcn_sched_barrier(0);
#pragma unroll
    for (int p = 0; p < 2; ++p) {
      const s16x8 pf = pp[p];
      const u32x4 v0 = {v[p][0][0][0], v[p][0][0][1], v[p][0][1][0], v[p][0][1][1]};
      const u32x4 v1 = {v[p][1][0][0], v[p][1][0][1], v[p][1][1][0], v[p][1][1][1]};
      su[p] = __builtin_amdgcn_mfma_f32_16x16x32_bf16(ones, pf, z, 0, 0, 0);
      o0[p] = __builtin_amdgcn_mfma_f32_16x16x32_bf16(__builtin_bit_cast(s16x8, v0), pf, z, 0, 0, 0);
      o1[p] = __builtin_amdgcn_mfma_f32_16x16x32_bf16(__builtin_bit_cast(s16x8, v1), pf, z, 0, 0, 0);
    }
    __builtin_amdgcn_sched_barrier(0);
    u32x2 y[2], yl;
    unsigned hm = (unsigned)hd - 1u;
    asm volatile("" : "+s"(hm));
#pragma unroll
    for (int p = 0; p < 2; ++p) {
      const float inv = __builtin_amdgcn_rcpf(su[p][0]);
      y[p] = u32x2{pack2(o0[p][0] * inv, o0[p][1] * inv), pack2(o0[p][2] * inv, o0[p][3] * inv)};
      const unsigned la = pack2(o1[p][0] * inv, o1[p][1] * inv), lb = pack2(o1[p][2] * inv, o1[p][3] * inv);
      yl[p] = (la & hm) | (lb & ~hm);
    }
    const auto s0 = __builtin_amdgcn_permlane16_swap(y[0][0], y[1][0], false, false);
    const auto s1 = __builtin_amdgcn_permlane16_swap(y[0][1], y[1][1], false, false);
    const u32x4 w = {s0[0], s1[0], s0[1], s1[1]};
    asm volatile("global_store_dwordx4 %0, %1, %4 sc1\n\ts_mov_b64 exec, 0xffff\n\tglobal_store_dwordx2 %2, %3, %4 sc1\n\ts_mov_b64 exec, -1\n\ts_nop 0" ::"v"(oa),
                 "v"(w), "v"(pa), "v"(yl), "s"(ob)
                 : "memory");
  }
};

#define NL_MAIN 664
DEVI void math_start(char* smem, const u16* b_sp, const u16* wqkv, char* ws, int bid, int wave, int lane) {
  const unsigned lds0 = (unsigned)(size_t)smem;
  const int tid = wave * 64 + lane, idx = bid >> 3;
  if (wave < 3) {
    static_assert(2 * SZ_ZP == (size_t)256 * 2048 && NCNT <= 16, "mark split");
    const u32x4 mk = {0xffffffffu, 0xffffffffu, 0xffffffffu, 0xffffffffu};
    char* c = ws + OFF_CNT + (size_t)((lane >> 3) * 256 + bid) * 128 + (lane & 7) * 16;
    pk_st16(wave < 2 ? ws + OFF_ZP + (size_t)((bid * 2 + wave) * 64 + lane) * 16 : c, mk);
    if (wave == 2 && lane < (NCNT - 8) * 8) pk_st16(c + 8 * 256 * 128, mk);
  }
  if (wave == 3) {
    const int li = idx * 54 + (lane < 54 ? lane : 53);
    touch64(uni_ptr((const char*)wqkv), (unsigned)(li * 1152 + 512), lds0 + (unsigned)(K1R_DUMMY + 768));
  }
  if (wave == 5 && idx == 31) touch64(uni_ptr((const char*)b_sp), (unsigned)lane * 64u, lds0 + (unsigned)(K1R_DUMMY + 768));
  const u32x4 ones = {0x3f803f80u, 0x3f803f80u, 0x3f803f80u, 0x3f803f80u};
  if (tid < 256) *reinterpret_cast<u32x4*>(smem + (tid >> 6) * K1R::SB + K1R::XB + (tid & 63) * 288 + 240) = ones;
  else if (tid < 288) *reinterpret_cast<u32x4*>(smem + ((tid - 256) >> 3) * K1R::SB + (tid & 7) * 16) = ones;
  else if (tid < 296) *reinterpret_cast<u32x4*>(smem + LW_OFF + (tid - 288) * 16) = u32x4{0u, 0u, 0u, 0u};
  asm volatile("s_waitcnt lgkmcnt(0)" ::: "memory");
}

template <int NST, int NS, int NSW, int WOFF, int XW, int X0, int S0, unsigned BAR>
struct ResX {
  static constexpr int WS = 4 * 4608, R = 10, V1 = 5 * NS, NV = V1 + 10 * (NST - NS), NG = (NSW + 2) / 3;
  s16x8 xf[XW][2];
  s16x8 wf[R];
  unsigned wrd[2][NG], xo, ea, fa, fv, tag;
  const char* xp[3];
  f32x4 (&acc)[2][5];
  DEVI ResX(f32x4 (&a)[2][5]) : acc(a) {}
  static constexpr int st(int V) { return V < V1 ? V / 5 : NS + (V - V1) / 10; }
  static constexpr int hf(int V) { return V < V1 ? 0 : ((V - V1) / 5) & 1; }
  static constexpr bool first(int V) { return V < V1 ? V % 5 == 0 : (V - V1) % 10 == 0; }
  static constexpr int wreq(int V) {
    int n = V + 5;
    if (V >= V1 - 4) n += V < V1 ? V - (V1 - 5) : 4;
    return n < NV ? n : NV;
  }
  static constexpr int XU = V1 + 1, XF = V1 + 11, XR = V1 + 15, XC = V1 + 21;
  static constexpr int oth(int V) { return NS && ((V >= XU && V <= XF && (V - XU) % 2 == 0) || V == XR || (V > XC && V <= XC + 5)) ? 1 : 0; }
  static constexpr int newer(int V) {
    const int F = V + 1 < NV ? V + 1 : V;
    int v = V - 1, n = 0;
    for (; v >= 0 && wreq(v - 1) > F; --v) n += wreq(v) - wreq(v - 1) + oth(v);
    return n + wreq(v) - 1 - F + (v >= 0 ? oth(v) : 0);
  }
  static constexpr int xreq(int T) {
    int n = X0 + 2 * (T + 1);
    if (n > T + XW) n = T + XW;
    if (NS && T <= 11 && n > 18) n = 18;
    if (T < 0) n = X0;
    return n < NST ? n : NST;
  }
  static_assert(NS == 0 || (xreq(NS - 2) == NST && XC + 5 < NV), "the last steps only compute and swap");
  template <int T, int M>
  DEVI void xld() {
    asm volatile("global_load_dwordx4 %0, %1, %2 sc1" : "=v"(xf[T % XW][M]) : "v"(xo), "s"(xp[M == 0 ? 0 : (T < NS ? 1 : 2)]));
    if constexpr (M == 1 && T + 1 < NST) asm volatile("v_add_u32 %0, 0x2000, %0" : "+v"(xo));
  }
  template <int V>
  DEVI void rd() {
    constexpr int T = st(V), sl = (T / 2 + S0) % NSW;
    asm volatile("ds_read_b128 %0, %1 offset:%2" : "=v"(wf[V % R]) : "v"(wrd[hf(V)][sl / 3]), "n"((sl % 3) * WS + (T & 1) * 9216 + (V % 5) * 32));
  }
  template <int V, int M, int H>
  DEVI void mf() {
    acc[M][V % 5] = __builtin_amdgcn_mfma_f32_16x16x32_bf16(wf[V % R], xf[st(V) % XW][H], acc[M][V % 5], 0, 0, 0);
    __builtin_amdgcn_sched_barrier(0);
  }
  template <int V>
  DEVI void unit() {
    constexpr int T = st(V), n = V % 5, nx = xreq(T) - xreq(T - 1), nw = wreq(V) - wreq(V - 1);
    constexpr int vm = 2 * (xreq(T - 1) - 1 - T);
    static_assert(newer(V) <= 15, "lgkmcnt range");
    if constexpr (first(V) && V % 2 == 0) asm volatile("s_waitcnt vmcnt(%0) lgkmcnt(%1)" ::"n"(vm), "n"(newer(V)));
    else if constexpr (first(V)) asm volatile("s_waitcnt vmcnt(%0)" ::"n"(vm));
    else if constexpr (V % 2 == 0) asm volatile("s_waitcnt lgkmcnt(%0)" ::"n"(newer(V)));
    __builtin_amdgcn_sched_barrier(0);
    if constexpr (V < V1) mf<V, V & 1, V & 1>();
    else mf<V, 0, hf(V)>();
    if constexpr (nw > 0) rd<wreq(V - 1)>();
    __builtin_amdgcn_sched_barrier(0);
    if constexpr (V < V1) {
      mf<V, 1 - (V & 1), 1 - (V & 1)>();
      if constexpr (nw > 1) rd<wreq(V - 1) + 1>();
      if constexpr (nx == 2 && n < 4) xld<xreq(T - 1) + n / 2, n % 2>();
      if constexpr (nx == 1 && n == (T & 1)) xld<xreq(T - 1), 0>();
      if constexpr (nx == 1 && n == (T & 1) + 2) xld<xreq(T - 1), 1>();
      if constexpr (n == 4 && (T & 1) && ((BAR >> (T / 2)) & 1u)) bar();
    } else if constexpr (NS == 0) {
      constexpr int k = (V % 10) / 2;
      if constexpr (V % 2 == 0 && k < 2 * nx) xld<xreq(T - 1) + k / 2, k % 2>();
      if constexpr (V % 10 == 8 && nx > 0 && xreq(T) == NST)
        asm volatile("s_mov_b64 exec, 1\n\tds_add_u32 %0, %1\n\ts_mov_b64 exec, -1" ::"v"(fa), "v"(fv));
    } else {
      if constexpr (oth(V) && V < XF) asm volatile("ds_write_b128 %0, %1 offset:%2" ::"v"(ea), "v"(acc[1][(V - XU) / 2]), "n"((V - XU) * 512));
      if constexpr (V == XF) asm volatile("ds_write_b32 %0, %1" ::"v"(fa), "v"(fv));
      if constexpr (V == XF + 2) asm volatile("v_xor_b32 %0, 4, %0" : "+v"(fa));
      if constexpr (V == XR) asm volatile("ds_read_b32 %0, %1" : "+v"(fv) : "v"(fa));
      if constexpr (V == XR + 2) asm volatile("v_xor_b32 %0, 0x2000, %0" : "+v"(ea));
      if constexpr (V == XC) {
        unsigned c = 1u << 18;
        asm volatile(
            "s_waitcnt lgkmcnt(%4)\n\tv_cmp_ne_u32 vcc, %3, %0\n\ts_cbranch_vccnz .Lw%=\n\t.subsection 1\n.Lw%=:\n\tds_read_b32 %0, %2\n\t"
            "s_waitcnt lgkmcnt(0)\n\tv_cmp_ne_u32 vcc, %3, %0\n\t" SAFE_OUT(".Lr%=") "s_cbranch_vccnz .Lw%=\n\ts_branch .Lr%=\n\t.subsection 0\n.Lr%=:"
            : "+v"(fv), NOP_(c)
            : "v"(fa), "s"(tag), "n"(wreq(XC) - wreq(XR))
            : "vcc", "scc");
      }
      if constexpr (oth(V) && V > XC) asm volatile("ds_read_b128 %0, %1 offset:%2" : "=v"(acc[1][V - XC - 1]) : "v"(ea), "n"((V - XC - 1) * 1024));
    }
    __builtin_amdgcn_sched_barrier(0);
  }
  template <int... V>
  DEVI void units(std::integer_sequence<int, V...>) {
    (unit<V>(), ...);
  }
  DEVI void run() {
    units(std::make_integer_sequence<int, NV>{});
    if constexpr (NS > 0) {
      asm volatile("s_waitcnt lgkmcnt(0)" : "+v"(acc[1][0]), "+v"(acc[1][1]), "+v"(acc[1][2]), "+v"(acc[1][3]), "+v"(acc[1][4]));
#pragma unroll
      for (int i = 0; i < 5; ++i) acc[0][i] += acc[1][i];
    }
  }
  template <int... V>
  DEVI void pro(std::integer_sequence<int, V...>) {
    (rd<V>(), ...);
  }
  DEVI void prow() { pro(std::make_integer_sequence<int, wreq(-1)>{}); }
  template <int T0, int... T>
  DEVI void prox(std::integer_sequence<int, T...>) {
    ((xld<T0 + T, 0>(), xld<T0 + T, 1>()), ...);
  }
  DEVI void setup(unsigned lds0, const char* xbase, int wave, int lane, unsigned red, unsigned nth) {
    const int g = lane >> 4, r = lane & 15, kq = wave & 1;
    xp[0] = xbase + wave * 256;
    xp[1] = xbase + (wave ^ 1) * 256;
    xp[2] = xp[0] + (kq ? -4096 : 4096);
    xo = (unsigned)((4 * kq + g) * 1024 + r * 16);
#pragma unroll
    for (int i = 0; i < 2 * NG; ++i) {
      wrd[i / NG][i % NG] = lds0 + (unsigned)(WOFF + (i % NG) * 3 * WS + (kq ^ (i / NG)) * 4608 + (2 * (r & 7) + 16 * (g & 1) + (g >> 1)) * 144 + (r >> 3) * 16);
      if (i < NG || NS == 0 || i % NG == ((NS / 2 + S0) % NSW) / 3) asm volatile("" : "+v"(wrd[i / NG][i % NG]));
    }
    asm volatile("" : "+v"(xo));
    if constexpr (NS > 0) {
      ea = lds0 + red + (unsigned)((wave ^ 1) * 8192 + lane * 16);
      fa = lds0 + (unsigned)(LW_OFF + (LW_PAIR + wave) * 4);
      fv = tag = nth;
      asm volatile("" : "+v"(ea), "+v"(fa), "+v"(fv));
    }
  }
  template <int N>
  DEVI void init(u32x2 (&xv)[5]) {
    asm volatile("s_waitcnt vmcnt(%5)" : "+v"(xv[0]), "+v"(xv[1]), "+v"(xv[2]), "+v"(xv[3]), "+v"(xv[4]) : "n"(N));
#pragma unroll
    for (int i = 0; i < 5; ++i) {
      acc[0][i] = f32x4{bf_lo(xv[i][0]), bf_hi(xv[i][0]), bf_lo(xv[i][1]), bf_hi(xv[i][1])};
      asm volatile("" : "+v"(acc[0][i]));
      acc[1][i] = f32x4{0.f, 0.f, 0.f, 0.f};
    }
  }
};

constexpr int P5_XW = 8, P5_X0 = 5, P2_X0 = 4;
constexpr unsigned P5_BAR = 0x2aaau;
constexpr int P5_W = 15 * 1024;
constexpr int P2_W = K1_ST_OFF - 18 * 4608;
constexpr int P5_RED = 0;
static_assert(P5_RED % 16384 == 0 && P5_RED + 3 * 8192 + 5120 <= P5_W + 18432, "exchange area");
static_assert(P2_W >= K1_T_OFF + 144 * K1_TP + 64 && P2_W % 16 == 0 && P5_W + 8 * 18432 <= LW_OFF, "LDS placement");

struct ResIn {
  const char* xp;
  unsigned o, o2, st;
  DEVI void setup(const char* src, bool rm, int rb, int ct, int wave, int lane) {
    const int g = lane >> 4, g2 = g & 1, rl = wave * 16 + (lane & 15);
    xp = uni_ptr(rm ? src + ((size_t)rb * 64 * D_ + ct * 72) * 2 : src + (size_t)(rb * 72 + ct * 9) * 1024);
    o = rm ? (unsigned)(rl * (D_ * 2) + g * 8) : (unsigned)((g >> 1) * 1024 + rl * 16 + g2 * 8);
    o2 = rm ? (unsigned)(rl * (D_ * 2) + g2 * 8 + 128) : (unsigned)(8192 + rl * 16 + g2 * 8);
    st = rm ? 32u : 2048u;
    asm volatile("" : "+v"(o), "+v"(o2));
    asm volatile("" : "+s"(xp));
  }
  DEVI void load(u32x2 (&xv)[5]) {
#pragma unroll
    for (int i = 0; i < 4; ++i) asm volatile("global_load_dwordx2 %0, %1, %2 sc1" : "=v"(xv[i]) : "v"(o), "s"(xp + i * st));
    asm volatile("global_load_dwordx2 %0, %1, %2 sc1" : "=v"(xv[4]) : "v"(o2), "s"(xp));
  }
};

DEVI void p2_gemm(f32x4 (&acc)[2][5], u32x2 (&xv)[5], unsigned lds0, const char* ob, int layer, int wave, int lane MK_ARG) {
  ResX<9, 0, 5, P2_W, 9, P2_X0, 0, 0u> gm(acc);
  gm.setup(lds0, ob, wave, lane, 0u, 0u);
  unsigned a = (gm.xo & ~1023u) | ((gm.xo & 1023u) >> 2);
  gm.xo += 8192u;
  gm.fa = lds0 + (unsigned)(LW_OFF + (LW_T + layer) * 4);
  gm.fv = vconst(1u);
  asm volatile("" : "+v"(a), "+v"(gm.xo), "+v"(gm.fa));
  const char* pp[2] = {gm.xp[0] - wave * 192, gm.xp[2] - wave * 192};
  place();
  bar();
  MK(6);
  unsigned e[2][4];
#pragma unroll
  for (int d = 0; d < 8; ++d)
    asm volatile("global_load_dword %0, %1, %2 offset:%3 sc1" : "=v"(e[d / 4][d % 4]) : "v"(a), "s"(pp[d / 4]), "n"((d % 4) * 256));
  gm.template prox<1>(std::make_integer_sequence<int, P2_X0 - 1>{});
  gm.prow();
  gm.template init<6 + 2 * P2_X0>(xv);
  asm volatile("s_waitcnt vmcnt(%8)"
               : "+v"(e[0][0]), "+v"(e[0][1]), "+v"(e[0][2]), "+v"(e[0][3]), "+v"(e[1][0]), "+v"(e[1][1]), "+v"(e[1][2]), "+v"(e[1][3])
               : "n"(2 * P2_X0 - 2));
  gm.xf[0][0] = __builtin_bit_cast(s16x8, u32x4{e[0][0], e[0][1], e[0][2], e[0][3]});
  gm.xf[0][1] = __builtin_bit_cast(s16x8, u32x4{e[1][0], e[1][1], e[1][2], e[1][3]});
  __builtin_amdgcn_sched_barrier(0);
  gm.run();
}

struct ResOut {
  const char *xp[3], *sp;
  unsigned xo, so;
  DEVI void setup(char* xim, char* xs, u16* out, bool fin, int rb, int ct, int wave, int lane) {
    const int g = lane >> 4, rl = wave * 16 + (lane & 15);
    xp[0] = uni_ptr(fin ? (const char*)(out + (size_t)rb * 64 * D_ + ct * 72) : xim + (size_t)(rb * 72 + ct * 9) * 1024);
    xp[1] = xp[0] + (fin ? 64 : 4096);
    xp[2] = xp[0] + (fin ? 128 : 8192);
    sp = uni_ptr(xs + (size_t)(rb * 8 + ct) * 512);
    xo = (unsigned)(((g & 1) * 2 + (g >> 1)) * (fin ? 16 : 1024) + rl * (fin ? D_ * 2 : 16));
    so = (unsigned)(rl * 8);
    asm volatile("" : "+v"(xo), "+v"(so), "+s"(xp[0]), "+s"(xp[1]), "+s"(xp[2]), "+s"(sp));
  }
  DEVI void run(const f32x4 (&a)[5], bool fin) {
    u32x2 y[5];
    u32x4 v[3];
#pragma unroll
    for (int nt = 0; nt < 5; ++nt) y[nt] = u32x2{pack2(a[nt][0], a[nt][1]), pack2(a[nt][2], a[nt][3])};
#pragma unroll
    for (int i = 0; i < 3; ++i) {
      const auto s0 = __builtin_amdgcn_permlane16_swap(y[2 * i][0], y[i < 2 ? 2 * i + 1 : 4][0], false, false);
      const auto s1 = __builtin_amdgcn_permlane16_swap(y[2 * i][1], y[i < 2 ? 2 * i + 1 : 4][1], false, false);
      v[i] = u32x4{s0[0], s1[0], s0[1], s1[1]};
    }
    if (fin) {
      asm volatile("global_store_dwordx4 %0, %1, %4\n\tglobal_store_dwordx4 %0, %2, %5\n\ts_mov_b64 exec, 0xffff\n\tglobal_store_dwordx4 %0, %3, %6\n\t"
                   "s_mov_b64 exec, -1\n\ts_nop 0" ::"v"(xo), "v"(v[0]), "v"(v[1]), "v"(v[2]), "s"(xp[0]), "s"(xp[1]), "s"(xp[2])
                   : "memory");
      return;
    }
    asm volatile("global_store_dwordx4 %0, %1, %4 sc1\n\tglobal_store_dwordx4 %0, %2, %5 sc1\n\ts_mov_b64 exec, 0xffff\n\t"
                 "global_store_dwordx4 %0, %3, %6 sc1\n\ts_mov_b64 exec, -1\n\ts_nop 0" ::"v"(xo), "v"(v[0]), "v"(v[1]), "v"(v[2]),
                 "s"(xp[0]), "s"(xp[1]), "s"(xp[2])
                 : "memory");
    const u32x4 one4 = {vconst(0x3f803f80u), vconst(0x3f803f80u), vconst(0x3f803f80u), vconst(0x3f803f80u)};
    const bool lo = __builtin_amdgcn_inverse_ballot_w64(0xffffull);
    v[2] = u32x4{lo ? v[2][0] : 0u, lo ? v[2][1] : 0u, lo ? v[2][2] : 0u, lo ? v[2][3] : 0u};
    f32x4 s = {0.f, 0.f, 0.f, 0.f}, q = s;
#pragma unroll
    for (int i = 0; i < 3; ++i) {
      const s16x8 u = __builtin_bit_cast(s16x8, v[i]);
      q = __builtin_amdgcn_mfma_f32_16x16x32_bf16(u, u, q, 0, 0, 0);
      s = __builtin_amdgcn_mfma_f32_16x16x32_bf16(__builtin_bit_cast(s16x8, one4), u, s, 0, 0, 0);
    }
    float q0 = q[0], q1 = q[1], q2 = q[2], q3 = q[3];
    const bool b0 = __builtin_amdgcn_inverse_ballot_w64(0xaaaaaaaaaaaaaaaaull), b1 = __builtin_amdgcn_inverse_ballot_w64(0xccccccccccccccccull);
    q0 = b0 ? q1 : q0;
    q2 = b0 ? q3 : q2;
    asm("" : "+v"(q0), "+v"(q2));
    const f32x2 sq = {s[0], b1 ? q2 : q0};
    asm volatile("s_mov_b64 exec, %3\n\tglobal_store_dwordx2 %0, %1, %2 sc1\n\ts_mov_b64 exec, -1" ::"v"(so), "v"(sq), "s"(sp), "s"(0xf0000f0000f0000full) : "memory");
  }
};

using K3C = GemmCfg<128, 144>;
constexpr int K3_Z1_OFF = K3C::LDS_BYTES;
constexpr int K3_C2_OFF = K3_Z1_OFF + K3C::XB;
constexpr int K3_ZC_OFF = K3_C2_OFF + 2048;
constexpr int K3_DM_OFF = K3_ZC_OFF + 1024;
constexpr int K3_ST_OFF = K3_Z1_OFF + 4 * 5120;
constexpr int K3_C1_OFF = K3_ST_OFF + 2048;
constexpr int K3_BS_OFF = K3_C1_OFF + 576;
static_assert(K3_BS_OFF + 512 <= LW_OFF, "K3 LDS");
static_assert(P5_W + 19 * 1024 == K3C::SB && K3C::SB == 34 * 1024, "w_out ring");

DEVI void red4(float (&t)[4], float& s, float& q) {
#pragma unroll
  for (int j = 0; j < 2; ++j) {
    const auto p = __builtin_amdgcn_permlane16_swap(__float_as_uint(t[j]), __float_as_uint(t[2 + j]), false, false);
    const unsigned v = __float_as_uint(__uint_as_float(p[0]) + __uint_as_float(p[1]));
    const auto w = __builtin_amdgcn_permlane32_swap(v, v, false, false);
    t[j] = __uint_as_float(w[0]) + __uint_as_float(w[1]);
    asm volatile("" : "+v"(t[j]));
  }
  s = t[0];
  q = t[1];
}
DEVI void red8(const f32x4& a, const f32x4& b, float& s, float& q) {
  float t[4];
#pragma unroll
  for (int j = 0; j < 4; ++j) {
    t[j] = a[j] + b[j];
    asm volatile("" : "+v"(t[j]));
  }
  red4(t, s, q);
}
DEVI void red16(const f32x4& a, const f32x4& b, const f32x4& c, const f32x4& d, float& s, float& q) {
  float t[4];
#pragma unroll
  for (int j = 0; j < 4; ++j) {
    float u = a[j] + b[j], v = c[j] + d[j];
    asm volatile("" : "+v"(u), "+v"(v));
    t[j] = u + v;
    asm volatile("" : "+v"(t[j]));
  }
  red4(t, s, q);
}

constexpr int b_k(int s) { return s < 2 ? s : (s < 4 ? 5 + s : (s < 6 ? 1 + s : (s == 6 ? 4 : s - 5))); }
template <bool B>
struct UMath {
  static constexpr int D = 4, R = 6, NV = 162, SB = K3C::SB, P = K3C::P;
  s16x8 wf[R], xf[2][2];
  s16x8 (&xc)[4][2][2];
  f32x4 (&acc)[2][9];
  f32x4 rc[4][2];
  f32x2 cs[9];
  unsigned ra, ca;
  template <int I>
  DEVI void cread() {
    if constexpr (I < 8) asm volatile("ds_read_b128 %0, %1 offset:%2" : "=v"(rc[I / 2][I % 2]) : "v"(ra), "n"((I / 2) * 512 + (I % 2) * 16));
    else if constexpr (I < 17) asm volatile("ds_read_b64 %0, %1 offset:%2" : "=v"(cs[I - 8]) : "v"(ca), "n"((I - 8) * 128));
  }
  unsigned xa, wa;
  unsigned fa, fw, fv;
  DEVI UMath(f32x4 (&a)[2][9], s16x8 (&x)[4][2][2]) : xc(x), acc(a) {}
  static constexpr int wslot(int s) { return B ? (s + 1) % 4 : s % 4; }
  static constexpr int xslot(int s) { return B ? b_k(s) % 5 : s % 5; }
  static constexpr bool res(int q) { return !B || b_k(q / 2) >= 4; }
  static constexpr bool chk(int s) { return s >= 3 && s < 7; }
  static constexpr bool xop(int v) { return v + D < NV && (v + D) % 9 < 2 && res((v + D) / 9); }
  static constexpr int ops1(int v) { return (xop(v) || (B && ((v % 18 == 9 && chk(v / 18)) || (v % 18 == 0 && v > 0)))) ? 1 : 0; }
  static constexpr int newer(int V) {
    int v0 = (V % 9 == 8 ? V : V + 1) - D, n = 0;
    if (v0 >= 0 && !(V % 9 == 0 && res(V / 9))) n += ops1(v0);
    for (int v = v0 + 1; v < V; ++v)
      if (v >= 0) n += (v + D < NV ? 1 : (B ? 0 : 4)) + ops1(v);
    return n;
  }
  template <int V>
  DEVI void wread() {
    asm volatile("ds_read_b128 %0, %1 offset:%2" : "=v"(wf[V % R]) : "v"(wa), "n"((wslot(V / 18) & 1) * SB + ((V % 18) / 9) * 32 * P + (V % 9) * 32));
  }
  template <int Q, int M>
  DEVI void xread() {
    asm volatile("ds_read_b128 %0, %1 offset:%2" : "=v"(xf[Q & 1][M]) : "v"(xa), "n"((xslot(Q / 2) & 1) * SB + (Q & 1) * 8192 + M * (B ? 256 : 64)));
  }
  template <int V, int M_>
  DEVI void mf() {
    constexpr int M = ((V % 9) & 1) ? 1 - M_ : M_, q = V / 9;
    if constexpr (!B) acc[M][V % 9] = __builtin_amdgcn_mfma_f32_16x16x32_bf16(xf[q & 1][M], wf[V % R], acc[M][V % 9], 0, 0, 0);
    else if constexpr (res(q)) acc[M][V % 9] = __builtin_amdgcn_mfma_f32_16x16x32_bf16(wf[V % R], xf[q & 1][M], acc[M][V % 9], 0, 0, 0);
    else acc[M][V % 9] = __builtin_amdgcn_mfma_f32_16x16x32_bf16(wf[V % R], xc[b_k(q / 2)][q & 1][M], acc[M][V % 9], 0, 0, 0);
    __builtin_amdgcn_sched_barrier(0);
  }
  template <int V>
  DEVI void unit() {
    constexpr int U = V % 18, s = V / 18;
    static_assert(newer(V) <= 15, "lgkmcnt range");
    if constexpr ((V % 9) % 2 == 0 && V > 0) asm volatile("s_waitcnt lgkmcnt(%0)" ::"n"(newer(V)));
    __builtin_amdgcn_sched_barrier(0);
    mf<V, 0>();
    if constexpr (V + D < NV) wread<V + D>();
    if constexpr (!B && V + D >= NV) {
      cread<(V + D - NV) * 4>();
      cread<(V + D - NV) * 4 + 1>();
    }
    if constexpr (s < 8 && U == 13 && (wslot(s + 1) >> 1) != (wslot(s) >> 1))
      asm volatile("v_add_u32 %0, %1, %0" : "+v"(wa) : "n"(((wslot(s + 1) >> 1) - (wslot(s) >> 1)) * 2 * SB));
    __builtin_amdgcn_sched_barrier(0);
    mf<V, 1>();
    if constexpr (xop(V)) xread<(V + D) / 9, (V + D) % 9>();
    if constexpr (!B && V + D >= NV) {
      cread<(V + D - NV) * 4 + 2>();
      cread<(V + D - NV) * 4 + 3>();
      if constexpr (V == NV - 1) cread<16>();
    }
    if constexpr (s < 8) {
      if constexpr (U == 11 && res(2 * s + 2) && (xslot(s + 1) >> 1) != (xslot(s) >> 1))
        asm volatile("v_add_u32 %0, %1, %0" : "+v"(xa) : "n"(((xslot(s + 1) >> 1) - (xslot(s) >> 1)) * 2 * SB));
      if constexpr (!B && U == 13) __builtin_amdgcn_s_barrier();
      if constexpr (B && U == 9 && chk(s)) asm volatile("ds_read_u8 %0, %1 offset:%2" : "+v"(fv) : "v"(fa), "n"(s == 6 ? 0 : 8));
      unsigned n = 1u << 18;
      if constexpr (B && U == 13 && chk(s)) {
        asm volatile(
            "v_cmp_gt_u32 vcc, %3, %0\n\ts_cbranch_vccnz .Lw%=\n\t.subsection 1\n.Lw%=:\n\tds_read_u8 %0, %2 offset:%4\n\ts_waitcnt lgkmcnt(0)\n\t"
            "v_cmp_gt_u32 vcc, %3, %0\n\t" SAFE_OUT(".Lr%=") "s_cbranch_vccnz .Lw%=\n\ts_branch .Lr%=\n\t.subsection 0\n.Lr%=:"
            : "+v"(fv), NOP_(n)
            : "v"(fa), "n"(s == 6 ? 4 : s + 2), "n"(s == 6 ? 0 : 8)
            : "vcc", "scc");
      }
    }
    if constexpr (B && U == 15) asm volatile("v_mov_b32 %0, %1" : "+v"(fv) : "n"(s + 1));
    if constexpr (B && ((U == 0 && s > 0) || V == NV - 1)) asm volatile("ds_write_b8 %0, %1" ::"v"(fw), "v"(fv));
    __builtin_amdgcn_sched_barrier(0);
  }
  template <int... V>
  DEVI void units(std::integer_sequence<int, V...>) {
    (unit<V>(), ...);
  }
  DEVI void first() {
    if constexpr (!B) {
      xread<0, 0>();
      xread<0, 1>();
    }
    wread<0>();
    wread<1>();
    wread<2>();
    wread<3>();
    asm volatile("s_waitcnt lgkmcnt(0)");
  }
  DEVI void run() {
    units(std::make_integer_sequence<int, NV>{});
#pragma unroll
    for (int m = 0; m < 2; ++m)
#pragma unroll
      for (int n = 0; n < 9; ++n) asm volatile("" : "+v"(acc[m][n]));
  }
};

struct CMath {
  static constexpr int D = 4, R = 6, SB = K3C::SB;
  s16x8 wf[R], xf[2][2];
  f32x4 (&acc)[2][9];
  unsigned wa[2], xa[2][2];
  DEVI CMath(f32x4 (&a)[2][9]) : acc(a) {}
  static constexpr int xop(int u, bool last) { return (u + D) % 9 < 2 && !(last && u + D >= 18) ? 1 : 0; }
  static constexpr int newer(int U, bool last) {
    int v0 = (U % 9 == 8 ? U : U + 1) - D, n = 0;
    if (U % 9 != 0) n += xop((v0 + 18) % 18, last && v0 >= 0);
    for (int v = v0 + 1; v < U; ++v) n += (last && v + D >= 18 ? 0 : 1) + xop((v + 18) % 18, last && v >= 0);
    return n;
  }
  template <int SL, int U>
  DEVI void wread() {
    asm volatile("ds_read_b128 %0, %1 offset:%2" : "=v"(wf[U % R]) : "v"(wa[SL / 2]), "n"((SL & 1) * SB + U * 1024));
  }
  template <int SL, int H, int M>
  DEVI void xread() {
    asm volatile("ds_read_b128 %0, %1 offset:%2" : "=v"(xf[H][M]) : "v"(xa[SL / 2][H]), "n"((SL & 1) * SB + M * 2048));
  }
  template <int U, int M_>
  DEVI void mf() {
    constexpr int M = ((U % 9) & 1) ? 1 - M_ : M_;
    acc[M][U % 9] = __builtin_amdgcn_mfma_f32_16x16x32_bf16(wf[U % R], xf[U / 9][M], acc[M][U % 9], 0, 0, 0);
    __builtin_amdgcn_sched_barrier(0);
  }
  template <int SL, bool BAR, bool LAST, int U>
  DEVI void unit() {
    constexpr int PU = (U + D) % 18, PS = U + D < 18 ? SL : (SL + 1) % 4;
    if constexpr ((U % 9) % 2 == 0) asm volatile("s_waitcnt lgkmcnt(%0)" ::"n"(newer(U, LAST)));
    __builtin_amdgcn_sched_barrier(0);
    mf<U, 0>();
    if constexpr (!(LAST && U + D >= 18)) wread<PS, PU>();
    __builtin_amdgcn_sched_barrier(0);
    mf<U, 1>();
    if constexpr (xop(U, LAST)) xread<PS, PU / 9, PU % 9>();
    if constexpr (BAR && U == 13) __builtin_amdgcn_s_barrier();
    __builtin_amdgcn_sched_barrier(0);
  }
  template <int SL, bool BAR, bool LAST, int... U>
  DEVI void units(std::integer_sequence<int, U...>) {
    (unit<SL, BAR, LAST, U>(), ...);
  }
  template <int SL, bool BAR = true, bool LAST = false>
  DEVI void stage() {
    units<SL, BAR, LAST>(std::make_integer_sequence<int, 18>{});
  }
  DEVI void setup(unsigned lds0, int wm, int lane) {
    const int g = lane >> 4, r = lane & 15;
#pragma unroll
    for (int i = 0; i < 2; ++i) {
      wa[i] = lds0 + (unsigned)(i * 2 * SB + K3C::XB + lane * 16);
#pragma unroll
      for (int h = 0; h < 2; ++h) xa[i][h] = lds0 + (unsigned)(i * 2 * SB + (wm * 32 + r) * 128 + (((4 * h + g) ^ ((r >> 1) & 7)) * 16));
    }
    asm volatile("" : "+v"(wa[0]), "+v"(wa[1]), "+v"(xa[0][0]), "+v"(xa[0][1]), "+v"(xa[1][0]), "+v"(xa[1][1]));
  }
  DEVI void first() {
    xread<2, 0, 0>();
    xread<2, 0, 1>();
    wread<2, 0>();
    wread<2, 1>();
    wread<2, 2>();
    wread<2, 3>();
    asm volatile("s_waitcnt lgkmcnt(0)");
  }
};

constexpr int NR1 = 3;
DEVI void p3_g0(char* smem, char* __restrict__ ws, int layer, int mi, int ni, int wm, int lane MK_ARG) {
  constexpr int SB = K3C::SB;
  const unsigned lds0 = (unsigned)(size_t)smem;
  const int g = lane >> 4, r = lane & 15;
  const unsigned lw = lds0 + (unsigned)(LW_OFF + layer * 4), me = 1u << (8 * wm);
  f32x4 ac[2][9];
#pragma unroll
  for (int mt = 0; mt < 2; ++mt)
#pragma unroll
    for (int j = 0; j < 9; ++j) ac[mt][j] = f32x4{0.f, 0.f, 0.f, 0.f};
  unsigned ra = lds0 + (unsigned)(K3_ST_OFF + (wm * 32 + g * 8) * 4), ca = lds0 + (unsigned)(K3_C2_OFF + r * 8);
  asm volatile("" : "+v"(ra), "+v"(ca));
  s16x8 xc[4][2][2];
  UMath<false> gm(ac, xc);
  gm.ra = ra;
  gm.ca = ca;
  gm.xa = lds0 + (unsigned)(g * 2048 + (wm * 32 + 8 * (r >> 2) + (r & 3)) * 16);
  gm.wa = lds0 + (unsigned)(K3C::XB + (g * 8 + (r & 7)) * K3C::P + (r >> 3) * 16);
  unsigned adw = lw + LW_AD * 4, mev = vconst(me), po = (unsigned)(ni * 1024 + lane * 4);
  const char* zp = uni_ptr(ws + OFF_ZP + (size_t)layer * SZ_ZP + (size_t)mi * 16384 + wm * 256);
  asm volatile("" : "+v"(gm.xa), "+v"(gm.wa), "+v"(adw), "+v"(po));
  asm volatile("" : "+s"(zp));
  place();
  bar();
  gm.first();
  gm.run();
  asm volatile("s_mov_b64 exec, 1\n\tds_add_u32 %0, %1\n\ts_mov_b64 exec, -1" ::"v"(adw), "v"(mev) : "memory");
  MK(11);
  f32x2 z[9][4];
  f32x2 sm[4], sq[4];
  {
    f32x4 (&rc)[4][2] = gm.rc;
    f32x2 (&cs)[9] = gm.cs;
    asm volatile("s_waitcnt lgkmcnt(1)"
                 : "+v"(rc[0][0]), "+v"(rc[0][1]), "+v"(rc[1][0]), "+v"(rc[1][1]), "+v"(rc[2][0]), "+v"(rc[2][1]), "+v"(rc[3][0]), "+v"(rc[3][1]));
#pragma unroll
    for (int j = 0; j < 9; ++j) asm volatile("" : "+v"(cs[j]));
    auto RC = [&](int k, int i) { return f32x2{rc[k][i / 2][(i % 2) * 2], rc[k][i / 2][(i % 2) * 2 + 1]}; };
#pragma unroll
    for (int j = 0; j < 9; ++j) {
      f32x2 u[4], w[4];
#pragma unroll
      for (int i = 0; i < 4; ++i) u[i] = __builtin_elementwise_fma(RC(0, i), cs[j], f32x2{ac[i / 2][j][(i % 2) * 2], ac[i / 2][j][(i % 2) * 2 + 1]});
#pragma unroll
      for (int i = 0; i < 4; ++i) w[i] = u[i] * u[i];
#pragma unroll
      for (int i = 0; i < 4; ++i) w[i] = __builtin_elementwise_fma(w[i], RC(3, i), RC(2, i));
#pragma unroll
      for (int i = 0; i < 4; ++i) w[i] = u[i] * w[i];
      __builtin_amdgcn_sched_barrier(0);
#pragma unroll
      for (int i = 0; i < 4; ++i) w[i] = f32x2{__builtin_amdgcn_exp2f(w[i][0]), __builtin_amdgcn_exp2f(w[i][1])};
      __builtin_amdgcn_sched_barrier(0);
#pragma unroll
      for (int i = 0; i < 4; ++i) w[i] = __builtin_elementwise_fma(w[i], RC(1, i), RC(1, i));
      __builtin_amdgcn_sched_barrier(0);
#pragma unroll
      for (int i = 0; i < 4; ++i) w[i] = f32x2{__builtin_amdgcn_rcpf(w[i][0]), __builtin_amdgcn_rcpf(w[i][1])};
      __builtin_amdgcn_sched_barrier(0);
#pragma unroll
      for (int i = 0; i < 4; ++i) z[j][i] = u[i] * w[i];
#pragma unroll
      for (int i = 0; i < 4; ++i) {
        if (j == 1) sm[i] = z[0][i] + z[1][i];
        if (j > 1) sm[i] += z[j][i];
      }
#pragma unroll
      for (int i = 0; i < 4; ++i) sq[i] = j ? __builtin_elementwise_fma(z[j][i], z[j][i], sq[i]) : z[j][i] * z[j][i];
      asm volatile("" : "+v"(z[j][0]), "+v"(z[j][1]), "+v"(z[j][2]), "+v"(z[j][3]));
      __builtin_amdgcn_sched_barrier(0);
    }
  }
  f32x4 e;
  {
    float v[4][4], d[2][4], f[4];
#pragma unroll
    for (int i = 0; i < 4; ++i) {
      v[i][0] = sm[i][0];
      v[i][1] = sq[i][0];
      v[i][2] = sm[i][1];
      v[i][3] = sq[i][1];
    }
    asm volatile("s_nop 1" : "+v"(v[3][1]), "+v"(v[3][3]), "+v"(v[2][1]), "+v"(v[2][3]));
#pragma unroll
    for (int i = 0; i < 2; ++i)
#pragma unroll
      for (int c = 0; c < 4; ++c)
        asm volatile("v_add_f32_dpp %0, %1, %1 row_ror:8 row_mask:0xf bank_mask:0x3\n\tv_add_f32_dpp %0, %2, %2 row_ror:8 row_mask:0xf bank_mask:0xc"
                     : "=&v"(d[i][c])
                     : "v"(v[i][c]), "v"(v[i + 2][c]));
#pragma unroll
    for (int c = 0; c < 4; ++c)
      asm volatile("v_add_f32_dpp %0, %1, %1 row_shl:4 row_mask:0xf bank_mask:0x5\n\tv_add_f32_dpp %0, %2, %2 row_shr:4 row_mask:0xf bank_mask:0xa"
                   : "=&v"(f[c])
                   : "v"(d[0][c]), "v"(d[1][c]));
    asm volatile(
        "v_add_f32_dpp %0, %0, %0 quad_perm:[1,0,3,2] row_mask:0xf bank_mask:0xf\n\t"
        "v_add_f32_dpp %1, %1, %1 quad_perm:[1,0,3,2] row_mask:0xf bank_mask:0xf\n\t"
        "v_add_f32_dpp %2, %2, %2 quad_perm:[1,0,3,2] row_mask:0xf bank_mask:0xf\n\t"
        "v_add_f32_dpp %3, %3, %3 quad_perm:[1,0,3,2] row_mask:0xf bank_mask:0xf\n\t"
        "v_add_f32_dpp %0, %0, %0 quad_perm:[2,3,0,1] row_mask:0xf bank_mask:0xf\n\t"
        "v_add_f32_dpp %1, %1, %1 quad_perm:[2,3,0,1] row_mask:0xf bank_mask:0xf\n\t"
        "v_add_f32_dpp %2, %2, %2 quad_perm:[2,3,0,1] row_mask:0xf bank_mask:0xf\n\t"
        "v_add_f32_dpp %3, %3, %3 quad_perm:[2,3,0,1] row_mask:0xf bank_mask:0xf"
        : "+v"(f[0]), "+v"(f[1]), "+v"(f[2]), "+v"(f[3]));
    e = f32x4{f[0], __uint_as_float(__float_as_uint(f[1]) & 0x7fffffffu), f[2], __uint_as_float(__float_as_uint(f[3]) & 0x7fffffffu)};
  }
  asm volatile("s_mov_b64 exec, %3\n\tglobal_store_dwordx4 %0, %1, %2 sc1\n\ts_mov_b64 exec, -1\n\ts_nop 0" ::"v"(po), "v"(e), "s"(zp),
               "s"(0x1111111111111111ull)
               : "memory");
  asm volatile("s_mov_b64 exec, 1\n\tds_add_u32 %0, %1\n\ts_mov_b64 exec, -1" ::"v"(adw), "v"(mev) : "memory");
  __builtin_amdgcn_s_setprio(2);
  MK(12);
  const char* dst = uni_ptr(ws + OFF_Z2 + (size_t)layer * SZ_Z + (size_t)ni * S_ * 288 + (size_t)(mi * 4 + wm) * 9216 + 4096);
  const unsigned lane16 = (unsigned)lane * 16u;
  u32x4 wb[9];
  {
    static_assert(b_k(7) == 2 && b_k(8) == 3, "order");
    const char* wp = uni_ptr(ws + OFF_PI + (size_t)ni * (18 * K3C::WB) + 11 * K3C::WB + wm * 9216 + 4096);
#pragma unroll
    for (int j = 0; j < 8; ++j) asm volatile("global_load_dwordx4 %0, %1, %2 offset:%3 sc1" : "=v"(wb[j]) : "v"(lane16), "s"(wp), "n"(j * 1024 - 4096) : "memory");
    asm volatile("global_load_dwordx4 %0, %1, %2 sc1" : "=v"(wb[8]) : "v"(lane16 + 4096u), "s"(wp) : "memory");
  }
  u32x4 y[9];
  {
    const unsigned src = (unsigned)(g * 4096 + r * 16);
    unsigned zw = lds0 + (unsigned)(K3_ZC_OFF + (wm * 32 + 2 * r + (g & 1)) * 4), zr = lds0 + (unsigned)(K3_ZC_OFF + (wm * 32 + g * 8) * 4);
    asm volatile("" : "+v"(zw), "+v"(zr));
    f32x4 a, b, c, d;
    __builtin_amdgcn_s_sleep(6);
#pragma unroll 1
    for (unsigned spin = 0; spin < SPIN_MEM; ++spin) {
      asm volatile(
          "global_load_dwordx4 %0, %4, %5 sc1\n\t"
          "global_load_dwordx4 %1, %4, %5 offset:1024 sc1\n\t"
          "global_load_dwordx4 %2, %4, %5 offset:2048 sc1\n\t"
          "global_load_dwordx4 %3, %4, %5 offset:3072 sc1\n\t"
          "s_waitcnt vmcnt(0)"
          : "=&v"(a), "=&v"(b), "=&v"(c), "=&v"(d)
          : "v"(src), "s"(zp)
          : "memory");
      const unsigned bad = (__float_as_uint(a[1]) | __float_as_uint(a[3]) | __float_as_uint(b[1]) | __float_as_uint(b[3]) |
                            __float_as_uint(c[1]) | __float_as_uint(c[3]) | __float_as_uint(d[1]) | __float_as_uint(d[3])) >> 31;
      if (!__any(bad)) break;
    }
    MK(13);
    float s, q, zmb, zi;
    red16(a, b, c, d, s, q);
    ln_finish(s, q, 1.0f / (float)FH_, zmb, zi);
    f32x4 R[2], M[2];
    asm volatile(
        "ds_write2st64_b32 %4, %5, %6 offset1:2\n\t"
        "ds_read_b128 %0, %7\n\tds_read_b128 %2, %7 offset:512\n\tds_read_b128 %1, %7 offset:16\n\tds_read_b128 %3, %7 offset:528\n\t"
        "s_waitcnt lgkmcnt(0)"
        : "=&v"(R[0]), "=&v"(R[1]), "=&v"(M[0]), "=&v"(M[1])
        : "v"(zw), "v"(zi), "v"(-zmb * zi), "v"(zr)
        : "memory");
    MK(28);
    auto st = [&](int j) {
      if (j < 8) asm volatile("global_store_dwordx4 %0, %1, %2 offset:%3 sc1\n\ts_nop 1" ::"v"(lane16), "v"(y[j]), "s"(dst), "n"(j * 1024 - 4096) : "memory");
      else asm volatile("global_store_dwordx4 %0, %1, %2 sc1\n\ts_nop 1" ::"v"(lane16 + 4096u), "v"(y[j]), "s"(dst) : "memory");
    };
#pragma unroll
    for (int j = 0; j < 9; ++j) {
      {
        float t[8];
#pragma unroll
        for (int k = 0; k < 8; ++k) {
          t[k] = __builtin_fmaf(z[j][k / 2][k % 2], R[k / 4][k % 4], M[k / 4][k % 4]);
          asm volatile("" : "+v"(t[k]));
        }
        y[j] = u32x4{pack2(t[0], t[1]), pack2(t[2], t[3]), pack2(t[4], t[5]), pack2(t[6], t[7])};
      }
      st(j);
      __builtin_amdgcn_sched_barrier(0);
    }
    MK(29);
  }
  lds_inc(lw + LW_AD * 4, me);
  MK(24);
  __builtin_amdgcn_s_setprio(2);
  lds_spin4<2>(lw + LW_S * 4, 4u + (unsigned)(wm >> 1));
  {
    const unsigned fa = lds0 + (unsigned)((wm >> 1) * SB + K3C::XB + (wm & 1) * 9216) + lane16;
#pragma unroll
    for (int j = 0; j < 9; ++j) {
      asm volatile("ds_write_b128 %0, %1 offset:%2" ::"v"(fa), "v"(wb[j]), "n"(j * 1024) : "memory");
    }
    lds_inc(lw + LW_AD * 4, me);
  }
  announce(lw + LW_Z * 4, 4u, ws, CN_Z + layer, lane * 16 + ni, lane < 16);
  lds_inc(lw + LW_AD * 4, me);
  MK(27);
  f32x4 acc[2][9];
  {
    float bs[2];
    asm volatile("ds_read2_b32 %0, %1 offset1:16\n\ts_waitcnt lgkmcnt(0)"
                 : "=v"(*reinterpret_cast<f32x2*>(bs))
                 : "v"(lds0 + (unsigned)(K3_BS_OFF + (wm * 32 + r) * 4))
                 : "memory");
    lds_spin4<2>(lw + LW_S * 4, 6u + (unsigned)(wm >> 1));
    const unsigned fa = lds0 + (unsigned)((2 + (wm >> 1)) * SB + K3C::XB + (wm & 1) * 9216) + lane16;
#pragma unroll
    for (int j = 0; j < 9; ++j) {
      asm volatile("ds_write_b128 %0, %1 offset:%2" ::"v"(fa), "v"(y[j]), "n"(j * 1024) : "memory");
    }
    lds_inc(lw + LW_AD * 4, me);
#pragma unroll
    for (int mt = 0; mt < 2; ++mt) {
      f32x4 b4 = {bs[mt], bs[mt], bs[mt], bs[mt]};
      asm volatile("" : "+v"(b4));
#pragma unroll
      for (int j = 0; j < 9; ++j) acc[mt][j] = b4;
    }
  }
  u32x4 z1[9];
  const char* hp = uni_ptr(ws + OFF_H + (size_t)layer * SZ_Z + (size_t)((mi * 2 + (wm >> 1)) * 288 + ni * 18) * 1024);
  unsigned ho = (unsigned)((lane >> 5) * 1024 + ((wm & 1) * 32 + (lane & 31)) * 16);
  unsigned zb = lds0 + (unsigned)(wm * 4096) + lane16, zf = lw + (unsigned)(LW_S * 4 + wm);
  asm volatile("" : "+v"(ho), "+v"(zb), "+v"(zf));
  asm volatile("" : "+s"(hp));
  {
    CMath gm(acc);
    gm.setup(lds0, wm, lane);
    unsigned za = lds0 + (unsigned)(K3_Z1_OFF + wm * 5120) + lane16;
    asm volatile("" : "+v"(za));
    MK(15);
    lds_spin4<2>(lw + LW_L * 4, 8u);
    lds_spin4(lw + LW_AD * 4, 6u);
    MK(17);
    gm.first();
    __builtin_amdgcn_s_setprio(0);
    gm.stage<2, false>();
    gm.stage<3>();
    place();
#pragma unroll 1
    for (int t = 0; t < 7; ++t) {
      gm.stage<0>();
#pragma unroll
      for (int j = 0; j < NR1; ++j) asm volatile("ds_read_b128 %0, %1 offset:%2" : "=v"(z1[j]) : "v"(za), "n"(j * 1024));
      gm.stage<1>();
#pragma unroll
      for (int j = 0; j < NR1; ++j) asm volatile("" : "+v"(z1[j]));
      gm.stage<2>();
      gm.stage<3>();
    }
    gm.stage<0>();
    gm.stage<1, false, true>();
#pragma unroll
    for (int j = 0; j < 18; ++j) asm volatile("" : "+v"(acc[j / 9][j % 9]));
  }
  MK(16);
  if (NR1 < 5) lds_inc(lw + LW_AD * 4, me);
  unsigned zv;
  u32x4 zl[4];
#pragma unroll
  for (int j = 0; j < 9; ++j) {
    const bool sec = j >= NR1 + 3;
    if (j == NR1 || j == NR1 + 4) {
      unsigned n = 1u << 18;
      asm volatile(
          "s_waitcnt lgkmcnt(0)\n\tv_cmp_gt_u32 vcc, %8, %0\n\ts_cbranch_vccnz .Lw%=\n\t.subsection 1\n.Lw%=:\n\tds_read_u8 %0, %6\n\tds_read_b128 %2, %7 offset:%9\n\t"
          "ds_read_b128 %3, %7 offset:%9+1024\n\tds_read_b128 %4, %7 offset:%9+2048\n\tds_read_b128 %5, %7 offset:%9+3072\n\ts_waitcnt lgkmcnt(0)\n\t"
          "v_cmp_gt_u32 vcc, %8, %0\n\t" SAFE_OUT(".Lr%=") "s_cbranch_vccnz .Lw%=\n\ts_branch .Lr%=\n\t.subsection 0\n.Lr%=:"
          : "+v"(zv), NOP_(n), "+v"(zl[0]), "+v"(zl[1]), "+v"(zl[2]), "+v"(zl[3])
          : "v"(zf), "v"(zb), "n"(sec ? 11 : 10), "n"(sec ? SB : 0)
          : "memory", "vcc", "scc");
#pragma unroll
      for (int k = 0; k < 4; ++k)
        if (j + k < 9) z1[j + k] = zl[k];
    }
    if (j == NR1 - 1 || (j == NR1 + 3 && j < 8))
      asm volatile("ds_read_u8 %0, %5\n\tds_read_b128 %1, %6 offset:%7\n\tds_read_b128 %2, %6 offset:%7+1024\n\tds_read_b128 %3, %6 offset:%7+2048\n\t"
                   "ds_read_b128 %4, %6 offset:%7+3072"
                   : "=&v"(zv), "=&v"(zl[0]), "=&v"(zl[1]), "=&v"(zl[2]), "=&v"(zl[3])
                   : "v"(zf), "v"(zb), "n"(sec ? SB : 0)
                   : "memory");
    asm volatile("" : "+v"(z1[j]));
    u32x2 h[2];
#pragma unroll
    for (int mt = 0; mt < 2; ++mt) {
      const f32x4 a = acc[mt][j];
      h[mt] = u32x2{pack2v(f32x2{bf_lo(z1[j][2 * mt]), bf_hi(z1[j][2 * mt])} * f32x2{a[0], a[1]}),
                    pack2v(f32x2{bf_lo(z1[j][2 * mt + 1]), bf_hi(z1[j][2 * mt + 1])} * f32x2{a[2], a[3]})};
    }
    const auto s0 = __builtin_amdgcn_permlane16_swap(h[0][0], h[1][0], false, false);
    const auto s1 = __builtin_amdgcn_permlane16_swap(h[0][1], h[1][1], false, false);
    const u32x4 v = {s0[0], s1[0], s0[1], s1[1]};
    asm volatile("global_store_dwordx4 %0, %1, %2 offset:%3 sc1\n\ts_nop 1" ::"v"(ho), "v"(v), "s"(hp + (j / 2) * 4096), "n"((j % 2) * 2048) : "memory");
    __builtin_amdgcn_sched_barrier(0);
  }
  MK(18);
}

DEVI void k3_const(char* smem, char* ws, const u16* b_sp, int layer, int bid, int mi, int ni, int wm) {
  const int lane = lane_id(), l = lane < 54 ? lane : 53, p = l / 18;
  const char* kp = uni_ptr(ws + OFF_CSK + bid * 2048 + wm * 512);
  const unsigned q16 = (unsigned)(l - p * 18) * 16u;
  unsigned bs;
  f32x4 t;
  lds_spin((unsigned)(size_t)smem + (unsigned)(LW_OFF + (layer ? LW_T + 1 : LW_HELLO) * 4), layer ? 12u : 2u);
  asm volatile("global_load_ushort %0, %1, %2" : "=v"(bs) : "v"((unsigned)(lane & 31) * 2u), "s"(uni_ptr((const char*)(b_sp + mi * 128 + wm * 32))) : "memory");
  if (layer) {
    asm volatile("global_load_dwordx4 %0, %1, %2 sc1\n\ts_waitcnt vmcnt(0)" : "=&v"(t) : "v"(q16), "s"(kp) : "memory");
  } else {
    const char* cp = uni_ptr(ws + OFF_CSP + ni * 1152 + wm * 288);
    f32x4 a[6];
#pragma unroll
    for (int i = 0; i < 6; ++i)
      asm volatile("global_load_dwordx4 %0, %1, %2 sc1" : "=v"(a[i]) : "v"(q16 + (unsigned)(p * (6 * F_ * 4))), "s"(cp + i * (F_ * 4)) : "memory");
    asm volatile("s_waitcnt vmcnt(0)" : "+v"(a[0]), "+v"(a[1]), "+v"(a[2]), "+v"(a[3]), "+v"(a[4]), "+v"(a[5]));
    t = ((a[0] + a[1]) + (a[2] + a[3])) + (a[4] + a[5]);
#pragma unroll
    for (int j = 0; j < 4; ++j)
      t[j] += __int_as_float(__builtin_amdgcn_ds_bpermute((lane + 18) * 4, __float_as_int(t[j]))) +
              __int_as_float(__builtin_amdgcn_ds_bpermute((lane + 36) * 4, __float_as_int(t[j])));
  }
  asm volatile("" : "+v"(bs));
  const unsigned lds0 = (unsigned)(size_t)smem;
  f32x4 t0 = wm < 2 ? f32x4{t[0], t[0], t[1], t[1]} : t, t1 = wm < 2 ? f32x4{t[2], t[2], t[3], t[3]} : t;
  unsigned a0 = lds0 + (unsigned)(wm < 2 ? K3_C2_OFF + wm * 576 + lane * 32 : K3_C1_OFF + (wm - 2) * 288 + lane * 16), a1 = a0 + (wm < 2 ? 16u : 0u);
  unsigned ab = lds0 + (unsigned)(K3_BS_OFF + (wm * 32 + (lane & 31)) * 4);
  bs <<= 16;
  asm volatile("" : "+v"(t0), "+v"(t1), "+v"(a0), "+v"(a1), "+v"(ab), "+v"(bs));
  bar();
  asm volatile("ds_write_b32 %4, %5\n\ts_mov_b64 exec, 0x3ffff\n\tds_write_b128 %0, %2\n\tds_write_b128 %1, %3\n\ts_mov_b64 exec, -1" ::"v"(a0), "v"(a1),
               "v"(t0), "v"(t1), "v"(ab), "v"(bs)
               : "memory");
  if (layer == 0 && lane < 24) asm volatile("global_store_dwordx4 %0, %1, %2\n\ts_nop 1" ::"v"((unsigned)lane * 16u), "v"(t), "s"(kp) : "memory");
}

DEVI void p3_g1(char* smem, char* __restrict__ ws, int layer, int mi, int wm, int lane MK_ARG) {
  const unsigned lds0 = (unsigned)(size_t)smem;
  const int g = lane >> 4, r = lane & 15;
  const unsigned lw = lds0 + (unsigned)(LW_OFF + layer * 4);
  const unsigned xrd = lds0 + (unsigned)(g * 2048 + (wm * 32 + r) * 16);
  s16x8 xc[4][2][2];
  {
    const char* xs = uni_ptr(ws + OFF_XS + (size_t)layer * 2 * SZ_XS + (size_t)(mi * 2 + (wm >> 1)) * 4096 + (wm & 1) * 256);
    f32x4 a, b;
    auto pick = [&](int t) {
      bar();
      asm volatile(
          "ds_read_b128 %0, %4\n\tds_read_b128 %1, %4 offset:256\n\tds_read_b128 %2, %4 offset:8192\n\tds_read_b128 %3, %4 offset:8448\n\t"
          "s_waitcnt lgkmcnt(0)"
          : "=&v"(xc[t][0][0]), "=&v"(xc[t][0][1]), "=&v"(xc[t][1][0]), "=&v"(xc[t][1][1])
          : "v"(xrd + (unsigned)(t * K3C::SB))
          : "memory");
    };
    __builtin_amdgcn_s_setprio(2);
    pick(0);
    pick(1);
    asm volatile("global_load_dwordx4 %0, %2, %3 sc1\n\tglobal_load_dwordx4 %1, %2, %3 offset:512 sc1"
                 : "=&v"(a), "=&v"(b)
                 : "v"((unsigned)(g * 1024 + r * 16)), "s"(xs)
                 : "memory");
    pick(2);
    pick(3);
    bar();
    asm volatile("s_waitcnt vmcnt(0)" : "+v"(a), "+v"(b) : : "memory");
    float s, q, mb, rinv;
    red8(a, b, s, q);
    bar();
    asm volatile("" : "+v"(s), "+v"(q));
    ln_finish(s, q, 1.0f / (float)D_, mb, rinv);
    asm volatile("" : "+v"(mb), "+v"(rinv));
    bar();
    asm volatile("" : "+v"(mb), "+v"(rinv));
    const float k0 = -2.0f * 0.7978845608028654f * 1.4426950408889634f, k1 = k0 * 0.044715f;
    float sd = __builtin_amdgcn_rcpf(rinv), c0 = k0 * rinv, c1 = k1 * rinv * rinv * rinv;
    asm volatile("" : "+v"(sd));
    asm volatile("" : "+v"(c0));
    asm volatile("" : "+v"(c1));
    const float e0 = lane < 32 ? -mb : c0, e1 = lane < 32 ? sd : c1;
    asm volatile("ds_write2st64_b32 %0, %1, %2 offset1:2" ::"v"(
                     lds0 + (unsigned)(K3_ST_OFF + (g >> 1) * 1024 + (wm * 32 + 2 * r + (g & 1)) * 4)),
                 "v"(e0), "v"(e1)
                 : "memory");
    bar();
    asm volatile("s_waitcnt lgkmcnt(0)" ::: "memory");
    bar();
    __builtin_amdgcn_s_setprio(0);
  }
  f32x4 ac[2][9];
#pragma unroll
  for (int mt = 0; mt < 2; ++mt)
#pragma unroll
    for (int j = 0; j < 9; ++j) ac[mt][j] = f32x4{0.f, 0.f, 0.f, 0.f};
  {
    UMath<true> gm(ac, xc);
    gm.xa = xrd;
    gm.wa = lds0 + (unsigned)(K3C::XB + (g * 8 + (r & 7)) * K3C::P + (r >> 3) * 16);
    gm.fa = lw + (unsigned)(LW_AD * 4 + (lane & 3));
    gm.fw = lw + (unsigned)(LW_S * 4 + wm);
    gm.fv = 0u;
    asm volatile("" : "+v"(gm.fa), "+v"(gm.fw), "+v"(gm.fv), "+v"(gm.xa), "+v"(gm.wa));
    __builtin_amdgcn_s_sleep(40);
    lds_spin4<4>(lw + LW_L * 4, 4u);
    gm.first();
    place();
    lds_spin4(lw + LW_AD * 4, 2u);
    __builtin_amdgcn_s_setprio(1);
    gm.run();
    __builtin_amdgcn_s_setprio(0);
  }
  MK(14);
  f32x2 rc[4];
  f32x4 cs[9];
  const int l2 = lane_id();
  {
    const unsigned ra = lds0 + (unsigned)(K3_ST_OFF + (wm * 32 + (l2 & 15)) * 4), ca = lds0 + (unsigned)(K3_C1_OFF + (l2 & 48));
#pragma unroll
    for (int k = 0; k < 4; ++k) asm volatile("ds_read2_b32 %0, %1 offset1:16" : "=v"(rc[k]) : "v"(ra + (unsigned)(k * 512)) : "memory");
#pragma unroll
    for (int j = 0; j < 9; ++j) asm volatile("ds_read_b128 %0, %1 offset:%2" : "=v"(cs[j]) : "v"(ca), "n"(j * 64) : "memory");
    asm volatile("s_waitcnt lgkmcnt(0)" : "+v"(rc[0]), "+v"(rc[1]), "+v"(rc[2]), "+v"(rc[3]));
#pragma unroll
    for (int j = 0; j < 9; ++j) asm volatile("" : "+v"(cs[j]));
  }
  const unsigned za = lds0 + (unsigned)(K3_Z1_OFF + wm * 5120 + l2 * 16);
  u32x4 z1[9];
#pragma unroll
  for (int t = 0; t < 18; ++t) {
    const int j = t / 2, mt = t % 2;
    float u[4], w[4];
#pragma unroll
    for (int i = 0; i < 4; ++i) asm volatile("v_fma_f32 %0, %1, %2, %3" : "=v"(u[i]) : "v"(rc[0][mt]), "v"(cs[j][i]), "v"(ac[mt][j][i]));
#pragma unroll
    for (int i = 0; i < 4; ++i) asm volatile("v_mul_f32 %0, %1, %1" : "=v"(w[i]) : "v"(u[i]));
#pragma unroll
    for (int i = 0; i < 4; ++i) asm volatile("v_fma_f32 %0, %0, %1, %2" : "+v"(w[i]) : "v"(rc[3][mt]), "v"(rc[2][mt]));
#pragma unroll
    for (int i = 0; i < 4; ++i) asm volatile("v_mul_f32 %0, %0, %1" : "+v"(w[i]) : "v"(u[i]));
#pragma unroll
    for (int i = 0; i < 4; ++i) asm volatile("v_exp_f32 %0, %0" : "+v"(w[i]));
#pragma unroll
    for (int i = 0; i < 4; ++i) asm volatile("v_fma_f32 %0, %0, %1, %1" : "+v"(w[i]) : "v"(rc[1][mt]));
#pragma unroll
    for (int i = 0; i < 4; ++i) asm volatile("v_rcp_f32 %0, %0" : "+v"(w[i]));
#pragma unroll
    for (int i = 0; i < 4; ++i) asm volatile("v_mul_f32 %0, %0, %1" : "+v"(w[i]) : "v"(u[i]));
    z1[j][2 * mt] = pack2(w[0], w[1]);
    z1[j][2 * mt + 1] = pack2(w[2], w[3]);
    if (mt && j < NR1) asm volatile("ds_write_b128 %0, %1 offset:%2" ::"v"(za), "v"(z1[j]), "n"(j * 1024) : "memory");
    if (t == 9) asm volatile("s_waitcnt lgkmcnt(0)" ::: "memory");
    __builtin_amdgcn_sched_barrier(0);
    bar();
    __builtin_amdgcn_sched_barrier(0);
  }
  MK(15);
#pragma unroll 1
  for (int t = 20; t < 32; ++t) bar();
  __builtin_amdgcn_s_sleep(9);
#pragma unroll
  for (int k = NR1; k < 9; ++k) {
    if (k == NR1 + 4) lds_spin4(lw + LW_AD * 4, 7u);
    asm volatile("ds_write_b128 %0, %1 offset:%2" ::"v"(lds0 + (unsigned)(wm * 4096 + l2 * 16)), "v"(z1[k]),
                 "n"(((k - NR1) / 4) * K3C::SB + ((k - NR1) % 4) * 1024)
                 : "memory");
    if (k == NR1 + 3 || k == 8) asm volatile("ds_write_b8 %0, %1" ::"v"(lw + (unsigned)(LW_S * 4 + wm)), "v"(vconst(k == NR1 + 3 ? 10u : 11u)) : "memory");
  }
}

constexpr int W5A = 22;
constexpr int NPF = 30;
struct Loader {
  u32x4 pf[NPF];
  unsigned lds0, lane16;
  int wl, lane;

  template <int R0, int N, int P0, int PE>
  DEVI void pf_load(const char* src) {
    const unsigned o = lane16 + (unsigned)(wl * 1024);
#pragma unroll
    for (int t = 0; t < N; ++t) {
      unsigned a = o + (unsigned)((P0 + 4 * t) * 1024);
      if (P0 + 4 * t + 3 >= PE && P0 + 4 * t + wl >= PE) a = lane16;
      asm volatile("global_load_dwordx4 %0, %1, %2 sc1" : "=v"(pf[R0 + t]) : "v"(a), "s"(src) : "memory");
    }
  }
  template <int R0, int N>
  DEVI void pf_pin() {
#pragma unroll
    for (int t = 0; t < N; ++t) asm volatile("" : "+v"(pf[R0 + t]));
  }
  template <int R0, int N>
  DEVI void pf_undef() {
#pragma unroll
    for (int t = 0; t < N; ++t) asm volatile("" : "=v"(pf[R0 + t]));
  }
  template <int PB, int A, int N>
  DEVI void pf_put(unsigned dst) {
    constexpr int T0 = (A - PB) / 4, T1 = (A + N - 1 - PB) / 4;
    const unsigned a = lds0 + dst + lane16 + (unsigned)((wl + PB + 4 * T0 - A + 4) * 1024) - 4096u;
#pragma unroll
    for (int t = T0; t <= T1; ++t) {
      const int p0 = PB + 4 * t;
      if ((p0 >= A || p0 + wl >= A) && (p0 + 3 < A + N || p0 + wl < A + N))
        asm volatile("ds_write_b128 %0, %1 offset:%2" ::"v"(a), "v"(pf[t]), "n"((t - T0) * 4096) : "memory");
    }
  }
  template <int N>
  DEVI void dma_run(const char* src, unsigned dst) {
    constexpr int B = N / 4, R = N % 4;
    const unsigned f = (unsigned)(wl * B + (wl < R ? wl : R)) * 1024u, o = lane16 + f, m = lds0 + dst + f;
    dma_seq<B>(src, o, m);
    if (wl < R) dma16_sc1(src + B * 1024, o, m + (unsigned)(B * 1024));
  }
  template <int V>
  DEVI void wait_w() {
    asm volatile("s_cmp_lt_u32 %0, 2\n\ts_cbranch_scc1 1f\n\ts_waitcnt vmcnt(%1)\n1:\n\ts_waitcnt vmcnt(%2)" ::"s"(wl), "n"(V - 1), "n"(V) : "memory", "scc");
  }
  template <int V>
  DEVI void wbar() {
    asm volatile("s_waitcnt vmcnt(%0) lgkmcnt(0)" ::"n"(V) : "memory");
    bar();
  }

  DEVI void p1(char* smem, char* ws, int bid, int rt, int hp MK_ARG) {
    constexpr int SB = K1C::SB;
    static_assert(K1C::XB == 18 * 1024 && K1C::WB == 14 * 1024, "K1 shape");
    const char* wp = uni_ptr(ws + OFF_PQ + (size_t)hp * (9 * K1C::WB) + 4 * K1C::WB);
    static_assert(3 * SB + K1C::XB >= P5_W + 5 * 18432, "free while the GEMM before ends");
    const char* wq = wp - 4 * K1C::WB;
    dma_run<14>(wq + 3 * K1C::WB, (unsigned)(3 * SB + K1C::XB));
    dma_run<28>(wp, (unsigned)K1_WX);
    if (wl == 3) dma16_sc1(uni_ptr(ws + OFF_CSQ + bid * 512), lane16 & 511u, lds0 + (unsigned)K1_CS_OFF);
    __builtin_amdgcn_s_setprio(0);
    wait_vm<0>();
    const bool lo = wl < 2;
    const int x0 = lo ? wl * 5 : wl * 4 + 2, w0 = lo ? wl * 3 : wl * 4 - 2;
    unsigned xo[5];
#pragma unroll
    for (int i = 0; i < 5; ++i) {
      const int c = (x0 + i) * 64 + lane;
      const int ch = c / 144;
      int gr = rt * 128 - 16 + (c - ch * 144);
      if (gr < 0) gr = 0;
      xo[i] = (unsigned)(((gr >> 6) * 72 + ch + 3 - (i & 3)) * 1024 + (gr & 63) * 16);
      asm volatile("" : "+v"(xo[i]));
    }
    const char* xp = uni_ptr(ws + OFF_X1 + SZ_X - 3072);
    wp += 2 * K1C::WB + w0 * 1024;
    auto dma_x = [&](unsigned so) {
      const unsigned m = lds0 + so + (unsigned)(x0 * 1024);
      dma4<4, true>(xp, xo[0], xo[1], xo[2], xo[3], m);
      if (lo) dma16_sc1(xp, xo[4], m + 4096u);
      xp += 8192;
    };
    auto dma_w = [&](unsigned so) {
      const unsigned m = lds0 + so + (unsigned)(K1C::XB + w0 * 1024);
      dma_seq<3>(wp, lane16, m);
      if (!lo) dma16_sc1(wp + 3072, lane16, m + 3072u);
      wp += K1C::WB;
    };
    const char* xs = uni_ptr(ws + OFF_XS + SZ_XS);
    const int g = lane >> 4, p2 = 64 + (lane & 7);
    int ra = rt * 128 - 16 + wl * 32 + 2 * (lane & 15);
    if (ra < 0) ra = 0;
    unsigned sa = (unsigned)(((ra >> 6) * 8 + g * 2) * 512 + (ra & 63) * 8);
    unsigned sb = (unsigned)(((rt * 2 + 1) * 8 + (lane >> 3)) * 512 + (2 * p2 - 80) * 8);
    unsigned sw = lds0 + (unsigned)(K1_ST_OFF + (2 * (lane < 32 ? wl * 16 + (lane & 15) : p2) + (g & 1)) * 8);
    asm volatile("" : "+v"(sa), "+v"(sb), "+v"(sw));
    MK(22);
    __builtin_amdgcn_s_setprio(3);
    bar();
    dma_run<14>(wq, (unsigned)K1C::XB);
    dma_run<14>(wq + K1C::WB, (unsigned)(SB + K1C::XB));
    dma_run<14>(wq + 2 * K1C::WB, (unsigned)(2 * SB + K1C::XB));
    MK(1);
    bar();
    dma_x(0u);
    dma_x((unsigned)SB);
    wbar<4>();
    MK(29);
    dma_x((unsigned)(2 * SB));
    dma_x((unsigned)(3 * SB));
    wbar<8>();
    MX(1, 3);
    dma_x(0u);
    wbar<8>();
    MX(1, 4);
    dma_x((unsigned)SB);
    f32x4 a, b, e;
    asm volatile("global_load_dwordx4 %0, %3, %5 sc1\n\tglobal_load_dwordx4 %1, %3, %5 offset:512 sc1\n\tglobal_load_dwordx4 %2, %4, %5 sc1"
                 : "=&v"(a), "=&v"(b), "=&v"(e)
                 : "v"(sa), "v"(sb), "s"(xs)
                 : "memory");
    wbar<11>();
    MX(1, 5);
    dma_w((unsigned)SB);
    dma_x((unsigned)(2 * SB));
    wbar<15>();
    MX(1, 6);
    dma_w((unsigned)(2 * SB));
    dma_x((unsigned)(3 * SB));
    wbar<19>();
    MX(1, 7);
    dma_w((unsigned)(3 * SB));
    dma_x(0u);
    wbar<16>();
    MX(1, 8);
    asm volatile("" : "+v"(a), "+v"(b), "+v"(e));
    float s, q;
    {
      float t[4] = {e[0], e[1], e[2], e[3]}, s2, q2;
      asm volatile(
          "v_add_f32_dpp %0, %0, %0 row_ror:8 row_mask:0xf bank_mask:0xf\n\tv_add_f32_dpp %1, %1, %1 row_ror:8 row_mask:0xf bank_mask:0xf\n\t"
          "v_add_f32_dpp %2, %2, %2 row_ror:8 row_mask:0xf bank_mask:0xf\n\tv_add_f32_dpp %3, %3, %3 row_ror:8 row_mask:0xf bank_mask:0xf\n\ts_nop 1"
          : "+v"(t[0]), "+v"(t[1]), "+v"(t[2]), "+v"(t[3]));
      red4(t, s2, q2);
      red8(a, b, s, q);
      s = lane < 32 ? s : s2;
      q = lane < 32 ? q : q2;
      asm volatile("" : "+v"(s), "+v"(q));
    }
    wbar<8>();
    MX(1, 9);
    {
      asm volatile("" : "+v"(s), "+v"(q));
      float mb, rinv;
      ln_finish(s, q, 1.0f / (float)D_, mb, rinv);
      asm volatile("ds_write_b64 %0, %1" ::"v"(sw), "v"(f32x2{-mb, rinv}) : "memory");
    }
    wbar<0>();
    MK(30);
  }

  template <int I>
  DEVI void p5_step(const char* wp) {
    constexpr int T = 2 * I + 1;
    wbar<(T == 1 ? 63 : (T <= 11 ? 3 : 0))>();
    if constexpr (T <= 9) {
      const unsigned wo = (unsigned)(12288 + (wl & 1) * 3072), so = (unsigned)(P5_W + ((T + 2 + (wl >> 1)) & 7) * 18432);
      dma_seq<3>(wp + (T + 7) * 18432, lane16 + wo + (unsigned)((wl >> 1) * 18432), lds0 + so + wo);
      pf_put<0, 12 * (T - 1), 12>((unsigned)(P5_W + ((T + 2) % 8) * 18432));
      pf_put<0, 12 * T, 12>((unsigned)(P5_W + ((T + 3) % 8) * 18432));
    }
  }
  template <int... S>
  DEVI void p5_steps(const char* wp, std::integer_sequence<int, S...>) {
    (p5_step<S>(wp), ...);
  }

  template <int T0, int T1>
  DEVI void w5_load(const char* w5p) {
    const unsigned o = lane16 + (unsigned)(wl * 1024);
#pragma unroll
    for (int t = T0; t < T1; ++t)
      asm volatile("global_load_dwordx4 %0, %1, %2 sc1" : "=v"(pf[t]) : "v"(o + (unsigned)((t / 3 + 8) * 18432 + (t % 3) * 4096)), "s"(w5p) : "memory");
  }
  DEVI void p35(char* smem, char* ws, const u16* w_sp, int layer, int bid, int mi, int ni, int rb, int ct, int hp MK_ARG) {
    using C = K3C;
    constexpr int NS = C::NS, SB = C::SB, NDX = 4, NDW = 5, VPS = NDX + NDW;
    const char* wip = uni_ptr(ws + OFF_PI + (size_t)ni * (18 * C::WB));
    if (layer) {
      pf_put<-48, 36, 18>((unsigned)(2 * SB + C::XB));
      pf_put<-48, 54, 18>((unsigned)(3 * SB + C::XB));
    } else {
      dma_run<18>(wip + 2 * C::WB, (unsigned)(2 * SB + C::XB));
      dma_run<18>(wip + 3 * C::WB, (unsigned)(3 * SB + C::XB));
    }
    dma_run<18>(wip + C::WB, (unsigned)(SB + C::XB));
    const char* w5p = uni_ptr(ws + OFF_PW + (size_t)ct * (36 * 9216));
    const int w0 = wl < 2 ? wl * 5120 : wl * 4096 + 2048;
    const unsigned m0x = lds0 + (unsigned)(wl * 4096), m0w = lds0 + (unsigned)(C::XB + w0);
    const unsigned wo = lane16 + (unsigned)w0;
    const unsigned lw = lds0 + (unsigned)(LW_OFF + layer * 4), me = 1u << (8 * wl);
    const unsigned wo4 = wo + 4096u;
    auto dma_w = [&](const char* wp, unsigned so) {
      dma_seq<4>(wp, wo, m0w + so);
      if (wl < 2) dma16_sc1(wp, wo4, m0w + so + 4096u);
    };
    {
      const char* xp = uni_ptr(ws + OFF_X1 + (size_t)layer * 2 * SZ_X + (size_t)(mi * 2 * 72 + wl * 2) * 1024 - 1024);
      const unsigned xo0 = lane16 + 1024u, xo1 = lane16 + 72u * 1024u, xo3 = lane16 + 71u * 1024u;
      auto dma_x = [&](int k, unsigned so) { dma4<4, true>(xp + k * 8192, xo0, xo1, lane16, xo3, m0x + so); };
      MK(10);
      bar();
      dma_x(0, 0u);
      dma_x(1, (unsigned)SB);
      wait_vm<4>(); MX(3, 2); wbar<4>(); MX(2, 2);
      MK(25);
      {
        dma_x(2, (unsigned)(2 * SB));
        dma_x(3, (unsigned)(3 * SB));
        dma_x(4, (unsigned)(4 * SB));
        wbar<12>(); MX(2, 3);
        dma_x(5, 0u);
        wait_vm<12>();
      }
      bar(); MX(2, 4);
      dma_w(wip + 4 * C::WB, 0u);
      dma_x(6, (unsigned)SB);
      wait_w<8 + VPS>();
#pragma unroll 1
      for (int t = 1; t < 3; ++t) {
        bar(); MX(2, 4 + t);
        dma_w(wip + (t + 4) * C::WB, (unsigned)(t * SB));
        dma_x(t + 6, (unsigned)((t + 1) * SB));
        wait_w<NDX + VPS>();
      }
      bar(); MX(2, 7);
      dma_w(wip + 7 * C::WB, (unsigned)(3 * SB));
      wait_w<VPS>();
      bar(); MX(2, 8);
      dma_w(wip + 8 * C::WB, 0u);
      wait_w<NDW>();
      bar(); MX(2, 9);
      const char* wp = wip + 9 * C::WB;
      dma_w(wp + b_k(0) * C::WB, (unsigned)SB);
      if (layer == 0) {
        const char* sp = uni_ptr((const char*)w_sp + (size_t)(bid * 4 + wl) * 8192);
        const unsigned so = (unsigned)((lane >> 5) * 4096 + (lane & 31) * 16);
#pragma unroll
        for (int i = 0; i < WSP_V; ++i) dma16(sp + i * 512, so, lds0 + (unsigned)K3_DM_OFF);
        dma16(sp + (bid >> 4) * 256, so & ~0x100u, lds0 + (unsigned)K3_DM_OFF);
      }
      w5_load<0, 4>(w5p);
      wait_w<NDW + 4>();
      MX(3, 10); bar(); MX(2, 10);
      dma_w(wp + b_k(1) * C::WB, (unsigned)(2 * SB));
      w5_load<4, 8>(w5p);
      MK(26);
      lds_spin4(lw + LW_AD * 4, 1u);
      dma_w(wp + b_k(2) * C::WB, (unsigned)(3 * SB));
      dma_w(wp + b_k(3) * C::WB, 0u);
      w5_load<8, W5A>(w5p);
      wait_vm<W5A - 8>();
      lds_inc(lw + LW_L * 4, 4u * me);
      w5_load<W5A, 30>(w5p);
      MK(11);
      static_for<4, 7>([&](auto T_) {
        constexpr int t = T_;
        lds_spin4<2>(lw + LW_S * 4, (unsigned)(t - 3));
        MX(2, 12 + t);
        dma_w(wp + b_k(t) * C::WB, (unsigned)(((t + 1) % 4) * SB));
        if constexpr (t > 4) {
          wait_w<NDW>();
          lds_inc(lw + LW_L * 4, me);
        }
      });
      MX(2, 19);
      wait_vm<0>();
      lds_inc(lw + LW_L * 4, me);
      pf_pin<0, 30>();
      MK(13);
    }
    {
      unsigned xoff[NDX];
#pragma unroll
      for (int i = 0; i < NDX; ++i) {
        const int c = wl * 256 + i * 64 + lane;
        const int row = c >> 3, cc = c & 7;
        xoff[i] = (unsigned)((mi * 128 + row) * S_ * 2 - i * 1024) + (unsigned)((cc ^ ((row >> 1) & 7)) * 16);
      }
      auto dma_x = [&](const char* xp, unsigned so) { dma4<4, false>(xp, xoff[0], xoff[1], xoff[2], xoff[3], m0x + so); };
      const char* xp = uni_ptr((const char*)w_sp);
      const char* wp = uni_ptr(ws + OFF_Z2 + (size_t)layer * SZ_Z + (size_t)ni * S_ * 288);
      lds_spin4<2>(lw + LW_AD * 4, 5u);
      dma_x(xp + mi * 256, (unsigned)(2 * SB));
      dma_x(xp + mi * 256 + 128, (unsigned)(3 * SB));
      MK(24);
      int left = 2 * mi;
      auto next = [&]() {
        const int d = --left == 0 ? 3 : 1;
        xp += d * 128;
        wp += d * C::WB;
      };
      if (mi == 0) {
        xp += 256;
        wp += 2 * C::WB;
      }
      dma_x(xp, 0u);
      lds_spin4(lw + LW_S * 4, 6u);
      dma_x(xp + 128, (unsigned)SB);
      wait_vm<2 * NDX>();
      lds_inc(lw + LW_L * 4, me);
      MK(27);
      if (wl == 0) {
        cnt_wait(ws, CN_Z + layer, mi * 16 + ni, 16u);
        lds_wr32(lw + LW_GO * 4, 1u);
      } else {
        lds_spin(lw + LW_GO * 4, 1u);
      }
      MK(14);
      lds_spin4(lw + LW_S * 4, 8u);
      dma_w(wp, 0u);
      lds_spin4(lw + LW_S * 4, 9u);
      dma_w(wp + C::WB, (unsigned)SB);
      next();
      next();
      unsigned slot = (unsigned)(2 * SB);
      auto cissue = [&]() {
        dma_x(xp, slot);
        dma_w(wp, slot);
        next();
        slot = (slot == (unsigned)((NS - 1) * SB)) ? 0u : slot + (unsigned)SB;
      };
      auto cstep = [&]() {
        bar();
        cissue();
      };
      wait_w<NDW>();
      MK(15);
      int j = 2;
      if (layer == 0) {
        const char* sp = uni_ptr((const char*)w_sp + (size_t)(bid * 4 + wl) * 8192 + WSP_V * 512);
        unsigned d = 0u;
#pragma unroll 1
        for (; j < 2 + 32 - 4 * WSP_V; ++j) {
          cstep();
          asm volatile("s_load_dword %0, %1, 0\n\ts_load_dword %0, %1, 64\n\ts_load_dword %0, %1, 0x1000\n\ts_load_dword %0, %1, 0x1040"
                       : "+s"(d)
                       : "s"(sp));
          sp += 128;
          wait_w<VPS>();
        }
#pragma unroll 1
        for (int e = j + 4; j < e; ++j) {
          cstep();
          asm volatile("" : "+s"(d));
          wait_w<VPS>();
        }
        asm volatile("s_waitcnt lgkmcnt(0)" : "+s"(d));
      }
      place();
#pragma unroll 1
      for (; j <= 29; ++j) {
        cstep();
        wait_w<VPS>();
      }
      bar();
    }
    dma_run<33>(w5p, (unsigned)(P5_W + 54 * 1024));
    dma_run<23>(w5p + 67 * 1024, (unsigned)(P5_W + 121 * 1024));
    if (wl == 3) dma16_sc1(w5p + 143 * 1024, lane16, lds0 + (unsigned)(P5_W + 53 * 1024));
    wait_vm<14>();
    bar();
    dma_run<34>(w5p + 33 * 1024, (unsigned)(P5_W + 87 * 1024));
    wait_vm<0>();
    bar();
    if (wl) {
      const unsigned f = (unsigned)(wl - 1) * 18432u;
      dma_seq<17>(w5p + 90 * 1024, lane16 + f, lds0 + (unsigned)P5_W + f);
      if (wl < 3) dma16_sc1(w5p + 107 * 1024, lane16 + f, lds0 + (unsigned)(P5_W + 17 * 1024) + f);
    } else {
      cnt_wait(ws, CN_H + layer, rb * 8 + ct, 8u);
    }
    MK(20);
    bar();
    p5_steps(w5p, std::make_integer_sequence<int, 7>{});
    if (layer == 0) {
      if (bid < 24 && wl == 0 && lane < 8) pk_st16(ws + OFF_HELLO + bid * 128 + lane16, u32x4{0u, 0u, 0u, 0u});
    }
  }
};

__global__ __launch_bounds__(768, 3) void k_main(const u16* __restrict__ x0, const u16* __restrict__ wqkv, const u16* __restrict__ wo,
                                                 const u16* __restrict__ w_in, const u16* __restrict__ w_sp,
                                                 const u16* __restrict__ b_sp, const u16* __restrict__ w_out,
                                                 u16* __restrict__ out, char* ws) {
  extern __shared__ __attribute__((aligned(16))) char smem[];
  const char* code0 = code_here();
  (void)code0;
  const unsigned lds0 = (unsigned)(size_t)smem;
  int wave = __builtin_amdgcn_readfirstlane((int)threadIdx.x >> 6);
  int bid = blockIdx.x;
#define FRESH() asm volatile("" : "+s"(bid), "+s"(ws), "+s"(wave))
#define XCD (bid & 7)
#define IDX (bid >> 3)
#define MI ((XCD >> 2) * 8 + (IDX >> 2))
#define NI ((XCD & 1) * 8 + (XCD & 2) * 2 + (IDX & 3))
#define RB (XCD * 4 + (IDX >> 3))
#define CT (IDX & 7)
#define RT ((XCD >> 1) * 4 + (IDX >> 3))
#define HP ((XCD & 1) * 8 + (IDX & 7))
  MK(0);
  if (wave == 2) {
    constexpr int PER = (2 * NL_MAIN + 31) / 32;
    const int lane = lane_id();
    static_assert(PER <= 64, "one load");
    int li = IDX * PER + (lane < PER ? lane : PER - 1);
    if (li > 2 * NL_MAIN - 1) li = 2 * NL_MAIN - 1;
    touch64(code0, (unsigned)li * 64u, lds0 + (unsigned)(K1R_DUMMY + 768));
  }
  if (wave >= 3 && wave < 8) {
    constexpr int ST = 64;
    const int k = (7 - wave) * 64 + lane_id();
    const bool isx = k < 9216 / ST;
    int o = isx ? bid * 9216 + k * ST : bid * 7776 + (k - 9216 / ST) * ST;
    if (!isx && k - 9216 / ST > 7775 / ST) o = bid * 7776;
    const char* p = (isx ? (const char*)x0 : (const char*)wqkv) + o;
    asm volatile("s_mov_b32 m0, %1\n\ts_nop 0\n\tglobal_load_lds_dword %0, off" ::"v"(p), "s"(lds0 + (unsigned)K1R_DUMMY) : "memory");
  }

  if (__builtin_expect(wave >= 8, 1)) {
    __builtin_amdgcn_s_setprio(3);
    {
      const int lane = lane_id();
      RawLd rl;
      const bool cross = (((HP * 72) & ~15) & 127) > 48;
      const int c16 = IDX >> 1;
      const int cj = (int)((0x65743210u >> (4 * XCD)) & 7u) * 16 + c16;
      const int job = cross ? (cj < 88 ? 128 + cj : 999) : XCD * 16 + c16;
      const int pj = cross ? cj - 88 : ((XCD & 5) == 0 && (c16 & 3) == (c16 >> 2) ? 40 + XCD * 2 + (c16 >> 2) : -1);
      rl.front(x0, wqkv, wo, ws, bid, lds0, wave - 8, lane, RT, HP, pj MF_PASS);
      if (job < 216) k1_raw_loader<true>(rl, w_in, w_out, ws, rl.tag, bid, lds0, wave - 8, lane, RT, HP, CT, job, pj MF_PASS);
      else k1_raw_loader<false>(rl, w_in, w_out, ws, rl.tag, bid, lds0, wave - 8, lane, RT, HP, CT, job, pj MF_PASS);
    }
    Loader ld;
    ld.lds0 = lds0;
#pragma unroll 1
    for (int layer = 0; layer < 2; ++layer) {
      FRESH();
      ld.wl = wave - 8;
      ld.lane = lane_id();
      ld.lane16 = (unsigned)ld.lane * 16u;
      const char* wip = uni_ptr(ws + OFF_PI + (size_t)NI * (18 * K3C::WB));
      if (layer) {
        ld.p1(smem, ws, bid, RT, HP MK_PASS);
        ld.pf_load<0, 21, 0, 81>(uni_ptr(ws + OFF_PO + (size_t)CT * (18 * 4608)));
        ld.pf_load<21, 9, 36, 72>(wip);
        wait_vm<9>();
        ld.pf_pin<0, 21>();
        lds_spin(lds0 + (unsigned)(LW_OFF + (LW_T + layer) * 4), 8u);
        __builtin_amdgcn_s_sleep(4);
        ld.pf_put<0, 0, 40>((unsigned)P2_W);
        wait_vm<0>();
        ld.pf_pin<21, 9>();
        bar();
        ld.pf_put<0, 40, 41>((unsigned)(P2_W + 40 * 1024));
      } else {
        ld.pf_undef<21, 9>();
        MK(3);
        MK(4);
        bar();
        ld.dma_run<81>(uni_ptr(ws + OFF_PO + (size_t)CT * (18 * 4608)), (unsigned)P2_W);
      }
      if (layer == 0 && ld.wl == 0) cnt_wait(ws, CN_O + layer, RB * 8 + CT, 16u);
      MK(6);
      asm volatile("s_waitcnt vmcnt(0) lgkmcnt(0)" ::: "memory");
      bar();
      lds_spin(lds0 + (unsigned)(LW_OFF + (LW_T + layer) * 4), 12u);
      if (layer == 0) {
        if (ld.wl == 0) {
          cnt_wait(ws, CN_PK, 8 + NI, 36u);
          lds_wr32(lds0 + (unsigned)(LW_OFF + LW_HELLO * 4), 2u);
        } else {
          lds_spin(lds0 + (unsigned)(LW_OFF + LW_HELLO * 4), 2u);
        }
        MK(7);
      }
      static_assert(2 * K3C::SB <= P2_W, "free during the first residual GEMM");
      ld.dma_run<18>(wip, (unsigned)K3C::XB);
      MX(6, 24);
      if (layer == 0 && ld.wl == 0) cnt_wait(ws, CN_PK, 24 + CT, 72u);
      wait_vm<0>();
      ld.pf_pin<21, 9>();
      MX(6, 25);
      bar();
      FRESH();
      ld.wl = wave - 8;
      ld.p35(smem, ws, w_sp, layer, bid, MI, NI, RB, CT, HP MK_PASS);
    }
  } else {
    MF(12);
    math_start(smem, b_sp, wqkv, ws, bid, wave, lane_id());
    MF(13);
#pragma unroll 1
    for (int layer = 0; layer < 2; ++layer) {
      FRESH();
      {
        const int lane = lane_id(), r0 = RT * 128;
        float* csq = reinterpret_cast<float*>(ws + OFF_CSQ + bid * 512);
        Attn at;
        at.setup(lds0 + (unsigned)(layer ? K1_T_OFF : K1R_T_OFF), ws + OFF_O + (size_t)layer * SZ_X, r0, HP, wave, lane);
        if (layer) {
          if ((wave & 2) == 0) k1_gemm<2, 4>(smem, ws, RT, HP, (wave & 1) * 2 + (wave & 4), 0, wave >= 4, lane MK_PASS);
          else if (wave < 4) k1_gemm<3, 3>(smem, ws, RT, HP, (wave & 1) * 3, 4, false, lane MK_PASS);
          else if (wave == 6) k1_gemm<2, 3>(smem, ws, RT, HP, 6, 4, true, lane MK_PASS);
          else k1_gemm<1, 7>(smem, ws, RT, HP, 8, 0, true, lane MK_PASS);
        } else {
          if (wave == 0) k1_gemm_raw<3, false>(smem, HP, 0, lane MF_PASS);
          else if (wave < 4) k1_gemm_raw<2, false>(smem, HP, 1 + 2 * wave, lane MF_PASS);
          else if (wave < 7) k1_gemm_raw<2, true>(smem, HP, 2 * (wave - 4), lane MF_PASS);
          else k1_gemm_raw<3, true>(smem, HP, 6, lane MF_PASS);
        }
        msync(lds0 + (unsigned)(LW_OFF + (LW_T + layer) * 4));
        MK(3);
        at.run(r0, HP, wave, lane);
        if (layer == 0) {
          if (wave == 0 && lane < 32)
            asm volatile("global_store_dwordx4 %0, %1, %2 sc1\n\ts_nop 1" ::"v"((unsigned)lane * 16u),
                         "v"(*reinterpret_cast<const u32x4*>(smem + K1R_CS_OFF + lane * 16)), "s"(uni_ptr((const char*)csq))
                         : "memory");
          lds_spin(lds0 + (unsigned)(LW_OFF + LW_HELLO * 4), 1u);
        }
        MK(4);
        announce(lds0 + (unsigned)(LW_OFF + (LW_O + layer * 22 + (wave >> 2)) * 4), 4u, ws, CN_O + layer, (RT * 2 + (wave >> 2)) * 8 + lane,
                 lane < 8);
        MK(5);
        bar();
      }
      FRESH();
      {
        if (wave < 4) {
          const int lane = lane_id();
          u32x2 xv[5];
          ResIn ri;
          ri.setup(layer ? ws + OFF_X1 + SZ_X : (const char*)x0, layer == 0, RB, CT, wave, lane);
          ri.load(xv);
          ResOut ro;
          ro.setup(ws + OFF_X1 + (size_t)layer * 2 * SZ_X, ws + OFF_XS + (size_t)layer * 2 * SZ_XS, out, false, RB, CT, wave, lane);
          f32x4 acc[2][5];
          p2_gemm(acc, xv, lds0, uni_ptr(ws + OFF_O + (size_t)layer * SZ_X + (size_t)RB * (72 * 1024)), layer, wave, lane MK_PASS);
          MK(7);
          ro.run(acc[0], false);
          MK(8);
          announce(lds0 + (unsigned)(LW_OFF + (LW_X1 + layer) * 4), 4u, ws, CN_X1 + layer, (RB >> 1) * 16 + lane, lane < 16);
          MK(9);
          bar();
          FRESH();
          if (wave == 3) cnt_wait(ws, CN_X1 + layer, MI * 16 + NI, 16u);
          bar();
          MK(10);
          p3_g0(smem, ws, layer, MI, NI, wave, lane_id() MK_PASS);
        } else {
          if (layer && wave == 5) cnt_wait(ws, CN_O + layer, RB * 8 + CT, 16u);
          bar();
          k3_const(smem, ws, b_sp, layer, bid, MI, NI, wave & 3);
          MX(6, 26);
          bar();
          MK(10);
          FRESH();
          p3_g1(smem, ws, layer, MI, wave & 3, lane_id() MK_PASS);
        }
      }
      FRESH();
      {
        const int lane = lane_id();
        if (wave < 4) {
          u32x2 xv[5];
          ResIn ri;
          ri.setup(ws + OFF_X1 + (size_t)layer * 2 * SZ_X, false, RB, CT, wave, lane);
          f32x4 acc[2][5];
          ResOut ro;
          ro.setup(ws + OFF_X1 + SZ_X, ws + OFF_XS + SZ_XS, out, layer != 0, RB, CT, wave, lane);
          ResX<36, 33, 8, P5_W, P5_XW, P5_X0, 3, P5_BAR> gm(acc);
          gm.setup(lds0, uni_ptr(ws + OFF_H + (size_t)layer * SZ_Z + (size_t)RB * (288 * 1024)), wave, lane, (unsigned)P5_RED, layer + 1u);
          announce(lds0 + (unsigned)(LW_OFF + (LW_H + layer * 2 + (wave >> 1)) * 4), 2u, ws, ((NI >= 8) ? CN_H2 : CN_H) + layer,
                   (MI * 2 + (wave >> 1)) * 8 + lane, lane < 8);
          MK(19);
          bar();
          ri.load(xv);
          gm.prow();
          place();
          bar();
          MK(20);
          gm.prox<0>(std::make_integer_sequence<int, P5_X0>{});
          gm.init<2 * P5_X0>(xv);
          __builtin_amdgcn_sched_barrier(0);
          gm.run();
          MK(21);
          ro.run(acc[0], layer != 0);
          MK(22);
          if (layer == 0) {
            announce(lds0 + (unsigned)(LW_OFF + LW_X2 * 4), 4u, ws, CN_X2, (RB >> 1) * 16 + lane,
                     lane < 16 || (lane < 32 && (RB & 1) && RB < 31));
            MK(23);
          }
        } else {
          bar();
          bar();
#pragma unroll
          for (int i = 0; i < 7; ++i) {
            if (i == 1 && wave == 5) {
              unsigned f;
              const char* cp = uni_ptr(ws + OFF_CNT + (size_t)((CN_H2 + layer) * 256 + RB * 8 + CT) * 128);
              asm volatile("global_load_dword %0, %1, %2 sc1" : "=v"(f) : "v"(vconst(0u)), "s"(cp) : "memory");
              bar();
              asm volatile("s_waitcnt vmcnt(0)" : "+v"(f) : : "memory");
              if (__builtin_amdgcn_readfirstlane(f) != 7u) cnt_wait(ws, CN_H2 + layer, RB * 8 + CT, 8u);
              continue;
            }
            asm volatile("s_waitcnt lgkmcnt(0)" ::: "memory");
            bar();
          }
          MK(22);
        }
        if (layer == 0 && wave < 4) bar();
      }
    }
  }
}

}

extern "C" void kernel_launch(void* const* d_in, const int* in_sizes, int n_in, void* d_out, int out_size,
                              void* d_ws, size_t ws_size, hipStream_t stream) {
  const u16* x0 = (const u16*)d_in[0];
  const u16* wqkv = (const u16*)d_in[1];
  const u16* wo = (const u16*)d_in[2];
  const u16* w_in = (const u16*)d_in[4];
  const u16* w_sp = (const u16*)d_in[5];
  const u16* b_sp = (const u16*)d_in[6];
  const u16* w_out = (const u16*)d_in[7];
  char* ws = (char*)d_ws;
  (void)hipFuncSetAttribute((const void*)k_main, hipFuncAttributeMaxDynamicSharedMemorySize, LDS_ALL);
  k_main<<<256, 768, LDS_ALL, stream>>>(x0, wqkv, wo, w_in, w_sp, b_sp, w_out, (u16*)d_out, ws);
}
```

```cpp
#include <hip/hip_runtime.h>
#include <hip/hip_bf16.h>
#include <stdint.h>

#define DEVI __device__ __forceinline__

typedef unsigned short u16;
typedef __attribute__((ext_vector_type(8))) short s16x8;
typedef __attribute__((ext_vector_type(4))) short s16x4;
typedef __attribute__((ext_vector_type(4))) float f32x4;
typedef __attribute__((ext_vector_type(2))) float f32x2;
typedef __attribute__((ext_vector_type(2))) __bf16 bf16x2;
typedef __attribute__((ext_vector_type(2))) unsigned u32x2;
typedef __attribute__((ext_vector_type(4))) unsigned u32x4;

#define MK(I) ((void)0)
#define MX(S, I) ((void)0)
#define MF(I) ((void)0)
#define MF_ARG
#define MF_PASS
#define MK_ARG
#define MK_PASS

#define SPIN_MEM (1u << 22)
#define SAFE_OUT(L)
#define SAFE_AND
#define NOP_(n) "=s"(n)

namespace {

constexpr int S_ = 2048, D_ = 576, F_ = 4608, FH_ = 2304;
constexpr int NQKV = 3 * D_;

constexpr size_t SZ_X = (size_t)S_ * D_ * 2;
constexpr size_t SZ_Z = (size_t)S_ * FH_ * 2;
constexpr size_t OFF_X1 = 0;
constexpr size_t OFF_O = OFF_X1 + 3 * SZ_X;
constexpr size_t OFF_Z2 = OFF_O + 2 * SZ_X;
constexpr size_t OFF_H = OFF_Z2 + 2 * SZ_Z;
constexpr size_t SZ_XS = (size_t)S_ * 8 * 8;
constexpr size_t OFF_XS = OFF_H + 2 * SZ_Z;
constexpr size_t OFF_CSQ = OFF_XS + 3 * SZ_XS;
constexpr size_t OFF_ZP = OFF_CSQ + 256 * 512;
constexpr size_t SZ_ZP = (size_t)16 * 16 * 1024;
constexpr size_t OFF_PQ = OFF_ZP + 2 * SZ_ZP;
constexpr size_t OFF_PO = OFF_PQ + (size_t)16 * 9 * 64 * 112 * 2;
constexpr size_t OFF_PI = OFF_PO + (size_t)D_ * D_ * 2;
constexpr size_t OFF_PW = OFF_PI + (size_t)32 * 9 * 64 * 144 * 2;
constexpr size_t OFF_CSP = OFF_PW + (size_t)FH_ * D_ * 2;
constexpr size_t OFF_CNT = OFF_CSP + (size_t)18 * F_ * 4;
constexpr int CN_X1 = 0, CN_H = 2, CN_X2 = 4, CN_O = 5, CN_Z = 7, CN_PK = 9, CN_H2 = 10, NCNT = 12;
constexpr size_t OFF_TAG = OFF_CNT + (size_t)NCNT * 256 * 128;
constexpr size_t OFF_HELLO = OFF_TAG + 128;
constexpr size_t OFF_POT = OFF_HELLO + 2048;
constexpr size_t OFF_CSK = OFF_POT + 1024;
constexpr size_t WS_END = OFF_CSK + 256 * 2048;

DEVI float bf_lo(unsigned u) { return __uint_as_float(u << 16); }
DEVI float bf_hi(unsigned u) { return __uint_as_float(u & 0xffff0000u); }
DEVI unsigned pack2(float a, float b) {
  f32x2 v = {a, b};
  bf16x2 r = __builtin_convertvector(v, bf16x2);
  return __builtin_bit_cast(unsigned, r);
}
DEVI float rbf(float a) {
  return bf_lo(pack2(a, 0.f));
}

DEVI float sum_g(float x) {
  const unsigned u = __float_as_uint(x);
  const auto p = __builtin_amdgcn_permlane32_swap(u, u, false, false);
  const unsigned v = __float_as_uint(__uint_as_float(p[0]) + __uint_as_float(p[1]));
  const auto q = __builtin_amdgcn_permlane16_swap(v, v, false, false);
  return __uint_as_float(q[0]) + __uint_as_float(q[1]);
}

DEVI unsigned vconst(unsigned c) {
  unsigned r;
  asm volatile("v_mov_b32 %0, %1" : "=v"(r) : "s"(c));
  return r;
}
DEVI void place() {
  __builtin_amdgcn_sched_barrier(0);
  asm volatile(".p2align 6");
  __builtin_amdgcn_sched_barrier(0);
}
DEVI const char* uni_ptr(const char* p) {
  unsigned long long v = (unsigned long long)p;
  unsigned lo = __builtin_amdgcn_readfirstlane((unsigned)v), hi = __builtin_amdgcn_readfirstlane((unsigned)(v >> 32));
  asm("s_nop 4" : "+s"(lo), "+s"(hi));
  return (const char*)(((unsigned long long)hi << 32) | lo);
}

DEVI void dma16(const char* sbase, unsigned voff, unsigned m0val) {
  asm volatile("s_mov_b32 m0, %2\n\ts_nop 0\n\tglobal_load_lds_dwordx4 %0, %1"
               :
               : "v"(voff), "s"(sbase), "s"(m0val)
               : "memory");
}

DEVI void ln_finish(float s, float q, float invn, float& mb, float& rinv) {
  float mean = s * invn;
  asm("" : "+v"(mean));
  float var = fmaxf(__builtin_fmaf(-mean, mean, q * invn), 0.f);
  mb = rbf(mean);
  float v = rbf(var);
  float t = rbf(v + 1e-5f);
  float sd = rbf(__builtin_amdgcn_sqrtf(t));
  rinv = __builtin_amdgcn_rcpf(sd);
}

DEVI unsigned pack2v(f32x2 v) { return pack2(v[0], v[1]); }

template <int BM_, int BN_>
struct GemmCfg {
  static constexpr int BM = BM_, BN = BN_, NS = 4;
  static constexpr int P = BN * 2;
  static constexpr int XB = BM * 128, WB = 64 * P, SB = XB + WB;
  static constexpr int LDS_BYTES = NS * SB;
};

DEVI int swap23(int r) { return (r & ~12) | ((r & 4) << 1) | ((r & 8) >> 1); }

DEVI void lds_wr32(unsigned addr, unsigned v) { asm volatile("ds_write_b32 %0, %1" ::"v"(addr), "v"(v) : "memory"); }

template <class C, int MT, int NT, int D, int R, bool XCM, int WT, bool SQ = false, unsigned long long WS = 0>
struct GemmMath {
  static constexpr int WO = WT ? 1 : 2;
  static constexpr int NS = C::NS, SB = C::SB, P = C::P, NU = 2 * NT;
  static constexpr int XPU = MT <= 2 ? MT : MT - 2;
  static constexpr int XU = (MT + XPU - 1) / XPU;
  static_assert(NU % R == 0 && R > D && D <= NT && XU <= D, "shape");
  s16x4 wlo[R], whi[R];
  s16x8 wf[R];
  s16x8 xf[2][MT];
  unsigned xrd[2], wrd, xa[2], wa, so;
  f32x4 (&acc)[MT][NT];
  f32x4 accq[SQ ? MT : 1];
  unsigned long long wtab = WS;
  DEVI GemmMath(f32x4 (&a)[MT][NT]) : acc(a) {}

  template <int PU>
  DEVI void wread_lo() {
    constexpr int off = (PU / NT) * 32 * P + (PU % NT) * (WT == 2 ? 1024 : 32);
    if constexpr (WT) asm volatile("ds_read_b128 %0, %1 offset:%2" : "=v"(wf[PU % R]) : "v"(wa), "n"(off));
    else asm volatile("ds_read_b64_tr_b16 %0, %1 offset:%2" : "=v"(wlo[PU % R]) : "v"(wa), "n"(off));
  }
  template <int PU>
  DEVI void wread_hi() {
    if constexpr (!WT)
      asm volatile("ds_read_b64_tr_b16 %0, %1 offset:%2" : "=v"(whi[PU % R]) : "v"(wa), "n"((PU / NT) * 32 * P + (PU % NT) * 32 + 8 * P));
  }
  template <int H, int M>
  DEVI void xread1() {
    asm volatile("ds_read_b128 %0, %1 offset:%2" : "=v"(xf[H][M]) : "v"(xa[H]), "n"(M * (XCM ? 256 : 2048)));
  }
  static constexpr int xops(int v) {
    const int k = (v + D) % NT;
    if (k >= XU) return 0;
    const int n = MT - k * XPU;
    return n < XPU ? n : XPU;
  }
  static constexpr int ops(int v, bool last) { return (last && v + D >= NU) ? 0 : WO + xops(v); }
  static constexpr int newer(int U, bool last) {
    int n = 0;
    int v0 = U - D;
    if (U % NT == 0) v0 += XU - 1;
    else n += v0 >= 0 ? xops(v0) : xops(v0 + NU);
    for (int v = v0 + 1; v < U; ++v) n += v >= 0 ? ops(v, last) : ops(v + NU, false);
    return n;
  }
  template <int U, int M_>
  DEVI void mf() {
    constexpr int M = ((U % NT) & 1) ? MT - 1 - M_ : M_;
    s16x8 w;
    if constexpr (WT) w = wf[U % R];
    else w = __builtin_shufflevector(wlo[U % R], whi[U % R], 0, 1, 2, 3, 4, 5, 6, 7);
    acc[M][U % NT] = __builtin_amdgcn_mfma_f32_16x16x32_bf16(w, xf[U / NT][M], acc[M][U % NT], 0, 0, 0);
    __builtin_amdgcn_sched_barrier(0);
  }
  DEVI void pin() {
#pragma unroll
    for (int m = 0; m < MT; ++m)
#pragma unroll
      for (int n = 0; n < NT; ++n) asm volatile("" : "+v"(acc[m][n]));
  }
  template <int U, int I>
  DEVI void dsop() {
    constexpr int PU = (U + D) % NU;
    if constexpr (I == 0) wread_lo<PU>();
    else if constexpr (I == 1) wread_hi<PU>();
    else if constexpr (I - 2 < xops(U)) {
      constexpr int k = (U + D) % NT;
      xread1<((U + D - k) % NU) / NT, k * XPU + I - 2>();
    }
  }
  template <int U, bool LAST, bool BAR, int M>
  DEVI void slot() {
    constexpr bool PF = !(LAST && U + D >= NU);
    mf<U, M>();
    if constexpr (M == 0 && PF && U + D == NU) {
      if constexpr (BAR) __builtin_amdgcn_s_barrier();
      so = (so == (unsigned)((NS - 1) * SB)) ? 0u : so + (unsigned)SB;
      wa = wrd + (WS ? ((unsigned)wtab & 63u) << 11 : so);
      wtab >>= 6;
      xa[0] = xrd[0] + so;
      xa[1] = xrd[1] + so;
      __builtin_amdgcn_sched_barrier(0);
    }
    if constexpr (PF) {
      dsop<U, M>();
      if constexpr (M == MT - 1) {
        if constexpr (MT <= 1) dsop<U, 1>();
        if constexpr (MT <= 2) {
          dsop<U, 2>();
          dsop<U, 3>();
        }
      }
      __builtin_amdgcn_sched_barrier(0);
    }
  }
  template <int U, bool LAST, bool BAR, int... M>
  DEVI void unit(std::integer_sequence<int, M...>) {
    static_assert(newer(U, LAST) <= 15, "lgkmcnt range");
    asm volatile("s_waitcnt lgkmcnt(%0)" ::"n"(newer(U, LAST)));
    __builtin_amdgcn_sched_barrier(0);
    (slot<U, LAST, BAR, M>(), ...);
    if constexpr (SQ && (U % NT) < MT) {
      constexpr int m = U % NT;
      accq[m] = __builtin_amdgcn_mfma_f32_16x16x32_bf16(xf[U / NT][m], xf[U / NT][m], accq[m], 0, 0, 0);
      __builtin_amdgcn_sched_barrier(0);
    }
    __builtin_amdgcn_sched_barrier(0);
  }
  template <bool LAST, bool BAR, int... U>
  DEVI void stage(std::integer_sequence<int, U...>) {
    (unit<U, LAST, BAR>(std::make_integer_sequence<int, MT>{}), ...);
  }
  template <int... U>
  DEVI void prow(std::integer_sequence<int, U...>) {
    ((wread_lo<U>(), wread_hi<U>()), ...);
  }
  template <int... M>
  DEVI void prox(std::integer_sequence<int, M...>) {
    (xread1<0, M>(), ...);
  }
  DEVI void setup(unsigned lds0, int mt0, int nt0, int lane, unsigned so0) {
    const int g = lane >> 4, r = lane & 15, q = (lane >> 2) & 3, p = lane & 3;
#pragma unroll
    for (int h = 0; h < 2; ++h)
      xrd[h] = XCM ? lds0 + (unsigned)((4 * h + g) * (C::BM * 16) + (mt0 * 16 + r) * 16)
                   : lds0 + (unsigned)((mt0 * 16 + r) * 128 + (((4 * h + g) ^ ((r >> 1) & 7)) * 16));
    wrd = WT == 2   ? lds0 + (unsigned)(C::XB + nt0 * 1024 + lane * 16)
          : WT == 1 ? lds0 + (unsigned)(C::XB + (g * 8 + (r & 7)) * P + (r >> 3) * 16 + nt0 * 32)
                    : lds0 + (unsigned)(C::XB + (16 * (g >> 1) + 4 * (g & 1) + q) * P + 8 * p + nt0 * 32);
    so = so0;
    xa[0] = xrd[0] + so;
    xa[1] = xrd[1] + so;
    wa = wrd + so;
  }
  DEVI void first() {
    prox(std::make_integer_sequence<int, MT>{});
    prow(std::make_integer_sequence<int, D>{});
    asm volatile("s_waitcnt lgkmcnt(0)");
  }
  template <int N, bool BAR = true>
  DEVI void mid() {
    if constexpr (N > 1) place();
#pragma unroll 1
    for (int s = 0; s < N; ++s) stage<false, BAR>(std::make_integer_sequence<int, NU>{});
  }
  DEVI void last() {
    stage<true, true>(std::make_integer_sequence<int, NU>{});
    pin();
  }
  template <int KS>
  DEVI void run(unsigned lds0, int mt0, int nt0, int lane, unsigned so0) {
    setup(lds0, mt0, nt0, lane, so0);
    __builtin_amdgcn_s_barrier();
    first();
    mid<KS - 1>();
    last();
  }
};

DEVI const char* code_here() {
  unsigned long long pc;
  asm volatile("s_getpc_b64 %0" : "=s"(pc));
  return (const char*)pc;
}
DEVI int okmap(int p) {
  const int ch = p >> 3, e = p & 7, c = (ch - 8) & 3;
  return ch < 8 ? (2 * ch + (e >> 2)) * 36 + 16 + (e & 3) : ((ch - 8) >> 2) * 36 + c * 8 + (c >> 1) * 4 + e;
}

DEVI void pk_st16(void* p, u32x4 v) {
  asm volatile("global_store_dwordx4 %0, %1, off sc1\n\ts_nop 1" ::"v"(p), "v"(v) : "memory");
}

using K1C = GemmCfg<144, 112>;
constexpr int K1_TP = 240;
constexpr int K1_T_OFF = K1C::SB;
constexpr int K1_ST_OFF = 163840 - 128 - 1152;
constexpr int K1_CS_OFF = 4 * K1C::SB;
constexpr int K1_WX = 4 * K1C::SB + 2048;
constexpr unsigned long long K1_WS = 16ull | 32ull << 6 | 48ull << 12 | 56ull << 18 | 63ull << 24 | 16ull << 30 | 32ull << 36 | 48ull << 42;
static_assert(K1C::SB == 16 * 2048 && K1_WX - K1C::XB == 56 * 2048 && K1C::WB == 7 * 2048 && K1_WX + 2 * K1C::WB <= K1_ST_OFF, "K1 w places");
static_assert(K1_T_OFF + 144 * K1_TP + 64 <= 2 * K1C::SB + K1C::XB, "K1 LDS alias");
using K1R = GemmCfg<144, 144>;
constexpr int K1R_T_OFF = K1R::SB;
constexpr int K1R_CS_OFF = K1R_T_OFF + 144 * K1_TP;
constexpr int K1R_ST_OFF = K1R_CS_OFF + 512;
constexpr int K1R_DUMMY = K1R::LDS_BYTES;
constexpr int K1R_PO = K1R_DUMMY + 1024;
constexpr int K1R_CNT = 163840 - 128 + 28 * 4;
#define SPIN_ASM(TEST)                                                                                                   \
  if constexpr (SL <= 1)                                                                                                 \
    asm volatile(".La%=:\n\tds_read_b32 %0, %3\n\ts_waitcnt lgkmcnt(0)\n\tv_readfirstlane_b32 %2, %0\n\t" TEST SAFE_AND \
                 "s_cbranch_scc1 .La%="                                                                                  \
                 : "=&v"(v), NOP_(c), "=&s"(t)                                                                           \
                 : "v"(addr), "s"(k)                                                                                     \
                 : "memory", "scc");                                                                                     \
  else                                                                                                                   \
    asm volatile("s_branch .Lb%=\n.La%=:\n\ts_sleep %5\n.Lb%=:\n\tds_read_b32 %0, %3\n\ts_waitcnt lgkmcnt(0)\n\t"     \
                 "v_readfirstlane_b32 %2, %0\n\t" TEST SAFE_AND "s_cbranch_scc1 .La%="                                  \
                 : "=&v"(v), NOP_(c), "=&s"(t)                                                                           \
                 : "v"(addr), "s"(k), "n"(SL)                                                                            \
                 : "memory", "scc")
template <int SL>
DEVI void spin_ge(unsigned addr, unsigned k) {
  unsigned v, c = 1u << 18, t;
  SPIN_ASM("s_cmp_lt_u32 %2, %4\n\t");
}
template <int SL>
DEVI void spin_b4(unsigned addr, unsigned n) {
  unsigned v, c = 1u << 18, t;
  const unsigned k = (0x80u - n) * 0x01010101u;
  SPIN_ASM("s_add_u32 %2, %2, %4\n\ts_andn2_b32 %2, 0x80808080, %2\n\t");
}
DEVI void msync(unsigned addr) {
  asm volatile("s_waitcnt lgkmcnt(0)" ::: "memory");
  asm volatile("s_mov_b64 exec, 1\n\tds_add_u32 %0, %1\n\ts_mov_b64 exec, -1" ::"v"(addr), "v"(vconst(1u)) : "memory");
  spin_ge<0>(addr, 8u);
}
static_assert(K1R_PO + 13824 + 256 <= 163840 - 128 && K1R_ST_OFF + 144 * 8 <= 3 * K1R::SB, "K1R LDS");

DEVI void touch64(const char* sbase, unsigned voff, unsigned m0val) {
  asm volatile("s_mov_b32 m0, %2\n\ts_nop 0\n\tglobal_load_lds_dword %0, %1" : : "v"(voff), "s"(sbase), "s"(m0val) : "memory");
}

template <int MT, bool G1>
DEVI void k1_gemm_raw(char* smem, int hp, int mt0, int lane MF_ARG) {
  constexpr int NT = 4, nt0 = G1 ? 4 : 0;
  const unsigned lds0 = (unsigned)(size_t)smem;
  const int g = lane >> 4, r = lane & 15;
  unsigned ta[NT];
#pragma unroll
  for (int nt = 0; nt < NT; ++nt) {
    const int j = (nt0 + nt) * 16 + g * 4, seg = j / 40, cj = j - seg * 40 - (hp & 1) * 4;
    const int to = (seg < 3 && cj >= 0 && cj < 36) ? seg * 80 + cj * 2 + (cj >= 20 ? 8 : 0) : (seg < 3 ? 40 : 120);
    ta[nt] = lds0 + (unsigned)(K1R_T_OFF + (mt0 * 16 + r) * K1_TP + to);
    asm volatile("" : "+v"(ta[nt]));
  }
  unsigned sa = lds0 + (unsigned)(K1R_ST_OFF + (mt0 * 16 + r) * 8), ca = lds0 + (unsigned)(K1R_CS_OFF + (nt0 * 16 + g * 4) * 4);
  asm volatile("" : "+v"(sa), "+v"(ca));
  f32x4 acc[MT][NT];
#pragma unroll
  for (int mt = 0; mt < MT; ++mt)
#pragma unroll
    for (int nt = 0; nt < NT; ++nt) acc[mt][nt] = f32x4{0.f, 0.f, 0.f, 0.f};
  {
    GemmMath<K1R, MT, 4, 3, 4, false, 0, G1> gm(acc);
    if constexpr (G1) {
#pragma unroll
      for (int mt = 0; mt < MT; ++mt) gm.accq[mt] = f32x4{0.f, 0.f, 0.f, 0.f};
    }
    MF(14);
    if constexpr (!G1) {
      if (mt0 < 7) asm volatile("s_waitcnt vmcnt(0)" ::: "memory");
    }
    MF(15);
    gm.template run<9>(lds0, mt0, nt0, lane, 0u);
    MF(2);
    if constexpr (G1) {
#pragma unroll
      for (int mt = 0; mt < MT; ++mt) {
        const f32x4 q = gm.accq[mt];
        const float d = (r & 2) ? ((r & 1) ? q[3] : q[2]) : ((r & 1) ? q[1] : q[0]);
        const float qq = sum_g(g == (r >> 2) ? d : 0.f);
        float mb, rinv;
        ln_finish(acc[mt][3][0], qq, 1.0f / (float)D_, mb, rinv);
        const f32x2 st = {-mb, rinv};
        if (g == 2) asm volatile("ds_write_b64 %0, %1 offset:%2" ::"v"(sa), "v"(st), "n"(mt * 128) : "memory");
      }
    }
  }
  if (mt0 == 0 && r == 0) {
#pragma unroll
    for (int nt = 0; nt < NT; ++nt) *reinterpret_cast<f32x4*>(smem + K1R_CS_OFF + ((nt0 + nt) * 16 + g * 4) * 4) = acc[0][nt];
  }
  msync(lds0 + (unsigned)K1R_CNT);
  MF(3);
  f32x2 st[MT];
  f32x4 cs[NT];
#pragma unroll
  for (int nt = 0; nt < NT; ++nt) asm volatile("ds_read_b128 %0, %1 offset:%2" : "=v"(cs[nt]) : "v"(ca), "n"(nt * 64) : "memory");
#pragma unroll
  for (int mt = 0; mt < MT; ++mt) asm volatile("ds_read_b64 %0, %1 offset:%2" : "=v"(st[mt]) : "v"(sa), "n"(mt * 128) : "memory");
#pragma unroll
  for (int i = 0; i < MT; ++i) asm volatile("s_waitcnt lgkmcnt(0)" : "+v"(st[i]));
#pragma unroll
  for (int i = 0; i < NT; ++i) asm volatile("" : "+v"(cs[i]));
#pragma unroll
  for (int mt = 0; mt < MT; ++mt) {
    f32x2 M = {st[mt][0], st[mt][0]}, R = {st[mt][1], st[mt][1]};
    asm volatile("" : "+v"(M), "+v"(R));
#pragma unroll
    for (int nt = 0; nt < NT; ++nt) {
      const f32x2 v0 = R * __builtin_elementwise_fma(M, f32x2{cs[nt][0], cs[nt][1]}, f32x2{acc[mt][nt][0], acc[mt][nt][1]});
      const f32x2 v1 = R * __builtin_elementwise_fma(M, f32x2{cs[nt][2], cs[nt][3]}, f32x2{acc[mt][nt][2], acc[mt][nt][3]});
      acc[mt][nt][0] = __uint_as_float(pack2v(v0));
      acc[mt][nt][1] = __uint_as_float(pack2v(v1));
      const f32x2 pk = {acc[mt][nt][0], acc[mt][nt][1]};
      asm volatile("ds_write_b64 %0, %1 offset:%2" ::"v"(ta[nt]), "v"(pk), "n"(mt * 16 * K1_TP) : "memory");
    }
  }
}

template <int N>
DEVI void wait_vm() {
  asm volatile("s_waitcnt vmcnt(%0)" ::"n"(N) : "memory");
}

constexpr int LDS_ALL = 163840;
constexpr int LW_OFF = LDS_ALL - 128;
constexpr int LW_GO = 0, LW_T = 2, LW_O = 4  , LW_X1 = 6, LW_H = 8, LW_X2 = 12, LW_PK = 13, LW_Z = 14, LW_PAIR = 16, LW_AD = 20,
              LW_L = 22, LW_S = 24, LW_ST = 29, LW_HELLO = 30, LW_Q = 31;
DEVI void announce(unsigned addr, unsigned n, char* ws, int kind, int who, bool act) {
  const char* cb = uni_ptr(ws + OFF_CNT + (size_t)kind * 256 * 128);
  const unsigned long long mask = __ballot(act);
  unsigned t, u;
  asm volatile(
      "s_waitcnt vmcnt(0)\n\t"
      "s_mov_b64 exec, 1\n\t"
      "ds_add_rtn_u32 %0, %2, %3\n\t"
      "s_waitcnt lgkmcnt(0)\n\t"
      "v_readfirstlane_b32 %1, %0\n\t"
      "s_cmp_eq_u32 %1, %4\n\t"
      "s_cselect_b64 exec, %5, 0\n\t"
      "global_atomic_add %6, %3, %7 sc1\n\t"
      "s_mov_b64 exec, -1\n\t"
      "s_nop 0"
      : "=&v"(t), "=&s"(u)
      : "v"(addr), "v"(vconst(1u)), "s"(n - 1u), "s"(mask), "v"((unsigned)(who & 255) * 128u), "s"(cb)
      : "memory", "scc");
}
DEVI void cnt_wait(const char* ws, int kind, int who, unsigned n) {
  const char* cp = uni_ptr(ws + OFF_CNT + (size_t)(kind * 256 + who) * 128);
  unsigned v, c = SPIN_MEM, t;
  asm volatile(
      ".La%=:\n\tglobal_load_dword %0, %3, %4 sc1\n\ts_sub_u32 %1, %1, 1\n\ts_waitcnt vmcnt(0)\n\tv_readfirstlane_b32 %2, %0\n\ts_cmp_lg_u32 %2, %5\n\t"
      "s_cselect_b32 %2, %1, 0\n\ts_cmp_lg_u32 %2, 0\n\ts_cbranch_scc1 .La%="
      : "=&v"(v), "+s"(c), "=&s"(t)
      : "v"(vconst(0u)), "s"(cp), "s"(n - 1u)
      : "memory", "scc");
}
DEVI void dma16_sc1(const char* sbase, unsigned voff, unsigned m0val) {
  asm volatile("s_mov_b32 m0, %2\n\ts_nop 0\n\tglobal_load_lds_dwordx4 %0, %1 sc1"
               :
               : "v"(voff), "s"(sbase), "s"(m0val)
               : "memory");
}
template <int N, bool SC1>
DEVI void dma4(const char* sbase, unsigned v0, unsigned v1, unsigned v2, unsigned v3, unsigned m0val) {
#define DMA4(S)                                                                                                               \
  asm volatile("s_mov_b32 m0, %5\n\ts_nop 0\n\tglobal_load_lds_dwordx4 %0, %4" S "\n\t.if %6 > 1\n\tglobal_load_lds_dwordx4 %1, %4 offset:1024" S \
               "\n\t.endif\n\t.if %6 > 2\n\tglobal_load_lds_dwordx4 %2, %4 offset:2048" S                                    \
               "\n\t.endif\n\t.if %6 > 3\n\tglobal_load_lds_dwordx4 %3, %4 offset:3072" S "\n\t.endif"                      \
               :                                                                                                              \
               : "v"(v0), "v"(v1), "v"(v2), "v"(v3), "s"(sbase), "s"(m0val), "n"(N)                                           \
               : "memory")
  if constexpr (SC1) DMA4(" sc1");
  else DMA4("");
}
template <int N>
DEVI void dma_seq(const char* src, unsigned voff, unsigned m0val) {
  if constexpr (N > 0) {
    dma4<(N < 4 ? N : 4), true>(src, voff, voff, voff, voff, m0val);
    dma_seq<N - 4>(src + 4096, voff, m0val + 4096u);
  }
}
DEVI void bar() { __builtin_amdgcn_s_barrier(); }
DEVI void lds_inc(unsigned addr, unsigned by = 1u) {
  asm volatile("s_mov_b64 exec, 1\n\tds_add_u32 %0, %1\n\ts_mov_b64 exec, -1" ::"v"(addr), "v"(vconst(by)) : "memory");
}
DEVI void lds_spin(unsigned addr, unsigned n) {
  spin_ge<1>(addr, n);
}
template <int SL = 1>
DEVI void lds_spin4(unsigned addr, unsigned n) {
  spin_b4<SL>(addr, n);
}
DEVI int lane_id() {
  int l;
  asm volatile("v_mbcnt_lo_u32_b32 %0, -1, 0\n\tv_mbcnt_hi_u32_b32 %0, -1, %0" : "=v"(l));
  __builtin_assume((unsigned)l < 64u);
  return l;
}

template <int I>
DEVI u32x4 tr8(const u32x4 (&v)[8]) {
  constexpr unsigned sel = (I & 1) ? 0x07060302u : 0x05040100u;
  return u32x4{__builtin_amdgcn_perm(v[1][I / 2], v[0][I / 2], sel), __builtin_amdgcn_perm(v[3][I / 2], v[2][I / 2], sel),
               __builtin_amdgcn_perm(v[5][I / 2], v[4][I / 2], sel), __builtin_amdgcn_perm(v[7][I / 2], v[6][I / 2], sel)};
}
template <int PITCH, bool SUM, int... I>
DEVI void tr8_store(const u32x4 (&v)[8], unsigned dst, float (&cs)[8], std::integer_sequence<int, I...>) {
  auto one = [&](auto I_) {
    const u32x4 o = tr8<I_>(v);
    const unsigned d_ = dst;
    asm volatile("ds_write_b128 %0, %1 offset:%2" ::"v"(d_), "v"(o), "n"(I_ * PITCH) : "memory");
    if constexpr (SUM) {
#pragma unroll
      for (int d = 0; d < 4; ++d) {
        const unsigned od = o[d];
        cs[I_] = __builtin_amdgcn_fdot2_f32_bf16(__builtin_bit_cast(bf16x2, od), __builtin_bit_cast(bf16x2, 0x3f803f80u), cs[I_], false);
      }
    }
  };
  (one(std::integral_constant<int, I>{}), ...);
}
template <int NB>
DEVI void lds_out(unsigned src, char* dst, int lane) {
  constexpr int N = (NB + 1023) / 1024;
  u32x4 t[N];
  const unsigned a = src + (unsigned)lane * 16u;
#pragma unroll
  for (int i = 0; i < N; ++i) asm volatile("ds_read_b128 %0, %1 offset:%2" : "=v"(t[i]) : "v"(a), "n"(i * 1024) : "memory");
  const char* d = uni_ptr(dst);
#pragma unroll
  for (int i = 0; i < N; ++i) {
    asm volatile("s_waitcnt lgkmcnt(%1)" : "+v"(t[i]) : "n"(N - 1 - i) : "memory");
    if (i < N - 1 || lane < (NB - (N - 1) * 1024) / 16)
      asm volatile("global_store_dwordx4 %0, %1, %2 sc1\n\ts_nop 1" ::"v"((unsigned)lane * 16u), "v"(t[i]), "s"(d + i * 1024) : "memory");
  }
}
template <int A, int B, class F>
DEVI void static_for(F f) {
  if constexpr (A < B) {
    f(std::integral_constant<int, A>{});
    static_for<A + 1, B>(f);
  }
}
constexpr int WSP_V = 3;
constexpr int RAW_S1 = 6;
constexpr int RAW_S0 = 4;
template <int E>
struct RawX {
  static constexpr int first(int s) {
    return s <= RAW_S0 ? 0 : (s > RAW_S1 ? E : (E * (s - RAW_S0) + RAW_S1 - RAW_S0) / (RAW_S1 + 1 - RAW_S0));
  }
  static constexpr int n(int s) { return first(s + 1) - first(s); }
};
struct RawLd {
  static constexpr int VPS = 10, SB = K1R::SB;
  unsigned xoff[5], woff[5], m0x;
  unsigned long long xact0, wact[5], tag;
  const char *xp, *wp;
  int qb;
  DEVI void q_hook(int st, char* ws, unsigned lds0, int hp) {
    if ((qb >> 1) == st) {
      const int lane = lane_id(), g = lane >> 4;
      const unsigned a = lds0 + (unsigned)((st & 3) * SB + K1R::XB + ((qb & 1) * 32 + 16 * (g >> 1) + 4 * (g & 1) + ((lane >> 2) & 3)) * 288 +
                                           (lane & 3) * 8 + (hp & 1) * 8);
      const unsigned a2 = a + ((lane & 3) >= 1 ? 8u : 0u), a4 = a + ((lane & 3) >= 2 ? 8u : 0u);
      u32x2 qf[7][2];
#pragma unroll
      for (int nt = 0; nt < 7; ++nt)
        asm volatile("ds_read_b64_tr_b16 %0, %2 offset:%3\n\tds_read_b64_tr_b16 %1, %2 offset:%4"
                     : "=&v"(qf[nt][0]), "=&v"(qf[nt][1])
                     : "v"(nt == 2 ? a2 : (nt == 4 ? a4 : a)), "n"(nt * 32 + (nt > 4 ? 16 : (nt > 2 ? 8 : 0))),
                       "n"(2304 + nt * 32 + (nt > 4 ? 16 : (nt > 2 ? 8 : 0)))
                     : "memory");
      const char* d = uni_ptr(ws + OFF_PQ + (size_t)(hp * 18 + qb) * 7168 + 3072);
#pragma unroll
      for (int nt = 0; nt < 7; ++nt) {
        asm volatile("s_waitcnt lgkmcnt(%2)" : "+v"(qf[nt][0]), "+v"(qf[nt][1]) : "n"(12 - 2 * nt) : "memory");
        const u32x4 v = {qf[nt][0][0], qf[nt][0][1], qf[nt][1][0], qf[nt][1][1]};
        asm volatile("global_store_dwordx4 %0, %1, %2 offset:%3 sc1\n\ts_nop 1" ::"v"((unsigned)lane * 16u), "v"(v), "s"(d), "n"(nt * 1024 - 3072) : "memory");
      }
      if (st == 8) lds_wr32(lds0 + (unsigned)(LW_OFF + LW_Q * 4), 1u);
    }
  }
  DEVI void stage_x(unsigned so) {
    asm volatile(
        "s_mov_b32 m0, %6\n\ts_mov_b64 exec, %7\n\tglobal_load_lds_dwordx4 %0, %5\n\ts_mov_b64 exec, -1\n\t"
        "global_load_lds_dwordx4 %1, %5 offset:1024\n\tglobal_load_lds_dwordx4 %2, %5 offset:2048\n\tglobal_load_lds_dwordx4 %3, %5 offset:3072\n\t"
        "s_add_u32 m0, m0, 0x1000\n\ts_mov_b32 exec_hi, 0\n\tglobal_load_lds_dwordx4 %4, %5\n\ts_mov_b32 exec_hi, -1" ::"v"(xoff[0]),
        "v"(xoff[1]), "v"(xoff[2]), "v"(xoff[3]), "v"(xoff[4]), "s"(xp), "s"(m0x + so), "s"(xact0)
        : "memory", "scc");
    xp += 128;
  }
  DEVI void stage_w(unsigned so) {
    asm volatile(
        "s_mov_b32 m0, %6\n\ts_mov_b64 exec, %7\n\tglobal_load_lds_dwordx4 %0, %5\n\ts_mov_b64 exec, %8\n\t"
        "global_load_lds_dwordx4 %1, %5 offset:1024\n\ts_mov_b64 exec, %9\n\tglobal_load_lds_dwordx4 %2, %5 offset:2048\n\t"
        "s_mov_b64 exec, %10\n\tglobal_load_lds_dwordx4 %3, %5 offset:3072\n\ts_add_u32 m0, m0, 0x1000\n\ts_mov_b64 exec, %11\n\t"
        "global_load_lds_dwordx4 %4, %5\n\ts_mov_b64 exec, -1" ::"v"(woff[0]),
        "v"(woff[1]), "v"(woff[2]), "v"(woff[3]), "v"(woff[4]), "s"(wp), "s"(m0x + so + (unsigned)K1R::XB), "s"(wact[0]), "s"(wact[1]),
        "s"(wact[2]), "s"(wact[3]), "s"(wact[4])
        : "memory", "scc");
    wp += 64 * NQKV * 2;
  }
  DEVI void stage(unsigned so) {
    stage_x(so);
    stage_w(so);
  }
  DEVI void front(const u16* x, const u16* wqkv, const u16* wo, char* ws, int bid, unsigned lds0, int wl, int lane, int rt, int hp,
                  int pj MF_ARG) {
    xp = uni_ptr((const char*)x - 3072);
    wp = uni_ptr((const char*)wqkv + ((hp * 72) & ~15) - 3072);
    m0x = lds0 + (unsigned)(wl * 288 * 16);
    xact0 = wl ? ~0ull : ~0xffull;
    const int row0 = wl * 36 + (lane >> 3);
#pragma unroll
    for (int i = 0; i < 5; ++i) {
      const int row = row0 + i * 8;
      int gr = rt * 128 - 16 + row;
      if (gr < 0) gr = 0;
      xoff[i] = (unsigned)(gr * (D_ * 2) + (3 - (i & 3)) * 1024) + (unsigned)(((lane ^ (row >> 1)) & 7) * 16);
    }
    stage_x(0u);
    u32x2 tg;
    asm volatile("global_load_dwordx2 %0, %1, %2 sc1" : "=v"(tg) : "v"(0u), "s"(uni_ptr(ws + OFF_TAG)) : "memory");
    asm volatile("" : "+v"(lane));
#pragma unroll
    for (int i = 0; i < 5; ++i) {
      const unsigned c = (unsigned)(i * 64 + (lane & 63));
      const unsigned wr = (c * 3641u) >> 16, wc = c - wr * 18u, seg = (wc * 13u) >> 6;
      wact[i] = __ballot(wc < 15u) & (i < 4 ? ~0ull : 0xffffffffull);
      woff[i] = (unsigned)(wl * 16 + swap23((int)wr)) * (unsigned)(NQKV * 2) + seg * (unsigned)(D_ * 2 - 80) + wc * 16u + (unsigned)((3 - (i & 3)) * 1024);
    }
    stage_w(0u);
    stage((unsigned)SB);
    MF(10);
    asm volatile("s_waitcnt vmcnt(%1)" : "+v"(tg) : "n"(VPS) : "memory");
    tag = (((unsigned long long)(unsigned)__builtin_amdgcn_readfirstlane(tg[1]) << 32) | (unsigned)__builtin_amdgcn_readfirstlane(tg[0])) +
          0x9e3779b97f4a7c15ull;
    MF(1);
    __builtin_amdgcn_s_barrier();
    MF(15);
    if (wl == 0 && lane == 0)
      asm volatile("global_store_dwordx2 %0, %1, off sc1\n\ts_nop 1" ::"v"(ws + OFF_HELLO + bid * 8), "v"(tag) : "memory");
    stage((unsigned)(2 * SB));
    qb = wl == 3 ? rt : ((wl == 2 && rt < 2) ? 16 + rt : 99);
    q_hook(0, ws, lds0, hp);
    const int nb = pj;
    const bool po = pj >= 0 && wl < 2;
    u32x4 v[8];
    unsigned da = 0u;
    if (po) {
      const int l54 = lane < 54 ? lane : 53, gl = l54 / 9, c = l54 - gl * 9, kl = wl * 6 + gl, t = nb / 6, kg = (nb - t * 6) * 12 + kl;
      da = lds0 + (unsigned)(K1R_PO + (kl >> 2) * 4608 + (16 * (kl & 1) + ((kl >> 1) & 1)) * 144 + c * 16);
      const unsigned sa[2] = {(unsigned)(okmap(kg * 8) * D_ + t * 72 + c * 8) * 2u, (unsigned)(okmap(kg * 8 + 4) * D_ + t * 72 + c * 8) * 2u};
      const char* ps = uni_ptr((const char*)wo);
#pragma unroll
      for (int j = 0; j < 8; ++j)
        asm volatile("global_load_dwordx4 %0, %1, %2 offset:%3" : "=v"(v[j]) : "v"(sa[j / 4]), "s"(ps), "n"((j % 4) * D_ * 2) : "memory");
    } else {
#pragma unroll
      for (int j = 0; j < 8; ++j) asm volatile("" : "=v"(v[j]));
    }
    unsigned slot = (unsigned)(3 * SB);
#pragma unroll 1
    for (int S = 0; S < RAW_S0; ++S) {
      wait_vm<VPS>();
      __builtin_amdgcn_s_barrier();
      if (S == 0) MF(16);
      if (S == 1) MF(17);
      if (S == 2) MF(18);
      if (S == 3) MF(19);
      stage(slot);
      slot = (slot == (unsigned)(3 * SB)) ? 0u : slot + (unsigned)SB;
      q_hook(S + 1, ws, lds0, hp);
      if (po && S == 1) {
        float cs[8];
        asm volatile("s_nop 0 ; LOOPWAIT" : "+v"(v[0]), "+v"(v[1]), "+v"(v[2]), "+v"(v[3]), "+v"(v[4]), "+v"(v[5]), "+v"(v[6]), "+v"(v[7]));
        tr8_store<288, false>(v, da, cs, std::make_integer_sequence<int, 8>{});
        asm volatile("s_waitcnt lgkmcnt(0)" ::: "memory");
      }
      if (po && S == 2) lds_out<6912>(lds0 + (unsigned)(K1R_PO + wl * 6912), ws + OFF_PO + (size_t)nb * 13824 + wl * 6912, lane);
    }
  }
};
template <bool JOB>
DEVI void k1_raw_loader(RawLd& ld, const u16* __restrict__ w_in, const u16* __restrict__ w_out, char* __restrict__ ws,
                        unsigned long long tag, int bid, unsigned lds0, int wl, int lane, int rt, int hp, int ct, int job, int pj MF_ARG) {
  constexpr int VPS = RawLd::VPS, SB = RawLd::SB, E = JOB ? 16 : 0;
  const bool isi = job < 144;
  const int kb = isi ? job >> 3 : job - 144;
  const int pitch = isi ? F_ * 2 : D_ * 2;
  const int l36 = lane < 36 ? lane : 35;
  unsigned sa = (unsigned)l36 * 16u, da;
  if (isi) da = lds0 + (unsigned)((l36 >= 18 ? 9216 - 288 : 0) + wl * 8 * 288 + l36 * 16);
  else da = lds0 + (unsigned)((l36 / 9) * (4608 - 144) + (16 * (wl & 1) + (wl >> 1)) * 144 + l36 * 16);
  asm volatile("" : "+v"(sa), "+v"(da));
  u32x4 v[2][8];
  const char* ps = uni_ptr((const char*)(isi ? w_in + (job & 7) * 576 : w_out) + (size_t)(kb * 32 + wl * 8) * pitch);
  auto extra = [&](auto J_) {
    constexpr int j = J_;
    const char* q = ps + (j / 2) * pitch;
    u32x4 t;
    asm volatile("global_load_dwordx4 %0, %1, %2 offset:%3" : "=v"(t) : "v"(sa), "s"(q), "n"((j % 2) * 576) : "memory");
    v[j % 2][j / 2] = t;
  };
  auto step = [&](auto S_) {
    constexpr int S = S_;
    wait_vm<RawX<E>::n(S - 2) + (S <= 6 ? VPS : 0) + RawX<E>::n(S - 1)>();
    __builtin_amdgcn_s_barrier();
    MF(16 + S);
    if constexpr (S == RAW_S0) {
      static_assert(RAW_S0 >= 4, "wo blocks");
      if (pj >= 0 && wl == 0 && lane == 0)
        asm volatile("global_store_dwordx2 %0, %1, off sc1\n\ts_nop 1" ::"v"(ws + OFF_POT + (pj / 6) * 128 + (pj % 6) * 8), "v"(tag) : "memory");
    }
    if constexpr (S <= 5) ld.stage((unsigned)(((S + 3) % 4) * SB));
    ld.q_hook(S + 1, ws, lds0, hp);
    static_for<RawX<E>::first(S), RawX<E>::first(S + 1)>(extra);
  };
  const char* hp_ = ws + OFF_HELLO + lane * 32;
  u32x4 a, b;
  if (wl == 1) asm volatile("global_load_dwordx4 %0, %2, off sc1\n\tglobal_load_dwordx4 %1, %2, off offset:16 sc1" : "=&v"(a), "=&v"(b) : "v"(hp_) : "memory");
  else asm volatile("" : "=v"(a), "=v"(b));
  static_for<RAW_S0, 8>(step);
  MF(2);
  if (wl == 1) {
    asm volatile("" : "+v"(a), "+v"(b));
    const unsigned t0 = (unsigned)tag, t1 = (unsigned)(tag >> 32);
#pragma unroll 1
    for (unsigned spin = 0; spin < SPIN_MEM; ++spin) {
      const unsigned bad = ((a[0] ^ t0) | (a[2] ^ t0) | (b[0] ^ t0) | (b[2] ^ t0)) | ((a[1] ^ t1) | (a[3] ^ t1) | (b[1] ^ t1) | (b[3] ^ t1));
      if (!__any(bad != 0u)) break;
      __builtin_amdgcn_s_sleep(4);
      asm volatile("global_load_dwordx4 %0, %2, off sc1\n\tglobal_load_dwordx4 %1, %2, off offset:16 sc1\n\ts_waitcnt vmcnt(0)"
                   : "=&v"(a), "=&v"(b)
                   : "v"(hp_)
                   : "memory");
    }
    lds_wr32(lds0 + (unsigned)(LW_OFF + LW_HELLO * 4), 1u);
    if (bid == 0 && lane == 0) asm volatile("global_store_dwordx2 %0, %1, off sc1\n\ts_nop 1" ::"v"(ws + OFF_TAG), "v"(tag) : "memory");
  }
  MF(11);
  if (rt < 2) lds_spin(lds0 + (unsigned)(LW_OFF + LW_Q * 4), 1u);
  lds_spin(lds0 + (unsigned)K1R_CNT, 8u);
  if constexpr (JOB) {
#pragma unroll
    for (int b = 0; b < 2; ++b)
      asm volatile("s_waitcnt vmcnt(0)"
                   : "+v"(v[b][0]), "+v"(v[b][1]), "+v"(v[b][2]), "+v"(v[b][3]), "+v"(v[b][4]), "+v"(v[b][5]), "+v"(v[b][6]), "+v"(v[b][7])
                   :
                   : "memory");
  }
  MF(12);
  const char* tp = ws + OFF_POT + ct * 128 + (lane < 6 ? lane : 5) * 8;
  u32x2 f;
  asm volatile("global_load_dwordx2 %0, %1, off sc1" : "=v"(f) : "v"(tp) : "memory");
  if constexpr (JOB) {
    static_assert(SB == 4 * 9216, "staging");
#pragma unroll
    for (int b = 0; b < 2; ++b) {
      float cs[8] = {0.f, 0.f, 0.f, 0.f, 0.f, 0.f, 0.f, 0.f};
      tr8_store<288, true>(v[b], da + (unsigned)(b * 18432), cs, std::make_integer_sequence<int, 8>{});
      const f32x4 c0 = {cs[0], cs[1], cs[2], cs[3]}, c1 = {cs[4], cs[5], cs[6], cs[7]};
      const unsigned ca = lds0 + (unsigned)(3 * SB + wl * 2304) + sa * 2u;
      asm volatile("ds_write_b128 %0, %1 offset:%2" ::"v"(ca), "v"(c0), "n"(b * 1152) : "memory");
      asm volatile("ds_write_b128 %0, %1 offset:%2" ::"v"(ca), "v"(c1), "n"(b * 1152 + 16) : "memory");
    }
    lds_inc(lds0 + (unsigned)(LW_OFF + LW_ST * 4));
    lds_spin(lds0 + (unsigned)(LW_OFF + LW_ST * 4), 4u);
    if (isi) {
      const int j = (job & 7) * 4 + wl, ti = j < 16 ? 2 * j + 1 : 2 * (j - 16);
      lds_out<9216>(lds0 + (unsigned)(wl * 9216), ws + OFF_PI + (size_t)(ti * 18 + kb) * 9216, lane);
      f32x4 c[4];
      const unsigned ca = lds0 + (unsigned)(3 * SB + wl * 576) + sa;
#pragma unroll
      for (int i = 0; i < 4; ++i) asm volatile("ds_read_b128 %0, %1 offset:%2" : "=v"(c[i]) : "v"(ca), "n"(i * 2304) : "memory");
      asm volatile("s_waitcnt lgkmcnt(0)" : "+v"(c[0]), "+v"(c[1]), "+v"(c[2]), "+v"(c[3]));
      const f32x4 t = (c[0] + c[1]) + (c[2] + c[3]);
      if (lane < 36) pk_st16(ws + OFF_CSP + ((size_t)kb * F_ + ti * 144) * 4 + sa, __builtin_bit_cast(u32x4, t));
    } else {
      lds_out<4608>(lds0 + (unsigned)(wl * 9216), ws + OFF_PW + (size_t)(wl * 144 + kb) * 4608, lane);
      lds_out<4608>(lds0 + (unsigned)(wl * 9216 + 4608), ws + OFF_PW + (size_t)(wl * 144 + 72 + kb) * 4608, lane);
    }
  }
  MF(8);
  announce(lds0 + (unsigned)(LW_OFF + LW_PK * 4), 4u, ws, CN_PK, isi ? 8 + (job & 3) * 4 + lane : 24 + lane, JOB && lane < (isi ? 4 : 8));
  MF(9);
  if (wl == 0) {
    asm volatile("" : "+v"(f));
#pragma unroll 1
    for (unsigned spin = 0; spin < SPIN_MEM; ++spin) {
      if (!__any(f[0] != (unsigned)tag || f[1] != (unsigned)(tag >> 32))) break;
      __builtin_amdgcn_s_sleep(2);
      asm volatile("global_load_dwordx2 %0, %1, off sc1\n\ts_waitcnt vmcnt(0)" : "=&v"(f) : "v"(tp) : "memory");
    }
  }
}

template <int MT, int NT>
DEVI void k1_gemm(char* smem, const char* ws, int rt, int hp, int mt0, int nt0,
                  bool late, int lane MK_ARG) {
  const unsigned lds0 = (unsigned)(size_t)smem;
  const int g = lane >> 4;
  f32x4 cs[NT];
  unsigned ta[NT];
  unsigned cad[NT];
#pragma unroll
  for (int nt = 0; nt < NT; ++nt) {
    int c = (nt0 + nt) * 16 + g * 4;
    if (c > 104) c = 104;
    const int part = c / 36, ci = c - part * 36;
    cad[nt] = lds0 + (unsigned)(K1_CS_OFF + (part * 40 + (hp & 1) * 4 + ci) * 4);
    asm volatile("" : "+v"(cad[nt]));
    const int to = (nt0 + nt) * 16 + g * 4 > 104 ? 120 : part * 80 + ci * 2 + (ci >= 20 ? 8 : 0);
    ta[nt] = lds0 + (unsigned)(K1_T_OFF + (mt0 * 16 + (lane & 15)) * K1_TP + to);
    asm volatile("" : "+v"(ta[nt]));
  }
  unsigned sa = lds0 + (unsigned)(K1_ST_OFF + (mt0 * 16 + (lane & 15)) * 8);
  asm volatile("" : "+v"(sa));
  f32x4 acc[MT][NT];
#pragma unroll
  for (int mt = 0; mt < MT; ++mt)
#pragma unroll
    for (int nt = 0; nt < NT; ++nt) acc[mt][nt] = f32x4{0.f, 0.f, 0.f, 0.f};
  f32x2 st[MT];
  if (late) bar();
  if constexpr (NT == 7) cnt_wait(ws, CN_X2, rt * 16 + hp, rt ? 24u : 16u);
  bar();
  MK(1);
  {
    GemmMath<K1C, MT, NT, (NT == 7 ? 5 : 3), (NT == 3 ? 6 : NT), true, 2, false, K1_WS> gm(acc);
    gm.setup(lds0, mt0, nt0, lane, 0u);
    bar();
#pragma unroll
    for (int nt = 0; nt < NT; ++nt) asm volatile("ds_read_b128 %0, %1" : "=v"(cs[nt]) : "v"(cad[nt]));
    gm.first();
    gm.template mid<8>();
#pragma unroll
    for (int mt = 0; mt < MT; ++mt) asm volatile("ds_read_b64 %0, %1 offset:%2" : "=v"(st[mt]) : "v"(sa), "n"(mt * 128));
    asm volatile("s_waitcnt vmcnt(0)" ::: "memory");
    gm.last();
  }
#pragma unroll
  for (int nt = 0; nt < NT; ++nt) asm volatile("" : "+v"(cs[nt]));
#pragma unroll
  for (int mt = 0; mt < MT; ++mt) asm volatile("" : "+v"(st[mt]));
  MK(2);
#pragma unroll
  for (int mt = 0; mt < MT; ++mt) {
    f32x2 M = {st[mt][0], st[mt][0]}, R = {st[mt][1], st[mt][1]};
    asm volatile("" : "+v"(M), "+v"(R));
#pragma unroll
    for (int nt = 0; nt < NT; ++nt) {
      const f32x2 v0 = R * __builtin_elementwise_fma(M, f32x2{cs[nt][0], cs[nt][1]}, f32x2{acc[mt][nt][0], acc[mt][nt][1]});
      const f32x2 v1 = R * __builtin_elementwise_fma(M, f32x2{cs[nt][2], cs[nt][3]}, f32x2{acc[mt][nt][2], acc[mt][nt][3]});
      acc[mt][nt][0] = __uint_as_float(pack2v(v0));
      acc[mt][nt][1] = __uint_as_float(pack2v(v1));
      const f32x2 pk = {acc[mt][nt][0], acc[mt][nt][1]};
      asm volatile("ds_write_b64 %0, %1 offset:%2" ::"v"(ta[nt]), "v"(pk), "n"(mt * 16 * K1_TP) : "memory");
    }
  }
}

DEVI float vmax(float a, float b) {
  float r;
  asm("v_max_f32 %0, %1, %2" : "=v"(r) : "v"(a), "v"(b));
  return r;
}
struct Attn {
  unsigned ka, kb, va, vb, m0, m1, m2, pm[2], oa, pa;
  f32x4 mk;
  unsigned long long sel[4];
  const char* ob;
  DEVI void setup(unsigned T, char* o, int r0, int hp, int wave, int lane) {
    const int g = lane >> 4, r = lane & 15, blk = wave >> 1, hd = wave & 1;
    const unsigned co = (unsigned)(g < 2 ? hd * 48 + g * 16 : 32);
    ka = T + (unsigned)((blk * 32 + 2 * r) * K1_TP) + co;
    kb = T + (unsigned)((blk * 32 + 2 * (r & 7)) * K1_TP) + co;
    const int vr = 4 * g + ((lane >> 2) & 3);
    va = T + (unsigned)((blk * 32 + 2 * vr) * K1_TP + (lane & 3) * 8);
    vb = T + (unsigned)((blk * 32 + 2 * (vr & 7)) * K1_TP + (lane & 3) * 8);
    m0 = (g < 2 || (g == 2 && hd == 0)) ? ~0u : 0u;
    m1 = (g < 2 || (g == 2 && hd == 1)) ? ~0u : 0u;
    m2 = g < 2 ? ~0u : 0u;
    pm[0] = pm[1] = 0u;
    const float ci = __uint_as_float(vconst(0xff800000u));
    const unsigned ch = vconst(0xffffu);
#pragma unroll
    for (int e = 0; e < 4; ++e) {
      const int w1 = r + 8 - 4 * g - e;
      mk[e] = (w1 & 15) <= 7 ? 0.f : ci;
      sel[e] = __ballot(w1 >= 8);
      if (w1 >= 8) pm[e >> 1] |= ch << (16 * (e & 1));
    }
    oa = (unsigned)((8 + hp * 4 + hd * 2 + (g >> 1)) * 1024 + ((blk & 1) * 32 + 2 * r + (g & 1)) * 16);
    pa = ((oa & 1023u) >> 2) + (unsigned)((hp * 2 + hd) * 256);
    ob = uni_ptr(o + (size_t)((r0 >> 6) + (wave >> 2)) * (72 * 1024));
    asm volatile("" : "+v"(ka), "+v"(kb), "+v"(va), "+v"(vb), "+v"(m0), "+v"(m1), "+v"(m2), "+v"(pm[0]), "+v"(pm[1]), "+v"(oa));
    asm volatile("" : "+v"(mk), "+v"(pa), "+s"(ob), "+s"(sel[0]), "+s"(sel[1]), "+s"(sel[2]), "+s"(sel[3]));
  }
  DEVI void run(int r0, int hp, int wave, int lane) {
    const int hd = wave & 1;
    u32x4 q[2], k0[2], k1[2];
    u32x2 v[2][2][2];
#pragma unroll
    for (int p = 0; p < 2; ++p) {
      asm volatile("ds_read_b128 %0, %1 offset:%2" : "=v"(q[p]) : "v"(ka), "n"((16 + p) * K1_TP) : "memory");
      asm volatile("ds_read_b128 %0, %1 offset:%2" : "=v"(k0[p]) : "v"(ka), "n"(p * K1_TP + 80) : "memory");
      asm volatile("ds_read_b128 %0, %1 offset:%2" : "=v"(k1[p]) : "v"(kb), "n"((32 + p) * K1_TP + 80) : "memory");
    }
    const unsigned vh = (unsigned)hd * 48u;
#pragma unroll
    for (int p = 0; p < 2; ++p) {
      asm volatile("ds_read_b64_tr_b16 %0, %1 offset:%2" : "=v"(v[p][0][0]) : "v"(va + vh), "n"(p * K1_TP + 160) : "memory");
      asm volatile("ds_read_b64_tr_b16 %0, %1 offset:%2" : "=v"(v[p][0][1]) : "v"(vb + vh), "n"((32 + p) * K1_TP + 160) : "memory");
      asm volatile("ds_read_b64_tr_b16 %0, %1 offset:%2" : "=v"(v[p][1][0]) : "v"(va), "n"(p * K1_TP + 192) : "memory");
      asm volatile("ds_read_b64_tr_b16 %0, %1 offset:%2" : "=v"(v[p][1][1]) : "v"(vb), "n"((32 + p) * K1_TP + 192) : "memory");
    }
    const u32x4 one4 = {vconst(0x3f803f80u), vconst(0x3f803f80u), vconst(0x3f803f80u), vconst(0x3f803f80u)};
    const s16x8 ones = __builtin_bit_cast(s16x8, one4);
    const float c = 0.23570226039551584f * 1.4426950408889634f;
    const f32x4 z = {0.f, 0.f, 0.f, 0.f};
    f32x4 sc[2][2];
#pragma unroll
    for (int p = 0; p < 2; ++p) {
      asm volatile("s_waitcnt lgkmcnt(%3)" : "+v"(q[p]), "+v"(k0[p]), "+v"(k1[p]) : "n"(11 - 3 * p));
      const s16x8 qf = __builtin_bit_cast(s16x8, q[p] & u32x4{m0, m1, m2, m2});
      sc[p][0] = __builtin_amdgcn_mfma_f32_16x16x32_bf16(__builtin_bit_cast(s16x8, k0[p]), qf, mk, 0, 0, 0);
      sc[p][1] = __builtin_amdgcn_mfma_f32_16x16x32_bf16(__builtin_bit_cast(s16x8, k1[p]), qf, mk, 0, 0, 0);
      __builtin_amdgcn_sched_barrier(0);
    }
    if (__builtin_expect(r0 == 0 && wave < 2, 0)) {
#pragma unroll
      for (int p = 0; p < 2; ++p)
#pragma unroll
        for (int e = 0; e < 4; ++e)
          if (lane < 32) sc[p][0][e] = mk[e] - 4.242640687e9f;
    }
    float lg[2][4], mx[2];
#pragma unroll
    for (int p = 0; p < 2; ++p) {
#pragma unroll
      for (int e = 0; e < 4; ++e) lg[p][e] = __builtin_amdgcn_inverse_ballot_w64(sel[e]) ? sc[p][1][e] : sc[p][0][e];
      asm("v_max3_f32 %0, %1, %2, %3\n\tv_max_f32 %0, %0, %4" : "=&v"(mx[p]) : "v"(lg[p][0]), "v"(lg[p][1]), "v"(lg[p][2]), "v"(lg[p][3]));
    }
    {
      const auto a = __builtin_amdgcn_permlane32_swap(__float_as_uint(mx[0]), __float_as_uint(mx[1]), false, false);
      const unsigned b = __float_as_uint(vmax(__uint_as_float(a[0]), __uint_as_float(a[1])));
      const auto d = __builtin_amdgcn_permlane16_swap(b, b, false, false);
      const unsigned f = __float_as_uint(vmax(__uint_as_float(d[0]), __uint_as_float(d[1])) * -c);
      const auto h = __builtin_amdgcn_permlane32_swap(f, f, false, false);
      mx[0] = __uint_as_float(h[0]);
      mx[1] = __uint_as_float(h[1]);
    }
#pragma unroll
    for (int p = 0; p < 2; ++p)
#pragma unroll
      for (int e = 0; e < 4; ++e) lg[p][e] = __builtin_amdgcn_exp2f(__builtin_fmaf(lg[p][e], c, mx[p]));
    asm volatile("s_waitcnt lgkmcnt(0)"
                 : "+v"(v[0][0][0]), "+v"(v[0][0][1]), "+v"(v[0][1][0]), "+v"(v[0][1][1]), "+v"(v[1][0][0]), "+v"(v[1][0][1]),
                   "+v"(v[1][1][0]), "+v"(v[1][1][1]));
    __builtin_amdgcn_sched_barrier(0);
    f32x4 su[2], o0[2], o1[2];
    s16x8 pp[2];
#pragma unroll
    for (int p = 0; p < 2; ++p) {
      const unsigned p0 = pack2(lg[p][0], lg[p][1]), p1 = pack2(lg[p][2], lg[p][3]);
      pp[p] = __builtin_bit_cast(s16x8, u32x4{p0 & ~pm[0], p1 & ~pm[1], p0 & pm[0], p1 & pm[1]});
    }
    __builtin_amdgcn_sched_barrier(0);
#pragma unroll
    for (int p = 0; p < 2; ++p) {
      const s16x8 pf = pp[p];
      const u32x4 v0 = {v[p][0][0][0], v[p][0][0][1], v[p][0][1][0], v[p][0][1][1]};
      const u32x4 v1 = {v[p][1][0][0], v[p][1][0][1], v[p][1][1][0], v[p][1][1][1]};
      su[p] = __builtin_amdgcn_mfma_f32_16x16x32_bf16(ones, pf, z, 0, 0, 0);
      o0[p] = __builtin_amdgcn_mfma_f32_16x16x32_bf16(__builtin_bit_cast(s16x8, v0), pf, z, 0, 0, 0);
      o1[p] = __builtin_amdgcn_mfma_f32_16x16x32_bf16(__builtin_bit_cast(s16x8, v1), pf, z, 0, 0, 0);
    }
    __builtin_amdgcn_sched_barrier(0);
    u32x2 y[2], yl;
    unsigned hm = (unsigned)hd - 1u;
    asm volatile("" : "+s"(hm));
#pragma unroll
    for (int p = 0; p < 2; ++p) {
      const float inv = __builtin_amdgcn_rcpf(su[p][0]);
      y[p] = u32x2{pack2(o0[p][0] * inv, o0[p][1] * inv), pack2(o0[p][2] * inv, o0[p][3] * inv)};
      const unsigned la = pack2(o1[p][0] * inv, o1[p][1] * inv), lb = pack2(o1[p][2] * inv, o1[p][3] * inv);
      yl[p] = (la & hm) | (lb & ~hm);
    }
    const auto s0 = __builtin_amdgcn_permlane16_swap(y[0][0], y[1][0], false, false);
    const auto s1 = __builtin_amdgcn_permlane16_swap(y[0][1], y[1][1], false, false);
    const u32x4 w = {s0[0], s1[0], s0[1], s1[1]};
    asm volatile("global_store_dwordx4 %0, %1, %4 sc1\n\ts_mov_b64 exec, 0xffff\n\tglobal_store_dwordx2 %2, %3, %4 sc1\n\ts_mov_b64 exec, -1\n\ts_nop 0" ::"v"(oa),
                 "v"(w), "v"(pa), "v"(yl), "s"(ob)
                 : "memory");
  }
};

#define NL_MAIN 660
DEVI void math_start(char* smem, const u16* b_sp, const u16* wqkv, char* ws, int bid, int wave, int lane) {
  const unsigned lds0 = (unsigned)(size_t)smem;
  const int tid = wave * 64 + lane, idx = bid >> 3;
  if (wave < 3) {
    static_assert(2 * SZ_ZP == (size_t)256 * 2048 && NCNT <= 16, "mark split");
    const u32x4 mk = {0xffffffffu, 0xffffffffu, 0xffffffffu, 0xffffffffu};
    char* c = ws + OFF_CNT + (size_t)((lane >> 3) * 256 + bid) * 128 + (lane & 7) * 16;
    pk_st16(wave < 2 ? ws + OFF_ZP + (size_t)((bid * 2 + wave) * 64 + lane) * 16 : c, mk);
    if (wave == 2 && lane < (NCNT - 8) * 8) pk_st16(c + 8 * 256 * 128, mk);
  }
  if (wave == 3) {
    const int li = idx * 54 + (lane < 54 ? lane : 53);
    touch64(uni_ptr((const char*)wqkv), (unsigned)(li * 1152 + 512), lds0 + (unsigned)(K1R_DUMMY + 768));
  }
  if (wave == 5 && idx == 31) touch64(uni_ptr((const char*)b_sp), (unsigned)lane * 64u, lds0 + (unsigned)(K1R_DUMMY + 768));
  const u32x4 ones = {0x3f803f80u, 0x3f803f80u, 0x3f803f80u, 0x3f803f80u};
  if (tid < 256) *reinterpret_cast<u32x4*>(smem + (tid >> 6) * K1R::SB + K1R::XB + (tid & 63) * 288 + 240) = ones;
  else if (tid < 288) *reinterpret_cast<u32x4*>(smem + ((tid - 256) >> 3) * K1R::SB + (tid & 7) * 16) = ones;
  else if (tid < 296) *reinterpret_cast<u32x4*>(smem + LW_OFF + (tid - 288) * 16) = u32x4{0u, 0u, 0u, 0u};
  asm volatile("s_waitcnt lgkmcnt(0)" ::: "memory");
}

template <int NST, int NS, int NSW, int WOFF, int XW, int X0, int S0, unsigned BAR>
struct ResX {
  static constexpr int WS = 4 * 4608, R = 10, V1 = 5 * NS, NV = V1 + 10 * (NST - NS), NG = (NSW + 2) / 3;
  s16x8 xf[XW][2];
  s16x8 wf[R];
  unsigned wrd[2][NG], xo, ea, fa, fv, tag;
  const char* xp[3];
  f32x4 (&acc)[2][5];
  DEVI ResX(f32x4 (&a)[2][5]) : acc(a) {}
  static constexpr int st(int V) { return V < V1 ? V / 5 : NS + (V - V1) / 10; }
  static constexpr int hf(int V) { return V < V1 ? 0 : ((V - V1) / 5) & 1; }
  static constexpr bool first(int V) { return V < V1 ? V % 5 == 0 : (V - V1) % 10 == 0; }
  static constexpr int wreq(int V) {
    int n = V + 5;
    if (V >= V1 - 4) n += V < V1 ? V - (V1 - 5) : 4;
    return n < NV ? n : NV;
  }
  static constexpr int XU = V1 + 1, XF = V1 + 11, XR = V1 + 15, XC = V1 + 21;
  static constexpr int oth(int V) { return NS && ((V >= XU && V <= XF && (V - XU) % 2 == 0) || V == XR || (V > XC && V <= XC + 5)) ? 1 : 0; }
  static constexpr int newer(int V) {
    const int F = V + 1 < NV ? V + 1 : V;
    int v = V - 1, n = 0;
    for (; v >= 0 && wreq(v - 1) > F; --v) n += wreq(v) - wreq(v - 1) + oth(v);
    return n + wreq(v) - 1 - F + (v >= 0 ? oth(v) : 0);
  }
  static constexpr int xreq(int T) {
    int n = X0 + 2 * (T + 1);
    if (n > T + XW) n = T + XW;
    if (NS && T <= 11 && n > 18) n = 18;
    if (T < 0) n = X0;
    return n < NST ? n : NST;
  }
  static_assert(NS == 0 || (xreq(NS - 2) == NST && XC + 5 < NV), "the last steps only compute and swap");
  template <int T, int M>
  DEVI void xld() {
    asm volatile("global_load_dwordx4 %0, %1, %2 sc1" : "=v"(xf[T % XW][M]) : "v"(xo), "s"(xp[M == 0 ? 0 : (T < NS ? 1 : 2)]));
    if constexpr (M == 1 && T + 1 < NST) asm volatile("v_add_u32 %0, 0x2000, %0" : "+v"(xo));
  }
  template <int V>
  DEVI void rd() {
    constexpr int T = st(V), sl = (T / 2 + S0) % NSW;
    asm volatile("ds_read_b128 %0, %1 offset:%2" : "=v"(wf[V % R]) : "v"(wrd[hf(V)][sl / 3]), "n"((sl % 3) * WS + (T & 1) * 9216 + (V % 5) * 32));
  }
  template <int V, int M, int H>
  DEVI void mf() {
    acc[M][V % 5] = __builtin_amdgcn_mfma_f32_16x16x32_bf16(wf[V % R], xf[st(V) % XW][H], acc[M][V % 5], 0, 0, 0);
    __builtin_amdgcn_sched_barrier(0);
  }
  template <int V>
  DEVI void unit() {
    constexpr int T = st(V), n = V % 5, nx = xreq(T) - xreq(T - 1), nw = wreq(V) - wreq(V - 1);
    constexpr int vm = 2 * (xreq(T - 1) - 1 - T);
    static_assert(newer(V) <= 15, "lgkmcnt range");
    if constexpr (first(V) && V % 2 == 0) asm volatile("s_waitcnt vmcnt(%0) lgkmcnt(%1)" ::"n"(vm), "n"(newer(V)));
    else if constexpr (first(V)) asm volatile("s_waitcnt vmcnt(%0)" ::"n"(vm));
    else if constexpr (V % 2 == 0) asm volatile("s_waitcnt lgkmcnt(%0)" ::"n"(newer(V)));
    __builtin_amdgcn_sched_barrier(0);
    if constexpr (V < V1) mf<V, V & 1, V & 1>();
    else mf<V, 0, hf(V)>();
    if constexpr (nw > 0) rd<wreq(V - 1)>();
    __builtin_amdgcn_sched_barrier(0);
    if constexpr (V < V1) {
      mf<V, 1 - (V & 1), 1 - (V & 1)>();
      if constexpr (nw > 1) rd<wreq(V - 1) + 1>();
      if constexpr (nx == 2 && n < 4) xld<xreq(T - 1) + n / 2, n % 2>();
      if constexpr (nx == 1 && n == (T & 1)) xld<xreq(T - 1), 0>();
      if constexpr (nx == 1 && n == (T & 1) + 2) xld<xreq(T - 1), 1>();
      if constexpr (n == 4 && (T & 1) && ((BAR >> (T / 2)) & 1u)) bar();
    } else if constexpr (NS == 0) {
      constexpr int k = (V % 10) / 2;
      if constexpr (V % 2 == 0 && k < 2 * nx) xld<xreq(T - 1) + k / 2, k % 2>();
      if constexpr (V % 10 == 8 && nx > 0 && xreq(T) == NST)
        asm volatile("s_mov_b64 exec, 1\n\tds_add_u32 %0, %1\n\ts_mov_b64 exec, -1" ::"v"(fa), "v"(fv));
    } else {
      if constexpr (oth(V) && V < XF) asm volatile("ds_write_b128 %0, %1 offset:%2" ::"v"(ea), "v"(acc[1][(V - XU) / 2]), "n"((V - XU) * 512));
      if constexpr (V == XF) asm volatile("ds_write_b32 %0, %1" ::"v"(fa), "v"(fv));
      if constexpr (V == XF + 2) asm volatile("v_xor_b32 %0, 4, %0" : "+v"(fa));
      if constexpr (V == XR) asm volatile("ds_read_b32 %0, %1" : "+v"(fv) : "v"(fa));
      if constexpr (V == XR + 2) asm volatile("v_xor_b32 %0, 0x2000, %0" : "+v"(ea));
      if constexpr (V == XC) {
        unsigned c = 1u << 18;
        asm volatile(
            "s_waitcnt lgkmcnt(%4)\n\tv_cmp_ne_u32 vcc, %3, %0\n\ts_cbranch_vccnz .Lw%=\n\t.subsection 1\n.Lw%=:\n\tds_read_b32 %0, %2\n\t"
            "s_waitcnt lgkmcnt(0)\n\tv_cmp_ne_u32 vcc, %3, %0\n\t" SAFE_OUT(".Lr%=") "s_cbranch_vccnz .Lw%=\n\ts_branch .Lr%=\n\t.subsection 0\n.Lr%=:"
            : "+v"(fv), NOP_(c)
            : "v"(fa), "s"(tag), "n"(wreq(XC) - wreq(XR))
            : "vcc", "scc");
      }
      if constexpr (oth(V) && V > XC) asm volatile("ds_read_b128 %0, %1 offset:%2" : "=v"(acc[1][V - XC - 1]) : "v"(ea), "n"((V - XC - 1) * 1024));
    }
    __builtin_amdgcn_sched_barrier(0);
  }
  template <int... V>
  DEVI void units(std::integer_sequence<int, V...>) {
    (unit<V>(), ...);
  }
  DEVI void run() {
    units(std::make_integer_sequence<int, NV>{});
    if constexpr (NS > 0) {
      asm volatile("s_waitcnt lgkmcnt(0)" : "+v"(acc[1][0]), "+v"(acc[1][1]), "+v"(acc[1][2]), "+v"(acc[1][3]), "+v"(acc[1][4]));
#pragma unroll
      for (int i = 0; i < 5; ++i) acc[0][i] += acc[1][i];
    }
  }
  template <int... V>
  DEVI void pro(std::integer_sequence<int, V...>) {
    (rd<V>(), ...);
  }
  DEVI void prow() { pro(std::make_integer_sequence<int, wreq(-1)>{}); }
  template <int T0, int... T>
  DEVI void prox(std::integer_sequence<int, T...>) {
    ((xld<T0 + T, 0>(), xld<T0 + T, 1>()), ...);
  }
  DEVI void setup(unsigned lds0, const char* xbase, int wave, int lane, unsigned red, unsigned nth) {
    const int g = lane >> 4, r = lane & 15, kq = wave & 1;
    xp[0] = xbase + wave * 256;
    xp[1] = xbase + (wave ^ 1) * 256;
    xp[2] = xp[0] + (kq ? -4096 : 4096);
    xo = (unsigned)((4 * kq + g) * 1024 + r * 16);
#pragma unroll
    for (int i = 0; i < 2 * NG; ++i) {
      wrd[i / NG][i % NG] = lds0 + (unsigned)(WOFF + (i % NG) * 3 * WS + (kq ^ (i / NG)) * 4608 + (2 * (r & 7) + 16 * (g & 1) + (g >> 1)) * 144 + (r >> 3) * 16);
      if (i < NG || NS == 0 || i % NG == ((NS / 2 + S0) % NSW) / 3) asm volatile("" : "+v"(wrd[i / NG][i % NG]));
    }
    asm volatile("" : "+v"(xo));
    if constexpr (NS > 0) {
      ea = lds0 + red + (unsigned)((wave ^ 1) * 8192 + lane * 16);
      fa = lds0 + (unsigned)(LW_OFF + (LW_PAIR + wave) * 4);
      fv = tag = nth;
      asm volatile("" : "+v"(ea), "+v"(fa), "+v"(fv));
    }
  }
  template <int N>
  DEVI void init(u32x2 (&xv)[5]) {
    asm volatile("s_waitcnt vmcnt(%5)" : "+v"(xv[0]), "+v"(xv[1]), "+v"(xv[2]), "+v"(xv[3]), "+v"(xv[4]) : "n"(N));
#pragma unroll
    for (int i = 0; i < 5; ++i) {
      acc[0][i] = f32x4{bf_lo(xv[i][0]), bf_hi(xv[i][0]), bf_lo(xv[i][1]), bf_hi(xv[i][1])};
      asm volatile("" : "+v"(acc[0][i]));
      acc[1][i] = f32x4{0.f, 0.f, 0.f, 0.f};
    }
  }
};

constexpr int P5_XW = 8, P5_X0 = 5, P2_X0 = 4;
constexpr unsigned P5_BAR = 0x2aaau;
constexpr int P5_W = 15 * 1024;
constexpr int P2_W = K1_ST_OFF - 18 * 4608;
constexpr int P5_RED = 0;
static_assert(P5_RED % 16384 == 0 && P5_RED + 3 * 8192 + 5120 <= P5_W + 18432, "exchange area");
static_assert(P2_W >= K1_T_OFF + 144 * K1_TP + 64 && P2_W % 16 == 0 && P5_W + 8 * 18432 <= LW_OFF, "LDS placement");

struct ResIn {
  const char* xp;
  unsigned o, o2, st;
  DEVI void setup(const char* src, bool rm, int rb, int ct, int wave, int lane) {
    const int g = lane >> 4, g2 = g & 1, rl = wave * 16 + (lane & 15);
    xp = uni_ptr(rm ? src + ((size_t)rb * 64 * D_ + ct * 72) * 2 : src + (size_t)(rb * 72 + ct * 9) * 1024);
    o = rm ? (unsigned)(rl * (D_ * 2) + g * 8) : (unsigned)((g >> 1) * 1024 + rl * 16 + g2 * 8);
    o2 = rm ? (unsigned)(rl * (D_ * 2) + g2 * 8 + 128) : (unsigned)(8192 + rl * 16 + g2 * 8);
    st = rm ? 32u : 2048u;
    asm volatile("" : "+v"(o), "+v"(o2));
    asm volatile("" : "+s"(xp));
  }
  DEVI void load(u32x2 (&xv)[5]) {
#pragma unroll
    for (int i = 0; i < 4; ++i) asm volatile("global_load_dwordx2 %0, %1, %2 sc1" : "=v"(xv[i]) : "v"(o), "s"(xp + i * st));
    asm volatile("global_load_dwordx2 %0, %1, %2 sc1" : "=v"(xv[4]) : "v"(o2), "s"(xp));
  }
};

DEVI void p2_gemm(f32x4 (&acc)[2][5], u32x2 (&xv)[5], unsigned lds0, const char* ob, int layer, int wave, int lane MK_ARG) {
  ResX<9, 0, 5, P2_W, 9, P2_X0, 0, 0u> gm(acc);
  gm.setup(lds0, ob, wave, lane, 0u, 0u);
  unsigned a = (gm.xo & ~1023u) | ((gm.xo & 1023u) >> 2);
  gm.xo += 8192u;
  gm.fa = lds0 + (unsigned)(LW_OFF + (LW_T + layer) * 4);
  gm.fv = vconst(1u);
  asm volatile("" : "+v"(a), "+v"(gm.xo), "+v"(gm.fa));
  const char* pp[2] = {gm.xp[0] - wave * 192, gm.xp[2] - wave * 192};
  place();
  bar();
  MK(6);
  unsigned e[2][4];
#pragma unroll
  for (int d = 0; d < 8; ++d)
    asm volatile("global_load_dword %0, %1, %2 offset:%3 sc1" : "=v"(e[d / 4][d % 4]) : "v"(a), "s"(pp[d / 4]), "n"((d % 4) * 256));
  gm.template prox<1>(std::make_integer_sequence<int, P2_X0 - 1>{});
  gm.prow();
  gm.template init<6 + 2 * P2_X0>(xv);
  asm volatile("s_waitcnt vmcnt(%8)"
               : "+v"(e[0][0]), "+v"(e[0][1]), "+v"(e[0][2]), "+v"(e[0][3]), "+v"(e[1][0]), "+v"(e[1][1]), "+v"(e[1][2]), "+v"(e[1][3])
               : "n"(2 * P2_X0 - 2));
  gm.xf[0][0] = __builtin_bit_cast(s16x8, u32x4{e[0][0], e[0][1], e[0][2], e[0][3]});
  gm.xf[0][1] = __builtin_bit_cast(s16x8, u32x4{e[1][0], e[1][1], e[1][2], e[1][3]});
  __builtin_amdgcn_sched_barrier(0);
  gm.run();
}

struct ResOut {
  const char *xp[3], *sp;
  unsigned xo, so;
  DEVI void setup(char* xim, char* xs, u16* out, bool fin, int rb, int ct, int wave, int lane) {
    const int g = lane >> 4, rl = wave * 16 + (lane & 15);
    xp[0] = uni_ptr(fin ? (const char*)(out + (size_t)rb * 64 * D_ + ct * 72) : xim + (size_t)(rb * 72 + ct * 9) * 1024);
    xp[1] = xp[0] + (fin ? 64 : 4096);
    xp[2] = xp[0] + (fin ? 128 : 8192);
    sp = uni_ptr(xs + (size_t)(rb * 8 + ct) * 512);
    xo = (unsigned)(((g & 1) * 2 + (g >> 1)) * (fin ? 16 : 1024) + rl * (fin ? D_ * 2 : 16));
    so = (unsigned)(rl * 8);
    asm volatile("" : "+v"(xo), "+v"(so), "+s"(xp[0]), "+s"(xp[1]), "+s"(xp[2]), "+s"(sp));
  }
  DEVI void run(const f32x4 (&a)[5], bool fin) {
    u32x2 y[5];
    u32x4 v[3];
#pragma unroll
    for (int nt = 0; nt < 5; ++nt) y[nt] = u32x2{pack2(a[nt][0], a[nt][1]), pack2(a[nt][2], a[nt][3])};
#pragma unroll
    for (int i = 0; i < 3; ++i) {
      const auto s0 = __builtin_amdgcn_permlane16_swap(y[2 * i][0], y[i < 2 ? 2 * i + 1 : 4][0], false, false);
      const auto s1 = __builtin_amdgcn_permlane16_swap(y[2 * i][1], y[i < 2 ? 2 * i + 1 : 4][1], false, false);
      v[i] = u32x4{s0[0], s1[0], s0[1], s1[1]};
    }
    if (fin) {
      asm volatile("global_store_dwordx4 %0, %1, %4\n\tglobal_store_dwordx4 %0, %2, %5\n\ts_mov_b64 exec, 0xffff\n\tglobal_store_dwordx4 %0, %3, %6\n\t"
                   "s_mov_b64 exec, -1\n\ts_nop 0" ::"v"(xo), "v"(v[0]), "v"(v[1]), "v"(v[2]), "s"(xp[0]), "s"(xp[1]), "s"(xp[2])
                   : "memory");
      return;
    }
    asm volatile("global_store_dwordx4 %0, %1, %4 sc1\n\tglobal_store_dwordx4 %0, %2, %5 sc1\n\ts_mov_b64 exec, 0xffff\n\t"
                 "global_store_dwordx4 %0, %3, %6 sc1\n\ts_mov_b64 exec, -1\n\ts_nop 0" ::"v"(xo), "v"(v[0]), "v"(v[1]), "v"(v[2]),
                 "s"(xp[0]), "s"(xp[1]), "s"(xp[2])
                 : "memory");
    const u32x4 one4 = {vconst(0x3f803f80u), vconst(0x3f803f80u), vconst(0x3f803f80u), vconst(0x3f803f80u)};
    const bool lo = __builtin_amdgcn_inverse_ballot_w64(0xffffull);
    v[2] = u32x4{lo ? v[2][0] : 0u, lo ? v[2][1] : 0u, lo ? v[2][2] : 0u, lo ? v[2][3] : 0u};
    f32x4 s = {0.f, 0.f, 0.f, 0.f}, q = s;
#pragma unroll
    for (int i = 0; i < 3; ++i) {
      const s16x8 u = __builtin_bit_cast(s16x8, v[i]);
      q = __builtin_amdgcn_mfma_f32_16x16x32_bf16(u, u, q, 0, 0, 0);
      s = __builtin_amdgcn_mfma_f32_16x16x32_bf16(__builtin_bit_cast(s16x8, one4), u, s, 0, 0, 0);
    }
    float q0 = q[0], q1 = q[1], q2 = q[2], q3 = q[3];
    const bool b0 = __builtin_amdgcn_inverse_ballot_w64(0xaaaaaaaaaaaaaaaaull), b1 = __builtin_amdgcn_inverse_ballot_w64(0xccccccccccccccccull);
    q0 = b0 ? q1 : q0;
    q2 = b0 ? q3 : q2;
    asm("" : "+v"(q0), "+v"(q2));
    const f32x2 sq = {s[0], b1 ? q2 : q0};
    asm volatile("s_mov_b64 exec, %3\n\tglobal_store_dwordx2 %0, %1, %2 sc1\n\ts_mov_b64 exec, -1" ::"v"(so), "v"(sq), "s"(sp), "s"(0xf0000f0000f0000full) : "memory");
  }
};

using K3C = GemmCfg<128, 144>;
constexpr int K3_Z1_OFF = K3C::LDS_BYTES;
constexpr int K3_C2_OFF = K3_Z1_OFF + K3C::XB;
constexpr int K3_ZC_OFF = K3_C2_OFF + 2048;
constexpr int K3_DM_OFF = K3_ZC_OFF + 1024;
constexpr int K3_ST_OFF = K3_Z1_OFF + 4 * 5120;
constexpr int K3_C1_OFF = K3_ST_OFF + 2048;
constexpr int K3_BS_OFF = K3_C1_OFF + 576;
static_assert(K3_BS_OFF + 512 <= LW_OFF, "K3 LDS");
static_assert(P5_W + 19 * 1024 == K3C::SB && K3C::SB == 34 * 1024, "w_out ring");

DEVI void red4(float (&t)[4], float& s, float& q) {
#pragma unroll
  for (int j = 0; j < 2; ++j) {
    const auto p = __builtin_amdgcn_permlane16_swap(__float_as_uint(t[j]), __float_as_uint(t[2 + j]), false, false);
    const unsigned v = __float_as_uint(__uint_as_float(p[0]) + __uint_as_float(p[1]));
    const auto w = __builtin_amdgcn_permlane32_swap(v, v, false, false);
    t[j] = __uint_as_float(w[0]) + __uint_as_float(w[1]);
    asm volatile("" : "+v"(t[j]));
  }
  s = t[0];
  q = t[1];
}
DEVI void red8(const f32x4& a, const f32x4& b, float& s, float& q) {
  float t[4];
#pragma unroll
  for (int j = 0; j < 4; ++j) {
    t[j] = a[j] + b[j];
    asm volatile("" : "+v"(t[j]));
  }
  red4(t, s, q);
}
DEVI void red16(const f32x4& a, const f32x4& b, const f32x4& c, const f32x4& d, float& s, float& q) {
  float t[4];
#pragma unroll
  for (int j = 0; j < 4; ++j) {
    float u = a[j] + b[j], v = c[j] + d[j];
    asm volatile("" : "+v"(u), "+v"(v));
    t[j] = u + v;
    asm volatile("" : "+v"(t[j]));
  }
  red4(t, s, q);
}

constexpr int b_k(int s) { return s < 2 ? s : (s < 4 ? 5 + s : (s < 6 ? 1 + s : (s == 6 ? 4 : s - 5))); }
template <bool B>
struct UMath {
  static constexpr int D = 4, R = 6, NV = 162, SB = K3C::SB, P = K3C::P;
  s16x8 wf[R], xf[2][2];
  s16x8 (&xc)[4][2][2];
  f32x4 (&acc)[2][9];
  f32x4 rc[4][2];
  f32x2 cs[9];
  unsigned ra, ca;
  template <int I>
  DEVI void cread() {
    if constexpr (I < 8) asm volatile("ds_read_b128 %0, %1 offset:%2" : "=v"(rc[I / 2][I % 2]) : "v"(ra), "n"((I / 2) * 512 + (I % 2) * 16));
    else if constexpr (I < 17) asm volatile("ds_read_b64 %0, %1 offset:%2" : "=v"(cs[I - 8]) : "v"(ca), "n"((I - 8) * 128));
  }
  unsigned xa, wa;
  unsigned fa, fw, fv;
  DEVI UMath(f32x4 (&a)[2][9], s16x8 (&x)[4][2][2]) : xc(x), acc(a) {}
  static constexpr int wslot(int s) { return B ? (s + 1) % 4 : s % 4; }
  static constexpr int xslot(int s) { return B ? b_k(s) % 5 : s % 5; }
  static constexpr bool res(int q) { return !B || b_k(q / 2) >= 4; }
  static constexpr bool chk(int s) { return s >= 3 && s < 7; }
  static constexpr bool xop(int v) { return v + D < NV && (v + D) % 9 < 2 && res((v + D) / 9); }
  static constexpr int ops1(int v) { return (xop(v) || (B && ((v % 18 == 9 && chk(v / 18)) || (v % 18 == 0 && v > 0)))) ? 1 : 0; }
  static constexpr int newer(int V) {
    int v0 = (V % 9 == 8 ? V : V + 1) - D, n = 0;
    if (v0 >= 0 && !(V % 9 == 0 && res(V / 9))) n += ops1(v0);
    for (int v = v0 + 1; v < V; ++v)
      if (v >= 0) n += (v + D < NV ? 1 : (B ? 0 : 4)) + ops1(v);
    return n;
  }
  template <int V>
  DEVI void wread() {
    asm volatile("ds_read_b128 %0, %1 offset:%2" : "=v"(wf[V % R]) : "v"(wa), "n"((wslot(V / 18) & 1) * SB + ((V % 18) / 9) * 32 * P + (V % 9) * 32));
  }
  template <int Q, int M>
  DEVI void xread() {
    asm volatile("ds_read_b128 %0, %1 offset:%2" : "=v"(xf[Q & 1][M]) : "v"(xa), "n"((xslot(Q / 2) & 1) * SB + (Q & 1) * 8192 + M * (B ? 256 : 64)));
  }
  template <int V, int M_>
  DEVI void mf() {
    constexpr int M = ((V % 9) & 1) ? 1 - M_ : M_, q = V / 9;
    if constexpr (!B) acc[M][V % 9] = __builtin_amdgcn_mfma_f32_16x16x32_bf16(xf[q & 1][M], wf[V % R], acc[M][V % 9], 0, 0, 0);
    else if constexpr (res(q)) acc[M][V % 9] = __builtin_amdgcn_mfma_f32_16x16x32_bf16(wf[V % R], xf[q & 1][M], acc[M][V % 9], 0, 0, 0);
    else acc[M][V % 9] = __builtin_amdgcn_mfma_f32_16x16x32_bf16(wf[V % R], xc[b_k(q / 2)][q & 1][M], acc[M][V % 9], 0, 0, 0);
    __builtin_amdgcn_sched_barrier(0);
  }
  template <int V>
  DEVI void unit() {
    constexpr int U = V % 18, s = V / 18;
    static_assert(newer(V) <= 15, "lgkmcnt range");
    if constexpr ((V % 9) % 2 == 0 && V > 0) asm volatile("s_waitcnt lgkmcnt(%0)" ::"n"(newer(V)));
    __builtin_amdgcn_sched_barrier(0);
    mf<V, 0>();
    if constexpr (V + D < NV) wread<V + D>();
    if constexpr (!B && V + D >= NV) {
      cread<(V + D - NV) * 4>();
      cread<(V + D - NV) * 4 + 1>();
    }
    if constexpr (s < 8 && U == 13 && (wslot(s + 1) >> 1) != (wslot(s) >> 1))
      asm volatile("v_add_u32 %0, %1, %0" : "+v"(wa) : "n"(((wslot(s + 1) >> 1) - (wslot(s) >> 1)) * 2 * SB));
    __builtin_amdgcn_sched_barrier(0);
    mf<V, 1>();
    if constexpr (xop(V)) xread<(V + D) / 9, (V + D) % 9>();
    if constexpr (!B && V + D >= NV) {
      cread<(V + D - NV) * 4 + 2>();
      cread<(V + D - NV) * 4 + 3>();
      if constexpr (V == NV - 1) cread<16>();
    }
    if constexpr (s < 8) {
      if constexpr (U == 11 && res(2 * s + 2) && (xslot(s + 1) >> 1) != (xslot(s) >> 1))
        asm volatile("v_add_u32 %0, %1, %0" : "+v"(xa) : "n"(((xslot(s + 1) >> 1) - (xslot(s) >> 1)) * 2 * SB));
      if constexpr (!B && U == 13) __builtin_amdgcn_s_barrier();
      if constexpr (B && U == 9 && chk(s)) asm volatile("ds_read_u8 %0, %1 offset:%2" : "+v"(fv) : "v"(fa), "n"(s == 6 ? 0 : 8));
      unsigned n = 1u << 18;
      if constexpr (B && U == 13 && chk(s)) {
        asm volatile(
            "v_cmp_gt_u32 vcc, %3, %0\n\ts_cbranch_vccnz .Lw%=\n\t.subsection 1\n.Lw%=:\n\tds_read_u8 %0, %2 offset:%4\n\ts_waitcnt lgkmcnt(0)\n\t"
            "v_cmp_gt_u32 vcc, %3, %0\n\t" SAFE_OUT(".Lr%=") "s_cbranch_vccnz .Lw%=\n\ts_branch .Lr%=\n\t.subsection 0\n.Lr%=:"
            : "+v"(fv), NOP_(n)
            : "v"(fa), "n"(s == 6 ? 4 : s + 2), "n"(s == 6 ? 0 : 8)
            : "vcc", "scc");
      }
    }
    if constexpr (B && U == 15) asm volatile("v_mov_b32 %0, %1" : "+v"(fv) : "n"(s + 1));
    if constexpr (B && ((U == 0 && s > 0) || V == NV - 1)) asm volatile("ds_write_b8 %0, %1" ::"v"(fw), "v"(fv));
    __builtin_amdgcn_sched_barrier(0);
  }
  template <int... V>
  DEVI void units(std::integer_sequence<int, V...>) {
    (unit<V>(), ...);
  }
  DEVI void first() {
    if constexpr (!B) {
      xread<0, 0>();
      xread<0, 1>();
    }
    wread<0>();
    wread<1>();
    wread<2>();
    wread<3>();
    asm volatile("s_waitcnt lgkmcnt(0)");
  }
  DEVI void run() {
    units(std::make_integer_sequence<int, NV>{});
#pragma unroll
    for (int m = 0; m < 2; ++m)
#pragma unroll
      for (int n = 0; n < 9; ++n) asm volatile("" : "+v"(acc[m][n]));
  }
};

struct CMath {
  static constexpr int D = 4, R = 6, SB = K3C::SB;
  s16x8 wf[R], xf[2][2];
  f32x4 (&acc)[2][9];
  unsigned wa[2], xa[2][2];
  DEVI CMath(f32x4 (&a)[2][9]) : acc(a) {}
  static constexpr int xop(int u, bool last) { return (u + D) % 9 < 2 && !(last && u + D >= 18) ? 1 : 0; }
  static constexpr int newer(int U, bool last) {
    int v0 = (U % 9 == 8 ? U : U + 1) - D, n = 0;
    if (U % 9 != 0) n += xop((v0 + 18) % 18, last && v0 >= 0);
    for (int v = v0 + 1; v < U; ++v) n += (last && v + D >= 18 ? 0 : 1) + xop((v + 18) % 18, last && v >= 0);
    return n;
  }
  template <int SL, int U>
  DEVI void wread() {
    asm volatile("ds_read_b128 %0, %1 offset:%2" : "=v"(wf[U % R]) : "v"(wa[SL / 2]), "n"((SL & 1) * SB + U * 1024));
  }
  template <int SL, int H, int M>
  DEVI void xread() {
    asm volatile("ds_read_b128 %0, %1 offset:%2" : "=v"(xf[H][M]) : "v"(xa[SL / 2][H]), "n"((SL & 1) * SB + M * 2048));
  }
  template <int U, int M_>
  DEVI void mf() {
    constexpr int M = ((U % 9) & 1) ? 1 - M_ : M_;
    acc[M][U % 9] = __builtin_amdgcn_mfma_f32_16x16x32_bf16(wf[U % R], xf[U / 9][M], acc[M][U % 9], 0, 0, 0);
    __builtin_amdgcn_sched_barrier(0);
  }
  template <int SL, bool BAR, bool LAST, int U>
  DEVI void unit() {
    constexpr int PU = (U + D) % 18, PS = U + D < 18 ? SL : (SL + 1) % 4;
    if constexpr ((U % 9) % 2 == 0) asm volatile("s_waitcnt lgkmcnt(%0)" ::"n"(newer(U, LAST)));
    __builtin_amdgcn_sched_barrier(0);
    mf<U, 0>();
    if constexpr (!(LAST && U + D >= 18)) wread<PS, PU>();
    __builtin_amdgcn_sched_barrier(0);
    mf<U, 1>();
    if constexpr (xop(U, LAST)) xread<PS, PU / 9, PU % 9>();
    if constexpr (BAR && U == 13) __builtin_amdgcn_s_barrier();
    __builtin_amdgcn_sched_barrier(0);
  }
  template <int SL, bool BAR, bool LAST, int... U>
  DEVI void units(std::integer_sequence<int, U...>) {
    (unit<SL, BAR, LAST, U>(), ...);
  }
  template <int SL, bool BAR = true, bool LAST = false>
  DEVI void stage() {
    units<SL, BAR, LAST>(std::make_integer_sequence<int, 18>{});
  }
  DEVI void setup(unsigned lds0, int wm, int lane) {
    const int g = lane >> 4, r = lane & 15;
#pragma unroll
    for (int i = 0; i < 2; ++i) {
      wa[i] = lds0 + (unsigned)(i * 2 * SB + K3C::XB + lane * 16);
#pragma unroll
      for (int h = 0; h < 2; ++h) xa[i][h] = lds0 + (unsigned)(i * 2 * SB + (wm * 32 + r) * 128 + (((4 * h + g) ^ ((r >> 1) & 7)) * 16));
    }
    asm volatile("" : "+v"(wa[0]), "+v"(wa[1]), "+v"(xa[0][0]), "+v"(xa[0][1]), "+v"(xa[1][0]), "+v"(xa[1][1]));
  }
  DEVI void first() {
    xread<2, 0, 0>();
    xread<2, 0, 1>();
    wread<2, 0>();
    wread<2, 1>();
    wread<2, 2>();
    wread<2, 3>();
    asm volatile("s_waitcnt lgkmcnt(0)");
  }
};

constexpr int NR1 = 3;
DEVI void p3_g0(char* smem, char* __restrict__ ws, int layer, int mi, int ni, int wm, int lane MK_ARG) {
  constexpr int SB = K3C::SB;
  const unsigned lds0 = (unsigned)(size_t)smem;
  const int g = lane >> 4, r = lane & 15;
  const unsigned lw = lds0 + (unsigned)(LW_OFF + layer * 4), me = 1u << (8 * wm);
  f32x4 ac[2][9];
#pragma unroll
  for (int mt = 0; mt < 2; ++mt)
#pragma unroll
    for (int j = 0; j < 9; ++j) ac[mt][j] = f32x4{0.f, 0.f, 0.f, 0.f};
  unsigned ra = lds0 + (unsigned)(K3_ST_OFF + (wm * 32 + g * 8) * 4), ca = lds0 + (unsigned)(K3_C2_OFF + r * 8);
  asm volatile("" : "+v"(ra), "+v"(ca));
  s16x8 xc[4][2][2];
  UMath<false> gm(ac, xc);
  gm.ra = ra;
  gm.ca = ca;
  gm.xa = lds0 + (unsigned)(g * 2048 + (wm * 32 + 8 * (r >> 2) + (r & 3)) * 16);
  gm.wa = lds0 + (unsigned)(K3C::XB + (g * 8 + (r & 7)) * K3C::P + (r >> 3) * 16);
  unsigned adw = lw + LW_AD * 4, mev = vconst(me), po = (unsigned)(ni * 1024 + lane * 4);
  const char* zp = uni_ptr(ws + OFF_ZP + (size_t)layer * SZ_ZP + (size_t)mi * 16384 + wm * 256);
  asm volatile("" : "+v"(gm.xa), "+v"(gm.wa), "+v"(adw), "+v"(po));
  asm volatile("" : "+s"(zp));
  place();
  bar();
  gm.first();
  gm.run();
  asm volatile("s_mov_b64 exec, 1\n\tds_add_u32 %0, %1\n\ts_mov_b64 exec, -1" ::"v"(adw), "v"(mev) : "memory");
  MK(11);
  f32x2 z[9][4];
  f32x2 sm[4], sq[4];
  {
    f32x4 (&rc)[4][2] = gm.rc;
    f32x2 (&cs)[9] = gm.cs;
    asm volatile("s_waitcnt lgkmcnt(1)"
                 : "+v"(rc[0][0]), "+v"(rc[0][1]), "+v"(rc[1][0]), "+v"(rc[1][1]), "+v"(rc[2][0]), "+v"(rc[2][1]), "+v"(rc[3][0]), "+v"(rc[3][1]));
#pragma unroll
    for (int j = 0; j < 9; ++j) asm volatile("" : "+v"(cs[j]));
    auto RC = [&](int k, int i) { return f32x2{rc[k][i / 2][(i % 2) * 2], rc[k][i / 2][(i % 2) * 2 + 1]}; };
#pragma unroll
    for (int j = 0; j < 9; ++j) {
      f32x2 u[4], w[4];
#pragma unroll
      for (int i = 0; i < 4; ++i) u[i] = __builtin_elementwise_fma(RC(0, i), cs[j], f32x2{ac[i / 2][j][(i % 2) * 2], ac[i / 2][j][(i % 2) * 2 + 1]});
#pragma unroll
      for (int i = 0; i < 4; ++i) w[i] = u[i] * u[i];
#pragma unroll
      for (int i = 0; i < 4; ++i) w[i] = __builtin_elementwise_fma(w[i], RC(3, i), RC(2, i));
#pragma unroll
      for (int i = 0; i < 4; ++i) w[i] = u[i] * w[i];
      __builtin_amdgcn_sched_barrier(0);
#pragma unroll
      for (int i = 0; i < 4; ++i) w[i] = f32x2{__builtin_amdgcn_exp2f(w[i][0]), __builtin_amdgcn_exp2f(w[i][1])};
      __builtin_amdgcn_sched_barrier(0);
#pragma unroll
      for (int i = 0; i < 4; ++i) w[i] = __builtin_elementwise_fma(w[i], RC(1, i), RC(1, i));
      __builtin_amdgcn_sched_barrier(0);
#pragma unroll
      for (int i = 0; i < 4; ++i) w[i] = f32x2{__builtin_amdgcn_rcpf(w[i][0]), __builtin_amdgcn_rcpf(w[i][1])};
      __builtin_amdgcn_sched_barrier(0);
#pragma unroll
      for (int i = 0; i < 4; ++i) z[j][i] = u[i] * w[i];
#pragma unroll
      for (int i = 0; i < 4; ++i) {
        if (j == 1) sm[i] = z[0][i] + z[1][i];
        if (j > 1) sm[i] += z[j][i];
      }
#pragma unroll
      for (int i = 0; i < 4; ++i) sq[i] = j ? __builtin_elementwise_fma(z[j][i], z[j][i], sq[i]) : z[j][i] * z[j][i];
      asm volatile("" : "+v"(z[j][0]), "+v"(z[j][1]), "+v"(z[j][2]), "+v"(z[j][3]));
      __builtin_amdgcn_sched_barrier(0);
    }
  }
  f32x4 e;
  {
    float v[4][4], d[2][4], f[4];
#pragma unroll
    for (int i = 0; i < 4; ++i) {
      v[i][0] = sm[i][0];
      v[i][1] = sq[i][0];
      v[i][2] = sm[i][1];
      v[i][3] = sq[i][1];
    }
    asm volatile("s_nop 1" : "+v"(v[3][1]), "+v"(v[3][3]), "+v"(v[2][1]), "+v"(v[2][3]));
#pragma unroll
    for (int i = 0; i < 2; ++i)
#pragma unroll
      for (int c = 0; c < 4; ++c)
        asm volatile("v_add_f32_dpp %0, %1, %1 row_ror:8 row_mask:0xf bank_mask:0x3\n\tv_add_f32_dpp %0, %2, %2 row_ror:8 row_mask:0xf bank_mask:0xc"
                     : "=&v"(d[i][c])
                     : "v"(v[i][c]), "v"(v[i + 2][c]));
#pragma unroll
    for (int c = 0; c < 4; ++c)
      asm volatile("v_add_f32_dpp %0, %1, %1 row_shl:4 row_mask:0xf bank_mask:0x5\n\tv_add_f32_dpp %0, %2, %2 row_shr:4 row_mask:0xf bank_mask:0xa"
                   : "=&v"(f[c])
                   : "v"(d[0][c]), "v"(d[1][c]));
    asm volatile(
        "v_add_f32_dpp %0, %0, %0 quad_perm:[1,0,3,2] row_mask:0xf bank_mask:0xf\n\t"
        "v_add_f32_dpp %1, %1, %1 quad_perm:[1,0,3,2] row_mask:0xf bank_mask:0xf\n\t"
        "v_add_f32_dpp %2, %2, %2 quad_perm:[1,0,3,2] row_mask:0xf bank_mask:0xf\n\t"
        "v_add_f32_dpp %3, %3, %3 quad_perm:[1,0,3,2] row_mask:0xf bank_mask:0xf\n\t"
        "v_add_f32_dpp %0, %0, %0 quad_perm:[2,3,0,1] row_mask:0xf bank_mask:0xf\n\t"
        "v_add_f32_dpp %1, %1, %1 quad_perm:[2,3,0,1] row_mask:0xf bank_mask:0xf\n\t"
        "v_add_f32_dpp %2, %2, %2 quad_perm:[2,3,0,1] row_mask:0xf bank_mask:0xf\n\t"
        "v_add_f32_dpp %3, %3, %3 quad_perm:[2,3,0,1] row_mask:0xf bank_mask:0xf"
        : "+v"(f[0]), "+v"(f[1]), "+v"(f[2]), "+v"(f[3]));
    e = f32x4{f[0], __uint_as_float(__float_as_uint(f[1]) & 0x7fffffffu), f[2], __uint_as_float(__float_as_uint(f[3]) & 0x7fffffffu)};
  }
  asm volatile("s_mov_b64 exec, %3\n\tglobal_store_dwordx4 %0, %1, %2 sc1\n\ts_mov_b64 exec, -1\n\ts_nop 0" ::"v"(po), "v"(e), "s"(zp),
               "s"(0x1111111111111111ull)
               : "memory");
  asm volatile("s_mov_b64 exec, 1\n\tds_add_u32 %0, %1\n\ts_mov_b64 exec, -1" ::"v"(adw), "v"(mev) : "memory");
  __builtin_amdgcn_s_setprio(2);
  MK(12);
  const char* dst = uni_ptr(ws + OFF_Z2 + (size_t)layer * SZ_Z + (size_t)ni * S_ * 288 + (size_t)(mi * 4 + wm) * 9216 + 4096);
  const unsigned lane16 = (unsigned)lane * 16u;
  u32x4 wb[9];
  {
    static_assert(b_k(7) == 2 && b_k(8) == 3, "order");
    const char* wp = uni_ptr(ws + OFF_PI + (size_t)ni * (18 * K3C::WB) + 11 * K3C::WB + wm * 9216 + 4096);
#pragma unroll
    for (int j = 0; j < 8; ++j) asm volatile("global_load_dwordx4 %0, %1, %2 offset:%3 sc1" : "=v"(wb[j]) : "v"(lane16), "s"(wp), "n"(j * 1024 - 4096) : "memory");
    asm volatile("global_load_dwordx4 %0, %1, %2 sc1" : "=v"(wb[8]) : "v"(lane16 + 4096u), "s"(wp) : "memory");
  }
  u32x4 y[9];
  {
    const unsigned src = (unsigned)(g * 4096 + r * 16);
    unsigned zw = lds0 + (unsigned)(K3_ZC_OFF + (wm * 32 + 2 * r + (g & 1)) * 4), zr = lds0 + (unsigned)(K3_ZC_OFF + (wm * 32 + g * 8) * 4);
    asm volatile("" : "+v"(zw), "+v"(zr));
    f32x4 a, b, c, d;
    __builtin_amdgcn_s_sleep(6);
#pragma unroll 1
    for (unsigned spin = 0; spin < SPIN_MEM; ++spin) {
      asm volatile(
          "global_load_dwordx4 %0, %4, %5 sc1\n\t"
          "global_load_dwordx4 %1, %4, %5 offset:1024 sc1\n\t"
          "global_load_dwordx4 %2, %4, %5 offset:2048 sc1\n\t"
          "global_load_dwordx4 %3, %4, %5 offset:3072 sc1\n\t"
          "s_waitcnt vmcnt(0)"
          : "=&v"(a), "=&v"(b), "=&v"(c), "=&v"(d)
          : "v"(src), "s"(zp)
          : "memory");
      const unsigned bad = (__float_as_uint(a[1]) | __float_as_uint(a[3]) | __float_as_uint(b[1]) | __float_as_uint(b[3]) |
                            __float_as_uint(c[1]) | __float_as_uint(c[3]) | __float_as_uint(d[1]) | __float_as_uint(d[3])) >> 31;
      if (!__any(bad)) break;
    }
    MK(13);
    float s, q, zmb, zi;
    red16(a, b, c, d, s, q);
    ln_finish(s, q, 1.0f / (float)FH_, zmb, zi);
    f32x4 R[2], M[2];
    asm volatile(
        "ds_write2st64_b32 %4, %5, %6 offset1:2\n\t"
        "ds_read_b128 %0, %7\n\tds_read_b128 %2, %7 offset:512\n\tds_read_b128 %1, %7 offset:16\n\tds_read_b128 %3, %7 offset:528\n\t"
        "s_waitcnt lgkmcnt(0)"
        : "=&v"(R[0]), "=&v"(R[1]), "=&v"(M[0]), "=&v"(M[1])
        : "v"(zw), "v"(zi), "v"(-zmb * zi), "v"(zr)
        : "memory");
    MK(28);
    auto st = [&](int j) {
      if (j < 8) asm volatile("global_store_dwordx4 %0, %1, %2 offset:%3 sc1\n\ts_nop 1" ::"v"(lane16), "v"(y[j]), "s"(dst), "n"(j * 1024 - 4096) : "memory");
      else asm volatile("global_store_dwordx4 %0, %1, %2 sc1\n\ts_nop 1" ::"v"(lane16 + 4096u), "v"(y[j]), "s"(dst) : "memory");
    };
#pragma unroll
    for (int j = 0; j < 9; ++j) {
      {
        float t[8];
#pragma unroll
        for (int k = 0; k < 8; ++k) {
          t[k] = __builtin_fmaf(z[j][k / 2][k % 2], R[k / 4][k % 4], M[k / 4][k % 4]);
          asm volatile("" : "+v"(t[k]));
        }
        y[j] = u32x4{pack2(t[0], t[1]), pack2(t[2], t[3]), pack2(t[4], t[5]), pack2(t[6], t[7])};
      }
      st(j);
      __builtin_amdgcn_sched_barrier(0);
    }
    MK(29);
  }
  lds_inc(lw + LW_AD * 4, me);
  MK(24);
  __builtin_amdgcn_s_setprio(2);
  lds_spin4<2>(lw + LW_S * 4, 4u + (unsigned)(wm >> 1));
  {
    const unsigned fa = lds0 + (unsigned)((wm >> 1) * SB + K3C::XB + (wm & 1) * 9216) + lane16;
#pragma unroll
    for (int j = 0; j < 9; ++j) {
      asm volatile("ds_write_b128 %0, %1 offset:%2" ::"v"(fa), "v"(wb[j]), "n"(j * 1024) : "memory");
    }
    lds_inc(lw + LW_AD * 4, me);
  }
  announce(lw + LW_Z * 4, 4u, ws, CN_Z + layer, lane * 16 + ni, lane < 16);
  lds_inc(lw + LW_AD * 4, me);
  MK(27);
  f32x4 acc[2][9];
  {
    float bs[2];
    asm volatile("ds_read2_b32 %0, %1 offset1:16\n\ts_waitcnt lgkmcnt(0)"
                 : "=v"(*reinterpret_cast<f32x2*>(bs))
                 : "v"(lds0 + (unsigned)(K3_BS_OFF + (wm * 32 + r) * 4))
                 : "memory");
    lds_spin4<2>(lw + LW_S * 4, 6u + (unsigned)(wm >> 1));
    const unsigned fa = lds0 + (unsigned)((2 + (wm >> 1)) * SB + K3C::XB + (wm & 1) * 9216) + lane16;
#pragma unroll
    for (int j = 0; j < 9; ++j) {
      asm volatile("ds_write_b128 %0, %1 offset:%2" ::"v"(fa), "v"(y[j]), "n"(j * 1024) : "memory");
    }
    lds_inc(lw + LW_AD * 4, me);
#pragma unroll
    for (int mt = 0; mt < 2; ++mt) {
      f32x4 b4 = {bs[mt], bs[mt], bs[mt], bs[mt]};
      asm volatile("" : "+v"(b4));
#pragma unroll
      for (int j = 0; j < 9; ++j) acc[mt][j] = b4;
    }
  }
  u32x4 z1[9];
  const char* hp = uni_ptr(ws + OFF_H + (size_t)layer * SZ_Z + (size_t)((mi * 2 + (wm >> 1)) * 288 + ni * 18) * 1024);
  unsigned ho = (unsigned)((lane >> 5) * 1024 + ((wm & 1) * 32 + (lane & 31)) * 16);
  unsigned zb = lds0 + (unsigned)(wm * 4096) + lane16, zf = lw + (unsigned)(LW_S * 4 + wm);
  asm volatile("" : "+v"(ho), "+v"(zb), "+v"(zf));
  asm volatile("" : "+s"(hp));
  {
    CMath gm(acc);
    gm.setup(lds0, wm, lane);
    unsigned za = lds0 + (unsigned)(K3_Z1_OFF + wm * 5120) + lane16;
    asm volatile("" : "+v"(za));
    MK(15);
    lds_spin4<2>(lw + LW_L * 4, 8u);
    lds_spin4(lw + LW_AD * 4, 6u);
    MK(17);
    gm.first();
    __builtin_amdgcn_s_setprio(0);
    gm.stage<2, false>();
    gm.stage<3>();
    place();
#pragma unroll 1
    for (int t = 0; t < 7; ++t) {
      gm.stage<0>();
#pragma unroll
      for (int j = 0; j < NR1; ++j) asm volatile("ds_read_b128 %0, %1 offset:%2" : "=v"(z1[j]) : "v"(za), "n"(j * 1024));
      gm.stage<1>();
#pragma unroll
      for (int j = 0; j < NR1; ++j) asm volatile("" : "+v"(z1[j]));
      gm.stage<2>();
      gm.stage<3>();
    }
    gm.stage<0>();
    gm.stage<1, false, true>();
#pragma unroll
    for (int j = 0; j < 18; ++j) asm volatile("" : "+v"(acc[j / 9][j % 9]));
  }
  MK(16);
  if (NR1 < 5) lds_inc(lw + LW_AD * 4, me);
  unsigned zv;
  u32x4 zl[4];
#pragma unroll
  for (int j = 0; j < 9; ++j) {
    const bool sec = j >= NR1 + 3;
    if (j == NR1 || j == NR1 + 4) {
      unsigned n = 1u << 18;
      asm volatile(
          "s_waitcnt lgkmcnt(0)\n\tv_cmp_gt_u32 vcc, %8, %0\n\ts_cbranch_vccnz .Lw%=\n\t.subsection 1\n.Lw%=:\n\tds_read_u8 %0, %6\n\tds_read_b128 %2, %7 offset:%9\n\t"
          "ds_read_b128 %3, %7 offset:%9+1024\n\tds_read_b128 %4, %7 offset:%9+2048\n\tds_read_b128 %5, %7 offset:%9+3072\n\ts_waitcnt lgkmcnt(0)\n\t"
          "v_cmp_gt_u32 vcc, %8, %0\n\t" SAFE_OUT(".Lr%=") "s_cbranch_vccnz .Lw%=\n\ts_branch .Lr%=\n\t.subsection 0\n.Lr%=:"
          : "+v"(zv), NOP_(n), "+v"(zl[0]), "+v"(zl[1]), "+v"(zl[2]), "+v"(zl[3])
          : "v"(zf), "v"(zb), "n"(sec ? 11 : 10), "n"(sec ? SB : 0)
          : "memory", "vcc", "scc");
#pragma unroll
      for (int k = 0; k < 4; ++k)
        if (j + k < 9) z1[j + k] = zl[k];
    }
    if (j == NR1 - 1 || (j == NR1 + 3 && j < 8))
      asm volatile("ds_read_u8 %0, %5\n\tds_read_b128 %1, %6 offset:%7\n\tds_read_b128 %2, %6 offset:%7+1024\n\tds_read_b128 %3, %6 offset:%7+2048\n\t"
                   "ds_read_b128 %4, %6 offset:%7+3072"
                   : "=&v"(zv), "=&v"(zl[0]), "=&v"(zl[1]), "=&v"(zl[2]), "=&v"(zl[3])
                   : "v"(zf), "v"(zb), "n"(sec ? SB : 0)
                   : "memory");
    asm volatile("" : "+v"(z1[j]));
    u32x2 h[2];
#pragma unroll
    for (int mt = 0; mt < 2; ++mt) {
      const f32x4 a = acc[mt][j];
      h[mt] = u32x2{pack2v(f32x2{bf_lo(z1[j][2 * mt]), bf_hi(z1[j][2 * mt])} * f32x2{a[0], a[1]}),
                    pack2v(f32x2{bf_lo(z1[j][2 * mt + 1]), bf_hi(z1[j][2 * mt + 1])} * f32x2{a[2], a[3]})};
    }
    const auto s0 = __builtin_amdgcn_permlane16_swap(h[0][0], h[1][0], false, false);
    const auto s1 = __builtin_amdgcn_permlane16_swap(h[0][1], h[1][1], false, false);
    const u32x4 v = {s0[0], s1[0], s0[1], s1[1]};
    asm volatile("global_store_dwordx4 %0, %1, %2 offset:%3 sc1\n\ts_nop 1" ::"v"(ho), "v"(v), "s"(hp + (j / 2) * 4096), "n"((j % 2) * 2048) : "memory");
    __builtin_amdgcn_sched_barrier(0);
  }
  MK(18);
}

DEVI void k3_const(char* smem, char* ws, const u16* b_sp, int layer, int bid, int mi, int ni, int wm) {
  const int lane = lane_id(), l = lane < 54 ? lane : 53, p = l / 18;
  const char* kp = uni_ptr(ws + OFF_CSK + bid * 2048 + wm * 512);
  const unsigned q16 = (unsigned)(l - p * 18) * 16u;
  unsigned bs;
  f32x4 t;
  lds_spin((unsigned)(size_t)smem + (unsigned)(LW_OFF + (layer ? LW_T + 1 : LW_HELLO) * 4), layer ? 12u : 2u);
  asm volatile("global_load_ushort %0, %1, %2" : "=v"(bs) : "v"((unsigned)(lane & 31) * 2u), "s"(uni_ptr((const char*)(b_sp + mi * 128 + wm * 32))) : "memory");
  if (layer) {
    asm volatile("global_load_dwordx4 %0, %1, %2 sc1\n\ts_waitcnt vmcnt(0)" : "=&v"(t) : "v"(q16), "s"(kp) : "memory");
  } else {
    const char* cp = uni_ptr(ws + OFF_CSP + ni * 1152 + wm * 288);
    f32x4 a[6];
#pragma unroll
    for (int i = 0; i < 6; ++i)
      asm volatile("global_load_dwordx4 %0, %1, %2 sc1" : "=v"(a[i]) : "v"(q16 + (unsigned)(p * (6 * F_ * 4))), "s"(cp + i * (F_ * 4)) : "memory");
    asm volatile("s_waitcnt vmcnt(0)" : "+v"(a[0]), "+v"(a[1]), "+v"(a[2]), "+v"(a[3]), "+v"(a[4]), "+v"(a[5]));
    t = ((a[0] + a[1]) + (a[2] + a[3])) + (a[4] + a[5]);
#pragma unroll
    for (int j = 0; j < 4; ++j)
      t[j] += __int_as_float(__builtin_amdgcn_ds_bpermute((lane + 18) * 4, __float_as_int(t[j]))) +
              __int_as_float(__builtin_amdgcn_ds_bpermute((lane + 36) * 4, __float_as_int(t[j])));
  }
  asm volatile("" : "+v"(bs));
  const unsigned lds0 = (unsigned)(size_t)smem;
  f32x4 t0 = wm < 2 ? f32x4{t[0], t[0], t[1], t[1]} : t, t1 = wm < 2 ? f32x4{t[2], t[2], t[3], t[3]} : t;
  unsigned a0 = lds0 + (unsigned)(wm < 2 ? K3_C2_OFF + wm * 576 + lane * 32 : K3_C1_OFF + (wm - 2) * 288 + lane * 16), a1 = a0 + (wm < 2 ? 16u : 0u);
  unsigned ab = lds0 + (unsigned)(K3_BS_OFF + (wm * 32 + (lane & 31)) * 4);
  bs <<= 16;
  asm volatile("" : "+v"(t0), "+v"(t1), "+v"(a0), "+v"(a1), "+v"(ab), "+v"(bs));
  bar();
  asm volatile("ds_write_b32 %4, %5\n\ts_mov_b64 exec, 0x3ffff\n\tds_write_b128 %0, %2\n\tds_write_b128 %1, %3\n\ts_mov_b64 exec, -1" ::"v"(a0), "v"(a1),
               "v"(t0), "v"(t1), "v"(ab), "v"(bs)
               : "memory");
  if (layer == 0 && lane < 24) asm volatile("global_store_dwordx4 %0, %1, %2\n\ts_nop 1" ::"v"((unsigned)lane * 16u), "v"(t), "s"(kp) : "memory");
}

DEVI void p3_g1(char* smem, char* __restrict__ ws, int layer, int mi, int wm, int lane MK_ARG) {
  const unsigned lds0 = (unsigned)(size_t)smem;
  const int g = lane >> 4, r = lane & 15;
  const unsigned lw = lds0 + (unsigned)(LW_OFF + layer * 4);
  const unsigned xrd = lds0 + (unsigned)(g * 2048 + (wm * 32 + r) * 16);
  s16x8 xc[4][2][2];
  {
    const char* xs = uni_ptr(ws + OFF_XS + (size_t)layer * 2 * SZ_XS + (size_t)(mi * 2 + (wm >> 1)) * 4096 + (wm & 1) * 256);
    f32x4 a, b;
    auto pick = [&](int t) {
      bar();
      asm volatile(
          "ds_read_b128 %0, %4\n\tds_read_b128 %1, %4 offset:256\n\tds_read_b128 %2, %4 offset:8192\n\tds_read_b128 %3, %4 offset:8448\n\t"
          "s_waitcnt lgkmcnt(0)"
          : "=&v"(xc[t][0][0]), "=&v"(xc[t][0][1]), "=&v"(xc[t][1][0]), "=&v"(xc[t][1][1])
          : "v"(xrd + (unsigned)(t * K3C::SB))
          : "memory");
    };
    __builtin_amdgcn_s_setprio(2);
    pick(0);
    pick(1);
    asm volatile("global_load_dwordx4 %0, %2, %3 sc1\n\tglobal_load_dwordx4 %1, %2, %3 offset:512 sc1"
                 : "=&v"(a), "=&v"(b)
                 : "v"((unsigned)(g * 1024 + r * 16)), "s"(xs)
                 : "memory");
    pick(2);
    pick(3);
    bar();
    asm volatile("s_waitcnt vmcnt(0)" : "+v"(a), "+v"(b) : : "memory");
    float s, q, mb, rinv;
    red8(a, b, s, q);
    bar();
    asm volatile("" : "+v"(s), "+v"(q));
    ln_finish(s, q, 1.0f / (float)D_, mb, rinv);
    asm volatile("" : "+v"(mb), "+v"(rinv));
    bar();
    asm volatile("" : "+v"(mb), "+v"(rinv));
    const float k0 = -2.0f * 0.7978845608028654f * 1.4426950408889634f, k1 = k0 * 0.044715f;
    float sd = __builtin_amdgcn_rcpf(rinv), c0 = k0 * rinv, c1 = k1 * rinv * rinv * rinv;
    asm volatile("" : "+v"(sd));
    asm volatile("" : "+v"(c0));
    asm volatile("" : "+v"(c1));
    const float e0 = lane < 32 ? -mb : c0, e1 = lane < 32 ? sd : c1;
    asm volatile("ds_write2st64_b32 %0, %1, %2 offset1:2" ::"v"(
                     lds0 + (unsigned)(K3_ST_OFF + (g >> 1) * 1024 + (wm * 32 + 2 * r + (g & 1)) * 4)),
                 "v"(e0), "v"(e1)
                 : "memory");
    bar();
    asm volatile("s_waitcnt lgkmcnt(0)" ::: "memory");
    bar();
    __builtin_amdgcn_s_setprio(0);
  }
  f32x4 ac[2][9];
#pragma unroll
  for (int mt = 0; mt < 2; ++mt)
#pragma unroll
    for (int j = 0; j < 9; ++j) ac[mt][j] = f32x4{0.f, 0.f, 0.f, 0.f};
  {
    UMath<true> gm(ac, xc);
    gm.xa = xrd;
    gm.wa = lds0 + (unsigned)(K3C::XB + (g * 8 + (r & 7)) * K3C::P + (r >> 3) * 16);
    gm.fa = lw + (unsigned)(LW_AD * 4 + (lane & 3));
    gm.fw = lw + (unsigned)(LW_S * 4 + wm);
    gm.fv = 0u;
    asm volatile("" : "+v"(gm.fa), "+v"(gm.fw), "+v"(gm.fv), "+v"(gm.xa), "+v"(gm.wa));
    __builtin_amdgcn_s_sleep(40);
    lds_spin4<4>(lw + LW_L * 4, 4u);
    gm.first();
    place();
    lds_spin4(lw + LW_AD * 4, 2u);
    __builtin_amdgcn_s_setprio(1);
    gm.run();
    __builtin_amdgcn_s_setprio(0);
  }
  MK(14);
  f32x2 rc[4];
  f32x4 cs[9];
  const int l2 = lane_id();
  {
    const unsigned ra = lds0 + (unsigned)(K3_ST_OFF + (wm * 32 + (l2 & 15)) * 4), ca = lds0 + (unsigned)(K3_C1_OFF + (l2 & 48));
#pragma unroll
    for (int k = 0; k < 4; ++k) asm volatile("ds_read2_b32 %0, %1 offset1:16" : "=v"(rc[k]) : "v"(ra + (unsigned)(k * 512)) : "memory");
#pragma unroll
    for (int j = 0; j < 9; ++j) asm volatile("ds_read_b128 %0, %1 offset:%2" : "=v"(cs[j]) : "v"(ca), "n"(j * 64) : "memory");
    asm volatile("s_waitcnt lgkmcnt(0)" : "+v"(rc[0]), "+v"(rc[1]), "+v"(rc[2]), "+v"(rc[3]));
#pragma unroll
    for (int j = 0; j < 9; ++j) asm volatile("" : "+v"(cs[j]));
  }
  const unsigned za = lds0 + (unsigned)(K3_Z1_OFF + wm * 5120 + l2 * 16);
  u32x4 z1[9];
#pragma unroll
  for (int t = 0; t < 18; ++t) {
    const int j = t / 2, mt = t % 2;
    float u[4], w[4];
#define Q4(A, B, C, D) A "\n\t" B "\n\t" C "\n\t" D "\n\t"
    asm volatile(Q4("v_fma_f32 %0, %8, %12, %16", "v_fma_f32 %1, %8, %13, %17", "v_fma_f32 %2, %8, %14, %18", "v_fma_f32 %3, %8, %15, %19")
                 Q4("v_mul_f32 %4, %0, %0", "v_mul_f32 %5, %1, %1", "v_mul_f32 %6, %2, %2", "v_mul_f32 %7, %3, %3")
                 Q4("v_fma_f32 %4, %4, %11, %10", "v_fma_f32 %5, %5, %11, %10", "v_fma_f32 %6, %6, %11, %10", "v_fma_f32 %7, %7, %11, %10")
                 Q4("v_mul_f32 %4, %4, %0", "v_mul_f32 %5, %5, %1", "v_mul_f32 %6, %6, %2", "v_mul_f32 %7, %7, %3")
                 Q4("v_exp_f32 %4, %4", "v_exp_f32 %5, %5", "v_exp_f32 %6, %6", "v_exp_f32 %7, %7")
                 Q4("v_fma_f32 %4, %4, %9, %9", "v_fma_f32 %5, %5, %9, %9", "v_fma_f32 %6, %6, %9, %9", "v_fma_f32 %7, %7, %9, %9")
                 Q4("v_rcp_f32 %4, %4", "v_rcp_f32 %5, %5", "v_rcp_f32 %6, %6", "v_rcp_f32 %7, %7")
                 Q4("v_mul_f32 %4, %4, %0", "v_mul_f32 %5, %5, %1", "v_mul_f32 %6, %6, %2", "v_mul_f32 %7, %7, %3")
                 : "=&v"(u[0]), "=&v"(u[1]), "=&v"(u[2]), "=&v"(u[3]), "=&v"(w[0]), "=&v"(w[1]), "=&v"(w[2]), "=&v"(w[3])
                 : "v"(rc[0][mt]), "v"(rc[1][mt]), "v"(rc[2][mt]), "v"(rc[3][mt]), "v"(cs[j][0]), "v"(cs[j][1]), "v"(cs[j][2]), "v"(cs[j][3]),
                   "v"(ac[mt][j][0]), "v"(ac[mt][j][1]), "v"(ac[mt][j][2]), "v"(ac[mt][j][3]));
    z1[j][2 * mt] = pack2(w[0], w[1]);
    z1[j][2 * mt + 1] = pack2(w[2], w[3]);
    if (mt && j < NR1) asm volatile("ds_write_b128 %0, %1 offset:%2" ::"v"(za), "v"(z1[j]), "n"(j * 1024) : "memory");
    if (t == 9) asm volatile("s_waitcnt lgkmcnt(0)" ::: "memory");
    __builtin_amdgcn_sched_barrier(0);
    bar();
    __builtin_amdgcn_sched_barrier(0);
  }
  MK(15);
#pragma unroll 1
  for (int t = 20; t < 32; ++t) bar();
  __builtin_amdgcn_s_sleep(9);
#pragma unroll
  for (int k = NR1; k < 9; ++k) {
    if (k == NR1 + 4) lds_spin4(lw + LW_AD * 4, 7u);
    asm volatile("ds_write_b128 %0, %1 offset:%2" ::"v"(lds0 + (unsigned)(wm * 4096 + l2 * 16)), "v"(z1[k]),
                 "n"(((k - NR1) / 4) * K3C::SB + ((k - NR1) % 4) * 1024)
                 : "memory");
    if (k == NR1 + 3 || k == 8) asm volatile("ds_write_b8 %0, %1" ::"v"(lw + (unsigned)(LW_S * 4 + wm)), "v"(vconst(k == NR1 + 3 ? 10u : 11u)) : "memory");
  }
}

constexpr int W5A = 22;
constexpr int NPF = 30;
struct Loader {
  u32x4 pf[NPF];
  unsigned lds0, lane16;
  int wl, lane;

  template <int R0, int N, int P0, int PE>
  DEVI void pf_load(const char* src) {
    const unsigned o = lane16 + (unsigned)(wl * 1024);
#pragma unroll
    for (int t = 0; t < N; ++t) {
      unsigned a = o + (unsigned)((P0 + 4 * t) * 1024);
      if (P0 + 4 * t + 3 >= PE && P0 + 4 * t + wl >= PE) a = lane16;
      asm volatile("global_load_dwordx4 %0, %1, %2 sc1" : "=v"(pf[R0 + t]) : "v"(a), "s"(src) : "memory");
    }
  }
  template <int R0, int N>
  DEVI void pf_pin() {
#pragma unroll
    for (int t = 0; t < N; ++t) asm volatile("" : "+v"(pf[R0 + t]));
  }
  template <int R0, int N>
  DEVI void pf_undef() {
#pragma unroll
    for (int t = 0; t < N; ++t) asm volatile("" : "=v"(pf[R0 + t]));
  }
  template <int PB, int A, int N>
  DEVI void pf_put(unsigned dst) {
    constexpr int T0 = (A - PB) / 4, T1 = (A + N - 1 - PB) / 4;
    const unsigned a = lds0 + dst + lane16 + (unsigned)((wl + PB + 4 * T0 - A + 4) * 1024) - 4096u;
#pragma unroll
    for (int t = T0; t <= T1; ++t) {
      const int p0 = PB + 4 * t;
      if ((p0 >= A || p0 + wl >= A) && (p0 + 3 < A + N || p0 + wl < A + N))
        asm volatile("ds_write_b128 %0, %1 offset:%2" ::"v"(a), "v"(pf[t]), "n"((t - T0) * 4096) : "memory");
    }
  }
  template <int N>
  DEVI void dma_run(const char* src, unsigned dst) {
    constexpr int B = N / 4, R = N % 4;
    const unsigned f = (unsigned)(wl * B + (wl < R ? wl : R)) * 1024u, o = lane16 + f, m = lds0 + dst + f;
    dma_seq<B>(src, o, m);
    if (wl < R) dma16_sc1(src + B * 1024, o, m + (unsigned)(B * 1024));
  }
  template <int V>
  DEVI void wait_w() {
    asm volatile("s_cmp_lt_u32 %0, 2\n\ts_cbranch_scc1 1f\n\ts_waitcnt vmcnt(%1)\n1:\n\ts_waitcnt vmcnt(%2)" ::"s"(wl), "n"(V - 1), "n"(V) : "memory", "scc");
  }
  template <int V>
  DEVI void wbar() {
    asm volatile("s_waitcnt vmcnt(%0) lgkmcnt(0)" ::"n"(V) : "memory");
    bar();
  }

  DEVI void p1(char* smem, char* ws, int bid, int rt, int hp MK_ARG) {
    constexpr int SB = K1C::SB;
    static_assert(K1C::XB == 18 * 1024 && K1C::WB == 14 * 1024, "K1 shape");
    const char* wp = uni_ptr(ws + OFF_PQ + (size_t)hp * (9 * K1C::WB) + 4 * K1C::WB);
    static_assert(3 * SB + K1C::XB >= P5_W + 5 * 18432, "free while the GEMM before ends");
    const char* wq = wp - 4 * K1C::WB;
    dma_run<14>(wq + 3 * K1C::WB, (unsigned)(3 * SB + K1C::XB));
    dma_run<28>(wp, (unsigned)K1_WX);
    if (wl == 3) dma16_sc1(uni_ptr(ws + OFF_CSQ + bid * 512), lane16 & 511u, lds0 + (unsigned)K1_CS_OFF);
    __builtin_amdgcn_s_setprio(0);
    wait_vm<0>();
    const bool lo = wl < 2;
    const int x0 = lo ? wl * 5 : wl * 4 + 2, w0 = lo ? wl * 3 : wl * 4 - 2;
    unsigned xo[5];
#pragma unroll
    for (int i = 0; i < 5; ++i) {
      const int c = (x0 + i) * 64 + lane;
      const int ch = c / 144;
      int gr = rt * 128 - 16 + (c - ch * 144);
      if (gr < 0) gr = 0;
      xo[i] = (unsigned)(((gr >> 6) * 72 + ch + 3 - (i & 3)) * 1024 + (gr & 63) * 16);
      asm volatile("" : "+v"(xo[i]));
    }
    const char* xp = uni_ptr(ws + OFF_X1 + SZ_X - 3072);
    wp += 2 * K1C::WB + w0 * 1024;
    auto dma_x = [&](unsigned so) {
      const unsigned m = lds0 + so + (unsigned)(x0 * 1024);
      dma4<4, true>(xp, xo[0], xo[1], xo[2], xo[3], m);
      if (lo) dma16_sc1(xp, xo[4], m + 4096u);
      xp += 8192;
    };
    auto dma_w = [&](unsigned so) {
      const unsigned m = lds0 + so + (unsigned)(K1C::XB + w0 * 1024);
      dma_seq<3>(wp, lane16, m);
      if (!lo) dma16_sc1(wp + 3072, lane16, m + 3072u);
      wp += K1C::WB;
    };
    const char* xs = uni_ptr(ws + OFF_XS + SZ_XS);
    const int g = lane >> 4, p2 = 64 + (lane & 7);
    int ra = rt * 128 - 16 + wl * 32 + 2 * (lane & 15);
    if (ra < 0) ra = 0;
    unsigned sa = (unsigned)(((ra >> 6) * 8 + g * 2) * 512 + (ra & 63) * 8);
    unsigned sb = (unsigned)(((rt * 2 + 1) * 8 + (lane >> 3)) * 512 + (2 * p2 - 80) * 8);
    unsigned sw = lds0 + (unsigned)(K1_ST_OFF + (2 * (lane < 32 ? wl * 16 + (lane & 15) : p2) + (g & 1)) * 8);
    asm volatile("" : "+v"(sa), "+v"(sb), "+v"(sw));
    MK(22);
    __builtin_amdgcn_s_setprio(3);
    bar();
    dma_run<14>(wq, (unsigned)K1C::XB);
    dma_run<14>(wq + K1C::WB, (unsigned)(SB + K1C::XB));
    dma_run<14>(wq + 2 * K1C::WB, (unsigned)(2 * SB + K1C::XB));
    MK(1);
    bar();
    dma_x(0u);
    dma_x((unsigned)SB);
    wbar<4>();
    MK(29);
    dma_x((unsigned)(2 * SB));
    dma_x((unsigned)(3 * SB));
    wbar<8>();
    MX(1, 3);
    dma_x(0u);
    wbar<8>();
    MX(1, 4);
    dma_x((unsigned)SB);
    f32x4 a, b, e;
    asm volatile("global_load_dwordx4 %0, %3, %5 sc1\n\tglobal_load_dwordx4 %1, %3, %5 offset:512 sc1\n\tglobal_load_dwordx4 %2, %4, %5 sc1"
                 : "=&v"(a), "=&v"(b), "=&v"(e)
                 : "v"(sa), "v"(sb), "s"(xs)
                 : "memory");
    wbar<11>();
    MX(1, 5);
    dma_w((unsigned)SB);
    dma_x((unsigned)(2 * SB));
    wbar<15>();
    MX(1, 6);
    dma_w((unsigned)(2 * SB));
    dma_x((unsigned)(3 * SB));
    wbar<19>();
    MX(1, 7);
    dma_w((unsigned)(3 * SB));
    dma_x(0u);
    wbar<16>();
    MX(1, 8);
    asm volatile("" : "+v"(a), "+v"(b), "+v"(e));
    float s, q;
    {
      float t[4] = {e[0], e[1], e[2], e[3]}, s2, q2;
      asm volatile(
          "v_add_f32_dpp %0, %0, %0 row_ror:8 row_mask:0xf bank_mask:0xf\n\tv_add_f32_dpp %1, %1, %1 row_ror:8 row_mask:0xf bank_mask:0xf\n\t"
          "v_add_f32_dpp %2, %2, %2 row_ror:8 row_mask:0xf bank_mask:0xf\n\tv_add_f32_dpp %3, %3, %3 row_ror:8 row_mask:0xf bank_mask:0xf\n\ts_nop 1"
          : "+v"(t[0]), "+v"(t[1]), "+v"(t[2]), "+v"(t[3]));
      red4(t, s2, q2);
      red8(a, b, s, q);
      s = lane < 32 ? s : s2;
      q = lane < 32 ? q : q2;
      asm volatile("" : "+v"(s), "+v"(q));
    }
    wbar<8>();
    MX(1, 9);
    {
      asm volatile("" : "+v"(s), "+v"(q));
      float mb, rinv;
      ln_finish(s, q, 1.0f / (float)D_, mb, rinv);
      asm volatile("ds_write_b64 %0, %1" ::"v"(sw), "v"(f32x2{-mb, rinv}) : "memory");
    }
    wbar<0>();
    MK(30);
  }

  template <int I>
  DEVI void p5_step(const char* wp) {
    constexpr int T = 2 * I + 1;
    wbar<(T == 1 ? 63 : (T <= 11 ? 3 : 0))>();
    if constexpr (T <= 9) {
      const unsigned wo = (unsigned)(12288 + (wl & 1) * 3072), so = (unsigned)(P5_W + ((T + 2 + (wl >> 1)) & 7) * 18432);
      dma_seq<3>(wp + (T + 7) * 18432, lane16 + wo + (unsigned)((wl >> 1) * 18432), lds0 + so + wo);
      pf_put<0, 12 * (T - 1), 12>((unsigned)(P5_W + ((T + 2) % 8) * 18432));
      pf_put<0, 12 * T, 12>((unsigned)(P5_W + ((T + 3) % 8) * 18432));
    }
  }
  template <int... S>
  DEVI void p5_steps(const char* wp, std::integer_sequence<int, S...>) {
    (p5_step<S>(wp), ...);
  }

  template <int T0, int T1>
  DEVI void w5_load(const char* w5p) {
    const unsigned o = lane16 + (unsigned)(wl * 1024);
#pragma unroll
    for (int t = T0; t < T1; ++t)
      asm volatile("global_load_dwordx4 %0, %1, %2 sc1" : "=v"(pf[t]) : "v"(o + (unsigned)((t / 3 + 8) * 18432 + (t % 3) * 4096)), "s"(w5p) : "memory");
  }
  DEVI void p35(char* smem, char* ws, const u16* w_sp, int layer, int bid, int mi, int ni, int rb, int ct, int hp MK_ARG) {
    using C = K3C;
    constexpr int NS = C::NS, SB = C::SB, NDX = 4, NDW = 5, VPS = NDX + NDW;
    const char* wip = uni_ptr(ws + OFF_PI + (size_t)ni * (18 * C::WB));
    if (layer) {
      pf_put<-48, 36, 18>((unsigned)(2 * SB + C::XB));
      pf_put<-48, 54, 18>((unsigned)(3 * SB + C::XB));
    } else {
      dma_run<18>(wip + 2 * C::WB, (unsigned)(2 * SB + C::XB));
      dma_run<18>(wip + 3 * C::WB, (unsigned)(3 * SB + C::XB));
    }
    dma_run<18>(wip + C::WB, (unsigned)(SB + C::XB));
    const char* w5p = uni_ptr(ws + OFF_PW + (size_t)ct * (36 * 9216));
    const int w0 = wl < 2 ? wl * 5120 : wl * 4096 + 2048;
    const unsigned m0x = lds0 + (unsigned)(wl * 4096), m0w = lds0 + (unsigned)(C::XB + w0);
    const unsigned wo = lane16 + (unsigned)w0;
    const unsigned lw = lds0 + (unsigned)(LW_OFF + layer * 4), me = 1u << (8 * wl);
    const unsigned wo4 = wo + 4096u;
    auto dma_w = [&](const char* wp, unsigned so) {
      dma_seq<4>(wp, wo, m0w + so);
      if (wl < 2) dma16_sc1(wp, wo4, m0w + so + 4096u);
    };
    {
      const char* xp = uni_ptr(ws + OFF_X1 + (size_t)layer * 2 * SZ_X + (size_t)(mi * 2 * 72 + wl * 2) * 1024 - 1024);
      const unsigned xo0 = lane16 + 1024u, xo1 = lane16 + 72u * 1024u, xo3 = lane16 + 71u * 1024u;
      auto dma_x = [&](int k, unsigned so) { dma4<4, true>(xp + k * 8192, xo0, xo1, lane16, xo3, m0x + so); };
      MK(10);
      bar();
      dma_x(0, 0u);
      dma_x(1, (unsigned)SB);
      wait_vm<4>(); MX(3, 2); wbar<4>(); MX(2, 2);
      MK(25);
      {
        dma_x(2, (unsigned)(2 * SB));
        dma_x(3, (unsigned)(3 * SB));
        dma_x(4, (unsigned)(4 * SB));
        wbar<12>(); MX(2, 3);
        dma_x(5, 0u);
        wait_vm<12>();
      }
      bar(); MX(2, 4);
      dma_w(wip + 4 * C::WB, 0u);
      dma_x(6, (unsigned)SB);
      wait_w<8 + VPS>();
#pragma unroll 1
      for (int t = 1; t < 3; ++t) {
        bar(); MX(2, 4 + t);
        dma_w(wip + (t + 4) * C::WB, (unsigned)(t * SB));
        dma_x(t + 6, (unsigned)((t + 1) * SB));
        wait_w<NDX + VPS>();
      }
      bar(); MX(2, 7);
      dma_w(wip + 7 * C::WB, (unsigned)(3 * SB));
      wait_w<VPS>();
      bar(); MX(2, 8);
      dma_w(wip + 8 * C::WB, 0u);
      wait_w<NDW>();
      bar(); MX(2, 9);
      const char* wp = wip + 9 * C::WB;
      dma_w(wp + b_k(0) * C::WB, (unsigned)SB);
      if (layer == 0) {
        const char* sp = uni_ptr((const char*)w_sp + (size_t)(bid * 4 + wl) * 8192);
        const unsigned so = (unsigned)((lane >> 5) * 4096 + (lane & 31) * 16);
#pragma unroll
        for (int i = 0; i < WSP_V; ++i) dma16(sp + i * 512, so, lds0 + (unsigned)K3_DM_OFF);
        dma16(sp + (bid >> 4) * 256, so & ~0x100u, lds0 + (unsigned)K3_DM_OFF);
      }
      w5_load<0, 4>(w5p);
      wait_w<NDW + 4>();
      MX(3, 10); bar(); MX(2, 10);
      dma_w(wp + b_k(1) * C::WB, (unsigned)(2 * SB));
      w5_load<4, 8>(w5p);
      MK(26);
      lds_spin4(lw + LW_AD * 4, 1u);
      dma_w(wp + b_k(2) * C::WB, (unsigned)(3 * SB));
      dma_w(wp + b_k(3) * C::WB, 0u);
      w5_load<8, W5A>(w5p);
      wait_vm<W5A - 8>();
      lds_inc(lw + LW_L * 4, 4u * me);
      w5_load<W5A, 30>(w5p);
      MK(11);
      static_for<4, 7>([&](auto T_) {
        constexpr int t = T_;
        lds_spin4<2>(lw + LW_S * 4, (unsigned)(t - 3));
        MX(2, 12 + t);
        dma_w(wp + b_k(t) * C::WB, (unsigned)(((t + 1) % 4) * SB));
        if constexpr (t > 4) {
          wait_w<NDW>();
          lds_inc(lw + LW_L * 4, me);
        }
      });
      MX(2, 19);
      wait_vm<0>();
      lds_inc(lw + LW_L * 4, me);
      pf_pin<0, 30>();
      MK(13);
    }
    {
      unsigned xoff[NDX];
#pragma unroll
      for (int i = 0; i < NDX; ++i) {
        const int c = wl * 256 + i * 64 + lane;
        const int row = c >> 3, cc = c & 7;
        xoff[i] = (unsigned)((mi * 128 + row) * S_ * 2 - i * 1024) + (unsigned)((cc ^ ((row >> 1) & 7)) * 16);
      }
      auto dma_x = [&](const char* xp, unsigned so) { dma4<4, false>(xp, xoff[0], xoff[1], xoff[2], xoff[3], m0x + so); };
      const char* xp = uni_ptr((const char*)w_sp);
      const char* wp = uni_ptr(ws + OFF_Z2 + (size_t)layer * SZ_Z + (size_t)ni * S_ * 288);
      lds_spin4<2>(lw + LW_AD * 4, 5u);
      dma_x(xp + mi * 256, (unsigned)(2 * SB));
      dma_x(xp + mi * 256 + 128, (unsigned)(3 * SB));
      MK(24);
      int left = 2 * mi;
      auto next = [&]() {
        const int d = --left == 0 ? 3 : 1;
        xp += d * 128;
        wp += d * C::WB;
      };
      if (mi == 0) {
        xp += 256;
        wp += 2 * C::WB;
      }
      dma_x(xp, 0u);
      lds_spin4(lw + LW_S * 4, 6u);
      dma_x(xp + 128, (unsigned)SB);
      wait_vm<2 * NDX>();
      lds_inc(lw + LW_L * 4, me);
      MK(27);
      if (wl == 0) {
        cnt_wait(ws, CN_Z + layer, mi * 16 + ni, 16u);
        lds_wr32(lw + LW_GO * 4, 1u);
      } else {
        lds_spin(lw + LW_GO * 4, 1u);
      }
      MK(14);
      lds_spin4(lw + LW_S * 4, 8u);
      dma_w(wp, 0u);
      lds_spin4(lw + LW_S * 4, 9u);
      dma_w(wp + C::WB, (unsigned)SB);
      next();
      next();
      unsigned slot = (unsigned)(2 * SB);
      auto cissue = [&]() {
        dma_x(xp, slot);
        dma_w(wp, slot);
        next();
        slot = (slot == (unsigned)((NS - 1) * SB)) ? 0u : slot + (unsigned)SB;
      };
      auto cstep = [&]() {
        bar();
        cissue();
      };
      wait_w<NDW>();
      MK(15);
      int j = 2;
      if (layer == 0) {
        const char* sp = uni_ptr((const char*)w_sp + (size_t)(bid * 4 + wl) * 8192 + WSP_V * 512);
        unsigned d = 0u;
#pragma unroll 1
        for (; j < 2 + 32 - 4 * WSP_V; ++j) {
          cstep();
          asm volatile("s_load_dword %0, %1, 0\n\ts_load_dword %0, %1, 64\n\ts_load_dword %0, %1, 0x1000\n\ts_load_dword %0, %1, 0x1040"
                       : "+s"(d)
                       : "s"(sp));
          sp += 128;
          wait_w<VPS>();
        }
#pragma unroll 1
        for (int e = j + 4; j < e; ++j) {
          cstep();
          asm volatile("" : "+s"(d));
          wait_w<VPS>();
        }
        asm volatile("s_waitcnt lgkmcnt(0)" : "+s"(d));
      }
      place();
#pragma unroll 1
      for (; j <= 29; ++j) {
        cstep();
        wait_w<VPS>();
      }
      bar();
    }
    dma_run<33>(w5p, (unsigned)(P5_W + 54 * 1024));
    dma_run<23>(w5p + 67 * 1024, (unsigned)(P5_W + 121 * 1024));
    if (wl == 3) dma16_sc1(w5p + 143 * 1024, lane16, lds0 + (unsigned)(P5_W + 53 * 1024));
    wait_vm<14>();
    bar();
    dma_run<34>(w5p + 33 * 1024, (unsigned)(P5_W + 87 * 1024));
    wait_vm<0>();
    bar();
    if (wl) {
      const unsigned f = (unsigned)(wl - 1) * 18432u;
      dma_seq<17>(w5p + 90 * 1024, lane16 + f, lds0 + (unsigned)P5_W + f);
      if (wl < 3) dma16_sc1(w5p + 107 * 1024, lane16 + f, lds0 + (unsigned)(P5_W + 17 * 1024) + f);
    } else {
      cnt_wait(ws, CN_H + layer, rb * 8 + ct, 8u);
    }
    MK(20);
    bar();
    p5_steps(w5p, std::make_integer_sequence<int, 7>{});
    if (layer == 0) {
      if (bid < 24 && wl == 0 && lane < 8) pk_st16(ws + OFF_HELLO + bid * 128 + lane16, u32x4{0u, 0u, 0u, 0u});
    }
  }
};

__global__ __launch_bounds__(768, 3) void k_main(const u16* __restrict__ x0, const u16* __restrict__ wqkv, const u16* __restrict__ wo,
                                                 const u16* __restrict__ w_in, const u16* __restrict__ w_sp,
                                                 const u16* __restrict__ b_sp, const u16* __restrict__ w_out,
                                                 u16* __restrict__ out, char* ws) {
  extern __shared__ __attribute__((aligned(16))) char smem[];
  const char* code0 = code_here();
  (void)code0;
  const unsigned lds0 = (unsigned)(size_t)smem;
  int wave = __builtin_amdgcn_readfirstlane((int)threadIdx.x >> 6);
  int bid = blockIdx.x;
#define FRESH() asm volatile("" : "+s"(bid), "+s"(ws), "+s"(wave))
#define XCD (bid & 7)
#define IDX (bid >> 3)
#define MI ((XCD >> 2) * 8 + (IDX >> 2))
#define NI ((XCD & 1) * 8 + (XCD & 2) * 2 + (IDX & 3))
#define RB (XCD * 4 + (IDX >> 3))
#define CT (IDX & 7)
#define RT ((XCD >> 1) * 4 + (IDX >> 3))
#define HP ((XCD & 1) * 8 + (IDX & 7))
  MK(0);
  if (wave == 2) {
    constexpr int PER = (2 * NL_MAIN + 31) / 32;
    const int lane = lane_id();
    static_assert(PER <= 64, "one load");
    int li = IDX * PER + (lane < PER ? lane : PER - 1);
    if (li > 2 * NL_MAIN - 1) li = 2 * NL_MAIN - 1;
    touch64(code0, (unsigned)li * 64u, lds0 + (unsigned)(K1R_DUMMY + 768));
  }
  if (wave >= 3 && wave < 8) {
    constexpr int ST = 64;
    const int k = (7 - wave) * 64 + lane_id();
    const bool isx = k < 9216 / ST;
    int o = isx ? bid * 9216 + k * ST : bid * 7776 + (k - 9216 / ST) * ST;
    if (!isx && k - 9216 / ST > 7775 / ST) o = bid * 7776;
    const char* p = (isx ? (const char*)x0 : (const char*)wqkv) + o;
    asm volatile("s_mov_b32 m0, %1\n\ts_nop 0\n\tglobal_load_lds_dword %0, off" ::"v"(p), "s"(lds0 + (unsigned)K1R_DUMMY) : "memory");
  }

  if (__builtin_expect(wave >= 8, 1)) {
    __builtin_amdgcn_s_setprio(3);
    {
      const int lane = lane_id();
      RawLd rl;
      const bool cross = (((HP * 72) & ~15) & 127) > 48;
      const int c16 = IDX >> 1;
      const int cj = (int)((0x65743210u >> (4 * XCD)) & 7u) * 16 + c16;
      const int job = cross ? (cj < 88 ? 128 + cj : 999) : XCD * 16 + c16;
      const int pj = cross ? cj - 88 : ((XCD & 5) == 0 && (c16 & 3) == (c16 >> 2) ? 40 + XCD * 2 + (c16 >> 2) : -1);
      rl.front(x0, wqkv, wo, ws, bid, lds0, wave - 8, lane, RT, HP, pj MF_PASS);
      if (job < 216) k1_raw_loader<true>(rl, w_in, w_out, ws, rl.tag, bid, lds0, wave - 8, lane, RT, HP, CT, job, pj MF_PASS);
      else k1_raw_loader<false>(rl, w_in, w_out, ws, rl.tag, bid, lds0, wave - 8, lane, RT, HP, CT, job, pj MF_PASS);
    }
    Loader ld;
    ld.lds0 = lds0;
#pragma unroll 1
    for (int layer = 0; layer < 2; ++layer) {
      FRESH();
      ld.wl = wave - 8;
      ld.lane = lane_id();
      ld.lane16 = (unsigned)ld.lane * 16u;
      const char* wip = uni_ptr(ws + OFF_PI + (size_t)NI * (18 * K3C::WB));
      if (layer) {
        ld.p1(smem, ws, bid, RT, HP MK_PASS);
        ld.pf_load<0, 21, 0, 81>(uni_ptr(ws + OFF_PO + (size_t)CT * (18 * 4608)));
        ld.pf_load<21, 9, 36, 72>(wip);
        wait_vm<9>();
        ld.pf_pin<0, 21>();
        lds_spin(lds0 + (unsigned)(LW_OFF + (LW_T + layer) * 4), 8u);
        __builtin_amdgcn_s_sleep(4);
        ld.pf_put<0, 0, 40>((unsigned)P2_W);
        wait_vm<0>();
        ld.pf_pin<21, 9>();
        bar();
        ld.pf_put<0, 40, 41>((unsigned)(P2_W + 40 * 1024));
      } else {
        ld.pf_undef<21, 9>();
        MK(3);
        MK(4);
        bar();
        ld.dma_run<81>(uni_ptr(ws + OFF_PO + (size_t)CT * (18 * 4608)), (unsigned)P2_W);
      }
      if (layer == 0 && ld.wl == 0) cnt_wait(ws, CN_O + layer, RB * 8 + CT, 16u);
      MK(6);
      asm volatile("s_waitcnt vmcnt(0) lgkmcnt(0)" ::: "memory");
      bar();
      lds_spin(lds0 + (unsigned)(LW_OFF + (LW_T + layer) * 4), 12u);
      if (layer == 0) {
        if (ld.wl == 0) {
          cnt_wait(ws, CN_PK, 8 + NI, 36u);
          lds_wr32(lds0 + (unsigned)(LW_OFF + LW_HELLO * 4), 2u);
        } else {
          lds_spin(lds0 + (unsigned)(LW_OFF + LW_HELLO * 4), 2u);
        }
        MK(7);
      }
      static_assert(2 * K3C::SB <= P2_W, "free during the first residual GEMM");
      ld.dma_run<18>(wip, (unsigned)K3C::XB);
      MX(6, 24);
      if (layer == 0 && ld.wl == 0) cnt_wait(ws, CN_PK, 24 + CT, 72u);
      wait_vm<0>();
      ld.pf_pin<21, 9>();
      MX(6, 25);
      bar();
      FRESH();
      ld.wl = wave - 8;
      ld.p35(smem, ws, w_sp, layer, bid, MI, NI, RB, CT, HP MK_PASS);
    }
  } else {
    MF(12);
    math_start(smem, b_sp, wqkv, ws, bid, wave, lane_id());
    MF(13);
#pragma unroll 1
    for (int layer = 0; layer < 2; ++layer) {
      FRESH();
      {
        const int lane = lane_id(), r0 = RT * 128;
        float* csq = reinterpret_cast<float*>(ws + OFF_CSQ + bid * 512);
        Attn at;
        at.setup(lds0 + (unsigned)(layer ? K1_T_OFF : K1R_T_OFF), ws + OFF_O + (size_t)layer * SZ_X, r0, HP, wave, lane);
        if (layer) {
          if ((wave & 2) == 0) k1_gemm<2, 4>(smem, ws, RT, HP, (wave & 1) * 2 + (wave & 4), 0, wave >= 4, lane MK_PASS);
          else if (wave < 4) k1_gemm<3, 3>(smem, ws, RT, HP, (wave & 1) * 3, 4, false, lane MK_PASS);
          else if (wave == 6) k1_gemm<2, 3>(smem, ws, RT, HP, 6, 4, true, lane MK_PASS);
          else k1_gemm<1, 7>(smem, ws, RT, HP, 8, 0, true, lane MK_PASS);
        } else {
          if (wave == 0) k1_gemm_raw<3, false>(smem, HP, 0, lane MF_PASS);
          else if (wave < 4) k1_gemm_raw<2, false>(smem, HP, 1 + 2 * wave, lane MF_PASS);
          else if (wave < 7) k1_gemm_raw<2, true>(smem, HP, 2 * (wave - 4), lane MF_PASS);
          else k1_gemm_raw<3, true>(smem, HP, 6, lane MF_PASS);
        }
        msync(lds0 + (unsigned)(LW_OFF + (LW_T + layer) * 4));
        MK(3);
        at.run(r0, HP, wave, lane);
        if (layer == 0) {
          if (wave == 0 && lane < 32)
            asm volatile("global_store_dwordx4 %0, %1, %2 sc1\n\ts_nop 1" ::"v"((unsigned)lane * 16u),
                         "v"(*reinterpret_cast<const u32x4*>(smem + K1R_CS_OFF + lane * 16)), "s"(uni_ptr((const char*)csq))
                         : "memory");
          lds_spin(lds0 + (unsigned)(LW_OFF + LW_HELLO * 4), 1u);
        }
        MK(4);
        announce(lds0 + (unsigned)(LW_OFF + (LW_O + layer * 22 + (wave >> 2)) * 4), 4u, ws, CN_O + layer, (RT * 2 + (wave >> 2)) * 8 + lane,
                 lane < 8);
        MK(5);
        bar();
      }
      FRESH();
      {
        if (wave < 4) {
          const int lane = lane_id();
          u32x2 xv[5];
          ResIn ri;
          ri.setup(layer ? ws + OFF_X1 + SZ_X : (const char*)x0, layer == 0, RB, CT, wave, lane);
          ri.load(xv);
          ResOut ro;
          ro.setup(ws + OFF_X1 + (size_t)layer * 2 * SZ_X, ws + OFF_XS + (size_t)layer * 2 * SZ_XS, out, false, RB, CT, wave, lane);
          f32x4 acc[2][5];
          p2_gemm(acc, xv, lds0, uni_ptr(ws + OFF_O + (size_t)layer * SZ_X + (size_t)RB * (72 * 1024)), layer, wave, lane MK_PASS);
          MK(7);
          ro.run(acc[0], false);
          MK(8);
          announce(lds0 + (unsigned)(LW_OFF + (LW_X1 + layer) * 4), 4u, ws, CN_X1 + layer, (RB >> 1) * 16 + lane, lane < 16);
          MK(9);
          bar();
          FRESH();
          if (wave == 3) cnt_wait(ws, CN_X1 + layer, MI * 16 + NI, 16u);
          bar();
          MK(10);
          p3_g0(smem, ws, layer, MI, NI, wave, lane_id() MK_PASS);
        } else {
          if (layer && wave == 5) cnt_wait(ws, CN_O + layer, RB * 8 + CT, 16u);
          bar();
          k3_const(smem, ws, b_sp, layer, bid, MI, NI, wave & 3);
          MX(6, 26);
          bar();
          MK(10);
          FRESH();
          p3_g1(smem, ws, layer, MI, wave & 3, lane_id() MK_PASS);
        }
      }
      FRESH();
      {
        const int lane = lane_id();
        if (wave < 4) {
          u32x2 xv[5];
          ResIn ri;
          ri.setup(ws + OFF_X1 + (size_t)layer * 2 * SZ_X, false, RB, CT, wave, lane);
          f32x4 acc[2][5];
          ResOut ro;
          ro.setup(ws + OFF_X1 + SZ_X, ws + OFF_XS + SZ_XS, out, layer != 0, RB, CT, wave, lane);
          ResX<36, 33, 8, P5_W, P5_XW, P5_X0, 3, P5_BAR> gm(acc);
          gm.setup(lds0, uni_ptr(ws + OFF_H + (size_t)layer * SZ_Z + (size_t)RB * (288 * 1024)), wave, lane, (unsigned)P5_RED, layer + 1u);
          announce(lds0 + (unsigned)(LW_OFF + (LW_H + layer * 2 + (wave >> 1)) * 4), 2u, ws, ((NI >= 8) ? CN_H2 : CN_H) + layer,
                   (MI * 2 + (wave >> 1)) * 8 + lane, lane < 8);
          MK(19);
          bar();
          ri.load(xv);
          gm.prow();
          place();
          bar();
          MK(20);
          gm.prox<0>(std::make_integer_sequence<int, P5_X0>{});
          gm.init<2 * P5_X0>(xv);
          __builtin_amdgcn_sched_barrier(0);
          gm.run();
          MK(21);
          ro.run(acc[0], layer != 0);
          MK(22);
          if (layer == 0) {
            announce(lds0 + (unsigned)(LW_OFF + LW_X2 * 4), 4u, ws, CN_X2, (RB >> 1) * 16 + lane,
                     lane < 16 || (lane < 32 && (RB & 1) && RB < 31));
            MK(23);
          }
        } else {
          bar();
          bar();
#pragma unroll
          for (int i = 0; i < 7; ++i) {
            if (i == 1 && wave == 5) {
              unsigned f;
              const char* cp = uni_ptr(ws + OFF_CNT + (size_t)((CN_H2 + layer) * 256 + RB * 8 + CT) * 128);
              asm volatile("global_load_dword %0, %1, %2 sc1" : "=v"(f) : "v"(vconst(0u)), "s"(cp) : "memory");
              bar();
              asm volatile("s_waitcnt vmcnt(0)" : "+v"(f) : : "memory");
              if (__builtin_amdgcn_readfirstlane(f) != 7u) cnt_wait(ws, CN_H2 + layer, RB * 8 + CT, 8u);
              continue;
            }
            asm volatile("s_waitcnt lgkmcnt(0)" ::: "memory");
            bar();
          }
          MK(22);
        }
        if (layer == 0 && wave < 4) bar();
      }
    }
  }
}

}

extern "C" void kernel_launch(void* const* d_in, const int* in_sizes, int n_in, void* d_out, int out_size,
                              void* d_ws, size_t ws_size, hipStream_t stream) {
  const u16* x0 = (const u16*)d_in[0];
  const u16* wqkv = (const u16*)d_in[1];
  const u16* wo = (const u16*)d_in[2];
  const u16* w_in = (const u16*)d_in[4];
  const u16* w_sp = (const u16*)d_in[5];
  const u16* b_sp = (const u16*)d_in[6];
  const u16* w_out = (const u16*)d_in[7];
  char* ws = (char*)d_ws;
  (void)hipFuncSetAttribute((const void*)k_main, hipFuncAttributeMaxDynamicSharedMemorySize, LDS_ALL);
  k_main<<<256, 768, LDS_ALL, stream>>>(x0, wqkv, wo, w_in, w_sp, b_sp, w_out, (u16*)d_out, ws);
}
```

```cpp
#include <hip/hip_runtime.h>
#include <hip/hip_bf16.h>
#include <stdint.h>

#define DEVI __device__ __forceinline__

typedef unsigned short u16;
typedef __attribute__((ext_vector_type(8))) short s16x8;
typedef __attribute__((ext_vector_type(4))) short s16x4;
typedef __attribute__((ext_vector_type(4))) float f32x4;
typedef __attribute__((ext_vector_type(2))) float f32x2;
typedef __attribute__((ext_vector_type(2))) __bf16 bf16x2;
typedef __attribute__((ext_vector_type(2))) unsigned u32x2;
typedef __attribute__((ext_vector_type(4))) unsigned u32x4;

#define MK(I) ((void)0)
#define MX(S, I) ((void)0)
#define MF(I) ((void)0)
#define MF_ARG
#define MF_PASS
#define MK_ARG
#define MK_PASS

#define SPIN_MEM (1u << 22)
#define SAFE_OUT(L)
#define SAFE_AND
#define NOP_(n) "=s"(n)

namespace {

constexpr int S_ = 2048, D_ = 576, F_ = 4608, FH_ = 2304;
constexpr int NQKV = 3 * D_;

constexpr size_t SZ_X = (size_t)S_ * D_ * 2;
constexpr size_t SZ_Z = (size_t)S_ * FH_ * 2;
constexpr size_t OFF_X1 = 0;
constexpr size_t OFF_O = OFF_X1 + 3 * SZ_X;
constexpr size_t OFF_Z2 = OFF_O + 2 * SZ_X;
constexpr size_t OFF_H = OFF_Z2 + 2 * SZ_Z;
constexpr size_t SZ_XS = (size_t)S_ * 8 * 8;
constexpr size_t OFF_XS = OFF_H + 2 * SZ_Z;
constexpr size_t OFF_CSQ = OFF_XS + 3 * SZ_XS;
constexpr size_t OFF_ZP = OFF_CSQ + 256 * 512;
constexpr size_t SZ_ZP = (size_t)16 * 16 * 1024;
constexpr size_t OFF_PQ = OFF_ZP + 2 * SZ_ZP;
constexpr size_t OFF_PO = OFF_PQ + (size_t)16 * 9 * 64 * 112 * 2;
constexpr size_t OFF_PI = OFF_PO + (size_t)D_ * D_ * 2;
constexpr size_t OFF_PW = OFF_PI + (size_t)32 * 9 * 64 * 144 * 2;
constexpr size_t OFF_CSP = OFF_PW + (size_t)FH_ * D_ * 2;
constexpr size_t OFF_CNT = OFF_CSP + (size_t)18 * F_ * 4;
constexpr int CN_X1 = 0, CN_H = 2, CN_X2 = 4, CN_O = 5, CN_Z = 7, CN_PK = 9, CN_H2 = 10, CN_Z2 = 12, NCNT = 14;
constexpr size_t OFF_TAG = OFF_CNT + (size_t)NCNT * 256 * 128;
constexpr size_t OFF_HELLO = OFF_TAG + 128;
constexpr size_t OFF_POT = OFF_HELLO + 2048;
constexpr size_t OFF_CSK = OFF_POT + 1024;
constexpr size_t WS_END = OFF_CSK + 256 * 2048;

DEVI float bf_lo(unsigned u) { return __uint_as_float(u << 16); }
DEVI float bf_hi(unsigned u) { return __uint_as_float(u & 0xffff0000u); }
DEVI unsigned pack2(float a, float b) {
  f32x2 v = {a, b};
  bf16x2 r = __builtin_convertvector(v, bf16x2);
  return __builtin_bit_cast(unsigned, r);
}
DEVI float rbf(float a) {
  return bf_lo(pack2(a, 0.f));
}

DEVI float sum_g(float x) {
  const unsigned u = __float_as_uint(x);
  const auto p = __builtin_amdgcn_permlane32_swap(u, u, false, false);
  const unsigned v = __float_as_uint(__uint_as_float(p[0]) + __uint_as_float(p[1]));
  const auto q = __builtin_amdgcn_permlane16_swap(v, v, false, false);
  return __uint_as_float(q[0]) + __uint_as_float(q[1]);
}

DEVI unsigned vconst(unsigned c) {
  unsigned r;
  asm volatile("v_mov_b32 %0, %1" : "=v"(r) : "s"(c));
  return r;
}
DEVI void place() {
  __builtin_amdgcn_sched_barrier(0);
  asm volatile(".p2align 6");
  __builtin_amdgcn_sched_barrier(0);
}
DEVI const char* uni_ptr(const char* p) {
  unsigned long long v = (unsigned long long)p;
  unsigned lo = __builtin_amdgcn_readfirstlane((unsigned)v), hi = __builtin_amdgcn_readfirstlane((unsigned)(v >> 32));
  asm("s_nop 4" : "+s"(lo), "+s"(hi));
  return (const char*)(((unsigned long long)hi << 32) | lo);
}

DEVI void dma16(const char* sbase, unsigned voff, unsigned m0val) {
  asm volatile("s_mov_b32 m0, %2\n\ts_nop 0\n\tglobal_load_lds_dwordx4 %0, %1"
               :
               : "v"(voff), "s"(sbase), "s"(m0val)
               : "memory");
}

DEVI void ln_finish(float s, float q, float invn, float& mb, float& rinv) {
  float mean = s * invn;
  asm("" : "+v"(mean));
  float var = fmaxf(__builtin_fmaf(-mean, mean, q * invn), 0.f);
  mb = rbf(mean);
  float v = rbf(var);
  float t = rbf(v + 1e-5f);
  float sd = rbf(__builtin_amdgcn_sqrtf(t));
  rinv = __builtin_amdgcn_rcpf(sd);
}

DEVI unsigned pack2v(f32x2 v) { return pack2(v[0], v[1]); }

template <int BM_, int BN_>
struct GemmCfg {
  static constexpr int BM = BM_, BN = BN_, NS = 4;
  static constexpr int P = BN * 2;
  static constexpr int XB = BM * 128, WB = 64 * P, SB = XB + WB;
  static constexpr int LDS_BYTES = NS * SB;
};

DEVI int swap23(int r) { return (r & ~12) | ((r & 4) << 1) | ((r & 8) >> 1); }

DEVI void lds_wr32(unsigned addr, unsigned v) { asm volatile("ds_write_b32 %0, %1" ::"v"(addr), "v"(v) : "memory"); }

template <class C, int MT, int NT, int D, int R, bool XCM, int WT, bool SQ = false, unsigned long long WS = 0>
struct GemmMath {
  static constexpr int WO = WT ? 1 : 2;
  static constexpr int NS = C::NS, SB = C::SB, P = C::P, NU = 2 * NT;
  static constexpr int XPU = MT <= 2 ? MT : MT - 2;
  static constexpr int XU = (MT + XPU - 1) / XPU;
  static_assert(NU % R == 0 && R > D && D <= NT && XU <= D, "shape");
  s16x4 wlo[R], whi[R];
  s16x8 wf[R];
  s16x8 xf[2][MT];
  unsigned xrd[2], wrd, xa[2], wa, so;
  f32x4 (&acc)[MT][NT];
  f32x4 accq[SQ ? MT : 1];
  unsigned long long wtab = WS;
  DEVI GemmMath(f32x4 (&a)[MT][NT]) : acc(a) {}

  template <int PU>
  DEVI void wread_lo() {
    constexpr int off = (PU / NT) * 32 * P + (PU % NT) * (WT == 2 ? 1024 : 32);
    if constexpr (WT) asm volatile("ds_read_b128 %0, %1 offset:%2" : "=v"(wf[PU % R]) : "v"(wa), "n"(off));
    else asm volatile("ds_read_b64_tr_b16 %0, %1 offset:%2" : "=v"(wlo[PU % R]) : "v"(wa), "n"(off));
  }
  template <int PU>
  DEVI void wread_hi() {
    if constexpr (!WT)
      asm volatile("ds_read_b64_tr_b16 %0, %1 offset:%2" : "=v"(whi[PU % R]) : "v"(wa), "n"((PU / NT) * 32 * P + (PU % NT) * 32 + 8 * P));
  }
  template <int H, int M>
  DEVI void xread1() {
    asm volatile("ds_read_b128 %0, %1 offset:%2" : "=v"(xf[H][M]) : "v"(xa[H]), "n"(M * (XCM ? 256 : 2048)));
  }
  static constexpr int xops(int v) {
    const int k = (v + D) % NT;
    if (k >= XU) return 0;
    const int n = MT - k * XPU;
    return n < XPU ? n : XPU;
  }
  static constexpr int ops(int v, bool last) { return (last && v + D >= NU) ? 0 : WO + xops(v); }
  static constexpr int newer(int U, bool last) {
    int n = 0;
    int v0 = U - D;
    if (U % NT == 0) v0 += XU - 1;
    else n += v0 >= 0 ? xops(v0) : xops(v0 + NU);
    for (int v = v0 + 1; v < U; ++v) n += v >= 0 ? ops(v, last) : ops(v + NU, false);
    return n;
  }
  template <int U, int M_>
  DEVI void mf() {
    constexpr int M = ((U % NT) & 1) ? MT - 1 - M_ : M_;
    s16x8 w;
    if constexpr (WT) w = wf[U % R];
    else w = __builtin_shufflevector(wlo[U % R], whi[U % R], 0, 1, 2, 3, 4, 5, 6, 7);
    acc[M][U % NT] = __builtin_amdgcn_mfma_f32_16x16x32_bf16(w, xf[U / NT][M], acc[M][U % NT], 0, 0, 0);
    __builtin_amdgcn_sched_barrier(0);
  }
  DEVI void pin() {
#pragma unroll
    for (int m = 0; m < MT; ++m)
#pragma unroll
      for (int n = 0; n < NT; ++n) asm volatile("" : "+v"(acc[m][n]));
  }
  template <int U, int I>
  DEVI void dsop() {
    constexpr int PU = (U + D) % NU;
    if constexpr (I == 0) wread_lo<PU>();
    else if constexpr (I == 1) wread_hi<PU>();
    else if constexpr (I - 2 < xops(U)) {
      constexpr int k = (U + D) % NT;
      xread1<((U + D - k) % NU) / NT, k * XPU + I - 2>();
    }
  }
  template <int U, bool LAST, bool BAR, int M>
  DEVI void slot() {
    constexpr bool PF = !(LAST && U + D >= NU);
    mf<U, M>();
    if constexpr (M == 0 && PF && U + D == NU) {
      if constexpr (BAR) __builtin_amdgcn_s_barrier();
      so = (so == (unsigned)((NS - 1) * SB)) ? 0u : so + (unsigned)SB;
      wa = wrd + (WS ? ((unsigned)wtab & 63u) << 11 : so);
      wtab >>= 6;
      xa[0] = xrd[0] + so;
      xa[1] = xrd[1] + so;
      __builtin_amdgcn_sched_barrier(0);
    }
    if constexpr (PF) {
      dsop<U, M>();
      if constexpr (M == MT - 1) {
        if constexpr (MT <= 1) dsop<U, 1>();
        if constexpr (MT <= 2) {
          dsop<U, 2>();
          dsop<U, 3>();
        }
      }
      __builtin_amdgcn_sched_barrier(0);
    }
  }
  template <int U, bool LAST, bool BAR, int... M>
  DEVI void unit(std::integer_sequence<int, M...>) {
    static_assert(newer(U, LAST) <= 15, "lgkmcnt range");
    asm volatile("s_waitcnt lgkmcnt(%0)" ::"n"(newer(U, LAST)));
    __builtin_amdgcn_sched_barrier(0);
    (slot<U, LAST, BAR, M>(), ...);
    if constexpr (SQ && (U % NT) < MT) {
      constexpr int m = U % NT;
      accq[m] = __builtin_amdgcn_mfma_f32_16x16x32_bf16(xf[U / NT][m], xf[U / NT][m], accq[m], 0, 0, 0);
      __builtin_amdgcn_sched_barrier(0);
    }
    __builtin_amdgcn_sched_barrier(0);
  }
  template <bool LAST, bool BAR, int... U>
  DEVI void stage(std::integer_sequence<int, U...>) {
    (unit<U, LAST, BAR>(std::make_integer_sequence<int, MT>{}), ...);
  }
  template <int... U>
  DEVI void prow(std::integer_sequence<int, U...>) {
    ((wread_lo<U>(), wread_hi<U>()), ...);
  }
  template <int... M>
  DEVI void prox(std::integer_sequence<int, M...>) {
    (xread1<0, M>(), ...);
  }
  DEVI void setup(unsigned lds0, int mt0, int nt0, int lane, unsigned so0) {
    const int g = lane >> 4, r = lane & 15, q = (lane >> 2) & 3, p = lane & 3;
#pragma unroll
    for (int h = 0; h < 2; ++h)
      xrd[h] = XCM ? lds0 + (unsigned)((4 * h + g) * (C::BM * 16) + (mt0 * 16 + r) * 16)
                   : lds0 + (unsigned)((mt0 * 16 + r) * 128 + (((4 * h + g) ^ ((r >> 1) & 7)) * 16));
    wrd = WT == 2   ? lds0 + (unsigned)(C::XB + nt0 * 1024 + lane * 16)
          : WT == 1 ? lds0 + (unsigned)(C::XB + (g * 8 + (r & 7)) * P + (r >> 3) * 16 + nt0 * 32)
                    : lds0 + (unsigned)(C::XB + (16 * (g >> 1) + 4 * (g & 1) + q) * P + 8 * p + nt0 * 32);
    so = so0;
    xa[0] = xrd[0] + so;
    xa[1] = xrd[1] + so;
    wa = wrd + so;
  }
  DEVI void first() {
    prox(std::make_integer_sequence<int, MT>{});
    prow(std::make_integer_sequence<int, D>{});
    asm volatile("s_waitcnt lgkmcnt(0)");
  }
  template <int N, bool BAR = true>
  DEVI void mid() {
    if constexpr (N > 1) place();
#pragma unroll 1
    for (int s = 0; s < N; ++s) stage<false, BAR>(std::make_integer_sequence<int, NU>{});
  }
  DEVI void last() {
    stage<true, true>(std::make_integer_sequence<int, NU>{});
    pin();
  }
  template <int KS>
  DEVI void run(unsigned lds0, int mt0, int nt0, int lane, unsigned so0) {
    setup(lds0, mt0, nt0, lane, so0);
    __builtin_amdgcn_s_barrier();
    first();
    mid<KS - 1>();
    last();
  }
};

DEVI const char* code_here() {
  unsigned long long pc;
  asm volatile("s_getpc_b64 %0" : "=s"(pc));
  return (const char*)pc;
}
DEVI int okmap(int p) {
  const int ch = p >> 3, e = p & 7, c = (ch - 8) & 3;
  return ch < 8 ? (2 * ch + (e >> 2)) * 36 + 16 + (e & 3) : ((ch - 8) >> 2) * 36 + c * 8 + (c >> 1) * 4 + e;
}

DEVI void pk_st16(void* p, u32x4 v) {
  asm volatile("global_store_dwordx4 %0, %1, off sc1\n\ts_nop 1" ::"v"(p), "v"(v) : "memory");
}

using K1C = GemmCfg<144, 112>;
constexpr int K1_TP = 240;
constexpr int K1_T_OFF = K1C::SB;
constexpr int K1_ST_OFF = 163840 - 128 - 1152;
constexpr int K1_CS_OFF = 4 * K1C::SB;
constexpr int K1_WX = 4 * K1C::SB + 2048;
constexpr unsigned long long K1_WS = 16ull | 32ull << 6 | 48ull << 12 | 56ull << 18 | 63ull << 24 | 16ull << 30 | 32ull << 36 | 48ull << 42;
static_assert(K1C::SB == 16 * 2048 && K1_WX - K1C::XB == 56 * 2048 && K1C::WB == 7 * 2048 && K1_WX + 2 * K1C::WB <= K1_ST_OFF, "K1 w places");
static_assert(K1_T_OFF + 144 * K1_TP + 64 <= 2 * K1C::SB + K1C::XB, "K1 LDS alias");
using K1R = GemmCfg<144, 144>;
constexpr int K1R_T_OFF = K1R::SB;
constexpr int K1R_CS_OFF = K1R_T_OFF + 144 * K1_TP;
constexpr int K1R_ST_OFF = K1R_CS_OFF + 512;
constexpr int K1R_DUMMY = K1R::LDS_BYTES;
constexpr int K1R_PO = K1R_DUMMY + 1024;
constexpr int K1R_CNT = 163840 - 128 + 28 * 4;
#define SPIN_ASM(TEST)                                                                                                   \
  if constexpr (SL <= 1)                                                                                                 \
    asm volatile(".La%=:\n\tds_read_b32 %0, %3\n\ts_waitcnt lgkmcnt(0)\n\tv_readfirstlane_b32 %2, %0\n\t" TEST SAFE_AND \
                 "s_cbranch_scc1 .La%="                                                                                  \
                 : "=&v"(v), NOP_(c), "=&s"(t)                                                                           \
                 : "v"(addr), "s"(k)                                                                                     \
                 : "memory", "scc");                                                                                     \
  else                                                                                                                   \
    asm volatile("s_branch .Lb%=\n.La%=:\n\ts_sleep %5\n.Lb%=:\n\tds_read_b32 %0, %3\n\ts_waitcnt lgkmcnt(0)\n\t"     \
                 "v_readfirstlane_b32 %2, %0\n\t" TEST SAFE_AND "s_cbranch_scc1 .La%="                                  \
                 : "=&v"(v), NOP_(c), "=&s"(t)                                                                           \
                 : "v"(addr), "s"(k), "n"(SL)                                                                            \
                 : "memory", "scc")
template <int SL>
DEVI void spin_ge(unsigned addr, unsigned k) {
  unsigned v, c = 1u << 18, t;
  SPIN_ASM("s_cmp_lt_u32 %2, %4\n\t");
}
template <int SL>
DEVI void spin_b4(unsigned addr, unsigned n) {
  unsigned v, c = 1u << 18, t;
  const unsigned k = (0x80u - n) * 0x01010101u;
  SPIN_ASM("s_add_u32 %2, %2, %4\n\ts_andn2_b32 %2, 0x80808080, %2\n\t");
}
DEVI void msync(unsigned addr) {
  asm volatile("s_waitcnt lgkmcnt(0)" ::: "memory");
  asm volatile("s_mov_b64 exec, 1\n\tds_add_u32 %0, %1\n\ts_mov_b64 exec, -1" ::"v"(addr), "v"(vconst(1u)) : "memory");
  spin_ge<0>(addr, 8u);
}
static_assert(K1R_PO + 13824 + 256 <= 163840 - 128 && K1R_ST_OFF + 144 * 8 <= 3 * K1R::SB, "K1R LDS");

DEVI void touch64(const char* sbase, unsigned voff, unsigned m0val) {
  asm volatile("s_mov_b32 m0, %2\n\ts_nop 0\n\tglobal_load_lds_dword %0, %1" : : "v"(voff), "s"(sbase), "s"(m0val) : "memory");
}

template <int MT, bool G1>
DEVI void k1_gemm_raw(char* smem, int hp, int mt0, int lane MF_ARG) {
  constexpr int NT = 4, nt0 = G1 ? 4 : 0;
  const unsigned lds0 = (unsigned)(size_t)smem;
  const int g = lane >> 4, r = lane & 15;
  unsigned ta[NT];
#pragma unroll
  for (int nt = 0; nt < NT; ++nt) {
    const int j = (nt0 + nt) * 16 + g * 4, seg = j / 40, cj = j - seg * 40 - (hp & 1) * 4;
    const int to = (seg < 3 && cj >= 0 && cj < 36) ? seg * 80 + cj * 2 + (cj >= 20 ? 8 : 0) : (seg < 3 ? 40 : 120);
    ta[nt] = lds0 + (unsigned)(K1R_T_OFF + (mt0 * 16 + r) * K1_TP + to);
    asm volatile("" : "+v"(ta[nt]));
  }
  unsigned sa = lds0 + (unsigned)(K1R_ST_OFF + (mt0 * 16 + r) * 8), ca = lds0 + (unsigned)(K1R_CS_OFF + (nt0 * 16 + g * 4) * 4);
  asm volatile("" : "+v"(sa), "+v"(ca));
  f32x4 acc[MT][NT];
#pragma unroll
  for (int mt = 0; mt < MT; ++mt)
#pragma unroll
    for (int nt = 0; nt < NT; ++nt) acc[mt][nt] = f32x4{0.f, 0.f, 0.f, 0.f};
  {
    GemmMath<K1R, MT, 4, 3, 4, false, 0, G1> gm(acc);
    if constexpr (G1) {
#pragma unroll
      for (int mt = 0; mt < MT; ++mt) gm.accq[mt] = f32x4{0.f, 0.f, 0.f, 0.f};
    }
    MF(14);
    if constexpr (!G1) {
      if (mt0 < 7) asm volatile("s_waitcnt vmcnt(0)" ::: "memory");
    }
    MF(15);
    gm.template run<9>(lds0, mt0, nt0, lane, 0u);
    MF(2);
    if constexpr (G1) {
#pragma unroll
      for (int mt = 0; mt < MT; ++mt) {
        const f32x4 q = gm.accq[mt];
        const float d = (r & 2) ? ((r & 1) ? q[3] : q[2]) : ((r & 1) ? q[1] : q[0]);
        const float qq = sum_g(g == (r >> 2) ? d : 0.f);
        float mb, rinv;
        ln_finish(acc[mt][3][0], qq, 1.0f / (float)D_, mb, rinv);
        const f32x2 st = {-mb, rinv};
        if (g == 2) asm volatile("ds_write_b64 %0, %1 offset:%2" ::"v"(sa), "v"(st), "n"(mt * 128) : "memory");
      }
    }
  }
  if (mt0 == 0 && r == 0) {
#pragma unroll
    for (int nt = 0; nt < NT; ++nt) *reinterpret_cast<f32x4*>(smem + K1R_CS_OFF + ((nt0 + nt) * 16 + g * 4) * 4) = acc[0][nt];
  }
  msync(lds0 + (unsigned)K1R_CNT);
  MF(3);
  f32x2 st[MT];
  f32x4 cs[NT];
#pragma unroll
  for (int nt = 0; nt < NT; ++nt) asm volatile("ds_read_b128 %0, %1 offset:%2" : "=v"(cs[nt]) : "v"(ca), "n"(nt * 64) : "memory");
#pragma unroll
  for (int mt = 0; mt < MT; ++mt) asm volatile("ds_read_b64 %0, %1 offset:%2" : "=v"(st[mt]) : "v"(sa), "n"(mt * 128) : "memory");
#pragma unroll
  for (int i = 0; i < MT; ++i) asm volatile("s_waitcnt lgkmcnt(0)" : "+v"(st[i]));
#pragma unroll
  for (int i = 0; i < NT; ++i) asm volatile("" : "+v"(cs[i]));
#pragma unroll
  for (int mt = 0; mt < MT; ++mt) {
    f32x2 M = {st[mt][0], st[mt][0]}, R = {st[mt][1], st[mt][1]};
    asm volatile("" : "+v"(M), "+v"(R));
#pragma unroll
    for (int nt = 0; nt < NT; ++nt) {
      const f32x2 v0 = R * __builtin_elementwise_fma(M, f32x2{cs[nt][0], cs[nt][1]}, f32x2{acc[mt][nt][0], acc[mt][nt][1]});
      const f32x2 v1 = R * __builtin_elementwise_fma(M, f32x2{cs[nt][2], cs[nt][3]}, f32x2{acc[mt][nt][2], acc[mt][nt][3]});
      acc[mt][nt][0] = __uint_as_float(pack2v(v0));
      acc[mt][nt][1] = __uint_as_float(pack2v(v1));
      const f32x2 pk = {acc[mt][nt][0], acc[mt][nt][1]};
      asm volatile("ds_write_b64 %0, %1 offset:%2" ::"v"(ta[nt]), "v"(pk), "n"(mt * 16 * K1_TP) : "memory");
    }
  }
}

template <int N>
DEVI void wait_vm() {
  asm volatile("s_waitcnt vmcnt(%0)" ::"n"(N) : "memory");
}

constexpr int LDS_ALL = 163840;
constexpr int LW_OFF = LDS_ALL - 128;
constexpr int LW_GO = 0, LW_T = 2, LW_O = 4  , LW_X1 = 6, LW_H = 8, LW_X2 = 12, LW_PK = 13, LW_Z = 14, LW_PAIR = 16, LW_AD = 20,
              LW_L = 22, LW_S = 24, LW_ST = 29, LW_HELLO = 30, LW_Q = 31;
DEVI void announce(unsigned addr, unsigned n, char* ws, int kind, int who, bool act) {
  const char* cb = uni_ptr(ws + OFF_CNT + (size_t)kind * 256 * 128);
  const unsigned long long mask = __ballot(act);
  unsigned t, u;
  asm volatile(
      "s_waitcnt vmcnt(0)\n\t"
      "s_mov_b64 exec, 1\n\t"
      "ds_add_rtn_u32 %0, %2, %3\n\t"
      "s_waitcnt lgkmcnt(0)\n\t"
      "v_readfirstlane_b32 %1, %0\n\t"
      "s_cmp_eq_u32 %1, %4\n\t"
      "s_cselect_b64 exec, %5, 0\n\t"
      "global_atomic_add %6, %3, %7 sc1\n\t"
      "s_mov_b64 exec, -1\n\t"
      "s_nop 0"
      : "=&v"(t), "=&s"(u)
      : "v"(addr), "v"(vconst(1u)), "s"(n - 1u), "s"(mask), "v"((unsigned)(who & 255) * 128u), "s"(cb)
      : "memory", "scc");
}
DEVI void cnt_wait(const char* ws, int kind, int who, unsigned n) {
  const char* cp = uni_ptr(ws + OFF_CNT + (size_t)(kind * 256 + who) * 128);
  unsigned v, c = SPIN_MEM, t;
  asm volatile(
      ".La%=:\n\tglobal_load_dword %0, %3, %4 sc1\n\ts_sub_u32 %1, %1, 1\n\ts_waitcnt vmcnt(0)\n\tv_readfirstlane_b32 %2, %0\n\ts_cmp_lg_u32 %2, %5\n\t"
      "s_cselect_b32 %2, %1, 0\n\ts_cmp_lg_u32 %2, 0\n\ts_cbranch_scc1 .La%="
      : "=&v"(v), "+s"(c), "=&s"(t)
      : "v"(vconst(0u)), "s"(cp), "s"(n - 1u)
      : "memory", "scc");
}
DEVI void dma16_sc1(const char* sbase, unsigned voff, unsigned m0val) {
  asm volatile("s_mov_b32 m0, %2\n\ts_nop 0\n\tglobal_load_lds_dwordx4 %0, %1 sc1"
               :
               : "v"(voff), "s"(sbase), "s"(m0val)
               : "memory");
}
template <int N, bool SC1>
DEVI void dma4(const char* sbase, unsigned v0, unsigned v1, unsigned v2, unsigned v3, unsigned m0val) {
#define DMA4(S)                                                                                                               \
  asm volatile("s_mov_b32 m0, %5\n\ts_nop 0\n\tglobal_load_lds_dwordx4 %0, %4" S "\n\t.if %6 > 1\n\tglobal_load_lds_dwordx4 %1, %4 offset:1024" S \
               "\n\t.endif\n\t.if %6 > 2\n\tglobal_load_lds_dwordx4 %2, %4 offset:2048" S                                    \
               "\n\t.endif\n\t.if %6 > 3\n\tglobal_load_lds_dwordx4 %3, %4 offset:3072" S "\n\t.endif"                      \
               :                                                                                                              \
               : "v"(v0), "v"(v1), "v"(v2), "v"(v3), "s"(sbase), "s"(m0val), "n"(N)                                           \
               : "memory")
  if constexpr (SC1) DMA4(" sc1");
  else DMA4("");
}
template <int N>
DEVI void dma_seq(const char* src, unsigned voff, unsigned m0val) {
  if constexpr (N > 0) {
    dma4<(N < 4 ? N : 4), true>(src, voff, voff, voff, voff, m0val);
    dma_seq<N - 4>(src + 4096, voff, m0val + 4096u);
  }
}
DEVI void bar() { __builtin_amdgcn_s_barrier(); }
DEVI void lds_inc(unsigned addr, unsigned by = 1u) {
  asm volatile("s_mov_b64 exec, 1\n\tds_add_u32 %0, %1\n\ts_mov_b64 exec, -1" ::"v"(addr), "v"(vconst(by)) : "memory");
}
DEVI void lds_spin(unsigned addr, unsigned n) {
  spin_ge<1>(addr, n);
}
template <int SL = 1>
DEVI void lds_spin4(unsigned addr, unsigned n) {
  spin_b4<SL>(addr, n);
}
DEVI int lane_id() {
  int l;
  asm volatile("v_mbcnt_lo_u32_b32 %0, -1, 0\n\tv_mbcnt_hi_u32_b32 %0, -1, %0" : "=v"(l));
  __builtin_assume((unsigned)l < 64u);
  return l;
}

template <int I>
DEVI u32x4 tr8(const u32x4 (&v)[8]) {
  constexpr unsigned sel = (I & 1) ? 0x07060302u : 0x05040100u;
  return u32x4{__builtin_amdgcn_perm(v[1][I / 2], v[0][I / 2], sel), __builtin_amdgcn_perm(v[3][I / 2], v[2][I / 2], sel),
               __builtin_amdgcn_perm(v[5][I / 2], v[4][I / 2], sel), __builtin_amdgcn_perm(v[7][I / 2], v[6][I / 2], sel)};
}
template <int PITCH, bool SUM, int... I>
DEVI void tr8_store(const u32x4 (&v)[8], unsigned dst, float (&cs)[8], std::integer_sequence<int, I...>) {
  auto one = [&](auto I_) {
    const u32x4 o = tr8<I_>(v);
    const unsigned d_ = dst;
    asm volatile("ds_write_b128 %0, %1 offset:%2" ::"v"(d_), "v"(o), "n"(I_ * PITCH) : "memory");
    if constexpr (SUM) {
#pragma unroll
      for (int d = 0; d < 4; ++d) {
        const unsigned od = o[d];
        cs[I_] = __builtin_amdgcn_fdot2_f32_bf16(__builtin_bit_cast(bf16x2, od), __builtin_bit_cast(bf16x2, 0x3f803f80u), cs[I_], false);
      }
    }
  };
  (one(std::integral_constant<int, I>{}), ...);
}
template <int NB>
DEVI void lds_out(unsigned src, char* dst, int lane) {
  constexpr int N = (NB + 1023) / 1024;
  u32x4 t[N];
  const unsigned a = src + (unsigned)lane * 16u;
#pragma unroll
  for (int i = 0; i < N; ++i) asm volatile("ds_read_b128 %0, %1 offset:%2" : "=v"(t[i]) : "v"(a), "n"(i * 1024) : "memory");
  const char* d = uni_ptr(dst);
#pragma unroll
  for (int i = 0; i < N; ++i) {
    asm volatile("s_waitcnt lgkmcnt(%1)" : "+v"(t[i]) : "n"(N - 1 - i) : "memory");
    if (i < N - 1 || lane < (NB - (N - 1) * 1024) / 16)
      asm volatile("global_store_dwordx4 %0, %1, %2 sc1\n\ts_nop 1" ::"v"((unsigned)lane * 16u), "v"(t[i]), "s"(d + i * 1024) : "memory");
  }
}
template <int A, int B, class F>
DEVI void static_for(F f) {
  if constexpr (A < B) {
    f(std::integral_constant<int, A>{});
    static_for<A + 1, B>(f);
  }
}
constexpr int WSP_V = 3;
constexpr int RAW_S1 = 6;
constexpr int RAW_S0 = 4;
template <int E>
struct RawX {
  static constexpr int first(int s) {
    return s <= RAW_S0 ? 0 : (s > RAW_S1 ? E : (E * (s - RAW_S0) + RAW_S1 - RAW_S0) / (RAW_S1 + 1 - RAW_S0));
  }
  static constexpr int n(int s) { return first(s + 1) - first(s); }
};
struct RawLd {
  static constexpr int VPS = 10, SB = K1R::SB;
  unsigned xoff[5], woff[5], m0x;
  unsigned long long xact0, wact[5], tag;
  const char *xp, *wp;
  int qb;
  DEVI void q_hook(int st, char* ws, unsigned lds0, int hp) {
    if ((qb >> 1) == st) {
      const int lane = lane_id(), g = lane >> 4;
      const unsigned a = lds0 + (unsigned)((st & 3) * SB + K1R::XB + ((qb & 1) * 32 + 16 * (g >> 1) + 4 * (g & 1) + ((lane >> 2) & 3)) * 288 +
                                           (lane & 3) * 8 + (hp & 1) * 8);
      const unsigned a2 = a + ((lane & 3) >= 1 ? 8u : 0u), a4 = a + ((lane & 3) >= 2 ? 8u : 0u);
      u32x2 qf[7][2];
#pragma unroll
      for (int nt = 0; nt < 7; ++nt)
        asm volatile("ds_read_b64_tr_b16 %0, %2 offset:%3\n\tds_read_b64_tr_b16 %1, %2 offset:%4"
                     : "=&v"(qf[nt][0]), "=&v"(qf[nt][1])
                     : "v"(nt == 2 ? a2 : (nt == 4 ? a4 : a)), "n"(nt * 32 + (nt > 4 ? 16 : (nt > 2 ? 8 : 0))),
                       "n"(2304 + nt * 32 + (nt > 4 ? 16 : (nt > 2 ? 8 : 0)))
                     : "memory");
      const char* d = uni_ptr(ws + OFF_PQ + (size_t)(hp * 18 + qb) * 7168 + 3072);
#pragma unroll
      for (int nt = 0; nt < 7; ++nt) {
        asm volatile("s_waitcnt lgkmcnt(%2)" : "+v"(qf[nt][0]), "+v"(qf[nt][1]) : "n"(12 - 2 * nt) : "memory");
        const u32x4 v = {qf[nt][0][0], qf[nt][0][1], qf[nt][1][0], qf[nt][1][1]};
        asm volatile("global_store_dwordx4 %0, %1, %2 offset:%3 sc1\n\ts_nop 1" ::"v"((unsigned)lane * 16u), "v"(v), "s"(d), "n"(nt * 1024 - 3072) : "memory");
      }
      if (st == 8) lds_wr32(lds0 + (unsigned)(LW_OFF + LW_Q * 4), 1u);
    }
  }
  DEVI void stage_x(unsigned so) {
    asm volatile(
        "s_mov_b32 m0, %6\n\ts_mov_b64 exec, %7\n\tglobal_load_lds_dwordx4 %0, %5\n\ts_mov_b64 exec, -1\n\t"
        "global_load_lds_dwordx4 %1, %5 offset:1024\n\tglobal_load_lds_dwordx4 %2, %5 offset:2048\n\tglobal_load_lds_dwordx4 %3, %5 offset:3072\n\t"
        "s_add_u32 m0, m0, 0x1000\n\ts_mov_b32 exec_hi, 0\n\tglobal_load_lds_dwordx4 %4, %5\n\ts_mov_b32 exec_hi, -1" ::"v"(xoff[0]),
        "v"(xoff[1]), "v"(xoff[2]), "v"(xoff[3]), "v"(xoff[4]), "s"(xp), "s"(m0x + so), "s"(xact0)
        : "memory", "scc");
    xp += 128;
  }
  DEVI void stage_w(unsigned so) {
    asm volatile(
        "s_mov_b32 m0, %6\n\ts_mov_b64 exec, %7\n\tglobal_load_lds_dwordx4 %0, %5\n\ts_mov_b64 exec, %8\n\t"
        "global_load_lds_dwordx4 %1, %5 offset:1024\n\ts_mov_b64 exec, %9\n\tglobal_load_lds_dwordx4 %2, %5 offset:2048\n\t"
        "s_mov_b64 exec, %10\n\tglobal_load_lds_dwordx4 %3, %5 offset:3072\n\ts_add_u32 m0, m0, 0x1000\n\ts_mov_b64 exec, %11\n\t"
        "global_load_lds_dwordx4 %4, %5\n\ts_mov_b64 exec, -1" ::"v"(woff[0]),
        "v"(woff[1]), "v"(woff[2]), "v"(woff[3]), "v"(woff[4]), "s"(wp), "s"(m0x + so + (unsigned)K1R::XB), "s"(wact[0]), "s"(wact[1]),
        "s"(wact[2]), "s"(wact[3]), "s"(wact[4])
        : "memory", "scc");
    wp += 64 * NQKV * 2;
  }
  DEVI void stage(unsigned so) {
    stage_x(so);
    stage_w(so);
  }
  DEVI void front(const u16* x, const u16* wqkv, const u16* wo, char* ws, int bid, unsigned lds0, int wl, int lane, int rt, int hp,
                  int pj MF_ARG) {
    xp = uni_ptr((const char*)x - 3072);
    wp = uni_ptr((const char*)wqkv + ((hp * 72) & ~15) - 3072);
    m0x = lds0 + (unsigned)(wl * 288 * 16);
    xact0 = wl ? ~0ull : ~0xffull;
    const int row0 = wl * 36 + (lane >> 3);
#pragma unroll
    for (int i = 0; i < 5; ++i) {
      const int row = row0 + i * 8;
      int gr = rt * 128 - 16 + row;
      if (gr < 0) gr = 0;
      xoff[i] = (unsigned)(gr * (D_ * 2) + (3 - (i & 3)) * 1024) + (unsigned)(((lane ^ (row >> 1)) & 7) * 16);
    }
    stage_x(0u);
    u32x2 tg;
    asm volatile("global_load_dwordx2 %0, %1, %2 sc1" : "=v"(tg) : "v"(0u), "s"(uni_ptr(ws + OFF_TAG)) : "memory");
    asm volatile("" : "+v"(lane));
#pragma unroll
    for (int i = 0; i < 5; ++i) {
      const unsigned c = (unsigned)(i * 64 + (lane & 63));
      const unsigned wr = (c * 3641u) >> 16, wc = c - wr * 18u, seg = (wc * 13u) >> 6;
      wact[i] = __ballot(wc < 15u) & (i < 4 ? ~0ull : 0xffffffffull);
      woff[i] = (unsigned)(wl * 16 + swap23((int)wr)) * (unsigned)(NQKV * 2) + seg * (unsigned)(D_ * 2 - 80) + wc * 16u + (unsigned)((3 - (i & 3)) * 1024);
    }
    stage_w(0u);
    stage((unsigned)SB);
    MF(10);
    asm volatile("s_waitcnt vmcnt(%1)" : "+v"(tg) : "n"(VPS) : "memory");
    tag = (((unsigned long long)(unsigned)__builtin_amdgcn_readfirstlane(tg[1]) << 32) | (unsigned)__builtin_amdgcn_readfirstlane(tg[0])) +
          0x9e3779b97f4a7c15ull;
    MF(1);
    __builtin_amdgcn_s_barrier();
    MF(15);
    if (wl == 0 && lane == 0)
      asm volatile("global_store_dwordx2 %0, %1, off sc1\n\ts_nop 1" ::"v"(ws + OFF_HELLO + bid * 8), "v"(tag) : "memory");
    stage((unsigned)(2 * SB));
    qb = wl == 3 ? rt : ((wl == 2 && rt < 2) ? 16 + rt : 99);
    q_hook(0, ws, lds0, hp);
    const int nb = pj;
    const bool po = pj >= 0 && wl < 2;
    u32x4 v[8];
    unsigned da = 0u;
    if (po) {
      const int l54 = lane < 54 ? lane : 53, gl = l54 / 9, c = l54 - gl * 9, kl = wl * 6 + gl, t = nb / 6, kg = (nb - t * 6) * 12 + kl;
      da = lds0 + (unsigned)(K1R_PO + (kl >> 2) * 4608 + (16 * (kl & 1) + ((kl >> 1) & 1)) * 144 + c * 16);
      const unsigned sa[2] = {(unsigned)(okmap(kg * 8) * D_ + t * 72 + c * 8) * 2u, (unsigned)(okmap(kg * 8 + 4) * D_ + t * 72 + c * 8) * 2u};
      const char* ps = uni_ptr((const char*)wo);
#pragma unroll
      for (int j = 0; j < 8; ++j)
        asm volatile("global_load_dwordx4 %0, %1, %2 offset:%3" : "=v"(v[j]) : "v"(sa[j / 4]), "s"(ps), "n"((j % 4) * D_ * 2) : "memory");
    } else {
#pragma unroll
      for (int j = 0; j < 8; ++j) asm volatile("" : "=v"(v[j]));
    }
    unsigned slot = (unsigned)(3 * SB);
#pragma unroll 1
    for (int S = 0; S < RAW_S0; ++S) {
      wait_vm<VPS>();
      __builtin_amdgcn_s_barrier();
      if (S == 0) MF(16);
      if (S == 1) MF(17);
      if (S == 2) MF(18);
      if (S == 3) MF(19);
      stage(slot);
      slot = (slot == (unsigned)(3 * SB)) ? 0u : slot + (unsigned)SB;
      q_hook(S + 1, ws, lds0, hp);
      if (po && S == 1) {
        float cs[8];
        asm volatile("s_nop 0 ; LOOPWAIT" : "+v"(v[0]), "+v"(v[1]), "+v"(v[2]), "+v"(v[3]), "+v"(v[4]), "+v"(v[5]), "+v"(v[6]), "+v"(v[7]));
        tr8_store<288, false>(v, da, cs, std::make_integer_sequence<int, 8>{});
        asm volatile("s_waitcnt lgkmcnt(0)" ::: "memory");
      }
      if (po && S == 2) lds_out<6912>(lds0 + (unsigned)(K1R_PO + wl * 6912), ws + OFF_PO + (size_t)nb * 13824 + wl * 6912, lane);
    }
  }
};
template <bool JOB>
DEVI void k1_raw_loader(RawLd& ld, const u16* __restrict__ w_in, const u16* __restrict__ w_out, char* __restrict__ ws,
                        unsigned long long tag, int bid, unsigned lds0, int wl, int lane, int rt, int hp, int ct, int job, int pj MF_ARG) {
  constexpr int VPS = RawLd::VPS, SB = RawLd::SB, E = JOB ? 16 : 0;
  const bool isi = job < 144;
  const int kb = isi ? job >> 3 : job - 144;
  const int pitch = isi ? F_ * 2 : D_ * 2;
  const int l36 = lane < 36 ? lane : 35;
  unsigned sa = (unsigned)l36 * 16u, da;
  if (isi) da = lds0 + (unsigned)((l36 >= 18 ? 9216 - 288 : 0) + wl * 8 * 288 + l36 * 16);
  else da = lds0 + (unsigned)((l36 / 9) * (4608 - 144) + (16 * (wl & 1) + (wl >> 1)) * 144 + l36 * 16);
  asm volatile("" : "+v"(sa), "+v"(da));
  u32x4 v[2][8];
  const char* ps = uni_ptr((const char*)(isi ? w_in + (job & 7) * 576 : w_out) + (size_t)(kb * 32 + wl * 8) * pitch);
  auto extra = [&](auto J_) {
    constexpr int j = J_;
    const char* q = ps + (j / 2) * pitch;
    u32x4 t;
    asm volatile("global_load_dwordx4 %0, %1, %2 offset:%3" : "=v"(t) : "v"(sa), "s"(q), "n"((j % 2) * 576) : "memory");
    v[j % 2][j / 2] = t;
  };
  auto step = [&](auto S_) {
    constexpr int S = S_;
    wait_vm<RawX<E>::n(S - 2) + (S <= 6 ? VPS : 0) + RawX<E>::n(S - 1)>();
    __builtin_amdgcn_s_barrier();
    MF(16 + S);
    if constexpr (S == RAW_S0) {
      static_assert(RAW_S0 >= 4, "wo blocks");
      if (pj >= 0 && wl == 0 && lane == 0)
        asm volatile("global_store_dwordx2 %0, %1, off sc1\n\ts_nop 1" ::"v"(ws + OFF_POT + (pj / 6) * 128 + (pj % 6) * 8), "v"(tag) : "memory");
    }
    if constexpr (S <= 5) ld.stage((unsigned)(((S + 3) % 4) * SB));
    ld.q_hook(S + 1, ws, lds0, hp);
    static_for<RawX<E>::first(S), RawX<E>::first(S + 1)>(extra);
  };
  const char* hp_ = ws + OFF_HELLO + lane * 32;
  u32x4 a, b;
  if (wl == 1) asm volatile("global_load_dwordx4 %0, %2, off sc1\n\tglobal_load_dwordx4 %1, %2, off offset:16 sc1" : "=&v"(a), "=&v"(b) : "v"(hp_) : "memory");
  else asm volatile("" : "=v"(a), "=v"(b));
  static_for<RAW_S0, 8>(step);
  MF(2);
  if (wl == 1) {
    asm volatile("" : "+v"(a), "+v"(b));
    const unsigned t0 = (unsigned)tag, t1 = (unsigned)(tag >> 32);
#pragma unroll 1
    for (unsigned spin = 0; spin < SPIN_MEM; ++spin) {
      const unsigned bad = ((a[0] ^ t0) | (a[2] ^ t0) | (b[0] ^ t0) | (b[2] ^ t0)) | ((a[1] ^ t1) | (a[3] ^ t1) | (b[1] ^ t1) | (b[3] ^ t1));
      if (!__any(bad != 0u)) break;
      __builtin_amdgcn_s_sleep(4);
      asm volatile("global_load_dwordx4 %0, %2, off sc1\n\tglobal_load_dwordx4 %1, %2, off offset:16 sc1\n\ts_waitcnt vmcnt(0)"
                   : "=&v"(a), "=&v"(b)
                   : "v"(hp_)
                   : "memory");
    }
    lds_wr32(lds0 + (unsigned)(LW_OFF + LW_HELLO * 4), 1u);
    if (bid == 0 && lane == 0) asm volatile("global_store_dwordx2 %0, %1, off sc1\n\ts_nop 1" ::"v"(ws + OFF_TAG), "v"(tag) : "memory");
  }
  MF(11);
  if (rt < 2) lds_spin(lds0 + (unsigned)(LW_OFF + LW_Q * 4), 1u);
  lds_spin(lds0 + (unsigned)K1R_CNT, 8u);
  if constexpr (JOB) {
#pragma unroll
    for (int b = 0; b < 2; ++b)
      asm volatile("s_waitcnt vmcnt(0)"
                   : "+v"(v[b][0]), "+v"(v[b][1]), "+v"(v[b][2]), "+v"(v[b][3]), "+v"(v[b][4]), "+v"(v[b][5]), "+v"(v[b][6]), "+v"(v[b][7])
                   :
                   : "memory");
  }
  MF(12);
  const char* tp = ws + OFF_POT + ct * 128 + (lane < 6 ? lane : 5) * 8;
  u32x2 f;
  asm volatile("global_load_dwordx2 %0, %1, off sc1" : "=v"(f) : "v"(tp) : "memory");
  if constexpr (JOB) {
    static_assert(SB == 4 * 9216, "staging");
#pragma unroll
    for (int b = 0; b < 2; ++b) {
      float cs[8] = {0.f, 0.f, 0.f, 0.f, 0.f, 0.f, 0.f, 0.f};
      tr8_store<288, true>(v[b], da + (unsigned)(b * 18432), cs, std::make_integer_sequence<int, 8>{});
      const f32x4 c0 = {cs[0], cs[1], cs[2], cs[3]}, c1 = {cs[4], cs[5], cs[6], cs[7]};
      const unsigned ca = lds0 + (unsigned)(3 * SB + wl * 2304) + sa * 2u;
      asm volatile("ds_write_b128 %0, %1 offset:%2" ::"v"(ca), "v"(c0), "n"(b * 1152) : "memory");
      asm volatile("ds_write_b128 %0, %1 offset:%2" ::"v"(ca), "v"(c1), "n"(b * 1152 + 16) : "memory");
    }
    lds_inc(lds0 + (unsigned)(LW_OFF + LW_ST * 4));
    lds_spin(lds0 + (unsigned)(LW_OFF + LW_ST * 4), 4u);
    if (isi) {
      const int j = (job & 7) * 4 + wl, ti = j < 16 ? 2 * j + 1 : 2 * (j - 16);
      lds_out<9216>(lds0 + (unsigned)(wl * 9216), ws + OFF_PI + (size_t)(ti * 18 + kb) * 9216, lane);
      f32x4 c[4];
      const unsigned ca = lds0 + (unsigned)(3 * SB + wl * 576) + sa;
#pragma unroll
      for (int i = 0; i < 4; ++i) asm volatile("ds_read_b128 %0, %1 offset:%2" : "=v"(c[i]) : "v"(ca), "n"(i * 2304) : "memory");
      asm volatile("s_waitcnt lgkmcnt(0)" : "+v"(c[0]), "+v"(c[1]), "+v"(c[2]), "+v"(c[3]));
      const f32x4 t = (c[0] + c[1]) + (c[2] + c[3]);
      if (lane < 36) pk_st16(ws + OFF_CSP + ((size_t)kb * F_ + ti * 144) * 4 + sa, __builtin_bit_cast(u32x4, t));
    } else {
      lds_out<4608>(lds0 + (unsigned)(wl * 9216), ws + OFF_PW + (size_t)(wl * 144 + kb) * 4608, lane);
      lds_out<4608>(lds0 + (unsigned)(wl * 9216 + 4608), ws + OFF_PW + (size_t)(wl * 144 + 72 + kb) * 4608, lane);
    }
  }
  MF(8);
  announce(lds0 + (unsigned)(LW_OFF + LW_PK * 4), 4u, ws, CN_PK, isi ? 8 + (job & 3) * 4 + lane : 24 + lane, JOB && lane < (isi ? 4 : 8));
  MF(9);
  if (wl == 0) {
    asm volatile("" : "+v"(f));
#pragma unroll 1
    for (unsigned spin = 0; spin < SPIN_MEM; ++spin) {
      if (!__any(f[0] != (unsigned)tag || f[1] != (unsigned)(tag >> 32))) break;
      __builtin_amdgcn_s_sleep(2);
      asm volatile("global_load_dwordx2 %0, %1, off sc1\n\ts_waitcnt vmcnt(0)" : "=&v"(f) : "v"(tp) : "memory");
    }
  }
}

template <int MT, int NT>
DEVI void k1_gemm(char* smem, const char* ws, int rt, int hp, int mt0, int nt0,
                  bool late, int lane MK_ARG) {
  const unsigned lds0 = (unsigned)(size_t)smem;
  const int g = lane >> 4;
  f32x4 cs[NT];
  unsigned ta[NT];
  unsigned cad[NT];
#pragma unroll
  for (int nt = 0; nt < NT; ++nt) {
    int c = (nt0 + nt) * 16 + g * 4;
    if (c > 104) c = 104;
    const int part = c / 36, ci = c - part * 36;
    cad[nt] = lds0 + (unsigned)(K1_CS_OFF + (part * 40 + (hp & 1) * 4 + ci) * 4);
    asm volatile("" : "+v"(cad[nt]));
    const int to = (nt0 + nt) * 16 + g * 4 > 104 ? 120 : part * 80 + ci * 2 + (ci >= 20 ? 8 : 0);
    ta[nt] = lds0 + (unsigned)(K1_T_OFF + (mt0 * 16 + (lane & 15)) * K1_TP + to);
    asm volatile("" : "+v"(ta[nt]));
  }
  unsigned sa = lds0 + (unsigned)(K1_ST_OFF + (mt0 * 16 + (lane & 15)) * 8);
  asm volatile("" : "+v"(sa));
  f32x4 acc[MT][NT];
#pragma unroll
  for (int mt = 0; mt < MT; ++mt)
#pragma unroll
    for (int nt = 0; nt < NT; ++nt) acc[mt][nt] = f32x4{0.f, 0.f, 0.f, 0.f};
  f32x2 st[MT];
  if (late) bar();
  if constexpr (NT == 7) cnt_wait(ws, CN_X2, rt * 16 + hp, rt ? 24u : 16u);
  bar();
  MK(1);
  {
    GemmMath<K1C, MT, NT, (NT == 7 ? 5 : 3), (NT == 3 ? 6 : NT), true, 2, false, K1_WS> gm(acc);
    gm.setup(lds0, mt0, nt0, lane, 0u);
    bar();
#pragma unroll
    for (int nt = 0; nt < NT; ++nt) asm volatile("ds_read_b128 %0, %1" : "=v"(cs[nt]) : "v"(cad[nt]));
    gm.first();
    gm.template mid<8>();
#pragma unroll
    for (int mt = 0; mt < MT; ++mt) asm volatile("ds_read_b64 %0, %1 offset:%2" : "=v"(st[mt]) : "v"(sa), "n"(mt * 128));
    asm volatile("s_waitcnt vmcnt(0)" ::: "memory");
    gm.last();
  }
#pragma unroll
  for (int nt = 0; nt < NT; ++nt) asm volatile("" : "+v"(cs[nt]));
#pragma unroll
  for (int mt = 0; mt < MT; ++mt) asm volatile("" : "+v"(st[mt]));
  MK(2);
#pragma unroll
  for (int mt = 0; mt < MT; ++mt) {
    f32x2 M = {st[mt][0], st[mt][0]}, R = {st[mt][1], st[mt][1]};
    asm volatile("" : "+v"(M), "+v"(R));
#pragma unroll
    for (int nt = 0; nt < NT; ++nt) {
      const f32x2 v0 = R * __builtin_elementwise_fma(M, f32x2{cs[nt][0], cs[nt][1]}, f32x2{acc[mt][nt][0], acc[mt][nt][1]});
      const f32x2 v1 = R * __builtin_elementwise_fma(M, f32x2{cs[nt][2], cs[nt][3]}, f32x2{acc[mt][nt][2], acc[mt][nt][3]});
      acc[mt][nt][0] = __uint_as_float(pack2v(v0));
      acc[mt][nt][1] = __uint_as_float(pack2v(v1));
      const f32x2 pk = {acc[mt][nt][0], acc[mt][nt][1]};
      asm volatile("ds_write_b64 %0, %1 offset:%2" ::"v"(ta[nt]), "v"(pk), "n"(mt * 16 * K1_TP) : "memory");
    }
  }
}

DEVI float vmax(float a, float b) {
  float r;
  asm("v_max_f32 %0, %1, %2" : "=v"(r) : "v"(a), "v"(b));
  return r;
}
struct Attn {
  unsigned ka, kb, va, vb, m0, m1, m2, pm[2], oa, pa;
  f32x4 mk;
  unsigned long long sel[4];
  const char* ob;
  DEVI void setup(unsigned T, char* o, int r0, int hp, int wave, int lane) {
    const int g = lane >> 4, r = lane & 15, blk = wave >> 1, hd = wave & 1;
    const unsigned co = (unsigned)(g < 2 ? hd * 48 + g * 16 : 32);
    ka = T + (unsigned)((blk * 32 + 2 * r) * K1_TP) + co;
    kb = T + (unsigned)((blk * 32 + 2 * (r & 7)) * K1_TP) + co;
    const int vr = 4 * g + ((lane >> 2) & 3);
    va = T + (unsigned)((blk * 32 + 2 * vr) * K1_TP + (lane & 3) * 8);
    vb = T + (unsigned)((blk * 32 + 2 * (vr & 7)) * K1_TP + (lane & 3) * 8);
    m0 = (g < 2 || (g == 2 && hd == 0)) ? ~0u : 0u;
    m1 = (g < 2 || (g == 2 && hd == 1)) ? ~0u : 0u;
    m2 = g < 2 ? ~0u : 0u;
    pm[0] = pm[1] = 0u;
    const float ci = __uint_as_float(vconst(0xff800000u));
    const unsigned ch = vconst(0xffffu);
#pragma unroll
    for (int e = 0; e < 4; ++e) {
      const int w1 = r + 8 - 4 * g - e;
      mk[e] = (w1 & 15) <= 7 ? 0.f : ci;
      sel[e] = __ballot(w1 >= 8);
      if (w1 >= 8) pm[e >> 1] |= ch << (16 * (e & 1));
    }
    oa = (unsigned)((8 + hp * 4 + hd * 2 + (g >> 1)) * 1024 + ((blk & 1) * 32 + 2 * r + (g & 1)) * 16);
    pa = ((oa & 1023u) >> 2) + (unsigned)((hp * 2 + hd) * 256);
    ob = uni_ptr(o + (size_t)((r0 >> 6) + (wave >> 2)) * (72 * 1024));
    asm volatile("" : "+v"(ka), "+v"(kb), "+v"(va), "+v"(vb), "+v"(m0), "+v"(m1), "+v"(m2), "+v"(pm[0]), "+v"(pm[1]), "+v"(oa));
    asm volatile("" : "+v"(mk), "+v"(pa), "+s"(ob), "+s"(sel[0]), "+s"(sel[1]), "+s"(sel[2]), "+s"(sel[3]));
  }
  DEVI void run(int r0, int hp, int wave, int lane) {
    const int hd = wave & 1;
    u32x4 q[2], k0[2], k1[2];
    u32x2 v[2][2][2];
#pragma unroll
    for (int p = 0; p < 2; ++p) {
      asm volatile("ds_read_b128 %0, %1 offset:%2" : "=v"(q[p]) : "v"(ka), "n"((16 + p) * K1_TP) : "memory");
      asm volatile("ds_read_b128 %0, %1 offset:%2" : "=v"(k0[p]) : "v"(ka), "n"(p * K1_TP + 80) : "memory");
      asm volatile("ds_read_b128 %0, %1 offset:%2" : "=v"(k1[p]) : "v"(kb), "n"((32 + p) * K1_TP + 80) : "memory");
    }
    const unsigned vh = (unsigned)hd * 48u;
#pragma unroll
    for (int p = 0; p < 2; ++p) {
      asm volatile("ds_read_b64_tr_b16 %0, %1 offset:%2" : "=v"(v[p][0][0]) : "v"(va + vh), "n"(p * K1_TP + 160) : "memory");
      asm volatile("ds_read_b64_tr_b16 %0, %1 offset:%2" : "=v"(v[p][0][1]) : "v"(vb + vh), "n"((32 + p) * K1_TP + 160) : "memory");
      asm volatile("ds_read_b64_tr_b16 %0, %1 offset:%2" : "=v"(v[p][1][0]) : "v"(va), "n"(p * K1_TP + 192) : "memory");
      asm volatile("ds_read_b64_tr_b16 %0, %1 offset:%2" : "=v"(v[p][1][1]) : "v"(vb), "n"((32 + p) * K1_TP + 192) : "memory");
    }
    const u32x4 one4 = {vconst(0x3f803f80u), vconst(0x3f803f80u), vconst(0x3f803f80u), vconst(0x3f803f80u)};
    const s16x8 ones = __builtin_bit_cast(s16x8, one4);
    const float c = 0.23570226039551584f * 1.4426950408889634f;
    const f32x4 z = {0.f, 0.f, 0.f, 0.f};
    f32x4 sc[2][2];
#pragma unroll
    for (int p = 0; p < 2; ++p) {
      asm volatile("s_waitcnt lgkmcnt(%3)" : "+v"(q[p]), "+v"(k0[p]), "+v"(k1[p]) : "n"(11 - 3 * p));
      const s16x8 qf = __builtin_bit_cast(s16x8, q[p] & u32x4{m0, m1, m2, m2});
      sc[p][0] = __builtin_amdgcn_mfma_f32_16x16x32_bf16(__builtin_bit_cast(s16x8, k0[p]), qf, mk, 0, 0, 0);
      sc[p][1] = __builtin_amdgcn_mfma_f32_16x16x32_bf16(__builtin_bit_cast(s16x8, k1[p]), qf, mk, 0, 0, 0);
      __builtin_amdgcn_sched_barrier(0);
    }
    if (__builtin_expect(r0 == 0 && wave < 2, 0)) {
#pragma unroll
      for (int p = 0; p < 2; ++p)
#pragma unroll
        for (int e = 0; e < 4; ++e)
          if (lane < 32) sc[p][0][e] = mk[e] - 4.242640687e9f;
    }
    float lg[2][4], mx[2];
#pragma unroll
    for (int p = 0; p < 2; ++p) {
#pragma unroll
      for (int e = 0; e < 4; ++e) lg[p][e] = __builtin_amdgcn_inverse_ballot_w64(sel[e]) ? sc[p][1][e] : sc[p][0][e];
      asm("v_max3_f32 %0, %1, %2, %3\n\tv_max_f32 %0, %0, %4" : "=&v"(mx[p]) : "v"(lg[p][0]), "v"(lg[p][1]), "v"(lg[p][2]), "v"(lg[p][3]));
    }
    {
      const auto a = __builtin_amdgcn_permlane32_swap(__float_as_uint(mx[0]), __float_as_uint(mx[1]), false, false);
      const unsigned b = __float_as_uint(vmax(__uint_as_float(a[0]), __uint_as_float(a[1])));
      const auto d = __builtin_amdgcn_permlane16_swap(b, b, false, false);
      const unsigned f = __float_as_uint(vmax(__uint_as_float(d[0]), __uint_as_float(d[1])) * -c);
      const auto h = __builtin_amdgcn_permlane32_swap(f, f, false, false);
      mx[0] = __uint_as_float(h[0]);
      mx[1] = __uint_as_float(h[1]);
    }
#pragma unroll
    for (int p = 0; p < 2; ++p)
#pragma unroll
      for (int e = 0; e < 4; ++e) lg[p][e] = __builtin_amdgcn_exp2f(__builtin_fmaf(lg[p][e], c, mx[p]));
    asm volatile("s_waitcnt lgkmcnt(0)"
                 : "+v"(v[0][0][0]), "+v"(v[0][0][1]), "+v"(v[0][1][0]), "+v"(v[0][1][1]), "+v"(v[1][0][0]), "+v"(v[1][0][1]),
                   "+v"(v[1][1][0]), "+v"(v[1][1][1]));
    __builtin_amdgcn_sched_barrier(0);
    f32x4 su[2], o0[2], o1[2];
    s16x8 pp[2];
#pragma unroll
    for (int p = 0; p < 2; ++p) {
      const unsigned p0 = pack2(lg[p][0], lg[p][1]), p1 = pack2(lg[p][2], lg[p][3]);
      pp[p] = __builtin_bit_cast(s16x8, u32x4{p0 & ~pm[0], p1 & ~pm[1], p0 & pm[0], p1 & pm[1]});
    }
    __builtin_amdgcn_sched_barrier(0);
#pragma unroll
    for (int p = 0; p < 2; ++p) {
      const s16x8 pf = pp[p];
      const u32x4 v0 = {v[p][0][0][0], v[p][0][0][1], v[p][0][1][0], v[p][0][1][1]};
      const u32x4 v1 = {v[p][1][0][0], v[p][1][0][1], v[p][1][1][0], v[p][1][1][1]};
      su[p] = __builtin_amdgcn_mfma_f32_16x16x32_bf16(ones, pf, z, 0, 0, 0);
      o0[p] = __builtin_amdgcn_mfma_f32_16x16x32_bf16(__builtin_bit_cast(s16x8, v0), pf, z, 0, 0, 0);
      o1[p] = __builtin_amdgcn_mfma_f32_16x16x32_bf16(__builtin_bit_cast(s16x8, v1), pf, z, 0, 0, 0);
    }
    __builtin_amdgcn_sched_barrier(0);
    u32x2 y[2], yl;
    unsigned hm = (unsigned)hd - 1u;
    asm volatile("" : "+s"(hm));
#pragma unroll
    for (int p = 0; p < 2; ++p) {
      const float inv = __builtin_amdgcn_rcpf(su[p][0]);
      y[p] = u32x2{pack2(o0[p][0] * inv, o0[p][1] * inv), pack2(o0[p][2] * inv, o0[p][3] * inv)};
      const unsigned la = pack2(o1[p][0] * inv, o1[p][1] * inv), lb = pack2(o1[p][2] * inv, o1[p][3] * inv);
      yl[p] = (la & hm) | (lb & ~hm);
    }
    const auto s0 = __builtin_amdgcn_permlane16_swap(y[0][0], y[1][0], false, false);
    const auto s1 = __builtin_amdgcn_permlane16_swap(y[0][1], y[1][1], false, false);
    const u32x4 w = {s0[0], s1[0], s0[1], s1[1]};
    asm volatile("global_store_dwordx4 %0, %1, %4 sc1\n\ts_mov_b64 exec, 0xffff\n\tglobal_store_dwordx2 %2, %3, %4 sc1\n\ts_mov_b64 exec, -1\n\ts_nop 0" ::"v"(oa),
                 "v"(w), "v"(pa), "v"(yl), "s"(ob)
                 : "memory");
  }
};

#define NL_MAIN 662
DEVI void math_start(char* smem, const u16* b_sp, const u16* wqkv, char* ws, int bid, int wave, int lane) {
  const unsigned lds0 = (unsigned)(size_t)smem;
  const int tid = wave * 64 + lane, idx = bid >> 3;
  if (wave < 3) {
    static_assert(2 * SZ_ZP == (size_t)256 * 2048 && NCNT <= 16, "mark split");
    const u32x4 mk = {0xffffffffu, 0xffffffffu, 0xffffffffu, 0xffffffffu};
    char* c = ws + OFF_CNT + (size_t)((lane >> 3) * 256 + bid) * 128 + (lane & 7) * 16;
    pk_st16(wave < 2 ? ws + OFF_ZP + (size_t)((bid * 2 + wave) * 64 + lane) * 16 : c, mk);
    if (wave == 2 && lane < (NCNT - 8) * 8) pk_st16(c + 8 * 256 * 128, mk);
  }
  if (wave == 3) {
    const int li = idx * 54 + (lane < 54 ? lane : 53);
    touch64(uni_ptr((const char*)wqkv), (unsigned)(li * 1152 + 512), lds0 + (unsigned)(K1R_DUMMY + 768));
  }
  if (wave == 5 && idx == 31) touch64(uni_ptr((const char*)b_sp), (unsigned)lane * 64u, lds0 + (unsigned)(K1R_DUMMY + 768));
  const u32x4 ones = {0x3f803f80u, 0x3f803f80u, 0x3f803f80u, 0x3f803f80u};
  if (tid < 256) *reinterpret_cast<u32x4*>(smem + (tid >> 6) * K1R::SB + K1R::XB + (tid & 63) * 288 + 240) = ones;
  else if (tid < 288) *reinterpret_cast<u32x4*>(smem + ((tid - 256) >> 3) * K1R::SB + (tid & 7) * 16) = ones;
  else if (tid < 296) *reinterpret_cast<u32x4*>(smem + LW_OFF + (tid - 288) * 16) = u32x4{0u, 0u, 0u, 0u};
  asm volatile("s_waitcnt lgkmcnt(0)" ::: "memory");
}

template <int NST, int NS, int NSW, int WOFF, int XW, int X0, int S0, unsigned BAR>
struct ResX {
  static constexpr int WS = 4 * 4608, R = 10, V1 = 5 * NS, NV = V1 + 10 * (NST - NS), NG = (NSW + 2) / 3;
  s16x8 xf[XW][2];
  s16x8 wf[R];
  unsigned wrd[2][NG], xo, ea, fa, fv, tag;
  const char* xp[3];
  f32x4 (&acc)[2][5];
  DEVI ResX(f32x4 (&a)[2][5]) : acc(a) {}
  static constexpr int st(int V) { return V < V1 ? V / 5 : NS + (V - V1) / 10; }
  static constexpr int hf(int V) { return V < V1 ? 0 : ((V - V1) / 5) & 1; }
  static constexpr bool first(int V) { return V < V1 ? V % 5 == 0 : (V - V1) % 10 == 0; }
  static constexpr int wreq(int V) {
    int n = V + 5;
    if (V >= V1 - 4) n += V < V1 ? V - (V1 - 5) : 4;
    return n < NV ? n : NV;
  }
  static constexpr int XU = V1 + 1, XF = V1 + 11, XR = V1 + 15, XC = V1 + 21;
  static constexpr int oth(int V) { return NS && ((V >= XU && V <= XF && (V - XU) % 2 == 0) || V == XR || (V > XC && V <= XC + 5)) ? 1 : 0; }
  static constexpr int newer(int V) {
    const int F = V + 1 < NV ? V + 1 : V;
    int v = V - 1, n = 0;
    for (; v >= 0 && wreq(v - 1) > F; --v) n += wreq(v) - wreq(v - 1) + oth(v);
    return n + wreq(v) - 1 - F + (v >= 0 ? oth(v) : 0);
  }
  static constexpr int xreq(int T) {
    int n = X0 + 2 * (T + 1);
    if (n > T + XW) n = T + XW;
    if (NS && T <= 11 && n > 18) n = 18;
    if (T < 0) n = X0;
    return n < NST ? n : NST;
  }
  static_assert(NS == 0 || (xreq(NS - 2) == NST && XC + 5 < NV), "the last steps only compute and swap");
  template <int T, int M>
  DEVI void xld() {
    asm volatile("global_load_dwordx4 %0, %1, %2 sc1" : "=v"(xf[T % XW][M]) : "v"(xo), "s"(xp[M == 0 ? 0 : (T < NS ? 1 : 2)]));
    if constexpr (M == 1 && T + 1 < NST) asm volatile("v_add_u32 %0, 0x2000, %0" : "+v"(xo));
  }
  template <int V>
  DEVI void rd() {
    constexpr int T = st(V), sl = (T / 2 + S0) % NSW;
    asm volatile("ds_read_b128 %0, %1 offset:%2" : "=v"(wf[V % R]) : "v"(wrd[hf(V)][sl / 3]), "n"((sl % 3) * WS + (T & 1) * 9216 + (V % 5) * 32));
  }
  template <int V, int M, int H>
  DEVI void mf() {
    acc[M][V % 5] = __builtin_amdgcn_mfma_f32_16x16x32_bf16(wf[V % R], xf[st(V) % XW][H], acc[M][V % 5], 0, 0, 0);
    __builtin_amdgcn_sched_barrier(0);
  }
  template <int V>
  DEVI void unit() {
    constexpr int T = st(V), n = V % 5, nx = xreq(T) - xreq(T - 1), nw = wreq(V) - wreq(V - 1);
    constexpr int vm = 2 * (xreq(T - 1) - 1 - T);
    static_assert(newer(V) <= 15, "lgkmcnt range");
    if constexpr (first(V) && V % 2 == 0) asm volatile("s_waitcnt vmcnt(%0) lgkmcnt(%1)" ::"n"(vm), "n"(newer(V)));
    else if constexpr (first(V)) asm volatile("s_waitcnt vmcnt(%0)" ::"n"(vm));
    else if constexpr (V % 2 == 0) asm volatile("s_waitcnt lgkmcnt(%0)" ::"n"(newer(V)));
    __builtin_amdgcn_sched_barrier(0);
    if constexpr (V < V1) mf<V, V & 1, V & 1>();
    else mf<V, 0, hf(V)>();
    if constexpr (nw > 0) rd<wreq(V - 1)>();
    __builtin_amdgcn_sched_barrier(0);
    if constexpr (V < V1) {
      mf<V, 1 - (V & 1), 1 - (V & 1)>();
      if constexpr (nw > 1) rd<wreq(V - 1) + 1>();
      if constexpr (nx == 2 && n < 4) xld<xreq(T - 1) + n / 2, n % 2>();
      if constexpr (nx == 1 && n == (T & 1)) xld<xreq(T - 1), 0>();
      if constexpr (nx == 1 && n == (T & 1) + 2) xld<xreq(T - 1), 1>();
      if constexpr (n == 4 && (T & 1) && ((BAR >> (T / 2)) & 1u)) bar();
    } else if constexpr (NS == 0) {
      constexpr int k = (V % 10) / 2;
      if constexpr (V % 2 == 0 && k < 2 * nx) xld<xreq(T - 1) + k / 2, k % 2>();
      if constexpr (V % 10 == 8 && nx > 0 && xreq(T) == NST)
        asm volatile("s_mov_b64 exec, 1\n\tds_add_u32 %0, %1\n\ts_mov_b64 exec, -1" ::"v"(fa), "v"(fv));
    } else {
      if constexpr (oth(V) && V < XF) asm volatile("ds_write_b128 %0, %1 offset:%2" ::"v"(ea), "v"(acc[1][(V - XU) / 2]), "n"((V - XU) * 512));
      if constexpr (V == XF) asm volatile("ds_write_b32 %0, %1" ::"v"(fa), "v"(fv));
      if constexpr (V == XF + 2) asm volatile("v_xor_b32 %0, 4, %0" : "+v"(fa));
      if constexpr (V == XR) asm volatile("ds_read_b32 %0, %1" : "+v"(fv) : "v"(fa));
      if constexpr (V == XR + 2) asm volatile("v_xor_b32 %0, 0x2000, %0" : "+v"(ea));
      if constexpr (V == XC) {
        unsigned c = 1u << 18;
        asm volatile(
            "s_waitcnt lgkmcnt(%4)\n\tv_cmp_ne_u32 vcc, %3, %0\n\ts_cbranch_vccnz .Lw%=\n\t.subsection 1\n.Lw%=:\n\tds_read_b32 %0, %2\n\t"
            "s_waitcnt lgkmcnt(0)\n\tv_cmp_ne_u32 vcc, %3, %0\n\t" SAFE_OUT(".Lr%=") "s_cbranch_vccnz .Lw%=\n\ts_branch .Lr%=\n\t.subsection 0\n.Lr%=:"
            : "+v"(fv), NOP_(c)
            : "v"(fa), "s"(tag), "n"(wreq(XC) - wreq(XR))
            : "vcc", "scc");
      }
      if constexpr (oth(V) && V > XC) asm volatile("ds_read_b128 %0, %1 offset:%2" : "=v"(acc[1][V - XC - 1]) : "v"(ea), "n"((V - XC - 1) * 1024));
    }
    __builtin_amdgcn_sched_barrier(0);
  }
  template <int... V>
  DEVI void units(std::integer_sequence<int, V...>) {
    (unit<V>(), ...);
  }
  DEVI void run() {
    units(std::make_integer_sequence<int, NV>{});
    if constexpr (NS > 0) {
      asm volatile("s_waitcnt lgkmcnt(0)" : "+v"(acc[1][0]), "+v"(acc[1][1]), "+v"(acc[1][2]), "+v"(acc[1][3]), "+v"(acc[1][4]));
#pragma unroll
      for (int i = 0; i < 5; ++i) acc[0][i] += acc[1][i];
    }
  }
  template <int... V>
  DEVI void pro(std::integer_sequence<int, V...>) {
    (rd<V>(), ...);
  }
  DEVI void prow() { pro(std::make_integer_sequence<int, wreq(-1)>{}); }
  template <int T0, int... T>
  DEVI void prox(std::integer_sequence<int, T...>) {
    ((xld<T0 + T, 0>(), xld<T0 + T, 1>()), ...);
  }
  DEVI void setup(unsigned lds0, const char* xbase, int wave, int lane, unsigned red, unsigned nth) {
    const int g = lane >> 4, r = lane & 15, kq = wave & 1;
    xp[0] = xbase + wave * 256;
    xp[1] = xbase + (wave ^ 1) * 256;
    xp[2] = xp[0] + (kq ? -4096 : 4096);
    xo = (unsigned)((4 * kq + g) * 1024 + r * 16);
#pragma unroll
    for (int i = 0; i < 2 * NG; ++i) {
      wrd[i / NG][i % NG] = lds0 + (unsigned)(WOFF + (i % NG) * 3 * WS + (kq ^ (i / NG)) * 4608 + (2 * (r & 7) + 16 * (g & 1) + (g >> 1)) * 144 + (r >> 3) * 16);
      if (i < NG || NS == 0 || i % NG == ((NS / 2 + S0) % NSW) / 3) asm volatile("" : "+v"(wrd[i / NG][i % NG]));
    }
    asm volatile("" : "+v"(xo));
    if constexpr (NS > 0) {
      ea = lds0 + red + (unsigned)((wave ^ 1) * 8192 + lane * 16);
      fa = lds0 + (unsigned)(LW_OFF + (LW_PAIR + wave) * 4);
      fv = tag = nth;
      asm volatile("" : "+v"(ea), "+v"(fa), "+v"(fv));
    }
  }
  template <int N>
  DEVI void init(u32x2 (&xv)[5]) {
    asm volatile("s_waitcnt vmcnt(%5)" : "+v"(xv[0]), "+v"(xv[1]), "+v"(xv[2]), "+v"(xv[3]), "+v"(xv[4]) : "n"(N));
#pragma unroll
    for (int i = 0; i < 5; ++i) {
      acc[0][i] = f32x4{bf_lo(xv[i][0]), bf_hi(xv[i][0]), bf_lo(xv[i][1]), bf_hi(xv[i][1])};
      asm volatile("" : "+v"(acc[0][i]));
      acc[1][i] = f32x4{0.f, 0.f, 0.f, 0.f};
    }
  }
};

constexpr int P5_XW = 8, P5_X0 = 5, P2_X0 = 4;
constexpr unsigned P5_BAR = 0x2aaau;
constexpr int P5_W = 15 * 1024;
constexpr int P2_W = K1_ST_OFF - 18 * 4608;
constexpr int P5_RED = 0;
static_assert(P5_RED % 16384 == 0 && P5_RED + 3 * 8192 + 5120 <= P5_W + 18432, "exchange area");
static_assert(P2_W >= K1_T_OFF + 144 * K1_TP + 64 && P2_W % 16 == 0 && P5_W + 8 * 18432 <= LW_OFF, "LDS placement");

struct ResIn {
  const char* xp;
  unsigned o, o2, st;
  DEVI void setup(const char* src, bool rm, int rb, int ct, int wave, int lane) {
    const int g = lane >> 4, g2 = g & 1, rl = wave * 16 + (lane & 15);
    xp = uni_ptr(rm ? src + ((size_t)rb * 64 * D_ + ct * 72) * 2 : src + (size_t)(rb * 72 + ct * 9) * 1024);
    o = rm ? (unsigned)(rl * (D_ * 2) + g * 8) : (unsigned)((g >> 1) * 1024 + rl * 16 + g2 * 8);
    o2 = rm ? (unsigned)(rl * (D_ * 2) + g2 * 8 + 128) : (unsigned)(8192 + rl * 16 + g2 * 8);
    st = rm ? 32u : 2048u;
    asm volatile("" : "+v"(o), "+v"(o2));
    asm volatile("" : "+s"(xp));
  }
  DEVI void load(u32x2 (&xv)[5]) {
#pragma unroll
    for (int i = 0; i < 4; ++i) asm volatile("global_load_dwordx2 %0, %1, %2 sc1" : "=v"(xv[i]) : "v"(o), "s"(xp + i * st));
    asm volatile("global_load_dwordx2 %0, %1, %2 sc1" : "=v"(xv[4]) : "v"(o2), "s"(xp));
  }
};

DEVI void p2_gemm(f32x4 (&acc)[2][5], u32x2 (&xv)[5], unsigned lds0, const char* ob, int layer, int wave, int lane MK_ARG) {
  ResX<9, 0, 5, P2_W, 9, P2_X0, 0, 0u> gm(acc);
  gm.setup(lds0, ob, wave, lane, 0u, 0u);
  unsigned a = (gm.xo & ~1023u) | ((gm.xo & 1023u) >> 2);
  gm.xo += 8192u;
  gm.fa = lds0 + (unsigned)(LW_OFF + (LW_T + layer) * 4);
  gm.fv = vconst(1u);
  asm volatile("" : "+v"(a), "+v"(gm.xo), "+v"(gm.fa));
  const char* pp[2] = {gm.xp[0] - wave * 192, gm.xp[2] - wave * 192};
  place();
  bar();
  MK(6);
  unsigned e[2][4];
#pragma unroll
  for (int d = 0; d < 8; ++d)
    asm volatile("global_load_dword %0, %1, %2 offset:%3 sc1" : "=v"(e[d / 4][d % 4]) : "v"(a), "s"(pp[d / 4]), "n"((d % 4) * 256));
  gm.template prox<1>(std::make_integer_sequence<int, P2_X0 - 1>{});
  gm.prow();
  gm.template init<6 + 2 * P2_X0>(xv);
  asm volatile("s_waitcnt vmcnt(%8)"
               : "+v"(e[0][0]), "+v"(e[0][1]), "+v"(e[0][2]), "+v"(e[0][3]), "+v"(e[1][0]), "+v"(e[1][1]), "+v"(e[1][2]), "+v"(e[1][3])
               : "n"(2 * P2_X0 - 2));
  gm.xf[0][0] = __builtin_bit_cast(s16x8, u32x4{e[0][0], e[0][1], e[0][2], e[0][3]});
  gm.xf[0][1] = __builtin_bit_cast(s16x8, u32x4{e[1][0], e[1][1], e[1][2], e[1][3]});
  __builtin_amdgcn_sched_barrier(0);
  gm.run();
}

struct ResOut {
  const char *xp[3], *sp;
  unsigned xo, so;
  DEVI void setup(char* xim, char* xs, u16* out, bool fin, int rb, int ct, int wave, int lane) {
    const int g = lane >> 4, rl = wave * 16 + (lane & 15);
    xp[0] = uni_ptr(fin ? (const char*)(out + (size_t)rb * 64 * D_ + ct * 72) : xim + (size_t)(rb * 72 + ct * 9) * 1024);
    xp[1] = xp[0] + (fin ? 64 : 4096);
    xp[2] = xp[0] + (fin ? 128 : 8192);
    sp = uni_ptr(xs + (size_t)(rb * 8 + ct) * 512);
    xo = (unsigned)(((g & 1) * 2 + (g >> 1)) * (fin ? 16 : 1024) + rl * (fin ? D_ * 2 : 16));
    so = (unsigned)(rl * 8);
    asm volatile("" : "+v"(xo), "+v"(so), "+s"(xp[0]), "+s"(xp[1]), "+s"(xp[2]), "+s"(sp));
  }
  DEVI void run(const f32x4 (&a)[5], bool fin) {
    u32x2 y[5];
    u32x4 v[3];
#pragma unroll
    for (int nt = 0; nt < 5; ++nt) y[nt] = u32x2{pack2(a[nt][0], a[nt][1]), pack2(a[nt][2], a[nt][3])};
#pragma unroll
    for (int i = 0; i < 3; ++i) {
      const auto s0 = __builtin_amdgcn_permlane16_swap(y[2 * i][0], y[i < 2 ? 2 * i + 1 : 4][0], false, false);
      const auto s1 = __builtin_amdgcn_permlane16_swap(y[2 * i][1], y[i < 2 ? 2 * i + 1 : 4][1], false, false);
      v[i] = u32x4{s0[0], s1[0], s0[1], s1[1]};
    }
    if (fin) {
      asm volatile("global_store_dwordx4 %0, %1, %4\n\tglobal_store_dwordx4 %0, %2, %5\n\ts_mov_b64 exec, 0xffff\n\tglobal_store_dwordx4 %0, %3, %6\n\t"
                   "s_mov_b64 exec, -1\n\ts_nop 0" ::"v"(xo), "v"(v[0]), "v"(v[1]), "v"(v[2]), "s"(xp[0]), "s"(xp[1]), "s"(xp[2])
                   : "memory");
      return;
    }
    asm volatile("global_store_dwordx4 %0, %1, %4 sc1\n\tglobal_store_dwordx4 %0, %2, %5 sc1\n\ts_mov_b64 exec, 0xffff\n\t"
                 "global_store_dwordx4 %0, %3, %6 sc1\n\ts_mov_b64 exec, -1\n\ts_nop 0" ::"v"(xo), "v"(v[0]), "v"(v[1]), "v"(v[2]),
                 "s"(xp[0]), "s"(xp[1]), "s"(xp[2])
                 : "memory");
    const u32x4 one4 = {vconst(0x3f803f80u), vconst(0x3f803f80u), vconst(0x3f803f80u), vconst(0x3f803f80u)};
    const bool lo = __builtin_amdgcn_inverse_ballot_w64(0xffffull);
    v[2] = u32x4{lo ? v[2][0] : 0u, lo ? v[2][1] : 0u, lo ? v[2][2] : 0u, lo ? v[2][3] : 0u};
    f32x4 s = {0.f, 0.f, 0.f, 0.f}, q = s;
#pragma unroll
    for (int i = 0; i < 3; ++i) {
      const s16x8 u = __builtin_bit_cast(s16x8, v[i]);
      q = __builtin_amdgcn_mfma_f32_16x16x32_bf16(u, u, q, 0, 0, 0);
      s = __builtin_amdgcn_mfma_f32_16x16x32_bf16(__builtin_bit_cast(s16x8, one4), u, s, 0, 0, 0);
    }
    float q0 = q[0], q1 = q[1], q2 = q[2], q3 = q[3];
    const bool b0 = __builtin_amdgcn_inverse_ballot_w64(0xaaaaaaaaaaaaaaaaull), b1 = __builtin_amdgcn_inverse_ballot_w64(0xccccccccccccccccull);
    q0 = b0 ? q1 : q0;
    q2 = b0 ? q3 : q2;
    asm("" : "+v"(q0), "+v"(q2));
    const f32x2 sq = {s[0], b1 ? q2 : q0};
    asm volatile("s_mov_b64 exec, %3\n\tglobal_store_dwordx2 %0, %1, %2 sc1\n\ts_mov_b64 exec, -1" ::"v"(so), "v"(sq), "s"(sp), "s"(0xf0000f0000f0000full) : "memory");
  }
};

using K3C = GemmCfg<128, 144>;
constexpr int K3_Z1_OFF = K3C::LDS_BYTES;
constexpr int K3_C2_OFF = K3_Z1_OFF + K3C::XB;
constexpr int K3_ZC_OFF = K3_C2_OFF + 2048;
constexpr int K3_DM_OFF = K3_ZC_OFF + 1024;
constexpr int K3_ST_OFF = K3_Z1_OFF + 4 * 5120;
constexpr int K3_C1_OFF = K3_ST_OFF + 2048;
constexpr int K3_BS_OFF = K3_C1_OFF + 576;
static_assert(K3_BS_OFF + 512 <= LW_OFF, "K3 LDS");
static_assert(P5_W + 19 * 1024 == K3C::SB && K3C::SB == 34 * 1024, "w_out ring");

DEVI void red4(float (&t)[4], float& s, float& q) {
#pragma unroll
  for (int j = 0; j < 2; ++j) {
    const auto p = __builtin_amdgcn_permlane16_swap(__float_as_uint(t[j]), __float_as_uint(t[2 + j]), false, false);
    const unsigned v = __float_as_uint(__uint_as_float(p[0]) + __uint_as_float(p[1]));
    const auto w = __builtin_amdgcn_permlane32_swap(v, v, false, false);
    t[j] = __uint_as_float(w[0]) + __uint_as_float(w[1]);
    asm volatile("" : "+v"(t[j]));
  }
  s = t[0];
  q = t[1];
}
DEVI void red8(const f32x4& a, const f32x4& b, float& s, float& q) {
  float t[4];
#pragma unroll
  for (int j = 0; j < 4; ++j) {
    t[j] = a[j] + b[j];
    asm volatile("" : "+v"(t[j]));
  }
  red4(t, s, q);
}
DEVI void red16(const f32x4& a, const f32x4& b, const f32x4& c, const f32x4& d, float& s, float& q) {
  float t[4];
#pragma unroll
  for (int j = 0; j < 4; ++j) {
    float u = a[j] + b[j], v = c[j] + d[j];
    asm volatile("" : "+v"(u), "+v"(v));
    t[j] = u + v;
    asm volatile("" : "+v"(t[j]));
  }
  red4(t, s, q);
}

constexpr int b_k(int s) { return s == 0 ? 0 : (s < 3 ? 6 + s : (s == 3 ? 1 : (s < 6 ? 1 + s : (s == 6 ? 4 : s - 5)))); }
constexpr int b_xp(int s) { return s == 0 ? 0 : (b_k(s) >= 4 ? (b_k(s) % 5) / 2 : b_xp(s - 1)); }
template <bool B>
struct UMath {
  static constexpr int D = 4, R = 6, NV = 162, SB = K3C::SB, P = K3C::P;
  s16x8 wf[R], xf[2][2];
  s16x8 (&xc)[4][2][2];
  f32x4 (&acc)[2][9];
  f32x4 rc[4][2];
  f32x2 cs[9];
  unsigned ra, ca;
  template <int I>
  DEVI void cread() {
    if constexpr (I < 8) asm volatile("ds_read_b128 %0, %1 offset:%2" : "=v"(rc[I / 2][I % 2]) : "v"(ra), "n"((I / 2) * 512 + (I % 2) * 16));
    else if constexpr (I < 17) asm volatile("ds_read_b64 %0, %1 offset:%2" : "=v"(cs[I - 8]) : "v"(ca), "n"((I - 8) * 128));
  }
  unsigned xa, wa;
  unsigned fa, fw, fv;
  DEVI UMath(f32x4 (&a)[2][9], s16x8 (&x)[4][2][2]) : xc(x), acc(a) {}
  static constexpr int wslot(int s) { return B ? (s + 1) % 4 : s % 4; }
  static constexpr int xslot(int s) { return B ? b_k(s) % 5 : s % 5; }
  static constexpr int xpair(int s) { return B ? b_xp(s) : xslot(s) / 2; }
  static constexpr bool res(int q) { return !B || b_k(q / 2) >= 4; }
  static constexpr bool chk(int s) { return s >= 3 && s < 7; }
  static constexpr bool xop(int v) { return v + D < NV && (v + D) % 9 < 2 && res((v + D) / 9); }
  static constexpr int ops1(int v) { return (xop(v) || (B && ((v % 18 == 9 && chk(v / 18)) || (v % 18 == 0 && v > 0)))) ? 1 : 0; }
  static constexpr int newer(int V) {
    int v0 = (V % 9 == 8 ? V : V + 1) - D, n = 0;
    if (v0 >= 0 && !(V % 9 == 0 && res(V / 9))) n += ops1(v0);
    for (int v = v0 + 1; v < V; ++v)
      if (v >= 0) n += (v + D < NV ? 1 : (B ? 0 : 4)) + ops1(v);
    return n;
  }
  template <int V>
  DEVI void wread() {
    asm volatile("ds_read_b128 %0, %1 offset:%2" : "=v"(wf[V % R]) : "v"(wa), "n"((wslot(V / 18) & 1) * SB + ((V % 18) / 9) * 32 * P + (V % 9) * 32));
  }
  template <int Q, int M>
  DEVI void xread() {
    asm volatile("ds_read_b128 %0, %1 offset:%2" : "=v"(xf[Q & 1][M]) : "v"(xa), "n"((xslot(Q / 2) & 1) * SB + (Q & 1) * 8192 + M * (B ? 256 : 64)));
  }
  template <int V, int M_>
  DEVI void mf() {
    constexpr int M = ((V % 9) & 1) ? 1 - M_ : M_, q = V / 9;
    if constexpr (!B) acc[M][V % 9] = __builtin_amdgcn_mfma_f32_16x16x32_bf16(xf[q & 1][M], wf[V % R], acc[M][V % 9], 0, 0, 0);
    else if constexpr (res(q)) acc[M][V % 9] = __builtin_amdgcn_mfma_f32_16x16x32_bf16(wf[V % R], xf[q & 1][M], acc[M][V % 9], 0, 0, 0);
    else acc[M][V % 9] = __builtin_amdgcn_mfma_f32_16x16x32_bf16(wf[V % R], xc[b_k(q / 2)][q & 1][M], acc[M][V % 9], 0, 0, 0);
    __builtin_amdgcn_sched_barrier(0);
  }
  template <int V>
  DEVI void unit() {
    constexpr int U = V % 18, s = V / 18;
    static_assert(newer(V) <= 15, "lgkmcnt range");
    if constexpr ((V % 9) % 2 == 0 && V > 0) asm volatile("s_waitcnt lgkmcnt(%0)" ::"n"(newer(V)));
    __builtin_amdgcn_sched_barrier(0);
    mf<V, 0>();
    if constexpr (V + D < NV) wread<V + D>();
    if constexpr (!B && V + D >= NV) {
      cread<(V + D - NV) * 4>();
      cread<(V + D - NV) * 4 + 1>();
    }
    if constexpr (s < 8 && U == 13 && (wslot(s + 1) >> 1) != (wslot(s) >> 1))
      asm volatile("v_add_u32 %0, %1, %0" : "+v"(wa) : "n"(((wslot(s + 1) >> 1) - (wslot(s) >> 1)) * 2 * SB));
    __builtin_amdgcn_sched_barrier(0);
    mf<V, 1>();
    if constexpr (xop(V)) xread<(V + D) / 9, (V + D) % 9>();
    if constexpr (!B && V + D >= NV) {
      cread<(V + D - NV) * 4 + 2>();
      cread<(V + D - NV) * 4 + 3>();
      if constexpr (V == NV - 1) cread<16>();
    }
    if constexpr (s < 8) {
      if constexpr (U == 11 && xpair(s + 1) != xpair(s)) asm volatile("v_add_u32 %0, %1, %0" : "+v"(xa) : "n"((xpair(s + 1) - xpair(s)) * 2 * SB));
      if constexpr (!B && U == 13) __builtin_amdgcn_s_barrier();
      if constexpr (B && U == 9 && chk(s)) asm volatile("ds_read_u8 %0, %1 offset:%2" : "+v"(fv) : "v"(fa), "n"(s == 6 ? 0 : 8));
      unsigned n = 1u << 18;
      if constexpr (B && U == 13 && chk(s)) {
        asm volatile(
            "v_cmp_gt_u32 vcc, %3, %0\n\ts_cbranch_vccnz .Lw%=\n\t.subsection 1\n.Lw%=:\n\tds_read_u8 %0, %2 offset:%4\n\ts_waitcnt lgkmcnt(0)\n\t"
            "v_cmp_gt_u32 vcc, %3, %0\n\t" SAFE_OUT(".Lr%=") "s_cbranch_vccnz .Lw%=\n\ts_branch .Lr%=\n\t.subsection 0\n.Lr%=:"
            : "+v"(fv), NOP_(n)
            : "v"(fa), "n"(s == 6 ? 4 : s + 2), "n"(s == 6 ? 0 : 8)
            : "vcc", "scc");
      }
    }
    if constexpr (B && U == 15) asm volatile("v_mov_b32 %0, %1" : "+v"(fv) : "n"(s + 1));
    if constexpr (B && ((U == 0 && s > 0) || V == NV - 1)) asm volatile("ds_write_b8 %0, %1" ::"v"(fw), "v"(fv));
    __builtin_amdgcn_sched_barrier(0);
  }
  template <int... V>
  DEVI void units(std::integer_sequence<int, V...>) {
    (unit<V>(), ...);
  }
  DEVI void first() {
    if constexpr (!B) {
      xread<0, 0>();
      xread<0, 1>();
    }
    wread<0>();
    wread<1>();
    wread<2>();
    wread<3>();
    asm volatile("s_waitcnt lgkmcnt(0)");
  }
  DEVI void run() {
    units(std::make_integer_sequence<int, NV>{});
#pragma unroll
    for (int m = 0; m < 2; ++m)
#pragma unroll
      for (int n = 0; n < 9; ++n) asm volatile("" : "+v"(acc[m][n]));
  }
};

struct CMath {
  static constexpr int D = 4, R = 6, SB = K3C::SB;
  s16x8 wf[R], xf[2][2];
  f32x4 (&acc)[2][9];
  unsigned wa[2], xa[2][2];
  DEVI CMath(f32x4 (&a)[2][9]) : acc(a) {}
  static constexpr int xop(int u, bool last) { return (u + D) % 9 < 2 && !(last && u + D >= 18) ? 1 : 0; }
  static constexpr int newer(int U, bool last) {
    int v0 = (U % 9 == 8 ? U : U + 1) - D, n = 0;
    if (U % 9 != 0) n += xop((v0 + 18) % 18, last && v0 >= 0);
    for (int v = v0 + 1; v < U; ++v) n += (last && v + D >= 18 ? 0 : 1) + xop((v + 18) % 18, last && v >= 0);
    return n;
  }
  template <int SL, int U>
  DEVI void wread() {
    asm volatile("ds_read_b128 %0, %1 offset:%2" : "=v"(wf[U % R]) : "v"(wa[SL / 2]), "n"((SL & 1) * SB + U * 1024));
  }
  template <int SL, int H, int M>
  DEVI void xread() {
    asm volatile("ds_read_b128 %0, %1 offset:%2" : "=v"(xf[H][M]) : "v"(xa[SL / 2][H]), "n"((SL & 1) * SB + M * 2048));
  }
  template <int U, int M_>
  DEVI void mf() {
    constexpr int M = ((U % 9) & 1) ? 1 - M_ : M_;
    acc[M][U % 9] = __builtin_amdgcn_mfma_f32_16x16x32_bf16(wf[U % R], xf[U / 9][M], acc[M][U % 9], 0, 0, 0);
    __builtin_amdgcn_sched_barrier(0);
  }
  template <int SL, bool BAR, bool LAST, int U>
  DEVI void unit() {
    constexpr int PU = (U + D) % 18, PS = U + D < 18 ? SL : (SL + 1) % 4;
    if constexpr ((U % 9) % 2 == 0) asm volatile("s_waitcnt lgkmcnt(%0)" ::"n"(newer(U, LAST)));
    __builtin_amdgcn_sched_barrier(0);
    mf<U, 0>();
    if constexpr (!(LAST && U + D >= 18)) wread<PS, PU>();
    __builtin_amdgcn_sched_barrier(0);
    mf<U, 1>();
    if constexpr (xop(U, LAST)) xread<PS, PU / 9, PU % 9>();
    if constexpr (BAR && U == 13) __builtin_amdgcn_s_barrier();
    __builtin_amdgcn_sched_barrier(0);
  }
  template <int SL, bool BAR, bool LAST, int... U>
  DEVI void units(std::integer_sequence<int, U...>) {
    (unit<SL, BAR, LAST, U>(), ...);
  }
  template <int SL, bool BAR = true, bool LAST = false>
  DEVI void stage() {
    units<SL, BAR, LAST>(std::make_integer_sequence<int, 18>{});
  }
  DEVI void setup(unsigned lds0, int wm, int lane) {
    const int g = lane >> 4, r = lane & 15;
#pragma unroll
    for (int i = 0; i < 2; ++i) {
      wa[i] = lds0 + (unsigned)(i * 2 * SB + K3C::XB + lane * 16);
#pragma unroll
      for (int h = 0; h < 2; ++h) xa[i][h] = lds0 + (unsigned)(i * 2 * SB + (wm * 32 + r) * 128 + (((4 * h + g) ^ ((r >> 1) & 7)) * 16));
    }
    asm volatile("" : "+v"(wa[0]), "+v"(wa[1]), "+v"(xa[0][0]), "+v"(xa[0][1]), "+v"(xa[1][0]), "+v"(xa[1][1]));
  }
  DEVI void first() {
    xread<2, 0, 0>();
    xread<2, 0, 1>();
    wread<2, 0>();
    wread<2, 1>();
    wread<2, 2>();
    wread<2, 3>();
    asm volatile("s_waitcnt lgkmcnt(0)");
  }
};

constexpr int ZQ = 4;
constexpr int NR1 = 3;
DEVI void p3_g0(char* smem, char* __restrict__ ws, int layer, int mi, int ni, int wm, int lane MK_ARG) {
  constexpr int SB = K3C::SB;
  const unsigned lds0 = (unsigned)(size_t)smem;
  const int g = lane >> 4, r = lane & 15;
  const unsigned lw = lds0 + (unsigned)(LW_OFF + layer * 4), me = 1u << (8 * wm);
  f32x4 ac[2][9];
#pragma unroll
  for (int mt = 0; mt < 2; ++mt)
#pragma unroll
    for (int j = 0; j < 9; ++j) ac[mt][j] = f32x4{0.f, 0.f, 0.f, 0.f};
  unsigned ra = lds0 + (unsigned)(K3_ST_OFF + (wm * 32 + g * 8) * 4), ca = lds0 + (unsigned)(K3_C2_OFF + r * 8);
  asm volatile("" : "+v"(ra), "+v"(ca));
  s16x8 xc[4][2][2];
  UMath<false> gm(ac, xc);
  gm.ra = ra;
  gm.ca = ca;
  gm.xa = lds0 + (unsigned)(g * 2048 + (wm * 32 + 8 * (r >> 2) + (r & 3)) * 16);
  gm.wa = lds0 + (unsigned)(K3C::XB + (g * 8 + (r & 7)) * K3C::P + (r >> 3) * 16);
  unsigned adw = lw + LW_AD * 4, mev = vconst(me), po = (unsigned)(ni * 1024 + lane * 4);
  const char* zp = uni_ptr(ws + OFF_ZP + (size_t)layer * SZ_ZP + (size_t)mi * 16384 + wm * 256);
  asm volatile("" : "+v"(gm.xa), "+v"(gm.wa), "+v"(adw), "+v"(po));
  asm volatile("" : "+s"(zp));
  place();
  bar();
  gm.first();
  gm.run();
  asm volatile("s_mov_b64 exec, 1\n\tds_add_u32 %0, %1\n\ts_mov_b64 exec, -1" ::"v"(adw), "v"(mev) : "memory");
  MK(11);
  f32x2 z[9][4];
  f32x2 sm[4], sq[4];
  {
    f32x4 (&rc)[4][2] = gm.rc;
    f32x2 (&cs)[9] = gm.cs;
    asm volatile("s_waitcnt lgkmcnt(1)"
                 : "+v"(rc[0][0]), "+v"(rc[0][1]), "+v"(rc[1][0]), "+v"(rc[1][1]), "+v"(rc[2][0]), "+v"(rc[2][1]), "+v"(rc[3][0]), "+v"(rc[3][1]));
#pragma unroll
    for (int j = 0; j < 9; ++j) asm volatile("" : "+v"(cs[j]));
    auto RC = [&](int k, int i) { return f32x2{rc[k][i / 2][(i % 2) * 2], rc[k][i / 2][(i % 2) * 2 + 1]}; };
#pragma unroll
    for (int j = 0; j < 9; ++j) {
      f32x2 u[4], w[4];
#pragma unroll
      for (int i = 0; i < 4; ++i) u[i] = __builtin_elementwise_fma(RC(0, i), cs[j], f32x2{ac[i / 2][j][(i % 2) * 2], ac[i / 2][j][(i % 2) * 2 + 1]});
#pragma unroll
      for (int i = 0; i < 4; ++i) w[i] = u[i] * u[i];
#pragma unroll
      for (int i = 0; i < 4; ++i) w[i] = __builtin_elementwise_fma(w[i], RC(3, i), RC(2, i));
#pragma unroll
      for (int i = 0; i < 4; ++i) w[i] = u[i] * w[i];
      __builtin_amdgcn_sched_barrier(0);
#pragma unroll
      for (int i = 0; i < 4; ++i) w[i] = f32x2{__builtin_amdgcn_exp2f(w[i][0]), __builtin_amdgcn_exp2f(w[i][1])};
      __builtin_amdgcn_sched_barrier(0);
#pragma unroll
      for (int i = 0; i < 4; ++i) w[i] = __builtin_elementwise_fma(w[i], RC(1, i), RC(1, i));
      __builtin_amdgcn_sched_barrier(0);
#pragma unroll
      for (int i = 0; i < 4; ++i) w[i] = f32x2{__builtin_amdgcn_rcpf(w[i][0]), __builtin_amdgcn_rcpf(w[i][1])};
      __builtin_amdgcn_sched_barrier(0);
#pragma unroll
      for (int i = 0; i < 4; ++i) z[j][i] = u[i] * w[i];
#pragma unroll
      for (int i = 0; i < 4; ++i) {
        if (j == 1) sm[i] = z[0][i] + z[1][i];
        if (j > 1) sm[i] += z[j][i];
      }
#pragma unroll
      for (int i = 0; i < 4; ++i) sq[i] = j ? __builtin_elementwise_fma(z[j][i], z[j][i], sq[i]) : z[j][i] * z[j][i];
      asm volatile("" : "+v"(z[j][0]), "+v"(z[j][1]), "+v"(z[j][2]), "+v"(z[j][3]));
      __builtin_amdgcn_sched_barrier(0);
    }
  }
  f32x4 e;
  {
    float v[4][4], d[2][4], f[4];
#pragma unroll
    for (int i = 0; i < 4; ++i) {
      v[i][0] = sm[i][0];
      v[i][1] = sq[i][0];
      v[i][2] = sm[i][1];
      v[i][3] = sq[i][1];
    }
    asm volatile("s_nop 1" : "+v"(v[3][1]), "+v"(v[3][3]), "+v"(v[2][1]), "+v"(v[2][3]));
#pragma unroll
    for (int i = 0; i < 2; ++i)
#pragma unroll
      for (int c = 0; c < 4; ++c)
        asm volatile("v_add_f32_dpp %0, %1, %1 row_ror:8 row_mask:0xf bank_mask:0x3\n\tv_add_f32_dpp %0, %2, %2 row_ror:8 row_mask:0xf bank_mask:0xc"
                     : "=&v"(d[i][c])
                     : "v"(v[i][c]), "v"(v[i + 2][c]));
#pragma unroll
    for (int c = 0; c < 4; ++c)
      asm volatile("v_add_f32_dpp %0, %1, %1 row_shl:4 row_mask:0xf bank_mask:0x5\n\tv_add_f32_dpp %0, %2, %2 row_shr:4 row_mask:0xf bank_mask:0xa"
                   : "=&v"(f[c])
                   : "v"(d[0][c]), "v"(d[1][c]));
    asm volatile(
        "v_add_f32_dpp %0, %0, %0 quad_perm:[1,0,3,2] row_mask:0xf bank_mask:0xf\n\t"
        "v_add_f32_dpp %1, %1, %1 quad_perm:[1,0,3,2] row_mask:0xf bank_mask:0xf\n\t"
        "v_add_f32_dpp %2, %2, %2 quad_perm:[1,0,3,2] row_mask:0xf bank_mask:0xf\n\t"
        "v_add_f32_dpp %3, %3, %3 quad_perm:[1,0,3,2] row_mask:0xf bank_mask:0xf\n\t"
        "v_add_f32_dpp %0, %0, %0 quad_perm:[2,3,0,1] row_mask:0xf bank_mask:0xf\n\t"
        "v_add_f32_dpp %1, %1, %1 quad_perm:[2,3,0,1] row_mask:0xf bank_mask:0xf\n\t"
        "v_add_f32_dpp %2, %2, %2 quad_perm:[2,3,0,1] row_mask:0xf bank_mask:0xf\n\t"
        "v_add_f32_dpp %3, %3, %3 quad_perm:[2,3,0,1] row_mask:0xf bank_mask:0xf"
        : "+v"(f[0]), "+v"(f[1]), "+v"(f[2]), "+v"(f[3]));
    e = f32x4{f[0], __uint_as_float(__float_as_uint(f[1]) & 0x7fffffffu), f[2], __uint_as_float(__float_as_uint(f[3]) & 0x7fffffffu)};
  }
  asm volatile("s_mov_b64 exec, %3\n\tglobal_store_dwordx4 %0, %1, %2 sc1\n\ts_mov_b64 exec, -1\n\ts_nop 0" ::"v"(po), "v"(e), "s"(zp),
               "s"(0x1111111111111111ull)
               : "memory");
  asm volatile("s_mov_b64 exec, 1\n\tds_add_u32 %0, %1\n\ts_mov_b64 exec, -1" ::"v"(adw), "v"(mev) : "memory");
  __builtin_amdgcn_s_setprio(2);
  MK(12);
  const char* dst = uni_ptr(ws + OFF_Z2 + (size_t)layer * SZ_Z + (size_t)ni * S_ * 288 + (size_t)(mi * 4 + wm) * 9216 + 4096);
  const unsigned lane16 = (unsigned)lane * 16u;
  u32x4 wb[9];
  {
    static_assert(b_k(7) == 2 && b_k(8) == 3, "order");
    const char* wp = uni_ptr(ws + OFF_PI + (size_t)ni * (18 * K3C::WB) + 11 * K3C::WB + wm * 9216 + 4096);
#pragma unroll
    for (int j = 0; j < 8; ++j) asm volatile("global_load_dwordx4 %0, %1, %2 offset:%3 sc1" : "=v"(wb[j]) : "v"(lane16), "s"(wp), "n"(j * 1024 - 4096) : "memory");
    asm volatile("global_load_dwordx4 %0, %1, %2 sc1" : "=v"(wb[8]) : "v"(lane16 + 4096u), "s"(wp) : "memory");
  }
  u32x4 y[9];
  {
    const unsigned src = (unsigned)(g * 4096 + r * 16);
    unsigned zw = lds0 + (unsigned)(K3_ZC_OFF + (wm * 32 + 2 * r + (g & 1)) * 4), zr = lds0 + (unsigned)(K3_ZC_OFF + (wm * 32 + g * 8) * 4);
    asm volatile("" : "+v"(zw), "+v"(zr));
    f32x4 a, b, c, d;
    __builtin_amdgcn_s_sleep(6);
#pragma unroll 1
    for (unsigned spin = 0; spin < SPIN_MEM; ++spin) {
      asm volatile(
          "global_load_dwordx4 %0, %4, %5 sc1\n\t"
          "global_load_dwordx4 %1, %4, %5 offset:1024 sc1\n\t"
          "global_load_dwordx4 %2, %4, %5 offset:2048 sc1\n\t"
          "global_load_dwordx4 %3, %4, %5 offset:3072 sc1\n\t"
          "s_waitcnt vmcnt(0)"
          : "=&v"(a), "=&v"(b), "=&v"(c), "=&v"(d)
          : "v"(src), "s"(zp)
          : "memory");
      const unsigned bad = (__float_as_uint(a[1]) | __float_as_uint(a[3]) | __float_as_uint(b[1]) | __float_as_uint(b[3]) |
                            __float_as_uint(c[1]) | __float_as_uint(c[3]) | __float_as_uint(d[1]) | __float_as_uint(d[3])) >> 31;
      if (!__any(bad)) break;
    }
    MK(13);
    float s, q, zmb, zi;
    red16(a, b, c, d, s, q);
    ln_finish(s, q, 1.0f / (float)FH_, zmb, zi);
    f32x4 R[2], M[2];
    asm volatile(
        "ds_write2st64_b32 %4, %5, %6 offset1:2\n\t"
        "ds_read_b128 %0, %7\n\tds_read_b128 %2, %7 offset:512\n\tds_read_b128 %1, %7 offset:16\n\tds_read_b128 %3, %7 offset:528\n\t"
        "s_waitcnt lgkmcnt(0)"
        : "=&v"(R[0]), "=&v"(R[1]), "=&v"(M[0]), "=&v"(M[1])
        : "v"(zw), "v"(zi), "v"(-zmb * zi), "v"(zr)
        : "memory");
    MK(28);
    auto st = [&](int j) {
      if (j < 8) asm volatile("global_store_dwordx4 %0, %1, %2 offset:%3 sc1\n\ts_nop 1" ::"v"(lane16), "v"(y[j]), "s"(dst), "n"(j * 1024 - 4096) : "memory");
      else asm volatile("global_store_dwordx4 %0, %1, %2 sc1\n\ts_nop 1" ::"v"(lane16 + 4096u), "v"(y[j]), "s"(dst) : "memory");
    };
#pragma unroll
    for (int j = 0; j < 9; ++j) {
      {
        float t[8];
#pragma unroll
        for (int k = 0; k < 8; ++k) {
          t[k] = __builtin_fmaf(z[j][k / 2][k % 2], R[k / 4][k % 4], M[k / 4][k % 4]);
          asm volatile("" : "+v"(t[k]));
        }
        y[j] = u32x4{pack2(t[0], t[1]), pack2(t[2], t[3]), pack2(t[4], t[5]), pack2(t[6], t[7])};
      }
      st(j);
      __builtin_amdgcn_sched_barrier(0);
    }
    MK(29);
  }
  lds_inc(lw + LW_AD * 4, me);
  MK(24);
  __builtin_amdgcn_s_setprio(2);
  lds_spin4<2>(lw + LW_S * 4, 4u + (unsigned)(wm >> 1));
  {
    const unsigned fa = lds0 + (unsigned)((wm >> 1) * SB + K3C::XB + (wm & 1) * 9216) + lane16;
#pragma unroll
    for (int j = 0; j < 9; ++j) {
      asm volatile("ds_write_b128 %0, %1 offset:%2" ::"v"(fa), "v"(wb[j]), "n"(j * 1024) : "memory");
    }
    lds_inc(lw + LW_AD * 4, me);
  }
  announce(lw + LW_Z * 4, 4u, ws, ((mi >= ZQ) ? CN_Z2 : CN_Z) + layer, lane * 16 + ni, lane < 16);
  lds_inc(lw + LW_AD * 4, me);
  MK(27);
  f32x4 acc[2][9];
  {
    float bs[2];
    asm volatile("ds_read2_b32 %0, %1 offset1:16\n\ts_waitcnt lgkmcnt(0)"
                 : "=v"(*reinterpret_cast<f32x2*>(bs))
                 : "v"(lds0 + (unsigned)(K3_BS_OFF + (wm * 32 + r) * 4))
                 : "memory");
    lds_spin4<2>(lw + LW_S * 4, 6u + (unsigned)(wm >> 1));
    const unsigned fa = lds0 + (unsigned)((2 + (wm >> 1)) * SB + K3C::XB + (wm & 1) * 9216) + lane16;
#pragma unroll
    for (int j = 0; j < 9; ++j) {
      asm volatile("ds_write_b128 %0, %1 offset:%2" ::"v"(fa), "v"(y[j]), "n"(j * 1024) : "memory");
    }
    lds_inc(lw + LW_AD * 4, me);
#pragma unroll
    for (int mt = 0; mt < 2; ++mt) {
      f32x4 b4 = {bs[mt], bs[mt], bs[mt], bs[mt]};
      asm volatile("" : "+v"(b4));
#pragma unroll
      for (int j = 0; j < 9; ++j) acc[mt][j] = b4;
    }
  }
  u32x4 z1[9];
  const char* hp = uni_ptr(ws + OFF_H + (size_t)layer * SZ_Z + (size_t)((mi * 2 + (wm >> 1)) * 288 + ni * 18) * 1024);
  unsigned ho = (unsigned)((lane >> 5) * 1024 + ((wm & 1) * 32 + (lane & 31)) * 16);
  unsigned zb = lds0 + (unsigned)(wm * 4096) + lane16, zf = lw + (unsigned)(LW_S * 4 + wm);
  asm volatile("" : "+v"(ho), "+v"(zb), "+v"(zf));
  asm volatile("" : "+s"(hp));
  {
    CMath gm(acc);
    gm.setup(lds0, wm, lane);
    unsigned za = lds0 + (unsigned)(K3_Z1_OFF + wm * 5120) + lane16;
    asm volatile("" : "+v"(za));
    MK(15);
    lds_spin4<2>(lw + LW_L * 4, 8u);
    lds_spin4(lw + LW_AD * 4, 6u);
    MK(17);
    gm.first();
    __builtin_amdgcn_s_setprio(0);
    gm.stage<2, false>();
    gm.stage<3>();
    place();
#pragma unroll 1
    for (int t = 0; t < 7; ++t) {
      gm.stage<0>();
#pragma unroll
      for (int j = 0; j < NR1; ++j) asm volatile("ds_read_b128 %0, %1 offset:%2" : "=v"(z1[j]) : "v"(za), "n"(j * 1024));
      gm.stage<1>();
#pragma unroll
      for (int j = 0; j < NR1; ++j) asm volatile("" : "+v"(z1[j]));
      gm.stage<2>();
      gm.stage<3>();
    }
    gm.stage<0>();
    gm.stage<1, false, true>();
#pragma unroll
    for (int j = 0; j < 18; ++j) asm volatile("" : "+v"(acc[j / 9][j % 9]));
  }
  MK(16);
  if (NR1 < 5) lds_inc(lw + LW_AD * 4, me);
  unsigned zv;
  u32x4 zl[4];
#pragma unroll
  for (int j = 0; j < 9; ++j) {
    const bool sec = j >= NR1 + 3;
    if (j == NR1 || j == NR1 + 4) {
      unsigned n = 1u << 18;
      asm volatile(
          "s_waitcnt lgkmcnt(0)\n\tv_cmp_gt_u32 vcc, %8, %0\n\ts_cbranch_vccnz .Lw%=\n\t.subsection 1\n.Lw%=:\n\tds_read_u8 %0, %6\n\tds_read_b128 %2, %7 offset:%9\n\t"
          "ds_read_b128 %3, %7 offset:%9+1024\n\tds_read_b128 %4, %7 offset:%9+2048\n\tds_read_b128 %5, %7 offset:%9+3072\n\ts_waitcnt lgkmcnt(0)\n\t"
          "v_cmp_gt_u32 vcc, %8, %0\n\t" SAFE_OUT(".Lr%=") "s_cbranch_vccnz .Lw%=\n\ts_branch .Lr%=\n\t.subsection 0\n.Lr%=:"
          : "+v"(zv), NOP_(n), "+v"(zl[0]), "+v"(zl[1]), "+v"(zl[2]), "+v"(zl[3])
          : "v"(zf), "v"(zb), "n"(sec ? 11 : 10), "n"(sec ? SB : 0)
          : "memory", "vcc", "scc");
#pragma unroll
      for (int k = 0; k < 4; ++k)
        if (j + k < 9) z1[j + k] = zl[k];
    }
    if (j == NR1 - 1 || (j == NR1 + 3 && j < 8))
      asm volatile("ds_read_u8 %0, %5\n\tds_read_b128 %1, %6 offset:%7\n\tds_read_b128 %2, %6 offset:%7+1024\n\tds_read_b128 %3, %6 offset:%7+2048\n\t"
                   "ds_read_b128 %4, %6 offset:%7+3072"
                   : "=&v"(zv), "=&v"(zl[0]), "=&v"(zl[1]), "=&v"(zl[2]), "=&v"(zl[3])
                   : "v"(zf), "v"(zb), "n"(sec ? SB : 0)
                   : "memory");
    asm volatile("" : "+v"(z1[j]));
    u32x2 h[2];
#pragma unroll
    for (int mt = 0; mt < 2; ++mt) {
      const f32x4 a = acc[mt][j];
      h[mt] = u32x2{pack2v(f32x2{bf_lo(z1[j][2 * mt]), bf_hi(z1[j][2 * mt])} * f32x2{a[0], a[1]}),
                    pack2v(f32x2{bf_lo(z1[j][2 * mt + 1]), bf_hi(z1[j][2 * mt + 1])} * f32x2{a[2], a[3]})};
    }
    const auto s0 = __builtin_amdgcn_permlane16_swap(h[0][0], h[1][0], false, false);
    const auto s1 = __builtin_amdgcn_permlane16_swap(h[0][1], h[1][1], false, false);
    const u32x4 v = {s0[0], s1[0], s0[1], s1[1]};
    asm volatile("global_store_dwordx4 %0, %1, %2 offset:%3 sc1\n\ts_nop 1" ::"v"(ho), "v"(v), "s"(hp + (j / 2) * 4096), "n"((j % 2) * 2048) : "memory");
    __builtin_amdgcn_sched_barrier(0);
  }
  MK(18);
}

DEVI void k3_const(char* smem, char* ws, const u16* b_sp, int layer, int bid, int mi, int ni, int wm) {
  const int lane = lane_id(), l = lane < 54 ? lane : 53, p = l / 18;
  const char* kp = uni_ptr(ws + OFF_CSK + bid * 2048 + wm * 512);
  const unsigned q16 = (unsigned)(l - p * 18) * 16u;
  unsigned bs;
  f32x4 t;
  lds_spin((unsigned)(size_t)smem + (unsigned)(LW_OFF + (layer ? LW_T + 1 : LW_HELLO) * 4), layer ? 12u : 2u);
  asm volatile("global_load_ushort %0, %1, %2" : "=v"(bs) : "v"((unsigned)(lane & 31) * 2u), "s"(uni_ptr((const char*)(b_sp + mi * 128 + wm * 32))) : "memory");
  if (layer) {
    asm volatile("global_load_dwordx4 %0, %1, %2 sc1\n\ts_waitcnt vmcnt(0)" : "=&v"(t) : "v"(q16), "s"(kp) : "memory");
  } else {
    const char* cp = uni_ptr(ws + OFF_CSP + ni * 1152 + wm * 288);
    f32x4 a[6];
#pragma unroll
    for (int i = 0; i < 6; ++i)
      asm volatile("global_load_dwordx4 %0, %1, %2 sc1" : "=v"(a[i]) : "v"(q16 + (unsigned)(p * (6 * F_ * 4))), "s"(cp + i * (F_ * 4)) : "memory");
    asm volatile("s_waitcnt vmcnt(0)" : "+v"(a[0]), "+v"(a[1]), "+v"(a[2]), "+v"(a[3]), "+v"(a[4]), "+v"(a[5]));
    t = ((a[0] + a[1]) + (a[2] + a[3])) + (a[4] + a[5]);
#pragma unroll
    for (int j = 0; j < 4; ++j)
      t[j] += __int_as_float(__builtin_amdgcn_ds_bpermute((lane + 18) * 4, __float_as_int(t[j]))) +
              __int_as_float(__builtin_amdgcn_ds_bpermute((lane + 36) * 4, __float_as_int(t[j])));
  }
  asm volatile("" : "+v"(bs));
  const unsigned lds0 = (unsigned)(size_t)smem;
  f32x4 t0 = wm < 2 ? f32x4{t[0], t[0], t[1], t[1]} : t, t1 = wm < 2 ? f32x4{t[2], t[2], t[3], t[3]} : t;
  unsigned a0 = lds0 + (unsigned)(wm < 2 ? K3_C2_OFF + wm * 576 + lane * 32 : K3_C1_OFF + (wm - 2) * 288 + lane * 16), a1 = a0 + (wm < 2 ? 16u : 0u);
  unsigned ab = lds0 + (unsigned)(K3_BS_OFF + (wm * 32 + (lane & 31)) * 4);
  bs <<= 16;
  asm volatile("" : "+v"(t0), "+v"(t1), "+v"(a0), "+v"(a1), "+v"(ab), "+v"(bs));
  bar();
  asm volatile("ds_write_b32 %4, %5\n\ts_mov_b64 exec, 0x3ffff\n\tds_write_b128 %0, %2\n\tds_write_b128 %1, %3\n\ts_mov_b64 exec, -1" ::"v"(a0), "v"(a1),
               "v"(t0), "v"(t1), "v"(ab), "v"(bs)
               : "memory");
  if (layer == 0 && lane < 24) asm volatile("global_store_dwordx4 %0, %1, %2\n\ts_nop 1" ::"v"((unsigned)lane * 16u), "v"(t), "s"(kp) : "memory");
}

DEVI void p3_g1(char* smem, char* __restrict__ ws, int layer, int mi, int ni, int wm, int lane MK_ARG) {
  const unsigned lds0 = (unsigned)(size_t)smem;
  const int g = lane >> 4, r = lane & 15;
  const unsigned lw = lds0 + (unsigned)(LW_OFF + layer * 4);
  const unsigned xrd = lds0 + (unsigned)(g * 2048 + (wm * 32 + r) * 16);
  s16x8 xc[4][2][2];
  {
    const char* xs = uni_ptr(ws + OFF_XS + (size_t)layer * 2 * SZ_XS + (size_t)(mi * 2 + (wm >> 1)) * 4096 + (wm & 1) * 256);
    f32x4 a, b;
    auto pick = [&](int t) {
      bar();
      asm volatile(
          "ds_read_b128 %0, %4\n\tds_read_b128 %1, %4 offset:256\n\tds_read_b128 %2, %4 offset:8192\n\tds_read_b128 %3, %4 offset:8448\n\t"
          "s_waitcnt lgkmcnt(0)"
          : "=&v"(xc[t][0][0]), "=&v"(xc[t][0][1]), "=&v"(xc[t][1][0]), "=&v"(xc[t][1][1])
          : "v"(xrd + (unsigned)(t * K3C::SB))
          : "memory");
    };
    __builtin_amdgcn_s_setprio(2);
    pick(0);
    pick(1);
    asm volatile("global_load_dwordx4 %0, %2, %3 sc1\n\tglobal_load_dwordx4 %1, %2, %3 offset:512 sc1"
                 : "=&v"(a), "=&v"(b)
                 : "v"((unsigned)(g * 1024 + r * 16)), "s"(xs)
                 : "memory");
    pick(2);
    pick(3);
    bar();
    asm volatile("s_waitcnt vmcnt(0)" : "+v"(a), "+v"(b) : : "memory");
    float s, q, mb, rinv;
    red8(a, b, s, q);
    bar();
    asm volatile("" : "+v"(s), "+v"(q));
    ln_finish(s, q, 1.0f / (float)D_, mb, rinv);
    asm volatile("" : "+v"(mb), "+v"(rinv));
    bar();
    asm volatile("" : "+v"(mb), "+v"(rinv));
    const float k0 = -2.0f * 0.7978845608028654f * 1.4426950408889634f, k1 = k0 * 0.044715f;
    float sd = __builtin_amdgcn_rcpf(rinv), c0 = k0 * rinv, c1 = k1 * rinv * rinv * rinv;
    asm volatile("" : "+v"(sd));
    asm volatile("" : "+v"(c0));
    asm volatile("" : "+v"(c1));
    const float e0 = lane < 32 ? -mb : c0, e1 = lane < 32 ? sd : c1;
    asm volatile("ds_write2st64_b32 %0, %1, %2 offset1:2" ::"v"(
                     lds0 + (unsigned)(K3_ST_OFF + (g >> 1) * 1024 + (wm * 32 + 2 * r + (g & 1)) * 4)),
                 "v"(e0), "v"(e1)
                 : "memory");
    bar();
    asm volatile("s_waitcnt lgkmcnt(0)" ::: "memory");
    bar();
    __builtin_amdgcn_s_setprio(0);
  }
  f32x4 ac[2][9];
#pragma unroll
  for (int mt = 0; mt < 2; ++mt)
#pragma unroll
    for (int j = 0; j < 9; ++j) ac[mt][j] = f32x4{0.f, 0.f, 0.f, 0.f};
  {
    UMath<true> gm(ac, xc);
    gm.xa = xrd;
    gm.wa = lds0 + (unsigned)(K3C::XB + (g * 8 + (r & 7)) * K3C::P + (r >> 3) * 16);
    gm.fa = lw + (unsigned)(LW_AD * 4 + (lane & 3));
    gm.fw = lw + (unsigned)(LW_S * 4 + wm);
    gm.fv = 0u;
    asm volatile("" : "+v"(gm.fa), "+v"(gm.fw), "+v"(gm.fv), "+v"(gm.xa), "+v"(gm.wa));
    __builtin_amdgcn_s_sleep(40);
    lds_spin4<4>(lw + LW_L * 4, 4u);
    gm.first();
    place();
    lds_spin4(lw + LW_AD * 4, 2u);
    __builtin_amdgcn_s_setprio(1);
    gm.run();
    __builtin_amdgcn_s_setprio(0);
  }
  MK(14);
  f32x2 rc[4];
  f32x4 cs[9];
  const int l2 = lane_id();
  {
    const unsigned ra = lds0 + (unsigned)(K3_ST_OFF + (wm * 32 + (l2 & 15)) * 4), ca = lds0 + (unsigned)(K3_C1_OFF + (l2 & 48));
#pragma unroll
    for (int k = 0; k < 4; ++k) asm volatile("ds_read2_b32 %0, %1 offset1:16" : "=v"(rc[k]) : "v"(ra + (unsigned)(k * 512)) : "memory");
#pragma unroll
    for (int j = 0; j < 9; ++j) asm volatile("ds_read_b128 %0, %1 offset:%2" : "=v"(cs[j]) : "v"(ca), "n"(j * 64) : "memory");
    asm volatile("s_waitcnt lgkmcnt(0)" : "+v"(rc[0]), "+v"(rc[1]), "+v"(rc[2]), "+v"(rc[3]));
#pragma unroll
    for (int j = 0; j < 9; ++j) asm volatile("" : "+v"(cs[j]));
  }
  const unsigned za = lds0 + (unsigned)(K3_Z1_OFF + wm * 5120 + l2 * 16);
  u32x4 z1[9];
  unsigned zf;
#pragma unroll
  for (int t = 0; t < 18; ++t) {
    const int j = t / 2, mt = t % 2;
    float u[4], w[4];
#define Q4(A, B, C, D) A "\n\t" B "\n\t" C "\n\t" D "\n\t"
    asm volatile(Q4("v_fma_f32 %0, %8, %12, %16", "v_fma_f32 %1, %8, %13, %17", "v_fma_f32 %2, %8, %14, %18", "v_fma_f32 %3, %8, %15, %19")
                 Q4("v_mul_f32 %4, %0, %0", "v_mul_f32 %5, %1, %1", "v_mul_f32 %6, %2, %2", "v_mul_f32 %7, %3, %3")
                 Q4("v_fma_f32 %4, %4, %11, %10", "v_fma_f32 %5, %5, %11, %10", "v_fma_f32 %6, %6, %11, %10", "v_fma_f32 %7, %7, %11, %10")
                 Q4("v_mul_f32 %4, %4, %0", "v_mul_f32 %5, %5, %1", "v_mul_f32 %6, %6, %2", "v_mul_f32 %7, %7, %3")
                 Q4("v_exp_f32 %4, %4", "v_exp_f32 %5, %5", "v_exp_f32 %6, %6", "v_exp_f32 %7, %7")
                 Q4("v_fma_f32 %4, %4, %9, %9", "v_fma_f32 %5, %5, %9, %9", "v_fma_f32 %6, %6, %9, %9", "v_fma_f32 %7, %7, %9, %9")
                 Q4("v_rcp_f32 %4, %4", "v_rcp_f32 %5, %5", "v_rcp_f32 %6, %6", "v_rcp_f32 %7, %7")
                 Q4("v_mul_f32 %4, %4, %0", "v_mul_f32 %5, %5, %1", "v_mul_f32 %6, %6, %2", "v_mul_f32 %7, %7, %3")
                 : "=&v"(u[0]), "=&v"(u[1]), "=&v"(u[2]), "=&v"(u[3]), "=&v"(w[0]), "=&v"(w[1]), "=&v"(w[2]), "=&v"(w[3])
                 : "v"(rc[0][mt]), "v"(rc[1][mt]), "v"(rc[2][mt]), "v"(rc[3][mt]), "v"(cs[j][0]), "v"(cs[j][1]), "v"(cs[j][2]), "v"(cs[j][3]),
                   "v"(ac[mt][j][0]), "v"(ac[mt][j][1]), "v"(ac[mt][j][2]), "v"(ac[mt][j][3]));
    z1[j][2 * mt] = pack2(w[0], w[1]);
    z1[j][2 * mt + 1] = pack2(w[2], w[3]);
    if (mt && j < NR1) asm volatile("ds_write_b128 %0, %1 offset:%2" ::"v"(za), "v"(z1[j]), "n"(j * 1024) : "memory");
    if (t == 9) asm volatile("s_waitcnt lgkmcnt(0)" ::: "memory");
    __builtin_amdgcn_sched_barrier(0);
    if (t == 2 * ZQ - 7)
      asm volatile("global_load_dword %0, %1, %2 sc1"
                   : "=v"(zf)
                   : "v"(vconst(0u)), "s"(uni_ptr(ws + OFF_CNT + (size_t)((CN_Z2 + layer) * 256 + mi * 16 + ni) * 128))
                   : "memory");
    if (t == 2 * ZQ - 4) {
      asm volatile("s_waitcnt vmcnt(0)" : "+v"(zf) : : "memory");
      if (wm == 1 && __builtin_amdgcn_readfirstlane(zf) != 15u - ZQ) cnt_wait(ws, CN_Z2 + layer, mi * 16 + ni, 16u - ZQ);
    }
    bar();
    __builtin_amdgcn_sched_barrier(0);
  }
  MK(15);
#pragma unroll 1
  for (int t = 20; t < 32; ++t) bar();
  __builtin_amdgcn_s_sleep(9);
#pragma unroll
  for (int k = NR1; k < 9; ++k) {
    if (k == NR1 + 4) lds_spin4(lw + LW_AD * 4, 7u);
    asm volatile("ds_write_b128 %0, %1 offset:%2" ::"v"(lds0 + (unsigned)(wm * 4096 + l2 * 16)), "v"(z1[k]),
                 "n"(((k - NR1) / 4) * K3C::SB + ((k - NR1) % 4) * 1024)
                 : "memory");
    if (k == NR1 + 3 || k == 8) asm volatile("ds_write_b8 %0, %1" ::"v"(lw + (unsigned)(LW_S * 4 + wm)), "v"(vconst(k == NR1 + 3 ? 10u : 11u)) : "memory");
  }
}

constexpr int W5A = 22;
constexpr int NPF = 30;
struct Loader {
  u32x4 pf[NPF];
  unsigned lds0, lane16;
  int wl, lane;

  template <int R0, int N, int P0, int PE>
  DEVI void pf_load(const char* src) {
    const unsigned o = lane16 + (unsigned)(wl * 1024);
#pragma unroll
    for (int t = 0; t < N; ++t) {
      unsigned a = o + (unsigned)((P0 + 4 * t) * 1024);
      if (P0 + 4 * t + 3 >= PE && P0 + 4 * t + wl >= PE) a = lane16;
      asm volatile("global_load_dwordx4 %0, %1, %2 sc1" : "=v"(pf[R0 + t]) : "v"(a), "s"(src) : "memory");
    }
  }
  template <int R0, int N>
  DEVI void pf_pin() {
#pragma unroll
    for (int t = 0; t < N; ++t) asm volatile("" : "+v"(pf[R0 + t]));
  }
  template <int R0, int N>
  DEVI void pf_undef() {
#pragma unroll
    for (int t = 0; t < N; ++t) asm volatile("" : "=v"(pf[R0 + t]));
  }
  template <int PB, int A, int N>
  DEVI void pf_put(unsigned dst) {
    constexpr int T0 = (A - PB) / 4, T1 = (A + N - 1 - PB) / 4;
    const unsigned a = lds0 + dst + lane16 + (unsigned)((wl + PB + 4 * T0 - A + 4) * 1024) - 4096u;
#pragma unroll
    for (int t = T0; t <= T1; ++t) {
      const int p0 = PB + 4 * t;
      if ((p0 >= A || p0 + wl >= A) && (p0 + 3 < A + N || p0 + wl < A + N))
        asm volatile("ds_write_b128 %0, %1 offset:%2" ::"v"(a), "v"(pf[t]), "n"((t - T0) * 4096) : "memory");
    }
  }
  template <int N>
  DEVI void dma_run(const char* src, unsigned dst) {
    constexpr int B = N / 4, R = N % 4;
    const unsigned f = (unsigned)(wl * B + (wl < R ? wl : R)) * 1024u, o = lane16 + f, m = lds0 + dst + f;
    dma_seq<B>(src, o, m);
    if (wl < R) dma16_sc1(src + B * 1024, o, m + (unsigned)(B * 1024));
  }
  template <int V>
  DEVI void wait_w() {
    asm volatile("s_cmp_lt_u32 %0, 2\n\ts_cbranch_scc1 1f\n\ts_waitcnt vmcnt(%1)\n1:\n\ts_waitcnt vmcnt(%2)" ::"s"(wl), "n"(V - 1), "n"(V) : "memory", "scc");
  }
  template <int V>
  DEVI void wbar() {
    asm volatile("s_waitcnt vmcnt(%0) lgkmcnt(0)" ::"n"(V) : "memory");
    bar();
  }

  DEVI void p1(char* smem, char* ws, int bid, int rt, int hp MK_ARG) {
    constexpr int SB = K1C::SB;
    static_assert(K1C::XB == 18 * 1024 && K1C::WB == 14 * 1024, "K1 shape");
    const char* wp = uni_ptr(ws + OFF_PQ + (size_t)hp * (9 * K1C::WB) + 4 * K1C::WB);
    static_assert(3 * SB + K1C::XB >= P5_W + 5 * 18432, "free while the GEMM before ends");
    const char* wq = wp - 4 * K1C::WB;
    dma_run<14>(wq + 3 * K1C::WB, (unsigned)(3 * SB + K1C::XB));
    dma_run<28>(wp, (unsigned)K1_WX);
    if (wl == 3) dma16_sc1(uni_ptr(ws + OFF_CSQ + bid * 512), lane16 & 511u, lds0 + (unsigned)K1_CS_OFF);
    __builtin_amdgcn_s_setprio(0);
    wait_vm<0>();
    const bool lo = wl < 2;
    const int x0 = lo ? wl * 5 : wl * 4 + 2, w0 = lo ? wl * 3 : wl * 4 - 2;
    unsigned xo[5];
#pragma unroll
    for (int i = 0; i < 5; ++i) {
      const int c = (x0 + i) * 64 + lane;
      const int ch = c / 144;
      int gr = rt * 128 - 16 + (c - ch * 144);
      if (gr < 0) gr = 0;
      xo[i] = (unsigned)(((gr >> 6) * 72 + ch + 3 - (i & 3)) * 1024 + (gr & 63) * 16);
      asm volatile("" : "+v"(xo[i]));
    }
    const char* xp = uni_ptr(ws + OFF_X1 + SZ_X - 3072);
    wp += 2 * K1C::WB + w0 * 1024;
    auto dma_x = [&](unsigned so) {
      const unsigned m = lds0 + so + (unsigned)(x0 * 1024);
      dma4<4, true>(xp, xo[0], xo[1], xo[2], xo[3], m);
      if (lo) dma16_sc1(xp, xo[4], m + 4096u);
      xp += 8192;
    };
    auto dma_w = [&](unsigned so) {
      const unsigned m = lds0 + so + (unsigned)(K1C::XB + w0 * 1024);
      dma_seq<3>(wp, lane16, m);
      if (!lo) dma16_sc1(wp + 3072, lane16, m + 3072u);
      wp += K1C::WB;
    };
    const char* xs = uni_ptr(ws + OFF_XS + SZ_XS);
    const int g = lane >> 4, p2 = 64 + (lane & 7);
    int ra = rt * 128 - 16 + wl * 32 + 2 * (lane & 15);
    if (ra < 0) ra = 0;
    unsigned sa = (unsigned)(((ra >> 6) * 8 + g * 2) * 512 + (ra & 63) * 8);
    unsigned sb = (unsigned)(((rt * 2 + 1) * 8 + (lane >> 3)) * 512 + (2 * p2 - 80) * 8);
    unsigned sw = lds0 + (unsigned)(K1_ST_OFF + (2 * (lane < 32 ? wl * 16 + (lane & 15) : p2) + (g & 1)) * 8);
    asm volatile("" : "+v"(sa), "+v"(sb), "+v"(sw));
    MK(22);
    __builtin_amdgcn_s_setprio(3);
    bar();
    dma_run<14>(wq, (unsigned)K1C::XB);
    dma_run<14>(wq + K1C::WB, (unsigned)(SB + K1C::XB));
    dma_run<14>(wq + 2 * K1C::WB, (unsigned)(2 * SB + K1C::XB));
    MK(1);
    bar();
    dma_x(0u);
    dma_x((unsigned)SB);
    wbar<4>();
    MK(29);
    dma_x((unsigned)(2 * SB));
    dma_x((unsigned)(3 * SB));
    wbar<8>();
    MX(1, 3);
    dma_x(0u);
    wbar<8>();
    MX(1, 4);
    dma_x((unsigned)SB);
    f32x4 a, b, e;
    asm volatile("global_load_dwordx4 %0, %3, %5 sc1\n\tglobal_load_dwordx4 %1, %3, %5 offset:512 sc1\n\tglobal_load_dwordx4 %2, %4, %5 sc1"
                 : "=&v"(a), "=&v"(b), "=&v"(e)
                 : "v"(sa), "v"(sb), "s"(xs)
                 : "memory");
    wbar<11>();
    MX(1, 5);
    dma_w((unsigned)SB);
    dma_x((unsigned)(2 * SB));
    wbar<15>();
    MX(1, 6);
    dma_w((unsigned)(2 * SB));
    dma_x((unsigned)(3 * SB));
    wbar<19>();
    MX(1, 7);
    dma_w((unsigned)(3 * SB));
    dma_x(0u);
    wbar<16>();
    MX(1, 8);
    asm volatile("" : "+v"(a), "+v"(b), "+v"(e));
    float s, q;
    {
      float t[4] = {e[0], e[1], e[2], e[3]}, s2, q2;
      asm volatile(
          "v_add_f32_dpp %0, %0, %0 row_ror:8 row_mask:0xf bank_mask:0xf\n\tv_add_f32_dpp %1, %1, %1 row_ror:8 row_mask:0xf bank_mask:0xf\n\t"
          "v_add_f32_dpp %2, %2, %2 row_ror:8 row_mask:0xf bank_mask:0xf\n\tv_add_f32_dpp %3, %3, %3 row_ror:8 row_mask:0xf bank_mask:0xf\n\ts_nop 1"
          : "+v"(t[0]), "+v"(t[1]), "+v"(t[2]), "+v"(t[3]));
      red4(t, s2, q2);
      red8(a, b, s, q);
      s = lane < 32 ? s : s2;
      q = lane < 32 ? q : q2;
      asm volatile("" : "+v"(s), "+v"(q));
    }
    wbar<8>();
    MX(1, 9);
    {
      asm volatile("" : "+v"(s), "+v"(q));
      float mb, rinv;
      ln_finish(s, q, 1.0f / (float)D_, mb, rinv);
      asm volatile("ds_write_b64 %0, %1" ::"v"(sw), "v"(f32x2{-mb, rinv}) : "memory");
    }
    wbar<0>();
    MK(30);
  }

  template <int I>
  DEVI void p5_step(const char* wp) {
    constexpr int T = 2 * I + 1;
    wbar<(T == 1 ? 63 : (T <= 11 ? 3 : 0))>();
    if constexpr (T <= 9) {
      const unsigned wo = (unsigned)(12288 + (wl & 1) * 3072), so = (unsigned)(P5_W + ((T + 2 + (wl >> 1)) & 7) * 18432);
      dma_seq<3>(wp + (T + 7) * 18432, lane16 + wo + (unsigned)((wl >> 1) * 18432), lds0 + so + wo);
      pf_put<0, 12 * (T - 1), 12>((unsigned)(P5_W + ((T + 2) % 8) * 18432));
      pf_put<0, 12 * T, 12>((unsigned)(P5_W + ((T + 3) % 8) * 18432));
    }
  }
  template <int... S>
  DEVI void p5_steps(const char* wp, std::integer_sequence<int, S...>) {
    (p5_step<S>(wp), ...);
  }

  template <int T0, int T1>
  DEVI void w5_load(const char* w5p) {
    const unsigned o = lane16 + (unsigned)(wl * 1024);
#pragma unroll
    for (int t = T0; t < T1; ++t)
      asm volatile("global_load_dwordx4 %0, %1, %2 sc1" : "=v"(pf[t]) : "v"(o + (unsigned)((t / 3 + 8) * 18432 + (t % 3) * 4096)), "s"(w5p) : "memory");
  }
  DEVI void p35(char* smem, char* ws, const u16* w_sp, int layer, int bid, int mi, int ni, int rb, int ct, int hp MK_ARG) {
    using C = K3C;
    constexpr int NS = C::NS, SB = C::SB, NDX = 4, NDW = 5, VPS = NDX + NDW;
    const char* wip = uni_ptr(ws + OFF_PI + (size_t)ni * (18 * C::WB));
    if (layer) {
      pf_put<-48, 36, 18>((unsigned)(2 * SB + C::XB));
      pf_put<-48, 54, 18>((unsigned)(3 * SB + C::XB));
    } else {
      dma_run<18>(wip + 2 * C::WB, (unsigned)(2 * SB + C::XB));
      dma_run<18>(wip + 3 * C::WB, (unsigned)(3 * SB + C::XB));
    }
    dma_run<18>(wip + C::WB, (unsigned)(SB + C::XB));
    const char* w5p = uni_ptr(ws + OFF_PW + (size_t)ct * (36 * 9216));
    const int w0 = wl < 2 ? wl * 5120 : wl * 4096 + 2048;
    const unsigned m0x = lds0 + (unsigned)(wl * 4096), m0w = lds0 + (unsigned)(C::XB + w0);
    const unsigned wo = lane16 + (unsigned)w0;
    const unsigned lw = lds0 + (unsigned)(LW_OFF + layer * 4), me = 1u << (8 * wl);
    const unsigned wo4 = wo + 4096u;
    auto dma_w = [&](const char* wp, unsigned so) {
      dma_seq<4>(wp, wo, m0w + so);
      if (wl < 2) dma16_sc1(wp, wo4, m0w + so + 4096u);
    };
    {
      const char* xp = uni_ptr(ws + OFF_X1 + (size_t)layer * 2 * SZ_X + (size_t)(mi * 2 * 72 + wl * 2) * 1024 - 1024);
      const unsigned xo0 = lane16 + 1024u, xo1 = lane16 + 72u * 1024u, xo3 = lane16 + 71u * 1024u;
      auto dma_x = [&](int k, unsigned so) { dma4<4, true>(xp + k * 8192, xo0, xo1, lane16, xo3, m0x + so); };
      MK(10);
      bar();
      dma_x(0, 0u);
      dma_x(1, (unsigned)SB);
      wait_vm<4>(); MX(3, 2); wbar<4>(); MX(2, 2);
      MK(25);
      {
        dma_x(2, (unsigned)(2 * SB));
        dma_x(3, (unsigned)(3 * SB));
        dma_x(4, (unsigned)(4 * SB));
        wbar<12>(); MX(2, 3);
        dma_x(5, 0u);
        wait_vm<12>();
      }
      bar(); MX(2, 4);
      dma_w(wip + 4 * C::WB, 0u);
      dma_x(6, (unsigned)SB);
      wait_w<8 + VPS>();
#pragma unroll 1
      for (int t = 1; t < 3; ++t) {
        bar(); MX(2, 4 + t);
        dma_w(wip + (t + 4) * C::WB, (unsigned)(t * SB));
        dma_x(t + 6, (unsigned)((t + 1) * SB));
        wait_w<NDX + VPS>();
      }
      bar(); MX(2, 7);
      dma_w(wip + 7 * C::WB, (unsigned)(3 * SB));
      wait_w<VPS>();
      bar(); MX(2, 8);
      dma_w(wip + 8 * C::WB, 0u);
      wait_w<NDW>();
      bar(); MX(2, 9);
      const char* wp = wip + 9 * C::WB;
      dma_w(wp + b_k(0) * C::WB, (unsigned)SB);
      if (layer == 0) {
        const char* sp = uni_ptr((const char*)w_sp + (size_t)(bid * 4 + wl) * 8192);
        const unsigned so = (unsigned)((lane >> 5) * 4096 + (lane & 31) * 16);
#pragma unroll
        for (int i = 0; i < WSP_V; ++i) dma16(sp + i * 512, so, lds0 + (unsigned)K3_DM_OFF);
        dma16(sp + (bid >> 4) * 256, so & ~0x100u, lds0 + (unsigned)K3_DM_OFF);
      }
      w5_load<0, 4>(w5p);
      wait_w<NDW + 4>();
      MX(3, 10); bar(); MX(2, 10);
      dma_w(wp + b_k(1) * C::WB, (unsigned)(2 * SB));
      w5_load<4, 8>(w5p);
      MK(26);
      lds_spin4(lw + LW_AD * 4, 1u);
      dma_w(wp + b_k(2) * C::WB, (unsigned)(3 * SB));
      dma_w(wp + b_k(3) * C::WB, 0u);
      w5_load<8, W5A>(w5p);
      wait_vm<W5A - 8>();
      lds_inc(lw + LW_L * 4, 4u * me);
      w5_load<W5A, 30>(w5p);
      MK(11);
    }
    {
      unsigned xoff[NDX];
#pragma unroll
      for (int i = 0; i < NDX; ++i) {
        const int c = wl * 256 + i * 64 + lane;
        const int row = c >> 3, cc = c & 7;
        xoff[i] = (unsigned)((mi * 128 + row) * S_ * 2 - i * 1024) + (unsigned)((cc ^ ((row >> 1) & 7)) * 16);
      }
      auto dma_x = [&](const char* xp, unsigned so) { dma4<4, false>(xp, xoff[0], xoff[1], xoff[2], xoff[3], m0x + so); };
      const char* xp = uni_ptr((const char*)w_sp);
      const char* wp = uni_ptr(ws + OFF_Z2 + (size_t)layer * SZ_Z + (size_t)ni * S_ * 288);
      static_for<4, 7>([&](auto T_) {
        constexpr int t = T_;
        lds_spin4<2>(lw + LW_S * 4, (unsigned)(t - 3));
        dma_w(wip + (9 + b_k(t)) * C::WB, (unsigned)(((t + 1) % 4) * SB));
        if constexpr (t > 4) {
          dma_x(xp + mi * 256 + (t - 5) * 128, (unsigned)((t - 3) * SB));
          wait_w<NDW + (t - 4) * NDX>();
          lds_inc(lw + LW_L * 4, me);
        }
      });
      wait_vm<NDX>();
      lds_inc(lw + LW_L * 4, me);
      wait_vm<0>();
      lds_inc(lw + LW_L * 4, me);
      pf_pin<0, 30>();
      MK(13);
      lds_spin4<2>(lw + LW_AD * 4, 5u);
      MK(24);
      int left = 2 * mi;
      auto next = [&]() {
        const int d = --left == 0 ? 3 : 1;
        xp += d * 128;
        wp += d * C::WB;
      };
      if (mi == 0) {
        xp += 256;
        wp += 2 * C::WB;
      }
      if (wl == 0) {
        cnt_wait(ws, CN_Z + layer, mi * 16 + ni, (unsigned)ZQ);
        lds_wr32(lw + LW_GO * 4, 1u);
      }
      lds_spin4<2>(lw + LW_AD * 4, 6u);
      dma_x(xp, 0u);
      dma_x(xp + 128, (unsigned)SB);
      spin_ge<2>(lw + LW_GO * 4, 1u);
      lds_spin4<2>(lw + LW_S * 4, 8u);
      dma_w(wp, 0u);
      lds_spin4<2>(lw + LW_S * 4, 9u);
      dma_w(wp + C::WB, (unsigned)SB);
      next();
      next();
      unsigned slot = (unsigned)(2 * SB);
      auto cissue = [&]() {
        dma_x(xp, slot);
        dma_w(wp, slot);
        next();
        slot = (slot == (unsigned)((NS - 1) * SB)) ? 0u : slot + (unsigned)SB;
      };
      auto cstep = [&]() {
        bar();
        cissue();
      };
      wait_w<NDW>();
      MK(15);
      int j = 2;
      if (layer == 0) {
        const char* sp = uni_ptr((const char*)w_sp + (size_t)(bid * 4 + wl) * 8192 + WSP_V * 512);
        unsigned d = 0u;
#pragma unroll 1
        for (; j < 2 + 32 - 4 * WSP_V; ++j) {
          cstep();
          asm volatile("s_load_dword %0, %1, 0\n\ts_load_dword %0, %1, 64\n\ts_load_dword %0, %1, 0x1000\n\ts_load_dword %0, %1, 0x1040"
                       : "+s"(d)
                       : "s"(sp));
          sp += 128;
          wait_w<VPS>();
        }
#pragma unroll 1
        for (int e = j + 4; j < e; ++j) {
          cstep();
          asm volatile("" : "+s"(d));
          wait_w<VPS>();
        }
        asm volatile("s_waitcnt lgkmcnt(0)" : "+s"(d));
      }
      place();
#pragma unroll 1
      for (; j <= 29; ++j) {
        cstep();
        wait_w<VPS>();
      }
      bar();
    }
    dma_run<33>(w5p, (unsigned)(P5_W + 54 * 1024));
    dma_run<23>(w5p + 67 * 1024, (unsigned)(P5_W + 121 * 1024));
    if (wl == 3) dma16_sc1(w5p + 143 * 1024, lane16, lds0 + (unsigned)(P5_W + 53 * 1024));
    wait_vm<14>();
    bar();
    dma_run<34>(w5p + 33 * 1024, (unsigned)(P5_W + 87 * 1024));
    wait_vm<0>();
    bar();
    if (wl) {
      const unsigned f = (unsigned)(wl - 1) * 18432u;
      dma_seq<17>(w5p + 90 * 1024, lane16 + f, lds0 + (unsigned)P5_W + f);
      if (wl < 3) dma16_sc1(w5p + 107 * 1024, lane16 + f, lds0 + (unsigned)(P5_W + 17 * 1024) + f);
    } else {
      cnt_wait(ws, CN_H + layer, rb * 8 + ct, 8u);
    }
    MK(20);
    bar();
    p5_steps(w5p, std::make_integer_sequence<int, 7>{});
    if (layer == 0) {
      if (bid < 24 && wl == 0 && lane < 8) pk_st16(ws + OFF_HELLO + bid * 128 + lane16, u32x4{0u, 0u, 0u, 0u});
    }
  }
};

__global__ __launch_bounds__(768, 3) void k_main(const u16* __restrict__ x0, const u16* __restrict__ wqkv, const u16* __restrict__ wo,
                                                 const u16* __restrict__ w_in, const u16* __restrict__ w_sp,
                                                 const u16* __restrict__ b_sp, const u16* __restrict__ w_out,
                                                 u16* __restrict__ out, char* ws) {
  extern __shared__ __attribute__((aligned(16))) char smem[];
  const char* code0 = code_here();
  (void)code0;
  const unsigned lds0 = (unsigned)(size_t)smem;
  int wave = __builtin_amdgcn_readfirstlane((int)threadIdx.x >> 6);
  int bid = blockIdx.x;
#define FRESH() asm volatile("" : "+s"(bid), "+s"(ws), "+s"(wave))
#define XCD (bid & 7)
#define IDX (bid >> 3)
#define MI ((XCD >> 2) * 8 + (IDX >> 2))
#define NI ((XCD & 1) * 8 + (XCD & 2) * 2 + (IDX & 3))
#define RB (XCD * 4 + (IDX >> 3))
#define CT (IDX & 7)
#define RT ((XCD >> 1) * 4 + (IDX >> 3))
#define HP ((XCD & 1) * 8 + (IDX & 7))
  MK(0);
  if (wave == 2) {
    constexpr int PER = (2 * NL_MAIN + 31) / 32;
    const int lane = lane_id();
    static_assert(PER <= 64, "one load");
    int li = IDX * PER + (lane < PER ? lane : PER - 1);
    if (li > 2 * NL_MAIN - 1) li = 2 * NL_MAIN - 1;
    touch64(code0, (unsigned)li * 64u, lds0 + (unsigned)(K1R_DUMMY + 768));
  }
  if (wave >= 3 && wave < 8) {
    constexpr int ST = 64;
    const int k = (7 - wave) * 64 + lane_id();
    const bool isx = k < 9216 / ST;
    int o = isx ? bid * 9216 + k * ST : bid * 7776 + (k - 9216 / ST) * ST;
    if (!isx && k - 9216 / ST > 7775 / ST) o = bid * 7776;
    const char* p = (isx ? (const char*)x0 : (const char*)wqkv) + o;
    asm volatile("s_mov_b32 m0, %1\n\ts_nop 0\n\tglobal_load_lds_dword %0, off" ::"v"(p), "s"(lds0 + (unsigned)K1R_DUMMY) : "memory");
  }

  if (__builtin_expect(wave >= 8, 1)) {
    __builtin_amdgcn_s_setprio(3);
    {
      const int lane = lane_id();
      RawLd rl;
      const bool cross = (((HP * 72) & ~15) & 127) > 48;
      const int c16 = IDX >> 1;
      const int cj = (int)((0x65743210u >> (4 * XCD)) & 7u) * 16 + c16;
      const int job = cross ? (cj < 88 ? 128 + cj : 999) : XCD * 16 + c16;
      const int pj = cross ? cj - 88 : ((XCD & 5) == 0 && (c16 & 3) == (c16 >> 2) ? 40 + XCD * 2 + (c16 >> 2) : -1);
      rl.front(x0, wqkv, wo, ws, bid, lds0, wave - 8, lane, RT, HP, pj MF_PASS);
      if (job < 216) k1_raw_loader<true>(rl, w_in, w_out, ws, rl.tag, bid, lds0, wave - 8, lane, RT, HP, CT, job, pj MF_PASS);
      else k1_raw_loader<false>(rl, w_in, w_out, ws, rl.tag, bid, lds0, wave - 8, lane, RT, HP, CT, job, pj MF_PASS);
    }
    Loader ld;
    ld.lds0 = lds0;
#pragma unroll 1
    for (int layer = 0; layer < 2; ++layer) {
      FRESH();
      ld.wl = wave - 8;
      ld.lane = lane_id();
      ld.lane16 = (unsigned)ld.lane * 16u;
      const char* wip = uni_ptr(ws + OFF_PI + (size_t)NI * (18 * K3C::WB));
      if (layer) {
        ld.p1(smem, ws, bid, RT, HP MK_PASS);
        ld.pf_load<0, 21, 0, 81>(uni_ptr(ws + OFF_PO + (size_t)CT * (18 * 4608)));
        ld.pf_load<21, 9, 36, 72>(wip);
        wait_vm<9>();
        ld.pf_pin<0, 21>();
        lds_spin(lds0 + (unsigned)(LW_OFF + (LW_T + layer) * 4), 8u);
        __builtin_amdgcn_s_sleep(4);
        ld.pf_put<0, 0, 40>((unsigned)P2_W);
        wait_vm<0>();
        ld.pf_pin<21, 9>();
        bar();
        ld.pf_put<0, 40, 41>((unsigned)(P2_W + 40 * 1024));
      } else {
        ld.pf_undef<21, 9>();
        MK(3);
        MK(4);
        bar();
        ld.dma_run<81>(uni_ptr(ws + OFF_PO + (size_t)CT * (18 * 4608)), (unsigned)P2_W);
      }
      if (layer == 0 && ld.wl == 0) cnt_wait(ws, CN_O + layer, RB * 8 + CT, 16u);
      MK(6);
      asm volatile("s_waitcnt vmcnt(0) lgkmcnt(0)" ::: "memory");
      bar();
      lds_spin(lds0 + (unsigned)(LW_OFF + (LW_T + layer) * 4), 12u);
      if (layer == 0) {
        if (ld.wl == 0) {
          cnt_wait(ws, CN_PK, 8 + NI, 36u);
          lds_wr32(lds0 + (unsigned)(LW_OFF + LW_HELLO * 4), 2u);
        } else {
          lds_spin(lds0 + (unsigned)(LW_OFF + LW_HELLO * 4), 2u);
        }
        MK(7);
      }
      static_assert(2 * K3C::SB <= P2_W, "free during the first residual GEMM");
      ld.dma_run<18>(wip, (unsigned)K3C::XB);
      MX(6, 24);
      if (layer == 0 && ld.wl == 0) cnt_wait(ws, CN_PK, 24 + CT, 72u);
      wait_vm<0>();
      ld.pf_pin<21, 9>();
      MX(6, 25);
      bar();
      FRESH();
      ld.wl = wave - 8;
      ld.p35(smem, ws, w_sp, layer, bid, MI, NI, RB, CT, HP MK_PASS);
    }
  } else {
    MF(12);
    math_start(smem, b_sp, wqkv, ws, bid, wave, lane_id());
    MF(13);
#pragma unroll 1
    for (int layer = 0; layer < 2; ++layer) {
      FRESH();
      {
        const int lane = lane_id(), r0 = RT * 128;
        float* csq = reinterpret_cast<float*>(ws + OFF_CSQ + bid * 512);
        Attn at;
        at.setup(lds0 + (unsigned)(layer ? K1_T_OFF : K1R_T_OFF), ws + OFF_O + (size_t)layer * SZ_X, r0, HP, wave, lane);
        if (layer) {
          if ((wave & 2) == 0) k1_gemm<2, 4>(smem, ws, RT, HP, (wave & 1) * 2 + (wave & 4), 0, wave >= 4, lane MK_PASS);
          else if (wave < 4) k1_gemm<3, 3>(smem, ws, RT, HP, (wave & 1) * 3, 4, false, lane MK_PASS);
          else if (wave == 6) k1_gemm<2, 3>(smem, ws, RT, HP, 6, 4, true, lane MK_PASS);
          else k1_gemm<1, 7>(smem, ws, RT, HP, 8, 0, true, lane MK_PASS);
        } else {
          if (wave == 0) k1_gemm_raw<3, false>(smem, HP, 0, lane MF_PASS);
          else if (wave < 4) k1_gemm_raw<2, false>(smem, HP, 1 + 2 * wave, lane MF_PASS);
          else if (wave < 7) k1_gemm_raw<2, true>(smem, HP, 2 * (wave - 4), lane MF_PASS);
          else k1_gemm_raw<3, true>(smem, HP, 6, lane MF_PASS);
        }
        msync(lds0 + (unsigned)(LW_OFF + (LW_T + layer) * 4));
        MK(3);
        at.run(r0, HP, wave, lane);
        if (layer == 0) {
          if (wave == 0 && lane < 32)
            asm volatile("global_store_dwordx4 %0, %1, %2 sc1\n\ts_nop 1" ::"v"((unsigned)lane * 16u),
                         "v"(*reinterpret_cast<const u32x4*>(smem + K1R_CS_OFF + lane * 16)), "s"(uni_ptr((const char*)csq))
                         : "memory");
          lds_spin(lds0 + (unsigned)(LW_OFF + LW_HELLO * 4), 1u);
        }
        MK(4);
        announce(lds0 + (unsigned)(LW_OFF + (LW_O + layer * 22 + (wave >> 2)) * 4), 4u, ws, CN_O + layer, (RT * 2 + (wave >> 2)) * 8 + lane,
                 lane < 8);
        MK(5);
        bar();
      }
      FRESH();
      {
        if (wave < 4) {
          const int lane = lane_id();
          u32x2 xv[5];
          ResIn ri;
          ri.setup(layer ? ws + OFF_X1 + SZ_X : (const char*)x0, layer == 0, RB, CT, wave, lane);
          ri.load(xv);
          ResOut ro;
          ro.setup(ws + OFF_X1 + (size_t)layer * 2 * SZ_X, ws + OFF_XS + (size_t)layer * 2 * SZ_XS, out, false, RB, CT, wave, lane);
          f32x4 acc[2][5];
          p2_gemm(acc, xv, lds0, uni_ptr(ws + OFF_O + (size_t)layer * SZ_X + (size_t)RB * (72 * 1024)), layer, wave, lane MK_PASS);
          MK(7);
          ro.run(acc[0], false);
          MK(8);
          announce(lds0 + (unsigned)(LW_OFF + (LW_X1 + layer) * 4), 4u, ws, CN_X1 + layer, (RB >> 1) * 16 + lane, lane < 16);
          MK(9);
          bar();
          FRESH();
          if (wave == 3) cnt_wait(ws, CN_X1 + layer, MI * 16 + NI, 16u);
          bar();
          MK(10);
          p3_g0(smem, ws, layer, MI, NI, wave, lane_id() MK_PASS);
        } else {
          if (layer && wave == 5) cnt_wait(ws, CN_O + layer, RB * 8 + CT, 16u);
          bar();
          k3_const(smem, ws, b_sp, layer, bid, MI, NI, wave & 3);
          MX(6, 26);
          bar();
          MK(10);
          FRESH();
          p3_g1(smem, ws, layer, MI, NI, wave & 3, lane_id() MK_PASS);
        }
      }
      FRESH();
      {
        const int lane = lane_id();
        if (wave < 4) {
          u32x2 xv[5];
          ResIn ri;
          ri.setup(ws + OFF_X1 + (size_t)layer * 2 * SZ_X, false, RB, CT, wave, lane);
          f32x4 acc[2][5];
          ResOut ro;
          ro.setup(ws + OFF_X1 + SZ_X, ws + OFF_XS + SZ_XS, out, layer != 0, RB, CT, wave, lane);
          ResX<36, 33, 8, P5_W, P5_XW, P5_X0, 3, P5_BAR> gm(acc);
          gm.setup(lds0, uni_ptr(ws + OFF_H + (size_t)layer * SZ_Z + (size_t)RB * (288 * 1024)), wave, lane, (unsigned)P5_RED, layer + 1u);
          announce(lds0 + (unsigned)(LW_OFF + (LW_H + layer * 2 + (wave >> 1)) * 4), 2u, ws, ((NI >= 8) ? CN_H2 : CN_H) + layer,
                   (MI * 2 + (wave >> 1)) * 8 + lane, lane < 8);
          MK(19);
          bar();
          ri.load(xv);
          gm.prow();
          place();
          bar();
          MK(20);
          gm.prox<0>(std::make_integer_sequence<int, P5_X0>{});
          gm.init<2 * P5_X0>(xv);
          __builtin_amdgcn_sched_barrier(0);
          gm.run();
          MK(21);
          ro.run(acc[0], layer != 0);
          MK(22);
          if (layer == 0) {
            announce(lds0 + (unsigned)(LW_OFF + LW_X2 * 4), 4u, ws, CN_X2, (RB >> 1) * 16 + lane,
                     lane < 16 || (lane < 32 && (RB & 1) && RB < 31));
            MK(23);
          }
        } else {
          bar();
          bar();
#pragma unroll
          for (int i = 0; i < 7; ++i) {
            if (i == 1 && wave == 5) {
              unsigned f;
              const char* cp = uni_ptr(ws + OFF_CNT + (size_t)((CN_H2 + layer) * 256 + RB * 8 + CT) * 128);
              asm volatile("global_load_dword %0, %1, %2 sc1" : "=v"(f) : "v"(vconst(0u)), "s"(cp) : "memory");
              bar();
              asm volatile("s_waitcnt vmcnt(0)" : "+v"(f) : : "memory");
              if (__builtin_amdgcn_readfirstlane(f) != 7u) cnt_wait(ws, CN_H2 + layer, RB * 8 + CT, 8u);
              continue;
            }
            asm volatile("s_waitcnt lgkmcnt(0)" ::: "memory");
            bar();
          }
          MK(22);
        }
        if (layer == 0 && wave < 4) bar();
      }
    }
  }
}

}

extern "C" void kernel_launch(void* const* d_in, const int* in_sizes, int n_in, void* d_out, int out_size,
                              void* d_ws, size_t ws_size, hipStream_t stream) {
  const u16* x0 = (const u16*)d_in[0];
  const u16* wqkv = (const u16*)d_in[1];
  const u16* wo = (const u16*)d_in[2];
  const u16* w_in = (const u16*)d_in[4];
  const u16* w_sp = (const u16*)d_in[5];
  const u16* b_sp = (const u16*)d_in[6];
  const u16* w_out = (const u16*)d_in[7];
  char* ws = (char*)d_ws;
  (void)hipFuncSetAttribute((const void*)k_main, hipFuncAttributeMaxDynamicSharedMemorySize, LDS_ALL);
  k_main<<<256, 768, LDS_ALL, stream>>>(x0, wqkv, wo, w_in, w_sp, b_sp, w_out, (u16*)d_out, ws);
}
```

```cpp
#include <hip/hip_runtime.h>
#include <hip/hip_bf16.h>
#include <stdint.h>

#define DEVI __device__ __forceinline__

typedef unsigned short u16;
typedef __attribute__((ext_vector_type(8))) short s16x8;
typedef __attribute__((ext_vector_type(4))) short s16x4;
typedef __attribute__((ext_vector_type(4))) float f32x4;
typedef __attribute__((ext_vector_type(2))) float f32x2;
typedef __attribute__((ext_vector_type(2))) __bf16 bf16x2;
typedef __attribute__((ext_vector_type(2))) unsigned u32x2;
typedef __attribute__((ext_vector_type(4))) unsigned u32x4;

#define MK(I) ((void)0)
#define MX(S, I) ((void)0)
#define MF(I) ((void)0)
#define MF_ARG
#define MF_PASS
#define MK_ARG
#define MK_PASS

#define SPIN_MEM (1u << 22)
#define SAFE_OUT(L)
#define SAFE_AND
#define NOP_(n) "=s"(n)

namespace {

constexpr int S_ = 2048, D_ = 576, F_ = 4608, FH_ = 2304;
constexpr int NQKV = 3 * D_;

constexpr size_t SZ_X = (size_t)S_ * D_ * 2;
constexpr size_t SZ_Z = (size_t)S_ * FH_ * 2;
constexpr size_t OFF_X1 = 0;
constexpr size_t OFF_O = OFF_X1 + 3 * SZ_X;
constexpr size_t OFF_Z2 = OFF_O + 2 * SZ_X;
constexpr size_t OFF_H = OFF_Z2 + 2 * SZ_Z;
constexpr size_t SZ_XS = (size_t)S_ * 8 * 8;
constexpr size_t OFF_XS = OFF_H + 2 * SZ_Z;
constexpr size_t OFF_CSQ = OFF_XS + 3 * SZ_XS;
constexpr size_t OFF_ZP = OFF_CSQ + 256 * 512;
constexpr size_t SZ_ZP = (size_t)16 * 16 * 1024;
constexpr size_t OFF_PQ = OFF_ZP + 2 * SZ_ZP;
constexpr size_t OFF_PO = OFF_PQ + (size_t)16 * 9 * 64 * 112 * 2;
constexpr size_t OFF_PI = OFF_PO + (size_t)D_ * D_ * 2;
constexpr size_t OFF_PW = OFF_PI + (size_t)32 * 9 * 64 * 144 * 2;
constexpr size_t OFF_CSP = OFF_PW + (size_t)FH_ * D_ * 2;
constexpr size_t OFF_CNT = OFF_CSP + (size_t)18 * F_ * 4;
constexpr int CN_X1 = 0, CN_H = 2, CN_X2 = 4, CN_O = 5, CN_Z = 7, CN_PK = 9, CN_H2 = 10, CN_Z2 = 12, NCNT = 14;
constexpr size_t OFF_TAG = OFF_CNT + (size_t)NCNT * 256 * 128;
constexpr size_t OFF_HELLO = OFF_TAG + 128;
constexpr size_t OFF_POT = OFF_HELLO + 2048;
constexpr size_t OFF_CSK = OFF_POT + 1024;
constexpr size_t WS_END = OFF_CSK + 256 * 2048;

DEVI float bf_lo(unsigned u) { return __uint_as_float(u << 16); }
DEVI float bf_hi(unsigned u) { return __uint_as_float(u & 0xffff0000u); }
DEVI unsigned pack2(float a, float b) {
  f32x2 v = {a, b};
  bf16x2 r = __builtin_convertvector(v, bf16x2);
  return __builtin_bit_cast(unsigned, r);
}
DEVI float rbf(float a) {
  return bf_lo(pack2(a, 0.f));
}

DEVI float sum_g(float x) {
  const unsigned u = __float_as_uint(x);
  const auto p = __builtin_amdgcn_permlane32_swap(u, u, false, false);
  const unsigned v = __float_as_uint(__uint_as_float(p[0]) + __uint_as_float(p[1]));
  const auto q = __builtin_amdgcn_permlane16_swap(v, v, false, false);
  return __uint_as_float(q[0]) + __uint_as_float(q[1]);
}

DEVI unsigned vconst(unsigned c) {
  unsigned r;
  asm volatile("v_mov_b32 %0, %1" : "=v"(r) : "s"(c));
  return r;
}
DEVI void place() {
  __builtin_amdgcn_sched_barrier(0);
  asm volatile(".p2align 6");
  __builtin_amdgcn_sched_barrier(0);
}
DEVI const char* uni_ptr(const char* p) {
  unsigned long long v = (unsigned long long)p;
  unsigned lo = __builtin_amdgcn_readfirstlane((unsigned)v), hi = __builtin_amdgcn_readfirstlane((unsigned)(v >> 32));
  asm("s_nop 4" : "+s"(lo), "+s"(hi));
  return (const char*)(((unsigned long long)hi << 32) | lo);
}

DEVI void dma16(const char* sbase, unsigned voff, unsigned m0val) {
  asm volatile("s_mov_b32 m0, %2\n\ts_nop 0\n\tglobal_load_lds_dwordx4 %0, %1"
               :
               : "v"(voff), "s"(sbase), "s"(m0val)
               : "memory");
}

DEVI void ln_finish(float s, float q, float invn, float& mb, float& rinv) {
  float mean = s * invn;
  asm("" : "+v"(mean));
  float var = fmaxf(__builtin_fmaf(-mean, mean, q * invn), 0.f);
  mb = rbf(mean);
  float v = rbf(var);
  float t = rbf(v + 1e-5f);
  float sd = rbf(__builtin_amdgcn_sqrtf(t));
  rinv = __builtin_amdgcn_rcpf(sd);
}

DEVI unsigned pack2v(f32x2 v) { return pack2(v[0], v[1]); }

template <int BM_, int BN_>
struct GemmCfg {
  static constexpr int BM = BM_, BN = BN_, NS = 4;
  static constexpr int P = BN * 2;
  static constexpr int XB = BM * 128, WB = 64 * P, SB = XB + WB;
  static constexpr int LDS_BYTES = NS * SB;
};

DEVI int swap23(int r) { return (r & ~12) | ((r & 4) << 1) | ((r & 8) >> 1); }

DEVI void lds_wr32(unsigned addr, unsigned v) { asm volatile("ds_write_b32 %0, %1" ::"v"(addr), "v"(v) : "memory"); }

template <class C, int MT, int NT, int D, int R, bool XCM, int WT, bool SQ = false, unsigned long long WS = 0>
struct GemmMath {
  static constexpr int WO = WT ? 1 : 2;
  static constexpr int NS = C::NS, SB = C::SB, P = C::P, NU = 2 * NT;
  static constexpr int XPU = MT <= 2 ? MT : MT - 2;
  static constexpr int XU = (MT + XPU - 1) / XPU;
  static_assert(NU % R == 0 && R > D && D <= NT && XU <= D, "shape");
  s16x4 wlo[R], whi[R];
  s16x8 wf[R];
  s16x8 xf[2][MT];
  unsigned xrd[2], wrd, xa[2], wa, so;
  f32x4 (&acc)[MT][NT];
  f32x4 accq[SQ ? MT : 1];
  unsigned long long wtab = WS;
  DEVI GemmMath(f32x4 (&a)[MT][NT]) : acc(a) {}

  template <int PU>
  DEVI void wread_lo() {
    constexpr int off = (PU / NT) * 32 * P + (PU % NT) * (WT == 2 ? 1024 : 32);
    if constexpr (WT) asm volatile("ds_read_b128 %0, %1 offset:%2" : "=v"(wf[PU % R]) : "v"(wa), "n"(off));
    else asm volatile("ds_read_b64_tr_b16 %0, %1 offset:%2" : "=v"(wlo[PU % R]) : "v"(wa), "n"(off));
  }
  template <int PU>
  DEVI void wread_hi() {
    if constexpr (!WT)
      asm volatile("ds_read_b64_tr_b16 %0, %1 offset:%2" : "=v"(whi[PU % R]) : "v"(wa), "n"((PU / NT) * 32 * P + (PU % NT) * 32 + 8 * P));
  }
  template <int H, int M>
  DEVI void xread1() {
    asm volatile("ds_read_b128 %0, %1 offset:%2" : "=v"(xf[H][M]) : "v"(xa[H]), "n"(M * (XCM ? 256 : 2048)));
  }
  static constexpr int xops(int v) {
    const int k = (v + D) % NT;
    if (k >= XU) return 0;
    const int n = MT - k * XPU;
    return n < XPU ? n : XPU;
  }
  static constexpr int ops(int v, bool last) { return (last && v + D >= NU) ? 0 : WO + xops(v); }
  static constexpr int newer(int U, bool last) {
    int n = 0;
    int v0 = U - D;
    if (U % NT == 0) v0 += XU - 1;
    else n += v0 >= 0 ? xops(v0) : xops(v0 + NU);
    for (int v = v0 + 1; v < U; ++v) n += v >= 0 ? ops(v, last) : ops(v + NU, false);
    return n;
  }
  template <int U, int M_>
  DEVI void mf() {
    constexpr int M = ((U % NT) & 1) ? MT - 1 - M_ : M_;
    s16x8 w;
    if constexpr (WT) w = wf[U % R];
    else w = __builtin_shufflevector(wlo[U % R], whi[U % R], 0, 1, 2, 3, 4, 5, 6, 7);
    acc[M][U % NT] = __builtin_amdgcn_mfma_f32_16x16x32_bf16(w, xf[U / NT][M], acc[M][U % NT], 0, 0, 0);
    __builtin_amdgcn_sched_barrier(0);
  }
  DEVI void pin() {
#pragma unroll
    for (int m = 0; m < MT; ++m)
#pragma unroll
      for (int n = 0; n < NT; ++n) asm volatile("" : "+v"(acc[m][n]));
  }
  template <int U, int I>
  DEVI void dsop() {
    constexpr int PU = (U + D) % NU;
    if constexpr (I == 0) wread_lo<PU>();
    else if constexpr (I == 1) wread_hi<PU>();
    else if constexpr (I - 2 < xops(U)) {
      constexpr int k = (U + D) % NT;
      xread1<((U + D - k) % NU) / NT, k * XPU + I - 2>();
    }
  }
  template <int U, bool LAST, bool BAR, int M>
  DEVI void slot() {
    constexpr bool PF = !(LAST && U + D >= NU);
    mf<U, M>();
    if constexpr (M == 0 && PF && U + D == NU) {
      if constexpr (BAR) __builtin_amdgcn_s_barrier();
      so = (so == (unsigned)((NS - 1) * SB)) ? 0u : so + (unsigned)SB;
      wa = wrd + (WS ? ((unsigned)wtab & 63u) << 11 : so);
      wtab >>= 6;
      xa[0] = xrd[0] + so;
      xa[1] = xrd[1] + so;
      __builtin_amdgcn_sched_barrier(0);
    }
    if constexpr (PF) {
      dsop<U, M>();
      if constexpr (M == MT - 1) {
        if constexpr (MT <= 1) dsop<U, 1>();
        if constexpr (MT <= 2) {
          dsop<U, 2>();
          dsop<U, 3>();
        }
      }
      __builtin_amdgcn_sched_barrier(0);
    }
  }
  template <int U, bool LAST, bool BAR, int... M>
  DEVI void unit(std::integer_sequence<int, M...>) {
    static_assert(newer(U, LAST) <= 15, "lgkmcnt range");
    asm volatile("s_waitcnt lgkmcnt(%0)" ::"n"(newer(U, LAST)));
    __builtin_amdgcn_sched_barrier(0);
    (slot<U, LAST, BAR, M>(), ...);
    if constexpr (SQ && (U % NT) < MT) {
      constexpr int m = U % NT;
      accq[m] = __builtin_amdgcn_mfma_f32_16x16x32_bf16(xf[U / NT][m], xf[U / NT][m], accq[m], 0, 0, 0);
      __builtin_amdgcn_sched_barrier(0);
    }
    __builtin_amdgcn_sched_barrier(0);
  }
  template <bool LAST, bool BAR, int... U>
  DEVI void stage(std::integer_sequence<int, U...>) {
    (unit<U, LAST, BAR>(std::make_integer_sequence<int, MT>{}), ...);
  }
  template <int... U>
  DEVI void prow(std::integer_sequence<int, U...>) {
    ((wread_lo<U>(), wread_hi<U>()), ...);
  }
  template <int... M>
  DEVI void prox(std::integer_sequence<int, M...>) {
    (xread1<0, M>(), ...);
  }
  DEVI void setup(unsigned lds0, int mt0, int nt0, int lane, unsigned so0) {
    const int g = lane >> 4, r = lane & 15, q = (lane >> 2) & 3, p = lane & 3;
#pragma unroll
    for (int h = 0; h < 2; ++h)
      xrd[h] = XCM ? lds0 + (unsigned)((4 * h + g) * (C::BM * 16) + (mt0 * 16 + r) * 16)
                   : lds0 + (unsigned)((mt0 * 16 + r) * 128 + (((4 * h + g) ^ ((r >> 1) & 7)) * 16));
    wrd = WT == 2   ? lds0 + (unsigned)(C::XB + nt0 * 1024 + lane * 16)
          : WT == 1 ? lds0 + (unsigned)(C::XB + (g * 8 + (r & 7)) * P + (r >> 3) * 16 + nt0 * 32)
                    : lds0 + (unsigned)(C::XB + (16 * (g >> 1) + 4 * (g & 1) + q) * P + 8 * p + nt0 * 32);
    so = so0;
    xa[0] = xrd[0] + so;
    xa[1] = xrd[1] + so;
    wa = wrd + so;
  }
  DEVI void first() {
    prox(std::make_integer_sequence<int, MT>{});
    prow(std::make_integer_sequence<int, D>{});
    asm volatile("s_waitcnt lgkmcnt(0)");
  }
  template <int N, bool BAR = true>
  DEVI void mid() {
    if constexpr (N > 1) place();
#pragma unroll 1
    for (int s = 0; s < N; ++s) stage<false, BAR>(std::make_integer_sequence<int, NU>{});
  }
  DEVI void last() {
    stage<true, true>(std::make_integer_sequence<int, NU>{});
    pin();
  }
  template <int KS>
  DEVI void run(unsigned lds0, int mt0, int nt0, int lane, unsigned so0) {
    setup(lds0, mt0, nt0, lane, so0);
    __builtin_amdgcn_s_barrier();
    first();
    mid<KS - 1>();
    last();
  }
};

DEVI const char* code_here() {
  unsigned long long pc;
  asm volatile("s_getpc_b64 %0" : "=s"(pc));
  return (const char*)pc;
}
DEVI int okmap(int p) {
  const int ch = p >> 3, e = p & 7, c = (ch - 8) & 3;
  return ch < 8 ? (2 * ch + (e >> 2)) * 36 + 16 + (e & 3) : ((ch - 8) >> 2) * 36 + c * 8 + (c >> 1) * 4 + e;
}

DEVI void pk_st16(void* p, u32x4 v) {
  asm volatile("global_store_dwordx4 %0, %1, off sc1\n\ts_nop 1" ::"v"(p), "v"(v) : "memory");
}

using K1C = GemmCfg<144, 112>;
constexpr int K1_TP = 240;
constexpr int K1_T_OFF = K1C::SB;
constexpr int K1_ST_OFF = 163840 - 128 - 1152;
constexpr int K1_CS_OFF = 4 * K1C::SB;
constexpr int K1_WX = 4 * K1C::SB + 2048;
constexpr unsigned long long K1_WS = 16ull | 32ull << 6 | 48ull << 12 | 56ull << 18 | 63ull << 24 | 16ull << 30 | 32ull << 36 | 48ull << 42;
static_assert(K1C::SB == 16 * 2048 && K1_WX - K1C::XB == 56 * 2048 && K1C::WB == 7 * 2048 && K1_WX + 2 * K1C::WB <= K1_ST_OFF, "K1 w places");
static_assert(K1_T_OFF + 144 * K1_TP + 64 <= 2 * K1C::SB + K1C::XB, "K1 LDS alias");
using K1R = GemmCfg<144, 144>;
constexpr int K1R_T_OFF = K1R::SB;
constexpr int K1R_CS_OFF = K1R_T_OFF + 144 * K1_TP;
constexpr int K1R_ST_OFF = K1R_CS_OFF + 512;
constexpr int K1R_DUMMY = K1R::LDS_BYTES;
constexpr int K1R_PO = K1R_DUMMY + 1024;
constexpr int K1R_CNT = 163840 - 128 + 28 * 4;
#define SPIN_ASM(TEST)                                                                                                   \
  if constexpr (SL <= 1)                                                                                                 \
    asm volatile(".La%=:\n\tds_read_b32 %0, %3\n\ts_waitcnt lgkmcnt(0)\n\tv_readfirstlane_b32 %2, %0\n\t" TEST SAFE_AND \
                 "s_cbranch_scc1 .La%="                                                                                  \
                 : "=&v"(v), NOP_(c), "=&s"(t)                                                                           \
                 : "v"(addr), "s"(k)                                                                                     \
                 : "memory", "scc");                                                                                     \
  else                                                                                                                   \
    asm volatile("s_branch .Lb%=\n.La%=:\n\ts_sleep %5\n.Lb%=:\n\tds_read_b32 %0, %3\n\ts_waitcnt lgkmcnt(0)\n\t"     \
                 "v_readfirstlane_b32 %2, %0\n\t" TEST SAFE_AND "s_cbranch_scc1 .La%="                                  \
                 : "=&v"(v), NOP_(c), "=&s"(t)                                                                           \
                 : "v"(addr), "s"(k), "n"(SL)                                                                            \
                 : "memory", "scc")
template <int SL>
DEVI void spin_ge(unsigned addr, unsigned k) {
  unsigned v, c = 1u << 18, t;
  SPIN_ASM("s_cmp_lt_u32 %2, %4\n\t");
}
template <int SL>
DEVI void spin_b4(unsigned addr, unsigned n) {
  unsigned v, c = 1u << 18, t;
  const unsigned k = (0x80u - n) * 0x01010101u;
  SPIN_ASM("s_add_u32 %2, %2, %4\n\ts_andn2_b32 %2, 0x80808080, %2\n\t");
}
DEVI void msync(unsigned addr) {
  asm volatile("s_waitcnt lgkmcnt(0)" ::: "memory");
  asm volatile("s_mov_b64 exec, 1\n\tds_add_u32 %0, %1\n\ts_mov_b64 exec, -1" ::"v"(addr), "v"(vconst(1u)) : "memory");
  spin_ge<0>(addr, 8u);
}
static_assert(K1R_PO + 13824 + 256 <= 163840 - 128 && K1R_ST_OFF + 144 * 8 <= 3 * K1R::SB, "K1R LDS");

DEVI void touch64(const char* sbase, unsigned voff, unsigned m0val) {
  asm volatile("s_mov_b32 m0, %2\n\ts_nop 0\n\tglobal_load_lds_dword %0, %1" : : "v"(voff), "s"(sbase), "s"(m0val) : "memory");
}

template <int MT, bool G1>
DEVI void k1_gemm_raw(char* smem, int hp, int mt0, int lane MF_ARG) {
  constexpr int NT = 4, nt0 = G1 ? 4 : 0;
  const unsigned lds0 = (unsigned)(size_t)smem;
  const int g = lane >> 4, r = lane & 15;
  unsigned ta[NT];
#pragma unroll
  for (int nt = 0; nt < NT; ++nt) {
    const int j = (nt0 + nt) * 16 + g * 4, seg = j / 40, cj = j - seg * 40 - (hp & 1) * 4;
    const int to = (seg < 3 && cj >= 0 && cj < 36) ? seg * 80 + cj * 2 + (cj >= 20 ? 8 : 0) : (seg < 3 ? 40 : 120);
    ta[nt] = lds0 + (unsigned)(K1R_T_OFF + (mt0 * 16 + r) * K1_TP + to);
    asm volatile("" : "+v"(ta[nt]));
  }
  unsigned sa = lds0 + (unsigned)(K1R_ST_OFF + (mt0 * 16 + r) * 8), ca = lds0 + (unsigned)(K1R_CS_OFF + (nt0 * 16 + g * 4) * 4);
  asm volatile("" : "+v"(sa), "+v"(ca));
  f32x4 acc[MT][NT];
#pragma unroll
  for (int mt = 0; mt < MT; ++mt)
#pragma unroll
    for (int nt = 0; nt < NT; ++nt) acc[mt][nt] = f32x4{0.f, 0.f, 0.f, 0.f};
  {
    GemmMath<K1R, MT, 4, 3, 4, false, 0, G1> gm(acc);
    if constexpr (G1) {
#pragma unroll
      for (int mt = 0; mt < MT; ++mt) gm.accq[mt] = f32x4{0.f, 0.f, 0.f, 0.f};
    }
    MF(14);
    if constexpr (!G1) {
      asm volatile("s_waitcnt vmcnt(0)" ::: "memory");
    }
    MF(15);
    gm.template run<9>(lds0, mt0, nt0, lane, 0u);
    MF(2);
    if constexpr (G1) {
#pragma unroll
      for (int mt = 0; mt < MT; ++mt) {
        const f32x4 q = gm.accq[mt];
        const float d = (r & 2) ? ((r & 1) ? q[3] : q[2]) : ((r & 1) ? q[1] : q[0]);
        const float qq = sum_g(g == (r >> 2) ? d : 0.f);
        float mb, rinv;
        ln_finish(acc[mt][3][0], qq, 1.0f / (float)D_, mb, rinv);
        const f32x2 st = {-mb, rinv};
        if (g == 2) asm volatile("ds_write_b64 %0, %1 offset:%2" ::"v"(sa), "v"(st), "n"(mt * 128) : "memory");
      }
    }
  }
  if (mt0 == 0 && r == 0) {
#pragma unroll
    for (int nt = 0; nt < NT; ++nt) *reinterpret_cast<f32x4*>(smem + K1R_CS_OFF + ((nt0 + nt) * 16 + g * 4) * 4) = acc[0][nt];
  }
  msync(lds0 + (unsigned)K1R_CNT);
  MF(3);
  f32x2 st[MT];
  f32x4 cs[NT];
#pragma unroll
  for (int nt = 0; nt < NT; ++nt) asm volatile("ds_read_b128 %0, %1 offset:%2" : "=v"(cs[nt]) : "v"(ca), "n"(nt * 64) : "memory");
#pragma unroll
  for (int mt = 0; mt < MT; ++mt) asm volatile("ds_read_b64 %0, %1 offset:%2" : "=v"(st[mt]) : "v"(sa), "n"(mt * 128) : "memory");
#pragma unroll
  for (int i = 0; i < MT; ++i) asm volatile("s_waitcnt lgkmcnt(0)" : "+v"(st[i]));
#pragma unroll
  for (int i = 0; i < NT; ++i) asm volatile("" : "+v"(cs[i]));
#pragma unroll
  for (int mt = 0; mt < MT; ++mt) {
    f32x2 M = {st[mt][0], st[mt][0]}, R = {st[mt][1], st[mt][1]};
    asm volatile("" : "+v"(M), "+v"(R));
#pragma unroll
    for (int nt = 0; nt < NT; ++nt) {
      const f32x2 v0 = R * __builtin_elementwise_fma(M, f32x2{cs[nt][0], cs[nt][1]}, f32x2{acc[mt][nt][0], acc[mt][nt][1]});
      const f32x2 v1 = R * __builtin_elementwise_fma(M, f32x2{cs[nt][2], cs[nt][3]}, f32x2{acc[mt][nt][2], acc[mt][nt][3]});
      acc[mt][nt][0] = __uint_as_float(pack2v(v0));
      acc[mt][nt][1] = __uint_as_float(pack2v(v1));
      const f32x2 pk = {acc[mt][nt][0], acc[mt][nt][1]};
      asm volatile("ds_write_b64 %0, %1 offset:%2" ::"v"(ta[nt]), "v"(pk), "n"(mt * 16 * K1_TP) : "memory");
    }
  }
}

template <int N>
DEVI void wait_vm() {
  asm volatile("s_waitcnt vmcnt(%0)" ::"n"(N) : "memory");
}

constexpr int LDS_ALL = 163840;
constexpr int LW_OFF = LDS_ALL - 128;
constexpr int LW_GO = 0, LW_T = 2, LW_O = 4  , LW_X1 = 6, LW_H = 8, LW_X2 = 12, LW_PK = 13, LW_Z = 14, LW_PAIR = 16, LW_AD = 20,
              LW_L = 22, LW_S = 24, LW_ST = 29, LW_HELLO = 30, LW_Q = 31;
DEVI void announce(unsigned addr, unsigned n, char* ws, int kind, int who, bool act) {
  const char* cb = uni_ptr(ws + OFF_CNT + (size_t)kind * 256 * 128);
  const unsigned long long mask = __ballot(act);
  unsigned t, u;
  asm volatile(
      "s_waitcnt vmcnt(0)\n\t"
      "s_mov_b64 exec, 1\n\t"
      "ds_add_rtn_u32 %0, %2, %3\n\t"
      "s_waitcnt lgkmcnt(0)\n\t"
      "v_readfirstlane_b32 %1, %0\n\t"
      "s_cmp_eq_u32 %1, %4\n\t"
      "s_cselect_b64 exec, %5, 0\n\t"
      "global_atomic_add %6, %3, %7 sc1\n\t"
      "s_mov_b64 exec, -1\n\t"
      "s_nop 0"
      : "=&v"(t), "=&s"(u)
      : "v"(addr), "v"(vconst(1u)), "s"(n - 1u), "s"(mask), "v"((unsigned)(who & 255) * 128u), "s"(cb)
      : "memory", "scc");
}
DEVI void cnt_wait(const char* ws, int kind, int who, unsigned n) {
  const char* cp = uni_ptr(ws + OFF_CNT + (size_t)(kind * 256 + who) * 128);
  unsigned v, c = SPIN_MEM, t;
  asm volatile(
      ".La%=:\n\tglobal_load_dword %0, %3, %4 sc1\n\ts_sub_u32 %1, %1, 1\n\ts_waitcnt vmcnt(0)\n\tv_readfirstlane_b32 %2, %0\n\ts_cmp_lg_u32 %2, %5\n\t"
      "s_cselect_b32 %2, %1, 0\n\ts_cmp_lg_u32 %2, 0\n\ts_cbranch_scc1 .La%="
      : "=&v"(v), "+s"(c), "=&s"(t)
      : "v"(vconst(0u)), "s"(cp), "s"(n - 1u)
      : "memory", "scc");
}
DEVI void dma16_sc1(const char* sbase, unsigned voff, unsigned m0val) {
  asm volatile("s_mov_b32 m0, %2\n\ts_nop 0\n\tglobal_load_lds_dwordx4 %0, %1 sc1"
               :
               : "v"(voff), "s"(sbase), "s"(m0val)
               : "memory");
}
template <int N, bool SC1>
DEVI void dma4(const char* sbase, unsigned v0, unsigned v1, unsigned v2, unsigned v3, unsigned m0val) {
#define DMA4(S)                                                                                                               \
  asm volatile("s_mov_b32 m0, %5\n\ts_nop 0\n\tglobal_load_lds_dwordx4 %0, %4" S "\n\t.if %6 > 1\n\tglobal_load_lds_dwordx4 %1, %4 offset:1024" S \
               "\n\t.endif\n\t.if %6 > 2\n\tglobal_load_lds_dwordx4 %2, %4 offset:2048" S                                    \
               "\n\t.endif\n\t.if %6 > 3\n\tglobal_load_lds_dwordx4 %3, %4 offset:3072" S "\n\t.endif"                      \
               :                                                                                                              \
               : "v"(v0), "v"(v1), "v"(v2), "v"(v3), "s"(sbase), "s"(m0val), "n"(N)                                           \
               : "memory")
  if constexpr (SC1) DMA4(" sc1");
  else DMA4("");
}
template <int N>
DEVI void dma_seq(const char* src, unsigned voff, unsigned m0val) {
  if constexpr (N > 0) {
    dma4<(N < 4 ? N : 4), true>(src, voff, voff, voff, voff, m0val);
    dma_seq<N - 4>(src + 4096, voff, m0val + 4096u);
  }
}
DEVI void bar() { __builtin_amdgcn_s_barrier(); }
DEVI void lds_inc(unsigned addr, unsigned by = 1u) {
  asm volatile("s_mov_b64 exec, 1\n\tds_add_u32 %0, %1\n\ts_mov_b64 exec, -1" ::"v"(addr), "v"(vconst(by)) : "memory");
}
DEVI void lds_spin(unsigned addr, unsigned n) {
  spin_ge<1>(addr, n);
}
template <int SL = 1>
DEVI void lds_spin4(unsigned addr, unsigned n) {
  spin_b4<SL>(addr, n);
}
DEVI int lane_id() {
  int l;
  asm volatile("v_mbcnt_lo_u32_b32 %0, -1, 0\n\tv_mbcnt_hi_u32_b32 %0, -1, %0" : "=v"(l));
  __builtin_assume((unsigned)l < 64u);
  return l;
}

template <int I>
DEVI u32x4 tr8(const u32x4 (&v)[8]) {
  constexpr unsigned sel = (I & 1) ? 0x07060302u : 0x05040100u;
  return u32x4{__builtin_amdgcn_perm(v[1][I / 2], v[0][I / 2], sel), __builtin_amdgcn_perm(v[3][I / 2], v[2][I / 2], sel),
               __builtin_amdgcn_perm(v[5][I / 2], v[4][I / 2], sel), __builtin_amdgcn_perm(v[7][I / 2], v[6][I / 2], sel)};
}
template <int PITCH, bool SUM, int... I>
DEVI void tr8_store(const u32x4 (&v)[8], unsigned dst, float (&cs)[8], std::integer_sequence<int, I...>) {
  auto one = [&](auto I_) {
    const u32x4 o = tr8<I_>(v);
    const unsigned d_ = dst;
    asm volatile("ds_write_b128 %0, %1 offset:%2" ::"v"(d_), "v"(o), "n"(I_ * PITCH) : "memory");
    if constexpr (SUM) {
#pragma unroll
      for (int d = 0; d < 4; ++d) {
        const unsigned od = o[d];
        cs[I_] = __builtin_amdgcn_fdot2_f32_bf16(__builtin_bit_cast(bf16x2, od), __builtin_bit_cast(bf16x2, 0x3f803f80u), cs[I_], false);
      }
    }
  };
  (one(std::integral_constant<int, I>{}), ...);
}
template <int NB>
DEVI void lds_out(unsigned src, char* dst, int lane) {
  constexpr int N = (NB + 1023) / 1024;
  u32x4 t[N];
  const unsigned a = src + (unsigned)lane * 16u;
#pragma unroll
  for (int i = 0; i < N; ++i) asm volatile("ds_read_b128 %0, %1 offset:%2" : "=v"(t[i]) : "v"(a), "n"(i * 1024) : "memory");
  const char* d = uni_ptr(dst);
#pragma unroll
  for (int i = 0; i < N; ++i) {
    asm volatile("s_waitcnt lgkmcnt(%1)" : "+v"(t[i]) : "n"(N - 1 - i) : "memory");
    if (i < N - 1 || lane < (NB - (N - 1) * 1024) / 16)
      asm volatile("global_store_dwordx4 %0, %1, %2 sc1\n\ts_nop 1" ::"v"((unsigned)lane * 16u), "v"(t[i]), "s"(d + i * 1024) : "memory");
  }
}
template <int A, int B, class F>
DEVI void static_for(F f) {
  if constexpr (A < B) {
    f(std::integral_constant<int, A>{});
    static_for<A + 1, B>(f);
  }
}
constexpr int WSP_V = 3;
constexpr int RAW_S1 = 6;
constexpr int RAW_S0 = 4;
template <int E>
struct RawX {
  static constexpr int first(int s) {
    return s <= RAW_S0 ? 0 : (s > RAW_S1 ? E : (E * (s - RAW_S0) + RAW_S1 - RAW_S0) / (RAW_S1 + 1 - RAW_S0));
  }
  static constexpr int n(int s) { return first(s + 1) - first(s); }
};
struct RawLd {
  static constexpr int VPS = 10, SB = K1R::SB;
  unsigned xoff[5], woff[5], m0x;
  unsigned long long xact0, wact[5], tag;
  const char *xp, *wp;
  int qb;
  DEVI void q_hook(int st, char* ws, unsigned lds0, int hp) {
    if ((qb >> 1) == st) {
      const int lane = lane_id(), g = lane >> 4;
      const unsigned a = lds0 + (unsigned)((st & 3) * SB + K1R::XB + ((qb & 1) * 32 + 16 * (g >> 1) + 4 * (g & 1) + ((lane >> 2) & 3)) * 288 +
                                           (lane & 3) * 8 + (hp & 1) * 8);
      const unsigned a2 = a + ((lane & 3) >= 1 ? 8u : 0u), a4 = a + ((lane & 3) >= 2 ? 8u : 0u);
      u32x2 qf[7][2];
#pragma unroll
      for (int nt = 0; nt < 7; ++nt)
        asm volatile("ds_read_b64_tr_b16 %0, %2 offset:%3\n\tds_read_b64_tr_b16 %1, %2 offset:%4"
                     : "=&v"(qf[nt][0]), "=&v"(qf[nt][1])
                     : "v"(nt == 2 ? a2 : (nt == 4 ? a4 : a)), "n"(nt * 32 + (nt > 4 ? 16 : (nt > 2 ? 8 : 0))),
                       "n"(2304 + nt * 32 + (nt > 4 ? 16 : (nt > 2 ? 8 : 0)))
                     : "memory");
      const char* d = uni_ptr(ws + OFF_PQ + (size_t)(hp * 18 + qb) * 7168 + 3072);
#pragma unroll
      for (int nt = 0; nt < 7; ++nt) {
        asm volatile("s_waitcnt lgkmcnt(%2)" : "+v"(qf[nt][0]), "+v"(qf[nt][1]) : "n"(12 - 2 * nt) : "memory");
        const u32x4 v = {qf[nt][0][0], qf[nt][0][1], qf[nt][1][0], qf[nt][1][1]};
        asm volatile("global_store_dwordx4 %0, %1, %2 offset:%3 sc1\n\ts_nop 1" ::"v"((unsigned)lane * 16u), "v"(v), "s"(d), "n"(nt * 1024 - 3072) : "memory");
      }
      if (st == 8) lds_wr32(lds0 + (unsigned)(LW_OFF + LW_Q * 4), 1u);
    }
  }
  DEVI void stage_x(unsigned so) {
    asm volatile(
        "s_mov_b32 m0, %6\n\ts_mov_b64 exec, %7\n\tglobal_load_lds_dwordx4 %0, %5\n\ts_mov_b64 exec, -1\n\t"
        "global_load_lds_dwordx4 %1, %5 offset:1024\n\tglobal_load_lds_dwordx4 %2, %5 offset:2048\n\tglobal_load_lds_dwordx4 %3, %5 offset:3072\n\t"
        "s_add_u32 m0, m0, 0x1000\n\ts_mov_b32 exec_hi, 0\n\tglobal_load_lds_dwordx4 %4, %5\n\ts_mov_b32 exec_hi, -1" ::"v"(xoff[0]),
        "v"(xoff[1]), "v"(xoff[2]), "v"(xoff[3]), "v"(xoff[4]), "s"(xp), "s"(m0x + so), "s"(xact0)
        : "memory", "scc");
    xp += 128;
  }
  DEVI void stage_w(unsigned so) {
    asm volatile(
        "s_mov_b32 m0, %6\n\ts_mov_b64 exec, %7\n\tglobal_load_lds_dwordx4 %0, %5\n\ts_mov_b64 exec, %8\n\t"
        "global_load_lds_dwordx4 %1, %5 offset:1024\n\ts_mov_b64 exec, %9\n\tglobal_load_lds_dwordx4 %2, %5 offset:2048\n\t"
        "s_mov_b64 exec, %10\n\tglobal_load_lds_dwordx4 %3, %5 offset:3072\n\ts_add_u32 m0, m0, 0x1000\n\ts_mov_b64 exec, %11\n\t"
        "global_load_lds_dwordx4 %4, %5\n\ts_mov_b64 exec, -1" ::"v"(woff[0]),
        "v"(woff[1]), "v"(woff[2]), "v"(woff[3]), "v"(woff[4]), "s"(wp), "s"(m0x + so + (unsigned)K1R::XB), "s"(wact[0]), "s"(wact[1]),
        "s"(wact[2]), "s"(wact[3]), "s"(wact[4])
        : "memory", "scc");
    wp += 64 * NQKV * 2;
  }
  DEVI void stage(unsigned so) {
    stage_x(so);
    stage_w(so);
  }
  template <class F>
  DEVI void front(const u16* x, const u16* wqkv, const u16* wo, char* ws, int bid, unsigned lds0, int wl, int lane, int rt, int hp,
                  int& pj, F jobs MF_ARG) {
    xp = uni_ptr((const char*)x - 3072);
    wp = uni_ptr((const char*)wqkv + ((hp * 72) & ~15) - 3072);
    m0x = lds0 + (unsigned)(wl * 288 * 16);
    xact0 = wl ? ~0ull : ~0xffull;
    const int row0 = wl * 36 + (lane >> 3);
#pragma unroll
    for (int i = 0; i < 5; ++i) {
      const int row = row0 + i * 8;
      int gr = rt * 128 - 16 + row;
      if (gr < 0) gr = 0;
      xoff[i] = (unsigned)(gr * (D_ * 2) + (3 - (i & 3)) * 1024) + (unsigned)(((lane ^ (row >> 1)) & 7) * 16);
    }
    stage_x(0u);
    u32x2 tg;
    asm volatile("global_load_dwordx2 %0, %1, %2 sc1" : "=v"(tg) : "v"(0u), "s"(uni_ptr(ws + OFF_TAG)) : "memory");
    asm volatile("" : "+v"(lane));
#pragma unroll
    for (int i = 0; i < 5; ++i) {
      const unsigned c = (unsigned)(i * 64 + (lane & 63));
      const unsigned wr = (c * 3641u) >> 16, wc = c - wr * 18u, seg = (wc * 13u) >> 6;
      wact[i] = __ballot(wc < 15u) & (i < 4 ? ~0ull : 0xffffffffull);
      woff[i] = (unsigned)(wl * 16 + swap23((int)wr)) * (unsigned)(NQKV * 2) + seg * (unsigned)(D_ * 2 - 80) + wc * 16u + (unsigned)((3 - (i & 3)) * 1024);
    }
    stage_w(0u);
    stage((unsigned)SB);
    jobs();
    MF(10);
    asm volatile("s_waitcnt vmcnt(%1)" : "+v"(tg) : "n"(VPS) : "memory");
    tag = (((unsigned long long)(unsigned)__builtin_amdgcn_readfirstlane(tg[1]) << 32) | (unsigned)__builtin_amdgcn_readfirstlane(tg[0])) +
          0x9e3779b97f4a7c15ull;
    MF(1);
    __builtin_amdgcn_s_barrier();
    MF(15);
    if (wl == 0 && lane == 0)
      asm volatile("global_store_dwordx2 %0, %1, off sc1\n\ts_nop 1" ::"v"(ws + OFF_HELLO + bid * 8), "v"(tag) : "memory");
    stage((unsigned)(2 * SB));
    qb = wl == 3 ? rt : ((wl == 2 && rt < 2) ? 16 + rt : 99);
    q_hook(0, ws, lds0, hp);
    const int nb = pj;
    const bool po = pj >= 0 && wl < 2;
    u32x4 v[8];
    unsigned da = 0u;
    if (po) {
      const int l54 = lane < 54 ? lane : 53, gl = l54 / 9, c = l54 - gl * 9, kl = wl * 6 + gl, t = nb / 6, kg = (nb - t * 6) * 12 + kl;
      da = lds0 + (unsigned)(K1R_PO + (kl >> 2) * 4608 + (16 * (kl & 1) + ((kl >> 1) & 1)) * 144 + c * 16);
      const unsigned sa[2] = {(unsigned)(okmap(kg * 8) * D_ + t * 72 + c * 8) * 2u, (unsigned)(okmap(kg * 8 + 4) * D_ + t * 72 + c * 8) * 2u};
      const char* ps = uni_ptr((const char*)wo);
#pragma unroll
      for (int j = 0; j < 8; ++j)
        asm volatile("global_load_dwordx4 %0, %1, %2 offset:%3" : "=v"(v[j]) : "v"(sa[j / 4]), "s"(ps), "n"((j % 4) * D_ * 2) : "memory");
    } else {
#pragma unroll
      for (int j = 0; j < 8; ++j) asm volatile("" : "=v"(v[j]));
    }
    unsigned slot = (unsigned)(3 * SB);
#pragma unroll 1
    for (int S = 0; S < RAW_S0; ++S) {
      wait_vm<VPS>();
      __builtin_amdgcn_s_barrier();
      if (S == 0) MF(16);
      if (S == 1) MF(17);
      if (S == 2) MF(18);
      if (S == 3) MF(19);
      stage(slot);
      slot = (slot == (unsigned)(3 * SB)) ? 0u : slot + (unsigned)SB;
      q_hook(S + 1, ws, lds0, hp);
      if (po && S == 1) {
        float cs[8];
        asm volatile("s_nop 0 ; LOOPWAIT" : "+v"(v[0]), "+v"(v[1]), "+v"(v[2]), "+v"(v[3]), "+v"(v[4]), "+v"(v[5]), "+v"(v[6]), "+v"(v[7]));
        tr8_store<288, false>(v, da, cs, std::make_integer_sequence<int, 8>{});
        asm volatile("s_waitcnt lgkmcnt(0)" ::: "memory");
      }
      if (po && S == 2) lds_out<6912>(lds0 + (unsigned)(K1R_PO + wl * 6912), ws + OFF_PO + (size_t)nb * 13824 + wl * 6912, lane);
    }
  }
};
template <bool JOB>
DEVI void k1_raw_loader(RawLd& ld, const u16* __restrict__ w_in, const u16* __restrict__ w_out, char* __restrict__ ws,
                        unsigned long long tag, int bid, unsigned lds0, int wl, int lane, int rt, int hp, int ct, int job, int pj MF_ARG) {
  constexpr int VPS = RawLd::VPS, SB = RawLd::SB, E = JOB ? 16 : 0;
  const bool isi = job < 144;
  const int kb = isi ? job >> 3 : job - 144;
  const int pitch = isi ? F_ * 2 : D_ * 2;
  const int l36 = lane < 36 ? lane : 35;
  unsigned sa = (unsigned)l36 * 16u, da;
  if (isi) da = lds0 + (unsigned)((l36 >= 18 ? 9216 - 288 : 0) + wl * 8 * 288 + l36 * 16);
  else da = lds0 + (unsigned)((l36 / 9) * (4608 - 144) + (16 * (wl & 1) + (wl >> 1)) * 144 + l36 * 16);
  asm volatile("" : "+v"(sa), "+v"(da));
  u32x4 v[2][8];
  const char* ps = uni_ptr((const char*)(isi ? w_in + (job & 7) * 576 : w_out) + (size_t)(kb * 32 + wl * 8) * pitch);
  auto extra = [&](auto J_) {
    constexpr int j = J_;
    const char* q = ps + (j / 2) * pitch;
    u32x4 t;
    asm volatile("global_load_dwordx4 %0, %1, %2 offset:%3" : "=v"(t) : "v"(sa), "s"(q), "n"((j % 2) * 576) : "memory");
    v[j % 2][j / 2] = t;
  };
  auto step = [&](auto S_) {
    constexpr int S = S_;
    wait_vm<RawX<E>::n(S - 2) + (S <= 6 ? VPS : 0) + RawX<E>::n(S - 1)>();
    __builtin_amdgcn_s_barrier();
    MF(16 + S);
    if constexpr (S == RAW_S0) {
      static_assert(RAW_S0 >= 4, "wo blocks");
      if (pj >= 0 && wl == 0 && lane == 0)
        asm volatile("global_store_dwordx2 %0, %1, off sc1\n\ts_nop 1" ::"v"(ws + OFF_POT + (pj / 6) * 128 + (pj % 6) * 8), "v"(tag) : "memory");
    }
    if constexpr (S <= 5) ld.stage((unsigned)(((S + 3) % 4) * SB));
    ld.q_hook(S + 1, ws, lds0, hp);
    static_for<RawX<E>::first(S), RawX<E>::first(S + 1)>(extra);
  };
  const char* hp_ = ws + OFF_HELLO + lane * 32;
  u32x4 a, b;
  if (wl == 1) asm volatile("global_load_dwordx4 %0, %2, off sc1\n\tglobal_load_dwordx4 %1, %2, off offset:16 sc1" : "=&v"(a), "=&v"(b) : "v"(hp_) : "memory");
  else asm volatile("" : "=v"(a), "=v"(b));
  static_for<RAW_S0, 8>(step);
  MF(2);
  if (wl == 1) {
    asm volatile("" : "+v"(a), "+v"(b));
    const unsigned t0 = (unsigned)tag, t1 = (unsigned)(tag >> 32);
#pragma unroll 1
    for (unsigned spin = 0; spin < SPIN_MEM; ++spin) {
      const unsigned bad = ((a[0] ^ t0) | (a[2] ^ t0) | (b[0] ^ t0) | (b[2] ^ t0)) | ((a[1] ^ t1) | (a[3] ^ t1) | (b[1] ^ t1) | (b[3] ^ t1));
      if (!__any(bad != 0u)) break;
      __builtin_amdgcn_s_sleep(4);
      asm volatile("global_load_dwordx4 %0, %2, off sc1\n\tglobal_load_dwordx4 %1, %2, off offset:16 sc1\n\ts_waitcnt vmcnt(0)"
                   : "=&v"(a), "=&v"(b)
                   : "v"(hp_)
                   : "memory");
    }
    lds_wr32(lds0 + (unsigned)(LW_OFF + LW_HELLO * 4), 1u);
    if (bid == 0 && lane == 0) asm volatile("global_store_dwordx2 %0, %1, off sc1\n\ts_nop 1" ::"v"(ws + OFF_TAG), "v"(tag) : "memory");
  }
  MF(11);
  if (rt < 2) lds_spin(lds0 + (unsigned)(LW_OFF + LW_Q * 4), 1u);
  lds_spin(lds0 + (unsigned)K1R_CNT, 8u);
  if constexpr (JOB) {
#pragma unroll
    for (int b = 0; b < 2; ++b)
      asm volatile("s_waitcnt vmcnt(0)"
                   : "+v"(v[b][0]), "+v"(v[b][1]), "+v"(v[b][2]), "+v"(v[b][3]), "+v"(v[b][4]), "+v"(v[b][5]), "+v"(v[b][6]), "+v"(v[b][7])
                   :
                   : "memory");
  }
  MF(12);
  const char* tp = ws + OFF_POT + ct * 128 + (lane < 6 ? lane : 5) * 8;
  u32x2 f;
  asm volatile("global_load_dwordx2 %0, %1, off sc1" : "=v"(f) : "v"(tp) : "memory");
  if constexpr (JOB) {
    static_assert(SB == 4 * 9216, "staging");
#pragma unroll
    for (int b = 0; b < 2; ++b) {
      float cs[8] = {0.f, 0.f, 0.f, 0.f, 0.f, 0.f, 0.f, 0.f};
      tr8_store<288, true>(v[b], da + (unsigned)(b * 18432), cs, std::make_integer_sequence<int, 8>{});
      const f32x4 c0 = {cs[0], cs[1], cs[2], cs[3]}, c1 = {cs[4], cs[5], cs[6], cs[7]};
      const unsigned ca = lds0 + (unsigned)(3 * SB + wl * 2304) + sa * 2u;
      asm volatile("ds_write_b128 %0, %1 offset:%2" ::"v"(ca), "v"(c0), "n"(b * 1152) : "memory");
      asm volatile("ds_write_b128 %0, %1 offset:%2" ::"v"(ca), "v"(c1), "n"(b * 1152 + 16) : "memory");
    }
    lds_inc(lds0 + (unsigned)(LW_OFF + LW_ST * 4));
    lds_spin(lds0 + (unsigned)(LW_OFF + LW_ST * 4), 4u);
    if (isi) {
      const int j = (job & 7) * 4 + wl, ti = j < 16 ? 2 * j + 1 : 2 * (j - 16);
      lds_out<9216>(lds0 + (unsigned)(wl * 9216), ws + OFF_PI + (size_t)(ti * 18 + kb) * 9216, lane);
      f32x4 c[4];
      const unsigned ca = lds0 + (unsigned)(3 * SB + wl * 576) + sa;
#pragma unroll
      for (int i = 0; i < 4; ++i) asm volatile("ds_read_b128 %0, %1 offset:%2" : "=v"(c[i]) : "v"(ca), "n"(i * 2304) : "memory");
      asm volatile("s_waitcnt lgkmcnt(0)" : "+v"(c[0]), "+v"(c[1]), "+v"(c[2]), "+v"(c[3]));
      const f32x4 t = (c[0] + c[1]) + (c[2] + c[3]);
      if (lane < 36) pk_st16(ws + OFF_CSP + ((size_t)kb * F_ + ti * 144) * 4 + sa, __builtin_bit_cast(u32x4, t));
    } else {
      lds_out<4608>(lds0 + (unsigned)(wl * 9216), ws + OFF_PW + (size_t)(wl * 144 + kb) * 4608, lane);
      lds_out<4608>(lds0 + (unsigned)(wl * 9216 + 4608), ws + OFF_PW + (size_t)(wl * 144 + 72 + kb) * 4608, lane);
    }
  }
  MF(8);
  announce(lds0 + (unsigned)(LW_OFF + LW_PK * 4), 4u, ws, CN_PK, isi ? 8 + (job & 3) * 4 + lane : 24 + lane, JOB && lane < (isi ? 4 : 8));
  MF(9);
  if (wl == 0) {
    asm volatile("" : "+v"(f));
#pragma unroll 1
    for (unsigned spin = 0; spin < SPIN_MEM; ++spin) {
      if (!__any(f[0] != (unsigned)tag || f[1] != (unsigned)(tag >> 32))) break;
      __builtin_amdgcn_s_sleep(2);
      asm volatile("global_load_dwordx2 %0, %1, off sc1\n\ts_waitcnt vmcnt(0)" : "=&v"(f) : "v"(tp) : "memory");
    }
  }
}

template <int MT, int NT>
DEVI void k1_gemm(char* smem, const char* ws, int rt, int hp, int mt0, int nt0,
                  bool late, int lane MK_ARG) {
  const unsigned lds0 = (unsigned)(size_t)smem;
  const int g = lane >> 4;
  f32x4 cs[NT];
  unsigned ta[NT];
  unsigned cad[NT];
#pragma unroll
  for (int nt = 0; nt < NT; ++nt) {
    int c = (nt0 + nt) * 16 + g * 4;
    if (c > 104) c = 104;
    const int part = c / 36, ci = c - part * 36;
    cad[nt] = lds0 + (unsigned)(K1_CS_OFF + (part * 40 + (hp & 1) * 4 + ci) * 4);
    asm volatile("" : "+v"(cad[nt]));
    const int to = (nt0 + nt) * 16 + g * 4 > 104 ? 120 : part * 80 + ci * 2 + (ci >= 20 ? 8 : 0);
    ta[nt] = lds0 + (unsigned)(K1_T_OFF + (mt0 * 16 + (lane & 15)) * K1_TP + to);
    asm volatile("" : "+v"(ta[nt]));
  }
  unsigned sa = lds0 + (unsigned)(K1_ST_OFF + (mt0 * 16 + (lane & 15)) * 8);
  asm volatile("" : "+v"(sa));
  f32x4 acc[MT][NT];
#pragma unroll
  for (int mt = 0; mt < MT; ++mt)
#pragma unroll
    for (int nt = 0; nt < NT; ++nt) acc[mt][nt] = f32x4{0.f, 0.f, 0.f, 0.f};
  f32x2 st[MT];
  if (late) bar();
  if constexpr (NT == 7) cnt_wait(ws, CN_X2, rt * 16 + hp, rt ? 24u : 16u);
  bar();
  MK(1);
  {
    GemmMath<K1C, MT, NT, (NT == 7 ? 5 : 3), (NT == 3 ? 6 : NT), true, 2, false, K1_WS> gm(acc);
    gm.setup(lds0, mt0, nt0, lane, 0u);
    bar();
#pragma unroll
    for (int nt = 0; nt < NT; ++nt) asm volatile("ds_read_b128 %0, %1" : "=v"(cs[nt]) : "v"(cad[nt]));
    gm.first();
    gm.template mid<8>();
#pragma unroll
    for (int mt = 0; mt < MT; ++mt) asm volatile("ds_read_b64 %0, %1 offset:%2" : "=v"(st[mt]) : "v"(sa), "n"(mt * 128));
    asm volatile("s_waitcnt vmcnt(0)" ::: "memory");
    gm.last();
  }
#pragma unroll
  for (int nt = 0; nt < NT; ++nt) asm volatile("" : "+v"(cs[nt]));
#pragma unroll
  for (int mt = 0; mt < MT; ++mt) asm volatile("" : "+v"(st[mt]));
  MK(2);
#pragma unroll
  for (int mt = 0; mt < MT; ++mt) {
    f32x2 M = {st[mt][0], st[mt][0]}, R = {st[mt][1], st[mt][1]};
    asm volatile("" : "+v"(M), "+v"(R));
#pragma unroll
    for (int nt = 0; nt < NT; ++nt) {
      const f32x2 v0 = R * __builtin_elementwise_fma(M, f32x2{cs[nt][0], cs[nt][1]}, f32x2{acc[mt][nt][0], acc[mt][nt][1]});
      const f32x2 v1 = R * __builtin_elementwise_fma(M, f32x2{cs[nt][2], cs[nt][3]}, f32x2{acc[mt][nt][2], acc[mt][nt][3]});
      acc[mt][nt][0] = __uint_as_float(pack2v(v0));
      acc[mt][nt][1] = __uint_as_float(pack2v(v1));
      const f32x2 pk = {acc[mt][nt][0], acc[mt][nt][1]};
      asm volatile("ds_write_b64 %0, %1 offset:%2" ::"v"(ta[nt]), "v"(pk), "n"(mt * 16 * K1_TP) : "memory");
    }
  }
}

DEVI float vmax(float a, float b) {
  float r;
  asm("v_max_f32 %0, %1, %2" : "=v"(r) : "v"(a), "v"(b));
  return r;
}
struct Attn {
  unsigned ka, kb, va, vb, m0, m1, m2, pm[2], oa, pa;
  f32x4 mk;
  unsigned long long sel[4];
  const char* ob;
  DEVI void setup(unsigned T, char* o, int r0, int hp, int wave, int lane) {
    const int g = lane >> 4, r = lane & 15, blk = wave >> 1, hd = wave & 1;
    const unsigned co = (unsigned)(g < 2 ? hd * 48 + g * 16 : 32);
    ka = T + (unsigned)((blk * 32 + 2 * r) * K1_TP) + co;
    kb = T + (unsigned)((blk * 32 + 2 * (r & 7)) * K1_TP) + co;
    const int vr = 4 * g + ((lane >> 2) & 3);
    va = T + (unsigned)((blk * 32 + 2 * vr) * K1_TP + (lane & 3) * 8);
    vb = T + (unsigned)((blk * 32 + 2 * (vr & 7)) * K1_TP + (lane & 3) * 8);
    m0 = (g < 2 || (g == 2 && hd == 0)) ? ~0u : 0u;
    m1 = (g < 2 || (g == 2 && hd == 1)) ? ~0u : 0u;
    m2 = g < 2 ? ~0u : 0u;
    pm[0] = pm[1] = 0u;
    const float ci = __uint_as_float(vconst(0xff800000u));
    const unsigned ch = vconst(0xffffu);
#pragma unroll
    for (int e = 0; e < 4; ++e) {
      const int w1 = r + 8 - 4 * g - e;
      mk[e] = (w1 & 15) <= 7 ? 0.f : ci;
      sel[e] = __ballot(w1 >= 8);
      if (w1 >= 8) pm[e >> 1] |= ch << (16 * (e & 1));
    }
    oa = (unsigned)((8 + hp * 4 + hd * 2 + (g >> 1)) * 1024 + ((blk & 1) * 32 + 2 * r + (g & 1)) * 16);
    pa = ((oa & 1023u) >> 2) + (unsigned)((hp * 2 + hd) * 256);
    ob = uni_ptr(o + (size_t)((r0 >> 6) + (wave >> 2)) * (72 * 1024));
    asm volatile("" : "+v"(ka), "+v"(kb), "+v"(va), "+v"(vb), "+v"(m0), "+v"(m1), "+v"(m2), "+v"(pm[0]), "+v"(pm[1]), "+v"(oa));
    asm volatile("" : "+v"(mk), "+v"(pa), "+s"(ob), "+s"(sel[0]), "+s"(sel[1]), "+s"(sel[2]), "+s"(sel[3]));
  }
  DEVI void run(int r0, int hp, int wave, int lane) {
    const int hd = wave & 1;
    u32x4 q[2], k0[2], k1[2];
    u32x2 v[2][2][2];
#pragma unroll
    for (int p = 0; p < 2; ++p) {
      asm volatile("ds_read_b128 %0, %1 offset:%2" : "=v"(q[p]) : "v"(ka), "n"((16 + p) * K1_TP) : "memory");
      asm volatile("ds_read_b128 %0, %1 offset:%2" : "=v"(k0[p]) : "v"(ka), "n"(p * K1_TP + 80) : "memory");
      asm volatile("ds_read_b128 %0, %1 offset:%2" : "=v"(k1[p]) : "v"(kb), "n"((32 + p) * K1_TP + 80) : "memory");
    }
    const unsigned vh = (unsigned)hd * 48u;
#pragma unroll
    for (int p = 0; p < 2; ++p) {
      asm volatile("ds_read_b64_tr_b16 %0, %1 offset:%2" : "=v"(v[p][0][0]) : "v"(va + vh), "n"(p * K1_TP + 160) : "memory");
      asm volatile("ds_read_b64_tr_b16 %0, %1 offset:%2" : "=v"(v[p][0][1]) : "v"(vb + vh), "n"((32 + p) * K1_TP + 160) : "memory");
      asm volatile("ds_read_b64_tr_b16 %0, %1 offset:%2" : "=v"(v[p][1][0]) : "v"(va), "n"(p * K1_TP + 192) : "memory");
      asm volatile("ds_read_b64_tr_b16 %0, %1 offset:%2" : "=v"(v[p][1][1]) : "v"(vb), "n"((32 + p) * K1_TP + 192) : "memory");
    }
    const u32x4 one4 = {vconst(0x3f803f80u), vconst(0x3f803f80u), vconst(0x3f803f80u), vconst(0x3f803f80u)};
    const s16x8 ones = __builtin_bit_cast(s16x8, one4);
    const float c = 0.23570226039551584f * 1.4426950408889634f;
    const f32x4 z = {0.f, 0.f, 0.f, 0.f};
    f32x4 sc[2][2];
#pragma unroll
    for (int p = 0; p < 2; ++p) {
      asm volatile("s_waitcnt lgkmcnt(%3)" : "+v"(q[p]), "+v"(k0[p]), "+v"(k1[p]) : "n"(11 - 3 * p));
      const s16x8 qf = __builtin_bit_cast(s16x8, q[p] & u32x4{m0, m1, m2, m2});
      sc[p][0] = __builtin_amdgcn_mfma_f32_16x16x32_bf16(__builtin_bit_cast(s16x8, k0[p]), qf, mk, 0, 0, 0);
      sc[p][1] = __builtin_amdgcn_mfma_f32_16x16x32_bf16(__builtin_bit_cast(s16x8, k1[p]), qf, mk, 0, 0, 0);
      __builtin_amdgcn_sched_barrier(0);
    }
    if (__builtin_expect(r0 == 0 && wave < 2, 0)) {
#pragma unroll
      for (int p = 0; p < 2; ++p)
#pragma unroll
        for (int e = 0; e < 4; ++e)
          if (lane < 32) sc[p][0][e] = mk[e] - 4.242640687e9f;
    }
    float lg[2][4], mx[2];
#pragma unroll
    for (int p = 0; p < 2; ++p) {
#pragma unroll
      for (int e = 0; e < 4; ++e) lg[p][e] = __builtin_amdgcn_inverse_ballot_w64(sel[e]) ? sc[p][1][e] : sc[p][0][e];
      asm("v_max3_f32 %0, %1, %2, %3\n\tv_max_f32 %0, %0, %4" : "=&v"(mx[p]) : "v"(lg[p][0]), "v"(lg[p][1]), "v"(lg[p][2]), "v"(lg[p][3]));
    }
    {
      const auto a = __builtin_amdgcn_permlane32_swap(__float_as_uint(mx[0]), __float_as_uint(mx[1]), false, false);
      const unsigned b = __float_as_uint(vmax(__uint_as_float(a[0]), __uint_as_float(a[1])));
      const auto d = __builtin_amdgcn_permlane16_swap(b, b, false, false);
      const unsigned f = __float_as_uint(vmax(__uint_as_float(d[0]), __uint_as_float(d[1])) * -c);
      const auto h = __builtin_amdgcn_permlane32_swap(f, f, false, false);
      mx[0] = __uint_as_float(h[0]);
      mx[1] = __uint_as_float(h[1]);
    }
#pragma unroll
    for (int p = 0; p < 2; ++p)
#pragma unroll
      for (int e = 0; e < 4; ++e) lg[p][e] = __builtin_amdgcn_exp2f(__builtin_fmaf(lg[p][e], c, mx[p]));
    asm volatile("s_waitcnt lgkmcnt(0)"
                 : "+v"(v[0][0][0]), "+v"(v[0][0][1]), "+v"(v[0][1][0]), "+v"(v[0][1][1]), "+v"(v[1][0][0]), "+v"(v[1][0][1]),
                   "+v"(v[1][1][0]), "+v"(v[1][1][1]));
    __builtin_amdgcn_sched_barrier(0);
    f32x4 su[2], o0[2], o1[2];
    s16x8 pp[2];
#pragma unroll
    for (int p = 0; p < 2; ++p) {
      const unsigned p0 = pack2(lg[p][0], lg[p][1]), p1 = pack2(lg[p][2], lg[p][3]);
      pp[p] = __builtin_bit_cast(s16x8, u32x4{p0 & ~pm[0], p1 & ~pm[1], p0 & pm[0], p1 & pm[1]});
    }
    __builtin_amdgcn_sched_barrier(0);
#pragma unroll
    for (int p = 0; p < 2; ++p) {
      const s16x8 pf = pp[p];
      const u32x4 v0 = {v[p][0][0][0], v[p][0][0][1], v[p][0][1][0], v[p][0][1][1]};
      const u32x4 v1 = {v[p][1][0][0], v[p][1][0][1], v[p][1][1][0], v[p][1][1][1]};
      su[p] = __builtin_amdgcn_mfma_f32_16x16x32_bf16(ones, pf, z, 0, 0, 0);
      o0[p] = __builtin_amdgcn_mfma_f32_16x16x32_bf16(__builtin_bit_cast(s16x8, v0), pf, z, 0, 0, 0);
      o1[p] = __builtin_amdgcn_mfma_f32_16x16x32_bf16(__builtin_bit_cast(s16x8, v1), pf, z, 0, 0, 0);
    }
    __builtin_amdgcn_sched_barrier(0);
    u32x2 y[2], yl;
    unsigned hm = (unsigned)hd - 1u;
    asm volatile("" : "+s"(hm));
#pragma unroll
    for (int p = 0; p < 2; ++p) {
      const float inv = __builtin_amdgcn_rcpf(su[p][0]);
      y[p] = u32x2{pack2(o0[p][0] * inv, o0[p][1] * inv), pack2(o0[p][2] * inv, o0[p][3] * inv)};
      const unsigned la = pack2(o1[p][0] * inv, o1[p][1] * inv), lb = pack2(o1[p][2] * inv, o1[p][3] * inv);
      yl[p] = (la & hm) | (lb & ~hm);
    }
    const auto s0 = __builtin_amdgcn_permlane16_swap(y[0][0], y[1][0], false, false);
    const auto s1 = __builtin_amdgcn_permlane16_swap(y[0][1], y[1][1], false, false);
    const u32x4 w = {s0[0], s1[0], s0[1], s1[1]};
    asm volatile("global_store_dwordx4 %0, %1, %4 sc1\n\ts_mov_b64 exec, 0xffff\n\tglobal_store_dwordx2 %2, %3, %4 sc1\n\ts_mov_b64 exec, -1\n\ts_nop 0" ::"v"(oa),
                 "v"(w), "v"(pa), "v"(yl), "s"(ob)
                 : "memory");
  }
};

#define NL_MAIN 658
DEVI void math_start(unsigned lds0, char* ws, int bid, int wave, int lane) {
  static_assert(2 * SZ_ZP == (size_t)256 * 2048 && NCNT <= 16 && OFF_CNT + NCNT * 32768 < 0xffffffffu, "mark split");
  const unsigned m1 = vconst(~0u), l = (unsigned)lane;
  const unsigned hi = wave < 2 ? 128u : 32768u;
  unsigned b0 = (unsigned)OFF_ZP + (unsigned)(bid * 2 + wave) * 1024u, b1 = (unsigned)OFF_CNT + (unsigned)((wave & 1) * 2048 + bid) * 128u;
  asm volatile("" : "+s"(b0), "+s"(b1));
  const unsigned base = wave < 2 ? b0 : b1;
  const unsigned long long act = wave < 3 ? ~0ull : (wave == 3 ? (1ull << ((NCNT - 8) * 8)) - 1ull : 0ull);
  asm volatile("s_mov_b64 exec, %3\n\tglobal_store_dwordx4 %0, %1, %2 sc1\n\ts_mov_b64 exec, -1\n\ts_nop 0" ::"v"(base + (l >> 3) * hi + (l & 7u) * 16u),
               "v"(u32x4{m1, m1, m1, m1}), "s"(ws), "s"(act)
               : "memory");
  unsigned a4 = (l < 32u ? (l >> 3) * (unsigned)K1R::SB : (unsigned)LW_OFF) + (l & 7u) * 16u, a0 = (unsigned)(wave * K1R::SB + K1R::XB + 240) + l * 288u;
  asm volatile("" : "+v"(a4), "+v"(a0));
  const unsigned a = wave < 4 ? a0 : a4;
  const unsigned v = (wave < 4 || l < 32u) ? 0x3f803f80u : 0u;
  asm volatile("s_mov_b64 exec, %2\n\tds_write_b128 %0, %1\n\ts_mov_b64 exec, -1\n\ts_waitcnt lgkmcnt(0)" ::"v"(lds0 + a), "v"(u32x4{v, v, v, v}),
               "s"(wave < 4 ? ~0ull : (wave == 4 ? 0xffffffffffull : 0ull))
               : "memory");
}

template <int NST, int NS, int NSW, int WOFF, int XW, int X0, int S0, unsigned BAR>
struct ResX {
  static constexpr int WS = 4 * 4608, R = 10, V1 = 5 * NS, NV = V1 + 10 * (NST - NS), NG = (NSW + 2) / 3;
  s16x8 xf[XW][2];
  s16x8 wf[R];
  unsigned wrd[2][NG], xo, ea, fa, fv, tag;
  const char* xp[3];
  f32x4 (&acc)[2][5];
  DEVI ResX(f32x4 (&a)[2][5]) : acc(a) {}
  static constexpr int st(int V) { return V < V1 ? V / 5 : NS + (V - V1) / 10; }
  static constexpr int hf(int V) { return V < V1 ? 0 : ((V - V1) / 5) & 1; }
  static constexpr bool first(int V) { return V < V1 ? V % 5 == 0 : (V - V1) % 10 == 0; }
  static constexpr int wreq(int V) {
    int n = V + 5;
    if (V >= V1 - 4) n += V < V1 ? V - (V1 - 5) : 4;
    return n < NV ? n : NV;
  }
  static constexpr int XU = V1 + 1, XF = V1 + 11, XR = V1 + 15, XC = V1 + 21;
  static constexpr int oth(int V) { return NS && ((V >= XU && V <= XF && (V - XU) % 2 == 0) || V == XR || (V > XC && V <= XC + 5)) ? 1 : 0; }
  static constexpr int newer(int V) {
    const int F = V + 1 < NV ? V + 1 : V;
    int v = V - 1, n = 0;
    for (; v >= 0 && wreq(v - 1) > F; --v) n += wreq(v) - wreq(v - 1) + oth(v);
    return n + wreq(v) - 1 - F + (v >= 0 ? oth(v) : 0);
  }
  static constexpr int xreq(int T) {
    int n = X0 + 2 * (T + 1);
    if (n > T + XW) n = T + XW;
    if (NS && T <= 11 && n > 18) n = 18;
    if (T < 0) n = X0;
    return n < NST ? n : NST;
  }
  static_assert(NS == 0 || (xreq(NS - 2) == NST && XC + 5 < NV), "the last steps only compute and swap");
  template <int T, int M>
  DEVI void xld() {
    asm volatile("global_load_dwordx4 %0, %1, %2 sc1" : "=v"(xf[T % XW][M]) : "v"(xo), "s"(xp[M == 0 ? 0 : (T < NS ? 1 : 2)]));
    if constexpr (M == 1 && T + 1 < NST) asm volatile("v_add_u32 %0, 0x2000, %0" : "+v"(xo));
  }
  template <int V>
  DEVI void rd() {
    constexpr int T = st(V), sl = (T / 2 + S0) % NSW;
    asm volatile("ds_read_b128 %0, %1 offset:%2" : "=v"(wf[V % R]) : "v"(wrd[hf(V)][sl / 3]), "n"((sl % 3) * WS + (T & 1) * 9216 + (V % 5) * 32));
  }
  template <int V, int M, int H>
  DEVI void mf() {
    acc[M][V % 5] = __builtin_amdgcn_mfma_f32_16x16x32_bf16(wf[V % R], xf[st(V) % XW][H], acc[M][V % 5], 0, 0, 0);
    __builtin_amdgcn_sched_barrier(0);
  }
  template <int V>
  DEVI void unit() {
    constexpr int T = st(V), n = V % 5, nx = xreq(T) - xreq(T - 1), nw = wreq(V) - wreq(V - 1);
    constexpr int vm = 2 * (xreq(T - 1) - 1 - T);
    static_assert(newer(V) <= 15, "lgkmcnt range");
    if constexpr (first(V) && V % 2 == 0) asm volatile("s_waitcnt vmcnt(%0) lgkmcnt(%1)" ::"n"(vm), "n"(newer(V)));
    else if constexpr (first(V)) asm volatile("s_waitcnt vmcnt(%0)" ::"n"(vm));
    else if constexpr (V % 2 == 0) asm volatile("s_waitcnt lgkmcnt(%0)" ::"n"(newer(V)));
    __builtin_amdgcn_sched_barrier(0);
    if constexpr (V < V1) mf<V, V & 1, V & 1>();
    else mf<V, 0, hf(V)>();
    if constexpr (nw > 0) rd<wreq(V - 1)>();
    __builtin_amdgcn_sched_barrier(0);
    if constexpr (V < V1) {
      mf<V, 1 - (V & 1), 1 - (V & 1)>();
      if constexpr (nw > 1) rd<wreq(V - 1) + 1>();
      if constexpr (nx == 2 && n < 4) xld<xreq(T - 1) + n / 2, n % 2>();
      if constexpr (nx == 1 && n == (T & 1)) xld<xreq(T - 1), 0>();
      if constexpr (nx == 1 && n == (T & 1) + 2) xld<xreq(T - 1), 1>();
      if constexpr (n == 4 && (T & 1) && ((BAR >> (T / 2)) & 1u)) bar();
    } else if constexpr (NS == 0) {
      constexpr int k = (V % 10) / 2;
      if constexpr (V % 2 == 0 && k < 2 * nx) xld<xreq(T - 1) + k / 2, k % 2>();
      if constexpr (V % 10 == 8 && nx > 0 && xreq(T) == NST)
        asm volatile("s_mov_b64 exec, 1\n\tds_add_u32 %0, %1\n\ts_mov_b64 exec, -1" ::"v"(fa), "v"(fv));
    } else {
      if constexpr (oth(V) && V < XF) asm volatile("ds_write_b128 %0, %1 offset:%2" ::"v"(ea), "v"(acc[1][(V - XU) / 2]), "n"((V - XU) * 512));
      if constexpr (V == XF) asm volatile("ds_write_b32 %0, %1" ::"v"(fa), "v"(fv));
      if constexpr (V == XF + 2) asm volatile("v_xor_b32 %0, 4, %0" : "+v"(fa));
      if constexpr (V == XR) asm volatile("ds_read_b32 %0, %1" : "+v"(fv) : "v"(fa));
      if constexpr (V == XR + 2) asm volatile("v_xor_b32 %0, 0x2000, %0" : "+v"(ea));
      if constexpr (V == XC) {
        unsigned c = 1u << 18;
        asm volatile(
            "s_waitcnt lgkmcnt(%4)\n\tv_cmp_ne_u32 vcc, %3, %0\n\ts_cbranch_vccnz .Lw%=\n\t.subsection 1\n.Lw%=:\n\tds_read_b32 %0, %2\n\t"
            "s_waitcnt lgkmcnt(0)\n\tv_cmp_ne_u32 vcc, %3, %0\n\t" SAFE_OUT(".Lr%=") "s_cbranch_vccnz .Lw%=\n\ts_branch .Lr%=\n\t.subsection 0\n.Lr%=:"
            : "+v"(fv), NOP_(c)
            : "v"(fa), "s"(tag), "n"(wreq(XC) - wreq(XR))
            : "vcc", "scc");
      }
      if constexpr (oth(V) && V > XC) asm volatile("ds_read_b128 %0, %1 offset:%2" : "=v"(acc[1][V - XC - 1]) : "v"(ea), "n"((V - XC - 1) * 1024));
    }
    __builtin_amdgcn_sched_barrier(0);
  }
  template <int... V>
  DEVI void units(std::integer_sequence<int, V...>) {
    (unit<V>(), ...);
  }
  DEVI void run() {
    units(std::make_integer_sequence<int, NV>{});
    if constexpr (NS > 0) {
      asm volatile("s_waitcnt lgkmcnt(0)" : "+v"(acc[1][0]), "+v"(acc[1][1]), "+v"(acc[1][2]), "+v"(acc[1][3]), "+v"(acc[1][4]));
#pragma unroll
      for (int i = 0; i < 5; ++i) acc[0][i] += acc[1][i];
    }
  }
  template <int... V>
  DEVI void pro(std::integer_sequence<int, V...>) {
    (rd<V>(), ...);
  }
  DEVI void prow() { pro(std::make_integer_sequence<int, wreq(-1)>{}); }
  template <int T0, int... T>
  DEVI void prox(std::integer_sequence<int, T...>) {
    ((xld<T0 + T, 0>(), xld<T0 + T, 1>()), ...);
  }
  DEVI void setup(unsigned lds0, const char* xbase, int wave, int lane, unsigned red, unsigned nth) {
    const int g = lane >> 4, r = lane & 15, kq = wave & 1;
    xp[0] = xbase + wave * 256;
    xp[1] = xbase + (wave ^ 1) * 256;
    xp[2] = xp[0] + (kq ? -4096 : 4096);
    xo = (unsigned)((4 * kq + g) * 1024 + r * 16);
#pragma unroll
    for (int i = 0; i < 2 * NG; ++i) {
      wrd[i / NG][i % NG] = lds0 + (unsigned)(WOFF + (i % NG) * 3 * WS + (kq ^ (i / NG)) * 4608 + (2 * (r & 7) + 16 * (g & 1) + (g >> 1)) * 144 + (r >> 3) * 16);
      if (i < NG || NS == 0 || i % NG == ((NS / 2 + S0) % NSW) / 3) asm volatile("" : "+v"(wrd[i / NG][i % NG]));
    }
    asm volatile("" : "+v"(xo));
    if constexpr (NS > 0) {
      ea = lds0 + red + (unsigned)((wave ^ 1) * 8192 + lane * 16);
      fa = lds0 + (unsigned)(LW_OFF + (LW_PAIR + wave) * 4);
      fv = tag = nth;
      asm volatile("" : "+v"(ea), "+v"(fa), "+v"(fv));
    }
  }
  template <int N>
  DEVI void init(u32x2 (&xv)[5]) {
    asm volatile("s_waitcnt vmcnt(%5)" : "+v"(xv[0]), "+v"(xv[1]), "+v"(xv[2]), "+v"(xv[3]), "+v"(xv[4]) : "n"(N));
#pragma unroll
    for (int i = 0; i < 5; ++i) {
      acc[0][i] = f32x4{bf_lo(xv[i][0]), bf_hi(xv[i][0]), bf_lo(xv[i][1]), bf_hi(xv[i][1])};
      asm volatile("" : "+v"(acc[0][i]));
      acc[1][i] = f32x4{0.f, 0.f, 0.f, 0.f};
    }
  }
};

constexpr int P5_XW = 8, P5_X0 = 5, P2_X0 = 4;
constexpr unsigned P5_BAR = 0x2aaau;
constexpr int P5_W = 15 * 1024;
constexpr int P2_W = K1_ST_OFF - 18 * 4608;
constexpr int P5_RED = 0;
static_assert(P5_RED % 16384 == 0 && P5_RED + 3 * 8192 + 5120 <= P5_W + 18432, "exchange area");
static_assert(P2_W >= K1_T_OFF + 144 * K1_TP + 64 && P2_W % 16 == 0 && P5_W + 8 * 18432 <= LW_OFF, "LDS placement");

struct ResIn {
  const char* xp;
  unsigned o, o2, st;
  DEVI void setup(const char* src, bool rm, int rb, int ct, int wave, int lane) {
    const int g = lane >> 4, g2 = g & 1, rl = wave * 16 + (lane & 15);
    xp = uni_ptr(rm ? src + ((size_t)rb * 64 * D_ + ct * 72) * 2 : src + (size_t)(rb * 72 + ct * 9) * 1024);
    o = rm ? (unsigned)(rl * (D_ * 2) + g * 8) : (unsigned)((g >> 1) * 1024 + rl * 16 + g2 * 8);
    o2 = rm ? (unsigned)(rl * (D_ * 2) + g2 * 8 + 128) : (unsigned)(8192 + rl * 16 + g2 * 8);
    st = rm ? 32u : 2048u;
    asm volatile("" : "+v"(o), "+v"(o2));
    asm volatile("" : "+s"(xp));
  }
  DEVI void load(u32x2 (&xv)[5]) {
#pragma unroll
    for (int i = 0; i < 4; ++i) asm volatile("global_load_dwordx2 %0, %1, %2 sc1" : "=v"(xv[i]) : "v"(o), "s"(xp + i * st));
    asm volatile("global_load_dwordx2 %0, %1, %2 sc1" : "=v"(xv[4]) : "v"(o2), "s"(xp));
  }
};

DEVI void p2_gemm(f32x4 (&acc)[2][5], u32x2 (&xv)[5], unsigned lds0, const char* ob, int layer, int wave, int lane MK_ARG) {
  ResX<9, 0, 5, P2_W, 9, P2_X0, 0, 0u> gm(acc);
  gm.setup(lds0, ob, wave, lane, 0u, 0u);
  unsigned a = (gm.xo & ~1023u) | ((gm.xo & 1023u) >> 2);
  gm.xo += 8192u;
  gm.fa = lds0 + (unsigned)(LW_OFF + (LW_T + layer) * 4);
  gm.fv = vconst(1u);
  asm volatile("" : "+v"(a), "+v"(gm.xo), "+v"(gm.fa));
  const char* pp[2] = {gm.xp[0] - wave * 192, gm.xp[2] - wave * 192};
  place();
  bar();
  MK(6);
  unsigned e[2][4];
#pragma unroll
  for (int d = 0; d < 8; ++d)
    asm volatile("global_load_dword %0, %1, %2 offset:%3 sc1" : "=v"(e[d / 4][d % 4]) : "v"(a), "s"(pp[d / 4]), "n"((d % 4) * 256));
  gm.template prox<1>(std::make_integer_sequence<int, P2_X0 - 1>{});
  gm.prow();
  gm.template init<6 + 2 * P2_X0>(xv);
  asm volatile("s_waitcnt vmcnt(%8)"
               : "+v"(e[0][0]), "+v"(e[0][1]), "+v"(e[0][2]), "+v"(e[0][3]), "+v"(e[1][0]), "+v"(e[1][1]), "+v"(e[1][2]), "+v"(e[1][3])
               : "n"(2 * P2_X0 - 2));
  gm.xf[0][0] = __builtin_bit_cast(s16x8, u32x4{e[0][0], e[0][1], e[0][2], e[0][3]});
  gm.xf[0][1] = __builtin_bit_cast(s16x8, u32x4{e[1][0], e[1][1], e[1][2], e[1][3]});
  __builtin_amdgcn_sched_barrier(0);
  gm.run();
}

struct ResOut {
  const char *xp[3], *sp;
  unsigned xo, so;
  DEVI void setup(char* xim, char* xs, u16* out, bool fin, int rb, int ct, int wave, int lane) {
    const int g = lane >> 4, rl = wave * 16 + (lane & 15);
    xp[0] = uni_ptr(fin ? (const char*)(out + (size_t)rb * 64 * D_ + ct * 72) : xim + (size_t)(rb * 72 + ct * 9) * 1024);
    xp[1] = xp[0] + (fin ? 64 : 4096);
    xp[2] = xp[0] + (fin ? 128 : 8192);
    sp = uni_ptr(xs + (size_t)(rb * 8 + ct) * 512);
    xo = (unsigned)(((g & 1) * 2 + (g >> 1)) * (fin ? 16 : 1024) + rl * (fin ? D_ * 2 : 16));
    so = (unsigned)(rl * 8);
    asm volatile("" : "+v"(xo), "+v"(so), "+s"(xp[0]), "+s"(xp[1]), "+s"(xp[2]), "+s"(sp));
  }
  DEVI void run(const f32x4 (&a)[5], bool fin) {
    u32x2 y[5];
    u32x4 v[3];
#pragma unroll
    for (int nt = 0; nt < 5; ++nt) y[nt] = u32x2{pack2(a[nt][0], a[nt][1]), pack2(a[nt][2], a[nt][3])};
#pragma unroll
    for (int i = 0; i < 3; ++i) {
      const auto s0 = __builtin_amdgcn_permlane16_swap(y[2 * i][0], y[i < 2 ? 2 * i + 1 : 4][0], false, false);
      const auto s1 = __builtin_amdgcn_permlane16_swap(y[2 * i][1], y[i < 2 ? 2 * i + 1 : 4][1], false, false);
      v[i] = u32x4{s0[0], s1[0], s0[1], s1[1]};
    }
    if (fin) {
      asm volatile("global_store_dwordx4 %0, %1, %4\n\tglobal_store_dwordx4 %0, %2, %5\n\ts_mov_b64 exec, 0xffff\n\tglobal_store_dwordx4 %0, %3, %6\n\t"
                   "s_mov_b64 exec, -1\n\ts_nop 0" ::"v"(xo), "v"(v[0]), "v"(v[1]), "v"(v[2]), "s"(xp[0]), "s"(xp[1]), "s"(xp[2])
                   : "memory");
      return;
    }
    asm volatile("global_store_dwordx4 %0, %1, %4 sc1\n\tglobal_store_dwordx4 %0, %2, %5 sc1\n\ts_mov_b64 exec, 0xffff\n\t"
                 "global_store_dwordx4 %0, %3, %6 sc1\n\ts_mov_b64 exec, -1\n\ts_nop 0" ::"v"(xo), "v"(v[0]), "v"(v[1]), "v"(v[2]),
                 "s"(xp[0]), "s"(xp[1]), "s"(xp[2])
                 : "memory");
    const u32x4 one4 = {vconst(0x3f803f80u), vconst(0x3f803f80u), vconst(0x3f803f80u), vconst(0x3f803f80u)};
    const bool lo = __builtin_amdgcn_inverse_ballot_w64(0xffffull);
    v[2] = u32x4{lo ? v[2][0] : 0u, lo ? v[2][1] : 0u, lo ? v[2][2] : 0u, lo ? v[2][3] : 0u};
    f32x4 s = {0.f, 0.f, 0.f, 0.f}, q = s;
#pragma unroll
    for (int i = 0; i < 3; ++i) {
      const s16x8 u = __builtin_bit_cast(s16x8, v[i]);
      q = __builtin_amdgcn_mfma_f32_16x16x32_bf16(u, u, q, 0, 0, 0);
      s = __builtin_amdgcn_mfma_f32_16x16x32_bf16(__builtin_bit_cast(s16x8, one4), u, s, 0, 0, 0);
    }
    float q0 = q[0], q1 = q[1], q2 = q[2], q3 = q[3];
    const bool b0 = __builtin_amdgcn_inverse_ballot_w64(0xaaaaaaaaaaaaaaaaull), b1 = __builtin_amdgcn_inverse_ballot_w64(0xccccccccccccccccull);
    q0 = b0 ? q1 : q0;
    q2 = b0 ? q3 : q2;
    asm("" : "+v"(q0), "+v"(q2));
    const f32x2 sq = {s[0], b1 ? q2 : q0};
    asm volatile("s_mov_b64 exec, %3\n\tglobal_store_dwordx2 %0, %1, %2 sc1\n\ts_mov_b64 exec, -1" ::"v"(so), "v"(sq), "s"(sp), "s"(0xf0000f0000f0000full) : "memory");
  }
};

using K3C = GemmCfg<128, 144>;
constexpr int K3_Z1_OFF = K3C::LDS_BYTES;
constexpr int K3_C2_OFF = K3_Z1_OFF + K3C::XB;
constexpr int K3_ZC_OFF = K3_C2_OFF + 2048;
constexpr int K3_DM_OFF = K3_ZC_OFF + 1024;
constexpr int K3_ST_OFF = K3_Z1_OFF + 4 * 5120;
constexpr int K3_C1_OFF = K3_ST_OFF + 2048;
constexpr int K3_BS_OFF = K3_C1_OFF + 576;
static_assert(K3_BS_OFF + 512 <= LW_OFF, "K3 LDS");
static_assert(P5_W + 19 * 1024 == K3C::SB && K3C::SB == 34 * 1024, "w_out ring");

DEVI void red4(float (&t)[4], float& s, float& q) {
#pragma unroll
  for (int j = 0; j < 2; ++j) {
    const auto p = __builtin_amdgcn_permlane16_swap(__float_as_uint(t[j]), __float_as_uint(t[2 + j]), false, false);
    const unsigned v = __float_as_uint(__uint_as_float(p[0]) + __uint_as_float(p[1]));
    const auto w = __builtin_amdgcn_permlane32_swap(v, v, false, false);
    t[j] = __uint_as_float(w[0]) + __uint_as_float(w[1]);
    asm volatile("" : "+v"(t[j]));
  }
  s = t[0];
  q = t[1];
}
DEVI void red8(const f32x4& a, const f32x4& b, float& s, float& q) {
  float t[4];
#pragma unroll
  for (int j = 0; j < 4; ++j) {
    t[j] = a[j] + b[j];
    asm volatile("" : "+v"(t[j]));
  }
  red4(t, s, q);
}
DEVI void red16(const f32x4& a, const f32x4& b, const f32x4& c, const f32x4& d, float& s, float& q) {
  float t[4];
#pragma unroll
  for (int j = 0; j < 4; ++j) {
    float u = a[j] + b[j], v = c[j] + d[j];
    asm volatile("" : "+v"(u), "+v"(v));
    t[j] = u + v;
    asm volatile("" : "+v"(t[j]));
  }
  red4(t, s, q);
}

constexpr int b_k(int s) { return s == 0 ? 0 : (s < 3 ? 6 + s : (s == 3 ? 1 : (s < 6 ? 1 + s : (s == 6 ? 4 : s - 5)))); }
constexpr int b_xp(int s) { return s == 0 ? 0 : (b_k(s) >= 4 ? (b_k(s) % 5) / 2 : b_xp(s - 1)); }
template <bool B>
struct UMath {
  static constexpr int D = 4, R = 6, NV = 162, SB = K3C::SB, P = K3C::P;
  s16x8 wf[R], xf[2][2];
  s16x8 (&xc)[4][2][2];
  f32x4 (&acc)[2][9];
  f32x4 rc[4][2];
  f32x2 cs[9];
  unsigned ra, ca;
  template <int I>
  DEVI void cread() {
    if constexpr (I < 8) asm volatile("ds_read_b128 %0, %1 offset:%2" : "=v"(rc[I / 2][I % 2]) : "v"(ra), "n"((I / 2) * 512 + (I % 2) * 16));
    else if constexpr (I < 17) asm volatile("ds_read_b64 %0, %1 offset:%2" : "=v"(cs[I - 8]) : "v"(ca), "n"((I - 8) * 128));
  }
  unsigned xa, wa;
  unsigned fa, fw, fv;
  DEVI UMath(f32x4 (&a)[2][9], s16x8 (&x)[4][2][2]) : xc(x), acc(a) {}
  static constexpr int wslot(int s) { return B ? (s + 1) % 4 : s % 4; }
  static constexpr int xslot(int s) { return B ? b_k(s) % 5 : s % 5; }
  static constexpr int xpair(int s) { return B ? b_xp(s) : xslot(s) / 2; }
  static constexpr bool res(int q) { return !B || b_k(q / 2) >= 4; }
  static constexpr bool chk(int s) { return s >= 3 && s < 7; }
  static constexpr bool xop(int v) { return v + D < NV && (v + D) % 9 < 2 && res((v + D) / 9); }
  static constexpr int ops1(int v) { return (xop(v) || (B && ((v % 18 == 9 && chk(v / 18)) || (v % 18 == 0 && v > 0)))) ? 1 : 0; }
  static constexpr int newer(int V) {
    int v0 = (V % 9 == 8 ? V : V + 1) - D, n = 0;
    if (v0 >= 0 && !(V % 9 == 0 && res(V / 9))) n += ops1(v0);
    for (int v = v0 + 1; v < V; ++v)
      if (v >= 0) n += (v + D < NV ? 1 : (B ? 0 : 4)) + ops1(v);
    return n;
  }
  template <int V>
  DEVI void wread() {
    asm volatile("ds_read_b128 %0, %1 offset:%2" : "=v"(wf[V % R]) : "v"(wa), "n"((wslot(V / 18) & 1) * SB + ((V % 18) / 9) * 32 * P + (V % 9) * 32));
  }
  template <int Q, int M>
  DEVI void xread() {
    asm volatile("ds_read_b128 %0, %1 offset:%2" : "=v"(xf[Q & 1][M]) : "v"(xa), "n"((xslot(Q / 2) & 1) * SB + (Q & 1) * 8192 + M * (B ? 256 : 64)));
  }
  template <int V, int M_>
  DEVI void mf() {
    constexpr int M = ((V % 9) & 1) ? 1 - M_ : M_, q = V / 9;
    if constexpr (!B) acc[M][V % 9] = __builtin_amdgcn_mfma_f32_16x16x32_bf16(xf[q & 1][M], wf[V % R], acc[M][V % 9], 0, 0, 0);
    else if constexpr (res(q)) acc[M][V % 9] = __builtin_amdgcn_mfma_f32_16x16x32_bf16(wf[V % R], xf[q & 1][M], acc[M][V % 9], 0, 0, 0);
    else acc[M][V % 9] = __builtin_amdgcn_mfma_f32_16x16x32_bf16(wf[V % R], xc[b_k(q / 2)][q & 1][M], acc[M][V % 9], 0, 0, 0);
    __builtin_amdgcn_sched_barrier(0);
  }
  template <int V>
  DEVI void unit() {
    constexpr int U = V % 18, s = V / 18;
    static_assert(newer(V) <= 15, "lgkmcnt range");
    if constexpr ((V % 9) % 2 == 0 && V > 0) asm volatile("s_waitcnt lgkmcnt(%0)" ::"n"(newer(V)));
    __builtin_amdgcn_sched_barrier(0);
    mf<V, 0>();
    if constexpr (V + D < NV) wread<V + D>();
    if constexpr (!B && V + D >= NV) {
      cread<(V + D - NV) * 4>();
      cread<(V + D - NV) * 4 + 1>();
    }
    if constexpr (s < 8 && U == 13 && (wslot(s + 1) >> 1) != (wslot(s) >> 1))
      asm volatile("v_add_u32 %0, %1, %0" : "+v"(wa) : "n"(((wslot(s + 1) >> 1) - (wslot(s) >> 1)) * 2 * SB));
    __builtin_amdgcn_sched_barrier(0);
    mf<V, 1>();
    if constexpr (xop(V)) xread<(V + D) / 9, (V + D) % 9>();
    if constexpr (!B && V + D >= NV) {
      cread<(V + D - NV) * 4 + 2>();
      cread<(V + D - NV) * 4 + 3>();
      if constexpr (V == NV - 1) cread<16>();
    }
    if constexpr (s < 8) {
      if constexpr (U == 11 && xpair(s + 1) != xpair(s)) asm volatile("v_add_u32 %0, %1, %0" : "+v"(xa) : "n"((xpair(s + 1) - xpair(s)) * 2 * SB));
      if constexpr (!B && U == 13) __builtin_amdgcn_s_barrier();
      if constexpr (B && U == 9 && chk(s)) asm volatile("ds_read_u8 %0, %1 offset:%2" : "+v"(fv) : "v"(fa), "n"(s == 6 ? 0 : 8));
      unsigned n = 1u << 18;
      if constexpr (B && U == 13 && chk(s)) {
        asm volatile(
            "v_cmp_gt_u32 vcc, %3, %0\n\ts_cbranch_vccnz .Lw%=\n\t.subsection 1\n.Lw%=:\n\tds_read_u8 %0, %2 offset:%4\n\ts_waitcnt lgkmcnt(0)\n\t"
            "v_cmp_gt_u32 vcc, %3, %0\n\t" SAFE_OUT(".Lr%=") "s_cbranch_vccnz .Lw%=\n\ts_branch .Lr%=\n\t.subsection 0\n.Lr%=:"
            : "+v"(fv), NOP_(n)
            : "v"(fa), "n"(s == 6 ? 4 : s + 2), "n"(s == 6 ? 0 : 8)
            : "vcc", "scc");
      }
    }
    if constexpr (B && U == 15) asm volatile("v_mov_b32 %0, %1" : "+v"(fv) : "n"(s + 1));
    if constexpr (B && ((U == 0 && s > 0) || V == NV - 1)) asm volatile("ds_write_b8 %0, %1" ::"v"(fw), "v"(fv));
    __builtin_amdgcn_sched_barrier(0);
  }
  template <int... V>
  DEVI void units(std::integer_sequence<int, V...>) {
    (unit<V>(), ...);
  }
  DEVI void first() {
    if constexpr (!B) {
      xread<0, 0>();
      xread<0, 1>();
    }
    wread<0>();
    wread<1>();
    wread<2>();
    wread<3>();
    asm volatile("s_waitcnt lgkmcnt(0)");
  }
  DEVI void run() {
    units(std::make_integer_sequence<int, NV>{});
#pragma unroll
    for (int m = 0; m < 2; ++m)
#pragma unroll
      for (int n = 0; n < 9; ++n) asm volatile("" : "+v"(acc[m][n]));
  }
};

struct CMath {
  static constexpr int D = 4, R = 6, SB = K3C::SB;
  s16x8 wf[R], xf[2][2];
  f32x4 (&acc)[2][9];
  unsigned wa[2], xa[2][2];
  DEVI CMath(f32x4 (&a)[2][9]) : acc(a) {}
  static constexpr int xop(int u, bool last) { return (u + D) % 9 < 2 && !(last && u + D >= 18) ? 1 : 0; }
  static constexpr int newer(int U, bool last) {
    int v0 = (U % 9 == 8 ? U : U + 1) - D, n = 0;
    if (U % 9 != 0) n += xop((v0 + 18) % 18, last && v0 >= 0);
    for (int v = v0 + 1; v < U; ++v) n += (last && v + D >= 18 ? 0 : 1) + xop((v + 18) % 18, last && v >= 0);
    return n;
  }
  template <int SL, int U>
  DEVI void wread() {
    asm volatile("ds_read_b128 %0, %1 offset:%2" : "=v"(wf[U % R]) : "v"(wa[SL / 2]), "n"((SL & 1) * SB + U * 1024));
  }
  template <int SL, int H, int M>
  DEVI void xread() {
    asm volatile("ds_read_b128 %0, %1 offset:%2" : "=v"(xf[H][M]) : "v"(xa[SL / 2][H]), "n"((SL & 1) * SB + M * 2048));
  }
  template <int U, int M_>
  DEVI void mf() {
    constexpr int M = ((U % 9) & 1) ? 1 - M_ : M_;
    acc[M][U % 9] = __builtin_amdgcn_mfma_f32_16x16x32_bf16(wf[U % R], xf[U / 9][M], acc[M][U % 9], 0, 0, 0);
    __builtin_amdgcn_sched_barrier(0);
  }
  template <int SL, bool BAR, bool LAST, int U>
  DEVI void unit() {
    constexpr int PU = (U + D) % 18, PS = U + D < 18 ? SL : (SL + 1) % 4;
    if constexpr ((U % 9) % 2 == 0) asm volatile("s_waitcnt lgkmcnt(%0)" ::"n"(newer(U, LAST)));
    __builtin_amdgcn_sched_barrier(0);
    mf<U, 0>();
    if constexpr (!(LAST && U + D >= 18)) wread<PS, PU>();
    __builtin_amdgcn_sched_barrier(0);
    mf<U, 1>();
    if constexpr (xop(U, LAST)) xread<PS, PU / 9, PU % 9>();
    if constexpr (BAR && U == 13) __builtin_amdgcn_s_barrier();
    __builtin_amdgcn_sched_barrier(0);
  }
  template <int SL, bool BAR, bool LAST, int... U>
  DEVI void units(std::integer_sequence<int, U...>) {
    (unit<SL, BAR, LAST, U>(), ...);
  }
  template <int SL, bool BAR = true, bool LAST = false>
  DEVI void stage() {
    units<SL, BAR, LAST>(std::make_integer_sequence<int, 18>{});
  }
  DEVI void setup(unsigned lds0, int wm, int lane) {
    const int g = lane >> 4, r = lane & 15;
#pragma unroll
    for (int i = 0; i < 2; ++i) {
      wa[i] = lds0 + (unsigned)(i * 2 * SB + K3C::XB + lane * 16);
#pragma unroll
      for (int h = 0; h < 2; ++h) xa[i][h] = lds0 + (unsigned)(i * 2 * SB + (wm * 32 + r) * 128 + (((4 * h + g) ^ ((r >> 1) & 7)) * 16));
    }
    asm volatile("" : "+v"(wa[0]), "+v"(wa[1]), "+v"(xa[0][0]), "+v"(xa[0][1]), "+v"(xa[1][0]), "+v"(xa[1][1]));
  }
  DEVI void first() {
    xread<2, 0, 0>();
    xread<2, 0, 1>();
    wread<2, 0>();
    wread<2, 1>();
    wread<2, 2>();
    wread<2, 3>();
    asm volatile("s_waitcnt lgkmcnt(0)");
  }
};

constexpr int ZQ = 4;
constexpr int NR1 = 3;
DEVI void p3_g0(char* smem, char* __restrict__ ws, int layer, int mi, int ni, int wm, int lane MK_ARG) {
  constexpr int SB = K3C::SB;
  const unsigned lds0 = (unsigned)(size_t)smem;
  const int g = lane >> 4, r = lane & 15;
  const unsigned lw = lds0 + (unsigned)(LW_OFF + layer * 4), me = 1u << (8 * wm);
  f32x4 ac[2][9];
#pragma unroll
  for (int mt = 0; mt < 2; ++mt)
#pragma unroll
    for (int j = 0; j < 9; ++j) ac[mt][j] = f32x4{0.f, 0.f, 0.f, 0.f};
  unsigned ra = lds0 + (unsigned)(K3_ST_OFF + (wm * 32 + g * 8) * 4), ca = lds0 + (unsigned)(K3_C2_OFF + r * 8);
  asm volatile("" : "+v"(ra), "+v"(ca));
  s16x8 xc[4][2][2];
  UMath<false> gm(ac, xc);
  gm.ra = ra;
  gm.ca = ca;
  gm.xa = lds0 + (unsigned)(g * 2048 + (wm * 32 + 8 * (r >> 2) + (r & 3)) * 16);
  gm.wa = lds0 + (unsigned)(K3C::XB + (g * 8 + (r & 7)) * K3C::P + (r >> 3) * 16);
  unsigned adw = lw + LW_AD * 4, mev = vconst(me), po = (unsigned)(ni * 1024 + lane * 4);
  const char* zp = uni_ptr(ws + OFF_ZP + (size_t)layer * SZ_ZP + (size_t)mi * 16384 + wm * 256);
  asm volatile("" : "+v"(gm.xa), "+v"(gm.wa), "+v"(adw), "+v"(po));
  asm volatile("" : "+s"(zp));
  place();
  bar();
  gm.first();
  gm.run();
  asm volatile("s_mov_b64 exec, 1\n\tds_add_u32 %0, %1\n\ts_mov_b64 exec, -1" ::"v"(adw), "v"(mev) : "memory");
  MK(11);
  f32x2 z[9][4];
  f32x2 sm[4], sq[4];
  {
    f32x4 (&rc)[4][2] = gm.rc;
    f32x2 (&cs)[9] = gm.cs;
    asm volatile("s_waitcnt lgkmcnt(1)"
                 : "+v"(rc[0][0]), "+v"(rc[0][1]), "+v"(rc[1][0]), "+v"(rc[1][1]), "+v"(rc[2][0]), "+v"(rc[2][1]), "+v"(rc[3][0]), "+v"(rc[3][1]));
#pragma unroll
    for (int j = 0; j < 9; ++j) asm volatile("" : "+v"(cs[j]));
    auto RC = [&](int k, int i) { return f32x2{rc[k][i / 2][(i % 2) * 2], rc[k][i / 2][(i % 2) * 2 + 1]}; };
#pragma unroll
    for (int j = 0; j < 9; ++j) {
      f32x2 u[4], w[4];
#pragma unroll
      for (int i = 0; i < 4; ++i) u[i] = __builtin_elementwise_fma(RC(0, i), cs[j], f32x2{ac[i / 2][j][(i % 2) * 2], ac[i / 2][j][(i % 2) * 2 + 1]});
#pragma unroll
      for (int i = 0; i < 4; ++i) w[i] = u[i] * u[i];
#pragma unroll
      for (int i = 0; i < 4; ++i) w[i] = __builtin_elementwise_fma(w[i], RC(3, i), RC(2, i));
#pragma unroll
      for (int i = 0; i < 4; ++i) w[i] = u[i] * w[i];
      __builtin_amdgcn_sched_barrier(0);
#pragma unroll
      for (int i = 0; i < 4; ++i) w[i] = f32x2{__builtin_amdgcn_exp2f(w[i][0]), __builtin_amdgcn_exp2f(w[i][1])};
      __builtin_amdgcn_sched_barrier(0);
#pragma unroll
      for (int i = 0; i < 4; ++i) w[i] = __builtin_elementwise_fma(w[i], RC(1, i), RC(1, i));
      __builtin_amdgcn_sched_barrier(0);
#pragma unroll
      for (int i = 0; i < 4; ++i) w[i] = f32x2{__builtin_amdgcn_rcpf(w[i][0]), __builtin_amdgcn_rcpf(w[i][1])};
      __builtin_amdgcn_sched_barrier(0);
#pragma unroll
      for (int i = 0; i < 4; ++i) z[j][i] = u[i] * w[i];
#pragma unroll
      for (int i = 0; i < 4; ++i) {
        if (j == 1) sm[i] = z[0][i] + z[1][i];
        if (j > 1) sm[i] += z[j][i];
      }
#pragma unroll
      for (int i = 0; i < 4; ++i) sq[i] = j ? __builtin_elementwise_fma(z[j][i], z[j][i], sq[i]) : z[j][i] * z[j][i];
      asm volatile("" : "+v"(z[j][0]), "+v"(z[j][1]), "+v"(z[j][2]), "+v"(z[j][3]));
      __builtin_amdgcn_sched_barrier(0);
    }
  }
  f32x4 e;
  {
    float v[4][4], d[2][4], f[4];
#pragma unroll
    for (int i = 0; i < 4; ++i) {
      v[i][0] = sm[i][0];
      v[i][1] = sq[i][0];
      v[i][2] = sm[i][1];
      v[i][3] = sq[i][1];
    }
    asm volatile("s_nop 1" : "+v"(v[3][1]), "+v"(v[3][3]), "+v"(v[2][1]), "+v"(v[2][3]));
#pragma unroll
    for (int i = 0; i < 2; ++i)
#pragma unroll
      for (int c = 0; c < 4; ++c)
        asm volatile("v_add_f32_dpp %0, %1, %1 row_ror:8 row_mask:0xf bank_mask:0x3\n\tv_add_f32_dpp %0, %2, %2 row_ror:8 row_mask:0xf bank_mask:0xc"
                     : "=&v"(d[i][c])
                     : "v"(v[i][c]), "v"(v[i + 2][c]));
#pragma unroll
    for (int c = 0; c < 4; ++c)
      asm volatile("v_add_f32_dpp %0, %1, %1 row_shl:4 row_mask:0xf bank_mask:0x5\n\tv_add_f32_dpp %0, %2, %2 row_shr:4 row_mask:0xf bank_mask:0xa"
                   : "=&v"(f[c])
                   : "v"(d[0][c]), "v"(d[1][c]));
    asm volatile(
        "v_add_f32_dpp %0, %0, %0 quad_perm:[1,0,3,2] row_mask:0xf bank_mask:0xf\n\t"
        "v_add_f32_dpp %1, %1, %1 quad_perm:[1,0,3,2] row_mask:0xf bank_mask:0xf\n\t"
        "v_add_f32_dpp %2, %2, %2 quad_perm:[1,0,3,2] row_mask:0xf bank_mask:0xf\n\t"
        "v_add_f32_dpp %3, %3, %3 quad_perm:[1,0,3,2] row_mask:0xf bank_mask:0xf\n\t"
        "v_add_f32_dpp %0, %0, %0 quad_perm:[2,3,0,1] row_mask:0xf bank_mask:0xf\n\t"
        "v_add_f32_dpp %1, %1, %1 quad_perm:[2,3,0,1] row_mask:0xf bank_mask:0xf\n\t"
        "v_add_f32_dpp %2, %2, %2 quad_perm:[2,3,0,1] row_mask:0xf bank_mask:0xf\n\t"
        "v_add_f32_dpp %3, %3, %3 quad_perm:[2,3,0,1] row_mask:0xf bank_mask:0xf"
        : "+v"(f[0]), "+v"(f[1]), "+v"(f[2]), "+v"(f[3]));
    e = f32x4{f[0], __uint_as_float(__float_as_uint(f[1]) & 0x7fffffffu), f[2], __uint_as_float(__float_as_uint(f[3]) & 0x7fffffffu)};
  }
  asm volatile("s_mov_b64 exec, %3\n\tglobal_store_dwordx4 %0, %1, %2 sc1\n\ts_mov_b64 exec, -1\n\ts_nop 0" ::"v"(po), "v"(e), "s"(zp),
               "s"(0x1111111111111111ull)
               : "memory");
  asm volatile("s_mov_b64 exec, 1\n\tds_add_u32 %0, %1\n\ts_mov_b64 exec, -1" ::"v"(adw), "v"(mev) : "memory");
  __builtin_amdgcn_s_setprio(2);
  MK(12);
  const char* dst = uni_ptr(ws + OFF_Z2 + (size_t)layer * SZ_Z + (size_t)ni * S_ * 288 + (size_t)(mi * 4 + wm) * 9216 + 4096);
  const unsigned lane16 = (unsigned)lane * 16u;
  u32x4 wb[9];
  {
    static_assert(b_k(7) == 2 && b_k(8) == 3, "order");
    const char* wp = uni_ptr(ws + OFF_PI + (size_t)ni * (18 * K3C::WB) + 11 * K3C::WB + wm * 9216 + 4096);
#pragma unroll
    for (int j = 0; j < 8; ++j) asm volatile("global_load_dwordx4 %0, %1, %2 offset:%3 sc1" : "=v"(wb[j]) : "v"(lane16), "s"(wp), "n"(j * 1024 - 4096) : "memory");
    asm volatile("global_load_dwordx4 %0, %1, %2 sc1" : "=v"(wb[8]) : "v"(lane16 + 4096u), "s"(wp) : "memory");
  }
  u32x4 y[9];
  {
    const unsigned src = (unsigned)(g * 4096 + r * 16);
    unsigned zw = lds0 + (unsigned)(K3_ZC_OFF + (wm * 32 + 2 * r + (g & 1)) * 4), zr = lds0 + (unsigned)(K3_ZC_OFF + (wm * 32 + g * 8) * 4);
    asm volatile("" : "+v"(zw), "+v"(zr));
    f32x4 a, b, c, d;
    __builtin_amdgcn_s_sleep(6);
#pragma unroll 1
    for (unsigned spin = 0; spin < SPIN_MEM; ++spin) {
      asm volatile(
          "global_load_dwordx4 %0, %4, %5 sc1\n\t"
          "global_load_dwordx4 %1, %4, %5 offset:1024 sc1\n\t"
          "global_load_dwordx4 %2, %4, %5 offset:2048 sc1\n\t"
          "global_load_dwordx4 %3, %4, %5 offset:3072 sc1\n\t"
          "s_waitcnt vmcnt(0)"
          : "=&v"(a), "=&v"(b), "=&v"(c), "=&v"(d)
          : "v"(src), "s"(zp)
          : "memory");
      const unsigned bad = (__float_as_uint(a[1]) | __float_as_uint(a[3]) | __float_as_uint(b[1]) | __float_as_uint(b[3]) |
                            __float_as_uint(c[1]) | __float_as_uint(c[3]) | __float_as_uint(d[1]) | __float_as_uint(d[3])) >> 31;
      if (!__any(bad)) break;
    }
    MK(13);
    float s, q, zmb, zi;
    red16(a, b, c, d, s, q);
    ln_finish(s, q, 1.0f / (float)FH_, zmb, zi);
    f32x4 R[2], M[2];
    asm volatile(
        "ds_write2st64_b32 %4, %5, %6 offset1:2\n\t"
        "ds_read_b128 %0, %7\n\tds_read_b128 %2, %7 offset:512\n\tds_read_b128 %1, %7 offset:16\n\tds_read_b128 %3, %7 offset:528\n\t"
        "s_waitcnt lgkmcnt(0)"
        : "=&v"(R[0]), "=&v"(R[1]), "=&v"(M[0]), "=&v"(M[1])
        : "v"(zw), "v"(zi), "v"(-zmb * zi), "v"(zr)
        : "memory");
    MK(28);
    auto st = [&](int j) {
      if (j < 8) asm volatile("global_store_dwordx4 %0, %1, %2 offset:%3 sc1\n\ts_nop 1" ::"v"(lane16), "v"(y[j]), "s"(dst), "n"(j * 1024 - 4096) : "memory");
      else asm volatile("global_store_dwordx4 %0, %1, %2 sc1\n\ts_nop 1" ::"v"(lane16 + 4096u), "v"(y[j]), "s"(dst) : "memory");
    };
#pragma unroll
    for (int j = 0; j < 9; ++j) {
      {
        float t[8];
#pragma unroll
        for (int k = 0; k < 8; ++k) {
          t[k] = __builtin_fmaf(z[j][k / 2][k % 2], R[k / 4][k % 4], M[k / 4][k % 4]);
          asm volatile("" : "+v"(t[k]));
        }
        y[j] = u32x4{pack2(t[0], t[1]), pack2(t[2], t[3]), pack2(t[4], t[5]), pack2(t[6], t[7])};
      }
      st(j);
      __builtin_amdgcn_sched_barrier(0);
    }
    MK(29);
  }
  lds_inc(lw + LW_AD * 4, me);
  MK(24);
  __builtin_amdgcn_s_setprio(2);
  lds_spin4<2>(lw + LW_S * 4, 4u + (unsigned)(wm >> 1));
  {
    const unsigned fa = lds0 + (unsigned)((wm >> 1) * SB + K3C::XB + (wm & 1) * 9216) + lane16;
#pragma unroll
    for (int j = 0; j < 9; ++j) {
      asm volatile("ds_write_b128 %0, %1 offset:%2" ::"v"(fa), "v"(wb[j]), "n"(j * 1024) : "memory");
    }
    lds_inc(lw + LW_AD * 4, me);
  }
  announce(lw + LW_Z * 4, 4u, ws, ((mi >= ZQ) ? CN_Z2 : CN_Z) + layer, lane * 16 + ni, lane < 16);
  lds_inc(lw + LW_AD * 4, me);
  MK(27);
  f32x4 acc[2][9];
  {
    float bs[2];
    asm volatile("ds_read2_b32 %0, %1 offset1:16\n\ts_waitcnt lgkmcnt(0)"
                 : "=v"(*reinterpret_cast<f32x2*>(bs))
                 : "v"(lds0 + (unsigned)(K3_BS_OFF + (wm * 32 + r) * 4))
                 : "memory");
    lds_spin4<2>(lw + LW_S * 4, 6u + (unsigned)(wm >> 1));
    const unsigned fa = lds0 + (unsigned)((2 + (wm >> 1)) * SB + K3C::XB + (wm & 1) * 9216) + lane16;
#pragma unroll
    for (int j = 0; j < 9; ++j) {
      asm volatile("ds_write_b128 %0, %1 offset:%2" ::"v"(fa), "v"(y[j]), "n"(j * 1024) : "memory");
    }
    lds_inc(lw + LW_AD * 4, me);
#pragma unroll
    for (int mt = 0; mt < 2; ++mt) {
      f32x4 b4 = {bs[mt], bs[mt], bs[mt], bs[mt]};
      asm volatile("" : "+v"(b4));
#pragma unroll
      for (int j = 0; j < 9; ++j) acc[mt][j] = b4;
    }
  }
  u32x4 z1[9];
  const char* hp = uni_ptr(ws + OFF_H + (size_t)layer * SZ_Z + (size_t)((mi * 2 + (wm >> 1)) * 288 + ni * 18) * 1024);
  unsigned ho = (unsigned)((lane >> 5) * 1024 + ((wm & 1) * 32 + (lane & 31)) * 16);
  unsigned zb = lds0 + (unsigned)(wm * 4096) + lane16, zf = lw + (unsigned)(LW_S * 4 + wm);
  asm volatile("" : "+v"(ho), "+v"(zb), "+v"(zf));
  asm volatile("" : "+s"(hp));
  {
    CMath gm(acc);
    gm.setup(lds0, wm, lane);
    unsigned za = lds0 + (unsigned)(K3_Z1_OFF + wm * 5120) + lane16;
    asm volatile("" : "+v"(za));
    MK(15);
    lds_spin4<2>(lw + LW_L * 4, 8u);
    lds_spin4(lw + LW_AD * 4, 6u);
    MK(17);
    gm.first();
    __builtin_amdgcn_s_setprio(0);
    gm.stage<2, false>();
    gm.stage<3>();
    place();
#pragma unroll 1
    for (int t = 0; t < 7; ++t) {
      gm.stage<0>();
#pragma unroll
      for (int j = 0; j < NR1; ++j) asm volatile("ds_read_b128 %0, %1 offset:%2" : "=v"(z1[j]) : "v"(za), "n"(j * 1024));
      gm.stage<1>();
#pragma unroll
      for (int j = 0; j < NR1; ++j) asm volatile("" : "+v"(z1[j]));
      gm.stage<2>();
      gm.stage<3>();
    }
    gm.stage<0>();
    gm.stage<1, false, true>();
#pragma unroll
    for (int j = 0; j < 18; ++j) asm volatile("" : "+v"(acc[j / 9][j % 9]));
  }
  MK(16);
  if (NR1 < 5) lds_inc(lw + LW_AD * 4, me);
  unsigned zv;
  u32x4 zl[4];
#pragma unroll
  for (int j = 0; j < 9; ++j) {
    const bool sec = j >= NR1 + 3;
    if (j == NR1 || j == NR1 + 4) {
      unsigned n = 1u << 18;
      asm volatile(
          "s_waitcnt lgkmcnt(0)\n\tv_cmp_gt_u32 vcc, %8, %0\n\ts_cbranch_vccnz .Lw%=\n\t.subsection 1\n.Lw%=:\n\tds_read_u8 %0, %6\n\tds_read_b128 %2, %7 offset:%9\n\t"
          "ds_read_b128 %3, %7 offset:%9+1024\n\tds_read_b128 %4, %7 offset:%9+2048\n\tds_read_b128 %5, %7 offset:%9+3072\n\ts_waitcnt lgkmcnt(0)\n\t"
          "v_cmp_gt_u32 vcc, %8, %0\n\t" SAFE_OUT(".Lr%=") "s_cbranch_vccnz .Lw%=\n\ts_branch .Lr%=\n\t.subsection 0\n.Lr%=:"
          : "+v"(zv), NOP_(n), "+v"(zl[0]), "+v"(zl[1]), "+v"(zl[2]), "+v"(zl[3])
          : "v"(zf), "v"(zb), "n"(sec ? 11 : 10), "n"(sec ? SB : 0)
          : "memory", "vcc", "scc");
#pragma unroll
      for (int k = 0; k < 4; ++k)
        if (j + k < 9) z1[j + k] = zl[k];
    }
    if (j == NR1 - 1 || (j == NR1 + 3 && j < 8))
      asm volatile("ds_read_u8 %0, %5\n\tds_read_b128 %1, %6 offset:%7\n\tds_read_b128 %2, %6 offset:%7+1024\n\tds_read_b128 %3, %6 offset:%7+2048\n\t"
                   "ds_read_b128 %4, %6 offset:%7+3072"
                   : "=&v"(zv), "=&v"(zl[0]), "=&v"(zl[1]), "=&v"(zl[2]), "=&v"(zl[3])
                   : "v"(zf), "v"(zb), "n"(sec ? SB : 0)
                   : "memory");
    asm volatile("" : "+v"(z1[j]));
    u32x2 h[2];
#pragma unroll
    for (int mt = 0; mt < 2; ++mt) {
      const f32x4 a = acc[mt][j];
      h[mt] = u32x2{pack2v(f32x2{bf_lo(z1[j][2 * mt]), bf_hi(z1[j][2 * mt])} * f32x2{a[0], a[1]}),
                    pack2v(f32x2{bf_lo(z1[j][2 * mt + 1]), bf_hi(z1[j][2 * mt + 1])} * f32x2{a[2], a[3]})};
    }
    const auto s0 = __builtin_amdgcn_permlane16_swap(h[0][0], h[1][0], false, false);
    const auto s1 = __builtin_amdgcn_permlane16_swap(h[0][1], h[1][1], false, false);
    const u32x4 v = {s0[0], s1[0], s0[1], s1[1]};
    asm volatile("global_store_dwordx4 %0, %1, %2 offset:%3 sc1\n\ts_nop 1" ::"v"(ho), "v"(v), "s"(hp + (j / 2) * 4096), "n"((j % 2) * 2048) : "memory");
    __builtin_amdgcn_sched_barrier(0);
  }
  MK(18);
}

DEVI void k3_const(char* smem, char* ws, const u16* b_sp, int layer, int bid, int mi, int ni, int wm) {
  const int lane = lane_id(), l = lane < 54 ? lane : 53, p = l / 18;
  const char* kp = uni_ptr(ws + OFF_CSK + bid * 2048 + wm * 512);
  const unsigned q16 = (unsigned)(l - p * 18) * 16u;
  unsigned bs;
  f32x4 t;
  lds_spin((unsigned)(size_t)smem + (unsigned)(LW_OFF + (layer ? LW_T + 1 : LW_HELLO) * 4), layer ? 12u : 2u);
  asm volatile("global_load_ushort %0, %1, %2" : "=v"(bs) : "v"((unsigned)(lane & 31) * 2u), "s"(uni_ptr((const char*)(b_sp + mi * 128 + wm * 32))) : "memory");
  if (layer) {
    asm volatile("global_load_dwordx4 %0, %1, %2 sc1\n\ts_waitcnt vmcnt(0)" : "=&v"(t) : "v"(q16), "s"(kp) : "memory");
  } else {
    const char* cp = uni_ptr(ws + OFF_CSP + ni * 1152 + wm * 288);
    f32x4 a[6];
#pragma unroll
    for (int i = 0; i < 6; ++i)
      asm volatile("global_load_dwordx4 %0, %1, %2 sc1" : "=v"(a[i]) : "v"(q16 + (unsigned)(p * (6 * F_ * 4))), "s"(cp + i * (F_ * 4)) : "memory");
    asm volatile("s_waitcnt vmcnt(0)" : "+v"(a[0]), "+v"(a[1]), "+v"(a[2]), "+v"(a[3]), "+v"(a[4]), "+v"(a[5]));
    t = ((a[0] + a[1]) + (a[2] + a[3])) + (a[4] + a[5]);
#pragma unroll
    for (int j = 0; j < 4; ++j)
      t[j] += __int_as_float(__builtin_amdgcn_ds_bpermute((lane + 18) * 4, __float_as_int(t[j]))) +
              __int_as_float(__builtin_amdgcn_ds_bpermute((lane + 36) * 4, __float_as_int(t[j])));
  }
  asm volatile("" : "+v"(bs));
  const unsigned lds0 = (unsigned)(size_t)smem;
  f32x4 t0 = wm < 2 ? f32x4{t[0], t[0], t[1], t[1]} : t, t1 = wm < 2 ? f32x4{t[2], t[2], t[3], t[3]} : t;
  unsigned a0 = lds0 + (unsigned)(wm < 2 ? K3_C2_OFF + wm * 576 + lane * 32 : K3_C1_OFF + (wm - 2) * 288 + lane * 16), a1 = a0 + (wm < 2 ? 16u : 0u);
  unsigned ab = lds0 + (unsigned)(K3_BS_OFF + (wm * 32 + (lane & 31)) * 4);
  bs <<= 16;
  asm volatile("" : "+v"(t0), "+v"(t1), "+v"(a0), "+v"(a1), "+v"(ab), "+v"(bs));
  bar();
  asm volatile("ds_write_b32 %4, %5\n\ts_mov_b64 exec, 0x3ffff\n\tds_write_b128 %0, %2\n\tds_write_b128 %1, %3\n\ts_mov_b64 exec, -1" ::"v"(a0), "v"(a1),
               "v"(t0), "v"(t1), "v"(ab), "v"(bs)
               : "memory");
  if (layer == 0 && lane < 24) asm volatile("global_store_dwordx4 %0, %1, %2\n\ts_nop 1" ::"v"((unsigned)lane * 16u), "v"(t), "s"(kp) : "memory");
}

DEVI void p3_g1(char* smem, char* __restrict__ ws, int layer, int mi, int ni, int wm, int lane MK_ARG) {
  const unsigned lds0 = (unsigned)(size_t)smem;
  const int g = lane >> 4, r = lane & 15;
  const unsigned lw = lds0 + (unsigned)(LW_OFF + layer * 4);
  const unsigned xrd = lds0 + (unsigned)(g * 2048 + (wm * 32 + r) * 16);
  s16x8 xc[4][2][2];
  {
    const char* xs = uni_ptr(ws + OFF_XS + (size_t)layer * 2 * SZ_XS + (size_t)(mi * 2 + (wm >> 1)) * 4096 + (wm & 1) * 256);
    f32x4 a, b;
    auto pick = [&](int t) {
      bar();
      asm volatile(
          "ds_read_b128 %0, %4\n\tds_read_b128 %1, %4 offset:256\n\tds_read_b128 %2, %4 offset:8192\n\tds_read_b128 %3, %4 offset:8448\n\t"
          "s_waitcnt lgkmcnt(0)"
          : "=&v"(xc[t][0][0]), "=&v"(xc[t][0][1]), "=&v"(xc[t][1][0]), "=&v"(xc[t][1][1])
          : "v"(xrd + (unsigned)(t * K3C::SB))
          : "memory");
    };
    __builtin_amdgcn_s_setprio(2);
    pick(0);
    pick(1);
    asm volatile("global_load_dwordx4 %0, %2, %3 sc1\n\tglobal_load_dwordx4 %1, %2, %3 offset:512 sc1"
                 : "=&v"(a), "=&v"(b)
                 : "v"((unsigned)(g * 1024 + r * 16)), "s"(xs)
                 : "memory");
    pick(2);
    pick(3);
    bar();
    asm volatile("s_waitcnt vmcnt(0)" : "+v"(a), "+v"(b) : : "memory");
    float s, q, mb, rinv;
    red8(a, b, s, q);
    bar();
    asm volatile("" : "+v"(s), "+v"(q));
    ln_finish(s, q, 1.0f / (float)D_, mb, rinv);
    asm volatile("" : "+v"(mb), "+v"(rinv));
    bar();
    asm volatile("" : "+v"(mb), "+v"(rinv));
    const float k0 = -2.0f * 0.7978845608028654f * 1.4426950408889634f, k1 = k0 * 0.044715f;
    float sd = __builtin_amdgcn_rcpf(rinv), c0 = k0 * rinv, c1 = k1 * rinv * rinv * rinv;
    asm volatile("" : "+v"(sd));
    asm volatile("" : "+v"(c0));
    asm volatile("" : "+v"(c1));
    const float e0 = lane < 32 ? -mb : c0, e1 = lane < 32 ? sd : c1;
    asm volatile("ds_write2st64_b32 %0, %1, %2 offset1:2" ::"v"(
                     lds0 + (unsigned)(K3_ST_OFF + (g >> 1) * 1024 + (wm * 32 + 2 * r + (g & 1)) * 4)),
                 "v"(e0), "v"(e1)
                 : "memory");
    bar();
    asm volatile("s_waitcnt lgkmcnt(0)" ::: "memory");
    bar();
    __builtin_amdgcn_s_setprio(0);
  }
  f32x4 ac[2][9];
#pragma unroll
  for (int mt = 0; mt < 2; ++mt)
#pragma unroll
    for (int j = 0; j < 9; ++j) ac[mt][j] = f32x4{0.f, 0.f, 0.f, 0.f};
  {
    UMath<true> gm(ac, xc);
    gm.xa = xrd;
    gm.wa = lds0 + (unsigned)(K3C::XB + (g * 8 + (r & 7)) * K3C::P + (r >> 3) * 16);
    gm.fa = lw + (unsigned)(LW_AD * 4 + (lane & 3));
    gm.fw = lw + (unsigned)(LW_S * 4 + wm);
    gm.fv = 0u;
    asm volatile("" : "+v"(gm.fa), "+v"(gm.fw), "+v"(gm.fv), "+v"(gm.xa), "+v"(gm.wa));
    __builtin_amdgcn_s_sleep(40);
    lds_spin4<4>(lw + LW_L * 4, 4u);
    gm.first();
    place();
    lds_spin4(lw + LW_AD * 4, 2u);
    __builtin_amdgcn_s_setprio(1);
    gm.run();
    __builtin_amdgcn_s_setprio(0);
  }
  MK(14);
  f32x2 rc[4];
  f32x4 cs[9];
  const int l2 = lane_id();
  {
    const unsigned ra = lds0 + (unsigned)(K3_ST_OFF + (wm * 32 + (l2 & 15)) * 4), ca = lds0 + (unsigned)(K3_C1_OFF + (l2 & 48));
#pragma unroll
    for (int k = 0; k < 4; ++k) asm volatile("ds_read2_b32 %0, %1 offset1:16" : "=v"(rc[k]) : "v"(ra + (unsigned)(k * 512)) : "memory");
#pragma unroll
    for (int j = 0; j < 9; ++j) asm volatile("ds_read_b128 %0, %1 offset:%2" : "=v"(cs[j]) : "v"(ca), "n"(j * 64) : "memory");
    asm volatile("s_waitcnt lgkmcnt(0)" : "+v"(rc[0]), "+v"(rc[1]), "+v"(rc[2]), "+v"(rc[3]));
#pragma unroll
    for (int j = 0; j < 9; ++j) asm volatile("" : "+v"(cs[j]));
  }
  const unsigned za = lds0 + (unsigned)(K3_Z1_OFF + wm * 5120 + l2 * 16);
  u32x4 z1[9];
  unsigned zf;
#pragma unroll
  for (int t = 0; t < 18; ++t) {
    const int j = t / 2, mt = t % 2;
    float u[4], w[4];
#define Q4(A, B, C, D) A "\n\t" B "\n\t" C "\n\t" D "\n\t"
    asm volatile(Q4("v_fma_f32 %0, %8, %12, %16", "v_fma_f32 %1, %8, %13, %17", "v_fma_f32 %2, %8, %14, %18", "v_fma_f32 %3, %8, %15, %19")
                 Q4("v_mul_f32 %4, %0, %0", "v_mul_f32 %5, %1, %1", "v_mul_f32 %6, %2, %2", "v_mul_f32 %7, %3, %3")
                 Q4("v_fma_f32 %4, %4, %11, %10", "v_fma_f32 %5, %5, %11, %10", "v_fma_f32 %6, %6, %11, %10", "v_fma_f32 %7, %7, %11, %10")
                 Q4("v_mul_f32 %4, %4, %0", "v_mul_f32 %5, %5, %1", "v_mul_f32 %6, %6, %2", "v_mul_f32 %7, %7, %3")
                 Q4("v_exp_f32 %4, %4", "v_exp_f32 %5, %5", "v_exp_f32 %6, %6", "v_exp_f32 %7, %7")
                 Q4("v_fma_f32 %4, %4, %9, %9", "v_fma_f32 %5, %5, %9, %9", "v_fma_f32 %6, %6, %9, %9", "v_fma_f32 %7, %7, %9, %9")
                 Q4("v_rcp_f32 %4, %4", "v_rcp_f32 %5, %5", "v_rcp_f32 %6, %6", "v_rcp_f32 %7, %7")
                 Q4("v_mul_f32 %4, %4, %0", "v_mul_f32 %5, %5, %1", "v_mul_f32 %6, %6, %2", "v_mul_f32 %7, %7, %3")
                 : "=&v"(u[0]), "=&v"(u[1]), "=&v"(u[2]), "=&v"(u[3]), "=&v"(w[0]), "=&v"(w[1]), "=&v"(w[2]), "=&v"(w[3])
                 : "v"(rc[0][mt]), "v"(rc[1][mt]), "v"(rc[2][mt]), "v"(rc[3][mt]), "v"(cs[j][0]), "v"(cs[j][1]), "v"(cs[j][2]), "v"(cs[j][3]),
                   "v"(ac[mt][j][0]), "v"(ac[mt][j][1]), "v"(ac[mt][j][2]), "v"(ac[mt][j][3]));
    z1[j][2 * mt] = pack2(w[0], w[1]);
    z1[j][2 * mt + 1] = pack2(w[2], w[3]);
    if (mt && j < NR1) asm volatile("ds_write_b128 %0, %1 offset:%2" ::"v"(za), "v"(z1[j]), "n"(j * 1024) : "memory");
    if (t == 9) asm volatile("s_waitcnt lgkmcnt(0)" ::: "memory");
    __builtin_amdgcn_sched_barrier(0);
    if (t == 2 * ZQ - 7)
      asm volatile("global_load_dword %0, %1, %2 sc1"
                   : "=v"(zf)
                   : "v"(vconst(0u)), "s"(uni_ptr(ws + OFF_CNT + (size_t)((CN_Z2 + layer) * 256 + mi * 16 + ni) * 128))
                   : "memory");
    if (t == 2 * ZQ - 4) {
      asm volatile("s_waitcnt vmcnt(0)" : "+v"(zf) : : "memory");
      if (wm == 1 && __builtin_amdgcn_readfirstlane(zf) != 15u - ZQ) cnt_wait(ws, CN_Z2 + layer, mi * 16 + ni, 16u - ZQ);
    }
    bar();
    __builtin_amdgcn_sched_barrier(0);
  }
  MK(15);
#pragma unroll 1
  for (int t = 20; t < 32; ++t) bar();
  __builtin_amdgcn_s_sleep(9);
#pragma unroll
  for (int k = NR1; k < 9; ++k) {
    if (k == NR1 + 4) lds_spin4(lw + LW_AD * 4, 7u);
    asm volatile("ds_write_b128 %0, %1 offset:%2" ::"v"(lds0 + (unsigned)(wm * 4096 + l2 * 16)), "v"(z1[k]),
                 "n"(((k - NR1) / 4) * K3C::SB + ((k - NR1) % 4) * 1024)
                 : "memory");
    if (k == NR1 + 3 || k == 8) asm volatile("ds_write_b8 %0, %1" ::"v"(lw + (unsigned)(LW_S * 4 + wm)), "v"(vconst(k == NR1 + 3 ? 10u : 11u)) : "memory");
  }
}

constexpr int W5A = 22;
constexpr int NPF = 30;
struct Loader {
  u32x4 pf[NPF];
  unsigned lds0, lane16;
  int wl, lane;

  template <int R0, int N, int P0, int PE>
  DEVI void pf_load(const char* src) {
    const unsigned o = lane16 + (unsigned)(wl * 1024);
#pragma unroll
    for (int t = 0; t < N; ++t) {
      unsigned a = o + (unsigned)((P0 + 4 * t) * 1024);
      if (P0 + 4 * t + 3 >= PE && P0 + 4 * t + wl >= PE) a = lane16;
      asm volatile("global_load_dwordx4 %0, %1, %2 sc1" : "=v"(pf[R0 + t]) : "v"(a), "s"(src) : "memory");
    }
  }
  template <int R0, int N>
  DEVI void pf_pin() {
#pragma unroll
    for (int t = 0; t < N; ++t) asm volatile("" : "+v"(pf[R0 + t]));
  }
  template <int R0, int N>
  DEVI void pf_undef() {
#pragma unroll
    for (int t = 0; t < N; ++t) asm volatile("" : "=v"(pf[R0 + t]));
  }
  template <int PB, int A, int N>
  DEVI void pf_put(unsigned dst) {
    constexpr int T0 = (A - PB) / 4, T1 = (A + N - 1 - PB) / 4;
    const unsigned a = lds0 + dst + lane16 + (unsigned)((wl + PB + 4 * T0 - A + 4) * 1024) - 4096u;
#pragma unroll
    for (int t = T0; t <= T1; ++t) {
      const int p0 = PB + 4 * t;
      if ((p0 >= A || p0 + wl >= A) && (p0 + 3 < A + N || p0 + wl < A + N))
        asm volatile("ds_write_b128 %0, %1 offset:%2" ::"v"(a), "v"(pf[t]), "n"((t - T0) * 4096) : "memory");
    }
  }
  template <int N>
  DEVI void dma_run(const char* src, unsigned dst) {
    constexpr int B = N / 4, R = N % 4;
    const unsigned f = (unsigned)(wl * B + (wl < R ? wl : R)) * 1024u, o = lane16 + f, m = lds0 + dst + f;
    dma_seq<B>(src, o, m);
    if (wl < R) dma16_sc1(src + B * 1024, o, m + (unsigned)(B * 1024));
  }
  template <int V>
  DEVI void wait_w() {
    asm volatile("s_cmp_lt_u32 %0, 2\n\ts_cbranch_scc1 1f\n\ts_waitcnt vmcnt(%1)\n1:\n\ts_waitcnt vmcnt(%2)" ::"s"(wl), "n"(V - 1), "n"(V) : "memory", "scc");
  }
  template <int V>
  DEVI void wbar() {
    asm volatile("s_waitcnt vmcnt(%0) lgkmcnt(0)" ::"n"(V) : "memory");
    bar();
  }

  DEVI void p1(char* smem, char* ws, int bid, int rt, int hp MK_ARG) {
    constexpr int SB = K1C::SB;
    static_assert(K1C::XB == 18 * 1024 && K1C::WB == 14 * 1024, "K1 shape");
    const char* wp = uni_ptr(ws + OFF_PQ + (size_t)hp * (9 * K1C::WB) + 4 * K1C::WB);
    static_assert(3 * SB + K1C::XB >= P5_W + 5 * 18432, "free while the GEMM before ends");
    const char* wq = wp - 4 * K1C::WB;
    dma_run<14>(wq + 3 * K1C::WB, (unsigned)(3 * SB + K1C::XB));
    dma_run<28>(wp, (unsigned)K1_WX);
    if (wl == 3) dma16_sc1(uni_ptr(ws + OFF_CSQ + bid * 512), lane16 & 511u, lds0 + (unsigned)K1_CS_OFF);
    __builtin_amdgcn_s_setprio(0);
    wait_vm<0>();
    const bool lo = wl < 2;
    const int x0 = lo ? wl * 5 : wl * 4 + 2, w0 = lo ? wl * 3 : wl * 4 - 2;
    unsigned xo[5];
#pragma unroll
    for (int i = 0; i < 5; ++i) {
      const int c = (x0 + i) * 64 + lane;
      const int ch = c / 144;
      int gr = rt * 128 - 16 + (c - ch * 144);
      if (gr < 0) gr = 0;
      xo[i] = (unsigned)(((gr >> 6) * 72 + ch + 3 - (i & 3)) * 1024 + (gr & 63) * 16);
      asm volatile("" : "+v"(xo[i]));
    }
    const char* xp = uni_ptr(ws + OFF_X1 + SZ_X - 3072);
    wp += 2 * K1C::WB + w0 * 1024;
    auto dma_x = [&](unsigned so) {
      const unsigned m = lds0 + so + (unsigned)(x0 * 1024);
      dma4<4, true>(xp, xo[0], xo[1], xo[2], xo[3], m);
      if (lo) dma16_sc1(xp, xo[4], m + 4096u);
      xp += 8192;
    };
    auto dma_w = [&](unsigned so) {
      const unsigned m = lds0 + so + (unsigned)(K1C::XB + w0 * 1024);
      dma_seq<3>(wp, lane16, m);
      if (!lo) dma16_sc1(wp + 3072, lane16, m + 3072u);
      wp += K1C::WB;
    };
    const char* xs = uni_ptr(ws + OFF_XS + SZ_XS);
    const int g = lane >> 4, p2 = 64 + (lane & 7);
    int ra = rt * 128 - 16 + wl * 32 + 2 * (lane & 15);
    if (ra < 0) ra = 0;
    unsigned sa = (unsigned)(((ra >> 6) * 8 + g * 2) * 512 + (ra & 63) * 8);
    unsigned sb = (unsigned)(((rt * 2 + 1) * 8 + (lane >> 3)) * 512 + (2 * p2 - 80) * 8);
    unsigned sw = lds0 + (unsigned)(K1_ST_OFF + (2 * (lane < 32 ? wl * 16 + (lane & 15) : p2) + (g & 1)) * 8);
    asm volatile("" : "+v"(sa), "+v"(sb), "+v"(sw));
    MK(22);
    __builtin_amdgcn_s_setprio(3);
    bar();
    dma_run<14>(wq, (unsigned)K1C::XB);
    dma_run<14>(wq + K1C::WB, (unsigned)(SB + K1C::XB));
    dma_run<14>(wq + 2 * K1C::WB, (unsigned)(2 * SB + K1C::XB));
    MK(1);
    bar();
    dma_x(0u);
    dma_x((unsigned)SB);
    wbar<4>();
    MK(29);
    dma_x((unsigned)(2 * SB));
    dma_x((unsigned)(3 * SB));
    wbar<8>();
    MX(1, 3);
    dma_x(0u);
    wbar<8>();
    MX(1, 4);
    dma_x((unsigned)SB);
    f32x4 a, b, e;
    asm volatile("global_load_dwordx4 %0, %3, %5 sc1\n\tglobal_load_dwordx4 %1, %3, %5 offset:512 sc1\n\tglobal_load_dwordx4 %2, %4, %5 sc1"
                 : "=&v"(a), "=&v"(b), "=&v"(e)
                 : "v"(sa), "v"(sb), "s"(xs)
                 : "memory");
    wbar<11>();
    MX(1, 5);
    dma_w((unsigned)SB);
    dma_x((unsigned)(2 * SB));
    wbar<15>();
    MX(1, 6);
    dma_w((unsigned)(2 * SB));
    dma_x((unsigned)(3 * SB));
    wbar<19>();
    MX(1, 7);
    dma_w((unsigned)(3 * SB));
    dma_x(0u);
    wbar<16>();
    MX(1, 8);
    asm volatile("" : "+v"(a), "+v"(b), "+v"(e));
    float s, q;
    {
      float t[4] = {e[0], e[1], e[2], e[3]}, s2, q2;
      asm volatile(
          "v_add_f32_dpp %0, %0, %0 row_ror:8 row_mask:0xf bank_mask:0xf\n\tv_add_f32_dpp %1, %1, %1 row_ror:8 row_mask:0xf bank_mask:0xf\n\t"
          "v_add_f32_dpp %2, %2, %2 row_ror:8 row_mask:0xf bank_mask:0xf\n\tv_add_f32_dpp %3, %3, %3 row_ror:8 row_mask:0xf bank_mask:0xf\n\ts_nop 1"
          : "+v"(t[0]), "+v"(t[1]), "+v"(t[2]), "+v"(t[3]));
      red4(t, s2, q2);
      red8(a, b, s, q);
      s = lane < 32 ? s : s2;
      q = lane < 32 ? q : q2;
      asm volatile("" : "+v"(s), "+v"(q));
    }
    wbar<8>();
    MX(1, 9);
    {
      asm volatile("" : "+v"(s), "+v"(q));
      float mb, rinv;
      ln_finish(s, q, 1.0f / (float)D_, mb, rinv);
      asm volatile("ds_write_b64 %0, %1" ::"v"(sw), "v"(f32x2{-mb, rinv}) : "memory");
    }
    wbar<0>();
    MK(30);
  }

  template <int I>
  DEVI void p5_step(const char* wp) {
    constexpr int T = 2 * I + 1;
    wbar<(T == 1 ? 63 : (T <= 11 ? 3 : 0))>();
    if constexpr (T <= 9) {
      const unsigned wo = (unsigned)(12288 + (wl & 1) * 3072), so = (unsigned)(P5_W + ((T + 2 + (wl >> 1)) & 7) * 18432);
      dma_seq<3>(wp + (T + 7) * 18432, lane16 + wo + (unsigned)((wl >> 1) * 18432), lds0 + so + wo);
      pf_put<0, 12 * (T - 1), 12>((unsigned)(P5_W + ((T + 2) % 8) * 18432));
      pf_put<0, 12 * T, 12>((unsigned)(P5_W + ((T + 3) % 8) * 18432));
    }
  }
  template <int... S>
  DEVI void p5_steps(const char* wp, std::integer_sequence<int, S...>) {
    (p5_step<S>(wp), ...);
  }

  template <int T0, int T1>
  DEVI void w5_load(const char* w5p) {
    const unsigned o = lane16 + (unsigned)(wl * 1024);
#pragma unroll
    for (int t = T0; t < T1; ++t)
      asm volatile("global_load_dwordx4 %0, %1, %2 sc1" : "=v"(pf[t]) : "v"(o + (unsigned)((t / 3 + 8) * 18432 + (t % 3) * 4096)), "s"(w5p) : "memory");
  }
  DEVI void p35(char* smem, char* ws, const u16* w_sp, int layer, int bid, int mi, int ni, int rb, int ct, int hp MK_ARG) {
    using C = K3C;
    constexpr int NS = C::NS, SB = C::SB, NDX = 4, NDW = 5, VPS = NDX + NDW;
    const char* wip = uni_ptr(ws + OFF_PI + (size_t)ni * (18 * C::WB));
    if (layer) {
      pf_put<-48, 36, 18>((unsigned)(2 * SB + C::XB));
      pf_put<-48, 54, 18>((unsigned)(3 * SB + C::XB));
    } else {
      dma_run<18>(wip + 2 * C::WB, (unsigned)(2 * SB + C::XB));
      dma_run<18>(wip + 3 * C::WB, (unsigned)(3 * SB + C::XB));
    }
    dma_run<18>(wip + C::WB, (unsigned)(SB + C::XB));
    const char* w5p = uni_ptr(ws + OFF_PW + (size_t)ct * (36 * 9216));
    const int w0 = wl < 2 ? wl * 5120 : wl * 4096 + 2048;
    const unsigned m0x = lds0 + (unsigned)(wl * 4096), m0w = lds0 + (unsigned)(C::XB + w0);
    const unsigned wo = lane16 + (unsigned)w0;
    const unsigned lw = lds0 + (unsigned)(LW_OFF + layer * 4), me = 1u << (8 * wl);
    const unsigned wo4 = wo + 4096u;
    auto dma_w = [&](const char* wp, unsigned so) {
      dma_seq<4>(wp, wo, m0w + so);
      if (wl < 2) dma16_sc1(wp, wo4, m0w + so + 4096u);
    };
    {
      const char* xp = uni_ptr(ws + OFF_X1 + (size_t)layer * 2 * SZ_X + (size_t)(mi * 2 * 72 + wl * 2) * 1024 - 1024);
      const unsigned xo0 = lane16 + 1024u, xo1 = lane16 + 72u * 1024u, xo3 = lane16 + 71u * 1024u;
      auto dma_x = [&](int k, unsigned so) { dma4<4, true>(xp + k * 8192, xo0, xo1, lane16, xo3, m0x + so); };
      MK(10);
      bar();
      dma_x(0, 0u);
      dma_x(1, (unsigned)SB);
      wait_vm<4>(); MX(3, 2); wbar<4>(); MX(2, 2);
      MK(25);
      {
        dma_x(2, (unsigned)(2 * SB));
        dma_x(3, (unsigned)(3 * SB));
        dma_x(4, (unsigned)(4 * SB));
        wbar<12>(); MX(2, 3);
        dma_x(5, 0u);
        wait_vm<12>();
      }
      bar(); MX(2, 4);
      dma_w(wip + 4 * C::WB, 0u);
      dma_x(6, (unsigned)SB);
      wait_w<8 + VPS>();
#pragma unroll 1
      for (int t = 1; t < 3; ++t) {
        bar(); MX(2, 4 + t);
        dma_w(wip + (t + 4) * C::WB, (unsigned)(t * SB));
        dma_x(t + 6, (unsigned)((t + 1) * SB));
        wait_w<NDX + VPS>();
      }
      bar(); MX(2, 7);
      dma_w(wip + 7 * C::WB, (unsigned)(3 * SB));
      wait_w<VPS>();
      bar(); MX(2, 8);
      dma_w(wip + 8 * C::WB, 0u);
      wait_w<NDW>();
      bar(); MX(2, 9);
      const char* wp = wip + 9 * C::WB;
      dma_w(wp + b_k(0) * C::WB, (unsigned)SB);
      if (layer == 0) {
        const char* sp = uni_ptr((const char*)w_sp + (size_t)(bid * 4 + wl) * 8192);
        const unsigned so = (unsigned)((lane >> 5) * 4096 + (lane & 31) * 16);
#pragma unroll
        for (int i = 0; i < WSP_V; ++i) dma16(sp + i * 512, so, lds0 + (unsigned)K3_DM_OFF);
        dma16(sp + (bid >> 4) * 256, so & ~0x100u, lds0 + (unsigned)K3_DM_OFF);
      }
      w5_load<0, 4>(w5p);
      wait_w<NDW + 4>();
      MX(3, 10); bar(); MX(2, 10);
      dma_w(wp + b_k(1) * C::WB, (unsigned)(2 * SB));
      w5_load<4, 8>(w5p);
      MK(26);
      lds_spin4(lw + LW_AD * 4, 1u);
      dma_w(wp + b_k(2) * C::WB, (unsigned)(3 * SB));
      dma_w(wp + b_k(3) * C::WB, 0u);
      w5_load<8, W5A>(w5p);
      wait_vm<W5A - 8>();
      lds_inc(lw + LW_L * 4, 4u * me);
      w5_load<W5A, 30>(w5p);
      MK(11);
    }
    {
      unsigned xoff[NDX];
#pragma unroll
      for (int i = 0; i < NDX; ++i) {
        const int c = wl * 256 + i * 64 + lane;
        const int row = c >> 3, cc = c & 7;
        xoff[i] = (unsigned)((mi * 128 + row) * S_ * 2 - i * 1024) + (unsigned)((cc ^ ((row >> 1) & 7)) * 16);
      }
      auto dma_x = [&](const char* xp, unsigned so) { dma4<4, false>(xp, xoff[0], xoff[1], xoff[2], xoff[3], m0x + so); };
      const char* xp = uni_ptr((const char*)w_sp);
      const char* wp = uni_ptr(ws + OFF_Z2 + (size_t)layer * SZ_Z + (size_t)ni * S_ * 288);
      static_for<4, 7>([&](auto T_) {
        constexpr int t = T_;
        lds_spin4<2>(lw + LW_S * 4, (unsigned)(t - 3));
        dma_w(wip + (9 + b_k(t)) * C::WB, (unsigned)(((t + 1) % 4) * SB));
        if constexpr (t > 4) {
          dma_x(xp + mi * 256 + (t - 5) * 128, (unsigned)((t - 3) * SB));
          wait_w<NDW + (t - 4) * NDX>();
          lds_inc(lw + LW_L * 4, me);
        }
      });
      wait_vm<NDX>();
      lds_inc(lw + LW_L * 4, me);
      wait_vm<0>();
      lds_inc(lw + LW_L * 4, me);
      pf_pin<0, 30>();
      MK(13);
      lds_spin4<2>(lw + LW_AD * 4, 5u);
      MK(24);
      int left = 2 * mi;
      auto next = [&]() {
        const int d = --left == 0 ? 3 : 1;
        xp += d * 128;
        wp += d * C::WB;
      };
      if (mi == 0) {
        xp += 256;
        wp += 2 * C::WB;
      }
      if (wl == 0) {
        cnt_wait(ws, CN_Z + layer, mi * 16 + ni, (unsigned)ZQ);
        lds_wr32(lw + LW_GO * 4, 1u);
      }
      lds_spin4<2>(lw + LW_AD * 4, 6u);
      dma_x(xp, 0u);
      dma_x(xp + 128, (unsigned)SB);
      spin_ge<2>(lw + LW_GO * 4, 1u);
      lds_spin4<2>(lw + LW_S * 4, 8u);
      dma_w(wp, 0u);
      lds_spin4<2>(lw + LW_S * 4, 9u);
      dma_w(wp + C::WB, (unsigned)SB);
      next();
      next();
      unsigned slot = (unsigned)(2 * SB);
      auto cissue = [&]() {
        dma_x(xp, slot);
        dma_w(wp, slot);
        next();
        slot = (slot == (unsigned)((NS - 1) * SB)) ? 0u : slot + (unsigned)SB;
      };
      auto cstep = [&]() {
        bar();
        cissue();
      };
      wait_w<NDW>();
      MK(15);
      int j = 2;
      if (layer == 0) {
        const char* sp = uni_ptr((const char*)w_sp + (size_t)(bid * 4 + wl) * 8192 + WSP_V * 512);
        unsigned d = 0u;
#pragma unroll 1
        for (; j < 2 + 32 - 4 * WSP_V; ++j) {
          cstep();
          asm volatile("s_load_dword %0, %1, 0\n\ts_load_dword %0, %1, 64\n\ts_load_dword %0, %1, 0x1000\n\ts_load_dword %0, %1, 0x1040"
                       : "+s"(d)
                       : "s"(sp));
          sp += 128;
          wait_w<VPS>();
        }
#pragma unroll 1
        for (int e = j + 4; j < e; ++j) {
          cstep();
          asm volatile("" : "+s"(d));
          wait_w<VPS>();
        }
        asm volatile("s_waitcnt lgkmcnt(0)" : "+s"(d));
      }
      place();
#pragma unroll 1
      for (; j <= 29; ++j) {
        cstep();
        wait_w<VPS>();
      }
      bar();
    }
    dma_run<33>(w5p, (unsigned)(P5_W + 54 * 1024));
    dma_run<23>(w5p + 67 * 1024, (unsigned)(P5_W + 121 * 1024));
    if (wl == 3) dma16_sc1(w5p + 143 * 1024, lane16, lds0 + (unsigned)(P5_W + 53 * 1024));
    wait_vm<14>();
    bar();
    dma_run<34>(w5p + 33 * 1024, (unsigned)(P5_W + 87 * 1024));
    wait_vm<0>();
    bar();
    if (wl) {
      const unsigned f = (unsigned)(wl - 1) * 18432u;
      dma_seq<17>(w5p + 90 * 1024, lane16 + f, lds0 + (unsigned)P5_W + f);
      if (wl < 3) dma16_sc1(w5p + 107 * 1024, lane16 + f, lds0 + (unsigned)(P5_W + 17 * 1024) + f);
    } else {
      cnt_wait(ws, CN_H + layer, rb * 8 + ct, 8u);
    }
    MK(20);
    bar();
    p5_steps(w5p, std::make_integer_sequence<int, 7>{});
    if (layer == 0) {
      if (bid < 24 && wl == 0 && lane < 8) pk_st16(ws + OFF_HELLO + bid * 128 + lane16, u32x4{0u, 0u, 0u, 0u});
    }
  }
};

__global__ __launch_bounds__(768, 3) void k_main(const u16* __restrict__ x0, const u16* __restrict__ wqkv, const u16* __restrict__ wo,
                                                 const u16* __restrict__ w_in, const u16* __restrict__ w_sp,
                                                 const u16* __restrict__ b_sp, const u16* __restrict__ w_out,
                                                 u16* __restrict__ out, char* ws) {
  extern __shared__ __attribute__((aligned(16))) char smem[];
  const char* code0 = code_here();
  (void)code0;
  const unsigned lds0 = (unsigned)(size_t)smem;
  int wave = __builtin_amdgcn_readfirstlane((int)threadIdx.x >> 6);
  int bid = blockIdx.x;
#define FRESH() asm volatile("" : "+s"(bid), "+s"(ws), "+s"(wave))
#define XCD (bid & 7)
#define IDX (bid >> 3)
#define MI ((XCD >> 2) * 8 + (IDX >> 2))
#define NI ((XCD & 1) * 8 + (XCD & 2) * 2 + (IDX & 3))
#define RB (XCD * 4 + (IDX >> 3))
#define CT (IDX & 7)
#define RT ((XCD >> 1) * 4 + (IDX >> 3))
#define HP ((XCD & 1) * 8 + (IDX & 7))
  MK(0);
  if (wave < 8) {
    constexpr int PER = (2 * NL_MAIN + 31) / 32;
    static_assert(PER <= 64 && 31 * PER < 2 * NL_MAIN, "one load");
    unsigned o, last, st, t;
    const char* p;
    asm volatile(
        "s_lshr_b32 %3, %5, 3\n\t"
        "s_cmp_eq_u32 %3, 31\n\ts_cselect_b32 %1, 63, 0\n\ts_cselect_b32 %2, %7, %6\n\t"
        "s_mul_i32 %0, %3, %8\n\t"
        "s_cmp_eq_u32 %4, 2\n\ts_cselect_b32 %0, %0, 0\n\ts_cselect_b32 %1, %2, %1\n\t"
        "s_mul_i32 %3, %3, 0xf300\n\ts_add_u32 %3, %3, 0x200\n\t"
        "s_cmp_eq_u32 %4, 3\n\ts_cselect_b32 %0, %3, %0\n\ts_cselect_b32 %1, 53, %1\n\ts_cselect_b32 %2, 0x480, 64\n\t"
        "s_mul_i32 %3, %5, 0x1e60\n\t"
        "s_cmp_eq_u32 %4, 5\n\ts_cselect_b32 %0, %3, %0\n\t"
        "s_add_u32 %3, %3, 0x1000\n\t"
        "s_cmp_eq_u32 %4, 4\n\ts_cselect_b32 %0, %3, %0\n\ts_cselect_b32 %1, 57, %1\n\t"
        "s_cmp_ge_u32 %4, 5\n\ts_cselect_b32 %1, 63, %1\n\t"
        "s_mul_i32 %3, %5, 0x2400\n\t"
        "s_cmp_eq_u32 %4, 7\n\ts_cselect_b32 %0, %3, %0\n\t"
        "s_add_u32 %3, %3, 0x1000\n\t"
        "s_cmp_eq_u32 %4, 6\n\ts_cselect_b32 %0, %3, %0\n\t"
        "s_add_u32 %3, %3, 0x1000\n\t"
        "s_cmp_eq_u32 %4, 1\n\ts_cselect_b32 %0, %3, %0\n\ts_cselect_b32 %1, 15, %1"
        : "=&s"(o), "=&s"(last), "=&s"(st), "=&s"(t)
        : "s"(wave), "s"(bid), "n"(PER - 1), "n"(2 * NL_MAIN - 1 - 31 * PER), "n"(PER * 64)
        : "scc");
    const unsigned l = (unsigned)lane_id();
    unsigned a = o + (l < last ? l : last) * st;
    asm volatile("" : "+v"(a));
    asm volatile("s_cmp_eq_u32 %1, 2\n\ts_cselect_b64 %0, %4, %5\n\ts_bitcmp1_b32 0x38, %1\n\ts_cselect_b64 %0, %3, %0\n\t"
                 "s_bitcmp1_b32 0xc2, %1\n\ts_cselect_b64 %0, %2, %0"
                 : "=&s"(p)
                 : "s"(wave), "s"(x0), "s"(wqkv), "s"(code0), "s"(b_sp)
                 : "scc");
    touch64(p, a, lds0 + (unsigned)K1R_DUMMY);
    MF(12);
    math_start(lds0, ws, bid, wave, (int)l);
    MF(13);
  }

  if (__builtin_expect(wave >= 8, 1)) {
    __builtin_amdgcn_s_setprio(3);
    {
      const int lane = lane_id();
      RawLd rl;
      int job, pj;
      rl.front(x0, wqkv, wo, ws, bid, lds0, wave - 8, lane, RT, HP, pj, [&]() {
        int b = bid;
        asm volatile("" : "+s"(b));
        const int xcd = b & 7, c16 = b >> 4, hp = (b & 1) * 8 + ((b >> 3) & 7);
        const bool cross = (((hp * 72) & ~15) & 127) > 48;
        const int cj = (int)((0x65743210u >> (4 * xcd)) & 7u) * 16 + c16;
        job = cross ? (cj < 88 ? 128 + cj : 999) : xcd * 16 + c16;
        pj = cross ? cj - 88 : ((xcd & 5) == 0 && (c16 & 3) == (c16 >> 2) ? 40 + xcd * 2 + (c16 >> 2) : -1);
      } MF_PASS);
      if (job < 216) k1_raw_loader<true>(rl, w_in, w_out, ws, rl.tag, bid, lds0, wave - 8, lane, RT, HP, CT, job, pj MF_PASS);
      else k1_raw_loader<false>(rl, w_in, w_out, ws, rl.tag, bid, lds0, wave - 8, lane, RT, HP, CT, job, pj MF_PASS);
    }
    Loader ld;
    ld.lds0 = lds0;
#pragma unroll 1
    for (int layer = 0; layer < 2; ++layer) {
      FRESH();
      ld.wl = wave - 8;
      ld.lane = lane_id();
      ld.lane16 = (unsigned)ld.lane * 16u;
      const char* wip = uni_ptr(ws + OFF_PI + (size_t)NI * (18 * K3C::WB));
      if (layer) {
        ld.p1(smem, ws, bid, RT, HP MK_PASS);
        ld.pf_load<0, 21, 0, 81>(uni_ptr(ws + OFF_PO + (size_t)CT * (18 * 4608)));
        ld.pf_load<21, 9, 36, 72>(wip);
        wait_vm<9>();
        ld.pf_pin<0, 21>();
        lds_spin(lds0 + (unsigned)(LW_OFF + (LW_T + layer) * 4), 8u);
        __builtin_amdgcn_s_sleep(4);
        ld.pf_put<0, 0, 40>((unsigned)P2_W);
        wait_vm<0>();
        ld.pf_pin<21, 9>();
        bar();
        ld.pf_put<0, 40, 41>((unsigned)(P2_W + 40 * 1024));
      } else {
        ld.pf_undef<21, 9>();
        MK(3);
        MK(4);
        bar();
        ld.dma_run<81>(uni_ptr(ws + OFF_PO + (size_t)CT * (18 * 4608)), (unsigned)P2_W);
      }
      if (layer == 0 && ld.wl == 0) cnt_wait(ws, CN_O + layer, RB * 8 + CT, 16u);
      MK(6);
      asm volatile("s_waitcnt vmcnt(0) lgkmcnt(0)" ::: "memory");
      bar();
      lds_spin(lds0 + (unsigned)(LW_OFF + (LW_T + layer) * 4), 12u);
      if (layer == 0) {
        if (ld.wl == 0) {
          cnt_wait(ws, CN_PK, 8 + NI, 36u);
          lds_wr32(lds0 + (unsigned)(LW_OFF + LW_HELLO * 4), 2u);
        } else {
          lds_spin(lds0 + (unsigned)(LW_OFF + LW_HELLO * 4), 2u);
        }
        MK(7);
      }
      static_assert(2 * K3C::SB <= P2_W, "free during the first residual GEMM");
      ld.dma_run<18>(wip, (unsigned)K3C::XB);
      MX(6, 24);
      if (layer == 0 && ld.wl == 0) cnt_wait(ws, CN_PK, 24 + CT, 72u);
      wait_vm<0>();
      ld.pf_pin<21, 9>();
      MX(6, 25);
      bar();
      FRESH();
      ld.wl = wave - 8;
      ld.p35(smem, ws, w_sp, layer, bid, MI, NI, RB, CT, HP MK_PASS);
    }
  } else {
#pragma unroll 1
    for (int layer = 0; layer < 2; ++layer) {
      FRESH();
      {
        const int lane = lane_id(), r0 = RT * 128;
        float* csq = reinterpret_cast<float*>(ws + OFF_CSQ + bid * 512);
        Attn at;
        at.setup(lds0 + (unsigned)(layer ? K1_T_OFF : K1R_T_OFF), ws + OFF_O + (size_t)layer * SZ_X, r0, HP, wave, lane);
        if (layer) {
          if ((wave & 2) == 0) k1_gemm<2, 4>(smem, ws, RT, HP, (wave & 1) * 2 + (wave & 4), 0, wave >= 4, lane MK_PASS);
          else if (wave < 4) k1_gemm<3, 3>(smem, ws, RT, HP, (wave & 1) * 3, 4, false, lane MK_PASS);
          else if (wave == 6) k1_gemm<2, 3>(smem, ws, RT, HP, 6, 4, true, lane MK_PASS);
          else k1_gemm<1, 7>(smem, ws, RT, HP, 8, 0, true, lane MK_PASS);
        } else {
          if (wave == 0) k1_gemm_raw<3, false>(smem, HP, 0, lane MF_PASS);
          else if (wave < 4) k1_gemm_raw<2, false>(smem, HP, 1 + 2 * wave, lane MF_PASS);
          else if (wave < 7) k1_gemm_raw<2, true>(smem, HP, 2 * (wave - 4), lane MF_PASS);
          else k1_gemm_raw<3, true>(smem, HP, 6, lane MF_PASS);
        }
        msync(lds0 + (unsigned)(LW_OFF + (LW_T + layer) * 4));
        MK(3);
        at.run(r0, HP, wave, lane);
        if (layer == 0) {
          if (wave == 0 && lane < 32)
            asm volatile("global_store_dwordx4 %0, %1, %2 sc1\n\ts_nop 1" ::"v"((unsigned)lane * 16u),
                         "v"(*reinterpret_cast<const u32x4*>(smem + K1R_CS_OFF + lane * 16)), "s"(uni_ptr((const char*)csq))
                         : "memory");
          lds_spin(lds0 + (unsigned)(LW_OFF + LW_HELLO * 4), 1u);
        }
        MK(4);
        announce(lds0 + (unsigned)(LW_OFF + (LW_O + layer * 22 + (wave >> 2)) * 4), 4u, ws, CN_O + layer, (RT * 2 + (wave >> 2)) * 8 + lane,
                 lane < 8);
        MK(5);
        bar();
      }
      FRESH();
      {
        if (wave < 4) {
          const int lane = lane_id();
          u32x2 xv[5];
          ResIn ri;
          ri.setup(layer ? ws + OFF_X1 + SZ_X : (const char*)x0, layer == 0, RB, CT, wave, lane);
          ri.load(xv);
          ResOut ro;
          ro.setup(ws + OFF_X1 + (size_t)layer * 2 * SZ_X, ws + OFF_XS + (size_t)layer * 2 * SZ_XS, out, false, RB, CT, wave, lane);
          f32x4 acc[2][5];
          p2_gemm(acc, xv, lds0, uni_ptr(ws + OFF_O + (size_t)layer * SZ_X + (size_t)RB * (72 * 1024)), layer, wave, lane MK_PASS);
          MK(7);
          ro.run(acc[0], false);
          MK(8);
          announce(lds0 + (unsigned)(LW_OFF + (LW_X1 + layer) * 4), 4u, ws, CN_X1 + layer, (RB >> 1) * 16 + lane, lane < 16);
          MK(9);
          bar();
          FRESH();
          if (wave == 3) cnt_wait(ws, CN_X1 + layer, MI * 16 + NI, 16u);
          bar();
          MK(10);
          p3_g0(smem, ws, layer, MI, NI, wave, lane_id() MK_PASS);
        } else {
          if (layer && wave == 5) cnt_wait(ws, CN_O + layer, RB * 8 + CT, 16u);
          bar();
          k3_const(smem, ws, b_sp, layer, bid, MI, NI, wave & 3);
          MX(6, 26);
          bar();
          MK(10);
          FRESH();
          p3_g1(smem, ws, layer, MI, NI, wave & 3, lane_id() MK_PASS);
        }
      }
      FRESH();
      {
        const int lane = lane_id();
        if (wave < 4) {
          u32x2 xv[5];
          ResIn ri;
          ri.setup(ws + OFF_X1 + (size_t)layer * 2 * SZ_X, false, RB, CT, wave, lane);
          f32x4 acc[2][5];
          ResOut ro;
          ro.setup(ws + OFF_X1 + SZ_X, ws + OFF_XS + SZ_XS, out, layer != 0, RB, CT, wave, lane);
          ResX<36, 33, 8, P5_W, P5_XW, P5_X0, 3, P5_BAR> gm(acc);
          gm.setup(lds0, uni_ptr(ws + OFF_H + (size_t)layer * SZ_Z + (size_t)RB * (288 * 1024)), wave, lane, (unsigned)P5_RED, layer + 1u);
          announce(lds0 + (unsigned)(LW_OFF + (LW_H + layer * 2 + (wave >> 1)) * 4), 2u, ws, ((NI >= 8) ? CN_H2 : CN_H) + layer,
                   (MI * 2 + (wave >> 1)) * 8 + lane, lane < 8);
          MK(19);
          bar();
          ri.load(xv);
          gm.prow();
          place();
          bar();
          MK(20);
          gm.prox<0>(std::make_integer_sequence<int, P5_X0>{});
          gm.init<2 * P5_X0>(xv);
          __builtin_amdgcn_sched_barrier(0);
          gm.run();
          MK(21);
          ro.run(acc[0], layer != 0);
          MK(22);
          if (layer == 0) {
            announce(lds0 + (unsigned)(LW_OFF + LW_X2 * 4), 4u, ws, CN_X2, (RB >> 1) * 16 + lane,
                     lane < 16 || (lane < 32 && (RB & 1) && RB < 31));
            MK(23);
          }
        } else {
          bar();
          bar();
#pragma unroll
          for (int i = 0; i < 7; ++i) {
            if (i == 1 && wave == 5) {
              unsigned f;
              const char* cp = uni_ptr(ws + OFF_CNT + (size_t)((CN_H2 + layer) * 256 + RB * 8 + CT) * 128);
              asm volatile("global_load_dword %0, %1, %2 sc1" : "=v"(f) : "v"(vconst(0u)), "s"(cp) : "memory");
              bar();
              asm volatile("s_waitcnt vmcnt(0)" : "+v"(f) : : "memory");
              if (__builtin_amdgcn_readfirstlane(f) != 7u) cnt_wait(ws, CN_H2 + layer, RB * 8 + CT, 8u);
              continue;
            }
            asm volatile("s_waitcnt lgkmcnt(0)" ::: "memory");
            bar();
          }
          MK(22);
        }
        if (layer == 0 && wave < 4) bar();
      }
    }
  }
}

}

extern "C" void kernel_launch(void* const* d_in, const int* in_sizes, int n_in, void* d_out, int out_size,
                              void* d_ws, size_t ws_size, hipStream_t stream) {
  const u16* x0 = (const u16*)d_in[0];
  const u16* wqkv = (const u16*)d_in[1];
  const u16* wo = (const u16*)d_in[2];
  const u16* w_in = (const u16*)d_in[4];
  const u16* w_sp = (const u16*)d_in[5];
  const u16* b_sp = (const u16*)d_in[6];
  const u16* w_out = (const u16*)d_in[7];
  char* ws = (char*)d_ws;
  (void)hipFuncSetAttribute((const void*)k_main, hipFuncAttributeMaxDynamicSharedMemorySize, LDS_ALL);
  k_main<<<256, 768, LDS_ALL, stream>>>(x0, wqkv, wo, w_in, w_sp, b_sp, w_out, (u16*)d_out, ws);
}
```

```cpp
#include <hip/hip_runtime.h>
#include <hip/hip_bf16.h>
#include <stdint.h>

#define DEVI __device__ __forceinline__

typedef unsigned short u16;
typedef __attribute__((ext_vector_type(8))) short s16x8;
typedef __attribute__((ext_vector_type(4))) short s16x4;
typedef __attribute__((ext_vector_type(4))) float f32x4;
typedef __attribute__((ext_vector_type(2))) float f32x2;
typedef __attribute__((ext_vector_type(2))) __bf16 bf16x2;
typedef __attribute__((ext_vector_type(2))) unsigned u32x2;
typedef __attribute__((ext_vector_type(4))) unsigned u32x4;

#define MK(I) ((void)0)
#define MX(S, I) ((void)0)
#define MF(I) ((void)0)
#define MF_ARG
#define MF_PASS
#define MK_ARG
#define MK_PASS

#define SPIN_MEM (1u << 22)
#define SAFE_OUT(L)
#define SAFE_AND
#define NOP_(n) "=s"(n)

#define ECT 1
#define SPT 3
#define SPS 1
namespace {

constexpr int S_ = 2048, D_ = 576, F_ = 4608, FH_ = 2304;
constexpr int NQKV = 3 * D_;

constexpr size_t SZ_X = (size_t)S_ * D_ * 2;
constexpr size_t SZ_Z = (size_t)S_ * FH_ * 2;
constexpr size_t OFF_X1 = 0;
constexpr size_t OFF_O = OFF_X1 + 3 * SZ_X;
constexpr size_t OFF_Z2 = OFF_O + 2 * SZ_X;
constexpr size_t OFF_H = OFF_Z2 + 2 * SZ_Z;
constexpr size_t SZ_XS = (size_t)S_ * 8 * 8;
constexpr size_t OFF_XS = OFF_H + 2 * SZ_Z;
constexpr size_t OFF_CSQ = OFF_XS + 3 * SZ_XS;
constexpr size_t OFF_ZP = OFF_CSQ + 256 * 512;
constexpr size_t SZ_ZP = (size_t)16 * 16 * 1024;
constexpr size_t OFF_PQ = OFF_ZP + 2 * SZ_ZP;
constexpr size_t OFF_PO = OFF_PQ + (size_t)16 * 9 * 64 * 112 * 2;
constexpr size_t OFF_PI = OFF_PO + (size_t)D_ * D_ * 2;
constexpr size_t OFF_PW = OFF_PI + (size_t)32 * 9 * 64 * 144 * 2;
constexpr size_t OFF_CSP = OFF_PW + (size_t)FH_ * D_ * 2;
constexpr size_t OFF_CNT = OFF_CSP + (size_t)18 * F_ * 4;
constexpr int CN_X1 = 0, CN_H = 2, CN_X2 = 4, CN_O = 5, CN_Z = 7, CN_PK = 9, CN_H2 = 10, CN_Z2 = 12, NCNT = 14;
constexpr size_t OFF_TAG = OFF_CNT + (size_t)NCNT * 256 * 128;
constexpr size_t OFF_HELLO = OFF_TAG + 128;
constexpr size_t OFF_POT = OFF_HELLO + 2048;
constexpr size_t OFF_CSK = OFF_POT + 1024;
constexpr size_t WS_END = OFF_CSK + 256 * 2048;

DEVI float bf_lo(unsigned u) { return __uint_as_float(u << 16); }
DEVI float bf_hi(unsigned u) { return __uint_as_float(u & 0xffff0000u); }
DEVI unsigned pack2(float a, float b) {
  f32x2 v = {a, b};
  bf16x2 r = __builtin_convertvector(v, bf16x2);
  return __builtin_bit_cast(unsigned, r);
}
DEVI float rbf(float a) {
  return bf_lo(pack2(a, 0.f));
}

DEVI float sum_g(float x) {
  const unsigned u = __float_as_uint(x);
  const auto p = __builtin_amdgcn_permlane32_swap(u, u, false, false);
  const unsigned v = __float_as_uint(__uint_as_float(p[0]) + __uint_as_float(p[1]));
  const auto q = __builtin_amdgcn_permlane16_swap(v, v, false, false);
  return __uint_as_float(q[0]) + __uint_as_float(q[1]);
}

DEVI unsigned vconst(unsigned c) {
  unsigned r;
  asm volatile("v_mov_b32 %0, %1" : "=v"(r) : "s"(c));
  return r;
}
DEVI void place() {
  __builtin_amdgcn_sched_barrier(0);
  asm volatile(".p2align 6");
  __builtin_amdgcn_sched_barrier(0);
}
DEVI const char* uni_ptr(const char* p) {
  unsigned long long v = (unsigned long long)p;
  unsigned lo = __builtin_amdgcn_readfirstlane((unsigned)v), hi = __builtin_amdgcn_readfirstlane((unsigned)(v >> 32));
  asm("s_nop 4" : "+s"(lo), "+s"(hi));
  return (const char*)(((unsigned long long)hi << 32) | lo);
}

DEVI void dma16(const char* sbase, unsigned voff, unsigned m0val) {
  asm volatile("s_mov_b32 m0, %2\n\ts_nop 0\n\tglobal_load_lds_dwordx4 %0, %1"
               :
               : "v"(voff), "s"(sbase), "s"(m0val)
               : "memory");
}

DEVI void ln_finish(float s, float q, float invn, float& mb, float& rinv) {
  float mean = s * invn;
  asm("" : "+v"(mean));
  float var = fmaxf(__builtin_fmaf(-mean, mean, q * invn), 0.f);
  mb = rbf(mean);
  float v = rbf(var);
  float t = rbf(v + 1e-5f);
  float sd = rbf(__builtin_amdgcn_sqrtf(t));
  rinv = __builtin_amdgcn_rcpf(sd);
}

DEVI unsigned pack2v(f32x2 v) { return pack2(v[0], v[1]); }

template <int BM_, int BN_>
struct GemmCfg {
  static constexpr int BM = BM_, BN = BN_, NS = 4;
  static constexpr int P = BN * 2;
  static constexpr int XB = BM * 128, WB = 64 * P, SB = XB + WB;
  static constexpr int LDS_BYTES = NS * SB;
};

DEVI int swap23(int r) { return (r & ~12) | ((r & 4) << 1) | ((r & 8) >> 1); }

DEVI void lds_wr32(unsigned addr, unsigned v) { asm volatile("ds_write_b32 %0, %1" ::"v"(addr), "v"(v) : "memory"); }

template <class C, int MT, int NT, int D, int R, bool XCM, int WT, bool SQ = false, unsigned long long WS = 0>
struct GemmMath {
  static constexpr int WO = WT ? 1 : 2;
  static constexpr int NS = C::NS, SB = C::SB, P = C::P, NU = 2 * NT;
  static constexpr int XPU = MT <= 2 ? MT : MT - 2;
  static constexpr int XU = (MT + XPU - 1) / XPU;
  static_assert(NU % R == 0 && R > D && D <= NT && XU <= D, "shape");
  s16x4 wlo[R], whi[R];
  s16x8 wf[R];
  s16x8 xf[2][MT];
  unsigned xrd[2], wrd, xa[2], wa, so;
  f32x4 (&acc)[MT][NT];
  f32x4 accq[SQ ? MT : 1];
  unsigned long long wtab = WS;
  DEVI GemmMath(f32x4 (&a)[MT][NT]) : acc(a) {}

  template <int PU>
  DEVI void wread_lo() {
    constexpr int off = (PU / NT) * 32 * P + (PU % NT) * (WT == 2 ? 1024 : 32);
    if constexpr (WT) asm volatile("ds_read_b128 %0, %1 offset:%2" : "=v"(wf[PU % R]) : "v"(wa), "n"(off));
    else asm volatile("ds_read_b64_tr_b16 %0, %1 offset:%2" : "=v"(wlo[PU % R]) : "v"(wa), "n"(off));
  }
  template <int PU>
  DEVI void wread_hi() {
    if constexpr (!WT)
      asm volatile("ds_read_b64_tr_b16 %0, %1 offset:%2" : "=v"(whi[PU % R]) : "v"(wa), "n"((PU / NT) * 32 * P + (PU % NT) * 32 + 8 * P));
  }
  template <int H, int M>
  DEVI void xread1() {
    asm volatile("ds_read_b128 %0, %1 offset:%2" : "=v"(xf[H][M]) : "v"(xa[H]), "n"(M * (XCM ? 256 : 2048)));
  }
  static constexpr int xops(int v) {
    const int k = (v + D) % NT;
    if (k >= XU) return 0;
    const int n = MT - k * XPU;
    return n < XPU ? n : XPU;
  }
  static constexpr int ops(int v, bool last) { return (last && v + D >= NU) ? 0 : WO + xops(v); }
  static constexpr int newer(int U, bool last) {
    int n = 0;
    int v0 = U - D;
    if (U % NT == 0) v0 += XU - 1;
    else n += v0 >= 0 ? xops(v0) : xops(v0 + NU);
    for (int v = v0 + 1; v < U; ++v) n += v >= 0 ? ops(v, last) : ops(v + NU, false);
    return n;
  }
  template <int U, int M_>
  DEVI void mf() {
    constexpr int M = ((U % NT) & 1) ? MT - 1 - M_ : M_;
    s16x8 w;
    if constexpr (WT) w = wf[U % R];
    else w = __builtin_shufflevector(wlo[U % R], whi[U % R], 0, 1, 2, 3, 4, 5, 6, 7);
    acc[M][U % NT] = __builtin_amdgcn_mfma_f32_16x16x32_bf16(w, xf[U / NT][M], acc[M][U % NT], 0, 0, 0);
    __builtin_amdgcn_sched_barrier(0);
  }
  DEVI void pin() {
#pragma unroll
    for (int m = 0; m < MT; ++m)
#pragma unroll
      for (int n = 0; n < NT; ++n) asm volatile("" : "+v"(acc[m][n]));
  }
  template <int U, int I>
  DEVI void dsop() {
    constexpr int PU = (U + D) % NU;
    if constexpr (I == 0) wread_lo<PU>();
    else if constexpr (I == 1) wread_hi<PU>();
    else if constexpr (I - 2 < xops(U)) {
      constexpr int k = (U + D) % NT;
      xread1<((U + D - k) % NU) / NT, k * XPU + I - 2>();
    }
  }
  template <int U, bool LAST, bool BAR, int M>
  DEVI void slot() {
    constexpr bool PF = !(LAST && U + D >= NU);
    mf<U, M>();
    if constexpr (M == 0 && PF && U + D == NU) {
      if constexpr (BAR) __builtin_amdgcn_s_barrier();
      so = (so == (unsigned)((NS - 1) * SB)) ? 0u : so + (unsigned)SB;
      wa = wrd + (WS ? ((unsigned)wtab & 63u) << 11 : so);
      wtab >>= 6;
      xa[0] = xrd[0] + so;
      xa[1] = xrd[1] + so;
      __builtin_amdgcn_sched_barrier(0);
    }
    if constexpr (PF) {
      dsop<U, M>();
      if constexpr (M == MT - 1) {
        if constexpr (MT <= 1) dsop<U, 1>();
        if constexpr (MT <= 2) {
          dsop<U, 2>();
          dsop<U, 3>();
        }
      }
      __builtin_amdgcn_sched_barrier(0);
    }
  }
  template <int U, bool LAST, bool BAR, int... M>
  DEVI void unit(std::integer_sequence<int, M...>) {
    static_assert(newer(U, LAST) <= 15, "lgkmcnt range");
    asm volatile("s_waitcnt lgkmcnt(%0)" ::"n"(newer(U, LAST)));
    __builtin_amdgcn_sched_barrier(0);
    (slot<U, LAST, BAR, M>(), ...);
    if constexpr (SQ && (U % NT) < MT) {
      constexpr int m = U % NT;
      accq[m] = __builtin_amdgcn_mfma_f32_16x16x32_bf16(xf[U / NT][m], xf[U / NT][m], accq[m], 0, 0, 0);
      __builtin_amdgcn_sched_barrier(0);
    }
    __builtin_amdgcn_sched_barrier(0);
  }
  template <bool LAST, bool BAR, int... U>
  DEVI void stage(std::integer_sequence<int, U...>) {
    (unit<U, LAST, BAR>(std::make_integer_sequence<int, MT>{}), ...);
  }
  template <int... U>
  DEVI void prow(std::integer_sequence<int, U...>) {
    ((wread_lo<U>(), wread_hi<U>()), ...);
  }
  template <int... M>
  DEVI void prox(std::integer_sequence<int, M...>) {
    (xread1<0, M>(), ...);
  }
  DEVI void setup(unsigned lds0, int mt0, int nt0, int lane, unsigned so0) {
    const int g = lane >> 4, r = lane & 15, q = (lane >> 2) & 3, p = lane & 3;
#pragma unroll
    for (int h = 0; h < 2; ++h)
      xrd[h] = XCM ? lds0 + (unsigned)((4 * h + g) * (C::BM * 16) + (mt0 * 16 + r) * 16)
                   : lds0 + (unsigned)((mt0 * 16 + r) * 128 + (((4 * h + g) ^ ((r >> 1) & 7)) * 16));
    wrd = WT == 2   ? lds0 + (unsigned)(C::XB + nt0 * 1024 + lane * 16)
          : WT == 1 ? lds0 + (unsigned)(C::XB + (g * 8 + (r & 7)) * P + (r >> 3) * 16 + nt0 * 32)
                    : lds0 + (unsigned)(C::XB + (16 * (g >> 1) + 4 * (g & 1) + q) * P + 8 * p + nt0 * 32);
    so = so0;
    xa[0] = xrd[0] + so;
    xa[1] = xrd[1] + so;
    wa = wrd + so;
  }
  DEVI void first() {
    prox(std::make_integer_sequence<int, MT>{});
    prow(std::make_integer_sequence<int, D>{});
    asm volatile("s_waitcnt lgkmcnt(0)");
  }
  template <int N, bool BAR = true>
  DEVI void mid() {
    if constexpr (N > 1) place();
#pragma unroll 1
    for (int s = 0; s < N; ++s) stage<false, BAR>(std::make_integer_sequence<int, NU>{});
  }
  DEVI void last() {
    stage<true, true>(std::make_integer_sequence<int, NU>{});
    pin();
  }
  template <int KS>
  DEVI void run(unsigned lds0, int mt0, int nt0, int lane, unsigned so0) {
    setup(lds0, mt0, nt0, lane, so0);
    __builtin_amdgcn_s_barrier();
    first();
    mid<KS - 1>();
    last();
  }
};

DEVI const char* code_here() {
  unsigned long long pc;
  asm volatile("s_getpc_b64 %0" : "=s"(pc));
  return (const char*)pc;
}
DEVI int okmap(int p) {
  const int ch = p >> 3, e = p & 7, c = (ch - 8) & 3;
  return ch < 8 ? (2 * ch + (e >> 2)) * 36 + 16 + (e & 3) : ((ch - 8) >> 2) * 36 + c * 8 + (c >> 1) * 4 + e;
}

DEVI void pk_st16(void* p, u32x4 v) {
  asm volatile("global_store_dwordx4 %0, %1, off sc1\n\ts_nop 1" ::"v"(p), "v"(v) : "memory");
}

using K1C = GemmCfg<144, 112>;
constexpr int K1_TP = 240;
constexpr int K1_T_OFF = K1C::SB;
constexpr int K1_ST_OFF = 163840 - 128 - 1152;
constexpr int K1_CS_OFF = 4 * K1C::SB;
constexpr int K1_WX = 4 * K1C::SB + 2048;
constexpr unsigned long long K1_WS = 16ull | 32ull << 6 | 48ull << 12 | 56ull << 18 | 63ull << 24 | 16ull << 30 | 32ull << 36 | 48ull << 42;
static_assert(K1C::SB == 16 * 2048 && K1_WX - K1C::XB == 56 * 2048 && K1C::WB == 7 * 2048 && K1_WX + 2 * K1C::WB <= K1_ST_OFF, "K1 w places");
static_assert(K1_T_OFF + 144 * K1_TP + 64 <= 2 * K1C::SB + K1C::XB, "K1 LDS alias");
using K1R = GemmCfg<144, 144>;
constexpr int K1R_T_OFF = K1R::SB;
constexpr int K1R_CS_OFF = K1R_T_OFF + 144 * K1_TP;
constexpr int K1R_ST_OFF = K1R_CS_OFF + 512;
constexpr int K1R_DUMMY = K1R::LDS_BYTES;
constexpr int K1R_PO = K1R_DUMMY + 1024;
constexpr int K1R_CNT = 163840 - 128 + 28 * 4;
#define SPIN_ASM(TEST)                                                                                                   \
  if constexpr (SL <= 1)                                                                                                 \
    asm volatile(".La%=:\n\tds_read_b32 %0, %3\n\ts_waitcnt lgkmcnt(0)\n\tv_readfirstlane_b32 %2, %0\n\t" TEST SAFE_AND \
                 "s_cbranch_scc1 .La%="                                                                                  \
                 : "=&v"(v), NOP_(c), "=&s"(t)                                                                           \
                 : "v"(addr), "s"(k)                                                                                     \
                 : "memory", "scc");                                                                                     \
  else                                                                                                                   \
    asm volatile("s_branch .Lb%=\n.La%=:\n\ts_sleep %5\n.Lb%=:\n\tds_read_b32 %0, %3\n\ts_waitcnt lgkmcnt(0)\n\t"     \
                 "v_readfirstlane_b32 %2, %0\n\t" TEST SAFE_AND "s_cbranch_scc1 .La%="                                  \
                 : "=&v"(v), NOP_(c), "=&s"(t)                                                                           \
                 : "v"(addr), "s"(k), "n"(SL)                                                                            \
                 : "memory", "scc")
template <int SL>
DEVI void spin_ge(unsigned addr, unsigned k) {
  unsigned v, c = 1u << 18, t;
  SPIN_ASM("s_cmp_lt_u32 %2, %4\n\t");
}
template <int SL>
DEVI void spin_b4(unsigned addr, unsigned n) {
  unsigned v, c = 1u << 18, t;
  const unsigned k = (0x80u - n) * 0x01010101u;
  SPIN_ASM("s_add_u32 %2, %2, %4\n\ts_andn2_b32 %2, 0x80808080, %2\n\t");
}
DEVI void msync(unsigned addr) {
  asm volatile("s_waitcnt lgkmcnt(0)" ::: "memory");
  asm volatile("s_mov_b64 exec, 1\n\tds_add_u32 %0, %1\n\ts_mov_b64 exec, -1" ::"v"(addr), "v"(vconst(1u)) : "memory");
  spin_ge<0>(addr, 8u);
}
static_assert(K1R_PO + 13824 + 256 <= 163840 - 128 && K1R_ST_OFF + 144 * 8 <= 3 * K1R::SB, "K1R LDS");

DEVI void touch64(const char* sbase, unsigned voff, unsigned m0val) {
  asm volatile("s_mov_b32 m0, %2\n\ts_nop 0\n\tglobal_load_lds_dword %0, %1" : : "v"(voff), "s"(sbase), "s"(m0val) : "memory");
}

template <int MT, bool G1>
DEVI void k1_gemm_raw(char* smem, int hp, int mt0, int lane MF_ARG) {
  constexpr int NT = 4, nt0 = G1 ? 4 : 0;
  const unsigned lds0 = (unsigned)(size_t)smem;
  const int g = lane >> 4, r = lane & 15;
  unsigned ta[NT];
#pragma unroll
  for (int nt = 0; nt < NT; ++nt) {
    const int j = (nt0 + nt) * 16 + g * 4, seg = j / 40, cj = j - seg * 40 - (hp & 1) * 4;
    const int to = (seg < 3 && cj >= 0 && cj < 36) ? seg * 80 + cj * 2 + (cj >= 20 ? 8 : 0) : (seg < 3 ? 40 : 120);
    ta[nt] = lds0 + (unsigned)(K1R_T_OFF + (mt0 * 16 + r) * K1_TP + to);
    asm volatile("" : "+v"(ta[nt]));
  }
  unsigned sa = lds0 + (unsigned)(K1R_ST_OFF + (mt0 * 16 + r) * 8), ca = lds0 + (unsigned)(K1R_CS_OFF + (nt0 * 16 + g * 4) * 4);
  asm volatile("" : "+v"(sa), "+v"(ca));
  f32x4 acc[MT][NT];
#pragma unroll
  for (int mt = 0; mt < MT; ++mt)
#pragma unroll
    for (int nt = 0; nt < NT; ++nt) acc[mt][nt] = f32x4{0.f, 0.f, 0.f, 0.f};
  {
    GemmMath<K1R, MT, 4, 3, 4, false, 0, G1> gm(acc);
    if constexpr (G1) {
#pragma unroll
      for (int mt = 0; mt < MT; ++mt) gm.accq[mt] = f32x4{0.f, 0.f, 0.f, 0.f};
    }
    MF(14);
    if constexpr (!G1) {
      asm volatile("s_waitcnt vmcnt(0)" ::: "memory");
    }
    MF(15);
    gm.template run<9>(lds0, mt0, nt0, lane, 0u);
    MF(2);
    if constexpr (G1) {
#pragma unroll
      for (int mt = 0; mt < MT; ++mt) {
        const f32x4 q = gm.accq[mt];
        const float d = (r & 2) ? ((r & 1) ? q[3] : q[2]) : ((r & 1) ? q[1] : q[0]);
        const float qq = sum_g(g == (r >> 2) ? d : 0.f);
        float mb, rinv;
        ln_finish(acc[mt][3][0], qq, 1.0f / (float)D_, mb, rinv);
        const f32x2 st = {-mb, rinv};
        if (g == 2) asm volatile("ds_write_b64 %0, %1 offset:%2" ::"v"(sa), "v"(st), "n"(mt * 128) : "memory");
      }
    }
  }
  if (mt0 == 0 && r == 0) {
#pragma unroll
    for (int nt = 0; nt < NT; ++nt) *reinterpret_cast<f32x4*>(smem + K1R_CS_OFF + ((nt0 + nt) * 16 + g * 4) * 4) = acc[0][nt];
  }
  msync(lds0 + (unsigned)K1R_CNT);
  MF(3);
  f32x2 st[MT];
  f32x4 cs[NT];
#pragma unroll
  for (int nt = 0; nt < NT; ++nt) asm volatile("ds_read_b128 %0, %1 offset:%2" : "=v"(cs[nt]) : "v"(ca), "n"(nt * 64) : "memory");
#pragma unroll
  for (int mt = 0; mt < MT; ++mt) asm volatile("ds_read_b64 %0, %1 offset:%2" : "=v"(st[mt]) : "v"(sa), "n"(mt * 128) : "memory");
#pragma unroll
  for (int i = 0; i < MT; ++i) asm volatile("s_waitcnt lgkmcnt(0)" : "+v"(st[i]));
#pragma unroll
  for (int i = 0; i < NT; ++i) asm volatile("" : "+v"(cs[i]));
#pragma unroll
  for (int mt = 0; mt < MT; ++mt) {
    f32x2 M = {st[mt][0], st[mt][0]}, R = {st[mt][1], st[mt][1]};
    asm volatile("" : "+v"(M), "+v"(R));
#pragma unroll
    for (int nt = 0; nt < NT; ++nt) {
      const f32x2 v0 = R * __builtin_elementwise_fma(M, f32x2{cs[nt][0], cs[nt][1]}, f32x2{acc[mt][nt][0], acc[mt][nt][1]});
      const f32x2 v1 = R * __builtin_elementwise_fma(M, f32x2{cs[nt][2], cs[nt][3]}, f32x2{acc[mt][nt][2], acc[mt][nt][3]});
      acc[mt][nt][0] = __uint_as_float(pack2v(v0));
      acc[mt][nt][1] = __uint_as_float(pack2v(v1));
      const f32x2 pk = {acc[mt][nt][0], acc[mt][nt][1]};
      asm volatile("ds_write_b64 %0, %1 offset:%2" ::"v"(ta[nt]), "v"(pk), "n"(mt * 16 * K1_TP) : "memory");
    }
  }
}

template <int N>
DEVI void wait_vm() {
  asm volatile("s_waitcnt vmcnt(%0)" ::"n"(N) : "memory");
}

constexpr int LDS_ALL = 163840;
constexpr int LW_OFF = LDS_ALL - 128;
constexpr int LW_GO = 0, LW_T = 2, LW_O = 4  , LW_X1 = 6, LW_H = 8, LW_X2 = 12, LW_PK = 13, LW_Z = 14, LW_PAIR = 16, LW_AD = 20,
              LW_L = 22, LW_S = 24, LW_ST = 29, LW_HELLO = 30, LW_Q = 31;
DEVI void announce(unsigned addr, unsigned n, char* ws, int kind, int who, bool act) {
  const char* cb = uni_ptr(ws + OFF_CNT + (size_t)kind * 256 * 128);
  const unsigned long long mask = __ballot(act);
  unsigned t, u;
  asm volatile(
      "s_waitcnt vmcnt(0)\n\t"
      "s_mov_b64 exec, 1\n\t"
      "ds_add_rtn_u32 %0, %2, %3\n\t"
      "s_waitcnt lgkmcnt(0)\n\t"
      "v_readfirstlane_b32 %1, %0\n\t"
      "s_cmp_eq_u32 %1, %4\n\t"
      "s_cselect_b64 exec, %5, 0\n\t"
      "global_atomic_add %6, %3, %7 sc1\n\t"
      "s_mov_b64 exec, -1\n\t"
      "s_nop 0"
      : "=&v"(t), "=&s"(u)
      : "v"(addr), "v"(vconst(1u)), "s"(n - 1u), "s"(mask), "v"((unsigned)(who & 255) * 128u), "s"(cb)
      : "memory", "scc");
}
DEVI void cnt_wait(const char* ws, int kind, int who, unsigned n) {
  const char* cp = uni_ptr(ws + OFF_CNT + (size_t)(kind * 256 + who) * 128);
  unsigned v, c = SPIN_MEM, t;
  asm volatile(
      ".La%=:\n\tglobal_load_dword %0, %3, %4 sc1\n\ts_sub_u32 %1, %1, 1\n\ts_waitcnt vmcnt(0)\n\tv_readfirstlane_b32 %2, %0\n\ts_cmp_lg_u32 %2, %5\n\t"
      "s_cselect_b32 %2, %1, 0\n\ts_cmp_lg_u32 %2, 0\n\ts_cbranch_scc1 .La%="
      : "=&v"(v), "+s"(c), "=&s"(t)
      : "v"(vconst(0u)), "s"(cp), "s"(n - 1u)
      : "memory", "scc");
}
DEVI void dma16_sc1(const char* sbase, unsigned voff, unsigned m0val) {
  asm volatile("s_mov_b32 m0, %2\n\ts_nop 0\n\tglobal_load_lds_dwordx4 %0, %1 sc1"
               :
               : "v"(voff), "s"(sbase), "s"(m0val)
               : "memory");
}
template <int N, bool SC1>
DEVI void dma4(const char* sbase, unsigned v0, unsigned v1, unsigned v2, unsigned v3, unsigned m0val) {
#define DMA4(S)                                                                                                               \
  asm volatile("s_mov_b32 m0, %5\n\ts_nop 0\n\tglobal_load_lds_dwordx4 %0, %4" S "\n\t.if %6 > 1\n\tglobal_load_lds_dwordx4 %1, %4 offset:1024" S \
               "\n\t.endif\n\t.if %6 > 2\n\tglobal_load_lds_dwordx4 %2, %4 offset:2048" S                                    \
               "\n\t.endif\n\t.if %6 > 3\n\tglobal_load_lds_dwordx4 %3, %4 offset:3072" S "\n\t.endif"                      \
               :                                                                                                              \
               : "v"(v0), "v"(v1), "v"(v2), "v"(v3), "s"(sbase), "s"(m0val), "n"(N)                                           \
               : "memory")
  if constexpr (SC1) DMA4(" sc1");
  else DMA4("");
}
template <int N>
DEVI void dma_seq(const char* src, unsigned voff, unsigned m0val) {
  if constexpr (N > 0) {
    dma4<(N < 4 ? N : 4), true>(src, voff, voff, voff, voff, m0val);
    dma_seq<N - 4>(src + 4096, voff, m0val + 4096u);
  }
}
DEVI void bar() { __builtin_amdgcn_s_barrier(); }
DEVI void lds_inc(unsigned addr, unsigned by = 1u) {
  asm volatile("s_mov_b64 exec, 1\n\tds_add_u32 %0, %1\n\ts_mov_b64 exec, -1" ::"v"(addr), "v"(vconst(by)) : "memory");
}
DEVI void lds_spin(unsigned addr, unsigned n) {
  spin_ge<1>(addr, n);
}
template <int SL = 1>
DEVI void lds_spin4(unsigned addr, unsigned n) {
  spin_b4<SL>(addr, n);
}
DEVI int lane_id() {
  int l;
  asm volatile("v_mbcnt_lo_u32_b32 %0, -1, 0\n\tv_mbcnt_hi_u32_b32 %0, -1, %0" : "=v"(l));
  __builtin_assume((unsigned)l < 64u);
  return l;
}

template <int I>
DEVI u32x4 tr8(const u32x4 (&v)[8]) {
  constexpr unsigned sel = (I & 1) ? 0x07060302u : 0x05040100u;
  return u32x4{__builtin_amdgcn_perm(v[1][I / 2], v[0][I / 2], sel), __builtin_amdgcn_perm(v[3][I / 2], v[2][I / 2], sel),
               __builtin_amdgcn_perm(v[5][I / 2], v[4][I / 2], sel), __builtin_amdgcn_perm(v[7][I / 2], v[6][I / 2], sel)};
}
template <int PITCH, bool SUM, int... I>
DEVI void tr8_store(const u32x4 (&v)[8], unsigned dst, float (&cs)[8], std::integer_sequence<int, I...>) {
  auto one = [&](auto I_) {
    const u32x4 o = tr8<I_>(v);
    const unsigned d_ = dst;
    asm volatile("ds_write_b128 %0, %1 offset:%2" ::"v"(d_), "v"(o), "n"(I_ * PITCH) : "memory");
    if constexpr (SUM) {
#pragma unroll
      for (int d = 0; d < 4; ++d) {
        const unsigned od = o[d];
        cs[I_] = __builtin_amdgcn_fdot2_f32_bf16(__builtin_bit_cast(bf16x2, od), __builtin_bit_cast(bf16x2, 0x3f803f80u), cs[I_], false);
      }
    }
  };
  (one(std::integral_constant<int, I>{}), ...);
}
template <int NB>
DEVI void lds_out(unsigned src, char* dst, int lane) {
  constexpr int N = (NB + 1023) / 1024;
  u32x4 t[N];
  const unsigned a = src + (unsigned)lane * 16u;
#pragma unroll
  for (int i = 0; i < N; ++i) asm volatile("ds_read_b128 %0, %1 offset:%2" : "=v"(t[i]) : "v"(a), "n"(i * 1024) : "memory");
  const char* d = uni_ptr(dst);
#pragma unroll
  for (int i = 0; i < N; ++i) {
    asm volatile("s_waitcnt lgkmcnt(%1)" : "+v"(t[i]) : "n"(N - 1 - i) : "memory");
    if (i < N - 1 || lane < (NB - (N - 1) * 1024) / 16)
      asm volatile("global_store_dwordx4 %0, %1, %2 sc1\n\ts_nop 1" ::"v"((unsigned)lane * 16u), "v"(t[i]), "s"(d + i * 1024) : "memory");
  }
}
template <int A, int B, class F>
DEVI void static_for(F f) {
  if constexpr (A < B) {
    f(std::integral_constant<int, A>{});
    static_for<A + 1, B>(f);
  }
}
constexpr int WSP_V = 3;
constexpr int RAW_S1 = 6;
constexpr int RAW_S0 = 4;
template <int E>
struct RawX {
  static constexpr int first(int s) {
    return s <= RAW_S0 ? 0 : (s > RAW_S1 ? E : (E * (s - RAW_S0) + RAW_S1 - RAW_S0) / (RAW_S1 + 1 - RAW_S0));
  }
  static constexpr int n(int s) { return first(s + 1) - first(s); }
};
struct RawLd {
  static constexpr int VPS = 10, SB = K1R::SB;
  unsigned xoff[5], woff[5], m0x;
  unsigned long long xact0, wact[5], tag;
  const char *xp, *wp;
  int qb;
  DEVI void q_hook(int st, char* ws, unsigned lds0, int hp) {
    if ((qb >> 1) == st) {
      const int lane = lane_id(), g = lane >> 4;
      const unsigned a = lds0 + (unsigned)((st & 3) * SB + K1R::XB + ((qb & 1) * 32 + 16 * (g >> 1) + 4 * (g & 1) + ((lane >> 2) & 3)) * 288 +
                                           (lane & 3) * 8 + (hp & 1) * 8);
      const unsigned a2 = a + ((lane & 3) >= 1 ? 8u : 0u), a4 = a + ((lane & 3) >= 2 ? 8u : 0u);
      u32x2 qf[7][2];
#pragma unroll
      for (int nt = 0; nt < 7; ++nt)
        asm volatile("ds_read_b64_tr_b16 %0, %2 offset:%3\n\tds_read_b64_tr_b16 %1, %2 offset:%4"
                     : "=&v"(qf[nt][0]), "=&v"(qf[nt][1])
                     : "v"(nt == 2 ? a2 : (nt == 4 ? a4 : a)), "n"(nt * 32 + (nt > 4 ? 16 : (nt > 2 ? 8 : 0))),
                       "n"(2304 + nt * 32 + (nt > 4 ? 16 : (nt > 2 ? 8 : 0)))
                     : "memory");
      const char* d = uni_ptr(ws + OFF_PQ + (size_t)(hp * 18 + qb) * 7168 + 3072);
#pragma unroll
      for (int nt = 0; nt < 7; ++nt) {
        asm volatile("s_waitcnt lgkmcnt(%2)" : "+v"(qf[nt][0]), "+v"(qf[nt][1]) : "n"(12 - 2 * nt) : "memory");
        const u32x4 v = {qf[nt][0][0], qf[nt][0][1], qf[nt][1][0], qf[nt][1][1]};
        asm volatile("global_store_dwordx4 %0, %1, %2 offset:%3 sc1\n\ts_nop 1" ::"v"((unsigned)lane * 16u), "v"(v), "s"(d), "n"(nt * 1024 - 3072) : "memory");
      }
      if (st == 8) lds_wr32(lds0 + (unsigned)(LW_OFF + LW_Q * 4), 1u);
    }
  }
  DEVI void stage_x(unsigned so) {
    asm volatile(
        "s_mov_b32 m0, %6\n\ts_mov_b64 exec, %7\n\tglobal_load_lds_dwordx4 %0, %5\n\ts_mov_b64 exec, -1\n\t"
        "global_load_lds_dwordx4 %1, %5 offset:1024\n\tglobal_load_lds_dwordx4 %2, %5 offset:2048\n\tglobal_load_lds_dwordx4 %3, %5 offset:3072\n\t"
        "s_add_u32 m0, m0, 0x1000\n\ts_mov_b32 exec_hi, 0\n\tglobal_load_lds_dwordx4 %4, %5\n\ts_mov_b32 exec_hi, -1" ::"v"(xoff[0]),
        "v"(xoff[1]), "v"(xoff[2]), "v"(xoff[3]), "v"(xoff[4]), "s"(xp), "s"(m0x + so), "s"(xact0)
        : "memory", "scc");
    xp += 128;
  }
  DEVI void stage_w(unsigned so) {
    asm volatile(
        "s_mov_b32 m0, %6\n\ts_mov_b64 exec, %7\n\tglobal_load_lds_dwordx4 %0, %5\n\ts_mov_b64 exec, %8\n\t"
        "global_load_lds_dwordx4 %1, %5 offset:1024\n\ts_mov_b64 exec, %9\n\tglobal_load_lds_dwordx4 %2, %5 offset:2048\n\t"
        "s_mov_b64 exec, %10\n\tglobal_load_lds_dwordx4 %3, %5 offset:3072\n\ts_add_u32 m0, m0, 0x1000\n\ts_mov_b64 exec, %11\n\t"
        "global_load_lds_dwordx4 %4, %5\n\ts_mov_b64 exec, -1" ::"v"(woff[0]),
        "v"(woff[1]), "v"(woff[2]), "v"(woff[3]), "v"(woff[4]), "s"(wp), "s"(m0x + so + (unsigned)K1R::XB), "s"(wact[0]), "s"(wact[1]),
        "s"(wact[2]), "s"(wact[3]), "s"(wact[4])
        : "memory", "scc");
    wp += 64 * NQKV * 2;
  }
  DEVI void stage(unsigned so) {
    stage_x(so);
    stage_w(so);
  }
  template <class F>
  DEVI void front(const u16* x, const u16* wqkv, const u16* wo, char* ws, int bid, unsigned lds0, int wl, int lane, int rt, int hp,
                  int& pj, F jobs, const char*& pg MF_ARG) {
    xp = uni_ptr((const char*)x - 3072);
    wp = uni_ptr((const char*)wqkv + ((hp * 72) & ~15) - 3072);
    m0x = lds0 + (unsigned)(wl * 288 * 16);
    xact0 = wl ? ~0ull : ~0xffull;
    const int row0 = wl * 36 + (lane >> 3);
#pragma unroll
    for (int i = 0; i < 5; ++i) {
      const int row = row0 + i * 8;
      int gr = rt * 128 - 16 + row;
      if (gr < 0) gr = 0;
      xoff[i] = (unsigned)(gr * (D_ * 2) + (3 - (i & 3)) * 1024) + (unsigned)(((lane ^ (row >> 1)) & 7) * 16);
    }
    stage_x(0u);
    u32x2 tg;
    asm volatile("global_load_dwordx2 %0, %1, %2 sc1" : "=v"(tg) : "v"(0u), "s"(uni_ptr(ws + OFF_TAG)) : "memory");
    asm volatile("" : "+v"(lane));
#pragma unroll
    for (int i = 0; i < 5; ++i) {
      const unsigned c = (unsigned)(i * 64 + (lane & 63));
      const unsigned wr = (c * 3641u) >> 16, wc = c - wr * 18u, seg = (wc * 13u) >> 6;
      wact[i] = __ballot(wc < 15u) & (i < 4 ? ~0ull : 0xffffffffull);
      woff[i] = (unsigned)(wl * 16 + swap23((int)wr)) * (unsigned)(NQKV * 2) + seg * (unsigned)(D_ * 2 - 80) + wc * 16u + (unsigned)((3 - (i & 3)) * 1024);
    }
    stage_w(0u);
    stage((unsigned)SB);
    jobs();
    MF(10);
    asm volatile("s_waitcnt vmcnt(%1)" : "+v"(tg) : "n"(VPS) : "memory");
    tag = (((unsigned long long)(unsigned)__builtin_amdgcn_readfirstlane(tg[1]) << 32) | (unsigned)__builtin_amdgcn_readfirstlane(tg[0])) +
          0x9e3779b97f4a7c15ull;
    MF(1);
    __builtin_amdgcn_s_barrier();
    MF(15);
    if (wl == 0 && lane == 0)
      asm volatile("global_store_dwordx2 %0, %1, off sc1\n\ts_nop 1" ::"v"(ws + OFF_HELLO + bid * 8), "v"(tag) : "memory");
    stage((unsigned)(2 * SB));
    qb = wl == 3 ? rt : ((wl == 2 && rt < 2) ? 16 + rt : 99);
    q_hook(0, ws, lds0, hp);
    const int nb = pj;
    const bool po = pj >= 0 && wl < 2;
    u32x4 v[8];
    unsigned da = 0u;
    if (po) {
      const int l54 = lane < 54 ? lane : 53, gl = l54 / 9, c = l54 - gl * 9, kl = wl * 6 + gl, t = nb / 6, kg = (nb - t * 6) * 12 + kl;
      da = lds0 + (unsigned)(K1R_PO + (kl >> 2) * 4608 + (16 * (kl & 1) + ((kl >> 1) & 1)) * 144 + c * 16);
      const unsigned sa[2] = {(unsigned)(okmap(kg * 8) * D_ + t * 72 + c * 8) * 2u, (unsigned)(okmap(kg * 8 + 4) * D_ + t * 72 + c * 8) * 2u};
      const char* ps = uni_ptr((const char*)wo);
#pragma unroll
      for (int j = 0; j < 8; ++j)
        asm volatile("global_load_dwordx4 %0, %1, %2 offset:%3" : "=v"(v[j]) : "v"(sa[j / 4]), "s"(ps), "n"((j % 4) * D_ * 2) : "memory");
    } else {
#pragma unroll
      for (int j = 0; j < 8; ++j) asm volatile("" : "=v"(v[j]));
    }
    unsigned slot = (unsigned)(3 * SB), pd = 0u;
#pragma unroll 1
    for (int S = 0; S < RAW_S0; ++S) {
      if (SPT && wl == 2 && S == SPS) {
        const char* q = ws + (size_t)(bid >> 3) * (2u << 20);
        if (SPT & 1) asm volatile("s_load_dword %0, %1, 0" : "+s"(pd) : "s"(q));
        if (SPT & 4) asm volatile("s_load_dword %0, %1, 0x80000\n\ts_load_dword %0, %2, 0\n\ts_load_dword %0, %2, 0x80000" : "+s"(pd) : "s"(q), "s"(q + 0x100000));
        if (SPT & 2) asm volatile("s_load_dword %0, %1, 0" : "+s"(pd) : "s"(pg));
      }
      asm volatile("" : "+s"(pd));
      wait_vm<VPS>();
      __builtin_amdgcn_s_barrier();
      if (S == 0) MF(16);
      if (S == 1) MF(17);
      if (S == 2) MF(18);
      if (S == 3) MF(19);
      stage(slot);
      slot = (slot == (unsigned)(3 * SB)) ? 0u : slot + (unsigned)SB;
      q_hook(S + 1, ws, lds0, hp);
      if (po && S == 1) {
        float cs[8];
        asm volatile("s_nop 0 ; LOOPWAIT" : "+v"(v[0]), "+v"(v[1]), "+v"(v[2]), "+v"(v[3]), "+v"(v[4]), "+v"(v[5]), "+v"(v[6]), "+v"(v[7]));
        tr8_store<288, false>(v, da, cs, std::make_integer_sequence<int, 8>{});
        asm volatile("s_waitcnt lgkmcnt(0)" ::: "memory");
      }
      if (po && S == 2) lds_out<6912>(lds0 + (unsigned)(K1R_PO + wl * 6912), ws + OFF_PO + (size_t)nb * 13824 + wl * 6912, lane);
    }
    asm volatile("s_waitcnt lgkmcnt(0)" : "+s"(pd));
  }
};
template <bool JOB>
DEVI void k1_raw_loader(RawLd& ld, const u16* __restrict__ w_in, const u16* __restrict__ w_out, char* __restrict__ ws,
                        unsigned long long tag, int bid, unsigned lds0, int wl, int lane, int rt, int hp, int ct, int job, int pj MF_ARG) {
  constexpr int VPS = RawLd::VPS, SB = RawLd::SB, E = JOB ? 16 : 0;
  const bool isi = job < 144;
  const int kb = isi ? job >> 3 : job - 144;
  const int pitch = isi ? F_ * 2 : D_ * 2;
  const int l36 = lane < 36 ? lane : 35;
  unsigned sa = (unsigned)l36 * 16u, da;
  if (isi) da = lds0 + (unsigned)((l36 >= 18 ? 9216 - 288 : 0) + wl * 8 * 288 + l36 * 16);
  else da = lds0 + (unsigned)((l36 / 9) * (4608 - 144) + (16 * (wl & 1) + (wl >> 1)) * 144 + l36 * 16);
  asm volatile("" : "+v"(sa), "+v"(da));
  u32x4 v[2][8];
  const char* ps = uni_ptr((const char*)(isi ? w_in + (job & 7) * 576 : w_out) + (size_t)(kb * 32 + wl * 8) * pitch);
  auto extra = [&](auto J_) {
    constexpr int j = J_;
    const char* q = ps + (j / 2) * pitch;
    u32x4 t;
    asm volatile("global_load_dwordx4 %0, %1, %2 offset:%3" : "=v"(t) : "v"(sa), "s"(q), "n"((j % 2) * 576) : "memory");
    v[j % 2][j / 2] = t;
  };
  auto step = [&](auto S_) {
    constexpr int S = S_;
    wait_vm<RawX<E>::n(S - 2) + (S <= 6 ? VPS : 0) + RawX<E>::n(S - 1)>();
    __builtin_amdgcn_s_barrier();
    MF(16 + S);
    if constexpr (S == RAW_S0) {
      static_assert(RAW_S0 >= 4, "wo blocks");
      if (pj >= 0 && wl == 0 && lane == 0)
        asm volatile("global_store_dwordx2 %0, %1, off sc1\n\ts_nop 1" ::"v"(ws + OFF_POT + (pj / 6) * 128 + (pj % 6) * 8), "v"(tag) : "memory");
    }
    if constexpr (S <= 5) ld.stage((unsigned)(((S + 3) % 4) * SB));
    ld.q_hook(S + 1, ws, lds0, hp);
    static_for<RawX<E>::first(S), RawX<E>::first(S + 1)>(extra);
  };
  const char* hp_ = ws + OFF_HELLO + lane * 32;
  u32x4 a, b;
  if (wl == 1) asm volatile("global_load_dwordx4 %0, %2, off sc1\n\tglobal_load_dwordx4 %1, %2, off offset:16 sc1" : "=&v"(a), "=&v"(b) : "v"(hp_) : "memory");
  else asm volatile("" : "=v"(a), "=v"(b));
  static_for<RAW_S0, 8>(step);
  MF(2);
  if (wl == 1) {
    asm volatile("" : "+v"(a), "+v"(b));
    const unsigned t0 = (unsigned)tag, t1 = (unsigned)(tag >> 32);
#pragma unroll 1
    for (unsigned spin = 0; spin < SPIN_MEM; ++spin) {
      const unsigned bad = ((a[0] ^ t0) | (a[2] ^ t0) | (b[0] ^ t0) | (b[2] ^ t0)) | ((a[1] ^ t1) | (a[3] ^ t1) | (b[1] ^ t1) | (b[3] ^ t1));
      if (!__any(bad != 0u)) break;
      __builtin_amdgcn_s_sleep(4);
      asm volatile("global_load_dwordx4 %0, %2, off sc1\n\tglobal_load_dwordx4 %1, %2, off offset:16 sc1\n\ts_waitcnt vmcnt(0)"
                   : "=&v"(a), "=&v"(b)
                   : "v"(hp_)
                   : "memory");
    }
    lds_wr32(lds0 + (unsigned)(LW_OFF + LW_HELLO * 4), 1u);
    if (bid == 0 && lane == 0) asm volatile("global_store_dwordx2 %0, %1, off sc1\n\ts_nop 1" ::"v"(ws + OFF_TAG), "v"(tag) : "memory");
  }
  MF(11);
  if (rt < 2) lds_spin(lds0 + (unsigned)(LW_OFF + LW_Q * 4), 1u);
  lds_spin(lds0 + (unsigned)K1R_CNT, 8u);
  if constexpr (JOB) {
#pragma unroll
    for (int b = 0; b < 2; ++b)
      asm volatile("s_waitcnt vmcnt(0)"
                   : "+v"(v[b][0]), "+v"(v[b][1]), "+v"(v[b][2]), "+v"(v[b][3]), "+v"(v[b][4]), "+v"(v[b][5]), "+v"(v[b][6]), "+v"(v[b][7])
                   :
                   : "memory");
  }
  MF(12);
  const char* tp = ws + OFF_POT + ct * 128 + (lane < 6 ? lane : 5) * 8;
  u32x2 f;
  asm volatile("global_load_dwordx2 %0, %1, off sc1" : "=v"(f) : "v"(tp) : "memory");
  if constexpr (JOB) {
    static_assert(SB == 4 * 9216, "staging");
#pragma unroll
    for (int b = 0; b < 2; ++b) {
      float cs[8] = {0.f, 0.f, 0.f, 0.f, 0.f, 0.f, 0.f, 0.f};
      tr8_store<288, true>(v[b], da + (unsigned)(b * 18432), cs, std::make_integer_sequence<int, 8>{});
      const f32x4 c0 = {cs[0], cs[1], cs[2], cs[3]}, c1 = {cs[4], cs[5], cs[6], cs[7]};
      const unsigned ca = lds0 + (unsigned)(3 * SB + wl * 2304) + sa * 2u;
      asm volatile("ds_write_b128 %0, %1 offset:%2" ::"v"(ca), "v"(c0), "n"(b * 1152) : "memory");
      asm volatile("ds_write_b128 %0, %1 offset:%2" ::"v"(ca), "v"(c1), "n"(b * 1152 + 16) : "memory");
    }
    lds_inc(lds0 + (unsigned)(LW_OFF + LW_ST * 4));
    lds_spin(lds0 + (unsigned)(LW_OFF + LW_ST * 4), 4u);
    if (isi) {
      const int j = (job & 7) * 4 + wl, ti = j < 16 ? 2 * j + 1 : 2 * (j - 16);
      lds_out<9216>(lds0 + (unsigned)(wl * 9216), ws + OFF_PI + (size_t)(ti * 18 + kb) * 9216, lane);
      f32x4 c[4];
      const unsigned ca = lds0 + (unsigned)(3 * SB + wl * 576) + sa;
#pragma unroll
      for (int i = 0; i < 4; ++i) asm volatile("ds_read_b128 %0, %1 offset:%2" : "=v"(c[i]) : "v"(ca), "n"(i * 2304) : "memory");
      asm volatile("s_waitcnt lgkmcnt(0)" : "+v"(c[0]), "+v"(c[1]), "+v"(c[2]), "+v"(c[3]));
      const f32x4 t = (c[0] + c[1]) + (c[2] + c[3]);
      if (lane < 36) pk_st16(ws + OFF_CSP + ((size_t)kb * F_ + ti * 144) * 4 + sa, __builtin_bit_cast(u32x4, t));
    } else {
      lds_out<4608>(lds0 + (unsigned)(wl * 9216), ws + OFF_PW + (size_t)(wl * 144 + kb) * 4608, lane);
      lds_out<4608>(lds0 + (unsigned)(wl * 9216 + 4608), ws + OFF_PW + (size_t)(wl * 144 + 72 + kb) * 4608, lane);
    }
  }
  MF(8);
  announce(lds0 + (unsigned)(LW_OFF + LW_PK * 4), 4u, ws, CN_PK, isi ? 8 + (job & 3) * 4 + lane : 24 + lane, JOB && lane < (isi ? 4 : 8));
  MF(9);
  if (wl == 0) {
    asm volatile("" : "+v"(f));
#pragma unroll 1
    for (unsigned spin = 0; spin < SPIN_MEM; ++spin) {
      if (!__any(f[0] != (unsigned)tag || f[1] != (unsigned)(tag >> 32))) break;
      __builtin_amdgcn_s_sleep(2);
      asm volatile("global_load_dwordx2 %0, %1, off sc1\n\ts_waitcnt vmcnt(0)" : "=&v"(f) : "v"(tp) : "memory");
    }
  }
}

template <int MT, int NT>
DEVI void k1_gemm(char* smem, const char* ws, int rt, int hp, int mt0, int nt0,
                  bool late, int lane MK_ARG) {
  const unsigned lds0 = (unsigned)(size_t)smem;
  const int g = lane >> 4;
  f32x4 cs[NT];
  unsigned ta[NT];
  unsigned cad[NT];
#pragma unroll
  for (int nt = 0; nt < NT; ++nt) {
    int c = (nt0 + nt) * 16 + g * 4;
    if (c > 104) c = 104;
    const int part = c / 36, ci = c - part * 36;
    cad[nt] = lds0 + (unsigned)(K1_CS_OFF + (part * 40 + (hp & 1) * 4 + ci) * 4);
    asm volatile("" : "+v"(cad[nt]));
    const int to = (nt0 + nt) * 16 + g * 4 > 104 ? 120 : part * 80 + ci * 2 + (ci >= 20 ? 8 : 0);
    ta[nt] = lds0 + (unsigned)(K1_T_OFF + (mt0 * 16 + (lane & 15)) * K1_TP + to);
    asm volatile("" : "+v"(ta[nt]));
  }
  unsigned sa = lds0 + (unsigned)(K1_ST_OFF + (mt0 * 16 + (lane & 15)) * 8);
  asm volatile("" : "+v"(sa));
  f32x4 acc[MT][NT];
#pragma unroll
  for (int mt = 0; mt < MT; ++mt)
#pragma unroll
    for (int nt = 0; nt < NT; ++nt) acc[mt][nt] = f32x4{0.f, 0.f, 0.f, 0.f};
  f32x2 st[MT];
  if (late) bar();
  if constexpr (NT == 7) cnt_wait(ws, CN_X2, rt * 16 + hp, rt ? 24u : 16u);
  bar();
  MK(1);
  {
    GemmMath<K1C, MT, NT, (NT == 7 ? 5 : 3), (NT == 3 ? 6 : NT), true, 2, false, K1_WS> gm(acc);
    gm.setup(lds0, mt0, nt0, lane, 0u);
    bar();
#pragma unroll
    for (int nt = 0; nt < NT; ++nt) asm volatile("ds_read_b128 %0, %1" : "=v"(cs[nt]) : "v"(cad[nt]));
    gm.first();
    gm.template mid<8>();
#pragma unroll
    for (int mt = 0; mt < MT; ++mt) asm volatile("ds_read_b64 %0, %1 offset:%2" : "=v"(st[mt]) : "v"(sa), "n"(mt * 128));
    asm volatile("s_waitcnt vmcnt(0)" ::: "memory");
    gm.last();
  }
#pragma unroll
  for (int nt = 0; nt < NT; ++nt) asm volatile("" : "+v"(cs[nt]));
#pragma unroll
  for (int mt = 0; mt < MT; ++mt) asm volatile("" : "+v"(st[mt]));
  MK(2);
#pragma unroll
  for (int mt = 0; mt < MT; ++mt) {
    f32x2 M = {st[mt][0], st[mt][0]}, R = {st[mt][1], st[mt][1]};
    asm volatile("" : "+v"(M), "+v"(R));
#pragma unroll
    for (int nt = 0; nt < NT; ++nt) {
      const f32x2 v0 = R * __builtin_elementwise_fma(M, f32x2{cs[nt][0], cs[nt][1]}, f32x2{acc[mt][nt][0], acc[mt][nt][1]});
      const f32x2 v1 = R * __builtin_elementwise_fma(M, f32x2{cs[nt][2], cs[nt][3]}, f32x2{acc[mt][nt][2], acc[mt][nt][3]});
      acc[mt][nt][0] = __uint_as_float(pack2v(v0));
      acc[mt][nt][1] = __uint_as_float(pack2v(v1));
      const f32x2 pk = {acc[mt][nt][0], acc[mt][nt][1]};
      asm volatile("ds_write_b64 %0, %1 offset:%2" ::"v"(ta[nt]), "v"(pk), "n"(mt * 16 * K1_TP) : "memory");
    }
  }
}

DEVI float vmax(float a, float b) {
  float r;
  asm("v_max_f32 %0, %1, %2" : "=v"(r) : "v"(a), "v"(b));
  return r;
}
struct Attn {
  unsigned ka, kb, va, vb, m0, m1, m2, pm[2], oa, pa;
  f32x4 mk;
  unsigned long long sel[4];
  const char* ob;
  DEVI void setup(unsigned T, char* o, int r0, int hp, int wave, int lane) {
    const int g = lane >> 4, r = lane & 15, blk = wave >> 1, hd = wave & 1;
    const unsigned co = (unsigned)(g < 2 ? hd * 48 + g * 16 : 32);
    ka = T + (unsigned)((blk * 32 + 2 * r) * K1_TP) + co;
    kb = T + (unsigned)((blk * 32 + 2 * (r & 7)) * K1_TP) + co;
    const int vr = 4 * g + ((lane >> 2) & 3);
    va = T + (unsigned)((blk * 32 + 2 * vr) * K1_TP + (lane & 3) * 8);
    vb = T + (unsigned)((blk * 32 + 2 * (vr & 7)) * K1_TP + (lane & 3) * 8);
    m0 = (g < 2 || (g == 2 && hd == 0)) ? ~0u : 0u;
    m1 = (g < 2 || (g == 2 && hd == 1)) ? ~0u : 0u;
    m2 = g < 2 ? ~0u : 0u;
    pm[0] = pm[1] = 0u;
    const float ci = __uint_as_float(vconst(0xff800000u));
    const unsigned ch = vconst(0xffffu);
#pragma unroll
    for (int e = 0; e < 4; ++e) {
      const int w1 = r + 8 - 4 * g - e;
      mk[e] = (w1 & 15) <= 7 ? 0.f : ci;
      sel[e] = __ballot(w1 >= 8);
      if (w1 >= 8) pm[e >> 1] |= ch << (16 * (e & 1));
    }
    oa = (unsigned)((8 + hp * 4 + hd * 2 + (g >> 1)) * 1024 + ((blk & 1) * 32 + 2 * r + (g & 1)) * 16);
    pa = ((oa & 1023u) >> 2) + (unsigned)((hp * 2 + hd) * 256);
    ob = uni_ptr(o + (size_t)((r0 >> 6) + (wave >> 2)) * (72 * 1024));
    asm volatile("" : "+v"(ka), "+v"(kb), "+v"(va), "+v"(vb), "+v"(m0), "+v"(m1), "+v"(m2), "+v"(pm[0]), "+v"(pm[1]), "+v"(oa));
    asm volatile("" : "+v"(mk), "+v"(pa), "+s"(ob), "+s"(sel[0]), "+s"(sel[1]), "+s"(sel[2]), "+s"(sel[3]));
  }
  DEVI void run(int r0, int hp, int wave, int lane) {
    const int hd = wave & 1;
    u32x4 q[2], k0[2], k1[2];
    u32x2 v[2][2][2];
#pragma unroll
    for (int p = 0; p < 2; ++p) {
      asm volatile("ds_read_b128 %0, %1 offset:%2" : "=v"(q[p]) : "v"(ka), "n"((16 + p) * K1_TP) : "memory");
      asm volatile("ds_read_b128 %0, %1 offset:%2" : "=v"(k0[p]) : "v"(ka), "n"(p * K1_TP + 80) : "memory");
      asm volatile("ds_read_b128 %0, %1 offset:%2" : "=v"(k1[p]) : "v"(kb), "n"((32 + p) * K1_TP + 80) : "memory");
    }
    const unsigned vh = (unsigned)hd * 48u;
#pragma unroll
    for (int p = 0; p < 2; ++p) {
      asm volatile("ds_read_b64_tr_b16 %0, %1 offset:%2" : "=v"(v[p][0][0]) : "v"(va + vh), "n"(p * K1_TP + 160) : "memory");
      asm volatile("ds_read_b64_tr_b16 %0, %1 offset:%2" : "=v"(v[p][0][1]) : "v"(vb + vh), "n"((32 + p) * K1_TP + 160) : "memory");
      asm volatile("ds_read_b64_tr_b16 %0, %1 offset:%2" : "=v"(v[p][1][0]) : "v"(va), "n"(p * K1_TP + 192) : "memory");
      asm volatile("ds_read_b64_tr_b16 %0, %1 offset:%2" : "=v"(v[p][1][1]) : "v"(vb), "n"((32 + p) * K1_TP + 192) : "memory");
    }
    const u32x4 one4 = {vconst(0x3f803f80u), vconst(0x3f803f80u), vconst(0x3f803f80u), vconst(0x3f803f80u)};
    const s16x8 ones = __builtin_bit_cast(s16x8, one4);
    const float c = 0.23570226039551584f * 1.4426950408889634f;
    const f32x4 z = {0.f, 0.f, 0.f, 0.f};
    f32x4 sc[2][2];
#pragma unroll
    for (int p = 0; p < 2; ++p) {
      asm volatile("s_waitcnt lgkmcnt(%3)" : "+v"(q[p]), "+v"(k0[p]), "+v"(k1[p]) : "n"(11 - 3 * p));
      const s16x8 qf = __builtin_bit_cast(s16x8, q[p] & u32x4{m0, m1, m2, m2});
      sc[p][0] = __builtin_amdgcn_mfma_f32_16x16x32_bf16(__builtin_bit_cast(s16x8, k0[p]), qf, mk, 0, 0, 0);
      sc[p][1] = __builtin_amdgcn_mfma_f32_16x16x32_bf16(__builtin_bit_cast(s16x8, k1[p]), qf, mk, 0, 0, 0);
      __builtin_amdgcn_sched_barrier(0);
    }
    if (__builtin_expect(r0 == 0 && wave < 2, 0)) {
#pragma unroll
      for (int p = 0; p < 2; ++p)
#pragma unroll
        for (int e = 0; e < 4; ++e)
          if (lane < 32) sc[p][0][e] = mk[e] - 4.242640687e9f;
    }
    float lg[2][4], mx[2];
#pragma unroll
    for (int p = 0; p < 2; ++p) {
#pragma unroll
      for (int e = 0; e < 4; ++e) lg[p][e] = __builtin_amdgcn_inverse_ballot_w64(sel[e]) ? sc[p][1][e] : sc[p][0][e];
      asm("v_max3_f32 %0, %1, %2, %3\n\tv_max_f32 %0, %0, %4" : "=&v"(mx[p]) : "v"(lg[p][0]), "v"(lg[p][1]), "v"(lg[p][2]), "v"(lg[p][3]));
    }
    {
      const auto a = __builtin_amdgcn_permlane32_swap(__float_as_uint(mx[0]), __float_as_uint(mx[1]), false, false);
      const unsigned b = __float_as_uint(vmax(__uint_as_float(a[0]), __uint_as_float(a[1])));
      const auto d = __builtin_amdgcn_permlane16_swap(b, b, false, false);
      const unsigned f = __float_as_uint(vmax(__uint_as_float(d[0]), __uint_as_float(d[1])) * -c);
      const auto h = __builtin_amdgcn_permlane32_swap(f, f, false, false);
      mx[0] = __uint_as_float(h[0]);
      mx[1] = __uint_as_float(h[1]);
    }
#pragma unroll
    for (int p = 0; p < 2; ++p)
#pragma unroll
      for (int e = 0; e < 4; ++e) lg[p][e] = __builtin_amdgcn_exp2f(__builtin_fmaf(lg[p][e], c, mx[p]));
    asm volatile("s_waitcnt lgkmcnt(0)"
                 : "+v"(v[0][0][0]), "+v"(v[0][0][1]), "+v"(v[0][1][0]), "+v"(v[0][1][1]), "+v"(v[1][0][0]), "+v"(v[1][0][1]),
                   "+v"(v[1][1][0]), "+v"(v[1][1][1]));
    __builtin_amdgcn_sched_barrier(0);
    f32x4 su[2], o0[2], o1[2];
    s16x8 pp[2];
#pragma unroll
    for (int p = 0; p < 2; ++p) {
      const unsigned p0 = pack2(lg[p][0], lg[p][1]), p1 = pack2(lg[p][2], lg[p][3]);
      pp[p] = __builtin_bit_cast(s16x8, u32x4{p0 & ~pm[0], p1 & ~pm[1], p0 & pm[0], p1 & pm[1]});
    }
    __builtin_amdgcn_sched_barrier(0);
#pragma unroll
    for (int p = 0; p < 2; ++p) {
      const s16x8 pf = pp[p];
      const u32x4 v0 = {v[p][0][0][0], v[p][0][0][1], v[p][0][1][0], v[p][0][1][1]};
      const u32x4 v1 = {v[p][1][0][0], v[p][1][0][1], v[p][1][1][0], v[p][1][1][1]};
      su[p] = __builtin_amdgcn_mfma_f32_16x16x32_bf16(ones, pf, z, 0, 0, 0);
      o0[p] = __builtin_amdgcn_mfma_f32_16x16x32_bf16(__builtin_bit_cast(s16x8, v0), pf, z, 0, 0, 0);
      o1[p] = __builtin_amdgcn_mfma_f32_16x16x32_bf16(__builtin_bit_cast(s16x8, v1), pf, z, 0, 0, 0);
    }
    __builtin_amdgcn_sched_barrier(0);
    u32x2 y[2], yl;
    unsigned hm = (unsigned)hd - 1u;
    asm volatile("" : "+s"(hm));
#pragma unroll
    for (int p = 0; p < 2; ++p) {
      const float inv = __builtin_amdgcn_rcpf(su[p][0]);
      y[p] = u32x2{pack2(o0[p][0] * inv, o0[p][1] * inv), pack2(o0[p][2] * inv, o0[p][3] * inv)};
      const unsigned la = pack2(o1[p][0] * inv, o1[p][1] * inv), lb = pack2(o1[p][2] * inv, o1[p][3] * inv);
      yl[p] = (la & hm) | (lb & ~hm);
    }
    const auto s0 = __builtin_amdgcn_permlane16_swap(y[0][0], y[1][0], false, false);
    const auto s1 = __builtin_amdgcn_permlane16_swap(y[0][1], y[1][1], false, false);
    const u32x4 w = {s0[0], s1[0], s0[1], s1[1]};
    asm volatile("global_store_dwordx4 %0, %1, %4 sc1\n\ts_mov_b64 exec, 0xffff\n\tglobal_store_dwordx2 %2, %3, %4 sc1\n\ts_mov_b64 exec, -1\n\ts_nop 0" ::"v"(oa),
                 "v"(w), "v"(pa), "v"(yl), "s"(ob)
                 : "memory");
  }
};

#define NL_MAIN 660
DEVI void math_start(unsigned lds0, char* ws, int bid, int wave, int lane) {
  static_assert(2 * SZ_ZP == (size_t)256 * 2048 && NCNT <= 16 && OFF_CNT + NCNT * 32768 < 0xffffffffu, "mark split");
  const unsigned m1 = vconst(~0u), l = (unsigned)lane;
  const unsigned hi = wave < 2 ? 128u : 32768u;
  unsigned b0 = (unsigned)OFF_ZP + (unsigned)(bid * 2 + wave) * 1024u, b1 = (unsigned)OFF_CNT + (unsigned)((wave & 1) * 2048 + bid) * 128u;
  asm volatile("" : "+s"(b0), "+s"(b1));
  const unsigned base = wave < 2 ? b0 : b1;
  const unsigned long long act = wave < 3 ? ~0ull : (wave == 3 ? (1ull << ((NCNT - 8) * 8)) - 1ull : 0ull);
  asm volatile("s_mov_b64 exec, %3\n\tglobal_store_dwordx4 %0, %1, %2 sc1\n\ts_mov_b64 exec, -1\n\ts_nop 0" ::"v"(base + (l >> 3) * hi + (l & 7u) * 16u),
               "v"(u32x4{m1, m1, m1, m1}), "s"(ws), "s"(act)
               : "memory");
  unsigned a4 = (l < 32u ? (l >> 3) * (unsigned)K1R::SB : (unsigned)LW_OFF) + (l & 7u) * 16u, a0 = (unsigned)(wave * K1R::SB + K1R::XB + 240) + l * 288u;
  asm volatile("" : "+v"(a4), "+v"(a0));
  const unsigned a = wave < 4 ? a0 : a4;
  const unsigned v = (wave < 4 || l < 32u) ? 0x3f803f80u : 0u;
  asm volatile("s_mov_b64 exec, %2\n\tds_write_b128 %0, %1\n\ts_mov_b64 exec, -1\n\ts_waitcnt lgkmcnt(0)" ::"v"(lds0 + a), "v"(u32x4{v, v, v, v}),
               "s"(wave < 4 ? ~0ull : (wave == 4 ? 0xffffffffffull : 0ull))
               : "memory");
}

template <int NST, int NS, int NSW, int WOFF, int XW, int X0, int S0, unsigned BAR>
struct ResX {
  static constexpr int WS = 4 * 4608, R = 10, V1 = 5 * NS, NV = V1 + 10 * (NST - NS), NG = (NSW + 2) / 3;
  s16x8 xf[XW][2];
  s16x8 wf[R];
  unsigned wrd[2][NG], xo, ea, fa, fv, tag;
  const char* xp[3];
  f32x4 (&acc)[2][5];
  DEVI ResX(f32x4 (&a)[2][5]) : acc(a) {}
  static constexpr int st(int V) { return V < V1 ? V / 5 : NS + (V - V1) / 10; }
  static constexpr int hf(int V) { return V < V1 ? 0 : ((V - V1) / 5) & 1; }
  static constexpr bool first(int V) { return V < V1 ? V % 5 == 0 : (V - V1) % 10 == 0; }
  static constexpr int wreq(int V) {
    int n = V + 5;
    if (V >= V1 - 4) n += V < V1 ? V - (V1 - 5) : 4;
    return n < NV ? n : NV;
  }
  static constexpr int XU = V1 + 1, XF = V1 + 11, XR = V1 + 15, XC = V1 + 21;
  static constexpr int oth(int V) { return NS && ((V >= XU && V <= XF && (V - XU) % 2 == 0) || V == XR || (V > XC && V <= XC + 5)) ? 1 : 0; }
  static constexpr int newer(int V) {
    const int F = V + 1 < NV ? V + 1 : V;
    int v = V - 1, n = 0;
    for (; v >= 0 && wreq(v - 1) > F; --v) n += wreq(v) - wreq(v - 1) + oth(v);
    return n + wreq(v) - 1 - F + (v >= 0 ? oth(v) : 0);
  }
  static constexpr int xreq(int T) {
    int n = X0 + 2 * (T + 1);
    if (n > T + XW) n = T + XW;
    if (NS && T <= 11 && n > 18) n = 18;
    if (T < 0) n = X0;
    return n < NST ? n : NST;
  }
  static_assert(NS == 0 || (xreq(NS - 2) == NST && XC + 5 < NV), "the last steps only compute and swap");
  template <int T, int M>
  DEVI void xld() {
    asm volatile("global_load_dwordx4 %0, %1, %2 sc1" : "=v"(xf[T % XW][M]) : "v"(xo), "s"(xp[M == 0 ? 0 : (T < NS ? 1 : 2)]));
    if constexpr (M == 1 && T + 1 < NST) asm volatile("v_add_u32 %0, 0x2000, %0" : "+v"(xo));
  }
  template <int V>
  DEVI void rd() {
    constexpr int T = st(V), sl = (T / 2 + S0) % NSW;
    asm volatile("ds_read_b128 %0, %1 offset:%2" : "=v"(wf[V % R]) : "v"(wrd[hf(V)][sl / 3]), "n"((sl % 3) * WS + (T & 1) * 9216 + (V % 5) * 32));
  }
  template <int V, int M, int H>
  DEVI void mf() {
    acc[M][V % 5] = __builtin_amdgcn_mfma_f32_16x16x32_bf16(wf[V % R], xf[st(V) % XW][H], acc[M][V % 5], 0, 0, 0);
    __builtin_amdgcn_sched_barrier(0);
  }
  template <int V>
  DEVI void unit() {
    constexpr int T = st(V), n = V % 5, nx = xreq(T) - xreq(T - 1), nw = wreq(V) - wreq(V - 1);
    constexpr int vm = 2 * (xreq(T - 1) - 1 - T);
    static_assert(newer(V) <= 15, "lgkmcnt range");
    if constexpr (first(V) && V % 2 == 0) asm volatile("s_waitcnt vmcnt(%0) lgkmcnt(%1)" ::"n"(vm), "n"(newer(V)));
    else if constexpr (first(V)) asm volatile("s_waitcnt vmcnt(%0)" ::"n"(vm));
    else if constexpr (V % 2 == 0) asm volatile("s_waitcnt lgkmcnt(%0)" ::"n"(newer(V)));
    __builtin_amdgcn_sched_barrier(0);
    if constexpr (V < V1) mf<V, V & 1, V & 1>();
    else mf<V, 0, hf(V)>();
    if constexpr (nw > 0) rd<wreq(V - 1)>();
    __builtin_amdgcn_sched_barrier(0);
    if constexpr (V < V1) {
      mf<V, 1 - (V & 1), 1 - (V & 1)>();
      if constexpr (nw > 1) rd<wreq(V - 1) + 1>();
      if constexpr (nx == 2 && n < 4) xld<xreq(T - 1) + n / 2, n % 2>();
      if constexpr (nx == 1 && n == (T & 1)) xld<xreq(T - 1), 0>();
      if constexpr (nx == 1 && n == (T & 1) + 2) xld<xreq(T - 1), 1>();
      if constexpr (n == 4 && (T & 1) && ((BAR >> (T / 2)) & 1u)) bar();
    } else if constexpr (NS == 0) {
      constexpr int k = (V % 10) / 2;
      if constexpr (V % 2 == 0 && k < 2 * nx) xld<xreq(T - 1) + k / 2, k % 2>();
      if constexpr (V % 10 == 8 && nx > 0 && xreq(T) == NST)
        asm volatile("s_mov_b64 exec, 1\n\tds_add_u32 %0, %1\n\ts_mov_b64 exec, -1" ::"v"(fa), "v"(fv));
    } else {
      if constexpr (oth(V) && V < XF) asm volatile("ds_write_b128 %0, %1 offset:%2" ::"v"(ea), "v"(acc[1][(V - XU) / 2]), "n"((V - XU) * 512));
      if constexpr (V == XF) asm volatile("ds_write_b32 %0, %1" ::"v"(fa), "v"(fv));
      if constexpr (V == XF + 2) asm volatile("v_xor_b32 %0, 4, %0" : "+v"(fa));
      if constexpr (V == XR) asm volatile("ds_read_b32 %0, %1" : "+v"(fv) : "v"(fa));
      if constexpr (V == XR + 2) asm volatile("v_xor_b32 %0, 0x2000, %0" : "+v"(ea));
      if constexpr (V == XC) {
        unsigned c = 1u << 18;
        asm volatile(
            "s_waitcnt lgkmcnt(%4)\n\tv_cmp_ne_u32 vcc, %3, %0\n\ts_cbranch_vccnz .Lw%=\n\t.subsection 1\n.Lw%=:\n\tds_read_b32 %0, %2\n\t"
            "s_waitcnt lgkmcnt(0)\n\tv_cmp_ne_u32 vcc, %3, %0\n\t" SAFE_OUT(".Lr%=") "s_cbranch_vccnz .Lw%=\n\ts_branch .Lr%=\n\t.subsection 0\n.Lr%=:"
            : "+v"(fv), NOP_(c)
            : "v"(fa), "s"(tag), "n"(wreq(XC) - wreq(XR))
            : "vcc", "scc");
      }
      if constexpr (oth(V) && V > XC) asm volatile("ds_read_b128 %0, %1 offset:%2" : "=v"(acc[1][V - XC - 1]) : "v"(ea), "n"((V - XC - 1) * 1024));
    }
    __builtin_amdgcn_sched_barrier(0);
  }
  template <int... V>
  DEVI void units(std::integer_sequence<int, V...>) {
    (unit<V>(), ...);
  }
  DEVI void run() {
    units(std::make_integer_sequence<int, NV>{});
    if constexpr (NS > 0) {
      asm volatile("s_waitcnt lgkmcnt(0)" : "+v"(acc[1][0]), "+v"(acc[1][1]), "+v"(acc[1][2]), "+v"(acc[1][3]), "+v"(acc[1][4]));
#pragma unroll
      for (int i = 0; i < 5; ++i) acc[0][i] += acc[1][i];
    }
  }
  template <int... V>
  DEVI void pro(std::integer_sequence<int, V...>) {
    (rd<V>(), ...);
  }
  DEVI void prow() { pro(std::make_integer_sequence<int, wreq(-1)>{}); }
  template <int T0, int... T>
  DEVI void prox(std::integer_sequence<int, T...>) {
    ((xld<T0 + T, 0>(), xld<T0 + T, 1>()), ...);
  }
  DEVI void setup(unsigned lds0, const char* xbase, int wave, int lane, unsigned red, unsigned nth) {
    const int g = lane >> 4, r = lane & 15, kq = wave & 1;
    xp[0] = xbase + wave * 256;
    xp[1] = xbase + (wave ^ 1) * 256;
    xp[2] = xp[0] + (kq ? -4096 : 4096);
    xo = (unsigned)((4 * kq + g) * 1024 + r * 16);
#pragma unroll
    for (int i = 0; i < 2 * NG; ++i) {
      wrd[i / NG][i % NG] = lds0 + (unsigned)(WOFF + (i % NG) * 3 * WS + (kq ^ (i / NG)) * 4608 + (2 * (r & 7) + 16 * (g & 1) + (g >> 1)) * 144 + (r >> 3) * 16);
      if (i < NG || NS == 0 || i % NG == ((NS / 2 + S0) % NSW) / 3) asm volatile("" : "+v"(wrd[i / NG][i % NG]));
    }
    asm volatile("" : "+v"(xo));
    if constexpr (NS > 0) {
      ea = lds0 + red + (unsigned)((wave ^ 1) * 8192 + lane * 16);
      fa = lds0 + (unsigned)(LW_OFF + (LW_PAIR + wave) * 4);
      fv = tag = nth;
      asm volatile("" : "+v"(ea), "+v"(fa), "+v"(fv));
    }
  }
  template <int N>
  DEVI void init(u32x2 (&xv)[5]) {
    asm volatile("s_waitcnt vmcnt(%5)" : "+v"(xv[0]), "+v"(xv[1]), "+v"(xv[2]), "+v"(xv[3]), "+v"(xv[4]) : "n"(N));
#pragma unroll
    for (int i = 0; i < 5; ++i) {
      acc[0][i] = f32x4{bf_lo(xv[i][0]), bf_hi(xv[i][0]), bf_lo(xv[i][1]), bf_hi(xv[i][1])};
      asm volatile("" : "+v"(acc[0][i]));
      acc[1][i] = f32x4{0.f, 0.f, 0.f, 0.f};
    }
  }
};

constexpr int P5_XW = 8, P5_X0 = 5, P2_X0 = 4;
constexpr unsigned P5_BAR = 0x2aaau;
constexpr int P5_W = 15 * 1024;
constexpr int P2_W = K1_ST_OFF - 18 * 4608;
constexpr int P5_RED = 0;
static_assert(P5_RED % 16384 == 0 && P5_RED + 3 * 8192 + 5120 <= P5_W + 18432, "exchange area");
static_assert(P2_W >= K1_T_OFF + 144 * K1_TP + 64 && P2_W % 16 == 0 && P5_W + 8 * 18432 <= LW_OFF, "LDS placement");

struct ResIn {
  const char* xp;
  unsigned o, o2, st;
  DEVI void setup(const char* src, bool rm, int rb, int ct, int wave, int lane) {
    const int g = lane >> 4, g2 = g & 1, rl = wave * 16 + (lane & 15);
    xp = uni_ptr(rm ? src + ((size_t)rb * 64 * D_ + ct * 72) * 2 : src + (size_t)(rb * 72 + ct * 9) * 1024);
    o = rm ? (unsigned)(rl * (D_ * 2) + g * 8) : (unsigned)((g >> 1) * 1024 + rl * 16 + g2 * 8);
    o2 = rm ? (unsigned)(rl * (D_ * 2) + g2 * 8 + 128) : (unsigned)(8192 + rl * 16 + g2 * 8);
    st = rm ? 32u : 2048u;
    asm volatile("" : "+v"(o), "+v"(o2));
    asm volatile("" : "+s"(xp));
  }
  DEVI void load(u32x2 (&xv)[5]) {
#pragma unroll
    for (int i = 0; i < 4; ++i) asm volatile("global_load_dwordx2 %0, %1, %2 sc1" : "=v"(xv[i]) : "v"(o), "s"(xp + i * st));
    asm volatile("global_load_dwordx2 %0, %1, %2 sc1" : "=v"(xv[4]) : "v"(o2), "s"(xp));
  }
};

DEVI void p2_gemm(f32x4 (&acc)[2][5], u32x2 (&xv)[5], unsigned lds0, const char* ob, int layer, int wave, int lane MK_ARG) {
  ResX<9, 0, 5, P2_W, 9, P2_X0, 0, 0u> gm(acc);
  gm.setup(lds0, ob, wave, lane, 0u, 0u);
  unsigned a = (gm.xo & ~1023u) | ((gm.xo & 1023u) >> 2);
  gm.xo += 8192u;
  gm.fa = lds0 + (unsigned)(LW_OFF + (LW_T + layer) * 4);
  gm.fv = vconst(1u);
  asm volatile("" : "+v"(a), "+v"(gm.xo), "+v"(gm.fa));
  const char* pp[2] = {gm.xp[0] - wave * 192, gm.xp[2] - wave * 192};
  place();
  bar();
  MK(6);
  unsigned e[2][4];
#pragma unroll
  for (int d = 0; d < 8; ++d)
    asm volatile("global_load_dword %0, %1, %2 offset:%3 sc1" : "=v"(e[d / 4][d % 4]) : "v"(a), "s"(pp[d / 4]), "n"((d % 4) * 256));
  gm.template prox<1>(std::make_integer_sequence<int, P2_X0 - 1>{});
  gm.prow();
  gm.template init<6 + 2 * P2_X0>(xv);
  asm volatile("s_waitcnt vmcnt(%8)"
               : "+v"(e[0][0]), "+v"(e[0][1]), "+v"(e[0][2]), "+v"(e[0][3]), "+v"(e[1][0]), "+v"(e[1][1]), "+v"(e[1][2]), "+v"(e[1][3])
               : "n"(2 * P2_X0 - 2));
  gm.xf[0][0] = __builtin_bit_cast(s16x8, u32x4{e[0][0], e[0][1], e[0][2], e[0][3]});
  gm.xf[0][1] = __builtin_bit_cast(s16x8, u32x4{e[1][0], e[1][1], e[1][2], e[1][3]});
  __builtin_amdgcn_sched_barrier(0);
  gm.run();
}

struct ResOut {
  const char *xp[3], *sp;
  unsigned xo, so;
  DEVI void setup(char* xim, char* xs, u16* out, bool fin, int rb, int ct, int wave, int lane) {
    const int g = lane >> 4, rl = wave * 16 + (lane & 15);
    xp[0] = uni_ptr(fin ? (const char*)(out + (size_t)rb * 64 * D_ + ct * 72) : xim + (size_t)(rb * 72 + ct * 9) * 1024);
    xp[1] = xp[0] + (fin ? 64 : 4096);
    xp[2] = xp[0] + (fin ? 128 : 8192);
    sp = uni_ptr(xs + (size_t)(rb * 8 + ct) * 512);
    xo = (unsigned)(((g & 1) * 2 + (g >> 1)) * (fin ? 16 : 1024) + rl * (fin ? D_ * 2 : 16));
    so = (unsigned)(rl * 8);
    asm volatile("" : "+v"(xo), "+v"(so), "+s"(xp[0]), "+s"(xp[1]), "+s"(xp[2]), "+s"(sp));
  }
  DEVI void run(const f32x4 (&a)[5], bool fin) {
    u32x2 y[5];
    u32x4 v[3];
#pragma unroll
    for (int nt = 0; nt < 5; ++nt) y[nt] = u32x2{pack2(a[nt][0], a[nt][1]), pack2(a[nt][2], a[nt][3])};
#pragma unroll
    for (int i = 0; i < 3; ++i) {
      const auto s0 = __builtin_amdgcn_permlane16_swap(y[2 * i][0], y[i < 2 ? 2 * i + 1 : 4][0], false, false);
      const auto s1 = __builtin_amdgcn_permlane16_swap(y[2 * i][1], y[i < 2 ? 2 * i + 1 : 4][1], false, false);
      v[i] = u32x4{s0[0], s1[0], s0[1], s1[1]};
    }
    if (fin) {
      asm volatile("global_store_dwordx4 %0, %1, %4\n\tglobal_store_dwordx4 %0, %2, %5\n\ts_mov_b64 exec, 0xffff\n\tglobal_store_dwordx4 %0, %3, %6\n\t"
                   "s_mov_b64 exec, -1\n\ts_nop 0" ::"v"(xo), "v"(v[0]), "v"(v[1]), "v"(v[2]), "s"(xp[0]), "s"(xp[1]), "s"(xp[2])
                   : "memory");
      return;
    }
    asm volatile("global_store_dwordx4 %0, %1, %4 sc1\n\tglobal_store_dwordx4 %0, %2, %5 sc1\n\ts_mov_b64 exec, 0xffff\n\t"
                 "global_store_dwordx4 %0, %3, %6 sc1\n\ts_mov_b64 exec, -1\n\ts_nop 0" ::"v"(xo), "v"(v[0]), "v"(v[1]), "v"(v[2]),
                 "s"(xp[0]), "s"(xp[1]), "s"(xp[2])
                 : "memory");
    const u32x4 one4 = {vconst(0x3f803f80u), vconst(0x3f803f80u), vconst(0x3f803f80u), vconst(0x3f803f80u)};
    const bool lo = __builtin_amdgcn_inverse_ballot_w64(0xffffull);
    v[2] = u32x4{lo ? v[2][0] : 0u, lo ? v[2][1] : 0u, lo ? v[2][2] : 0u, lo ? v[2][3] : 0u};
    f32x4 s = {0.f, 0.f, 0.f, 0.f}, q = s;
#pragma unroll
    for (int i = 0; i < 3; ++i) {
      const s16x8 u = __builtin_bit_cast(s16x8, v[i]);
      q = __builtin_amdgcn_mfma_f32_16x16x32_bf16(u, u, q, 0, 0, 0);
      s = __builtin_amdgcn_mfma_f32_16x16x32_bf16(__builtin_bit_cast(s16x8, one4), u, s, 0, 0, 0);
    }
    float q0 = q[0], q1 = q[1], q2 = q[2], q3 = q[3];
    const bool b0 = __builtin_amdgcn_inverse_ballot_w64(0xaaaaaaaaaaaaaaaaull), b1 = __builtin_amdgcn_inverse_ballot_w64(0xccccccccccccccccull);
    q0 = b0 ? q1 : q0;
    q2 = b0 ? q3 : q2;
    asm("" : "+v"(q0), "+v"(q2));
    const f32x2 sq = {s[0], b1 ? q2 : q0};
    asm volatile("s_mov_b64 exec, %3\n\tglobal_store_dwordx2 %0, %1, %2 sc1\n\ts_mov_b64 exec, -1" ::"v"(so), "v"(sq), "s"(sp), "s"(0xf0000f0000f0000full) : "memory");
  }
};

using K3C = GemmCfg<128, 144>;
constexpr int K3_Z1_OFF = K3C::LDS_BYTES;
constexpr int K3_C2_OFF = K3_Z1_OFF + K3C::XB;
constexpr int K3_ZC_OFF = K3_C2_OFF + 2048;
constexpr int K3_DM_OFF = K3_ZC_OFF + 1024;
constexpr int K3_ST_OFF = K3_Z1_OFF + 4 * 5120;
constexpr int K3_C1_OFF = K3_ST_OFF + 2048;
constexpr int K3_BS_OFF = K3_C1_OFF + 576;
static_assert(K3_BS_OFF + 512 <= LW_OFF, "K3 LDS");
static_assert(P5_W + 19 * 1024 == K3C::SB && K3C::SB == 34 * 1024, "w_out ring");

DEVI void red4(float (&t)[4], float& s, float& q) {
#pragma unroll
  for (int j = 0; j < 2; ++j) {
    const auto p = __builtin_amdgcn_permlane16_swap(__float_as_uint(t[j]), __float_as_uint(t[2 + j]), false, false);
    const unsigned v = __float_as_uint(__uint_as_float(p[0]) + __uint_as_float(p[1]));
    const auto w = __builtin_amdgcn_permlane32_swap(v, v, false, false);
    t[j] = __uint_as_float(w[0]) + __uint_as_float(w[1]);
    asm volatile("" : "+v"(t[j]));
  }
  s = t[0];
  q = t[1];
}
DEVI void red8(const f32x4& a, const f32x4& b, float& s, float& q) {
  float t[4];
#pragma unroll
  for (int j = 0; j < 4; ++j) {
    t[j] = a[j] + b[j];
    asm volatile("" : "+v"(t[j]));
  }
  red4(t, s, q);
}
DEVI void red16(const f32x4& a, const f32x4& b, const f32x4& c, const f32x4& d, float& s, float& q) {
  float t[4];
#pragma unroll
  for (int j = 0; j < 4; ++j) {
    float u = a[j] + b[j], v = c[j] + d[j];
    asm volatile("" : "+v"(u), "+v"(v));
    t[j] = u + v;
    asm volatile("" : "+v"(t[j]));
  }
  red4(t, s, q);
}

constexpr int b_k(int s) { return s == 0 ? 0 : (s < 3 ? 6 + s : (s == 3 ? 1 : (s < 6 ? 1 + s : (s == 6 ? 4 : s - 5)))); }
constexpr int b_xp(int s) { return s == 0 ? 0 : (b_k(s) >= 4 ? (b_k(s) % 5) / 2 : b_xp(s - 1)); }
template <bool B>
struct UMath {
  static constexpr int D = 4, R = 6, NV = 162, SB = K3C::SB, P = K3C::P;
  s16x8 wf[R], xf[2][2];
  s16x8 (&xc)[4][2][2];
  f32x4 (&acc)[2][9];
  f32x4 rc[4][2];
  f32x2 cs[9];
  unsigned ra, ca;
  template <int I>
  DEVI void cread() {
    if constexpr (I < 8) asm volatile("ds_read_b128 %0, %1 offset:%2" : "=v"(rc[I / 2][I % 2]) : "v"(ra), "n"((I / 2) * 512 + (I % 2) * 16));
    else if constexpr (I < 17) asm volatile("ds_read_b64 %0, %1 offset:%2" : "=v"(cs[I - 8]) : "v"(ca), "n"((I - 8) * 128));
  }
  unsigned xa, wa;
  unsigned fa, fw, fv;
  DEVI UMath(f32x4 (&a)[2][9], s16x8 (&x)[4][2][2]) : xc(x), acc(a) {}
  static constexpr int wslot(int s) { return B ? (s + 1) % 4 : s % 4; }
  static constexpr int xslot(int s) { return B ? b_k(s) % 5 : s % 5; }
  static constexpr int xpair(int s) { return B ? b_xp(s) : xslot(s) / 2; }
  static constexpr bool res(int q) { return !B || b_k(q / 2) >= 4; }
  static constexpr bool chk(int s) { return s >= 3 && s < 7; }
  static constexpr bool xop(int v) { return v + D < NV && (v + D) % 9 < 2 && res((v + D) / 9); }
  static constexpr int ops1(int v) { return (xop(v) || (B && ((v % 18 == 9 && chk(v / 18)) || (v % 18 == 0 && v > 0)))) ? 1 : 0; }
  static constexpr int newer(int V) {
    int v0 = (V % 9 == 8 ? V : V + 1) - D, n = 0;
    if (v0 >= 0 && !(V % 9 == 0 && res(V / 9))) n += ops1(v0);
    for (int v = v0 + 1; v < V; ++v)
      if (v >= 0) n += (v + D < NV ? 1 : (B ? 0 : 4)) + ops1(v);
    return n;
  }
  template <int V>
  DEVI void wread() {
    asm volatile("ds_read_b128 %0, %1 offset:%2" : "=v"(wf[V % R]) : "v"(wa), "n"((wslot(V / 18) & 1) * SB + ((V % 18) / 9) * 32 * P + (V % 9) * 32));
  }
  template <int Q, int M>
  DEVI void xread() {
    asm volatile("ds_read_b128 %0, %1 offset:%2" : "=v"(xf[Q & 1][M]) : "v"(xa), "n"((xslot(Q / 2) & 1) * SB + (Q & 1) * 8192 + M * (B ? 256 : 64)));
  }
  template <int V, int M_>
  DEVI void mf() {
    constexpr int M = ((V % 9) & 1) ? 1 - M_ : M_, q = V / 9;
    if constexpr (!B) acc[M][V % 9] = __builtin_amdgcn_mfma_f32_16x16x32_bf16(xf[q & 1][M], wf[V % R], acc[M][V % 9], 0, 0, 0);
    else if constexpr (res(q)) acc[M][V % 9] = __builtin_amdgcn_mfma_f32_16x16x32_bf16(wf[V % R], xf[q & 1][M], acc[M][V % 9], 0, 0, 0);
    else acc[M][V % 9] = __builtin_amdgcn_mfma_f32_16x16x32_bf16(wf[V % R], xc[b_k(q / 2)][q & 1][M], acc[M][V % 9], 0, 0, 0);
    __builtin_amdgcn_sched_barrier(0);
  }
  template <int V>
  DEVI void unit() {
    constexpr int U = V % 18, s = V / 18;
    static_assert(newer(V) <= 15, "lgkmcnt range");
    if constexpr ((V % 9) % 2 == 0 && V > 0) asm volatile("s_waitcnt lgkmcnt(%0)" ::"n"(newer(V)));
    __builtin_amdgcn_sched_barrier(0);
    mf<V, 0>();
    if constexpr (V + D < NV) wread<V + D>();
    if constexpr (!B && V + D >= NV) {
      cread<(V + D - NV) * 4>();
      cread<(V + D - NV) * 4 + 1>();
    }
    if constexpr (s < 8 && U == 13 && (wslot(s + 1) >> 1) != (wslot(s) >> 1))
      asm volatile("v_add_u32 %0, %1, %0" : "+v"(wa) : "n"(((wslot(s + 1) >> 1) - (wslot(s) >> 1)) * 2 * SB));
    __builtin_amdgcn_sched_barrier(0);
    mf<V, 1>();
    if constexpr (xop(V)) xread<(V + D) / 9, (V + D) % 9>();
    if constexpr (!B && V + D >= NV) {
      cread<(V + D - NV) * 4 + 2>();
      cread<(V + D - NV) * 4 + 3>();
      if constexpr (V == NV - 1) cread<16>();
    }
    if constexpr (s < 8) {
      if constexpr (U == 11 && xpair(s + 1) != xpair(s)) asm volatile("v_add_u32 %0, %1, %0" : "+v"(xa) : "n"((xpair(s + 1) - xpair(s)) * 2 * SB));
      if constexpr (!B && U == 13) __builtin_amdgcn_s_barrier();
      if constexpr (B && U == 9 && chk(s)) asm volatile("ds_read_u8 %0, %1 offset:%2" : "+v"(fv) : "v"(fa), "n"(s == 6 ? 0 : 8));
      unsigned n = 1u << 18;
      if constexpr (B && U == 13 && chk(s)) {
        asm volatile(
            "v_cmp_gt_u32 vcc, %3, %0\n\ts_cbranch_vccnz .Lw%=\n\t.subsection 1\n.Lw%=:\n\tds_read_u8 %0, %2 offset:%4\n\ts_waitcnt lgkmcnt(0)\n\t"
            "v_cmp_gt_u32 vcc, %3, %0\n\t" SAFE_OUT(".Lr%=") "s_cbranch_vccnz .Lw%=\n\ts_branch .Lr%=\n\t.subsection 0\n.Lr%=:"
            : "+v"(fv), NOP_(n)
            : "v"(fa), "n"(s == 6 ? 4 : s + 2), "n"(s == 6 ? 0 : 8)
            : "vcc", "scc");
      }
    }
    if constexpr (B && U == 15) asm volatile("v_mov_b32 %0, %1" : "+v"(fv) : "n"(s + 1));
    if constexpr (B && ((U == 0 && s > 0) || V == NV - 1)) asm volatile("ds_write_b8 %0, %1" ::"v"(fw), "v"(fv));
    __builtin_amdgcn_sched_barrier(0);
  }
  template <int... V>
  DEVI void units(std::integer_sequence<int, V...>) {
    (unit<V>(), ...);
  }
  DEVI void first() {
    if constexpr (!B) {
      xread<0, 0>();
      xread<0, 1>();
    }
    wread<0>();
    wread<1>();
    wread<2>();
    wread<3>();
    asm volatile("s_waitcnt lgkmcnt(0)");
  }
  DEVI void run() {
    units(std::make_integer_sequence<int, NV>{});
#pragma unroll
    for (int m = 0; m < 2; ++m)
#pragma unroll
      for (int n = 0; n < 9; ++n) asm volatile("" : "+v"(acc[m][n]));
  }
};

struct CMath {
  static constexpr int D = 4, R = 6, SB = K3C::SB;
  s16x8 wf[R], xf[2][2];
  f32x4 (&acc)[2][9];
  unsigned wa[2], xa[2][2];
  DEVI CMath(f32x4 (&a)[2][9]) : acc(a) {}
  static constexpr int xop(int u, bool last) { return (u + D) % 9 < 2 && !(last && u + D >= 18) ? 1 : 0; }
  static constexpr int newer(int U, bool last) {
    int v0 = (U % 9 == 8 ? U : U + 1) - D, n = 0;
    if (U % 9 != 0) n += xop((v0 + 18) % 18, last && v0 >= 0);
    for (int v = v0 + 1; v < U; ++v) n += (last && v + D >= 18 ? 0 : 1) + xop((v + 18) % 18, last && v >= 0);
    return n;
  }
  template <int SL, int U>
  DEVI void wread() {
    asm volatile("ds_read_b128 %0, %1 offset:%2" : "=v"(wf[U % R]) : "v"(wa[SL / 2]), "n"((SL & 1) * SB + U * 1024));
  }
  template <int SL, int H, int M>
  DEVI void xread() {
    asm volatile("ds_read_b128 %0, %1 offset:%2" : "=v"(xf[H][M]) : "v"(xa[SL / 2][H]), "n"((SL & 1) * SB + M * 2048));
  }
  template <int U, int M_>
  DEVI void mf() {
    constexpr int M = ((U % 9) & 1) ? 1 - M_ : M_;
    acc[M][U % 9] = __builtin_amdgcn_mfma_f32_16x16x32_bf16(wf[U % R], xf[U / 9][M], acc[M][U % 9], 0, 0, 0);
    __builtin_amdgcn_sched_barrier(0);
  }
  template <int SL, bool BAR, bool LAST, int U>
  DEVI void unit() {
    constexpr int PU = (U + D) % 18, PS = U + D < 18 ? SL : (SL + 1) % 4;
    if constexpr ((U % 9) % 2 == 0) asm volatile("s_waitcnt lgkmcnt(%0)" ::"n"(newer(U, LAST)));
    __builtin_amdgcn_sched_barrier(0);
    mf<U, 0>();
    if constexpr (!(LAST && U + D >= 18)) wread<PS, PU>();
    __builtin_amdgcn_sched_barrier(0);
    mf<U, 1>();
    if constexpr (xop(U, LAST)) xread<PS, PU / 9, PU % 9>();
    if constexpr (BAR && U == 13) __builtin_amdgcn_s_barrier();
    __builtin_amdgcn_sched_barrier(0);
  }
  template <int SL, bool BAR, bool LAST, int... U>
  DEVI void units(std::integer_sequence<int, U...>) {
    (unit<SL, BAR, LAST, U>(), ...);
  }
  template <int SL, bool BAR = true, bool LAST = false>
  DEVI void stage() {
    units<SL, BAR, LAST>(std::make_integer_sequence<int, 18>{});
  }
  DEVI void setup(unsigned lds0, int wm, int lane) {
    const int g = lane >> 4, r = lane & 15;
#pragma unroll
    for (int i = 0; i < 2; ++i) {
      wa[i] = lds0 + (unsigned)(i * 2 * SB + K3C::XB + lane * 16);
#pragma unroll
      for (int h = 0; h < 2; ++h) xa[i][h] = lds0 + (unsigned)(i * 2 * SB + (wm * 32 + r) * 128 + (((4 * h + g) ^ ((r >> 1) & 7)) * 16));
    }
    asm volatile("" : "+v"(wa[0]), "+v"(wa[1]), "+v"(xa[0][0]), "+v"(xa[0][1]), "+v"(xa[1][0]), "+v"(xa[1][1]));
  }
  DEVI void first() {
    xread<2, 0, 0>();
    xread<2, 0, 1>();
    wread<2, 0>();
    wread<2, 1>();
    wread<2, 2>();
    wread<2, 3>();
    asm volatile("s_waitcnt lgkmcnt(0)");
  }
};

constexpr int ZQ = 4;
constexpr int NR1 = 3;
DEVI void p3_g0(char* smem, char* __restrict__ ws, int layer, int mi, int ni, int wm, int lane MK_ARG) {
  constexpr int SB = K3C::SB;
  const unsigned lds0 = (unsigned)(size_t)smem;
  const int g = lane >> 4, r = lane & 15;
  const unsigned lw = lds0 + (unsigned)(LW_OFF + layer * 4), me = 1u << (8 * wm);
  f32x4 ac[2][9];
#pragma unroll
  for (int mt = 0; mt < 2; ++mt)
#pragma unroll
    for (int j = 0; j < 9; ++j) ac[mt][j] = f32x4{0.f, 0.f, 0.f, 0.f};
  unsigned ra = lds0 + (unsigned)(K3_ST_OFF + (wm * 32 + g * 8) * 4), ca = lds0 + (unsigned)(K3_C2_OFF + r * 8);
  asm volatile("" : "+v"(ra), "+v"(ca));
  s16x8 xc[4][2][2];
  UMath<false> gm(ac, xc);
  gm.ra = ra;
  gm.ca = ca;
  gm.xa = lds0 + (unsigned)(g * 2048 + (wm * 32 + 8 * (r >> 2) + (r & 3)) * 16);
  gm.wa = lds0 + (unsigned)(K3C::XB + (g * 8 + (r & 7)) * K3C::P + (r >> 3) * 16);
  unsigned adw = lw + LW_AD * 4, mev = vconst(me), po = (unsigned)(ni * 1024 + lane * 4);
  const char* zp = uni_ptr(ws + OFF_ZP + (size_t)layer * SZ_ZP + (size_t)mi * 16384 + wm * 256);
  asm volatile("" : "+v"(gm.xa), "+v"(gm.wa), "+v"(adw), "+v"(po));
  asm volatile("" : "+s"(zp));
  place();
  bar();
  gm.first();
  gm.run();
  asm volatile("s_mov_b64 exec, 1\n\tds_add_u32 %0, %1\n\ts_mov_b64 exec, -1" ::"v"(adw), "v"(mev) : "memory");
  MK(11);
  f32x2 z[9][4];
  f32x2 sm[4], sq[4];
  {
    f32x4 (&rc)[4][2] = gm.rc;
    f32x2 (&cs)[9] = gm.cs;
    asm volatile("s_waitcnt lgkmcnt(1)"
                 : "+v"(rc[0][0]), "+v"(rc[0][1]), "+v"(rc[1][0]), "+v"(rc[1][1]), "+v"(rc[2][0]), "+v"(rc[2][1]), "+v"(rc[3][0]), "+v"(rc[3][1]));
#pragma unroll
    for (int j = 0; j < 9; ++j) asm volatile("" : "+v"(cs[j]));
    auto RC = [&](int k, int i) { return f32x2{rc[k][i / 2][(i % 2) * 2], rc[k][i / 2][(i % 2) * 2 + 1]}; };
#pragma unroll
    for (int j = 0; j < 9; ++j) {
      f32x2 u[4], w[4];
#pragma unroll
      for (int i = 0; i < 4; ++i) u[i] = __builtin_elementwise_fma(RC(0, i), cs[j], f32x2{ac[i / 2][j][(i % 2) * 2], ac[i / 2][j][(i % 2) * 2 + 1]});
#pragma unroll
      for (int i = 0; i < 4; ++i) w[i] = u[i] * u[i];
#pragma unroll
      for (int i = 0; i < 4; ++i) w[i] = __builtin_elementwise_fma(w[i], RC(3, i), RC(2, i));
#pragma unroll
      for (int i = 0; i < 4; ++i) w[i] = u[i] * w[i];
      __builtin_amdgcn_sched_barrier(0);
#pragma unroll
      for (int i = 0; i < 4; ++i) w[i] = f32x2{__builtin_amdgcn_exp2f(w[i][0]), __builtin_amdgcn_exp2f(w[i][1])};
      __builtin_amdgcn_sched_barrier(0);
#pragma unroll
      for (int i = 0; i < 4; ++i) w[i] = __builtin_elementwise_fma(w[i], RC(1, i), RC(1, i));
      __builtin_amdgcn_sched_barrier(0);
#pragma unroll
      for (int i = 0; i < 4; ++i) w[i] = f32x2{__builtin_amdgcn_rcpf(w[i][0]), __builtin_amdgcn_rcpf(w[i][1])};
      __builtin_amdgcn_sched_barrier(0);
#pragma unroll
      for (int i = 0; i < 4; ++i) z[j][i] = u[i] * w[i];
#pragma unroll
      for (int i = 0; i < 4; ++i) {
        if (j == 1) sm[i] = z[0][i] + z[1][i];
        if (j > 1) sm[i] += z[j][i];
      }
#pragma unroll
      for (int i = 0; i < 4; ++i) sq[i] = j ? __builtin_elementwise_fma(z[j][i], z[j][i], sq[i]) : z[j][i] * z[j][i];
      asm volatile("" : "+v"(z[j][0]), "+v"(z[j][1]), "+v"(z[j][2]), "+v"(z[j][3]));
      __builtin_amdgcn_sched_barrier(0);
    }
  }
  f32x4 e;
  {
    float v[4][4], d[2][4], f[4];
#pragma unroll
    for (int i = 0; i < 4; ++i) {
      v[i][0] = sm[i][0];
      v[i][1] = sq[i][0];
      v[i][2] = sm[i][1];
      v[i][3] = sq[i][1];
    }
    asm volatile("s_nop 1" : "+v"(v[3][1]), "+v"(v[3][3]), "+v"(v[2][1]), "+v"(v[2][3]));
#pragma unroll
    for (int i = 0; i < 2; ++i)
#pragma unroll
      for (int c = 0; c < 4; ++c)
        asm volatile("v_add_f32_dpp %0, %1, %1 row_ror:8 row_mask:0xf bank_mask:0x3\n\tv_add_f32_dpp %0, %2, %2 row_ror:8 row_mask:0xf bank_mask:0xc"
                     : "=&v"(d[i][c])
                     : "v"(v[i][c]), "v"(v[i + 2][c]));
#pragma unroll
    for (int c = 0; c < 4; ++c)
      asm volatile("v_add_f32_dpp %0, %1, %1 row_shl:4 row_mask:0xf bank_mask:0x5\n\tv_add_f32_dpp %0, %2, %2 row_shr:4 row_mask:0xf bank_mask:0xa"
                   : "=&v"(f[c])
                   : "v"(d[0][c]), "v"(d[1][c]));
    asm volatile(
        "v_add_f32_dpp %0, %0, %0 quad_perm:[1,0,3,2] row_mask:0xf bank_mask:0xf\n\t"
        "v_add_f32_dpp %1, %1, %1 quad_perm:[1,0,3,2] row_mask:0xf bank_mask:0xf\n\t"
        "v_add_f32_dpp %2, %2, %2 quad_perm:[1,0,3,2] row_mask:0xf bank_mask:0xf\n\t"
        "v_add_f32_dpp %3, %3, %3 quad_perm:[1,0,3,2] row_mask:0xf bank_mask:0xf\n\t"
        "v_add_f32_dpp %0, %0, %0 quad_perm:[2,3,0,1] row_mask:0xf bank_mask:0xf\n\t"
        "v_add_f32_dpp %1, %1, %1 quad_perm:[2,3,0,1] row_mask:0xf bank_mask:0xf\n\t"
        "v_add_f32_dpp %2, %2, %2 quad_perm:[2,3,0,1] row_mask:0xf bank_mask:0xf\n\t"
        "v_add_f32_dpp %3, %3, %3 quad_perm:[2,3,0,1] row_mask:0xf bank_mask:0xf"
        : "+v"(f[0]), "+v"(f[1]), "+v"(f[2]), "+v"(f[3]));
    e = f32x4{f[0], __uint_as_float(__float_as_uint(f[1]) & 0x7fffffffu), f[2], __uint_as_float(__float_as_uint(f[3]) & 0x7fffffffu)};
  }
  asm volatile("s_mov_b64 exec, %3\n\tglobal_store_dwordx4 %0, %1, %2 sc1\n\ts_mov_b64 exec, -1\n\ts_nop 0" ::"v"(po), "v"(e), "s"(zp),
               "s"(0x1111111111111111ull)
               : "memory");
  asm volatile("s_mov_b64 exec, 1\n\tds_add_u32 %0, %1\n\ts_mov_b64 exec, -1" ::"v"(adw), "v"(mev) : "memory");
  __builtin_amdgcn_s_setprio(2);
  MK(12);
  const char* dst = uni_ptr(ws + OFF_Z2 + (size_t)layer * SZ_Z + (size_t)ni * S_ * 288 + (size_t)(mi * 4 + wm) * 9216 + 4096);
  const unsigned lane16 = (unsigned)lane * 16u;
  u32x4 wb[9];
  {
    static_assert(b_k(7) == 2 && b_k(8) == 3, "order");
    const char* wp = uni_ptr(ws + OFF_PI + (size_t)ni * (18 * K3C::WB) + 11 * K3C::WB + wm * 9216 + 4096);
#pragma unroll
    for (int j = 0; j < 8; ++j) asm volatile("global_load_dwordx4 %0, %1, %2 offset:%3 sc1" : "=v"(wb[j]) : "v"(lane16), "s"(wp), "n"(j * 1024 - 4096) : "memory");
    asm volatile("global_load_dwordx4 %0, %1, %2 sc1" : "=v"(wb[8]) : "v"(lane16 + 4096u), "s"(wp) : "memory");
  }
  u32x4 y[9];
  {
    const unsigned src = (unsigned)(g * 4096 + r * 16);
    unsigned zw = lds0 + (unsigned)(K3_ZC_OFF + (wm * 32 + 2 * r + (g & 1)) * 4), zr = lds0 + (unsigned)(K3_ZC_OFF + (wm * 32 + g * 8) * 4);
    asm volatile("" : "+v"(zw), "+v"(zr));
    f32x4 a, b, c, d;
    __builtin_amdgcn_s_sleep(6);
#pragma unroll 1
    for (unsigned spin = 0; spin < SPIN_MEM; ++spin) {
      asm volatile(
          "global_load_dwordx4 %0, %4, %5 sc1\n\t"
          "global_load_dwordx4 %1, %4, %5 offset:1024 sc1\n\t"
          "global_load_dwordx4 %2, %4, %5 offset:2048 sc1\n\t"
          "global_load_dwordx4 %3, %4, %5 offset:3072 sc1\n\t"
          "s_waitcnt vmcnt(0)"
          : "=&v"(a), "=&v"(b), "=&v"(c), "=&v"(d)
          : "v"(src), "s"(zp)
          : "memory");
      const unsigned bad = (__float_as_uint(a[1]) | __float_as_uint(a[3]) | __float_as_uint(b[1]) | __float_as_uint(b[3]) |
                            __float_as_uint(c[1]) | __float_as_uint(c[3]) | __float_as_uint(d[1]) | __float_as_uint(d[3])) >> 31;
      if (!__any(bad)) break;
    }
    MK(13);
    float s, q, zmb, zi;
    red16(a, b, c, d, s, q);
    ln_finish(s, q, 1.0f / (float)FH_, zmb, zi);
    f32x4 R[2], M[2];
    asm volatile(
        "ds_write2st64_b32 %4, %5, %6 offset1:2\n\t"
        "ds_read_b128 %0, %7\n\tds_read_b128 %2, %7 offset:512\n\tds_read_b128 %1, %7 offset:16\n\tds_read_b128 %3, %7 offset:528\n\t"
        "s_waitcnt lgkmcnt(0)"
        : "=&v"(R[0]), "=&v"(R[1]), "=&v"(M[0]), "=&v"(M[1])
        : "v"(zw), "v"(zi), "v"(-zmb * zi), "v"(zr)
        : "memory");
    MK(28);
    auto st = [&](int j) {
      if (j < 8) asm volatile("global_store_dwordx4 %0, %1, %2 offset:%3 sc1\n\ts_nop 1" ::"v"(lane16), "v"(y[j]), "s"(dst), "n"(j * 1024 - 4096) : "memory");
      else asm volatile("global_store_dwordx4 %0, %1, %2 sc1\n\ts_nop 1" ::"v"(lane16 + 4096u), "v"(y[j]), "s"(dst) : "memory");
    };
#pragma unroll
    for (int j = 0; j < 9; ++j) {
      {
        float t[8];
#pragma unroll
        for (int k = 0; k < 8; ++k) {
          t[k] = __builtin_fmaf(z[j][k / 2][k % 2], R[k / 4][k % 4], M[k / 4][k % 4]);
          asm volatile("" : "+v"(t[k]));
        }
        y[j] = u32x4{pack2(t[0], t[1]), pack2(t[2], t[3]), pack2(t[4], t[5]), pack2(t[6], t[7])};
      }
      st(j);
      __builtin_amdgcn_sched_barrier(0);
    }
    MK(29);
  }
  lds_inc(lw + LW_AD * 4, me);
  MK(24);
  __builtin_amdgcn_s_setprio(2);
  lds_spin4<2>(lw + LW_S * 4, 4u + (unsigned)(wm >> 1));
  {
    const unsigned fa = lds0 + (unsigned)((wm >> 1) * SB + K3C::XB + (wm & 1) * 9216) + lane16;
#pragma unroll
    for (int j = 0; j < 9; ++j) {
      asm volatile("ds_write_b128 %0, %1 offset:%2" ::"v"(fa), "v"(wb[j]), "n"(j * 1024) : "memory");
    }
    lds_inc(lw + LW_AD * 4, me);
  }
  announce(lw + LW_Z * 4, 4u, ws, ((mi >= ZQ) ? CN_Z2 : CN_Z) + layer, lane * 16 + ni, lane < 16);
  lds_inc(lw + LW_AD * 4, me);
  MK(27);
  f32x4 acc[2][9];
  {
    float bs[2];
    asm volatile("ds_read2_b32 %0, %1 offset1:16\n\ts_waitcnt lgkmcnt(0)"
                 : "=v"(*reinterpret_cast<f32x2*>(bs))
                 : "v"(lds0 + (unsigned)(K3_BS_OFF + (wm * 32 + r) * 4))
                 : "memory");
    lds_spin4<2>(lw + LW_S * 4, 6u + (unsigned)(wm >> 1));
    const unsigned fa = lds0 + (unsigned)((2 + (wm >> 1)) * SB + K3C::XB + (wm & 1) * 9216) + lane16;
#pragma unroll
    for (int j = 0; j < 9; ++j) {
      asm volatile("ds_write_b128 %0, %1 offset:%2" ::"v"(fa), "v"(y[j]), "n"(j * 1024) : "memory");
    }
    lds_inc(lw + LW_AD * 4, me);
#pragma unroll
    for (int mt = 0; mt < 2; ++mt) {
      f32x4 b4 = {bs[mt], bs[mt], bs[mt], bs[mt]};
      asm volatile("" : "+v"(b4));
#pragma unroll
      for (int j = 0; j < 9; ++j) acc[mt][j] = b4;
    }
  }
  u32x4 z1[9];
  const char* hp = uni_ptr(ws + OFF_H + (size_t)layer * SZ_Z + (size_t)((mi * 2 + (wm >> 1)) * 288 + ni * 18) * 1024);
  unsigned ho = (unsigned)((lane >> 5) * 1024 + ((wm & 1) * 32 + (lane & 31)) * 16);
  unsigned zb = lds0 + (unsigned)(wm * 4096) + lane16, zf = lw + (unsigned)(LW_S * 4 + wm);
  asm volatile("" : "+v"(ho), "+v"(zb), "+v"(zf));
  asm volatile("" : "+s"(hp));
  {
    CMath gm(acc);
    gm.setup(lds0, wm, lane);
    unsigned za = lds0 + (unsigned)(K3_Z1_OFF + wm * 5120) + lane16;
    asm volatile("" : "+v"(za));
    MK(15);
    lds_spin4<2>(lw + LW_L * 4, 8u);
    lds_spin4(lw + LW_AD * 4, 6u);
    MK(17);
    gm.first();
    __builtin_amdgcn_s_setprio(0);
    gm.stage<2, false>();
    gm.stage<3>();
    place();
#pragma unroll 1
    for (int t = 0; t < 7; ++t) {
      gm.stage<0>();
#pragma unroll
      for (int j = 0; j < NR1; ++j) asm volatile("ds_read_b128 %0, %1 offset:%2" : "=v"(z1[j]) : "v"(za), "n"(j * 1024));
      gm.stage<1>();
#pragma unroll
      for (int j = 0; j < NR1; ++j) asm volatile("" : "+v"(z1[j]));
      gm.stage<2>();
      gm.stage<3>();
    }
    gm.stage<0>();
    gm.stage<1, false, true>();
#pragma unroll
    for (int j = 0; j < 18; ++j) asm volatile("" : "+v"(acc[j / 9][j % 9]));
  }
  MK(16);
  if (NR1 < 5) lds_inc(lw + LW_AD * 4, me);
  unsigned zv;
  u32x4 zl[4];
#pragma unroll
  for (int j = 0; j < 9; ++j) {
    const bool sec = j >= NR1 + 3;
    if (j == NR1 || j == NR1 + 4) {
      unsigned n = 1u << 18;
      asm volatile(
          "s_waitcnt lgkmcnt(0)\n\tv_cmp_gt_u32 vcc, %8, %0\n\ts_cbranch_vccnz .Lw%=\n\t.subsection 1\n.Lw%=:\n\tds_read_u8 %0, %6\n\tds_read_b128 %2, %7 offset:%9\n\t"
          "ds_read_b128 %3, %7 offset:%9+1024\n\tds_read_b128 %4, %7 offset:%9+2048\n\tds_read_b128 %5, %7 offset:%9+3072\n\ts_waitcnt lgkmcnt(0)\n\t"
          "v_cmp_gt_u32 vcc, %8, %0\n\t" SAFE_OUT(".Lr%=") "s_cbranch_vccnz .Lw%=\n\ts_branch .Lr%=\n\t.subsection 0\n.Lr%=:"
          : "+v"(zv), NOP_(n), "+v"(zl[0]), "+v"(zl[1]), "+v"(zl[2]), "+v"(zl[3])
          : "v"(zf), "v"(zb), "n"(sec ? 11 : 10), "n"(sec ? SB : 0)
          : "memory", "vcc", "scc");
#pragma unroll
      for (int k = 0; k < 4; ++k)
        if (j + k < 9) z1[j + k] = zl[k];
    }
    if (j == NR1 - 1 || (j == NR1 + 3 && j < 8))
      asm volatile("ds_read_u8 %0, %5\n\tds_read_b128 %1, %6 offset:%7\n\tds_read_b128 %2, %6 offset:%7+1024\n\tds_read_b128 %3, %6 offset:%7+2048\n\t"
                   "ds_read_b128 %4, %6 offset:%7+3072"
                   : "=&v"(zv), "=&v"(zl[0]), "=&v"(zl[1]), "=&v"(zl[2]), "=&v"(zl[3])
                   : "v"(zf), "v"(zb), "n"(sec ? SB : 0)
                   : "memory");
    asm volatile("" : "+v"(z1[j]));
    u32x2 h[2];
#pragma unroll
    for (int mt = 0; mt < 2; ++mt) {
      const f32x4 a = acc[mt][j];
      h[mt] = u32x2{pack2v(f32x2{bf_lo(z1[j][2 * mt]), bf_hi(z1[j][2 * mt])} * f32x2{a[0], a[1]}),
                    pack2v(f32x2{bf_lo(z1[j][2 * mt + 1]), bf_hi(z1[j][2 * mt + 1])} * f32x2{a[2], a[3]})};
    }
    const auto s0 = __builtin_amdgcn_permlane16_swap(h[0][0], h[1][0], false, false);
    const auto s1 = __builtin_amdgcn_permlane16_swap(h[0][1], h[1][1], false, false);
    const u32x4 v = {s0[0], s1[0], s0[1], s1[1]};
    asm volatile("global_store_dwordx4 %0, %1, %2 offset:%3 sc1\n\ts_nop 1" ::"v"(ho), "v"(v), "s"(hp + (j / 2) * 4096), "n"((j % 2) * 2048) : "memory");
    __builtin_amdgcn_sched_barrier(0);
  }
  MK(18);
}

DEVI void k3_const(char* smem, char* ws, const u16* b_sp, int layer, int bid, int mi, int ni, int wm) {
  const int lane = lane_id(), l = lane < 54 ? lane : 53, p = l / 18;
  const char* kp = uni_ptr(ws + OFF_CSK + bid * 2048 + wm * 512);
  const unsigned q16 = (unsigned)(l - p * 18) * 16u;
  unsigned bs;
  f32x4 t;
  lds_spin((unsigned)(size_t)smem + (unsigned)(LW_OFF + (layer ? LW_T + 1 : LW_HELLO) * 4), layer ? 12u : 2u);
  asm volatile("global_load_ushort %0, %1, %2" : "=v"(bs) : "v"((unsigned)(lane & 31) * 2u), "s"(uni_ptr((const char*)(b_sp + mi * 128 + wm * 32))) : "memory");
  if (layer) {
    asm volatile("global_load_dwordx4 %0, %1, %2 sc1\n\ts_waitcnt vmcnt(0)" : "=&v"(t) : "v"(q16), "s"(kp) : "memory");
  } else {
    const char* cp = uni_ptr(ws + OFF_CSP + ni * 1152 + wm * 288);
    f32x4 a[6];
#pragma unroll
    for (int i = 0; i < 6; ++i)
      asm volatile("global_load_dwordx4 %0, %1, %2 sc1" : "=v"(a[i]) : "v"(q16 + (unsigned)(p * (6 * F_ * 4))), "s"(cp + i * (F_ * 4)) : "memory");
    asm volatile("s_waitcnt vmcnt(0)" : "+v"(a[0]), "+v"(a[1]), "+v"(a[2]), "+v"(a[3]), "+v"(a[4]), "+v"(a[5]));
    t = ((a[0] + a[1]) + (a[2] + a[3])) + (a[4] + a[5]);
#pragma unroll
    for (int j = 0; j < 4; ++j)
      t[j] += __int_as_float(__builtin_amdgcn_ds_bpermute((lane + 18) * 4, __float_as_int(t[j]))) +
              __int_as_float(__builtin_amdgcn_ds_bpermute((lane + 36) * 4, __float_as_int(t[j])));
  }
  asm volatile("" : "+v"(bs));
  const unsigned lds0 = (unsigned)(size_t)smem;
  f32x4 t0 = wm < 2 ? f32x4{t[0], t[0], t[1], t[1]} : t, t1 = wm < 2 ? f32x4{t[2], t[2], t[3], t[3]} : t;
  unsigned a0 = lds0 + (unsigned)(wm < 2 ? K3_C2_OFF + wm * 576 + lane * 32 : K3_C1_OFF + (wm - 2) * 288 + lane * 16), a1 = a0 + (wm < 2 ? 16u : 0u);
  unsigned ab = lds0 + (unsigned)(K3_BS_OFF + (wm * 32 + (lane & 31)) * 4);
  bs <<= 16;
  asm volatile("" : "+v"(t0), "+v"(t1), "+v"(a0), "+v"(a1), "+v"(ab), "+v"(bs));
  bar();
  asm volatile("ds_write_b32 %4, %5\n\ts_mov_b64 exec, 0x3ffff\n\tds_write_b128 %0, %2\n\tds_write_b128 %1, %3\n\ts_mov_b64 exec, -1" ::"v"(a0), "v"(a1),
               "v"(t0), "v"(t1), "v"(ab), "v"(bs)
               : "memory");
  if (layer == 0 && lane < 24) asm volatile("global_store_dwordx4 %0, %1, %2\n\ts_nop 1" ::"v"((unsigned)lane * 16u), "v"(t), "s"(kp) : "memory");
}

DEVI void p3_g1(char* smem, char* __restrict__ ws, int layer, int mi, int ni, int wm, int lane MK_ARG) {
  const unsigned lds0 = (unsigned)(size_t)smem;
  const int g = lane >> 4, r = lane & 15;
  const unsigned lw = lds0 + (unsigned)(LW_OFF + layer * 4);
  const unsigned xrd = lds0 + (unsigned)(g * 2048 + (wm * 32 + r) * 16);
  s16x8 xc[4][2][2];
  {
    const char* xs = uni_ptr(ws + OFF_XS + (size_t)layer * 2 * SZ_XS + (size_t)(mi * 2 + (wm >> 1)) * 4096 + (wm & 1) * 256);
    f32x4 a, b;
    auto pick = [&](int t) {
      bar();
      asm volatile(
          "ds_read_b128 %0, %4\n\tds_read_b128 %1, %4 offset:256\n\tds_read_b128 %2, %4 offset:8192\n\tds_read_b128 %3, %4 offset:8448\n\t"
          "s_waitcnt lgkmcnt(0)"
          : "=&v"(xc[t][0][0]), "=&v"(xc[t][0][1]), "=&v"(xc[t][1][0]), "=&v"(xc[t][1][1])
          : "v"(xrd + (unsigned)(t * K3C::SB))
          : "memory");
    };
    __builtin_amdgcn_s_setprio(2);
    pick(0);
    pick(1);
    asm volatile("global_load_dwordx4 %0, %2, %3 sc1\n\tglobal_load_dwordx4 %1, %2, %3 offset:512 sc1"
                 : "=&v"(a), "=&v"(b)
                 : "v"((unsigned)(g * 1024 + r * 16)), "s"(xs)
                 : "memory");
    pick(2);
    pick(3);
    bar();
    asm volatile("s_waitcnt vmcnt(0)" : "+v"(a), "+v"(b) : : "memory");
    float s, q, mb, rinv;
    red8(a, b, s, q);
    bar();
    asm volatile("" : "+v"(s), "+v"(q));
    ln_finish(s, q, 1.0f / (float)D_, mb, rinv);
    asm volatile("" : "+v"(mb), "+v"(rinv));
    bar();
    asm volatile("" : "+v"(mb), "+v"(rinv));
    const float k0 = -2.0f * 0.7978845608028654f * 1.4426950408889634f, k1 = k0 * 0.044715f;
    float sd = __builtin_amdgcn_rcpf(rinv), c0 = k0 * rinv, c1 = k1 * rinv * rinv * rinv;
    asm volatile("" : "+v"(sd));
    asm volatile("" : "+v"(c0));
    asm volatile("" : "+v"(c1));
    const float e0 = lane < 32 ? -mb : c0, e1 = lane < 32 ? sd : c1;
    asm volatile("ds_write2st64_b32 %0, %1, %2 offset1:2" ::"v"(
                     lds0 + (unsigned)(K3_ST_OFF + (g >> 1) * 1024 + (wm * 32 + 2 * r + (g & 1)) * 4)),
                 "v"(e0), "v"(e1)
                 : "memory");
    bar();
    asm volatile("s_waitcnt lgkmcnt(0)" ::: "memory");
    bar();
    __builtin_amdgcn_s_setprio(0);
  }
  f32x4 ac[2][9];
#pragma unroll
  for (int mt = 0; mt < 2; ++mt)
#pragma unroll
    for (int j = 0; j < 9; ++j) ac[mt][j] = f32x4{0.f, 0.f, 0.f, 0.f};
  {
    UMath<true> gm(ac, xc);
    gm.xa = xrd;
    gm.wa = lds0 + (unsigned)(K3C::XB + (g * 8 + (r & 7)) * K3C::P + (r >> 3) * 16);
    gm.fa = lw + (unsigned)(LW_AD * 4 + (lane & 3));
    gm.fw = lw + (unsigned)(LW_S * 4 + wm);
    gm.fv = 0u;
    asm volatile("" : "+v"(gm.fa), "+v"(gm.fw), "+v"(gm.fv), "+v"(gm.xa), "+v"(gm.wa));
    __builtin_amdgcn_s_sleep(40);
    lds_spin4<4>(lw + LW_L * 4, 4u);
    gm.first();
    place();
    lds_spin4(lw + LW_AD * 4, 2u);
    __builtin_amdgcn_s_setprio(1);
    gm.run();
    __builtin_amdgcn_s_setprio(0);
  }
  MK(14);
  f32x2 rc[4];
  f32x4 cs[9];
  const int l2 = lane_id();
  {
    const unsigned ra = lds0 + (unsigned)(K3_ST_OFF + (wm * 32 + (l2 & 15)) * 4), ca = lds0 + (unsigned)(K3_C1_OFF + (l2 & 48));
#pragma unroll
    for (int k = 0; k < 4; ++k) asm volatile("ds_read2_b32 %0, %1 offset1:16" : "=v"(rc[k]) : "v"(ra + (unsigned)(k * 512)) : "memory");
#pragma unroll
    for (int j = 0; j < 9; ++j) asm volatile("ds_read_b128 %0, %1 offset:%2" : "=v"(cs[j]) : "v"(ca), "n"(j * 64) : "memory");
    asm volatile("s_waitcnt lgkmcnt(0)" : "+v"(rc[0]), "+v"(rc[1]), "+v"(rc[2]), "+v"(rc[3]));
#pragma unroll
    for (int j = 0; j < 9; ++j) asm volatile("" : "+v"(cs[j]));
  }
  const unsigned za = lds0 + (unsigned)(K3_Z1_OFF + wm * 5120 + l2 * 16);
  u32x4 z1[9];
  unsigned zf;
#pragma unroll
  for (int t = 0; t < 18; ++t) {
    const int j = t / 2, mt = t % 2;
    float u[4], w[4];
#define Q4(A, B, C, D) A "\n\t" B "\n\t" C "\n\t" D "\n\t"
    asm volatile(Q4("v_fma_f32 %0, %8, %12, %16", "v_fma_f32 %1, %8, %13, %17", "v_fma_f32 %2, %8, %14, %18", "v_fma_f32 %3, %8, %15, %19")
                 Q4("v_mul_f32 %4, %0, %0", "v_mul_f32 %5, %1, %1", "v_mul_f32 %6, %2, %2", "v_mul_f32 %7, %3, %3")
                 Q4("v_fma_f32 %4, %4, %11, %10", "v_fma_f32 %5, %5, %11, %10", "v_fma_f32 %6, %6, %11, %10", "v_fma_f32 %7, %7, %11, %10")
                 Q4("v_mul_f32 %4, %4, %0", "v_mul_f32 %5, %5, %1", "v_mul_f32 %6, %6, %2", "v_mul_f32 %7, %7, %3")
                 Q4("v_exp_f32 %4, %4", "v_exp_f32 %5, %5", "v_exp_f32 %6, %6", "v_exp_f32 %7, %7")
                 Q4("v_fma_f32 %4, %4, %9, %9", "v_fma_f32 %5, %5, %9, %9", "v_fma_f32 %6, %6, %9, %9", "v_fma_f32 %7, %7, %9, %9")
                 Q4("v_rcp_f32 %4, %4", "v_rcp_f32 %5, %5", "v_rcp_f32 %6, %6", "v_rcp_f32 %7, %7")
                 Q4("v_mul_f32 %4, %4, %0", "v_mul_f32 %5, %5, %1", "v_mul_f32 %6, %6, %2", "v_mul_f32 %7, %7, %3")
                 : "=&v"(u[0]), "=&v"(u[1]), "=&v"(u[2]), "=&v"(u[3]), "=&v"(w[0]), "=&v"(w[1]), "=&v"(w[2]), "=&v"(w[3])
                 : "v"(rc[0][mt]), "v"(rc[1][mt]), "v"(rc[2][mt]), "v"(rc[3][mt]), "v"(cs[j][0]), "v"(cs[j][1]), "v"(cs[j][2]), "v"(cs[j][3]),
                   "v"(ac[mt][j][0]), "v"(ac[mt][j][1]), "v"(ac[mt][j][2]), "v"(ac[mt][j][3]));
    z1[j][2 * mt] = pack2(w[0], w[1]);
    z1[j][2 * mt + 1] = pack2(w[2], w[3]);
    if (mt && j < NR1) asm volatile("ds_write_b128 %0, %1 offset:%2" ::"v"(za), "v"(z1[j]), "n"(j * 1024) : "memory");
    if (t == 9) asm volatile("s_waitcnt lgkmcnt(0)" ::: "memory");
    __builtin_amdgcn_sched_barrier(0);
    if (t == 2 * ZQ - 7)
      asm volatile("global_load_dword %0, %1, %2 sc1"
                   : "=v"(zf)
                   : "v"(vconst(0u)), "s"(uni_ptr(ws + OFF_CNT + (size_t)((CN_Z2 + layer) * 256 + mi * 16 + ni) * 128))
                   : "memory");
    if (t == 2 * ZQ - 4) {
      asm volatile("s_waitcnt vmcnt(0)" : "+v"(zf) : : "memory");
      if (wm == 1 && __builtin_amdgcn_readfirstlane(zf) != 15u - ZQ) cnt_wait(ws, CN_Z2 + layer, mi * 16 + ni, 16u - ZQ);
    }
    bar();
    __builtin_amdgcn_sched_barrier(0);
  }
  MK(15);
#pragma unroll 1
  for (int t = 20; t < 32; ++t) bar();
  __builtin_amdgcn_s_sleep(9);
#pragma unroll
  for (int k = NR1; k < 9; ++k) {
    if (k == NR1 + 4) lds_spin4(lw + LW_AD * 4, 7u);
    asm volatile("ds_write_b128 %0, %1 offset:%2" ::"v"(lds0 + (unsigned)(wm * 4096 + l2 * 16)), "v"(z1[k]),
                 "n"(((k - NR1) / 4) * K3C::SB + ((k - NR1) % 4) * 1024)
                 : "memory");
    if (k == NR1 + 3 || k == 8) asm volatile("ds_write_b8 %0, %1" ::"v"(lw + (unsigned)(LW_S * 4 + wm)), "v"(vconst(k == NR1 + 3 ? 10u : 11u)) : "memory");
  }
}

constexpr int W5A = 22;
constexpr int NPF = 30;
struct Loader {
  u32x4 pf[NPF];
  unsigned lds0, lane16;
  int wl, lane;

  template <int R0, int N, int P0, int PE>
  DEVI void pf_load(const char* src) {
    const unsigned o = lane16 + (unsigned)(wl * 1024);
#pragma unroll
    for (int t = 0; t < N; ++t) {
      unsigned a = o + (unsigned)((P0 + 4 * t) * 1024);
      if (P0 + 4 * t + 3 >= PE && P0 + 4 * t + wl >= PE) a = lane16;
      asm volatile("global_load_dwordx4 %0, %1, %2 sc1" : "=v"(pf[R0 + t]) : "v"(a), "s"(src) : "memory");
    }
  }
  template <int R0, int N>
  DEVI void pf_pin() {
#pragma unroll
    for (int t = 0; t < N; ++t) asm volatile("" : "+v"(pf[R0 + t]));
  }
  template <int R0, int N>
  DEVI void pf_undef() {
#pragma unroll
    for (int t = 0; t < N; ++t) asm volatile("" : "=v"(pf[R0 + t]));
  }
  template <int PB, int A, int N>
  DEVI void pf_put(unsigned dst) {
    constexpr int T0 = (A - PB) / 4, T1 = (A + N - 1 - PB) / 4;
    const unsigned a = lds0 + dst + lane16 + (unsigned)((wl + PB + 4 * T0 - A + 4) * 1024) - 4096u;
#pragma unroll
    for (int t = T0; t <= T1; ++t) {
      const int p0 = PB + 4 * t;
      if ((p0 >= A || p0 + wl >= A) && (p0 + 3 < A + N || p0 + wl < A + N))
        asm volatile("ds_write_b128 %0, %1 offset:%2" ::"v"(a), "v"(pf[t]), "n"((t - T0) * 4096) : "memory");
    }
  }
  template <int N>
  DEVI void dma_run(const char* src, unsigned dst) {
    constexpr int B = N / 4, R = N % 4;
    const unsigned f = (unsigned)(wl * B + (wl < R ? wl : R)) * 1024u, o = lane16 + f, m = lds0 + dst + f;
    dma_seq<B>(src, o, m);
    if (wl < R) dma16_sc1(src + B * 1024, o, m + (unsigned)(B * 1024));
  }
  template <int V>
  DEVI void wait_w() {
    asm volatile("s_cmp_lt_u32 %0, 2\n\ts_cbranch_scc1 1f\n\ts_waitcnt vmcnt(%1)\n1:\n\ts_waitcnt vmcnt(%2)" ::"s"(wl), "n"(V - 1), "n"(V) : "memory", "scc");
  }
  template <int V>
  DEVI void wbar() {
    asm volatile("s_waitcnt vmcnt(%0) lgkmcnt(0)" ::"n"(V) : "memory");
    bar();
  }

  DEVI void p1(char* smem, char* ws, int bid, int rt, int hp MK_ARG) {
    constexpr int SB = K1C::SB;
    static_assert(K1C::XB == 18 * 1024 && K1C::WB == 14 * 1024, "K1 shape");
    const char* wp = uni_ptr(ws + OFF_PQ + (size_t)hp * (9 * K1C::WB) + 4 * K1C::WB);
    static_assert(3 * SB + K1C::XB >= P5_W + 5 * 18432, "free while the GEMM before ends");
    const char* wq = wp - 4 * K1C::WB;
    dma_run<14>(wq + 3 * K1C::WB, (unsigned)(3 * SB + K1C::XB));
    dma_run<28>(wp, (unsigned)K1_WX);
    if (wl == 3) dma16_sc1(uni_ptr(ws + OFF_CSQ + bid * 512), lane16 & 511u, lds0 + (unsigned)K1_CS_OFF);
    __builtin_amdgcn_s_setprio(0);
    wait_vm<0>();
    const bool lo = wl < 2;
    const int x0 = lo ? wl * 5 : wl * 4 + 2, w0 = lo ? wl * 3 : wl * 4 - 2;
    unsigned xo[5];
#pragma unroll
    for (int i = 0; i < 5; ++i) {
      const int c = (x0 + i) * 64 + lane;
      const int ch = c / 144;
      int gr = rt * 128 - 16 + (c - ch * 144);
      if (gr < 0) gr = 0;
      xo[i] = (unsigned)(((gr >> 6) * 72 + ch + 3 - (i & 3)) * 1024 + (gr & 63) * 16);
      asm volatile("" : "+v"(xo[i]));
    }
    const char* xp = uni_ptr(ws + OFF_X1 + SZ_X - 3072);
    wp += 2 * K1C::WB + w0 * 1024;
    auto dma_x = [&](unsigned so) {
      const unsigned m = lds0 + so + (unsigned)(x0 * 1024);
      dma4<4, true>(xp, xo[0], xo[1], xo[2], xo[3], m);
      if (lo) dma16_sc1(xp, xo[4], m + 4096u);
      xp += 8192;
    };
    auto dma_w = [&](unsigned so) {
      const unsigned m = lds0 + so + (unsigned)(K1C::XB + w0 * 1024);
      dma_seq<3>(wp, lane16, m);
      if (!lo) dma16_sc1(wp + 3072, lane16, m + 3072u);
      wp += K1C::WB;
    };
    const char* xs = uni_ptr(ws + OFF_XS + SZ_XS);
    const int g = lane >> 4, p2 = 64 + (lane & 7);
    int ra = rt * 128 - 16 + wl * 32 + 2 * (lane & 15);
    if (ra < 0) ra = 0;
    unsigned sa = (unsigned)(((ra >> 6) * 8 + g * 2) * 512 + (ra & 63) * 8);
    unsigned sb = (unsigned)(((rt * 2 + 1) * 8 + (lane >> 3)) * 512 + (2 * p2 - 80) * 8);
    unsigned sw = lds0 + (unsigned)(K1_ST_OFF + (2 * (lane < 32 ? wl * 16 + (lane & 15) : p2) + (g & 1)) * 8);
    asm volatile("" : "+v"(sa), "+v"(sb), "+v"(sw));
    MK(22);
    __builtin_amdgcn_s_setprio(3);
    bar();
    dma_run<14>(wq, (unsigned)K1C::XB);
    dma_run<14>(wq + K1C::WB, (unsigned)(SB + K1C::XB));
    dma_run<14>(wq + 2 * K1C::WB, (unsigned)(2 * SB + K1C::XB));
    MK(1);
    bar();
    dma_x(0u);
    dma_x((unsigned)SB);
    wbar<4>();
    MK(29);
    dma_x((unsigned)(2 * SB));
    dma_x((unsigned)(3 * SB));
    wbar<8>();
    MX(1, 3);
    dma_x(0u);
    wbar<8>();
    MX(1, 4);
    dma_x((unsigned)SB);
    f32x4 a, b, e;
    asm volatile("global_load_dwordx4 %0, %3, %5 sc1\n\tglobal_load_dwordx4 %1, %3, %5 offset:512 sc1\n\tglobal_load_dwordx4 %2, %4, %5 sc1"
                 : "=&v"(a), "=&v"(b), "=&v"(e)
                 : "v"(sa), "v"(sb), "s"(xs)
                 : "memory");
    wbar<11>();
    MX(1, 5);
    dma_w((unsigned)SB);
    dma_x((unsigned)(2 * SB));
    wbar<15>();
    MX(1, 6);
    dma_w((unsigned)(2 * SB));
    dma_x((unsigned)(3 * SB));
    wbar<19>();
    MX(1, 7);
    dma_w((unsigned)(3 * SB));
    dma_x(0u);
    wbar<16>();
    MX(1, 8);
    asm volatile("" : "+v"(a), "+v"(b), "+v"(e));
    float s, q;
    {
      float t[4] = {e[0], e[1], e[2], e[3]}, s2, q2;
      asm volatile(
          "v_add_f32_dpp %0, %0, %0 row_ror:8 row_mask:0xf bank_mask:0xf\n\tv_add_f32_dpp %1, %1, %1 row_ror:8 row_mask:0xf bank_mask:0xf\n\t"
          "v_add_f32_dpp %2, %2, %2 row_ror:8 row_mask:0xf bank_mask:0xf\n\tv_add_f32_dpp %3, %3, %3 row_ror:8 row_mask:0xf bank_mask:0xf\n\ts_nop 1"
          : "+v"(t[0]), "+v"(t[1]), "+v"(t[2]), "+v"(t[3]));
      red4(t, s2, q2);
      red8(a, b, s, q);
      s = lane < 32 ? s : s2;
      q = lane < 32 ? q : q2;
      asm volatile("" : "+v"(s), "+v"(q));
    }
    wbar<8>();
    MX(1, 9);
    {
      asm volatile("" : "+v"(s), "+v"(q));
      float mb, rinv;
      ln_finish(s, q, 1.0f / (float)D_, mb, rinv);
      asm volatile("ds_write_b64 %0, %1" ::"v"(sw), "v"(f32x2{-mb, rinv}) : "memory");
    }
    wbar<0>();
    MK(30);
  }

  template <int I>
  DEVI void p5_step(const char* wp) {
    constexpr int T = 2 * I + 1;
    wbar<(T == 1 ? 63 : (T <= 11 ? 3 : 0))>();
    if constexpr (T <= 9) {
      const unsigned wo = (unsigned)(12288 + (wl & 1) * 3072), so = (unsigned)(P5_W + ((T + 2 + (wl >> 1)) & 7) * 18432);
      dma_seq<3>(wp + (T + 7) * 18432, lane16 + wo + (unsigned)((wl >> 1) * 18432), lds0 + so + wo);
      pf_put<0, 12 * (T - 1), 12>((unsigned)(P5_W + ((T + 2) % 8) * 18432));
      pf_put<0, 12 * T, 12>((unsigned)(P5_W + ((T + 3) % 8) * 18432));
    }
  }
  template <int... S>
  DEVI void p5_steps(const char* wp, std::integer_sequence<int, S...>) {
    (p5_step<S>(wp), ...);
  }

  template <int T0, int T1>
  DEVI void w5_load(const char* w5p) {
    const unsigned o = lane16 + (unsigned)(wl * 1024);
#pragma unroll
    for (int t = T0; t < T1; ++t)
      asm volatile("global_load_dwordx4 %0, %1, %2 sc1" : "=v"(pf[t]) : "v"(o + (unsigned)((t / 3 + 8) * 18432 + (t % 3) * 4096)), "s"(w5p) : "memory");
  }
  DEVI void p35(char* smem, char* ws, const u16* w_sp, int layer, int bid, int mi, int ni, int rb, int ct, int hp MK_ARG) {
    using C = K3C;
    constexpr int NS = C::NS, SB = C::SB, NDX = 4, NDW = 5, VPS = NDX + NDW;
    const char* wip = uni_ptr(ws + OFF_PI + (size_t)ni * (18 * C::WB));
    if (layer) {
      pf_put<-48, 36, 18>((unsigned)(2 * SB + C::XB));
      pf_put<-48, 54, 18>((unsigned)(3 * SB + C::XB));
    } else {
      dma_run<18>(wip + 2 * C::WB, (unsigned)(2 * SB + C::XB));
      dma_run<18>(wip + 3 * C::WB, (unsigned)(3 * SB + C::XB));
    }
    dma_run<18>(wip + C::WB, (unsigned)(SB + C::XB));
    const char* w5p = uni_ptr(ws + OFF_PW + (size_t)ct * (36 * 9216));
    const int w0 = wl < 2 ? wl * 5120 : wl * 4096 + 2048;
    const unsigned m0x = lds0 + (unsigned)(wl * 4096), m0w = lds0 + (unsigned)(C::XB + w0);
    const unsigned wo = lane16 + (unsigned)w0;
    const unsigned lw = lds0 + (unsigned)(LW_OFF + layer * 4), me = 1u << (8 * wl);
    const unsigned wo4 = wo + 4096u;
    auto dma_w = [&](const char* wp, unsigned so) {
      dma_seq<4>(wp, wo, m0w + so);
      if (wl < 2) dma16_sc1(wp, wo4, m0w + so + 4096u);
    };
    {
      const char* xp = uni_ptr(ws + OFF_X1 + (size_t)layer * 2 * SZ_X + (size_t)(mi * 2 * 72 + wl * 2) * 1024 - 1024);
      const unsigned xo0 = lane16 + 1024u, xo1 = lane16 + 72u * 1024u, xo3 = lane16 + 71u * 1024u;
      auto dma_x = [&](int k, unsigned so) { dma4<4, true>(xp + k * 8192, xo0, xo1, lane16, xo3, m0x + so); };
      MK(10);
      bar();
      dma_x(0, 0u);
      dma_x(1, (unsigned)SB);
      wait_vm<4>(); MX(3, 2); wbar<4>(); MX(2, 2);
      MK(25);
      {
        dma_x(2, (unsigned)(2 * SB));
        dma_x(3, (unsigned)(3 * SB));
        dma_x(4, (unsigned)(4 * SB));
        wbar<12>(); MX(2, 3);
        dma_x(5, 0u);
        wait_vm<12>();
      }
      bar(); MX(2, 4);
      dma_w(wip + 4 * C::WB, 0u);
      dma_x(6, (unsigned)SB);
      wait_w<8 + VPS>();
#pragma unroll 1
      for (int t = 1; t < 3; ++t) {
        bar(); MX(2, 4 + t);
        dma_w(wip + (t + 4) * C::WB, (unsigned)(t * SB));
        dma_x(t + 6, (unsigned)((t + 1) * SB));
        wait_w<NDX + VPS>();
      }
      bar(); MX(2, 7);
      dma_w(wip + 7 * C::WB, (unsigned)(3 * SB));
      wait_w<VPS>();
      bar(); MX(2, 8);
      dma_w(wip + 8 * C::WB, 0u);
      wait_w<NDW>();
      bar(); MX(2, 9);
      const char* wp = wip + 9 * C::WB;
      dma_w(wp + b_k(0) * C::WB, (unsigned)SB);
      if (layer == 0) {
        const char* sp = uni_ptr((const char*)w_sp + (size_t)(bid * 4 + wl) * 8192);
        const unsigned so = (unsigned)((lane >> 5) * 4096 + (lane & 31) * 16);
#pragma unroll
        for (int i = 0; i < WSP_V; ++i) dma16(sp + i * 512, so, lds0 + (unsigned)K3_DM_OFF);
        dma16(sp + (bid >> 4) * 256, so & ~0x100u, lds0 + (unsigned)K3_DM_OFF);
      }
      w5_load<0, 4>(w5p);
      wait_w<NDW + 4>();
      MX(3, 10); bar(); MX(2, 10);
      dma_w(wp + b_k(1) * C::WB, (unsigned)(2 * SB));
      w5_load<4, 8>(w5p);
      MK(26);
      lds_spin4(lw + LW_AD * 4, 1u);
      dma_w(wp + b_k(2) * C::WB, (unsigned)(3 * SB));
      dma_w(wp + b_k(3) * C::WB, 0u);
      w5_load<8, W5A>(w5p);
      wait_vm<W5A - 8>();
      lds_inc(lw + LW_L * 4, 4u * me);
      w5_load<W5A, 30>(w5p);
      MK(11);
    }
    {
      unsigned xoff[NDX];
#pragma unroll
      for (int i = 0; i < NDX; ++i) {
        const int c = wl * 256 + i * 64 + lane;
        const int row = c >> 3, cc = c & 7;
        xoff[i] = (unsigned)((mi * 128 + row) * S_ * 2 - i * 1024) + (unsigned)((cc ^ ((row >> 1) & 7)) * 16);
      }
      auto dma_x = [&](const char* xp, unsigned so) { dma4<4, false>(xp, xoff[0], xoff[1], xoff[2], xoff[3], m0x + so); };
      const char* xp = uni_ptr((const char*)w_sp);
      const char* wp = uni_ptr(ws + OFF_Z2 + (size_t)layer * SZ_Z + (size_t)ni * S_ * 288);
      static_for<4, 7>([&](auto T_) {
        constexpr int t = T_;
        lds_spin4<2>(lw + LW_S * 4, (unsigned)(t - 3));
        dma_w(wip + (9 + b_k(t)) * C::WB, (unsigned)(((t + 1) % 4) * SB));
        if constexpr (t > 4) {
          dma_x(xp + mi * 256 + (t - 5) * 128, (unsigned)((t - 3) * SB));
          wait_w<NDW + (t - 4) * NDX>();
          lds_inc(lw + LW_L * 4, me);
        }
      });
      wait_vm<NDX>();
      lds_inc(lw + LW_L * 4, me);
      wait_vm<0>();
      lds_inc(lw + LW_L * 4, me);
      pf_pin<0, 30>();
      MK(13);
      lds_spin4<2>(lw + LW_AD * 4, 5u);
      MK(24);
      int left = 2 * mi;
      auto next = [&]() {
        const int d = --left == 0 ? 3 : 1;
        xp += d * 128;
        wp += d * C::WB;
      };
      if (mi == 0) {
        xp += 256;
        wp += 2 * C::WB;
      }
      if (wl == 0) {
        cnt_wait(ws, CN_Z + layer, mi * 16 + ni, (unsigned)ZQ);
        lds_wr32(lw + LW_GO * 4, 1u);
      }
      lds_spin4<2>(lw + LW_AD * 4, 6u);
      dma_x(xp, 0u);
      dma_x(xp + 128, (unsigned)SB);
      spin_ge<2>(lw + LW_GO * 4, 1u);
      lds_spin4<2>(lw + LW_S * 4, 8u);
      dma_w(wp, 0u);
      lds_spin4<2>(lw + LW_S * 4, 9u);
      dma_w(wp + C::WB, (unsigned)SB);
      next();
      next();
      unsigned slot = (unsigned)(2 * SB);
      auto cissue = [&]() {
        dma_x(xp, slot);
        dma_w(wp, slot);
        next();
        slot = (slot == (unsigned)((NS - 1) * SB)) ? 0u : slot + (unsigned)SB;
      };
      auto cstep = [&]() {
        bar();
        cissue();
      };
      wait_w<NDW>();
      MK(15);
      int j = 2;
      if (layer == 0) {
        const char* sp = uni_ptr((const char*)w_sp + (size_t)(bid * 4 + wl) * 8192 + WSP_V * 512);
        unsigned d = 0u;
#pragma unroll 1
        for (; j < 2 + 32 - 4 * WSP_V; ++j) {
          cstep();
          asm volatile("s_load_dword %0, %1, 0\n\ts_load_dword %0, %1, 64\n\ts_load_dword %0, %1, 0x1000\n\ts_load_dword %0, %1, 0x1040"
                       : "+s"(d)
                       : "s"(sp));
          sp += 128;
          wait_w<VPS>();
        }
#pragma unroll 1
        for (int e = j + 4; j < e; ++j) {
          cstep();
          asm volatile("" : "+s"(d));
          wait_w<VPS>();
        }
        asm volatile("s_waitcnt lgkmcnt(0)" : "+s"(d));
      }
      place();
#pragma unroll 1
      for (; j <= 29; ++j) {
        cstep();
        wait_w<VPS>();
      }
      bar();
    }
    dma_run<33>(w5p, (unsigned)(P5_W + 54 * 1024));
    dma_run<23>(w5p + 67 * 1024, (unsigned)(P5_W + 121 * 1024));
    if (wl == 3) dma16_sc1(w5p + 143 * 1024, lane16, lds0 + (unsigned)(P5_W + 53 * 1024));
    wait_vm<14>();
    bar();
    dma_run<34>(w5p + 33 * 1024, (unsigned)(P5_W + 87 * 1024));
    wait_vm<0>();
    bar();
    if (wl) {
      const unsigned f = (unsigned)(wl - 1) * 18432u;
      dma_seq<17>(w5p + 90 * 1024, lane16 + f, lds0 + (unsigned)P5_W + f);
      if (wl < 3) dma16_sc1(w5p + 107 * 1024, lane16 + f, lds0 + (unsigned)(P5_W + 17 * 1024) + f);
    } else {
      cnt_wait(ws, CN_H + layer, rb * 8 + ct, 8u);
    }
    MK(20);
    bar();
    p5_steps(w5p, std::make_integer_sequence<int, 7>{});
    if (layer == 0) {
      if (bid < 24 && wl == 0 && lane < 8) pk_st16(ws + OFF_HELLO + bid * 128 + lane16, u32x4{0u, 0u, 0u, 0u});
    }
  }
};

__global__ __launch_bounds__(768, 3) void k_main(const u16* __restrict__ x0, const u16* __restrict__ wqkv, const u16* __restrict__ wo,
                                                 const u16* __restrict__ w_in, const u16* __restrict__ w_sp,
                                                 const u16* __restrict__ b_sp, const u16* __restrict__ w_out,
                                                 u16* __restrict__ out, char* ws) {
  extern __shared__ __attribute__((aligned(16))) char smem[];
  const char* code0 = code_here();
  (void)code0;
  const unsigned lds0 = (unsigned)(size_t)smem;
  int wave = __builtin_amdgcn_readfirstlane((int)threadIdx.x >> 6);
  int bid = blockIdx.x;
#define FRESH() asm volatile("" : "+s"(bid), "+s"(ws), "+s"(wave))
#define XCD (bid & 7)
#define IDX (bid >> 3)
#define MI ((XCD >> 2) * 8 + (IDX >> 2))
#define NI ((XCD & 1) * 8 + (XCD & 2) * 2 + (IDX & 3))
#define RB (XCD * 4 + (IDX >> 3))
#define CT (IDX & 7)
#define RT ((XCD >> 1) * 4 + (IDX >> 3))
#define HP ((XCD & 1) * 8 + (IDX & 7))
  MK(0);
  if (wave < 8) {
    constexpr int PER = (2 * NL_MAIN + 31) / 32;
    static_assert(PER <= 64 && 31 * PER < 2 * NL_MAIN, "one load");
    unsigned o, last, st, t;
    const char* p;
    asm volatile(
        "s_lshr_b32 %3, %5, 3\n\t"
        "s_cmp_eq_u32 %3, 31\n\ts_cselect_b32 %1, 63, 0\n\ts_cselect_b32 %2, %7, %6\n\t"
        "s_mul_i32 %0, %3, %8\n\t"
        "s_cmp_eq_u32 %4, 2\n\ts_cselect_b32 %0, %0, 0\n\ts_cselect_b32 %1, %2, %1\n\t"
        "s_mul_i32 %3, %3, 0xf300\n\ts_add_u32 %3, %3, 0x200\n\t"
        "s_cmp_eq_u32 %4, 3\n\ts_cselect_b32 %0, %3, %0\n\ts_cselect_b32 %1, 53, %1\n\ts_cselect_b32 %2, 0x480, 64\n\t"
        "s_mul_i32 %3, %5, 0x1e60\n\t"
        "s_cmp_eq_u32 %4, 5\n\ts_cselect_b32 %0, %3, %0\n\t"
        "s_add_u32 %3, %3, 0x1000\n\t"
        "s_cmp_eq_u32 %4, 4\n\ts_cselect_b32 %0, %3, %0\n\ts_cselect_b32 %1, 57, %1\n\t"
        "s_cmp_ge_u32 %4, 5\n\ts_cselect_b32 %1, 63, %1\n\t"
        "s_mul_i32 %3, %5, 0x2400\n\t"
        "s_cmp_eq_u32 %4, 7\n\ts_cselect_b32 %0, %3, %0\n\t"
        "s_add_u32 %3, %3, 0x1000\n\t"
        "s_cmp_eq_u32 %4, 6\n\ts_cselect_b32 %0, %3, %0\n\t"
        "s_add_u32 %3, %3, 0x1000\n\t"
        "s_cmp_eq_u32 %4, 1\n\ts_cselect_b32 %0, %3, %0\n\ts_cselect_b32 %1, 15, %1"
        : "=&s"(o), "=&s"(last), "=&s"(st), "=&s"(t)
        : "s"(wave), "s"(bid), "n"(PER - 1), "n"(2 * NL_MAIN - 1 - 31 * PER), "n"(PER * 64)
        : "scc");
    const unsigned l = (unsigned)lane_id();
    unsigned a = o + (l < last ? l : last) * st;
    asm volatile("" : "+v"(a));
    if (ECT) asm volatile("s_cmp_lg_u32 %1, 2\n\ts_cbranch_scc1 1f\n\ts_mov_b32 m0, %3\n\ts_nop 0\n\tglobal_load_lds_dword %0, %2\n\tv_mov_b32 %0, 0\n1:"
                          : "+v"(a)
                          : "s"(wave), "s"(code0), "s"(lds0 + (unsigned)K1R_DUMMY)
                          : "memory", "scc");
    asm volatile("s_cmp_eq_u32 %1, 2\n\ts_cselect_b64 %0, %4, %5\n\ts_bitcmp1_b32 0x38, %1\n\ts_cselect_b64 %0, %3, %0\n\t"
                 "s_bitcmp1_b32 0xc2, %1\n\ts_cselect_b64 %0, %2, %0"
                 : "=&s"(p)
                 : "s"(wave), "s"(x0), "s"(wqkv), "s"(ECT ? (const char*)wo : code0), "s"(b_sp)
                 : "scc");
    touch64(p, a, lds0 + (unsigned)K1R_DUMMY);
    MF(12);
    math_start(lds0, ws, bid, wave, (int)l);
    MF(13);
  }

  if (__builtin_expect(wave >= 8, 1)) {
    __builtin_amdgcn_s_setprio(3);
    {
      const int lane = lane_id();
      RawLd rl;
      int job, pj;
      const char* pg;
      rl.front(x0, wqkv, wo, ws, bid, lds0, wave - 8, lane, RT, HP, pj, [&]() {
        int b = bid;
        asm volatile("" : "+s"(b));
        const int xcd = b & 7, c16 = b >> 4, hp = (b & 1) * 8 + ((b >> 3) & 7);
        const bool cross = (((hp * 72) & ~15) & 127) > 48;
        const int cj = (int)((0x65743210u >> (4 * xcd)) & 7u) * 16 + c16;
        job = cross ? (cj < 88 ? 128 + cj : 999) : xcd * 16 + c16;
        pj = cross ? cj - 88 : ((xcd & 5) == 0 && (c16 & 3) == (c16 >> 2) ? 40 + xcd * 2 + (c16 >> 2) : -1);
        const int k = b >> 3;
        pg = k < 11 ? (const char*)w_in + k * 0x80000 : k < 17 ? (const char*)w_out + (k - 11) * 0x80000 : k < 22 ? (const char*)out + (k - 17) * 0x80000
             : (const char*)w_sp + (k < 30 ? (k - 22) * 0x100000 : 0x7ffff0);
      }, pg MF_PASS);
      if (job < 216) k1_raw_loader<true>(rl, w_in, w_out, ws, rl.tag, bid, lds0, wave - 8, lane, RT, HP, CT, job, pj MF_PASS);
      else k1_raw_loader<false>(rl, w_in, w_out, ws, rl.tag, bid, lds0, wave - 8, lane, RT, HP, CT, job, pj MF_PASS);
    }
    Loader ld;
    ld.lds0 = lds0;
#pragma unroll 1
    for (int layer = 0; layer < 2; ++layer) {
      FRESH();
      ld.wl = wave - 8;
      ld.lane = lane_id();
      ld.lane16 = (unsigned)ld.lane * 16u;
      const char* wip = uni_ptr(ws + OFF_PI + (size_t)NI * (18 * K3C::WB));
      if (layer) {
        ld.p1(smem, ws, bid, RT, HP MK_PASS);
        ld.pf_load<0, 21, 0, 81>(uni_ptr(ws + OFF_PO + (size_t)CT * (18 * 4608)));
        ld.pf_load<21, 9, 36, 72>(wip);
        wait_vm<9>();
        ld.pf_pin<0, 21>();
        lds_spin(lds0 + (unsigned)(LW_OFF + (LW_T + layer) * 4), 8u);
        __builtin_amdgcn_s_sleep(4);
        ld.pf_put<0, 0, 40>((unsigned)P2_W);
        wait_vm<0>();
        ld.pf_pin<21, 9>();
        bar();
        ld.pf_put<0, 40, 41>((unsigned)(P2_W + 40 * 1024));
      } else {
        ld.pf_undef<21, 9>();
        MK(3);
        MK(4);
        bar();
        ld.dma_run<81>(uni_ptr(ws + OFF_PO + (size_t)CT * (18 * 4608)), (unsigned)P2_W);
      }
      if (layer == 0 && ld.wl == 0) cnt_wait(ws, CN_O + layer, RB * 8 + CT, 16u);
      MK(6);
      asm volatile("s_waitcnt vmcnt(0) lgkmcnt(0)" ::: "memory");
      bar();
      lds_spin(lds0 + (unsigned)(LW_OFF + (LW_T + layer) * 4), 12u);
      if (layer == 0) {
        if (ld.wl == 0) {
          cnt_wait(ws, CN_PK, 8 + NI, 36u);
          lds_wr32(lds0 + (unsigned)(LW_OFF + LW_HELLO * 4), 2u);
        } else {
          lds_spin(lds0 + (unsigned)(LW_OFF + LW_HELLO * 4), 2u);
        }
        MK(7);
      }
      static_assert(2 * K3C::SB <= P2_W, "free during the first residual GEMM");
      ld.dma_run<18>(wip, (unsigned)K3C::XB);
      MX(6, 24);
      if (layer == 0 && ld.wl == 0) cnt_wait(ws, CN_PK, 24 + CT, 72u);
      wait_vm<0>();
      ld.pf_pin<21, 9>();
      MX(6, 25);
      bar();
      FRESH();
      ld.wl = wave - 8;
      ld.p35(smem, ws, w_sp, layer, bid, MI, NI, RB, CT, HP MK_PASS);
    }
  } else {
#pragma unroll 1
    for (int layer = 0; layer < 2; ++layer) {
      FRESH();
      {
        const int lane = lane_id(), r0 = RT * 128;
        float* csq = reinterpret_cast<float*>(ws + OFF_CSQ + bid * 512);
        Attn at;
        at.setup(lds0 + (unsigned)(layer ? K1_T_OFF : K1R_T_OFF), ws + OFF_O + (size_t)layer * SZ_X, r0, HP, wave, lane);
        if (layer) {
          if ((wave & 2) == 0) k1_gemm<2, 4>(smem, ws, RT, HP, (wave & 1) * 2 + (wave & 4), 0, wave >= 4, lane MK_PASS);
          else if (wave < 4) k1_gemm<3, 3>(smem, ws, RT, HP, (wave & 1) * 3, 4, false, lane MK_PASS);
          else if (wave == 6) k1_gemm<2, 3>(smem, ws, RT, HP, 6, 4, true, lane MK_PASS);
          else k1_gemm<1, 7>(smem, ws, RT, HP, 8, 0, true, lane MK_PASS);
        } else {
          if (wave == 0) k1_gemm_raw<3, false>(smem, HP, 0, lane MF_PASS);
          else if (wave < 4) k1_gemm_raw<2, false>(smem, HP, 1 + 2 * wave, lane MF_PASS);
          else if (wave < 7) k1_gemm_raw<2, true>(smem, HP, 2 * (wave - 4), lane MF_PASS);
          else k1_gemm_raw<3, true>(smem, HP, 6, lane MF_PASS);
        }
        msync(lds0 + (unsigned)(LW_OFF + (LW_T + layer) * 4));
        MK(3);
        at.run(r0, HP, wave, lane);
        if (layer == 0) {
          if (wave == 0 && lane < 32)
            asm volatile("global_store_dwordx4 %0, %1, %2 sc1\n\ts_nop 1" ::"v"((unsigned)lane * 16u),
                         "v"(*reinterpret_cast<const u32x4*>(smem + K1R_CS_OFF + lane * 16)), "s"(uni_ptr((const char*)csq))
                         : "memory");
          lds_spin(lds0 + (unsigned)(LW_OFF + LW_HELLO * 4), 1u);
        }
        MK(4);
        announce(lds0 + (unsigned)(LW_OFF + (LW_O + layer * 22 + (wave >> 2)) * 4), 4u, ws, CN_O + layer, (RT * 2 + (wave >> 2)) * 8 + lane,
                 lane < 8);
        MK(5);
        bar();
      }
      FRESH();
      {
        if (wave < 4) {
          const int lane = lane_id();
          u32x2 xv[5];
          ResIn ri;
          ri.setup(layer ? ws + OFF_X1 + SZ_X : (const char*)x0, layer == 0, RB, CT, wave, lane);
          ri.load(xv);
          ResOut ro;
          ro.setup(ws + OFF_X1 + (size_t)layer * 2 * SZ_X, ws + OFF_XS + (size_t)layer * 2 * SZ_XS, out, false, RB, CT, wave, lane);
          f32x4 acc[2][5];
          p2_gemm(acc, xv, lds0, uni_ptr(ws + OFF_O + (size_t)layer * SZ_X + (size_t)RB * (72 * 1024)), layer, wave, lane MK_PASS);
          MK(7);
          ro.run(acc[0], false);
          MK(8);
          announce(lds0 + (unsigned)(LW_OFF + (LW_X1 + layer) * 4), 4u, ws, CN_X1 + layer, (RB >> 1) * 16 + lane, lane < 16);
          MK(9);
          bar();
          FRESH();
          if (wave == 3) cnt_wait(ws, CN_X1 + layer, MI * 16 + NI, 16u);
          bar();
          MK(10);
          p3_g0(smem, ws, layer, MI, NI, wave, lane_id() MK_PASS);
        } else {
          if (layer && wave == 5) cnt_wait(ws, CN_O + layer, RB * 8 + CT, 16u);
          bar();
          k3_const(smem, ws, b_sp, layer, bid, MI, NI, wave & 3);
          MX(6, 26);
          bar();
          MK(10);
          FRESH();
          p3_g1(smem, ws, layer, MI, NI, wave & 3, lane_id() MK_PASS);
        }
      }
      FRESH();
      {
        const int lane = lane_id();
        if (wave < 4) {
          u32x2 xv[5];
          ResIn ri;
          ri.setup(ws + OFF_X1 + (size_t)layer * 2 * SZ_X, false, RB, CT, wave, lane);
          f32x4 acc[2][5];
          ResOut ro;
          ro.setup(ws + OFF_X1 + SZ_X, ws + OFF_XS + SZ_XS, out, layer != 0, RB, CT, wave, lane);
          ResX<36, 33, 8, P5_W, P5_XW, P5_X0, 3, P5_BAR> gm(acc);
          gm.setup(lds0, uni_ptr(ws + OFF_H + (size_t)layer * SZ_Z + (size_t)RB * (288 * 1024)), wave, lane, (unsigned)P5_RED, layer + 1u);
          announce(lds0 + (unsigned)(LW_OFF + (LW_H + layer * 2 + (wave >> 1)) * 4), 2u, ws, ((NI >= 8) ? CN_H2 : CN_H) + layer,
                   (MI * 2 + (wave >> 1)) * 8 + lane, lane < 8);
          MK(19);
          bar();
          ri.load(xv);
          gm.prow();
          place();
          bar();
          MK(20);
          gm.prox<0>(std::make_integer_sequence<int, P5_X0>{});
          gm.init<2 * P5_X0>(xv);
          __builtin_amdgcn_sched_barrier(0);
          gm.run();
          MK(21);
          ro.run(acc[0], layer != 0);
          MK(22);
          if (layer == 0) {
            announce(lds0 + (unsigned)(LW_OFF + LW_X2 * 4), 4u, ws, CN_X2, (RB >> 1) * 16 + lane,
                     lane < 16 || (lane < 32 && (RB & 1) && RB < 31));
            MK(23);
          }
        } else {
          bar();
          bar();
#pragma unroll
          for (int i = 0; i < 7; ++i) {
            if (i == 1 && wave == 5) {
              unsigned f;
              const char* cp = uni_ptr(ws + OFF_CNT + (size_t)((CN_H2 + layer) * 256 + RB * 8 + CT) * 128);
              asm volatile("global_load_dword %0, %1, %2 sc1" : "=v"(f) : "v"(vconst(0u)), "s"(cp) : "memory");
              bar();
              asm volatile("s_waitcnt vmcnt(0)" : "+v"(f) : : "memory");
              if (__builtin_amdgcn_readfirstlane(f) != 7u) cnt_wait(ws, CN_H2 + layer, RB * 8 + CT, 8u);
              continue;
            }
            asm volatile("s_waitcnt lgkmcnt(0)" ::: "memory");
            bar();
          }
          MK(22);
        }
        if (layer == 0 && wave < 4) bar();
      }
    }
  }
}

}

extern "C" void kernel_launch(void* const* d_in, const int* in_sizes, int n_in, void* d_out, int out_size,
                              void* d_ws, size_t ws_size, hipStream_t stream) {
  const u16* x0 = (const u16*)d_in[0];
  const u16* wqkv = (const u16*)d_in[1];
  const u16* wo = (const u16*)d_in[2];
  const u16* w_in = (const u16*)d_in[4];
  const u16* w_sp = (const u16*)d_in[5];
  const u16* b_sp = (const u16*)d_in[6];
  const u16* w_out = (const u16*)d_in[7];
  char* ws = (char*)d_ws;
  (void)hipFuncSetAttribute((const void*)k_main, hipFuncAttributeMaxDynamicSharedMemorySize, LDS_ALL);
  k_main<<<256, 768, LDS_ALL, stream>>>(x0, wqkv, wo, w_in, w_sp, b_sp, w_out, (u16*)d_out, ws);
}
```

```cpp
#include <hip/hip_runtime.h>
#include <hip/hip_bf16.h>
#include <stdint.h>

#define DEVI __device__ __forceinline__

typedef unsigned short u16;
typedef __attribute__((ext_vector_type(8))) short s16x8;
typedef __attribute__((ext_vector_type(4))) short s16x4;
typedef __attribute__((ext_vector_type(4))) float f32x4;
typedef __attribute__((ext_vector_type(2))) float f32x2;
typedef __attribute__((ext_vector_type(2))) __bf16 bf16x2;
typedef __attribute__((ext_vector_type(2))) unsigned u32x2;
typedef __attribute__((ext_vector_type(4))) unsigned u32x4;

#define MK(I) ((void)0)
#define MX(S, I) ((void)0)
#define MF(I) ((void)0)
#define MF_ARG
#define MF_PASS
#define MK_ARG
#define MK_PASS

#define SPIN_MEM (1u << 22)
#define SAFE_OUT(L)
#define SAFE_AND
#define NOP_(n) "=s"(n)

namespace {

constexpr int S_ = 2048, D_ = 576, F_ = 4608, FH_ = 2304;
constexpr int NQKV = 3 * D_;

constexpr size_t SZ_X = (size_t)S_ * D_ * 2;
constexpr size_t SZ_Z = (size_t)S_ * FH_ * 2;
constexpr size_t OFF_X1 = 0;
constexpr size_t OFF_O = OFF_X1 + 3 * SZ_X;
constexpr size_t OFF_Z2 = OFF_O + 2 * SZ_X;
constexpr size_t OFF_H = OFF_Z2 + 2 * SZ_Z;
constexpr size_t SZ_XS = (size_t)S_ * 8 * 8;
constexpr size_t OFF_XS = OFF_H + 2 * SZ_Z;
constexpr size_t OFF_CSQ = OFF_XS + 3 * SZ_XS;
constexpr size_t OFF_ZP = OFF_CSQ + 256 * 512;
constexpr size_t SZ_ZP = (size_t)16 * 16 * 1024;
constexpr size_t OFF_PQ = OFF_ZP + 2 * SZ_ZP;
constexpr size_t OFF_PO = OFF_PQ + (size_t)16 * 9 * 64 * 112 * 2;
constexpr size_t OFF_PI = OFF_PO + (size_t)D_ * D_ * 2;
constexpr size_t OFF_PW = OFF_PI + (size_t)32 * 9 * 64 * 144 * 2;
constexpr size_t OFF_CSP = OFF_PW + (size_t)FH_ * D_ * 2;
constexpr size_t OFF_CNT = OFF_CSP + (size_t)18 * F_ * 4;
constexpr int CN_X1 = 0, CN_H = 2, CN_X2 = 4, CN_O = 5, CN_Z = 7, CN_PK = 9, CN_H2 = 10, CN_Z2 = 12, NCNT = 14;
constexpr size_t OFF_TAG = OFF_CNT + (size_t)NCNT * 256 * 128;
constexpr size_t OFF_HELLO = OFF_TAG + 128;
constexpr size_t OFF_POT = OFF_HELLO + 2048;
constexpr size_t OFF_CSK = OFF_POT + 1024;
constexpr size_t WS_END = OFF_CSK + 256 * 2048;
static_assert(WS_END <= (size_t)64 << 20, "workspace");

DEVI float bf_lo(unsigned u) { return __uint_as_float(u << 16); }
DEVI float bf_hi(unsigned u) { return __uint_as_float(u & 0xffff0000u); }
DEVI unsigned pack2(float a, float b) {
  f32x2 v = {a, b};
  bf16x2 r = __builtin_convertvector(v, bf16x2);
  return __builtin_bit_cast(unsigned, r);
}
DEVI float rbf(float a) {
  return bf_lo(pack2(a, 0.f));
}

DEVI float sum_g(float x) {
  const unsigned u = __float_as_uint(x);
  const auto p = __builtin_amdgcn_permlane32_swap(u, u, false, false);
  const unsigned v = __float_as_uint(__uint_as_float(p[0]) + __uint_as_float(p[1]));
  const auto q = __builtin_amdgcn_permlane16_swap(v, v, false, false);
  return __uint_as_float(q[0]) + __uint_as_float(q[1]);
}

DEVI unsigned vconst(unsigned c) {
  unsigned r;
  asm volatile("v_mov_b32 %0, %1" : "=v"(r) : "s"(c));
  return r;
}
DEVI void place() {
  __builtin_amdgcn_sched_barrier(0);
  asm volatile(".p2align 6");
  __builtin_amdgcn_sched_barrier(0);
}
DEVI const char* uni_ptr(const char* p) {
  unsigned long long v = (unsigned long long)p;
  unsigned lo = __builtin_amdgcn_readfirstlane((unsigned)v), hi = __builtin_amdgcn_readfirstlane((unsigned)(v >> 32));
  asm("s_nop 4" : "+s"(lo), "+s"(hi));
  return (const char*)(((unsigned long long)hi << 32) | lo);
}

DEVI void dma16(const char* sbase, unsigned voff, unsigned m0val) {
  asm volatile("s_mov_b32 m0, %2\n\ts_nop 0\n\tglobal_load_lds_dwordx4 %0, %1"
               :
               : "v"(voff), "s"(sbase), "s"(m0val)
               : "memory");
}

DEVI void ln_finish(float s, float q, float invn, float& mb, float& rinv) {
  float mean = s * invn;
  asm("" : "+v"(mean));
  float var = fmaxf(__builtin_fmaf(-mean, mean, q * invn), 0.f);
  mb = rbf(mean);
  float v = rbf(var);
  float t = rbf(v + 1e-5f);
  float sd = rbf(__builtin_amdgcn_sqrtf(t));
  rinv = __builtin_amdgcn_rcpf(sd);
}

DEVI unsigned pack2v(f32x2 v) { return pack2(v[0], v[1]); }

template <int BM_, int BN_>
struct GemmCfg {
  static constexpr int BM = BM_, BN = BN_, NS = 4;
  static constexpr int P = BN * 2;
  static constexpr int XB = BM * 128, WB = 64 * P, SB = XB + WB;
  static constexpr int LDS_BYTES = NS * SB;
};

DEVI int swap23(int r) { return (r & ~12) | ((r & 4) << 1) | ((r & 8) >> 1); }

DEVI void lds_wr32(unsigned addr, unsigned v) { asm volatile("ds_write_b32 %0, %1" ::"v"(addr), "v"(v) : "memory"); }

template <class C, int MT, int NT, int D, int R, bool XCM, int WT, bool SQ = false, unsigned long long WS = 0>
struct GemmMath {
  static constexpr int WO = WT ? 1 : 2;
  static constexpr int NS = C::NS, SB = C::SB, P = C::P, NU = 2 * NT;
  static constexpr int XPU = MT <= 2 ? MT : MT - 2;
  static constexpr int XU = (MT + XPU - 1) / XPU;
  static_assert(NU % R == 0 && R > D && D <= NT && XU <= D, "shape");
  s16x4 wlo[R], whi[R];
  s16x8 wf[R];
  s16x8 xf[2][MT];
  unsigned xrd[2], wrd, xa[2], wa, so;
  f32x4 (&acc)[MT][NT];
  f32x4 accq[SQ ? MT : 1];
  unsigned long long wtab = WS;
  DEVI GemmMath(f32x4 (&a)[MT][NT]) : acc(a) {}

  template <int PU>
  DEVI void wread_lo() {
    constexpr int off = (PU / NT) * 32 * P + (PU % NT) * (WT == 2 ? 1024 : 32);
    if constexpr (WT) asm volatile("ds_read_b128 %0, %1 offset:%2" : "=v"(wf[PU % R]) : "v"(wa), "n"(off));
    else asm volatile("ds_read_b64_tr_b16 %0, %1 offset:%2" : "=v"(wlo[PU % R]) : "v"(wa), "n"(off));
  }
  template <int PU>
  DEVI void wread_hi() {
    if constexpr (!WT)
      asm volatile("ds_read_b64_tr_b16 %0, %1 offset:%2" : "=v"(whi[PU % R]) : "v"(wa), "n"((PU / NT) * 32 * P + (PU % NT) * 32 + 8 * P));
  }
  template <int H, int M>
  DEVI void xread1() {
    asm volatile("ds_read_b128 %0, %1 offset:%2" : "=v"(xf[H][M]) : "v"(xa[H]), "n"(M * (XCM ? 256 : 2048)));
  }
  static constexpr int xops(int v) {
    const int k = (v + D) % NT;
    if (k >= XU) return 0;
    const int n = MT - k * XPU;
    return n < XPU ? n : XPU;
  }
  static constexpr int ops(int v, bool last) { return (last && v + D >= NU) ? 0 : WO + xops(v); }
  static constexpr int newer(int U, bool last) {
    int n = 0;
    int v0 = U - D;
    if (U % NT == 0) v0 += XU - 1;
    else n += v0 >= 0 ? xops(v0) : xops(v0 + NU);
    for (int v = v0 + 1; v < U; ++v) n += v >= 0 ? ops(v, last) : ops(v + NU, false);
    return n;
  }
  template <int U, int M_>
  DEVI void mf() {
    constexpr int M = ((U % NT) & 1) ? MT - 1 - M_ : M_;
    s16x8 w;
    if constexpr (WT) w = wf[U % R];
    else w = __builtin_shufflevector(wlo[U % R], whi[U % R], 0, 1, 2, 3, 4, 5, 6, 7);
    acc[M][U % NT] = __builtin_amdgcn_mfma_f32_16x16x32_bf16(w, xf[U / NT][M], acc[M][U % NT], 0, 0, 0);
    __builtin_amdgcn_sched_barrier(0);
  }
  DEVI void pin() {
#pragma unroll
    for (int m = 0; m < MT; ++m)
#pragma unroll
      for (int n = 0; n < NT; ++n) asm volatile("" : "+v"(acc[m][n]));
  }
  template <int U, int I>
  DEVI void dsop() {
    constexpr int PU = (U + D) % NU;
    if constexpr (I == 0) wread_lo<PU>();
    else if constexpr (I == 1) wread_hi<PU>();
    else if constexpr (I - 2 < xops(U)) {
      constexpr int k = (U + D) % NT;
      xread1<((U + D - k) % NU) / NT, k * XPU + I - 2>();
    }
  }
  template <int U, bool LAST, bool BAR, int M>
  DEVI void slot() {
    constexpr bool PF = !(LAST && U + D >= NU);
    mf<U, M>();
    if constexpr (M == 0 && PF && U + D == NU) {
      if constexpr (BAR) __builtin_amdgcn_s_barrier();
      so = (so == (unsigned)((NS - 1) * SB)) ? 0u : so + (unsigned)SB;
      wa = wrd + (WS ? ((unsigned)wtab & 63u) << 11 : so);
      wtab >>= 6;
      xa[0] = xrd[0] + so;
      xa[1] = xrd[1] + so;
      __builtin_amdgcn_sched_barrier(0);
    }
    if constexpr (PF) {
      dsop<U, M>();
      if constexpr (M == MT - 1) {
        if constexpr (MT <= 1) dsop<U, 1>();
        if constexpr (MT <= 2) {
          dsop<U, 2>();
          dsop<U, 3>();
        }
      }
      __builtin_amdgcn_sched_barrier(0);
    }
  }
  template <int U, bool LAST, bool BAR, int... M>
  DEVI void unit(std::integer_sequence<int, M...>) {
    static_assert(newer(U, LAST) <= 15, "lgkmcnt range");
    asm volatile("s_waitcnt lgkmcnt(%0)" ::"n"(newer(U, LAST)));
    __builtin_amdgcn_sched_barrier(0);
    (slot<U, LAST, BAR, M>(), ...);
    if constexpr (SQ && (U % NT) < MT) {
      constexpr int m = U % NT;
      accq[m] = __builtin_amdgcn_mfma_f32_16x16x32_bf16(xf[U / NT][m], xf[U / NT][m], accq[m], 0, 0, 0);
      __builtin_amdgcn_sched_barrier(0);
    }
    __builtin_amdgcn_sched_barrier(0);
  }
  template <bool LAST, bool BAR, int... U>
  DEVI void stage(std::integer_sequence<int, U...>) {
    (unit<U, LAST, BAR>(std::make_integer_sequence<int, MT>{}), ...);
  }
  template <int... U>
  DEVI void prow(std::integer_sequence<int, U...>) {
    ((wread_lo<U>(), wread_hi<U>()), ...);
  }
  template <int... M>
  DEVI void prox(std::integer_sequence<int, M...>) {
    (xread1<0, M>(), ...);
  }
  DEVI void setup(unsigned lds0, int mt0, int nt0, int lane, unsigned so0) {
    const int g = lane >> 4, r = lane & 15, q = (lane >> 2) & 3, p = lane & 3;
#pragma unroll
    for (int h = 0; h < 2; ++h)
      xrd[h] = XCM ? lds0 + (unsigned)((4 * h + g) * (C::BM * 16) + (mt0 * 16 + r) * 16)
                   : lds0 + (unsigned)((mt0 * 16 + r) * 128 + (((4 * h + g) ^ ((r >> 1) & 7)) * 16));
    wrd = WT == 2   ? lds0 + (unsigned)(C::XB + nt0 * 1024 + lane * 16)
          : WT == 1 ? lds0 + (unsigned)(C::XB + (g * 8 + (r & 7)) * P + (r >> 3) * 16 + nt0 * 32)
                    : lds0 + (unsigned)(C::XB + (16 * (g >> 1) + 4 * (g & 1) + q) * P + 8 * p + nt0 * 32);
    so = so0;
    xa[0] = xrd[0] + so;
    xa[1] = xrd[1] + so;
    wa = wrd + so;
  }
  DEVI void first() {
    prox(std::make_integer_sequence<int, MT>{});
    prow(std::make_integer_sequence<int, D>{});
    asm volatile("s_waitcnt lgkmcnt(0)");
  }
  template <int N, bool BAR = true>
  DEVI void mid() {
    if constexpr (N > 1) place();
#pragma unroll 1
    for (int s = 0; s < N; ++s) stage<false, BAR>(std::make_integer_sequence<int, NU>{});
  }
  DEVI void last() {
    stage<true, true>(std::make_integer_sequence<int, NU>{});
    pin();
  }
  template <int KS>
  DEVI void run(unsigned lds0, int mt0, int nt0, int lane, unsigned so0) {
    setup(lds0, mt0, nt0, lane, so0);
    __builtin_amdgcn_s_barrier();
    first();
    mid<KS - 1>();
    last();
  }
};

DEVI const char* code_here() {
  unsigned long long pc;
  asm volatile("s_getpc_b64 %0" : "=s"(pc));
  return (const char*)pc;
}
DEVI int okmap(int p) {
  const int ch = p >> 3, e = p & 7, c = (ch - 8) & 3;
  return ch < 8 ? (2 * ch + (e >> 2)) * 36 + 16 + (e & 3) : ((ch - 8) >> 2) * 36 + c * 8 + (c >> 1) * 4 + e;
}

DEVI void pk_st16(void* p, u32x4 v) {
  asm volatile("global_store_dwordx4 %0, %1, off sc1\n\ts_nop 1" ::"v"(p), "v"(v) : "memory");
}

using K1C = GemmCfg<144, 112>;
constexpr int K1_TP = 240;
constexpr int K1_T_OFF = K1C::SB;
constexpr int K1_ST_OFF = 163840 - 128 - 1152;
constexpr int K1_CS_OFF = 4 * K1C::SB;
constexpr int K1_WX = 4 * K1C::SB + 2048;
constexpr unsigned long long K1_WS = 16ull | 32ull << 6 | 48ull << 12 | 56ull << 18 | 63ull << 24 | 16ull << 30 | 32ull << 36 | 48ull << 42;
static_assert(K1C::SB == 16 * 2048 && K1_WX - K1C::XB == 56 * 2048 && K1C::WB == 7 * 2048 && K1_WX + 2 * K1C::WB <= K1_ST_OFF, "K1 w places");
static_assert(K1_T_OFF + 144 * K1_TP + 64 <= 2 * K1C::SB + K1C::XB, "K1 LDS alias");
using K1R = GemmCfg<144, 144>;
constexpr int K1R_T_OFF = K1R::SB;
constexpr int K1R_CS_OFF = K1R_T_OFF + 144 * K1_TP;
constexpr int K1R_ST_OFF = K1R_CS_OFF + 512;
constexpr int K1R_DUMMY = K1R::LDS_BYTES;
constexpr int K1R_PO = K1R_DUMMY + 1024;
constexpr int K1R_CNT = 163840 - 128 + 28 * 4;
#define SPIN_ASM(TEST)                                                                                                   \
  if constexpr (SL <= 1)                                                                                                 \
    asm volatile(".La%=:\n\tds_read_b32 %0, %3\n\ts_waitcnt lgkmcnt(0)\n\tv_readfirstlane_b32 %2, %0\n\t" TEST SAFE_AND \
                 "s_cbranch_scc1 .La%="                                                                                  \
                 : "=&v"(v), NOP_(c), "=&s"(t)                                                                           \
                 : "v"(addr), "s"(k)                                                                                     \
                 : "memory", "scc");                                                                                     \
  else                                                                                                                   \
    asm volatile("s_branch .Lb%=\n.La%=:\n\ts_sleep %5\n.Lb%=:\n\tds_read_b32 %0, %3\n\ts_waitcnt lgkmcnt(0)\n\t"     \
                 "v_readfirstlane_b32 %2, %0\n\t" TEST SAFE_AND "s_cbranch_scc1 .La%="                                  \
                 : "=&v"(v), NOP_(c), "=&s"(t)                                                                           \
                 : "v"(addr), "s"(k), "n"(SL)                                                                            \
                 : "memory", "scc")
template <int SL>
DEVI void spin_ge(unsigned addr, unsigned k) {
  unsigned v, c = 1u << 18, t;
  SPIN_ASM("s_cmp_lt_u32 %2, %4\n\t");
}
template <int SL>
DEVI void spin_b4(unsigned addr, unsigned n) {
  unsigned v, c = 1u << 18, t;
  const unsigned k = (0x80u - n) * 0x01010101u;
  SPIN_ASM("s_add_u32 %2, %2, %4\n\ts_andn2_b32 %2, 0x80808080, %2\n\t");
}
DEVI void msync(unsigned addr) {
  asm volatile("s_waitcnt lgkmcnt(0)" ::: "memory");
  asm volatile("s_mov_b64 exec, 1\n\tds_add_u32 %0, %1\n\ts_mov_b64 exec, -1" ::"v"(addr), "v"(vconst(1u)) : "memory");
  spin_ge<0>(addr, 8u);
}
static_assert(K1R_PO + 13824 + 256 <= 163840 - 128 && K1R_ST_OFF + 144 * 8 <= 3 * K1R::SB, "K1R LDS");

DEVI void touch64(const char* sbase, unsigned voff, unsigned m0val) {
  asm volatile("s_mov_b32 m0, %2\n\ts_nop 0\n\tglobal_load_lds_dword %0, %1" : : "v"(voff), "s"(sbase), "s"(m0val) : "memory");
}

template <int MT, bool G1>
DEVI void k1_gemm_raw(char* smem, int hp, int mt0, int lane MF_ARG) {
  constexpr int NT = 4, nt0 = G1 ? 4 : 0;
  const unsigned lds0 = (unsigned)(size_t)smem;
  const int g = lane >> 4, r = lane & 15;
  unsigned ta[NT];
#pragma unroll
  for (int nt = 0; nt < NT; ++nt) {
    const int j = (nt0 + nt) * 16 + g * 4, seg = j / 40, cj = j - seg * 40 - (hp & 1) * 4;
    const int to = (seg < 3 && cj >= 0 && cj < 36) ? seg * 80 + cj * 2 + (cj >= 20 ? 8 : 0) : (seg < 3 ? 40 : 120);
    ta[nt] = lds0 + (unsigned)(K1R_T_OFF + (mt0 * 16 + r) * K1_TP + to);
    asm volatile("" : "+v"(ta[nt]));
  }
  unsigned sa = lds0 + (unsigned)(K1R_ST_OFF + (mt0 * 16 + r) * 8), ca = lds0 + (unsigned)(K1R_CS_OFF + (nt0 * 16 + g * 4) * 4);
  asm volatile("" : "+v"(sa), "+v"(ca));
  f32x4 acc[MT][NT];
#pragma unroll
  for (int mt = 0; mt < MT; ++mt)
#pragma unroll
    for (int nt = 0; nt < NT; ++nt) acc[mt][nt] = f32x4{0.f, 0.f, 0.f, 0.f};
  {
    GemmMath<K1R, MT, 4, 3, 4, false, 0, G1> gm(acc);
    if constexpr (G1) {
#pragma unroll
      for (int mt = 0; mt < MT; ++mt) gm.accq[mt] = f32x4{0.f, 0.f, 0.f, 0.f};
    }
    MF(14);
    if constexpr (!G1) {
      asm volatile("s_waitcnt vmcnt(0)" ::: "memory");
    }
    MF(15);
    gm.template run<9>(lds0, mt0, nt0, lane, 0u);
    MF(2);
    if constexpr (G1) {
#pragma unroll
      for (int mt = 0; mt < MT; ++mt) {
        const f32x4 q = gm.accq[mt];
        const float d = (r & 2) ? ((r & 1) ? q[3] : q[2]) : ((r & 1) ? q[1] : q[0]);
        const float qq = sum_g(g == (r >> 2) ? d : 0.f);
        float mb, rinv;
        ln_finish(acc[mt][3][0], qq, 1.0f / (float)D_, mb, rinv);
        const f32x2 st = {-mb, rinv};
        if (g == 2) asm volatile("ds_write_b64 %0, %1 offset:%2" ::"v"(sa), "v"(st), "n"(mt * 128) : "memory");
      }
    }
  }
  if (mt0 == 0 && r == 0) {
#pragma unroll
    for (int nt = 0; nt < NT; ++nt) *reinterpret_cast<f32x4*>(smem + K1R_CS_OFF + ((nt0 + nt) * 16 + g * 4) * 4) = acc[0][nt];
  }
  msync(lds0 + (unsigned)K1R_CNT);
  MF(3);
  f32x2 st[MT];
  f32x4 cs[NT];
#pragma unroll
  for (int nt = 0; nt < NT; ++nt) asm volatile("ds_read_b128 %0, %1 offset:%2" : "=v"(cs[nt]) : "v"(ca), "n"(nt * 64) : "memory");
#pragma unroll
  for (int mt = 0; mt < MT; ++mt) asm volatile("ds_read_b64 %0, %1 offset:%2" : "=v"(st[mt]) : "v"(sa), "n"(mt * 128) : "memory");
#pragma unroll
  for (int i = 0; i < MT; ++i) asm volatile("s_waitcnt lgkmcnt(0)" : "+v"(st[i]));
#pragma unroll
  for (int i = 0; i < NT; ++i) asm volatile("" : "+v"(cs[i]));
#pragma unroll
  for (int mt = 0; mt < MT; ++mt) {
    f32x2 M = {st[mt][0], st[mt][0]}, R = {st[mt][1], st[mt][1]};
    asm volatile("" : "+v"(M), "+v"(R));
#pragma unroll
    for (int nt = 0; nt < NT; ++nt) {
      const f32x2 v0 = R * __builtin_elementwise_fma(M, f32x2{cs[nt][0], cs[nt][1]}, f32x2{acc[mt][nt][0], acc[mt][nt][1]});
      const f32x2 v1 = R * __builtin_elementwise_fma(M, f32x2{cs[nt][2], cs[nt][3]}, f32x2{acc[mt][nt][2], acc[mt][nt][3]});
      acc[mt][nt][0] = __uint_as_float(pack2v(v0));
      acc[mt][nt][1] = __uint_as_float(pack2v(v1));
      const f32x2 pk = {acc[mt][nt][0], acc[mt][nt][1]};
      asm volatile("ds_write_b64 %0, %1 offset:%2" ::"v"(ta[nt]), "v"(pk), "n"(mt * 16 * K1_TP) : "memory");
    }
  }
}

template <int N>
DEVI void wait_vm() {
  asm volatile("s_waitcnt vmcnt(%0)" ::"n"(N) : "memory");
}

constexpr int LDS_ALL = 163840;
constexpr int LW_OFF = LDS_ALL - 128;
constexpr int LW_GO = 0, LW_T = 2, LW_O = 4  , LW_X1 = 6, LW_H = 8, LW_X2 = 12, LW_PK = 13, LW_Z = 14, LW_PAIR = 16, LW_AD = 20,
              LW_L = 22, LW_S = 24, LW_ST = 29, LW_HELLO = 30, LW_Q = 31;
DEVI void announce(unsigned addr, unsigned n, char* ws, int kind, int who, bool act) {
  const char* cb = uni_ptr(ws + OFF_CNT + (size_t)kind * 256 * 128);
  const unsigned long long mask = __ballot(act);
  unsigned t, u;
  asm volatile(
      "s_waitcnt vmcnt(0)\n\t"
      "s_mov_b64 exec, 1\n\t"
      "ds_add_rtn_u32 %0, %2, %3\n\t"
      "s_waitcnt lgkmcnt(0)\n\t"
      "v_readfirstlane_b32 %1, %0\n\t"
      "s_cmp_eq_u32 %1, %4\n\t"
      "s_cselect_b64 exec, %5, 0\n\t"
      "global_atomic_add %6, %3, %7 sc1\n\t"
      "s_mov_b64 exec, -1\n\t"
      "s_nop 0"
      : "=&v"(t), "=&s"(u)
      : "v"(addr), "v"(vconst(1u)), "s"(n - 1u), "s"(mask), "v"((unsigned)(who & 255) * 128u), "s"(cb)
      : "memory", "scc");
}
DEVI void cnt_wait(const char* ws, int kind, int who, unsigned n) {
  const char* cp = uni_ptr(ws + OFF_CNT + (size_t)(kind * 256 + who) * 128);
  unsigned v, c = SPIN_MEM, t;
  asm volatile(
      ".La%=:\n\tglobal_load_dword %0, %3, %4 sc1\n\ts_sub_u32 %1, %1, 1\n\ts_waitcnt vmcnt(0)\n\tv_readfirstlane_b32 %2, %0\n\ts_cmp_lg_u32 %2, %5\n\t"
      "s_cselect_b32 %2, %1, 0\n\ts_cmp_lg_u32 %2, 0\n\ts_cbranch_scc1 .La%="
      : "=&v"(v), "+s"(c), "=&s"(t)
      : "v"(vconst(0u)), "s"(cp), "s"(n - 1u)
      : "memory", "scc");
}
DEVI void dma16_sc1(const char* sbase, unsigned voff, unsigned m0val) {
  asm volatile("s_mov_b32 m0, %2\n\ts_nop 0\n\tglobal_load_lds_dwordx4 %0, %1 sc1"
               :
               : "v"(voff), "s"(sbase), "s"(m0val)
               : "memory");
}
template <int N, bool SC1>
DEVI void dma4(const char* sbase, unsigned v0, unsigned v1, unsigned v2, unsigned v3, unsigned m0val) {
#define DMA4(S)                                                                                                               \
  asm volatile("s_mov_b32 m0, %5\n\ts_nop 0\n\tglobal_load_lds_dwordx4 %0, %4" S "\n\t.if %6 > 1\n\tglobal_load_lds_dwordx4 %1, %4 offset:1024" S \
               "\n\t.endif\n\t.if %6 > 2\n\tglobal_load_lds_dwordx4 %2, %4 offset:2048" S                                    \
               "\n\t.endif\n\t.if %6 > 3\n\tglobal_load_lds_dwordx4 %3, %4 offset:3072" S "\n\t.endif"                      \
               :                                                                                                              \
               : "v"(v0), "v"(v1), "v"(v2), "v"(v3), "s"(sbase), "s"(m0val), "n"(N)                                           \
               : "memory")
  if constexpr (SC1) DMA4(" sc1");
  else DMA4("");
}
template <int N>
DEVI void dma_seq(const char* src, unsigned voff, unsigned m0val) {
  if constexpr (N > 0) {
    dma4<(N < 4 ? N : 4), true>(src, voff, voff, voff, voff, m0val);
    dma_seq<N - 4>(src + 4096, voff, m0val + 4096u);
  }
}
DEVI void bar() { __builtin_amdgcn_s_barrier(); }
DEVI void lds_inc(unsigned addr, unsigned by = 1u) {
  asm volatile("s_mov_b64 exec, 1\n\tds_add_u32 %0, %1\n\ts_mov_b64 exec, -1" ::"v"(addr), "v"(vconst(by)) : "memory");
}
DEVI void lds_spin(unsigned addr, unsigned n) {
  spin_ge<1>(addr, n);
}
template <int SL = 1>
DEVI void lds_spin4(unsigned addr, unsigned n) {
  spin_b4<SL>(addr, n);
}
DEVI int lane_id() {
  int l;
  asm volatile("v_mbcnt_lo_u32_b32 %0, -1, 0\n\tv_mbcnt_hi_u32_b32 %0, -1, %0" : "=v"(l));
  __builtin_assume((unsigned)l < 64u);
  return l;
}

template <int I>
DEVI u32x4 tr8(const u32x4 (&v)[8]) {
  constexpr unsigned sel = (I & 1) ? 0x07060302u : 0x05040100u;
  return u32x4{__builtin_amdgcn_perm(v[1][I / 2], v[0][I / 2], sel), __builtin_amdgcn_perm(v[3][I / 2], v[2][I / 2], sel),
               __builtin_amdgcn_perm(v[5][I / 2], v[4][I / 2], sel), __builtin_amdgcn_perm(v[7][I / 2], v[6][I / 2], sel)};
}
template <int PITCH, bool SUM, int... I>
DEVI void tr8_store(const u32x4 (&v)[8], unsigned dst, float (&cs)[8], std::integer_sequence<int, I...>) {
  auto one = [&](auto I_) {
    const u32x4 o = tr8<I_>(v);
    const unsigned d_ = dst;
    asm volatile("ds_write_b128 %0, %1 offset:%2" ::"v"(d_), "v"(o), "n"(I_ * PITCH) : "memory");
    if constexpr (SUM) {
#pragma unroll
      for (int d = 0; d < 4; ++d) {
        const unsigned od = o[d];
        cs[I_] = __builtin_amdgcn_fdot2_f32_bf16(__builtin_bit_cast(bf16x2, od), __builtin_bit_cast(bf16x2, 0x3f803f80u), cs[I_], false);
      }
    }
  };
  (one(std::integral_constant<int, I>{}), ...);
}
template <int NB>
DEVI void lds_out(unsigned src, char* dst, int lane) {
  constexpr int N = (NB + 1023) / 1024;
  u32x4 t[N];
  const unsigned a = src + (unsigned)lane * 16u;
#pragma unroll
  for (int i = 0; i < N; ++i) asm volatile("ds_read_b128 %0, %1 offset:%2" : "=v"(t[i]) : "v"(a), "n"(i * 1024) : "memory");
  const char* d = uni_ptr(dst);
#pragma unroll
  for (int i = 0; i < N; ++i) {
    asm volatile("s_waitcnt lgkmcnt(%1)" : "+v"(t[i]) : "n"(N - 1 - i) : "memory");
    if (i < N - 1 || lane < (NB - (N - 1) * 1024) / 16)
      asm volatile("global_store_dwordx4 %0, %1, %2 sc1\n\ts_nop 1" ::"v"((unsigned)lane * 16u), "v"(t[i]), "s"(d + i * 1024) : "memory");
  }
}
template <int A, int B, class F>
DEVI void static_for(F f) {
  if constexpr (A < B) {
    f(std::integral_constant<int, A>{});
    static_for<A + 1, B>(f);
  }
}
constexpr int WSP_V = 3;
constexpr int RAW_S1 = 6;
constexpr int RAW_S0 = 4;
template <int E>
struct RawX {
  static constexpr int first(int s) {
    return s <= RAW_S0 ? 0 : (s > RAW_S1 ? E : (E * (s - RAW_S0) + RAW_S1 - RAW_S0) / (RAW_S1 + 1 - RAW_S0));
  }
  static constexpr int n(int s) { return first(s + 1) - first(s); }
};
struct RawLd {
  static constexpr int VPS = 10, SB = K1R::SB;
  unsigned xoff[5], woff[5], m0x;
  unsigned long long xact0, wact[5], tag;
  const char *xp, *wp;
  int qb;
  DEVI void q_hook(int st, char* ws, unsigned lds0, int hp) {
    if ((qb >> 1) == st) {
      const int lane = lane_id(), g = lane >> 4;
      const unsigned a = lds0 + (unsigned)((st & 3) * SB + K1R::XB + ((qb & 1) * 32 + 16 * (g >> 1) + 4 * (g & 1) + ((lane >> 2) & 3)) * 288 +
                                           (lane & 3) * 8 + (hp & 1) * 8);
      const unsigned a2 = a + ((lane & 3) >= 1 ? 8u : 0u), a4 = a + ((lane & 3) >= 2 ? 8u : 0u);
      u32x2 qf[7][2];
#pragma unroll
      for (int nt = 0; nt < 7; ++nt)
        asm volatile("ds_read_b64_tr_b16 %0, %2 offset:%3\n\tds_read_b64_tr_b16 %1, %2 offset:%4"
                     : "=&v"(qf[nt][0]), "=&v"(qf[nt][1])
                     : "v"(nt == 2 ? a2 : (nt == 4 ? a4 : a)), "n"(nt * 32 + (nt > 4 ? 16 : (nt > 2 ? 8 : 0))),
                       "n"(2304 + nt * 32 + (nt > 4 ? 16 : (nt > 2 ? 8 : 0)))
                     : "memory");
      const char* d = uni_ptr(ws + OFF_PQ + (size_t)(hp * 18 + qb) * 7168 + 3072);
#pragma unroll
      for (int nt = 0; nt < 7; ++nt) {
        asm volatile("s_waitcnt lgkmcnt(%2)" : "+v"(qf[nt][0]), "+v"(qf[nt][1]) : "n"(12 - 2 * nt) : "memory");
        const u32x4 v = {qf[nt][0][0], qf[nt][0][1], qf[nt][1][0], qf[nt][1][1]};
        asm volatile("global_store_dwordx4 %0, %1, %2 offset:%3 sc1\n\ts_nop 1" ::"v"((unsigned)lane * 16u), "v"(v), "s"(d), "n"(nt * 1024 - 3072) : "memory");
      }
      if (st == 8) lds_wr32(lds0 + (unsigned)(LW_OFF + LW_Q * 4), 1u);
    }
  }
  DEVI void stage_x(unsigned so) {
    asm volatile(
        "s_mov_b32 m0, %6\n\ts_mov_b64 exec, %7\n\tglobal_load_lds_dwordx4 %0, %5\n\ts_mov_b64 exec, -1\n\t"
        "global_load_lds_dwordx4 %1, %5 offset:1024\n\tglobal_load_lds_dwordx4 %2, %5 offset:2048\n\tglobal_load_lds_dwordx4 %3, %5 offset:3072\n\t"
        "s_add_u32 m0, m0, 0x1000\n\ts_mov_b32 exec_hi, 0\n\tglobal_load_lds_dwordx4 %4, %5\n\ts_mov_b32 exec_hi, -1" ::"v"(xoff[0]),
        "v"(xoff[1]), "v"(xoff[2]), "v"(xoff[3]), "v"(xoff[4]), "s"(xp), "s"(m0x + so), "s"(xact0)
        : "memory", "scc");
    xp += 128;
  }
  DEVI void stage_w(unsigned so) {
    asm volatile(
        "s_mov_b32 m0, %6\n\ts_mov_b64 exec, %7\n\tglobal_load_lds_dwordx4 %0, %5\n\ts_mov_b64 exec, %8\n\t"
        "global_load_lds_dwordx4 %1, %5 offset:1024\n\ts_mov_b64 exec, %9\n\tglobal_load_lds_dwordx4 %2, %5 offset:2048\n\t"
        "s_mov_b64 exec, %10\n\tglobal_load_lds_dwordx4 %3, %5 offset:3072\n\ts_add_u32 m0, m0, 0x1000\n\ts_mov_b64 exec, %11\n\t"
        "global_load_lds_dwordx4 %4, %5\n\ts_mov_b64 exec, -1" ::"v"(woff[0]),
        "v"(woff[1]), "v"(woff[2]), "v"(woff[3]), "v"(woff[4]), "s"(wp), "s"(m0x + so + (unsigned)K1R::XB), "s"(wact[0]), "s"(wact[1]),
        "s"(wact[2]), "s"(wact[3]), "s"(wact[4])
        : "memory", "scc");
    wp += 64 * NQKV * 2;
  }
  DEVI void stage(unsigned so) {
    stage_x(so);
    stage_w(so);
  }
  template <class F>
  DEVI void front(const u16* x, const u16* wqkv, const u16* wo, char* ws, int bid, unsigned lds0, int wl, int lane, int rt, int hp,
                  int& pj, F jobs, const char*& pg MF_ARG) {
    xp = uni_ptr((const char*)x - 3072);
    wp = uni_ptr((const char*)wqkv + ((hp * 72) & ~15) - 3072);
    m0x = lds0 + (unsigned)(wl * 288 * 16);
    xact0 = wl ? ~0ull : ~0xffull;
    const int row0 = wl * 36 + (lane >> 3);
#pragma unroll
    for (int i = 0; i < 5; ++i) {
      const int row = row0 + i * 8;
      int gr = rt * 128 - 16 + row;
      if (gr < 0) gr = 0;
      xoff[i] = (unsigned)(gr * (D_ * 2) + (3 - (i & 3)) * 1024) + (unsigned)(((lane ^ (row >> 1)) & 7) * 16);
    }
    stage_x(0u);
    u32x2 tg;
    asm volatile("global_load_dwordx2 %0, %1, %2 sc1" : "=v"(tg) : "v"(0u), "s"(uni_ptr(ws + OFF_TAG)) : "memory");
    asm volatile("" : "+v"(lane));
#pragma unroll
    for (int i = 0; i < 5; ++i) {
      const unsigned c = (unsigned)(i * 64 + (lane & 63));
      const unsigned wr = (c * 3641u) >> 16, wc = c - wr * 18u, seg = (wc * 13u) >> 6;
      wact[i] = __ballot(wc < 15u) & (i < 4 ? ~0ull : 0xffffffffull);
      woff[i] = (unsigned)(wl * 16 + swap23((int)wr)) * (unsigned)(NQKV * 2) + seg * (unsigned)(D_ * 2 - 80) + wc * 16u + (unsigned)((3 - (i & 3)) * 1024);
    }
    stage_w(0u);
    stage((unsigned)SB);
    jobs();
    MF(10);
    asm volatile("s_waitcnt vmcnt(%1)" : "+v"(tg) : "n"(VPS) : "memory");
    tag = (((unsigned long long)(unsigned)__builtin_amdgcn_readfirstlane(tg[1]) << 32) | (unsigned)__builtin_amdgcn_readfirstlane(tg[0])) +
          0x9e3779b97f4a7c15ull;
    MF(1);
    __builtin_amdgcn_s_barrier();
    MF(15);
    if (wl == 0 && lane == 0)
      asm volatile("global_store_dwordx2 %0, %1, off sc1\n\ts_nop 1" ::"v"(ws + OFF_HELLO + bid * 8), "v"(tag) : "memory");
    stage((unsigned)(2 * SB));
    qb = wl == 3 ? rt : ((wl == 2 && rt < 2) ? 16 + rt : 99);
    q_hook(0, ws, lds0, hp);
    const int nb = pj;
    const bool po = pj >= 0 && wl < 2;
    u32x4 v[8];
    unsigned da = 0u;
    if (po) {
      const int l54 = lane < 54 ? lane : 53, gl = l54 / 9, c = l54 - gl * 9, kl = wl * 6 + gl, t = nb / 6, kg = (nb - t * 6) * 12 + kl;
      da = lds0 + (unsigned)(K1R_PO + (kl >> 2) * 4608 + (16 * (kl & 1) + ((kl >> 1) & 1)) * 144 + c * 16);
      const unsigned sa[2] = {(unsigned)(okmap(kg * 8) * D_ + t * 72 + c * 8) * 2u, (unsigned)(okmap(kg * 8 + 4) * D_ + t * 72 + c * 8) * 2u};
      const char* ps = uni_ptr((const char*)wo);
#pragma unroll
      for (int j = 0; j < 8; ++j)
        asm volatile("global_load_dwordx4 %0, %1, %2 offset:%3" : "=v"(v[j]) : "v"(sa[j / 4]), "s"(ps), "n"((j % 4) * D_ * 2) : "memory");
    } else {
#pragma unroll
      for (int j = 0; j < 8; ++j) asm volatile("" : "=v"(v[j]));
    }
    unsigned slot = (unsigned)(3 * SB), pd = 0u;
#pragma unroll 1
    for (int S = 0; S < RAW_S0; ++S) {
      if (wl == 2 && S == 1) asm volatile("s_load_dword %0, %1, 0\n\ts_load_dword %0, %2, 0" : "+s"(pd) : "s"(ws + (size_t)(bid >> 3) * (2u << 20)), "s"(pg));
      asm volatile("" : "+s"(pd));
      wait_vm<VPS>();
      __builtin_amdgcn_s_barrier();
      if (S == 0) MF(16);
      if (S == 1) MF(17);
      if (S == 2) MF(18);
      if (S == 3) MF(19);
      stage(slot);
      slot = (slot == (unsigned)(3 * SB)) ? 0u : slot + (unsigned)SB;
      q_hook(S + 1, ws, lds0, hp);
      if (po && S == 1) {
        float cs[8];
        asm volatile("s_nop 0 ; LOOPWAIT" : "+v"(v[0]), "+v"(v[1]), "+v"(v[2]), "+v"(v[3]), "+v"(v[4]), "+v"(v[5]), "+v"(v[6]), "+v"(v[7]));
        tr8_store<288, false>(v, da, cs, std::make_integer_sequence<int, 8>{});
        asm volatile("s_waitcnt lgkmcnt(0)" ::: "memory");
      }
      if (po && S == 2) lds_out<6912>(lds0 + (unsigned)(K1R_PO + wl * 6912), ws + OFF_PO + (size_t)nb * 13824 + wl * 6912, lane);
    }
    asm volatile("s_waitcnt lgkmcnt(0)" : "+s"(pd));
  }
};
template <bool JOB>
DEVI void k1_raw_loader(RawLd& ld, const u16* __restrict__ w_in, const u16* __restrict__ w_out, char* __restrict__ ws,
                        unsigned long long tag, int bid, unsigned lds0, int wl, int lane, int rt, int hp, int ct, int job, int pj MF_ARG) {
  constexpr int VPS = RawLd::VPS, SB = RawLd::SB, E = JOB ? 16 : 0;
  const bool isi = job < 144;
  const int kb = isi ? job >> 3 : job - 144;
  const int pitch = isi ? F_ * 2 : D_ * 2;
  const int l36 = lane < 36 ? lane : 35;
  unsigned sa = (unsigned)l36 * 16u, da;
  if (isi) da = lds0 + (unsigned)((l36 >= 18 ? 9216 - 288 : 0) + wl * 8 * 288 + l36 * 16);
  else da = lds0 + (unsigned)((l36 / 9) * (4608 - 144) + (16 * (wl & 1) + (wl >> 1)) * 144 + l36 * 16);
  asm volatile("" : "+v"(sa), "+v"(da));
  u32x4 v[2][8];
  const char* ps = uni_ptr((const char*)(isi ? w_in + (job & 7) * 576 : w_out) + (size_t)(kb * 32 + wl * 8) * pitch);
  auto extra = [&](auto J_) {
    constexpr int j = J_;
    const char* q = ps + (j / 2) * pitch;
    u32x4 t;
    asm volatile("global_load_dwordx4 %0, %1, %2 offset:%3" : "=v"(t) : "v"(sa), "s"(q), "n"((j % 2) * 576) : "memory");
    v[j % 2][j / 2] = t;
  };
  auto step = [&](auto S_) {
    constexpr int S = S_;
    wait_vm<RawX<E>::n(S - 2) + (S <= 6 ? VPS : 0) + RawX<E>::n(S - 1)>();
    __builtin_amdgcn_s_barrier();
    MF(16 + S);
    if constexpr (S == RAW_S0) {
      static_assert(RAW_S0 >= 4, "wo blocks");
      if (pj >= 0 && wl == 0 && lane == 0)
        asm volatile("global_store_dwordx2 %0, %1, off sc1\n\ts_nop 1" ::"v"(ws + OFF_POT + (pj / 6) * 128 + (pj % 6) * 8), "v"(tag) : "memory");
    }
    if constexpr (S <= 5) ld.stage((unsigned)(((S + 3) % 4) * SB));
    ld.q_hook(S + 1, ws, lds0, hp);
    static_for<RawX<E>::first(S), RawX<E>::first(S + 1)>(extra);
  };
  const char* hp_ = ws + OFF_HELLO + lane * 32;
  u32x4 a, b;
  if (wl == 1) asm volatile("global_load_dwordx4 %0, %2, off sc1\n\tglobal_load_dwordx4 %1, %2, off offset:16 sc1" : "=&v"(a), "=&v"(b) : "v"(hp_) : "memory");
  else asm volatile("" : "=v"(a), "=v"(b));
  static_for<RAW_S0, 8>(step);
  MF(2);
  if (wl == 1) {
    asm volatile("" : "+v"(a), "+v"(b));
    const unsigned t0 = (unsigned)tag, t1 = (unsigned)(tag >> 32);
#pragma unroll 1
    for (unsigned spin = 0; spin < SPIN_MEM; ++spin) {
      const unsigned bad = ((a[0] ^ t0) | (a[2] ^ t0) | (b[0] ^ t0) | (b[2] ^ t0)) | ((a[1] ^ t1) | (a[3] ^ t1) | (b[1] ^ t1) | (b[3] ^ t1));
      if (!__any(bad != 0u)) break;
      __builtin_amdgcn_s_sleep(4);
      asm volatile("global_load_dwordx4 %0, %2, off sc1\n\tglobal_load_dwordx4 %1, %2, off offset:16 sc1\n\ts_waitcnt vmcnt(0)"
                   : "=&v"(a), "=&v"(b)
                   : "v"(hp_)
                   : "memory");
    }
    lds_wr32(lds0 + (unsigned)(LW_OFF + LW_HELLO * 4), 1u);
    if (bid == 0 && lane == 0) asm volatile("global_store_dwordx2 %0, %1, off sc1\n\ts_nop 1" ::"v"(ws + OFF_TAG), "v"(tag) : "memory");
  }
  MF(11);
  if (rt < 2) lds_spin(lds0 + (unsigned)(LW_OFF + LW_Q * 4), 1u);
  lds_spin(lds0 + (unsigned)K1R_CNT, 8u);
  if constexpr (JOB) {
#pragma unroll
    for (int b = 0; b < 2; ++b)
      asm volatile("s_waitcnt vmcnt(0)"
                   : "+v"(v[b][0]), "+v"(v[b][1]), "+v"(v[b][2]), "+v"(v[b][3]), "+v"(v[b][4]), "+v"(v[b][5]), "+v"(v[b][6]), "+v"(v[b][7])
                   :
                   : "memory");
  }
  MF(12);
  const char* tp = ws + OFF_POT + ct * 128 + (lane < 6 ? lane : 5) * 8;
  u32x2 f;
  asm volatile("global_load_dwordx2 %0, %1, off sc1" : "=v"(f) : "v"(tp) : "memory");
  if constexpr (JOB) {
    static_assert(SB == 4 * 9216, "staging");
#pragma unroll
    for (int b = 0; b < 2; ++b) {
      float cs[8] = {0.f, 0.f, 0.f, 0.f, 0.f, 0.f, 0.f, 0.f};
      tr8_store<288, true>(v[b], da + (unsigned)(b * 18432), cs, std::make_integer_sequence<int, 8>{});
      const f32x4 c0 = {cs[0], cs[1], cs[2], cs[3]}, c1 = {cs[4], cs[5], cs[6], cs[7]};
      const unsigned ca = lds0 + (unsigned)(3 * SB + wl * 2304) + sa * 2u;
      asm volatile("ds_write_b128 %0, %1 offset:%2" ::"v"(ca), "v"(c0), "n"(b * 1152) : "memory");
      asm volatile("ds_write_b128 %0, %1 offset:%2" ::"v"(ca), "v"(c1), "n"(b * 1152 + 16) : "memory");
    }
    lds_inc(lds0 + (unsigned)(LW_OFF + LW_ST * 4));
    lds_spin(lds0 + (unsigned)(LW_OFF + LW_ST * 4), 4u);
    if (isi) {
      const int j = (job & 7) * 4 + wl, ti = j < 16 ? 2 * j + 1 : 2 * (j - 16);
      lds_out<9216>(lds0 + (unsigned)(wl * 9216), ws + OFF_PI + (size_t)(ti * 18 + kb) * 9216, lane);
      f32x4 c[4];
      const unsigned ca = lds0 + (unsigned)(3 * SB + wl * 576) + sa;
#pragma unroll
      for (int i = 0; i < 4; ++i) asm volatile("ds_read_b128 %0, %1 offset:%2" : "=v"(c[i]) : "v"(ca), "n"(i * 2304) : "memory");
      asm volatile("s_waitcnt lgkmcnt(0)" : "+v"(c[0]), "+v"(c[1]), "+v"(c[2]), "+v"(c[3]));
      const f32x4 t = (c[0] + c[1]) + (c[2] + c[3]);
      if (lane < 36) pk_st16(ws + OFF_CSP + ((size_t)kb * F_ + ti * 144) * 4 + sa, __builtin_bit_cast(u32x4, t));
    } else {
      lds_out<4608>(lds0 + (unsigned)(wl * 9216), ws + OFF_PW + (size_t)(wl * 144 + kb) * 4608, lane);
      lds_out<4608>(lds0 + (unsigned)(wl * 9216 + 4608), ws + OFF_PW + (size_t)(wl * 144 + 72 + kb) * 4608, lane);
    }
  }
  MF(8);
  announce(lds0 + (unsigned)(LW_OFF + LW_PK * 4), 4u, ws, CN_PK, isi ? 8 + (job & 3) * 4 + lane : 24 + lane, JOB && lane < (isi ? 4 : 8));
  MF(9);
  if (wl == 0) {
    asm volatile("" : "+v"(f));
#pragma unroll 1
    for (unsigned spin = 0; spin < SPIN_MEM; ++spin) {
      if (!__any(f[0] != (unsigned)tag || f[1] != (unsigned)(tag >> 32))) break;
      __builtin_amdgcn_s_sleep(2);
      asm volatile("global_load_dwordx2 %0, %1, off sc1\n\ts_waitcnt vmcnt(0)" : "=&v"(f) : "v"(tp) : "memory");
    }
  }
}

template <int MT, int NT>
DEVI void k1_gemm(char* smem, const char* ws, int rt, int hp, int mt0, int nt0,
                  bool late, int lane MK_ARG) {
  const unsigned lds0 = (unsigned)(size_t)smem;
  const int g = lane >> 4;
  f32x4 cs[NT];
  unsigned ta[NT];
  unsigned cad[NT];
#pragma unroll
  for (int nt = 0; nt < NT; ++nt) {
    int c = (nt0 + nt) * 16 + g * 4;
    if (c > 104) c = 104;
    const int part = c / 36, ci = c - part * 36;
    cad[nt] = lds0 + (unsigned)(K1_CS_OFF + (part * 40 + (hp & 1) * 4 + ci) * 4);
    asm volatile("" : "+v"(cad[nt]));
    const int to = (nt0 + nt) * 16 + g * 4 > 104 ? 120 : part * 80 + ci * 2 + (ci >= 20 ? 8 : 0);
    ta[nt] = lds0 + (unsigned)(K1_T_OFF + (mt0 * 16 + (lane & 15)) * K1_TP + to);
    asm volatile("" : "+v"(ta[nt]));
  }
  unsigned sa = lds0 + (unsigned)(K1_ST_OFF + (mt0 * 16 + (lane & 15)) * 8);
  asm volatile("" : "+v"(sa));
  f32x4 acc[MT][NT];
#pragma unroll
  for (int mt = 0; mt < MT; ++mt)
#pragma unroll
    for (int nt = 0; nt < NT; ++nt) acc[mt][nt] = f32x4{0.f, 0.f, 0.f, 0.f};
  f32x2 st[MT];
  if (late) bar();
  if constexpr (NT == 7) cnt_wait(ws, CN_X2, rt * 16 + hp, rt ? 24u : 16u);
  bar();
  MK(1);
  {
    GemmMath<K1C, MT, NT, (NT == 7 ? 5 : 3), (NT == 3 ? 6 : NT), true, 2, false, K1_WS> gm(acc);
    gm.setup(lds0, mt0, nt0, lane, 0u);
    bar();
#pragma unroll
    for (int nt = 0; nt < NT; ++nt) asm volatile("ds_read_b128 %0, %1" : "=v"(cs[nt]) : "v"(cad[nt]));
    gm.first();
    gm.template mid<8>();
#pragma unroll
    for (int mt = 0; mt < MT; ++mt) asm volatile("ds_read_b64 %0, %1 offset:%2" : "=v"(st[mt]) : "v"(sa), "n"(mt * 128));
    asm volatile("s_waitcnt vmcnt(0)" ::: "memory");
    gm.last();
  }
#pragma unroll
  for (int nt = 0; nt < NT; ++nt) asm volatile("" : "+v"(cs[nt]));
#pragma unroll
  for (int mt = 0; mt < MT; ++mt) asm volatile("" : "+v"(st[mt]));
  MK(2);
#pragma unroll
  for (int mt = 0; mt < MT; ++mt) {
    f32x2 M = {st[mt][0], st[mt][0]}, R = {st[mt][1], st[mt][1]};
    asm volatile("" : "+v"(M), "+v"(R));
#pragma unroll
    for (int nt = 0; nt < NT; ++nt) {
      const f32x2 v0 = R * __builtin_elementwise_fma(M, f32x2{cs[nt][0], cs[nt][1]}, f32x2{acc[mt][nt][0], acc[mt][nt][1]});
      const f32x2 v1 = R * __builtin_elementwise_fma(M, f32x2{cs[nt][2], cs[nt][3]}, f32x2{acc[mt][nt][2], acc[mt][nt][3]});
      acc[mt][nt][0] = __uint_as_float(pack2v(v0));
      acc[mt][nt][1] = __uint_as_float(pack2v(v1));
      const f32x2 pk = {acc[mt][nt][0], acc[mt][nt][1]};
      asm volatile("ds_write_b64 %0, %1 offset:%2" ::"v"(ta[nt]), "v"(pk), "n"(mt * 16 * K1_TP) : "memory");
    }
  }
}

DEVI float vmax(float a, float b) {
  float r;
  asm("v_max_f32 %0, %1, %2" : "=v"(r) : "v"(a), "v"(b));
  return r;
}
struct Attn {
  unsigned ka, kb, va, vb, m0, m1, m2, pm[2], oa, pa;
  f32x4 mk;
  unsigned long long sel[4];
  const char* ob;
  DEVI void setup(unsigned T, char* o, int r0, int hp, int wave, int lane) {
    const int g = lane >> 4, r = lane & 15, blk = wave >> 1, hd = wave & 1;
    const unsigned co = (unsigned)(g < 2 ? hd * 48 + g * 16 : 32);
    ka = T + (unsigned)((blk * 32 + 2 * r) * K1_TP) + co;
    kb = T + (unsigned)((blk * 32 + 2 * (r & 7)) * K1_TP) + co;
    const int vr = 4 * g + ((lane >> 2) & 3);
    va = T + (unsigned)((blk * 32 + 2 * vr) * K1_TP + (lane & 3) * 8);
    vb = T + (unsigned)((blk * 32 + 2 * (vr & 7)) * K1_TP + (lane & 3) * 8);
    m0 = (g < 2 || (g == 2 && hd == 0)) ? ~0u : 0u;
    m1 = (g < 2 || (g == 2 && hd == 1)) ? ~0u : 0u;
    m2 = g < 2 ? ~0u : 0u;
    pm[0] = pm[1] = 0u;
    const float ci = __uint_as_float(vconst(0xff800000u));
    const unsigned ch = vconst(0xffffu);
#pragma unroll
    for (int e = 0; e < 4; ++e) {
      const int w1 = r + 8 - 4 * g - e;
      mk[e] = (w1 & 15) <= 7 ? 0.f : ci;
      sel[e] = __ballot(w1 >= 8);
      if (w1 >= 8) pm[e >> 1] |= ch << (16 * (e & 1));
    }
    oa = (unsigned)((8 + hp * 4 + hd * 2 + (g >> 1)) * 1024 + ((blk & 1) * 32 + 2 * r + (g & 1)) * 16);
    pa = ((oa & 1023u) >> 2) + (unsigned)((hp * 2 + hd) * 256);
    ob = uni_ptr(o + (size_t)((r0 >> 6) + (wave >> 2)) * (72 * 1024));
    asm volatile("" : "+v"(ka), "+v"(kb), "+v"(va), "+v"(vb), "+v"(m0), "+v"(m1), "+v"(m2), "+v"(pm[0]), "+v"(pm[1]), "+v"(oa));
    asm volatile("" : "+v"(mk), "+v"(pa), "+s"(ob), "+s"(sel[0]), "+s"(sel[1]), "+s"(sel[2]), "+s"(sel[3]));
  }
  DEVI void run(int r0, int hp, int wave, int lane) {
    const int hd = wave & 1;
    u32x4 q[2], k0[2], k1[2];
    u32x2 v[2][2][2];
#pragma unroll
    for (int p = 0; p < 2; ++p) {
      asm volatile("ds_read_b128 %0, %1 offset:%2" : "=v"(q[p]) : "v"(ka), "n"((16 + p) * K1_TP) : "memory");
      asm volatile("ds_read_b128 %0, %1 offset:%2" : "=v"(k0[p]) : "v"(ka), "n"(p * K1_TP + 80) : "memory");
      asm volatile("ds_read_b128 %0, %1 offset:%2" : "=v"(k1[p]) : "v"(kb), "n"((32 + p) * K1_TP + 80) : "memory");
    }
    const unsigned vh = (unsigned)hd * 48u;
#pragma unroll
    for (int p = 0; p < 2; ++p) {
      asm volatile("ds_read_b64_tr_b16 %0, %1 offset:%2" : "=v"(v[p][0][0]) : "v"(va + vh), "n"(p * K1_TP + 160) : "memory");
      asm volatile("ds_read_b64_tr_b16 %0, %1 offset:%2" : "=v"(v[p][0][1]) : "v"(vb + vh), "n"((32 + p) * K1_TP + 160) : "memory");
      asm volatile("ds_read_b64_tr_b16 %0, %1 offset:%2" : "=v"(v[p][1][0]) : "v"(va), "n"(p * K1_TP + 192) : "memory");
      asm volatile("ds_read_b64_tr_b16 %0, %1 offset:%2" : "=v"(v[p][1][1]) : "v"(vb), "n"((32 + p) * K1_TP + 192) : "memory");
    }
    const u32x4 one4 = {vconst(0x3f803f80u), vconst(0x3f803f80u), vconst(0x3f803f80u), vconst(0x3f803f80u)};
    const s16x8 ones = __builtin_bit_cast(s16x8, one4);
    const float c = 0.23570226039551584f * 1.4426950408889634f;
    const f32x4 z = {0.f, 0.f, 0.f, 0.f};
    f32x4 sc[2][2];
#pragma unroll
    for (int p = 0; p < 2; ++p) {
      asm volatile("s_waitcnt lgkmcnt(%3)" : "+v"(q[p]), "+v"(k0[p]), "+v"(k1[p]) : "n"(11 - 3 * p));
      const s16x8 qf = __builtin_bit_cast(s16x8, q[p] & u32x4{m0, m1, m2, m2});
      sc[p][0] = __builtin_amdgcn_mfma_f32_16x16x32_bf16(__builtin_bit_cast(s16x8, k0[p]), qf, mk, 0, 0, 0);
      sc[p][1] = __builtin_amdgcn_mfma_f32_16x16x32_bf16(__builtin_bit_cast(s16x8, k1[p]), qf, mk, 0, 0, 0);
      __builtin_amdgcn_sched_barrier(0);
    }
    if (__builtin_expect(r0 == 0 && wave < 2, 0)) {
#pragma unroll
      for (int p = 0; p < 2; ++p)
#pragma unroll
        for (int e = 0; e < 4; ++e)
          if (lane < 32) sc[p][0][e] = mk[e] - 4.242640687e9f;
    }
    float lg[2][4], mx[2];
#pragma unroll
    for (int p = 0; p < 2; ++p) {
#pragma unroll
      for (int e = 0; e < 4; ++e) lg[p][e] = __builtin_amdgcn_inverse_ballot_w64(sel[e]) ? sc[p][1][e] : sc[p][0][e];
      asm("v_max3_f32 %0, %1, %2, %3\n\tv_max_f32 %0, %0, %4" : "=&v"(mx[p]) : "v"(lg[p][0]), "v"(lg[p][1]), "v"(lg[p][2]), "v"(lg[p][3]));
    }
    {
      const auto a = __builtin_amdgcn_permlane32_swap(__float_as_uint(mx[0]), __float_as_uint(mx[1]), false, false);
      const unsigned b = __float_as_uint(vmax(__uint_as_float(a[0]), __uint_as_float(a[1])));
      const auto d = __builtin_amdgcn_permlane16_swap(b, b, false, false);
      const unsigned f = __float_as_uint(vmax(__uint_as_float(d[0]), __uint_as_float(d[1])) * -c);
      const auto h = __builtin_amdgcn_permlane32_swap(f, f, false, false);
      mx[0] = __uint_as_float(h[0]);
      mx[1] = __uint_as_float(h[1]);
    }
#pragma unroll
    for (int p = 0; p < 2; ++p)
#pragma unroll
      for (int e = 0; e < 4; ++e) lg[p][e] = __builtin_amdgcn_exp2f(__builtin_fmaf(lg[p][e], c, mx[p]));
    asm volatile("s_waitcnt lgkmcnt(0)"
                 : "+v"(v[0][0][0]), "+v"(v[0][0][1]), "+v"(v[0][1][0]), "+v"(v[0][1][1]), "+v"(v[1][0][0]), "+v"(v[1][0][1]),
                   "+v"(v[1][1][0]), "+v"(v[1][1][1]));
    __builtin_amdgcn_sched_barrier(0);
    f32x4 su[2], o0[2], o1[2];
    s16x8 pp[2];
#pragma unroll
    for (int p = 0; p < 2; ++p) {
      const unsigned p0 = pack2(lg[p][0], lg[p][1]), p1 = pack2(lg[p][2], lg[p][3]);
      pp[p] = __builtin_bit_cast(s16x8, u32x4{p0 & ~pm[0], p1 & ~pm[1], p0 & pm[0], p1 & pm[1]});
    }
    __builtin_amdgcn_sched_barrier(0);
#pragma unroll
    for (int p = 0; p < 2; ++p) {
      const s16x8 pf = pp[p];
      const u32x4 v0 = {v[p][0][0][0], v[p][0][0][1], v[p][0][1][0], v[p][0][1][1]};
      const u32x4 v1 = {v[p][1][0][0], v[p][1][0][1], v[p][1][1][0], v[p][1][1][1]};
      su[p] = __builtin_amdgcn_mfma_f32_16x16x32_bf16(ones, pf, z, 0, 0, 0);
      o0[p] = __builtin_amdgcn_mfma_f32_16x16x32_bf16(__builtin_bit_cast(s16x8, v0), pf, z, 0, 0, 0);
      o1[p] = __builtin_amdgcn_mfma_f32_16x16x32_bf16(__builtin_bit_cast(s16x8, v1), pf, z, 0, 0, 0);
    }
    __builtin_amdgcn_sched_barrier(0);
    u32x2 y[2], yl;
    unsigned hm = (unsigned)hd - 1u;
    asm volatile("" : "+s"(hm));
#pragma unroll
    for (int p = 0; p < 2; ++p) {
      const float inv = __builtin_amdgcn_rcpf(su[p][0]);
      y[p] = u32x2{pack2(o0[p][0] * inv, o0[p][1] * inv), pack2(o0[p][2] * inv, o0[p][3] * inv)};
      const unsigned la = pack2(o1[p][0] * inv, o1[p][1] * inv), lb = pack2(o1[p][2] * inv, o1[p][3] * inv);
      yl[p] = (la & hm) | (lb & ~hm);
    }
    const auto s0 = __builtin_amdgcn_permlane16_swap(y[0][0], y[1][0], false, false);
    const auto s1 = __builtin_amdgcn_permlane16_swap(y[0][1], y[1][1], false, false);
    const u32x4 w = {s0[0], s1[0], s0[1], s1[1]};
    asm volatile("global_store_dwordx4 %0, %1, %4 sc1\n\ts_mov_b64 exec, 0xffff\n\tglobal_store_dwordx2 %2, %3, %4 sc1\n\ts_mov_b64 exec, -1\n\ts_nop 0" ::"v"(oa),
                 "v"(w), "v"(pa), "v"(yl), "s"(ob)
                 : "memory");
  }
};

#define NL_MAIN 660
DEVI void math_start(unsigned lds0, char* ws, int bid, int wave, int lane) {
  static_assert(2 * SZ_ZP == (size_t)256 * 2048 && NCNT <= 16 && OFF_CNT + NCNT * 32768 < 0xffffffffu, "mark split");
  const unsigned m1 = vconst(~0u), l = (unsigned)lane;
  const unsigned hi = wave < 2 ? 128u : 32768u;
  unsigned b0 = (unsigned)OFF_ZP + (unsigned)(bid * 2 + wave) * 1024u, b1 = (unsigned)OFF_CNT + (unsigned)((wave & 1) * 2048 + bid) * 128u;
  asm volatile("" : "+s"(b0), "+s"(b1));
  const unsigned base = wave < 2 ? b0 : b1;
  const unsigned long long act = wave < 3 ? ~0ull : (wave == 3 ? (1ull << ((NCNT - 8) * 8)) - 1ull : 0ull);
  asm volatile("s_mov_b64 exec, %3\n\tglobal_store_dwordx4 %0, %1, %2 sc1\n\ts_mov_b64 exec, -1\n\ts_nop 0" ::"v"(base + (l >> 3) * hi + (l & 7u) * 16u),
               "v"(u32x4{m1, m1, m1, m1}), "s"(ws), "s"(act)
               : "memory");
  unsigned a4 = (l < 32u ? (l >> 3) * (unsigned)K1R::SB : (unsigned)LW_OFF) + (l & 7u) * 16u, a0 = (unsigned)(wave * K1R::SB + K1R::XB + 240) + l * 288u;
  asm volatile("" : "+v"(a4), "+v"(a0));
  const unsigned a = wave < 4 ? a0 : a4;
  const unsigned v = (wave < 4 || l < 32u) ? 0x3f803f80u : 0u;
  asm volatile("s_mov_b64 exec, %2\n\tds_write_b128 %0, %1\n\ts_mov_b64 exec, -1\n\ts_waitcnt lgkmcnt(0)" ::"v"(lds0 + a), "v"(u32x4{v, v, v, v}),
               "s"(wave < 4 ? ~0ull : (wave == 4 ? 0xffffffffffull : 0ull))
               : "memory");
}

template <int NST, int NS, int NSW, int WOFF, int XW, int X0, int S0, unsigned BAR>
struct ResX {
  static constexpr int WS = 4 * 4608, R = 10, V1 = 5 * NS, NV = V1 + 10 * (NST - NS), NG = (NSW + 2) / 3;
  s16x8 xf[XW][2];
  s16x8 wf[R];
  unsigned wrd[2][NG], xo, ea, fa, fv, tag;
  const char* xp[3];
  f32x4 (&acc)[2][5];
  DEVI ResX(f32x4 (&a)[2][5]) : acc(a) {}
  static constexpr int st(int V) { return V < V1 ? V / 5 : NS + (V - V1) / 10; }
  static constexpr int hf(int V) { return V < V1 ? 0 : ((V - V1) / 5) & 1; }
  static constexpr bool first(int V) { return V < V1 ? V % 5 == 0 : (V - V1) % 10 == 0; }
  static constexpr int wreq(int V) {
    int n = V + 5;
    if (V >= V1 - 4) n += V < V1 ? V - (V1 - 5) : 4;
    return n < NV ? n : NV;
  }
  static constexpr int XU = V1 + 1, XF = V1 + 11, XR = V1 + 15, XC = V1 + 21;
  static constexpr int oth(int V) { return NS && ((V >= XU && V <= XF && (V - XU) % 2 == 0) || V == XR || (V > XC && V <= XC + 5)) ? 1 : 0; }
  static constexpr int newer(int V) {
    const int F = V + 1 < NV ? V + 1 : V;
    int v = V - 1, n = 0;
    for (; v >= 0 && wreq(v - 1) > F; --v) n += wreq(v) - wreq(v - 1) + oth(v);
    return n + wreq(v) - 1 - F + (v >= 0 ? oth(v) : 0);
  }
  static constexpr int xreq(int T) {
    int n = X0 + 2 * (T + 1);
    if (n > T + XW) n = T + XW;
    if (NS && T <= 11 && n > 18) n = 18;
    if (T < 0) n = X0;
    return n < NST ? n : NST;
  }
  static_assert(NS == 0 || (xreq(NS - 2) == NST && XC + 5 < NV), "the last steps only compute and swap");
  template <int T, int M>
  DEVI void xld() {
    asm volatile("global_load_dwordx4 %0, %1, %2 sc1" : "=v"(xf[T % XW][M]) : "v"(xo), "s"(xp[M == 0 ? 0 : (T < NS ? 1 : 2)]));
    if constexpr (M == 1 && T + 1 < NST) asm volatile("v_add_u32 %0, 0x2000, %0" : "+v"(xo));
  }
  template <int V>
  DEVI void rd() {
    constexpr int T = st(V), sl = (T / 2 + S0) % NSW;
    asm volatile("ds_read_b128 %0, %1 offset:%2" : "=v"(wf[V % R]) : "v"(wrd[hf(V)][sl / 3]), "n"((sl % 3) * WS + (T & 1) * 9216 + (V % 5) * 32));
  }
  template <int V, int M, int H>
  DEVI void mf() {
    acc[M][V % 5] = __builtin_amdgcn_mfma_f32_16x16x32_bf16(wf[V % R], xf[st(V) % XW][H], acc[M][V % 5], 0, 0, 0);
    __builtin_amdgcn_sched_barrier(0);
  }
  template <int V>
  DEVI void unit() {
    constexpr int T = st(V), n = V % 5, nx = xreq(T) - xreq(T - 1), nw = wreq(V) - wreq(V - 1);
    constexpr int vm = 2 * (xreq(T - 1) - 1 - T);
    static_assert(newer(V) <= 15, "lgkmcnt range");
    if constexpr (first(V) && V % 2 == 0) asm volatile("s_waitcnt vmcnt(%0) lgkmcnt(%1)" ::"n"(vm), "n"(newer(V)));
    else if constexpr (first(V)) asm volatile("s_waitcnt vmcnt(%0)" ::"n"(vm));
    else if constexpr (V % 2 == 0) asm volatile("s_waitcnt lgkmcnt(%0)" ::"n"(newer(V)));
    __builtin_amdgcn_sched_barrier(0);
    if constexpr (V < V1) mf<V, V & 1, V & 1>();
    else mf<V, 0, hf(V)>();
    if constexpr (nw > 0) rd<wreq(V - 1)>();
    __builtin_amdgcn_sched_barrier(0);
    if constexpr (V < V1) {
      mf<V, 1 - (V & 1), 1 - (V & 1)>();
      if constexpr (nw > 1) rd<wreq(V - 1) + 1>();
      if constexpr (nx == 2 && n < 4) xld<xreq(T - 1) + n / 2, n % 2>();
      if constexpr (nx == 1 && n == (T & 1)) xld<xreq(T - 1), 0>();
      if constexpr (nx == 1 && n == (T & 1) + 2) xld<xreq(T - 1), 1>();
      if constexpr (n == 4 && (T & 1) && ((BAR >> (T / 2)) & 1u)) bar();
    } else if constexpr (NS == 0) {
      constexpr int k = (V % 10) / 2;
      if constexpr (V % 2 == 0 && k < 2 * nx) xld<xreq(T - 1) + k / 2, k % 2>();
      if constexpr (V % 10 == 8 && nx > 0 && xreq(T) == NST)
        asm volatile("s_mov_b64 exec, 1\n\tds_add_u32 %0, %1\n\ts_mov_b64 exec, -1" ::"v"(fa), "v"(fv));
    } else {
      if constexpr (oth(V) && V < XF) asm volatile("ds_write_b128 %0, %1 offset:%2" ::"v"(ea), "v"(acc[1][(V - XU) / 2]), "n"((V - XU) * 512));
      if constexpr (V == XF) asm volatile("ds_write_b32 %0, %1" ::"v"(fa), "v"(fv));
      if constexpr (V == XF + 2) asm volatile("v_xor_b32 %0, 4, %0" : "+v"(fa));
      if constexpr (V == XR) asm volatile("ds_read_b32 %0, %1" : "+v"(fv) : "v"(fa));
      if constexpr (V == XR + 2) asm volatile("v_xor_b32 %0, 0x2000, %0" : "+v"(ea));
      if constexpr (V == XC) {
        unsigned c = 1u << 18;
        asm volatile(
            "s_waitcnt lgkmcnt(%4)\n\tv_cmp_ne_u32 vcc, %3, %0\n\ts_cbranch_vccnz .Lw%=\n\t.subsection 1\n.Lw%=:\n\tds_read_b32 %0, %2\n\t"
            "s_waitcnt lgkmcnt(0)\n\tv_cmp_ne_u32 vcc, %3, %0\n\t" SAFE_OUT(".Lr%=") "s_cbranch_vccnz .Lw%=\n\ts_branch .Lr%=\n\t.subsection 0\n.Lr%=:"
            : "+v"(fv), NOP_(c)
            : "v"(fa), "s"(tag), "n"(wreq(XC) - wreq(XR))
            : "vcc", "scc");
      }
      if constexpr (oth(V) && V > XC) asm volatile("ds_read_b128 %0, %1 offset:%2" : "=v"(acc[1][V - XC - 1]) : "v"(ea), "n"((V - XC - 1) * 1024));
    }
    __builtin_amdgcn_sched_barrier(0);
  }
  template <int... V>
  DEVI void units(std::integer_sequence<int, V...>) {
    (unit<V>(), ...);
  }
  DEVI void run() {
    units(std::make_integer_sequence<int, NV>{});
    if constexpr (NS > 0) {
      asm volatile("s_waitcnt lgkmcnt(0)" : "+v"(acc[1][0]), "+v"(acc[1][1]), "+v"(acc[1][2]), "+v"(acc[1][3]), "+v"(acc[1][4]));
#pragma unroll
      for (int i = 0; i < 5; ++i) acc[0][i] += acc[1][i];
    }
  }
  template <int... V>
  DEVI void pro(std::integer_sequence<int, V...>) {
    (rd<V>(), ...);
  }
  DEVI void prow() { pro(std::make_integer_sequence<int, wreq(-1)>{}); }
  template <int T0, int... T>
  DEVI void prox(std::integer_sequence<int, T...>) {
    ((xld<T0 + T, 0>(), xld<T0 + T, 1>()), ...);
  }
  DEVI void setup(unsigned lds0, const char* xbase, int wave, int lane, unsigned red, unsigned nth) {
    const int g = lane >> 4, r = lane & 15, kq = wave & 1;
    xp[0] = xbase + wave * 256;
    xp[1] = xbase + (wave ^ 1) * 256;
    xp[2] = xp[0] + (kq ? -4096 : 4096);
    xo = (unsigned)((4 * kq + g) * 1024 + r * 16);
#pragma unroll
    for (int i = 0; i < 2 * NG; ++i) {
      wrd[i / NG][i % NG] = lds0 + (unsigned)(WOFF + (i % NG) * 3 * WS + (kq ^ (i / NG)) * 4608 + (2 * (r & 7) + 16 * (g & 1) + (g >> 1)) * 144 + (r >> 3) * 16);
      if (i < NG || NS == 0 || i % NG == ((NS / 2 + S0) % NSW) / 3) asm volatile("" : "+v"(wrd[i / NG][i % NG]));
    }
    asm volatile("" : "+v"(xo));
    if constexpr (NS > 0) {
      ea = lds0 + red + (unsigned)((wave ^ 1) * 8192 + lane * 16);
      fa = lds0 + (unsigned)(LW_OFF + (LW_PAIR + wave) * 4);
      fv = tag = nth;
      asm volatile("" : "+v"(ea), "+v"(fa), "+v"(fv));
    }
  }
  template <int N>
  DEVI void init(u32x2 (&xv)[5]) {
    asm volatile("s_waitcnt vmcnt(%5)" : "+v"(xv[0]), "+v"(xv[1]), "+v"(xv[2]), "+v"(xv[3]), "+v"(xv[4]) : "n"(N));
#pragma unroll
    for (int i = 0; i < 5; ++i) {
      acc[0][i] = f32x4{bf_lo(xv[i][0]), bf_hi(xv[i][0]), bf_lo(xv[i][1]), bf_hi(xv[i][1])};
      asm volatile("" : "+v"(acc[0][i]));
      acc[1][i] = f32x4{0.f, 0.f, 0.f, 0.f};
    }
  }
};

constexpr int P5_XW = 8, P5_X0 = 5, P2_X0 = 4;
constexpr unsigned P5_BAR = 0x2aaau;
constexpr int P5_W = 15 * 1024;
constexpr int P2_W = K1_ST_OFF - 18 * 4608;
constexpr int P5_RED = 0;
static_assert(P5_RED % 16384 == 0 && P5_RED + 3 * 8192 + 5120 <= P5_W + 18432, "exchange area");
static_assert(P2_W >= K1_T_OFF + 144 * K1_TP + 64 && P2_W % 16 == 0 && P5_W + 8 * 18432 <= LW_OFF, "LDS placement");

struct ResIn {
  const char* xp;
  unsigned o, o2, st;
  DEVI void setup(const char* src, bool rm, int rb, int ct, int wave, int lane) {
    const int g = lane >> 4, g2 = g & 1, rl = wave * 16 + (lane & 15);
    xp = uni_ptr(rm ? src + ((size_t)rb * 64 * D_ + ct * 72) * 2 : src + (size_t)(rb * 72 + ct * 9) * 1024);
    o = rm ? (unsigned)(rl * (D_ * 2) + g * 8) : (unsigned)((g >> 1) * 1024 + rl * 16 + g2 * 8);
    o2 = rm ? (unsigned)(rl * (D_ * 2) + g2 * 8 + 128) : (unsigned)(8192 + rl * 16 + g2 * 8);
    st = rm ? 32u : 2048u;
    asm volatile("" : "+v"(o), "+v"(o2));
    asm volatile("" : "+s"(xp));
  }
  DEVI void load(u32x2 (&xv)[5]) {
#pragma unroll
    for (int i = 0; i < 4; ++i) asm volatile("global_load_dwordx2 %0, %1, %2 sc1" : "=v"(xv[i]) : "v"(o), "s"(xp + i * st));
    asm volatile("global_load_dwordx2 %0, %1, %2 sc1" : "=v"(xv[4]) : "v"(o2), "s"(xp));
  }
};

DEVI void p2_gemm(f32x4 (&acc)[2][5], u32x2 (&xv)[5], unsigned lds0, const char* ob, int layer, int wave, int lane MK_ARG) {
  ResX<9, 0, 5, P2_W, 9, P2_X0, 0, 0u> gm(acc);
  gm.setup(lds0, ob, wave, lane, 0u, 0u);
  unsigned a = (gm.xo & ~1023u) | ((gm.xo & 1023u) >> 2);
  gm.xo += 8192u;
  gm.fa = lds0 + (unsigned)(LW_OFF + (LW_T + layer) * 4);
  gm.fv = vconst(1u);
  asm volatile("" : "+v"(a), "+v"(gm.xo), "+v"(gm.fa));
  const char* pp[2] = {gm.xp[0] - wave * 192, gm.xp[2] - wave * 192};
  place();
  bar();
  MK(6);
  unsigned e[2][4];
#pragma unroll
  for (int d = 0; d < 8; ++d)
    asm volatile("global_load_dword %0, %1, %2 offset:%3 sc1" : "=v"(e[d / 4][d % 4]) : "v"(a), "s"(pp[d / 4]), "n"((d % 4) * 256));
  gm.template prox<1>(std::make_integer_sequence<int, P2_X0 - 1>{});
  gm.prow();
  gm.template init<6 + 2 * P2_X0>(xv);
  asm volatile("s_waitcnt vmcnt(%8)"
               : "+v"(e[0][0]), "+v"(e[0][1]), "+v"(e[0][2]), "+v"(e[0][3]), "+v"(e[1][0]), "+v"(e[1][1]), "+v"(e[1][2]), "+v"(e[1][3])
               : "n"(2 * P2_X0 - 2));
  gm.xf[0][0] = __builtin_bit_cast(s16x8, u32x4{e[0][0], e[0][1], e[0][2], e[0][3]});
  gm.xf[0][1] = __builtin_bit_cast(s16x8, u32x4{e[1][0], e[1][1], e[1][2], e[1][3]});
  __builtin_amdgcn_sched_barrier(0);
  gm.run();
}

struct ResOut {
  const char *xp[3], *sp;
  unsigned xo, so;
  DEVI void setup(char* xim, char* xs, u16* out, bool fin, int rb, int ct, int wave, int lane) {
    const int g = lane >> 4, rl = wave * 16 + (lane & 15);
    xp[0] = uni_ptr(fin ? (const char*)(out + (size_t)rb * 64 * D_ + ct * 72) : xim + (size_t)(rb * 72 + ct * 9) * 1024);
    xp[1] = xp[0] + (fin ? 64 : 4096);
    xp[2] = xp[0] + (fin ? 128 : 8192);
    sp = uni_ptr(xs + (size_t)(rb * 8 + ct) * 512);
    xo = (unsigned)(((g & 1) * 2 + (g >> 1)) * (fin ? 16 : 1024) + rl * (fin ? D_ * 2 : 16));
    so = (unsigned)(rl * 8);
    asm volatile("" : "+v"(xo), "+v"(so), "+s"(xp[0]), "+s"(xp[1]), "+s"(xp[2]), "+s"(sp));
  }
  DEVI void run(const f32x4 (&a)[5], bool fin) {
    u32x2 y[5];
    u32x4 v[3];
#pragma unroll
    for (int nt = 0; nt < 5; ++nt) y[nt] = u32x2{pack2(a[nt][0], a[nt][1]), pack2(a[nt][2], a[nt][3])};
#pragma unroll
    for (int i = 0; i < 3; ++i) {
      const auto s0 = __builtin_amdgcn_permlane16_swap(y[2 * i][0], y[i < 2 ? 2 * i + 1 : 4][0], false, false);
      const auto s1 = __builtin_amdgcn_permlane16_swap(y[2 * i][1], y[i < 2 ? 2 * i + 1 : 4][1], false, false);
      v[i] = u32x4{s0[0], s1[0], s0[1], s1[1]};
    }
    if (fin) {
      asm volatile("global_store_dwordx4 %0, %1, %4\n\tglobal_store_dwordx4 %0, %2, %5\n\ts_mov_b64 exec, 0xffff\n\tglobal_store_dwordx4 %0, %3, %6\n\t"
                   "s_mov_b64 exec, -1\n\ts_nop 0" ::"v"(xo), "v"(v[0]), "v"(v[1]), "v"(v[2]), "s"(xp[0]), "s"(xp[1]), "s"(xp[2])
                   : "memory");
      return;
    }
    asm volatile("global_store_dwordx4 %0, %1, %4 sc1\n\tglobal_store_dwordx4 %0, %2, %5 sc1\n\ts_mov_b64 exec, 0xffff\n\t"
                 "global_store_dwordx4 %0, %3, %6 sc1\n\ts_mov_b64 exec, -1\n\ts_nop 0" ::"v"(xo), "v"(v[0]), "v"(v[1]), "v"(v[2]),
                 "s"(xp[0]), "s"(xp[1]), "s"(xp[2])
                 : "memory");
    const u32x4 one4 = {vconst(0x3f803f80u), vconst(0x3f803f80u), vconst(0x3f803f80u), vconst(0x3f803f80u)};
    const bool lo = __builtin_amdgcn_inverse_ballot_w64(0xffffull);
    v[2] = u32x4{lo ? v[2][0] : 0u, lo ? v[2][1] : 0u, lo ? v[2][2] : 0u, lo ? v[2][3] : 0u};
    f32x4 s = {0.f, 0.f, 0.f, 0.f}, q = s;
#pragma unroll
    for (int i = 0; i < 3; ++i) {
      const s16x8 u = __builtin_bit_cast(s16x8, v[i]);
      q = __builtin_amdgcn_mfma_f32_16x16x32_bf16(u, u, q, 0, 0, 0);
      s = __builtin_amdgcn_mfma_f32_16x16x32_bf16(__builtin_bit_cast(s16x8, one4), u, s, 0, 0, 0);
    }
    float q0 = q[0], q1 = q[1], q2 = q[2], q3 = q[3];
    const bool b0 = __builtin_amdgcn_inverse_ballot_w64(0xaaaaaaaaaaaaaaaaull), b1 = __builtin_amdgcn_inverse_ballot_w64(0xccccccccccccccccull);
    q0 = b0 ? q1 : q0;
    q2 = b0 ? q3 : q2;
    asm("" : "+v"(q0), "+v"(q2));
    const f32x2 sq = {s[0], b1 ? q2 : q0};
    asm volatile("s_mov_b64 exec, %3\n\tglobal_store_dwordx2 %0, %1, %2 sc1\n\ts_mov_b64 exec, -1" ::"v"(so), "v"(sq), "s"(sp), "s"(0xf0000f0000f0000full) : "memory");
  }
};

using K3C = GemmCfg<128, 144>;
constexpr int K3_Z1_OFF = K3C::LDS_BYTES;
constexpr int K3_C2_OFF = K3_Z1_OFF + K3C::XB;
constexpr int K3_ZC_OFF = K3_C2_OFF + 2048;
constexpr int K3_DM_OFF = K3_ZC_OFF + 1024;
constexpr int K3_ST_OFF = K3_Z1_OFF + 4 * 5120;
constexpr int K3_C1_OFF = K3_ST_OFF + 2048;
constexpr int K3_BS_OFF = K3_C1_OFF + 576;
static_assert(K3_BS_OFF + 512 <= LW_OFF, "K3 LDS");
static_assert(P5_W + 19 * 1024 == K3C::SB && K3C::SB == 34 * 1024, "w_out ring");

DEVI void red4(float (&t)[4], float& s, float& q) {
#pragma unroll
  for (int j = 0; j < 2; ++j) {
    const auto p = __builtin_amdgcn_permlane16_swap(__float_as_uint(t[j]), __float_as_uint(t[2 + j]), false, false);
    const unsigned v = __float_as_uint(__uint_as_float(p[0]) + __uint_as_float(p[1]));
    const auto w = __builtin_amdgcn_permlane32_swap(v, v, false, false);
    t[j] = __uint_as_float(w[0]) + __uint_as_float(w[1]);
    asm volatile("" : "+v"(t[j]));
  }
  s = t[0];
  q = t[1];
}
DEVI void red8(const f32x4& a, const f32x4& b, float& s, float& q) {
  float t[4];
#pragma unroll
  for (int j = 0; j < 4; ++j) {
    t[j] = a[j] + b[j];
    asm volatile("" : "+v"(t[j]));
  }
  red4(t, s, q);
}
DEVI void red16(const f32x4& a, const f32x4& b, const f32x4& c, const f32x4& d, float& s, float& q) {
  float t[4];
#pragma unroll
  for (int j = 0; j < 4; ++j) {
    float u = a[j] + b[j], v = c[j] + d[j];
    asm volatile("" : "+v"(u), "+v"(v));
    t[j] = u + v;
    asm volatile("" : "+v"(t[j]));
  }
  red4(t, s, q);
}

constexpr int b_k(int s) { return s == 0 ? 0 : (s < 3 ? 6 + s : (s == 3 ? 1 : (s < 6 ? 1 + s : (s == 6 ? 4 : s - 5)))); }
constexpr int b_xp(int s) { return s == 0 ? 0 : (b_k(s) >= 4 ? (b_k(s) % 5) / 2 : b_xp(s - 1)); }
template <bool B>
struct UMath {
  static constexpr int D = 4, R = 6, NV = 162, SB = K3C::SB, P = K3C::P;
  s16x8 wf[R], xf[2][2];
  s16x8 (&xc)[4][2][2];
  f32x4 (&acc)[2][9];
  f32x4 rc[4][2];
  f32x2 cs[9];
  unsigned ra, ca;
  template <int I>
  DEVI void cread() {
    if constexpr (I < 8) asm volatile("ds_read_b128 %0, %1 offset:%2" : "=v"(rc[I / 2][I % 2]) : "v"(ra), "n"((I / 2) * 512 + (I % 2) * 16));
    else if constexpr (I < 17) asm volatile("ds_read_b64 %0, %1 offset:%2" : "=v"(cs[I - 8]) : "v"(ca), "n"((I - 8) * 128));
  }
  unsigned xa, wa;
  unsigned fa, fw, fv;
  DEVI UMath(f32x4 (&a)[2][9], s16x8 (&x)[4][2][2]) : xc(x), acc(a) {}
  static constexpr int wslot(int s) { return B ? (s + 1) % 4 : s % 4; }
  static constexpr int xslot(int s) { return B ? b_k(s) % 5 : s % 5; }
  static constexpr int xpair(int s) { return B ? b_xp(s) : xslot(s) / 2; }
  static constexpr bool res(int q) { return !B || b_k(q / 2) >= 4; }
  static constexpr bool chk(int s) { return s >= 3 && s < 7; }
  static constexpr bool xop(int v) { return v + D < NV && (v + D) % 9 < 2 && res((v + D) / 9); }
  static constexpr int ops1(int v) { return (xop(v) || (B && ((v % 18 == 9 && chk(v / 18)) || (v % 18 == 0 && v > 0)))) ? 1 : 0; }
  static constexpr int newer(int V) {
    int v0 = (V % 9 == 8 ? V : V + 1) - D, n = 0;
    if (v0 >= 0 && !(V % 9 == 0 && res(V / 9))) n += ops1(v0);
    for (int v = v0 + 1; v < V; ++v)
      if (v >= 0) n += (v + D < NV ? 1 : (B ? 0 : 4)) + ops1(v);
    return n;
  }
  template <int V>
  DEVI void wread() {
    asm volatile("ds_read_b128 %0, %1 offset:%2" : "=v"(wf[V % R]) : "v"(wa), "n"((wslot(V / 18) & 1) * SB + ((V % 18) / 9) * 32 * P + (V % 9) * 32));
  }
  template <int Q, int M>
  DEVI void xread() {
    asm volatile("ds_read_b128 %0, %1 offset:%2" : "=v"(xf[Q & 1][M]) : "v"(xa), "n"((xslot(Q / 2) & 1) * SB + (Q & 1) * 8192 + M * (B ? 256 : 64)));
  }
  template <int V, int M_>
  DEVI void mf() {
    constexpr int M = ((V % 9) & 1) ? 1 - M_ : M_, q = V / 9;
    if constexpr (!B) acc[M][V % 9] = __builtin_amdgcn_mfma_f32_16x16x32_bf16(xf[q & 1][M], wf[V % R], acc[M][V % 9], 0, 0, 0);
    else if constexpr (res(q)) acc[M][V % 9] = __builtin_amdgcn_mfma_f32_16x16x32_bf16(wf[V % R], xf[q & 1][M], acc[M][V % 9], 0, 0, 0);
    else acc[M][V % 9] = __builtin_amdgcn_mfma_f32_16x16x32_bf16(wf[V % R], xc[b_k(q / 2)][q & 1][M], acc[M][V % 9], 0, 0, 0);
    __builtin_amdgcn_sched_barrier(0);
  }
  template <int V>
  DEVI void unit() {
    constexpr int U = V % 18, s = V / 18;
    static_assert(newer(V) <= 15, "lgkmcnt range");
    if constexpr ((V % 9) % 2 == 0 && V > 0) asm volatile("s_waitcnt lgkmcnt(%0)" ::"n"(newer(V)));
    __builtin_amdgcn_sched_barrier(0);
    mf<V, 0>();
    if constexpr (V + D < NV) wread<V + D>();
    if constexpr (!B && V + D >= NV) {
      cread<(V + D - NV) * 4>();
      cread<(V + D - NV) * 4 + 1>();
    }
    if constexpr (s < 8 && U == 13 && (wslot(s + 1) >> 1) != (wslot(s) >> 1))
      asm volatile("v_add_u32 %0, %1, %0" : "+v"(wa) : "n"(((wslot(s + 1) >> 1) - (wslot(s) >> 1)) * 2 * SB));
    __builtin_amdgcn_sched_barrier(0);
    mf<V, 1>();
    if constexpr (xop(V)) xread<(V + D) / 9, (V + D) % 9>();
    if constexpr (!B && V + D >= NV) {
      cread<(V + D - NV) * 4 + 2>();
      cread<(V + D - NV) * 4 + 3>();
      if constexpr (V == NV - 1) cread<16>();
    }
    if constexpr (s < 8) {
      if constexpr (U == 11 && xpair(s + 1) != xpair(s)) asm volatile("v_add_u32 %0, %1, %0" : "+v"(xa) : "n"((xpair(s + 1) - xpair(s)) * 2 * SB));
      if constexpr (!B && U == 13) __builtin_amdgcn_s_barrier();
      if constexpr (B && U == 9 && chk(s)) asm volatile("ds_read_u8 %0, %1 offset:%2" : "+v"(fv) : "v"(fa), "n"(s == 6 ? 0 : 8));
      unsigned n = 1u << 18;
      if constexpr (B && U == 13 && chk(s)) {
        asm volatile(
            "v_cmp_gt_u32 vcc, %3, %0\n\ts_cbranch_vccnz .Lw%=\n\t.subsection 1\n.Lw%=:\n\tds_read_u8 %0, %2 offset:%4\n\ts_waitcnt lgkmcnt(0)\n\t"
            "v_cmp_gt_u32 vcc, %3, %0\n\t" SAFE_OUT(".Lr%=") "s_cbranch_vccnz .Lw%=\n\ts_branch .Lr%=\n\t.subsection 0\n.Lr%=:"
            : "+v"(fv), NOP_(n)
            : "v"(fa), "n"(s == 6 ? 4 : s + 2), "n"(s == 6 ? 0 : 8)
            : "vcc", "scc");
      }
    }
    if constexpr (B && U == 15) asm volatile("v_mov_b32 %0, %1" : "+v"(fv) : "n"(s + 1));
    if constexpr (B && ((U == 0 && s > 0) || V == NV - 1)) asm volatile("ds_write_b8 %0, %1" ::"v"(fw), "v"(fv));
    __builtin_amdgcn_sched_barrier(0);
  }
  template <int... V>
  DEVI void units(std::integer_sequence<int, V...>) {
    (unit<V>(), ...);
  }
  DEVI void first() {
    if constexpr (!B) {
      xread<0, 0>();
      xread<0, 1>();
    }
    wread<0>();
    wread<1>();
    wread<2>();
    wread<3>();
    asm volatile("s_waitcnt lgkmcnt(0)");
  }
  DEVI void run() {
    units(std::make_integer_sequence<int, NV>{});
#pragma unroll
    for (int m = 0; m < 2; ++m)
#pragma unroll
      for (int n = 0; n < 9; ++n) asm volatile("" : "+v"(acc[m][n]));
  }
};

struct CMath {
  static constexpr int D = 4, R = 6, SB = K3C::SB;
  s16x8 wf[R], xf[2][2];
  f32x4 (&acc)[2][9];
  unsigned wa[2], xa[2][2];
  DEVI CMath(f32x4 (&a)[2][9]) : acc(a) {}
  static constexpr int xop(int u, bool last) { return (u + D) % 9 < 2 && !(last && u + D >= 18) ? 1 : 0; }
  static constexpr int newer(int U, bool last) {
    int v0 = (U % 9 == 8 ? U : U + 1) - D, n = 0;
    if (U % 9 != 0) n += xop((v0 + 18) % 18, last && v0 >= 0);
    for (int v = v0 + 1; v < U; ++v) n += (last && v + D >= 18 ? 0 : 1) + xop((v + 18) % 18, last && v >= 0);
    return n;
  }
  template <int SL, int U>
  DEVI void wread() {
    asm volatile("ds_read_b128 %0, %1 offset:%2" : "=v"(wf[U % R]) : "v"(wa[SL / 2]), "n"((SL & 1) * SB + U * 1024));
  }
  template <int SL, int H, int M>
  DEVI void xread() {
    asm volatile("ds_read_b128 %0, %1 offset:%2" : "=v"(xf[H][M]) : "v"(xa[SL / 2][H]), "n"((SL & 1) * SB + M * 2048));
  }
  template <int U, int M_>
  DEVI void mf() {
    constexpr int M = ((U % 9) & 1) ? 1 - M_ : M_;
    acc[M][U % 9] = __builtin_amdgcn_mfma_f32_16x16x32_bf16(wf[U % R], xf[U / 9][M], acc[M][U % 9], 0, 0, 0);
    __builtin_amdgcn_sched_barrier(0);
  }
  template <int SL, bool BAR, bool LAST, int U>
  DEVI void unit() {
    constexpr int PU = (U + D) % 18, PS = U + D < 18 ? SL : (SL + 1) % 4;
    if constexpr ((U % 9) % 2 == 0) asm volatile("s_waitcnt lgkmcnt(%0)" ::"n"(newer(U, LAST)));
    __builtin_amdgcn_sched_barrier(0);
    mf<U, 0>();
    if constexpr (!(LAST && U + D >= 18)) wread<PS, PU>();
    __builtin_amdgcn_sched_barrier(0);
    mf<U, 1>();
    if constexpr (xop(U, LAST)) xread<PS, PU / 9, PU % 9>();
    if constexpr (BAR && U == 13) __builtin_amdgcn_s_barrier();
    __builtin_amdgcn_sched_barrier(0);
  }
  template <int SL, bool BAR, bool LAST, int... U>
  DEVI void units(std::integer_sequence<int, U...>) {
    (unit<SL, BAR, LAST, U>(), ...);
  }
  template <int SL, bool BAR = true, bool LAST = false>
  DEVI void stage() {
    units<SL, BAR, LAST>(std::make_integer_sequence<int, 18>{});
  }
  DEVI void setup(unsigned lds0, int wm, int lane) {
    const int g = lane >> 4, r = lane & 15;
#pragma unroll
    for (int i = 0; i < 2; ++i) {
      wa[i] = lds0 + (unsigned)(i * 2 * SB + K3C::XB + lane * 16);
#pragma unroll
      for (int h = 0; h < 2; ++h) xa[i][h] = lds0 + (unsigned)(i * 2 * SB + (wm * 32 + r) * 128 + (((4 * h + g) ^ ((r >> 1) & 7)) * 16));
    }
    asm volatile("" : "+v"(wa[0]), "+v"(wa[1]), "+v"(xa[0][0]), "+v"(xa[0][1]), "+v"(xa[1][0]), "+v"(xa[1][1]));
  }
  DEVI void first() {
    xread<2, 0, 0>();
    xread<2, 0, 1>();
    wread<2, 0>();
    wread<2, 1>();
    wread<2, 2>();
    wread<2, 3>();
    asm volatile("s_waitcnt lgkmcnt(0)");
  }
};

constexpr int ZQ = 4;
constexpr int NR1 = 3;
DEVI void p3_g0(char* smem, char* __restrict__ ws, int layer, int mi, int ni, int wm, int lane MK_ARG) {
  constexpr int SB = K3C::SB;
  const unsigned lds0 = (unsigned)(size_t)smem;
  const int g = lane >> 4, r = lane & 15;
  const unsigned lw = lds0 + (unsigned)(LW_OFF + layer * 4), me = 1u << (8 * wm);
  f32x4 ac[2][9];
#pragma unroll
  for (int mt = 0; mt < 2; ++mt)
#pragma unroll
    for (int j = 0; j < 9; ++j) ac[mt][j] = f32x4{0.f, 0.f, 0.f, 0.f};
  unsigned ra = lds0 + (unsigned)(K3_ST_OFF + (wm * 32 + g * 8) * 4), ca = lds0 + (unsigned)(K3_C2_OFF + r * 8);
  asm volatile("" : "+v"(ra), "+v"(ca));
  s16x8 xc[4][2][2];
  UMath<false> gm(ac, xc);
  gm.ra = ra;
  gm.ca = ca;
  gm.xa = lds0 + (unsigned)(g * 2048 + (wm * 32 + 8 * (r >> 2) + (r & 3)) * 16);
  gm.wa = lds0 + (unsigned)(K3C::XB + (g * 8 + (r & 7)) * K3C::P + (r >> 3) * 16);
  unsigned adw = lw + LW_AD * 4, mev = vconst(me), po = (unsigned)(ni * 1024 + lane * 4);
  const char* zp = uni_ptr(ws + OFF_ZP + (size_t)layer * SZ_ZP + (size_t)mi * 16384 + wm * 256);
  asm volatile("" : "+v"(gm.xa), "+v"(gm.wa), "+v"(adw), "+v"(po));
  asm volatile("" : "+s"(zp));
  place();
  bar();
  gm.first();
  gm.run();
  asm volatile("s_mov_b64 exec, 1\n\tds_add_u32 %0, %1\n\ts_mov_b64 exec, -1" ::"v"(adw), "v"(mev) : "memory");
  MK(11);
  f32x2 z[9][4];
  f32x2 sm[4], sq[4];
  {
    f32x4 (&rc)[4][2] = gm.rc;
    f32x2 (&cs)[9] = gm.cs;
    asm volatile("s_waitcnt lgkmcnt(1)"
                 : "+v"(rc[0][0]), "+v"(rc[0][1]), "+v"(rc[1][0]), "+v"(rc[1][1]), "+v"(rc[2][0]), "+v"(rc[2][1]), "+v"(rc[3][0]), "+v"(rc[3][1]));
#pragma unroll
    for (int j = 0; j < 9; ++j) asm volatile("" : "+v"(cs[j]));
    auto RC = [&](int k, int i) { return f32x2{rc[k][i / 2][(i % 2) * 2], rc[k][i / 2][(i % 2) * 2 + 1]}; };
#pragma unroll
    for (int j = 0; j < 9; ++j) {
      f32x2 u[4], w[4];
#pragma unroll
      for (int i = 0; i < 4; ++i) u[i] = __builtin_elementwise_fma(RC(0, i), cs[j], f32x2{ac[i / 2][j][(i % 2) * 2], ac[i / 2][j][(i % 2) * 2 + 1]});
#pragma unroll
      for (int i = 0; i < 4; ++i) w[i] = u[i] * u[i];
#pragma unroll
      for (int i = 0; i < 4; ++i) w[i] = __builtin_elementwise_fma(w[i], RC(3, i), RC(2, i));
#pragma unroll
      for (int i = 0; i < 4; ++i) w[i] = u[i] * w[i];
      __builtin_amdgcn_sched_barrier(0);
#pragma unroll
      for (int i = 0; i < 4; ++i) w[i] = f32x2{__builtin_amdgcn_exp2f(w[i][0]), __builtin_amdgcn_exp2f(w[i][1])};
      __builtin_amdgcn_sched_barrier(0);
#pragma unroll
      for (int i = 0; i < 4; ++i) w[i] = __builtin_elementwise_fma(w[i], RC(1, i), RC(1, i));
      __builtin_amdgcn_sched_barrier(0);
#pragma unroll
      for (int i = 0; i < 4; ++i) w[i] = f32x2{__builtin_amdgcn_rcpf(w[i][0]), __builtin_amdgcn_rcpf(w[i][1])};
      __builtin_amdgcn_sched_barrier(0);
#pragma unroll
      for (int i = 0; i < 4; ++i) z[j][i] = u[i] * w[i];
#pragma unroll
      for (int i = 0; i < 4; ++i) {
        if (j == 1) sm[i] = z[0][i] + z[1][i];
        if (j > 1) sm[i] += z[j][i];
      }
#pragma unroll
      for (int i = 0; i < 4; ++i) sq[i] = j ? __builtin_elementwise_fma(z[j][i], z[j][i], sq[i]) : z[j][i] * z[j][i];
      asm volatile("" : "+v"(z[j][0]), "+v"(z[j][1]), "+v"(z[j][2]), "+v"(z[j][3]));
      __builtin_amdgcn_sched_barrier(0);
    }
  }
  f32x4 e;
  {
    float v[4][4], d[2][4], f[4];
#pragma unroll
    for (int i = 0; i < 4; ++i) {
      v[i][0] = sm[i][0];
      v[i][1] = sq[i][0];
      v[i][2] = sm[i][1];
      v[i][3] = sq[i][1];
    }
    asm volatile("s_nop 1" : "+v"(v[3][1]), "+v"(v[3][3]), "+v"(v[2][1]), "+v"(v[2][3]));
#pragma unroll
    for (int i = 0; i < 2; ++i)
#pragma unroll
      for (int c = 0; c < 4; ++c)
        asm volatile("v_add_f32_dpp %0, %1, %1 row_ror:8 row_mask:0xf bank_mask:0x3\n\tv_add_f32_dpp %0, %2, %2 row_ror:8 row_mask:0xf bank_mask:0xc"
                     : "=&v"(d[i][c])
                     : "v"(v[i][c]), "v"(v[i + 2][c]));
#pragma unroll
    for (int c = 0; c < 4; ++c)
      asm volatile("v_add_f32_dpp %0, %1, %1 row_shl:4 row_mask:0xf bank_mask:0x5\n\tv_add_f32_dpp %0, %2, %2 row_shr:4 row_mask:0xf bank_mask:0xa"
                   : "=&v"(f[c])
                   : "v"(d[0][c]), "v"(d[1][c]));
    asm volatile(
        "v_add_f32_dpp %0, %0, %0 quad_perm:[1,0,3,2] row_mask:0xf bank_mask:0xf\n\t"
        "v_add_f32_dpp %1, %1, %1 quad_perm:[1,0,3,2] row_mask:0xf bank_mask:0xf\n\t"
        "v_add_f32_dpp %2, %2, %2 quad_perm:[1,0,3,2] row_mask:0xf bank_mask:0xf\n\t"
        "v_add_f32_dpp %3, %3, %3 quad_perm:[1,0,3,2] row_mask:0xf bank_mask:0xf\n\t"
        "v_add_f32_dpp %0, %0, %0 quad_perm:[2,3,0,1] row_mask:0xf bank_mask:0xf\n\t"
        "v_add_f32_dpp %1, %1, %1 quad_perm:[2,3,0,1] row_mask:0xf bank_mask:0xf\n\t"
        "v_add_f32_dpp %2, %2, %2 quad_perm:[2,3,0,1] row_mask:0xf bank_mask:0xf\n\t"
        "v_add_f32_dpp %3, %3, %3 quad_perm:[2,3,0,1] row_mask:0xf bank_mask:0xf"
        : "+v"(f[0]), "+v"(f[1]), "+v"(f[2]), "+v"(f[3]));
    e = f32x4{f[0], __uint_as_float(__float_as_uint(f[1]) & 0x7fffffffu), f[2], __uint_as_float(__float_as_uint(f[3]) & 0x7fffffffu)};
  }
  asm volatile("s_mov_b64 exec, %3\n\tglobal_store_dwordx4 %0, %1, %2 sc1\n\ts_mov_b64 exec, -1\n\ts_nop 0" ::"v"(po), "v"(e), "s"(zp),
               "s"(0x1111111111111111ull)
               : "memory");
  asm volatile("s_mov_b64 exec, 1\n\tds_add_u32 %0, %1\n\ts_mov_b64 exec, -1" ::"v"(adw), "v"(mev) : "memory");
  __builtin_amdgcn_s_setprio(2);
  MK(12);
  const char* dst = uni_ptr(ws + OFF_Z2 + (size_t)layer * SZ_Z + (size_t)ni * S_ * 288 + (size_t)(mi * 4 + wm) * 9216 + 4096);
  const unsigned lane16 = (unsigned)lane * 16u;
  u32x4 wb[9];
  {
    static_assert(b_k(7) == 2 && b_k(8) == 3, "order");
    const char* wp = uni_ptr(ws + OFF_PI + (size_t)ni * (18 * K3C::WB) + 11 * K3C::WB + wm * 9216 + 4096);
#pragma unroll
    for (int j = 0; j < 8; ++j) asm volatile("global_load_dwordx4 %0, %1, %2 offset:%3 sc1" : "=v"(wb[j]) : "v"(lane16), "s"(wp), "n"(j * 1024 - 4096) : "memory");
    asm volatile("global_load_dwordx4 %0, %1, %2 sc1" : "=v"(wb[8]) : "v"(lane16 + 4096u), "s"(wp) : "memory");
  }
  u32x4 y[9];
  {
    const unsigned src = (unsigned)(g * 4096 + r * 16);
    unsigned zw = lds0 + (unsigned)(K3_ZC_OFF + (wm * 32 + 2 * r + (g & 1)) * 4), zr = lds0 + (unsigned)(K3_ZC_OFF + (wm * 32 + g * 8) * 4);
    asm volatile("" : "+v"(zw), "+v"(zr));
    f32x4 a, b, c, d;
    __builtin_amdgcn_s_sleep(6);
#pragma unroll 1
    for (unsigned spin = 0; spin < SPIN_MEM; ++spin) {
      asm volatile(
          "global_load_dwordx4 %0, %4, %5 sc1\n\t"
          "global_load_dwordx4 %1, %4, %5 offset:1024 sc1\n\t"
          "global_load_dwordx4 %2, %4, %5 offset:2048 sc1\n\t"
          "global_load_dwordx4 %3, %4, %5 offset:3072 sc1\n\t"
          "s_waitcnt vmcnt(0)"
          : "=&v"(a), "=&v"(b), "=&v"(c), "=&v"(d)
          : "v"(src), "s"(zp)
          : "memory");
      const unsigned bad = (__float_as_uint(a[1]) | __float_as_uint(a[3]) | __float_as_uint(b[1]) | __float_as_uint(b[3]) |
                            __float_as_uint(c[1]) | __float_as_uint(c[3]) | __float_as_uint(d[1]) | __float_as_uint(d[3])) >> 31;
      if (!__any(bad)) break;
    }
    MK(13);
    float s, q, zmb, zi;
    red16(a, b, c, d, s, q);
    ln_finish(s, q, 1.0f / (float)FH_, zmb, zi);
    f32x4 R[2], M[2];
    asm volatile(
        "ds_write2st64_b32 %4, %5, %6 offset1:2\n\t"
        "ds_read_b128 %0, %7\n\tds_read_b128 %2, %7 offset:512\n\tds_read_b128 %1, %7 offset:16\n\tds_read_b128 %3, %7 offset:528\n\t"
        "s_waitcnt lgkmcnt(0)"
        : "=&v"(R[0]), "=&v"(R[1]), "=&v"(M[0]), "=&v"(M[1])
        : "v"(zw), "v"(zi), "v"(-zmb * zi), "v"(zr)
        : "memory");
    MK(28);
    auto st = [&](int j) {
      if (j < 8) asm volatile("global_store_dwordx4 %0, %1, %2 offset:%3 sc1\n\ts_nop 1" ::"v"(lane16), "v"(y[j]), "s"(dst), "n"(j * 1024 - 4096) : "memory");
      else asm volatile("global_store_dwordx4 %0, %1, %2 sc1\n\ts_nop 1" ::"v"(lane16 + 4096u), "v"(y[j]), "s"(dst) : "memory");
    };
#pragma unroll
    for (int j = 0; j < 9; ++j) {
      {
        float t[8];
#pragma unroll
        for (int k = 0; k < 8; ++k) {
          t[k] = __builtin_fmaf(z[j][k / 2][k % 2], R[k / 4][k % 4], M[k / 4][k % 4]);
          asm volatile("" : "+v"(t[k]));
        }
        y[j] = u32x4{pack2(t[0], t[1]), pack2(t[2], t[3]), pack2(t[4], t[5]), pack2(t[6], t[7])};
      }
      st(j);
      __builtin_amdgcn_sched_barrier(0);
    }
    MK(29);
  }
  lds_inc(lw + LW_AD * 4, me);
  MK(24);
  __builtin_amdgcn_s_setprio(2);
  lds_spin4<2>(lw + LW_S * 4, 4u + (unsigned)(wm >> 1));
  {
    const unsigned fa = lds0 + (unsigned)((wm >> 1) * SB + K3C::XB + (wm & 1) * 9216) + lane16;
#pragma unroll
    for (int j = 0; j < 9; ++j) {
      asm volatile("ds_write_b128 %0, %1 offset:%2" ::"v"(fa), "v"(wb[j]), "n"(j * 1024) : "memory");
    }
    lds_inc(lw + LW_AD * 4, me);
  }
  announce(lw + LW_Z * 4, 4u, ws, ((mi >= ZQ) ? CN_Z2 : CN_Z) + layer, lane * 16 + ni, lane < 16);
  lds_inc(lw + LW_AD * 4, me);
  MK(27);
  f32x4 acc[2][9];
  {
    float bs[2];
    asm volatile("ds_read2_b32 %0, %1 offset1:16\n\ts_waitcnt lgkmcnt(0)"
                 : "=v"(*reinterpret_cast<f32x2*>(bs))
                 : "v"(lds0 + (unsigned)(K3_BS_OFF + (wm * 32 + r) * 4))
                 : "memory");
    lds_spin4<2>(lw + LW_S * 4, 6u + (unsigned)(wm >> 1));
    const unsigned fa = lds0 + (unsigned)((2 + (wm >> 1)) * SB + K3C::XB + (wm & 1) * 9216) + lane16;
#pragma unroll
    for (int j = 0; j < 9; ++j) {
      asm volatile("ds_write_b128 %0, %1 offset:%2" ::"v"(fa), "v"(y[j]), "n"(j * 1024) : "memory");
    }
    lds_inc(lw + LW_AD * 4, me);
#pragma unroll
    for (int mt = 0; mt < 2; ++mt) {
      f32x4 b4 = {bs[mt], bs[mt], bs[mt], bs[mt]};
      asm volatile("" : "+v"(b4));
#pragma unroll
      for (int j = 0; j < 9; ++j) acc[mt][j] = b4;
    }
  }
  u32x4 z1[9];
  const char* hp = uni_ptr(ws + OFF_H + (size_t)layer * SZ_Z + (size_t)((mi * 2 + (wm >> 1)) * 288 + ni * 18) * 1024);
  unsigned ho = (unsigned)((lane >> 5) * 1024 + ((wm & 1) * 32 + (lane & 31)) * 16);
  unsigned zb = lds0 + (unsigned)(wm * 4096) + lane16, zf = lw + (unsigned)(LW_S * 4 + wm);
  asm volatile("" : "+v"(ho), "+v"(zb), "+v"(zf));
  asm volatile("" : "+s"(hp));
  {
    CMath gm(acc);
    gm.setup(lds0, wm, lane);
    unsigned za = lds0 + (unsigned)(K3_Z1_OFF + wm * 5120) + lane16;
    asm volatile("" : "+v"(za));
    MK(15);
    lds_spin4<2>(lw + LW_L * 4, 8u);
    lds_spin4(lw + LW_AD * 4, 6u);
    MK(17);
    gm.first();
    __builtin_amdgcn_s_setprio(0);
    gm.stage<2, false>();
    gm.stage<3>();
    place();
#pragma unroll 1
    for (int t = 0; t < 7; ++t) {
      gm.stage<0>();
#pragma unroll
      for (int j = 0; j < NR1; ++j) asm volatile("ds_read_b128 %0, %1 offset:%2" : "=v"(z1[j]) : "v"(za), "n"(j * 1024));
      gm.stage<1>();
#pragma unroll
      for (int j = 0; j < NR1; ++j) asm volatile("" : "+v"(z1[j]));
      gm.stage<2>();
      gm.stage<3>();
    }
    gm.stage<0>();
    gm.stage<1, false, true>();
#pragma unroll
    for (int j = 0; j < 18; ++j) asm volatile("" : "+v"(acc[j / 9][j % 9]));
  }
  MK(16);
  if (NR1 < 5) lds_inc(lw + LW_AD * 4, me);
  unsigned zv;
  u32x4 zl[4];
#pragma unroll
  for (int j = 0; j < 9; ++j) {
    const bool sec = j >= NR1 + 3;
    if (j == NR1 || j == NR1 + 4) {
      unsigned n = 1u << 18;
      asm volatile(
          "s_waitcnt lgkmcnt(0)\n\tv_cmp_gt_u32 vcc, %8, %0\n\ts_cbranch_vccnz .Lw%=\n\t.subsection 1\n.Lw%=:\n\tds_read_u8 %0, %6\n\tds_read_b128 %2, %7 offset:%9\n\t"
          "ds_read_b128 %3, %7 offset:%9+1024\n\tds_read_b128 %4, %7 offset:%9+2048\n\tds_read_b128 %5, %7 offset:%9+3072\n\ts_waitcnt lgkmcnt(0)\n\t"
          "v_cmp_gt_u32 vcc, %8, %0\n\t" SAFE_OUT(".Lr%=") "s_cbranch_vccnz .Lw%=\n\ts_branch .Lr%=\n\t.subsection 0\n.Lr%=:"
          : "+v"(zv), NOP_(n), "+v"(zl[0]), "+v"(zl[1]), "+v"(zl[2]), "+v"(zl[3])
          : "v"(zf), "v"(zb), "n"(sec ? 11 : 10), "n"(sec ? SB : 0)
          : "memory", "vcc", "scc");
#pragma unroll
      for (int k = 0; k < 4; ++k)
        if (j + k < 9) z1[j + k] = zl[k];
    }
    if (j == NR1 - 1 || (j == NR1 + 3 && j < 8))
      asm volatile("ds_read_u8 %0, %5\n\tds_read_b128 %1, %6 offset:%7\n\tds_read_b128 %2, %6 offset:%7+1024\n\tds_read_b128 %3, %6 offset:%7+2048\n\t"
                   "ds_read_b128 %4, %6 offset:%7+3072"
                   : "=&v"(zv), "=&v"(zl[0]), "=&v"(zl[1]), "=&v"(zl[2]), "=&v"(zl[3])
                   : "v"(zf), "v"(zb), "n"(sec ? SB : 0)
                   : "memory");
    asm volatile("" : "+v"(z1[j]));
    u32x2 h[2];
#pragma unroll
    for (int mt = 0; mt < 2; ++mt) {
      const f32x4 a = acc[mt][j];
      h[mt] = u32x2{pack2v(f32x2{bf_lo(z1[j][2 * mt]), bf_hi(z1[j][2 * mt])} * f32x2{a[0], a[1]}),
                    pack2v(f32x2{bf_lo(z1[j][2 * mt + 1]), bf_hi(z1[j][2 * mt + 1])} * f32x2{a[2], a[3]})};
    }
    const auto s0 = __builtin_amdgcn_permlane16_swap(h[0][0], h[1][0], false, false);
    const auto s1 = __builtin_amdgcn_permlane16_swap(h[0][1], h[1][1], false, false);
    const u32x4 v = {s0[0], s1[0], s0[1], s1[1]};
    asm volatile("global_store_dwordx4 %0, %1, %2 offset:%3 sc1\n\ts_nop 1" ::"v"(ho), "v"(v), "s"(hp + (j / 2) * 4096), "n"((j % 2) * 2048) : "memory");
    __builtin_amdgcn_sched_barrier(0);
  }
  MK(18);
}

DEVI void k3_const(char* smem, char* ws, const u16* b_sp, int layer, int bid, int mi, int ni, int wm) {
  const int lane = lane_id(), l = lane < 54 ? lane : 53, p = l / 18;
  const char* kp = uni_ptr(ws + OFF_CSK + bid * 2048 + wm * 512);
  const unsigned q16 = (unsigned)(l - p * 18) * 16u;
  unsigned bs;
  f32x4 t;
  lds_spin((unsigned)(size_t)smem + (unsigned)(LW_OFF + (layer ? LW_T + 1 : LW_HELLO) * 4), layer ? 12u : 2u);
  asm volatile("global_load_ushort %0, %1, %2" : "=v"(bs) : "v"((unsigned)(lane & 31) * 2u), "s"(uni_ptr((const char*)(b_sp + mi * 128 + wm * 32))) : "memory");
  if (layer) {
    asm volatile("global_load_dwordx4 %0, %1, %2 sc1\n\ts_waitcnt vmcnt(0)" : "=&v"(t) : "v"(q16), "s"(kp) : "memory");
  } else {
    const char* cp = uni_ptr(ws + OFF_CSP + ni * 1152 + wm * 288);
    f32x4 a[6];
#pragma unroll
    for (int i = 0; i < 6; ++i)
      asm volatile("global_load_dwordx4 %0, %1, %2 sc1" : "=v"(a[i]) : "v"(q16 + (unsigned)(p * (6 * F_ * 4))), "s"(cp + i * (F_ * 4)) : "memory");
    asm volatile("s_waitcnt vmcnt(0)" : "+v"(a[0]), "+v"(a[1]), "+v"(a[2]), "+v"(a[3]), "+v"(a[4]), "+v"(a[5]));
    t = ((a[0] + a[1]) + (a[2] + a[3])) + (a[4] + a[5]);
#pragma unroll
    for (int j = 0; j < 4; ++j)
      t[j] += __int_as_float(__builtin_amdgcn_ds_bpermute((lane + 18) * 4, __float_as_int(t[j]))) +
              __int_as_float(__builtin_amdgcn_ds_bpermute((lane + 36) * 4, __float_as_int(t[j])));
  }
  asm volatile("" : "+v"(bs));
  const unsigned lds0 = (unsigned)(size_t)smem;
  f32x4 t0 = wm < 2 ? f32x4{t[0], t[0], t[1], t[1]} : t, t1 = wm < 2 ? f32x4{t[2], t[2], t[3], t[3]} : t;
  unsigned a0 = lds0 + (unsigned)(wm < 2 ? K3_C2_OFF + wm * 576 + lane * 32 : K3_C1_OFF + (wm - 2) * 288 + lane * 16), a1 = a0 + (wm < 2 ? 16u : 0u);
  unsigned ab = lds0 + (unsigned)(K3_BS_OFF + (wm * 32 + (lane & 31)) * 4);
  bs <<= 16;
  asm volatile("" : "+v"(t0), "+v"(t1), "+v"(a0), "+v"(a1), "+v"(ab), "+v"(bs));
  bar();
  asm volatile("ds_write_b32 %4, %5\n\ts_mov_b64 exec, 0x3ffff\n\tds_write_b128 %0, %2\n\tds_write_b128 %1, %3\n\ts_mov_b64 exec, -1" ::"v"(a0), "v"(a1),
               "v"(t0), "v"(t1), "v"(ab), "v"(bs)
               : "memory");
  if (layer == 0 && lane < 24) asm volatile("global_store_dwordx4 %0, %1, %2\n\ts_nop 1" ::"v"((unsigned)lane * 16u), "v"(t), "s"(kp) : "memory");
}

DEVI void p3_g1(char* smem, char* __restrict__ ws, int layer, int mi, int ni, int wm, int lane MK_ARG) {
  const unsigned lds0 = (unsigned)(size_t)smem;
  const int g = lane >> 4, r = lane & 15;
  const unsigned lw = lds0 + (unsigned)(LW_OFF + layer * 4);
  const unsigned xrd = lds0 + (unsigned)(g * 2048 + (wm * 32 + r) * 16);
  s16x8 xc[4][2][2];
  {
    const char* xs = uni_ptr(ws + OFF_XS + (size_t)layer * 2 * SZ_XS + (size_t)(mi * 2 + (wm >> 1)) * 4096 + (wm & 1) * 256);
    f32x4 a, b;
    auto pick = [&](int t) {
      bar();
      asm volatile(
          "ds_read_b128 %0, %4\n\tds_read_b128 %1, %4 offset:256\n\tds_read_b128 %2, %4 offset:8192\n\tds_read_b128 %3, %4 offset:8448\n\t"
          "s_waitcnt lgkmcnt(0)"
          : "=&v"(xc[t][0][0]), "=&v"(xc[t][0][1]), "=&v"(xc[t][1][0]), "=&v"(xc[t][1][1])
          : "v"(xrd + (unsigned)(t * K3C::SB))
          : "memory");
    };
    __builtin_amdgcn_s_setprio(2);
    pick(0);
    pick(1);
    asm volatile("global_load_dwordx4 %0, %2, %3 sc1\n\tglobal_load_dwordx4 %1, %2, %3 offset:512 sc1"
                 : "=&v"(a), "=&v"(b)
                 : "v"((unsigned)(g * 1024 + r * 16)), "s"(xs)
                 : "memory");
    pick(2);
    pick(3);
    bar();
    asm volatile("s_waitcnt vmcnt(0)" : "+v"(a), "+v"(b) : : "memory");
    float s, q, mb, rinv;
    red8(a, b, s, q);
    bar();
    asm volatile("" : "+v"(s), "+v"(q));
    ln_finish(s, q, 1.0f / (float)D_, mb, rinv);
    asm volatile("" : "+v"(mb), "+v"(rinv));
    bar();
    asm volatile("" : "+v"(mb), "+v"(rinv));
    const float k0 = -2.0f * 0.7978845608028654f * 1.4426950408889634f, k1 = k0 * 0.044715f;
    float sd = __builtin_amdgcn_rcpf(rinv), c0 = k0 * rinv, c1 = k1 * rinv * rinv * rinv;
    asm volatile("" : "+v"(sd));
    asm volatile("" : "+v"(c0));
    asm volatile("" : "+v"(c1));
    const float e0 = lane < 32 ? -mb : c0, e1 = lane < 32 ? sd : c1;
    asm volatile("ds_write2st64_b32 %0, %1, %2 offset1:2" ::"v"(
                     lds0 + (unsigned)(K3_ST_OFF + (g >> 1) * 1024 + (wm * 32 + 2 * r + (g & 1)) * 4)),
                 "v"(e0), "v"(e1)
                 : "memory");
    bar();
    asm volatile("s_waitcnt lgkmcnt(0)" ::: "memory");
    bar();
    __builtin_amdgcn_s_setprio(0);
  }
  f32x4 ac[2][9];
#pragma unroll
  for (int mt = 0; mt < 2; ++mt)
#pragma unroll
    for (int j = 0; j < 9; ++j) ac[mt][j] = f32x4{0.f, 0.f, 0.f, 0.f};
  {
    UMath<true> gm(ac, xc);
    gm.xa = xrd;
    gm.wa = lds0 + (unsigned)(K3C::XB + (g * 8 + (r & 7)) * K3C::P + (r >> 3) * 16);
    gm.fa = lw + (unsigned)(LW_AD * 4 + (lane & 3));
    gm.fw = lw + (unsigned)(LW_S * 4 + wm);
    gm.fv = 0u;
    asm volatile("" : "+v"(gm.fa), "+v"(gm.fw), "+v"(gm.fv), "+v"(gm.xa), "+v"(gm.wa));
    __builtin_amdgcn_s_sleep(40);
    lds_spin4<4>(lw + LW_L * 4, 4u);
    gm.first();
    place();
    lds_spin4(lw + LW_AD * 4, 2u);
    __builtin_amdgcn_s_setprio(1);
    gm.run();
    __builtin_amdgcn_s_setprio(0);
  }
  MK(14);
  f32x2 rc[4];
  f32x4 cs[9];
  const int l2 = lane_id();
  {
    const unsigned ra = lds0 + (unsigned)(K3_ST_OFF + (wm * 32 + (l2 & 15)) * 4), ca = lds0 + (unsigned)(K3_C1_OFF + (l2 & 48));
#pragma unroll
    for (int k = 0; k < 4; ++k) asm volatile("ds_read2_b32 %0, %1 offset1:16" : "=v"(rc[k]) : "v"(ra + (unsigned)(k * 512)) : "memory");
#pragma unroll
    for (int j = 0; j < 9; ++j) asm volatile("ds_read_b128 %0, %1 offset:%2" : "=v"(cs[j]) : "v"(ca), "n"(j * 64) : "memory");
    asm volatile("s_waitcnt lgkmcnt(0)" : "+v"(rc[0]), "+v"(rc[1]), "+v"(rc[2]), "+v"(rc[3]));
#pragma unroll
    for (int j = 0; j < 9; ++j) asm volatile("" : "+v"(cs[j]));
  }
  const unsigned za = lds0 + (unsigned)(K3_Z1_OFF + wm * 5120 + l2 * 16);
  u32x4 z1[9];
  unsigned zf;
#pragma unroll
  for (int t = 0; t < 18; ++t) {
    const int j = t / 2, mt = t % 2;
    float u[4], w[4];
#define Q4(A, B, C, D) A "\n\t" B "\n\t" C "\n\t" D "\n\t"
    asm volatile(Q4("v_fma_f32 %0, %8, %12, %16", "v_fma_f32 %1, %8, %13, %17", "v_fma_f32 %2, %8, %14, %18", "v_fma_f32 %3, %8, %15, %19")
                 Q4("v_mul_f32 %4, %0, %0", "v_mul_f32 %5, %1, %1", "v_mul_f32 %6, %2, %2", "v_mul_f32 %7, %3, %3")
                 Q4("v_fma_f32 %4, %4, %11, %10", "v_fma_f32 %5, %5, %11, %10", "v_fma_f32 %6, %6, %11, %10", "v_fma_f32 %7, %7, %11, %10")
                 Q4("v_mul_f32 %4, %4, %0", "v_mul_f32 %5, %5, %1", "v_mul_f32 %6, %6, %2", "v_mul_f32 %7, %7, %3")
                 Q4("v_exp_f32 %4, %4", "v_exp_f32 %5, %5", "v_exp_f32 %6, %6", "v_exp_f32 %7, %7")
                 Q4("v_fma_f32 %4, %4, %9, %9", "v_fma_f32 %5, %5, %9, %9", "v_fma_f32 %6, %6, %9, %9", "v_fma_f32 %7, %7, %9, %9")
                 Q4("v_rcp_f32 %4, %4", "v_rcp_f32 %5, %5", "v_rcp_f32 %6, %6", "v_rcp_f32 %7, %7")
                 Q4("v_mul_f32 %4, %4, %0", "v_mul_f32 %5, %5, %1", "v_mul_f32 %6, %6, %2", "v_mul_f32 %7, %7, %3")
                 : "=&v"(u[0]), "=&v"(u[1]), "=&v"(u[2]), "=&v"(u[3]), "=&v"(w[0]), "=&v"(w[1]), "=&v"(w[2]), "=&v"(w[3])
                 : "v"(rc[0][mt]), "v"(rc[1][mt]), "v"(rc[2][mt]), "v"(rc[3][mt]), "v"(cs[j][0]), "v"(cs[j][1]), "v"(cs[j][2]), "v"(cs[j][3]),
                   "v"(ac[mt][j][0]), "v"(ac[mt][j][1]), "v"(ac[mt][j][2]), "v"(ac[mt][j][3]));
    z1[j][2 * mt] = pack2(w[0], w[1]);
    z1[j][2 * mt + 1] = pack2(w[2], w[3]);
    if (mt && j < NR1) asm volatile("ds_write_b128 %0, %1 offset:%2" ::"v"(za), "v"(z1[j]), "n"(j * 1024) : "memory");
    if (t == 9) asm volatile("s_waitcnt lgkmcnt(0)" ::: "memory");
    __builtin_amdgcn_sched_barrier(0);
    if (t == 2 * ZQ - 7)
      asm volatile("global_load_dword %0, %1, %2 sc1"
                   : "=v"(zf)
                   : "v"(vconst(0u)), "s"(uni_ptr(ws + OFF_CNT + (size_t)((CN_Z2 + layer) * 256 + mi * 16 + ni) * 128))
                   : "memory");
    if (t == 2 * ZQ - 4) {
      asm volatile("s_waitcnt vmcnt(0)" : "+v"(zf) : : "memory");
      if (wm == 1 && __builtin_amdgcn_readfirstlane(zf) != 15u - ZQ) cnt_wait(ws, CN_Z2 + layer, mi * 16 + ni, 16u - ZQ);
    }
    bar();
    __builtin_amdgcn_sched_barrier(0);
  }
  MK(15);
#pragma unroll 1
  for (int t = 20; t < 32; ++t) bar();
  __builtin_amdgcn_s_sleep(9);
#pragma unroll
  for (int k = NR1; k < 9; ++k) {
    if (k == NR1 + 4) lds_spin4(lw + LW_AD * 4, 7u);
    asm volatile("ds_write_b128 %0, %1 offset:%2" ::"v"(lds0 + (unsigned)(wm * 4096 + l2 * 16)), "v"(z1[k]),
                 "n"(((k - NR1) / 4) * K3C::SB + ((k - NR1) % 4) * 1024)
                 : "memory");
    if (k == NR1 + 3 || k == 8) asm volatile("ds_write_b8 %0, %1" ::"v"(lw + (unsigned)(LW_S * 4 + wm)), "v"(vconst(k == NR1 + 3 ? 10u : 11u)) : "memory");
  }
}

constexpr int W5A = 22;
constexpr int NPF = 30;
struct Loader {
  u32x4 pf[NPF];
  unsigned lds0, lane16;
  int wl, lane;

  template <int R0, int N, int P0, int PE>
  DEVI void pf_load(const char* src) {
    const unsigned o = lane16 + (unsigned)(wl * 1024);
#pragma unroll
    for (int t = 0; t < N; ++t) {
      unsigned a = o + (unsigned)((P0 + 4 * t) * 1024);
      if (P0 + 4 * t + 3 >= PE && P0 + 4 * t + wl >= PE) a = lane16;
      asm volatile("global_load_dwordx4 %0, %1, %2 sc1" : "=v"(pf[R0 + t]) : "v"(a), "s"(src) : "memory");
    }
  }
  template <int R0, int N>
  DEVI void pf_pin() {
#pragma unroll
    for (int t = 0; t < N; ++t) asm volatile("" : "+v"(pf[R0 + t]));
  }
  template <int R0, int N>
  DEVI void pf_undef() {
#pragma unroll
    for (int t = 0; t < N; ++t) asm volatile("" : "=v"(pf[R0 + t]));
  }
  template <int PB, int A, int N>
  DEVI void pf_put(unsigned dst) {
    constexpr int T0 = (A - PB) / 4, T1 = (A + N - 1 - PB) / 4;
    const unsigned a = lds0 + dst + lane16 + (unsigned)((wl + PB + 4 * T0 - A + 4) * 1024) - 4096u;
#pragma unroll
    for (int t = T0; t <= T1; ++t) {
      const int p0 = PB + 4 * t;
      if ((p0 >= A || p0 + wl >= A) && (p0 + 3 < A + N || p0 + wl < A + N))
        asm volatile("ds_write_b128 %0, %1 offset:%2" ::"v"(a), "v"(pf[t]), "n"((t - T0) * 4096) : "memory");
    }
  }
  template <int N>
  DEVI void dma_run(const char* src, unsigned dst) {
    constexpr int B = N / 4, R = N % 4;
    const unsigned f = (unsigned)(wl * B + (wl < R ? wl : R)) * 1024u, o = lane16 + f, m = lds0 + dst + f;
    dma_seq<B>(src, o, m);
    if (wl < R) dma16_sc1(src + B * 1024, o, m + (unsigned)(B * 1024));
  }
  template <int V>
  DEVI void wait_w() {
    asm volatile("s_cmp_lt_u32 %0, 2\n\ts_cbranch_scc1 1f\n\ts_waitcnt vmcnt(%1)\n1:\n\ts_waitcnt vmcnt(%2)" ::"s"(wl), "n"(V - 1), "n"(V) : "memory", "scc");
  }
  template <int V>
  DEVI void wbar() {
    asm volatile("s_waitcnt vmcnt(%0) lgkmcnt(0)" ::"n"(V) : "memory");
    bar();
  }

  DEVI void p1(char* smem, char* ws, int bid, int rt, int hp MK_ARG) {
    constexpr int SB = K1C::SB;
    static_assert(K1C::XB == 18 * 1024 && K1C::WB == 14 * 1024, "K1 shape");
    const char* wp = uni_ptr(ws + OFF_PQ + (size_t)hp * (9 * K1C::WB) + 4 * K1C::WB);
    static_assert(3 * SB + K1C::XB >= P5_W + 5 * 18432, "free while the GEMM before ends");
    const char* wq = wp - 4 * K1C::WB;
    dma_run<14>(wq + 3 * K1C::WB, (unsigned)(3 * SB + K1C::XB));
    dma_run<28>(wp, (unsigned)K1_WX);
    if (wl == 3) dma16_sc1(uni_ptr(ws + OFF_CSQ + bid * 512), lane16 & 511u, lds0 + (unsigned)K1_CS_OFF);
    __builtin_amdgcn_s_setprio(0);
    wait_vm<0>();
    const bool lo = wl < 2;
    const int x0 = lo ? wl * 5 : wl * 4 + 2, w0 = lo ? wl * 3 : wl * 4 - 2;
    unsigned xo[5];
#pragma unroll
    for (int i = 0; i < 5; ++i) {
      const int c = (x0 + i) * 64 + lane;
      const int ch = c / 144;
      int gr = rt * 128 - 16 + (c - ch * 144);
      if (gr < 0) gr = 0;
      xo[i] = (unsigned)(((gr >> 6) * 72 + ch + 3 - (i & 3)) * 1024 + (gr & 63) * 16);
      asm volatile("" : "+v"(xo[i]));
    }
    const char* xp = uni_ptr(ws + OFF_X1 + SZ_X - 3072);
    wp += 2 * K1C::WB + w0 * 1024;
    auto dma_x = [&](unsigned so) {
      const unsigned m = lds0 + so + (unsigned)(x0 * 1024);
      dma4<4, true>(xp, xo[0], xo[1], xo[2], xo[3], m);
      if (lo) dma16_sc1(xp, xo[4], m + 4096u);
      xp += 8192;
    };
    auto dma_w = [&](unsigned so) {
      const unsigned m = lds0 + so + (unsigned)(K1C::XB + w0 * 1024);
      dma_seq<3>(wp, lane16, m);
      if (!lo) dma16_sc1(wp + 3072, lane16, m + 3072u);
      wp += K1C::WB;
    };
    const char* xs = uni_ptr(ws + OFF_XS + SZ_XS);
    const int g = lane >> 4, p2 = 64 + (lane & 7);
    int ra = rt * 128 - 16 + wl * 32 + 2 * (lane & 15);
    if (ra < 0) ra = 0;
    unsigned sa = (unsigned)(((ra >> 6) * 8 + g * 2) * 512 + (ra & 63) * 8);
    unsigned sb = (unsigned)(((rt * 2 + 1) * 8 + (lane >> 3)) * 512 + (2 * p2 - 80) * 8);
    unsigned sw = lds0 + (unsigned)(K1_ST_OFF + (2 * (lane < 32 ? wl * 16 + (lane & 15) : p2) + (g & 1)) * 8);
    asm volatile("" : "+v"(sa), "+v"(sb), "+v"(sw));
    MK(22);
    __builtin_amdgcn_s_setprio(3);
    bar();
    dma_run<14>(wq, (unsigned)K1C::XB);
    dma_run<14>(wq + K1C::WB, (unsigned)(SB + K1C::XB));
    dma_run<14>(wq + 2 * K1C::WB, (unsigned)(2 * SB + K1C::XB));
    MK(1);
    bar();
    dma_x(0u);
    dma_x((unsigned)SB);
    wbar<4>();
    MK(29);
    dma_x((unsigned)(2 * SB));
    dma_x((unsigned)(3 * SB));
    wbar<8>();
    MX(1, 3);
    dma_x(0u);
    wbar<8>();
    MX(1, 4);
    dma_x((unsigned)SB);
    f32x4 a, b, e;
    asm volatile("global_load_dwordx4 %0, %3, %5 sc1\n\tglobal_load_dwordx4 %1, %3, %5 offset:512 sc1\n\tglobal_load_dwordx4 %2, %4, %5 sc1"
                 : "=&v"(a), "=&v"(b), "=&v"(e)
                 : "v"(sa), "v"(sb), "s"(xs)
                 : "memory");
    wbar<11>();
    MX(1, 5);
    dma_w((unsigned)SB);
    dma_x((unsigned)(2 * SB));
    wbar<15>();
    MX(1, 6);
    dma_w((unsigned)(2 * SB));
    dma_x((unsigned)(3 * SB));
    wbar<19>();
    MX(1, 7);
    dma_w((unsigned)(3 * SB));
    dma_x(0u);
    wbar<16>();
    MX(1, 8);
    asm volatile("" : "+v"(a), "+v"(b), "+v"(e));
    float s, q;
    {
      float t[4] = {e[0], e[1], e[2], e[3]}, s2, q2;
      asm volatile(
          "v_add_f32_dpp %0, %0, %0 row_ror:8 row_mask:0xf bank_mask:0xf\n\tv_add_f32_dpp %1, %1, %1 row_ror:8 row_mask:0xf bank_mask:0xf\n\t"
          "v_add_f32_dpp %2, %2, %2 row_ror:8 row_mask:0xf bank_mask:0xf\n\tv_add_f32_dpp %3, %3, %3 row_ror:8 row_mask:0xf bank_mask:0xf\n\ts_nop 1"
          : "+v"(t[0]), "+v"(t[1]), "+v"(t[2]), "+v"(t[3]));
      red4(t, s2, q2);
      red8(a, b, s, q);
      s = lane < 32 ? s : s2;
      q = lane < 32 ? q : q2;
      asm volatile("" : "+v"(s), "+v"(q));
    }
    wbar<8>();
    MX(1, 9);
    {
      asm volatile("" : "+v"(s), "+v"(q));
      float mb, rinv;
      ln_finish(s, q, 1.0f / (float)D_, mb, rinv);
      asm volatile("ds_write_b64 %0, %1" ::"v"(sw), "v"(f32x2{-mb, rinv}) : "memory");
    }
    wbar<0>();
    MK(30);
  }

  template <int I>
  DEVI void p5_step(const char* wp) {
    constexpr int T = 2 * I + 1;
    wbar<(T == 1 ? 63 : (T <= 11 ? 3 : 0))>();
    if constexpr (T <= 9) {
      const unsigned wo = (unsigned)(12288 + (wl & 1) * 3072), so = (unsigned)(P5_W + ((T + 2 + (wl >> 1)) & 7) * 18432);
      dma_seq<3>(wp + (T + 7) * 18432, lane16 + wo + (unsigned)((wl >> 1) * 18432), lds0 + so + wo);
      pf_put<0, 12 * (T - 1), 12>((unsigned)(P5_W + ((T + 2) % 8) * 18432));
      pf_put<0, 12 * T, 12>((unsigned)(P5_W + ((T + 3) % 8) * 18432));
    }
  }
  template <int... S>
  DEVI void p5_steps(const char* wp, std::integer_sequence<int, S...>) {
    (p5_step<S>(wp), ...);
  }

  template <int T0, int T1>
  DEVI void w5_load(const char* w5p) {
    const unsigned o = lane16 + (unsigned)(wl * 1024);
#pragma unroll
    for (int t = T0; t < T1; ++t)
      asm volatile("global_load_dwordx4 %0, %1, %2 sc1" : "=v"(pf[t]) : "v"(o + (unsigned)((t / 3 + 8) * 18432 + (t % 3) * 4096)), "s"(w5p) : "memory");
  }
  DEVI void p35(char* smem, char* ws, const u16* w_sp, int layer, int bid, int mi, int ni, int rb, int ct, int hp MK_ARG) {
    using C = K3C;
    constexpr int NS = C::NS, SB = C::SB, NDX = 4, NDW = 5, VPS = NDX + NDW;
    const char* wip = uni_ptr(ws + OFF_PI + (size_t)ni * (18 * C::WB));
    if (layer) {
      pf_put<-48, 36, 18>((unsigned)(2 * SB + C::XB));
      pf_put<-48, 54, 18>((unsigned)(3 * SB + C::XB));
    } else {
      dma_run<18>(wip + 2 * C::WB, (unsigned)(2 * SB + C::XB));
      dma_run<18>(wip + 3 * C::WB, (unsigned)(3 * SB + C::XB));
    }
    dma_run<18>(wip + C::WB, (unsigned)(SB + C::XB));
    const char* w5p = uni_ptr(ws + OFF_PW + (size_t)ct * (36 * 9216));
    const int w0 = wl < 2 ? wl * 5120 : wl * 4096 + 2048;
    const unsigned m0x = lds0 + (unsigned)(wl * 4096), m0w = lds0 + (unsigned)(C::XB + w0);
    const unsigned wo = lane16 + (unsigned)w0;
    const unsigned lw = lds0 + (unsigned)(LW_OFF + layer * 4), me = 1u << (8 * wl);
    const unsigned wo4 = wo + 4096u;
    auto dma_w = [&](const char* wp, unsigned so) {
      dma_seq<4>(wp, wo, m0w + so);
      if (wl < 2) dma16_sc1(wp, wo4, m0w + so + 4096u);
    };
    {
      const char* xp = uni_ptr(ws + OFF_X1 + (size_t)layer * 2 * SZ_X + (size_t)(mi * 2 * 72 + wl * 2) * 1024 - 1024);
      const unsigned xo0 = lane16 + 1024u, xo1 = lane16 + 72u * 1024u, xo3 = lane16 + 71u * 1024u;
      auto dma_x = [&](int k, unsigned so) { dma4<4, true>(xp + k * 8192, xo0, xo1, lane16, xo3, m0x + so); };
      MK(10);
      bar();
      dma_x(0, 0u);
      dma_x(1, (unsigned)SB);
      wait_vm<4>(); MX(3, 2); wbar<4>(); MX(2, 2);
      MK(25);
      {
        dma_x(2, (unsigned)(2 * SB));
        dma_x(3, (unsigned)(3 * SB));
        dma_x(4, (unsigned)(4 * SB));
        wbar<12>(); MX(2, 3);
        dma_x(5, 0u);
        wait_vm<12>();
      }
      bar(); MX(2, 4);
      dma_w(wip + 4 * C::WB, 0u);
      dma_x(6, (unsigned)SB);
      wait_w<8 + VPS>();
#pragma unroll 1
      for (int t = 1; t < 3; ++t) {
        bar(); MX(2, 4 + t);
        dma_w(wip + (t + 4) * C::WB, (unsigned)(t * SB));
        dma_x(t + 6, (unsigned)((t + 1) * SB));
        wait_w<NDX + VPS>();
      }
      bar(); MX(2, 7);
      dma_w(wip + 7 * C::WB, (unsigned)(3 * SB));
      wait_w<VPS>();
      bar(); MX(2, 8);
      dma_w(wip + 8 * C::WB, 0u);
      wait_w<NDW>();
      bar(); MX(2, 9);
      const char* wp = wip + 9 * C::WB;
      dma_w(wp + b_k(0) * C::WB, (unsigned)SB);
      if (layer == 0) {
        const char* sp = uni_ptr((const char*)w_sp + (size_t)(bid * 4 + wl) * 8192);
        const unsigned so = (unsigned)((lane >> 5) * 4096 + (lane & 31) * 16);
#pragma unroll
        for (int i = 0; i < WSP_V; ++i) dma16(sp + i * 512, so, lds0 + (unsigned)K3_DM_OFF);
        dma16(sp + (bid >> 4) * 256, so & ~0x100u, lds0 + (unsigned)K3_DM_OFF);
      }
      w5_load<0, 4>(w5p);
      wait_w<NDW + 4>();
      MX(3, 10); bar(); MX(2, 10);
      dma_w(wp + b_k(1) * C::WB, (unsigned)(2 * SB));
      w5_load<4, 8>(w5p);
      MK(26);
      lds_spin4(lw + LW_AD * 4, 1u);
      dma_w(wp + b_k(2) * C::WB, (unsigned)(3 * SB));
      dma_w(wp + b_k(3) * C::WB, 0u);
      w5_load<8, W5A>(w5p);
      wait_vm<W5A - 8>();
      lds_inc(lw + LW_L * 4, 4u * me);
      w5_load<W5A, 30>(w5p);
      MK(11);
    }
    {
      unsigned xoff[NDX];
#pragma unroll
      for (int i = 0; i < NDX; ++i) {
        const int c = wl * 256 + i * 64 + lane;
        const int row = c >> 3, cc = c & 7;
        xoff[i] = (unsigned)((mi * 128 + row) * S_ * 2 - i * 1024) + (unsigned)((cc ^ ((row >> 1) & 7)) * 16);
      }
      auto dma_x = [&](const char* xp, unsigned so) { dma4<4, false>(xp, xoff[0], xoff[1], xoff[2], xoff[3], m0x + so); };
      const char* xp = uni_ptr((const char*)w_sp);
      const char* wp = uni_ptr(ws + OFF_Z2 + (size_t)layer * SZ_Z + (size_t)ni * S_ * 288);
      static_for<4, 7>([&](auto T_) {
        constexpr int t = T_;
        lds_spin4<2>(lw + LW_S * 4, (unsigned)(t - 3));
        dma_w(wip + (9 + b_k(t)) * C::WB, (unsigned)(((t + 1) % 4) * SB));
        if constexpr (t > 4) {
          dma_x(xp + mi * 256 + (t - 5) * 128, (unsigned)((t - 3) * SB));
          wait_w<NDW + (t - 4) * NDX>();
          lds_inc(lw + LW_L * 4, me);
        }
      });
      wait_vm<NDX>();
      lds_inc(lw + LW_L * 4, me);
      wait_vm<0>();
      lds_inc(lw + LW_L * 4, me);
      pf_pin<0, 30>();
      MK(13);
      lds_spin4<2>(lw + LW_AD * 4, 5u);
      MK(24);
      int left = 2 * mi;
      auto next = [&]() {
        const int d = --left == 0 ? 3 : 1;
        xp += d * 128;
        wp += d * C::WB;
      };
      if (mi == 0) {
        xp += 256;
        wp += 2 * C::WB;
      }
      if (wl == 0) {
        cnt_wait(ws, CN_Z + layer, mi * 16 + ni, (unsigned)ZQ);
        lds_wr32(lw + LW_GO * 4, 1u);
      }
      lds_spin4<2>(lw + LW_AD * 4, 6u);
      dma_x(xp, 0u);
      dma_x(xp + 128, (unsigned)SB);
      spin_ge<2>(lw + LW_GO * 4, 1u);
      lds_spin4<2>(lw + LW_S * 4, 8u);
      dma_w(wp, 0u);
      lds_spin4<2>(lw + LW_S * 4, 9u);
      dma_w(wp + C::WB, (unsigned)SB);
      next();
      next();
      unsigned slot = (unsigned)(2 * SB);
      auto cissue = [&]() {
        dma_x(xp, slot);
        dma_w(wp, slot);
        next();
        slot = (slot == (unsigned)((NS - 1) * SB)) ? 0u : slot + (unsigned)SB;
      };
      auto cstep = [&]() {
        bar();
        cissue();
      };
      wait_w<NDW>();
      MK(15);
      int j = 2;
      if (layer == 0) {
        const char* sp = uni_ptr((const char*)w_sp + (size_t)(bid * 4 + wl) * 8192 + WSP_V * 512);
        unsigned d = 0u;
#pragma unroll 1
        for (; j < 2 + 32 - 4 * WSP_V; ++j) {
          cstep();
          asm volatile("s_load_dword %0, %1, 0\n\ts_load_dword %0, %1, 64\n\ts_load_dword %0, %1, 0x1000\n\ts_load_dword %0, %1, 0x1040"
                       : "+s"(d)
                       : "s"(sp));
          sp += 128;
          wait_w<VPS>();
        }
#pragma unroll 1
        for (int e = j + 4; j < e; ++j) {
          cstep();
          asm volatile("" : "+s"(d));
          wait_w<VPS>();
        }
        asm volatile("s_waitcnt lgkmcnt(0)" : "+s"(d));
      }
      place();
#pragma unroll 1
      for (; j <= 29; ++j) {
        cstep();
        wait_w<VPS>();
      }
      bar();
    }
    dma_run<33>(w5p, (unsigned)(P5_W + 54 * 1024));
    dma_run<23>(w5p + 67 * 1024, (unsigned)(P5_W + 121 * 1024));
    if (wl == 3) dma16_sc1(w5p + 143 * 1024, lane16, lds0 + (unsigned)(P5_W + 53 * 1024));
    wait_vm<14>();
    bar();
    dma_run<34>(w5p + 33 * 1024, (unsigned)(P5_W + 87 * 1024));
    wait_vm<0>();
    bar();
    if (wl) {
      const unsigned f = (unsigned)(wl - 1) * 18432u;
      dma_seq<17>(w5p + 90 * 1024, lane16 + f, lds0 + (unsigned)P5_W + f);
      if (wl < 3) dma16_sc1(w5p + 107 * 1024, lane16 + f, lds0 + (unsigned)(P5_W + 17 * 1024) + f);
    } else {
      cnt_wait(ws, CN_H + layer, rb * 8 + ct, 8u);
    }
    MK(20);
    bar();
    p5_steps(w5p, std::make_integer_sequence<int, 7>{});
    if (layer == 0) {
      if (bid < 24 && wl == 0 && lane < 8) pk_st16(ws + OFF_HELLO + bid * 128 + lane16, u32x4{0u, 0u, 0u, 0u});
    }
  }
};

__global__ __launch_bounds__(768, 3) void k_main(const u16* __restrict__ x0, const u16* __restrict__ wqkv, const u16* __restrict__ wo,
                                                 const u16* __restrict__ w_in, const u16* __restrict__ w_sp,
                                                 const u16* __restrict__ b_sp, const u16* __restrict__ w_out,
                                                 u16* __restrict__ out, char* ws) {
  extern __shared__ __attribute__((aligned(16))) char smem[];
  const char* code0 = code_here();
  (void)code0;
  const unsigned lds0 = (unsigned)(size_t)smem;
  int wave = __builtin_amdgcn_readfirstlane((int)threadIdx.x >> 6);
  int bid = blockIdx.x;
#define FRESH() asm volatile("" : "+s"(bid), "+s"(ws), "+s"(wave))
#define XCD (bid & 7)
#define IDX (bid >> 3)
#define MI ((XCD >> 2) * 8 + (IDX >> 2))
#define NI ((XCD & 1) * 8 + (XCD & 2) * 2 + (IDX & 3))
#define RB (XCD * 4 + (IDX >> 3))
#define CT (IDX & 7)
#define RT ((XCD >> 1) * 4 + (IDX >> 3))
#define HP ((XCD & 1) * 8 + (IDX & 7))
  MK(0);
  if (wave < 8) {
    constexpr int PER = (2 * NL_MAIN + 31) / 32;
    static_assert(PER <= 64 && 31 * PER < 2 * NL_MAIN, "one load");
    unsigned o, last, st, t;
    const char* p;
    asm volatile(
        "s_lshr_b32 %3, %5, 3\n\t"
        "s_cmp_eq_u32 %3, 31\n\ts_cselect_b32 %1, 63, 0\n\ts_cselect_b32 %2, %7, %6\n\t"
        "s_mul_i32 %0, %3, %8\n\t"
        "s_cmp_eq_u32 %4, 2\n\ts_cselect_b32 %0, %0, 0\n\ts_cselect_b32 %1, %2, %1\n\t"
        "s_mul_i32 %3, %3, 0xf300\n\ts_add_u32 %3, %3, 0x200\n\t"
        "s_cmp_eq_u32 %4, 3\n\ts_cselect_b32 %0, %3, %0\n\ts_cselect_b32 %1, 53, %1\n\ts_cselect_b32 %2, 0x480, 64\n\t"
        "s_mul_i32 %3, %5, 0x1e60\n\t"
        "s_cmp_eq_u32 %4, 5\n\ts_cselect_b32 %0, %3, %0\n\t"
        "s_add_u32 %3, %3, 0x1000\n\t"
        "s_cmp_eq_u32 %4, 4\n\ts_cselect_b32 %0, %3, %0\n\ts_cselect_b32 %1, 57, %1\n\t"
        "s_cmp_ge_u32 %4, 5\n\ts_cselect_b32 %1, 63, %1\n\t"
        "s_mul_i32 %3, %5, 0x2400\n\t"
        "s_cmp_eq_u32 %4, 7\n\ts_cselect_b32 %0, %3, %0\n\t"
        "s_add_u32 %3, %3, 0x1000\n\t"
        "s_cmp_eq_u32 %4, 6\n\ts_cselect_b32 %0, %3, %0\n\t"
        "s_add_u32 %3, %3, 0x1000\n\t"
        "s_cmp_eq_u32 %4, 1\n\ts_cselect_b32 %0, %3, %0\n\ts_cselect_b32 %1, 15, %1"
        : "=&s"(o), "=&s"(last), "=&s"(st), "=&s"(t)
        : "s"(wave), "s"(bid), "n"(PER - 1), "n"(2 * NL_MAIN - 1 - 31 * PER), "n"(PER * 64)
        : "scc");
    const unsigned l = (unsigned)lane_id();
    unsigned a = o + (l < last ? l : last) * st;
    asm volatile("" : "+v"(a));
    asm volatile("s_cmp_lg_u32 %1, 2\n\ts_cbranch_scc1 1f\n\ts_mov_b32 m0, %3\n\ts_nop 0\n\tglobal_load_lds_dword %0, %2\n\tv_mov_b32 %0, 0\n1:"
                          : "+v"(a)
                          : "s"(wave), "s"(code0), "s"(lds0 + (unsigned)K1R_DUMMY)
                          : "memory", "scc");
    asm volatile("s_cmp_eq_u32 %1, 2\n\ts_cselect_b64 %0, %4, %5\n\ts_bitcmp1_b32 0x38, %1\n\ts_cselect_b64 %0, %3, %0\n\t"
                 "s_bitcmp1_b32 0xc2, %1\n\ts_cselect_b64 %0, %2, %0"
                 : "=&s"(p)
                 : "s"(wave), "s"(x0), "s"(wqkv), "s"(wo), "s"(b_sp)
                 : "scc");
    touch64(p, a, lds0 + (unsigned)K1R_DUMMY);
    MF(12);
    math_start(lds0, ws, bid, wave, (int)l);
    MF(13);
  }

  if (__builtin_expect(wave >= 8, 1)) {
    __builtin_amdgcn_s_setprio(3);
    {
      const int lane = lane_id();
      RawLd rl;
      int job, pj;
      const char* pg;
      rl.front(x0, wqkv, wo, ws, bid, lds0, wave - 8, lane, RT, HP, pj, [&]() {
        int b = bid;
        asm volatile("" : "+s"(b));
        const int xcd = b & 7, c16 = b >> 4, hp = (b & 1) * 8 + ((b >> 3) & 7);
        const bool cross = (((hp * 72) & ~15) & 127) > 48;
        const int cj = (int)((0x65743210u >> (4 * xcd)) & 7u) * 16 + c16;
        job = cross ? (cj < 88 ? 128 + cj : 999) : xcd * 16 + c16;
        pj = cross ? cj - 88 : ((xcd & 5) == 0 && (c16 & 3) == (c16 >> 2) ? 40 + xcd * 2 + (c16 >> 2) : -1);
        const int k = b >> 3;
        pg = k < 11 ? (const char*)w_in + k * 0x80000 : k < 17 ? (const char*)w_out + (k - 11) * 0x80000 : k < 22 ? (const char*)out + (k - 17) * 0x80000
             : (const char*)w_sp + (k < 30 ? (k - 22) * 0x100000 : 0x7ffff0);
      }, pg MF_PASS);
      if (job < 216) k1_raw_loader<true>(rl, w_in, w_out, ws, rl.tag, bid, lds0, wave - 8, lane, RT, HP, CT, job, pj MF_PASS);
      else k1_raw_loader<false>(rl, w_in, w_out, ws, rl.tag, bid, lds0, wave - 8, lane, RT, HP, CT, job, pj MF_PASS);
    }
    Loader ld;
    ld.lds0 = lds0;
#pragma unroll 1
    for (int layer = 0; layer < 2; ++layer) {
      FRESH();
      ld.wl = wave - 8;
      ld.lane = lane_id();
      ld.lane16 = (unsigned)ld.lane * 16u;
      const char* wip = uni_ptr(ws + OFF_PI + (size_t)NI * (18 * K3C::WB));
      if (layer) {
        ld.p1(smem, ws, bid, RT, HP MK_PASS);
        ld.pf_load<0, 21, 0, 81>(uni_ptr(ws + OFF_PO + (size_t)CT * (18 * 4608)));
        ld.pf_load<21, 9, 36, 72>(wip);
        wait_vm<9>();
        ld.pf_pin<0, 21>();
        lds_spin(lds0 + (unsigned)(LW_OFF + (LW_T + layer) * 4), 8u);
        __builtin_amdgcn_s_sleep(4);
        ld.pf_put<0, 0, 40>((unsigned)P2_W);
        wait_vm<0>();
        ld.pf_pin<21, 9>();
        bar();
        ld.pf_put<0, 40, 41>((unsigned)(P2_W + 40 * 1024));
      } else {
        ld.pf_undef<21, 9>();
        MK(3);
        MK(4);
        bar();
        ld.dma_run<81>(uni_ptr(ws + OFF_PO + (size_t)CT * (18 * 4608)), (unsigned)P2_W);
      }
      if (layer == 0 && ld.wl == 0) cnt_wait(ws, CN_O + layer, RB * 8 + CT, 16u);
      MK(6);
      asm volatile("s_waitcnt vmcnt(0) lgkmcnt(0)" ::: "memory");
      bar();
      lds_spin(lds0 + (unsigned)(LW_OFF + (LW_T + layer) * 4), 12u);
      if (layer == 0) {
        if (ld.wl == 0) {
          cnt_wait(ws, CN_PK, 8 + NI, 36u);
          lds_wr32(lds0 + (unsigned)(LW_OFF + LW_HELLO * 4), 2u);
        } else {
          lds_spin(lds0 + (unsigned)(LW_OFF + LW_HELLO * 4), 2u);
        }
        MK(7);
      }
      static_assert(2 * K3C::SB <= P2_W, "free during the first residual GEMM");
      ld.dma_run<18>(wip, (unsigned)K3C::XB);
      MX(6, 24);
      if (layer == 0 && ld.wl == 0) cnt_wait(ws, CN_PK, 24 + CT, 72u);
      wait_vm<0>();
      ld.pf_pin<21, 9>();
      MX(6, 25);
      bar();
      FRESH();
      ld.wl = wave - 8;
      ld.p35(smem, ws, w_sp, layer, bid, MI, NI, RB, CT, HP MK_PASS);
    }
  } else {
#pragma unroll 1
    for (int layer = 0; layer < 2; ++layer) {
      FRESH();
      {
        const int lane = lane_id(), r0 = RT * 128;
        float* csq = reinterpret_cast<float*>(ws + OFF_CSQ + bid * 512);
        Attn at;
        at.setup(lds0 + (unsigned)(layer ? K1_T_OFF : K1R_T_OFF), ws + OFF_O + (size_t)layer * SZ_X, r0, HP, wave, lane);
        if (layer) {
          if ((wave & 2) == 0) k1_gemm<2, 4>(smem, ws, RT, HP, (wave & 1) * 2 + (wave & 4), 0, wave >= 4, lane MK_PASS);
          else if (wave < 4) k1_gemm<3, 3>(smem, ws, RT, HP, (wave & 1) * 3, 4, false, lane MK_PASS);
          else if (wave == 6) k1_gemm<2, 3>(smem, ws, RT, HP, 6, 4, true, lane MK_PASS);
          else k1_gemm<1, 7>(smem, ws, RT, HP, 8, 0, true, lane MK_PASS);
        } else {
          if (wave == 0) k1_gemm_raw<3, false>(smem, HP, 0, lane MF_PASS);
          else if (wave < 4) k1_gemm_raw<2, false>(smem, HP, 1 + 2 * wave, lane MF_PASS);
          else if (wave < 7) k1_gemm_raw<2, true>(smem, HP, 2 * (wave - 4), lane MF_PASS);
          else k1_gemm_raw<3, true>(smem, HP, 6, lane MF_PASS);
        }
        msync(lds0 + (unsigned)(LW_OFF + (LW_T + layer) * 4));
        MK(3);
        at.run(r0, HP, wave, lane);
        if (layer == 0) {
          if (wave == 0 && lane < 32)
            asm volatile("global_store_dwordx4 %0, %1, %2 sc1\n\ts_nop 1" ::"v"((unsigned)lane * 16u),
                         "v"(*reinterpret_cast<const u32x4*>(smem + K1R_CS_OFF + lane * 16)), "s"(uni_ptr((const char*)csq))
                         : "memory");
          lds_spin(lds0 + (unsigned)(LW_OFF + LW_HELLO * 4), 1u);
        }
        MK(4);
        announce(lds0 + (unsigned)(LW_OFF + (LW_O + layer * 22 + (wave >> 2)) * 4), 4u, ws, CN_O + layer, (RT * 2 + (wave >> 2)) * 8 + lane,
                 lane < 8);
        MK(5);
        bar();
      }
      FRESH();
      {
        if (wave < 4) {
          const int lane = lane_id();
          u32x2 xv[5];
          ResIn ri;
          ri.setup(layer ? ws + OFF_X1 + SZ_X : (const char*)x0, layer == 0, RB, CT, wave, lane);
          ri.load(xv);
          ResOut ro;
          ro.setup(ws + OFF_X1 + (size_t)layer * 2 * SZ_X, ws + OFF_XS + (size_t)layer * 2 * SZ_XS, out, false, RB, CT, wave, lane);
          f32x4 acc[2][5];
          p2_gemm(acc, xv, lds0, uni_ptr(ws + OFF_O + (size_t)layer * SZ_X + (size_t)RB * (72 * 1024)), layer, wave, lane MK_PASS);
          MK(7);
          ro.run(acc[0], false);
          MK(8);
          announce(lds0 + (unsigned)(LW_OFF + (LW_X1 + layer) * 4), 4u, ws, CN_X1 + layer, (RB >> 1) * 16 + lane, lane < 16);
          MK(9);
          bar();
          FRESH();
          if (wave == 3) cnt_wait(ws, CN_X1 + layer, MI * 16 + NI, 16u);
          bar();
          MK(10);
          p3_g0(smem, ws, layer, MI, NI, wave, lane_id() MK_PASS);
        } else {
          if (layer && wave == 5) cnt_wait(ws, CN_O + layer, RB * 8 + CT, 16u);
          bar();
          k3_const(smem, ws, b_sp, layer, bid, MI, NI, wave & 3);
          MX(6, 26);
          bar();
          MK(10);
          FRESH();
          p3_g1(smem, ws, layer, MI, NI, wave & 3, lane_id() MK_PASS);
        }
      }
      FRESH();
      {
        const int lane = lane_id();
        if (wave < 4) {
          u32x2 xv[5];
          ResIn ri;
          ri.setup(ws + OFF_X1 + (size_t)layer * 2 * SZ_X, false, RB, CT, wave, lane);
          f32x4 acc[2][5];
          ResOut ro;
          ro.setup(ws + OFF_X1 + SZ_X, ws + OFF_XS + SZ_XS, out, layer != 0, RB, CT, wave, lane);
          ResX<36, 33, 8, P5_W, P5_XW, P5_X0, 3, P5_BAR> gm(acc);
          gm.setup(lds0, uni_ptr(ws + OFF_H + (size_t)layer * SZ_Z + (size_t)RB * (288 * 1024)), wave, lane, (unsigned)P5_RED, layer + 1u);
          announce(lds0 + (unsigned)(LW_OFF + (LW_H + layer * 2 + (wave >> 1)) * 4), 2u, ws, ((NI >= 8) ? CN_H2 : CN_H) + layer,
                   (MI * 2 + (wave >> 1)) * 8 + lane, lane < 8);
          MK(19);
          bar();
          ri.load(xv);
          gm.prow();
          place();
          bar();
          MK(20);
          gm.prox<0>(std::make_integer_sequence<int, P5_X0>{});
          gm.init<2 * P5_X0>(xv);
          __builtin_amdgcn_sched_barrier(0);
          gm.run();
          MK(21);
          ro.run(acc[0], layer != 0);
          MK(22);
          if (layer == 0) {
            announce(lds0 + (unsigned)(LW_OFF + LW_X2 * 4), 4u, ws, CN_X2, (RB >> 1) * 16 + lane,
                     lane < 16 || (lane < 32 && (RB & 1) && RB < 31));
            MK(23);
          }
        } else {
          bar();
          bar();
#pragma unroll
          for (int i = 0; i < 7; ++i) {
            if (i == 1 && wave == 5) {
              unsigned f;
              const char* cp = uni_ptr(ws + OFF_CNT + (size_t)((CN_H2 + layer) * 256 + RB * 8 + CT) * 128);
              asm volatile("global_load_dword %0, %1, %2 sc1" : "=v"(f) : "v"(vconst(0u)), "s"(cp) : "memory");
              bar();
              asm volatile("s_waitcnt vmcnt(0)" : "+v"(f) : : "memory");
              if (__builtin_amdgcn_readfirstlane(f) != 7u) cnt_wait(ws, CN_H2 + layer, RB * 8 + CT, 8u);
              continue;
            }
            asm volatile("s_waitcnt lgkmcnt(0)" ::: "memory");
            bar();
          }
          MK(22);
        }
        if (layer == 0 && wave < 4) bar();
      }
    }
  }
}

}

extern "C" void kernel_launch(void* const* d_in, const int* in_sizes, int n_in, void* d_out, int out_size,
                              void* d_ws, size_t ws_size, hipStream_t stream) {
  const u16* x0 = (const u16*)d_in[0];
  const u16* wqkv = (const u16*)d_in[1];
  const u16* wo = (const u16*)d_in[2];
  const u16* w_in = (const u16*)d_in[4];
  const u16* w_sp = (const u16*)d_in[5];
  const u16* b_sp = (const u16*)d_in[6];
  const u16* w_out = (const u16*)d_in[7];
  char* ws = (char*)d_ws;
  (void)hipFuncSetAttribute((const void*)k_main, hipFuncAttributeMaxDynamicSharedMemorySize, LDS_ALL);
  k_main<<<256, 768, LDS_ALL, stream>>>(x0, wqkv, wo, w_in, w_sp, b_sp, w_out, (u16*)d_out, ws);
}
```

```cpp
#include <hip/hip_runtime.h>
#include <hip/hip_bf16.h>
#include <stdint.h>

#define DEVI __device__ __forceinline__

typedef unsigned short u16;
typedef __attribute__((ext_vector_type(8))) short s16x8;
typedef __attribute__((ext_vector_type(4))) short s16x4;
typedef __attribute__((ext_vector_type(4))) float f32x4;
typedef __attribute__((ext_vector_type(2))) float f32x2;
typedef __attribute__((ext_vector_type(2))) __bf16 bf16x2;
typedef __attribute__((ext_vector_type(2))) unsigned u32x2;
typedef __attribute__((ext_vector_type(4))) unsigned u32x4;

#define MK(I) ((void)0)
#define MX(S, I) ((void)0)
#define MF(I) ((void)0)
#define MF_ARG
#define MF_PASS
#define MK_ARG
#define MK_PASS

#define SPIN_MEM (1u << 22)
#define SAFE_OUT(L)
#define SAFE_AND
#define NOP_(n) "=s"(n)

namespace {

constexpr int S_ = 2048, D_ = 576, F_ = 4608, FH_ = 2304;
constexpr int NQKV = 3 * D_;

constexpr size_t GAP = 256;
constexpr size_t SZ_X = (size_t)S_ * D_ * 2 + GAP;
constexpr size_t SZ_Z = (size_t)S_ * FH_ * 2 + GAP;
constexpr size_t OFF_X1 = GAP;
constexpr size_t OFF_O = OFF_X1 + 3 * SZ_X;
constexpr size_t OFF_Z2 = OFF_O + 2 * SZ_X;
constexpr size_t OFF_H = OFF_Z2 + 2 * SZ_Z;
constexpr size_t SZ_XS = (size_t)S_ * 8 * 8;
constexpr size_t OFF_XS = OFF_H + 2 * SZ_Z;
constexpr size_t OFF_CSQ = OFF_XS + 3 * SZ_XS;
constexpr size_t OFF_ZP = OFF_CSQ + 256 * 512;
constexpr size_t SZ_ZP = (size_t)16 * 16 * 1024;
constexpr size_t OFF_PQ = OFF_ZP + 2 * SZ_ZP;
constexpr size_t OFF_PO = OFF_PQ + (size_t)16 * 9 * 64 * 112 * 2 + GAP;
constexpr size_t OFF_PI = OFF_PO + (size_t)D_ * D_ * 2 + GAP;
constexpr size_t OFF_PW = OFF_PI + (size_t)32 * 9 * 64 * 144 * 2 + GAP;
constexpr size_t OFF_CSP = OFF_PW + (size_t)FH_ * D_ * 2 + GAP;
constexpr size_t OFF_CNT = OFF_CSP + (size_t)18 * F_ * 4;
constexpr int CN_X1 = 0, CN_H = 2, CN_X2 = 4, CN_O = 5, CN_Z = 7, CN_PK = 9, CN_H2 = 10, CN_Z2 = 12, NCNT = 14;
constexpr size_t OFF_TAG = OFF_CNT + (size_t)NCNT * 256 * 128;
constexpr size_t OFF_HELLO = OFF_TAG + 128;
constexpr size_t OFF_POT = OFF_HELLO + 2048;
constexpr size_t OFF_CSK = OFF_POT + 1024;
constexpr size_t WS_END = OFF_CSK + 256 * 2048;
static_assert(WS_END <= (size_t)64 << 20, "workspace");

DEVI float bf_lo(unsigned u) { return __uint_as_float(u << 16); }
DEVI float bf_hi(unsigned u) { return __uint_as_float(u & 0xffff0000u); }
DEVI unsigned pack2(float a, float b) {
  f32x2 v = {a, b};
  bf16x2 r = __builtin_convertvector(v, bf16x2);
  return __builtin_bit_cast(unsigned, r);
}
DEVI float rbf(float a) {
  return bf_lo(pack2(a, 0.f));
}

DEVI float sum_g(float x) {
  const unsigned u = __float_as_uint(x);
  const auto p = __builtin_amdgcn_permlane32_swap(u, u, false, false);
  const unsigned v = __float_as_uint(__uint_as_float(p[0]) + __uint_as_float(p[1]));
  const auto q = __builtin_amdgcn_permlane16_swap(v, v, false, false);
  return __uint_as_float(q[0]) + __uint_as_float(q[1]);
}

DEVI unsigned vconst(unsigned c) {
  unsigned r;
  asm volatile("v_mov_b32 %0, %1" : "=v"(r) : "s"(c));
  return r;
}
DEVI void place() {
  __builtin_amdgcn_sched_barrier(0);
  asm volatile(".p2align 6");
  __builtin_amdgcn_sched_barrier(0);
}
DEVI const char* uni_ptr(const char* p) {
  unsigned long long v = (unsigned long long)p;
  unsigned lo = __builtin_amdgcn_readfirstlane((unsigned)v), hi = __builtin_amdgcn_readfirstlane((unsigned)(v >> 32));
  asm("s_nop 4" : "+s"(lo), "+s"(hi));
  return (const char*)(((unsigned long long)hi << 32) | lo);
}

DEVI void dma16(const char* sbase, unsigned voff, unsigned m0val) {
  asm volatile("s_mov_b32 m0, %2\n\ts_nop 0\n\tglobal_load_lds_dwordx4 %0, %1"
               :
               : "v"(voff), "s"(sbase), "s"(m0val)
               : "memory");
}

DEVI void ln_finish(float s, float q, float invn, float& mb, float& rinv) {
  float mean = s * invn;
  asm("" : "+v"(mean));
  float var = fmaxf(__builtin_fmaf(-mean, mean, q * invn), 0.f);
  mb = rbf(mean);
  float v = rbf(var);
  float t = rbf(v + 1e-5f);
  float sd = rbf(__builtin_amdgcn_sqrtf(t));
  rinv = __builtin_amdgcn_rcpf(sd);
}

DEVI unsigned pack2v(f32x2 v) { return pack2(v[0], v[1]); }

template <int BM_, int BN_>
struct GemmCfg {
  static constexpr int BM = BM_, BN = BN_, NS = 4;
  static constexpr int P = BN * 2;
  static constexpr int XB = BM * 128, WB = 64 * P, SB = XB + WB;
  static constexpr int LDS_BYTES = NS * SB;
};

DEVI int swap23(int r) { return (r & ~12) | ((r & 4) << 1) | ((r & 8) >> 1); }

DEVI void lds_wr32(unsigned addr, unsigned v) { asm volatile("ds_write_b32 %0, %1" ::"v"(addr), "v"(v) : "memory"); }

template <class C, int MT, int NT, int D, int R, bool XCM, int WT, bool SQ = false, unsigned long long WS = 0>
struct GemmMath {
  static constexpr int WO = WT ? 1 : 2;
  static constexpr int NS = C::NS, SB = C::SB, P = C::P, NU = 2 * NT;
  static constexpr int XPU = MT <= 2 ? MT : MT - 2;
  static constexpr int XU = (MT + XPU - 1) / XPU;
  static_assert(NU % R == 0 && R > D && D <= NT && XU <= D, "shape");
  s16x4 wlo[R], whi[R];
  s16x8 wf[R];
  s16x8 xf[2][MT];
  unsigned xrd[2], wrd, xa[2], wa, so;
  f32x4 (&acc)[MT][NT];
  f32x4 accq[SQ ? MT : 1];
  unsigned long long wtab = WS;
  DEVI GemmMath(f32x4 (&a)[MT][NT]) : acc(a) {}

  template <int PU>
  DEVI void wread_lo() {
    constexpr int off = (PU / NT) * 32 * P + (PU % NT) * (WT == 2 ? 1024 : 32);
    if constexpr (WT) asm volatile("ds_read_b128 %0, %1 offset:%2" : "=v"(wf[PU % R]) : "v"(wa), "n"(off));
    else asm volatile("ds_read_b64_tr_b16 %0, %1 offset:%2" : "=v"(wlo[PU % R]) : "v"(wa), "n"(off));
  }
  template <int PU>
  DEVI void wread_hi() {
    if constexpr (!WT)
      asm volatile("ds_read_b64_tr_b16 %0, %1 offset:%2" : "=v"(whi[PU % R]) : "v"(wa), "n"((PU / NT) * 32 * P + (PU % NT) * 32 + 8 * P));
  }
  template <int H, int M>
  DEVI void xread1() {
    asm volatile("ds_read_b128 %0, %1 offset:%2" : "=v"(xf[H][M]) : "v"(xa[H]), "n"(M * (XCM ? 256 : 2048)));
  }
  static constexpr int xops(int v) {
    const int k = (v + D) % NT;
    if (k >= XU) return 0;
    const int n = MT - k * XPU;
    return n < XPU ? n : XPU;
  }
  static constexpr int ops(int v, bool last) { return (last && v + D >= NU) ? 0 : WO + xops(v); }
  static constexpr int newer(int U, bool last) {
    int n = 0;
    int v0 = U - D;
    if (U % NT == 0) v0 += XU - 1;
    else n += v0 >= 0 ? xops(v0) : xops(v0 + NU);
    for (int v = v0 + 1; v < U; ++v) n += v >= 0 ? ops(v, last) : ops(v + NU, false);
    return n;
  }
  template <int U, int M_>
  DEVI void mf() {
    constexpr int M = ((U % NT) & 1) ? MT - 1 - M_ : M_;
    s16x8 w;
    if constexpr (WT) w = wf[U % R];
    else w = __builtin_shufflevector(wlo[U % R], whi[U % R], 0, 1, 2, 3, 4, 5, 6, 7);
    acc[M][U % NT] = __builtin_amdgcn_mfma_f32_16x16x32_bf16(w, xf[U / NT][M], acc[M][U % NT], 0, 0, 0);
    __builtin_amdgcn_sched_barrier(0);
  }
  DEVI void pin() {
#pragma unroll
    for (int m = 0; m < MT; ++m)
#pragma unroll
      for (int n = 0; n < NT; ++n) asm volatile("" : "+v"(acc[m][n]));
  }
  template <int U, int I>
  DEVI void dsop() {
    constexpr int PU = (U + D) % NU;
    if constexpr (I == 0) wread_lo<PU>();
    else if constexpr (I == 1) wread_hi<PU>();
    else if constexpr (I - 2 < xops(U)) {
      constexpr int k = (U + D) % NT;
      xread1<((U + D - k) % NU) / NT, k * XPU + I - 2>();
    }
  }
  template <int U, bool LAST, bool BAR, int M>
  DEVI void slot() {
    constexpr bool PF = !(LAST && U + D >= NU);
    mf<U, M>();
    if constexpr (M == 0 && PF && U + D == NU) {
      if constexpr (BAR) __builtin_amdgcn_s_barrier();
      so = (so == (unsigned)((NS - 1) * SB)) ? 0u : so + (unsigned)SB;
      wa = wrd + (WS ? ((unsigned)wtab & 63u) << 11 : so);
      wtab >>= 6;
      xa[0] = xrd[0] + so;
      xa[1] = xrd[1] + so;
      __builtin_amdgcn_sched_barrier(0);
    }
    if constexpr (PF) {
      dsop<U, M>();
      if constexpr (M == MT - 1) {
        if constexpr (MT <= 1) dsop<U, 1>();
        if constexpr (MT <= 2) {
          dsop<U, 2>();
          dsop<U, 3>();
        }
      }
      __builtin_amdgcn_sched_barrier(0);
    }
  }
  template <int U, bool LAST, bool BAR, int... M>
  DEVI void unit(std::integer_sequence<int, M...>) {
    static_assert(newer(U, LAST) <= 15, "lgkmcnt range");
    asm volatile("s_waitcnt lgkmcnt(%0)" ::"n"(newer(U, LAST)));
    __builtin_amdgcn_sched_barrier(0);
    (slot<U, LAST, BAR, M>(), ...);
    if constexpr (SQ && (U % NT) < MT) {
      constexpr int m = U % NT;
      accq[m] = __builtin_amdgcn_mfma_f32_16x16x32_bf16(xf[U / NT][m], xf[U / NT][m], accq[m], 0, 0, 0);
      __builtin_amdgcn_sched_barrier(0);
    }
    __builtin_amdgcn_sched_barrier(0);
  }
  template <bool LAST, bool BAR, int... U>
  DEVI void stage(std::integer_sequence<int, U...>) {
    (unit<U, LAST, BAR>(std::make_integer_sequence<int, MT>{}), ...);
  }
  template <int... U>
  DEVI void prow(std::integer_sequence<int, U...>) {
    ((wread_lo<U>(), wread_hi<U>()), ...);
  }
  template <int... M>
  DEVI void prox(std::integer_sequence<int, M...>) {
    (xread1<0, M>(), ...);
  }
  DEVI void setup(unsigned lds0, int mt0, int nt0, int lane, unsigned so0) {
    const int g = lane >> 4, r = lane & 15, q = (lane >> 2) & 3, p = lane & 3;
#pragma unroll
    for (int h = 0; h < 2; ++h)
      xrd[h] = XCM ? lds0 + (unsigned)((4 * h + g) * (C::BM * 16) + (mt0 * 16 + r) * 16)
                   : lds0 + (unsigned)((mt0 * 16 + r) * 128 + (((4 * h + g) ^ ((r >> 1) & 7)) * 16));
    wrd = WT == 2   ? lds0 + (unsigned)(C::XB + nt0 * 1024 + lane * 16)
          : WT == 1 ? lds0 + (unsigned)(C::XB + (g * 8 + (r & 7)) * P + (r >> 3) * 16 + nt0 * 32)
                    : lds0 + (unsigned)(C::XB + (16 * (g >> 1) + 4 * (g & 1) + q) * P + 8 * p + nt0 * 32);
    so = so0;
    xa[0] = xrd[0] + so;
    xa[1] = xrd[1] + so;
    wa = wrd + so;
  }
  DEVI void first() {
    prox(std::make_integer_sequence<int, MT>{});
    prow(std::make_integer_sequence<int, D>{});
    asm volatile("s_waitcnt lgkmcnt(0)");
  }
  template <int N, bool BAR = true>
  DEVI void mid() {
    if constexpr (N > 1) place();
#pragma unroll 1
    for (int s = 0; s < N; ++s) stage<false, BAR>(std::make_integer_sequence<int, NU>{});
  }
  DEVI void last() {
    stage<true, true>(std::make_integer_sequence<int, NU>{});
    pin();
  }
  template <int KS>
  DEVI void run(unsigned lds0, int mt0, int nt0, int lane, unsigned so0) {
    setup(lds0, mt0, nt0, lane, so0);
    __builtin_amdgcn_s_barrier();
    first();
    mid<KS - 1>();
    last();
  }
};

DEVI const char* code_here() {
  unsigned long long pc;
  asm volatile("s_getpc_b64 %0" : "=s"(pc));
  return (const char*)pc;
}
DEVI int okmap(int p) {
  const int ch = p >> 3, e = p & 7, c = (ch - 8) & 3;
  return ch < 8 ? (2 * ch + (e >> 2)) * 36 + 16 + (e & 3) : ((ch - 8) >> 2) * 36 + c * 8 + (c >> 1) * 4 + e;
}

DEVI void pk_st16(void* p, u32x4 v) {
  asm volatile("global_store_dwordx4 %0, %1, off sc1\n\ts_nop 1" ::"v"(p), "v"(v) : "memory");
}

using K1C = GemmCfg<144, 112>;
constexpr int K1_TP = 240;
constexpr int K1_T_OFF = K1C::SB;
constexpr int K1_ST_OFF = 163840 - 128 - 1152;
constexpr int K1_CS_OFF = 4 * K1C::SB;
constexpr int K1_WX = 4 * K1C::SB + 2048;
constexpr unsigned long long K1_WS = 16ull | 32ull << 6 | 48ull << 12 | 56ull << 18 | 63ull << 24 | 16ull << 30 | 32ull << 36 | 48ull << 42;
static_assert(K1C::SB == 16 * 2048 && K1_WX - K1C::XB == 56 * 2048 && K1C::WB == 7 * 2048 && K1_WX + 2 * K1C::WB <= K1_ST_OFF, "K1 w places");
static_assert(K1_T_OFF + 144 * K1_TP + 64 <= 2 * K1C::SB + K1C::XB, "K1 LDS alias");
using K1R = GemmCfg<144, 144>;
constexpr int K1R_T_OFF = K1R::SB;
constexpr int K1R_CS_OFF = K1R_T_OFF + 144 * K1_TP;
constexpr int K1R_ST_OFF = K1R_CS_OFF + 512;
constexpr int K1R_DUMMY = K1R::LDS_BYTES;
constexpr int K1R_PO = K1R_DUMMY + 1024;
constexpr int K1R_CNT = 163840 - 128 + 28 * 4;
#define SPIN_ASM(TEST)                                                                                                   \
  if constexpr (SL <= 1)                                                                                                 \
    asm volatile(".La%=:\n\tds_read_b32 %0, %3\n\ts_waitcnt lgkmcnt(0)\n\tv_readfirstlane_b32 %2, %0\n\t" TEST SAFE_AND \
                 "s_cbranch_scc1 .La%="                                                                                  \
                 : "=&v"(v), NOP_(c), "=&s"(t)                                                                           \
                 : "v"(addr), "s"(k)                                                                                     \
                 : "memory", "scc");                                                                                     \
  else                                                                                                                   \
    asm volatile("s_branch .Lb%=\n.La%=:\n\ts_sleep %5\n.Lb%=:\n\tds_read_b32 %0, %3\n\ts_waitcnt lgkmcnt(0)\n\t"     \
                 "v_readfirstlane_b32 %2, %0\n\t" TEST SAFE_AND "s_cbranch_scc1 .La%="                                  \
                 : "=&v"(v), NOP_(c), "=&s"(t)                                                                           \
                 : "v"(addr), "s"(k), "n"(SL)                                                                            \
                 : "memory", "scc")
template <int SL>
DEVI void spin_ge(unsigned addr, unsigned k) {
  unsigned v, c = 1u << 18, t;
  SPIN_ASM("s_cmp_lt_u32 %2, %4\n\t");
}
template <int SL>
DEVI void spin_b4(unsigned addr, unsigned n) {
  unsigned v, c = 1u << 18, t;
  const unsigned k = (0x80u - n) * 0x01010101u;
  SPIN_ASM("s_add_u32 %2, %2, %4\n\ts_andn2_b32 %2, 0x80808080, %2\n\t");
}
DEVI void msync(unsigned addr) {
  asm volatile("s_waitcnt lgkmcnt(0)" ::: "memory");
  asm volatile("s_mov_b64 exec, 1\n\tds_add_u32 %0, %1\n\ts_mov_b64 exec, -1" ::"v"(addr), "v"(vconst(1u)) : "memory");
  spin_ge<0>(addr, 8u);
}
static_assert(K1R_PO + 13824 + 256 <= 163840 - 128 && K1R_ST_OFF + 144 * 8 <= 3 * K1R::SB, "K1R LDS");

DEVI void touch64(const char* sbase, unsigned voff, unsigned m0val) {
  asm volatile("s_mov_b32 m0, %2\n\ts_nop 0\n\tglobal_load_lds_dword %0, %1" : : "v"(voff), "s"(sbase), "s"(m0val) : "memory");
}

template <int MT, bool G1>
DEVI void k1_gemm_raw(char* smem, int hp, int mt0, int lane MF_ARG) {
  constexpr int NT = 4, nt0 = G1 ? 4 : 0;
  const unsigned lds0 = (unsigned)(size_t)smem;
  const int g = lane >> 4, r = lane & 15;
  unsigned ta[NT];
#pragma unroll
  for (int nt = 0; nt < NT; ++nt) {
    const int j = (nt0 + nt) * 16 + g * 4, seg = j / 40, cj = j - seg * 40 - (hp & 1) * 4;
    const int to = (seg < 3 && cj >= 0 && cj < 36) ? seg * 80 + cj * 2 + (cj >= 20 ? 8 : 0) : (seg < 3 ? 40 : 120);
    ta[nt] = lds0 + (unsigned)(K1R_T_OFF + (mt0 * 16 + r) * K1_TP + to);
    asm volatile("" : "+v"(ta[nt]));
  }
  unsigned sa = lds0 + (unsigned)(K1R_ST_OFF + (mt0 * 16 + r) * 8), ca = lds0 + (unsigned)(K1R_CS_OFF + (nt0 * 16 + g * 4) * 4);
  asm volatile("" : "+v"(sa), "+v"(ca));
  f32x4 acc[MT][NT];
#pragma unroll
  for (int mt = 0; mt < MT; ++mt)
#pragma unroll
    for (int nt = 0; nt < NT; ++nt) acc[mt][nt] = f32x4{0.f, 0.f, 0.f, 0.f};
  {
    GemmMath<K1R, MT, 4, 3, 4, false, 0, G1> gm(acc);
    if constexpr (G1) {
#pragma unroll
      for (int mt = 0; mt < MT; ++mt) gm.accq[mt] = f32x4{0.f, 0.f, 0.f, 0.f};
    }
    MF(14);
    if constexpr (!G1) {
      asm volatile("s_waitcnt vmcnt(0)" ::: "memory");
    }
    MF(15);
    gm.template run<9>(lds0, mt0, nt0, lane, 0u);
    MF(2);
    if constexpr (G1) {
#pragma unroll
      for (int mt = 0; mt < MT; ++mt) {
        const f32x4 q = gm.accq[mt];
        const float d = (r & 2) ? ((r & 1) ? q[3] : q[2]) : ((r & 1) ? q[1] : q[0]);
        const float qq = sum_g(g == (r >> 2) ? d : 0.f);
        float mb, rinv;
        ln_finish(acc[mt][3][0], qq, 1.0f / (float)D_, mb, rinv);
        const f32x2 st = {-mb, rinv};
        if (g == 2) asm volatile("ds_write_b64 %0, %1 offset:%2" ::"v"(sa), "v"(st), "n"(mt * 128) : "memory");
      }
    }
  }
  if (mt0 == 0 && r == 0) {
#pragma unroll
    for (int nt = 0; nt < NT; ++nt) *reinterpret_cast<f32x4*>(smem + K1R_CS_OFF + ((nt0 + nt) * 16 + g * 4) * 4) = acc[0][nt];
  }
  msync(lds0 + (unsigned)K1R_CNT);
  MF(3);
  f32x2 st[MT];
  f32x4 cs[NT];
#pragma unroll
  for (int nt = 0; nt < NT; ++nt) asm volatile("ds_read_b128 %0, %1 offset:%2" : "=v"(cs[nt]) : "v"(ca), "n"(nt * 64) : "memory");
#pragma unroll
  for (int mt = 0; mt < MT; ++mt) asm volatile("ds_read_b64 %0, %1 offset:%2" : "=v"(st[mt]) : "v"(sa), "n"(mt * 128) : "memory");
#pragma unroll
  for (int i = 0; i < MT; ++i) asm volatile("s_waitcnt lgkmcnt(0)" : "+v"(st[i]));
#pragma unroll
  for (int i = 0; i < NT; ++i) asm volatile("" : "+v"(cs[i]));
#pragma unroll
  for (int mt = 0; mt < MT; ++mt) {
    f32x2 M = {st[mt][0], st[mt][0]}, R = {st[mt][1], st[mt][1]};
    asm volatile("" : "+v"(M), "+v"(R));
#pragma unroll
    for (int nt = 0; nt < NT; ++nt) {
      const f32x2 v0 = R * __builtin_elementwise_fma(M, f32x2{cs[nt][0], cs[nt][1]}, f32x2{acc[mt][nt][0], acc[mt][nt][1]});
      const f32x2 v1 = R * __builtin_elementwise_fma(M, f32x2{cs[nt][2], cs[nt][3]}, f32x2{acc[mt][nt][2], acc[mt][nt][3]});
      acc[mt][nt][0] = __uint_as_float(pack2v(v0));
      acc[mt][nt][1] = __uint_as_float(pack2v(v1));
      const f32x2 pk = {acc[mt][nt][0], acc[mt][nt][1]};
      asm volatile("ds_write_b64 %0, %1 offset:%2" ::"v"(ta[nt]), "v"(pk), "n"(mt * 16 * K1_TP) : "memory");
    }
  }
}

template <int N>
DEVI void wait_vm() {
  asm volatile("s_waitcnt vmcnt(%0)" ::"n"(N) : "memory");
}

constexpr int LDS_ALL = 163840;
constexpr int LW_OFF = LDS_ALL - 128;
constexpr int LW_GO = 0, LW_T = 2, LW_O = 4  , LW_X1 = 6, LW_H = 8, LW_X2 = 12, LW_PK = 13, LW_Z = 14, LW_PAIR = 16, LW_AD = 20,
              LW_L = 22, LW_S = 24, LW_ST = 29, LW_HELLO = 30, LW_Q = 31;
DEVI void announce(unsigned addr, unsigned n, char* ws, int kind, int who, bool act) {
  const char* cb = uni_ptr(ws + OFF_CNT + (size_t)kind * 256 * 128);
  const unsigned long long mask = __ballot(act);
  unsigned t, u;
  asm volatile(
      "s_waitcnt vmcnt(0)\n\t"
      "s_mov_b64 exec, 1\n\t"
      "ds_add_rtn_u32 %0, %2, %3\n\t"
      "s_waitcnt lgkmcnt(0)\n\t"
      "v_readfirstlane_b32 %1, %0\n\t"
      "s_cmp_eq_u32 %1, %4\n\t"
      "s_cselect_b64 exec, %5, 0\n\t"
      "global_atomic_add %6, %3, %7 sc1\n\t"
      "s_mov_b64 exec, -1\n\t"
      "s_nop 0"
      : "=&v"(t), "=&s"(u)
      : "v"(addr), "v"(vconst(1u)), "s"(n - 1u), "s"(mask), "v"((unsigned)(who & 255) * 128u), "s"(cb)
      : "memory", "scc");
}
DEVI void cnt_wait(const char* ws, int kind, int who, unsigned n) {
  const char* cp = uni_ptr(ws + OFF_CNT + (size_t)(kind * 256 + who) * 128);
  unsigned v, c = SPIN_MEM, t;
  asm volatile(
      ".La%=:\n\tglobal_load_dword %0, %3, %4 sc1\n\ts_sub_u32 %1, %1, 1\n\ts_waitcnt vmcnt(0)\n\tv_readfirstlane_b32 %2, %0\n\ts_cmp_lg_u32 %2, %5\n\t"
      "s_cselect_b32 %2, %1, 0\n\ts_cmp_lg_u32 %2, 0\n\ts_cbranch_scc1 .La%="
      : "=&v"(v), "+s"(c), "=&s"(t)
      : "v"(vconst(0u)), "s"(cp), "s"(n - 1u)
      : "memory", "scc");
}
DEVI void dma16_sc1(const char* sbase, unsigned voff, unsigned m0val) {
  asm volatile("s_mov_b32 m0, %2\n\ts_nop 0\n\tglobal_load_lds_dwordx4 %0, %1 sc1"
               :
               : "v"(voff), "s"(sbase), "s"(m0val)
               : "memory");
}
template <int N, bool SC1>
DEVI void dma4(const char* sbase, unsigned v0, unsigned v1, unsigned v2, unsigned v3, unsigned m0val) {
#define DMA4(S)                                                                                                               \
  asm volatile("s_mov_b32 m0, %5\n\ts_nop 0\n\tglobal_load_lds_dwordx4 %0, %4" S "\n\t.if %6 > 1\n\tglobal_load_lds_dwordx4 %1, %4 offset:1024" S \
               "\n\t.endif\n\t.if %6 > 2\n\tglobal_load_lds_dwordx4 %2, %4 offset:2048" S                                    \
               "\n\t.endif\n\t.if %6 > 3\n\tglobal_load_lds_dwordx4 %3, %4 offset:3072" S "\n\t.endif"                      \
               :                                                                                                              \
               : "v"(v0), "v"(v1), "v"(v2), "v"(v3), "s"(sbase), "s"(m0val), "n"(N)                                           \
               : "memory")
  if constexpr (SC1) DMA4(" sc1");
  else DMA4("");
}
template <int N>
DEVI void dma_seq(const char* src, unsigned voff, unsigned m0val) {
  if constexpr (N > 0) {
    dma4<(N < 4 ? N : 4), true>(src, voff, voff, voff, voff, m0val);
    dma_seq<N - 4>(src + 4096, voff, m0val + 4096u);
  }
}
DEVI void bar() { __builtin_amdgcn_s_barrier(); }
DEVI void lds_inc(unsigned addr, unsigned by = 1u) {
  asm volatile("s_mov_b64 exec, 1\n\tds_add_u32 %0, %1\n\ts_mov_b64 exec, -1" ::"v"(addr), "v"(vconst(by)) : "memory");
}
DEVI void lds_spin(unsigned addr, unsigned n) {
  spin_ge<1>(addr, n);
}
template <int SL = 1>
DEVI void lds_spin4(unsigned addr, unsigned n) {
  spin_b4<SL>(addr, n);
}
DEVI int lane_id() {
  int l;
  asm volatile("v_mbcnt_lo_u32_b32 %0, -1, 0\n\tv_mbcnt_hi_u32_b32 %0, -1, %0" : "=v"(l));
  __builtin_assume((unsigned)l < 64u);
  return l;
}

template <int I>
DEVI u32x4 tr8(const u32x4 (&v)[8]) {
  constexpr unsigned sel = (I & 1) ? 0x07060302u : 0x05040100u;
  return u32x4{__builtin_amdgcn_perm(v[1][I / 2], v[0][I / 2], sel), __builtin_amdgcn_perm(v[3][I / 2], v[2][I / 2], sel),
               __builtin_amdgcn_perm(v[5][I / 2], v[4][I / 2], sel), __builtin_amdgcn_perm(v[7][I / 2], v[6][I / 2], sel)};
}
template <int PITCH, bool SUM, int... I>
DEVI void tr8_store(const u32x4 (&v)[8], unsigned dst, float (&cs)[8], std::integer_sequence<int, I...>) {
  auto one = [&](auto I_) {
    const u32x4 o = tr8<I_>(v);
    const unsigned d_ = dst;
    asm volatile("ds_write_b128 %0, %1 offset:%2" ::"v"(d_), "v"(o), "n"(I_ * PITCH) : "memory");
    if constexpr (SUM) {
#pragma unroll
      for (int d = 0; d < 4; ++d) {
        const unsigned od = o[d];
        cs[I_] = __builtin_amdgcn_fdot2_f32_bf16(__builtin_bit_cast(bf16x2, od), __builtin_bit_cast(bf16x2, 0x3f803f80u), cs[I_], false);
      }
    }
  };
  (one(std::integral_constant<int, I>{}), ...);
}
template <int NB>
DEVI void lds_out(unsigned src, char* dst, int lane) {
  constexpr int N = (NB + 1023) / 1024;
  u32x4 t[N];
  const unsigned a = src + (unsigned)lane * 16u;
#pragma unroll
  for (int i = 0; i < N; ++i) asm volatile("ds_read_b128 %0, %1 offset:%2" : "=v"(t[i]) : "v"(a), "n"(i * 1024) : "memory");
  const char* d = uni_ptr(dst);
#pragma unroll
  for (int i = 0; i < N; ++i) {
    asm volatile("s_waitcnt lgkmcnt(%1)" : "+v"(t[i]) : "n"(N - 1 - i) : "memory");
    if (i < N - 1 || lane < (NB - (N - 1) * 1024) / 16)
      asm volatile("global_store_dwordx4 %0, %1, %2 sc1\n\ts_nop 1" ::"v"((unsigned)lane * 16u), "v"(t[i]), "s"(d + i * 1024) : "memory");
  }
}
template <int A, int B, class F>
DEVI void static_for(F f) {
  if constexpr (A < B) {
    f(std::integral_constant<int, A>{});
    static_for<A + 1, B>(f);
  }
}
constexpr int WSP_V = 3;
constexpr int RAW_S1 = 6;
constexpr int RAW_S0 = 4;
template <int E>
struct RawX {
  static constexpr int first(int s) {
    return s <= RAW_S0 ? 0 : (s > RAW_S1 ? E : (E * (s - RAW_S0) + RAW_S1 - RAW_S0) / (RAW_S1 + 1 - RAW_S0));
  }
  static constexpr int n(int s) { return first(s + 1) - first(s); }
};
struct RawLd {
  static constexpr int VPS = 10, SB = K1R::SB;
  unsigned xoff[5], woff[5], m0x;
  unsigned long long xact0, wact[5], tag;
  const char *xp, *wp;
  int qb;
  DEVI void q_hook(int st, char* ws, unsigned lds0, int hp) {
    if ((qb >> 1) == st) {
      const int lane = lane_id(), g = lane >> 4;
      const unsigned a = lds0 + (unsigned)((st & 3) * SB + K1R::XB + ((qb & 1) * 32 + 16 * (g >> 1) + 4 * (g & 1) + ((lane >> 2) & 3)) * 288 +
                                           (lane & 3) * 8 + (hp & 1) * 8);
      const unsigned a2 = a + ((lane & 3) >= 1 ? 8u : 0u), a4 = a + ((lane & 3) >= 2 ? 8u : 0u);
      u32x2 qf[7][2];
#pragma unroll
      for (int nt = 0; nt < 7; ++nt)
        asm volatile("ds_read_b64_tr_b16 %0, %2 offset:%3\n\tds_read_b64_tr_b16 %1, %2 offset:%4"
                     : "=&v"(qf[nt][0]), "=&v"(qf[nt][1])
                     : "v"(nt == 2 ? a2 : (nt == 4 ? a4 : a)), "n"(nt * 32 + (nt > 4 ? 16 : (nt > 2 ? 8 : 0))),
                       "n"(2304 + nt * 32 + (nt > 4 ? 16 : (nt > 2 ? 8 : 0)))
                     : "memory");
      const char* d = uni_ptr(ws + OFF_PQ + (size_t)(hp * 18 + qb) * 7168 + 3072);
#pragma unroll
      for (int nt = 0; nt < 7; ++nt) {
        asm volatile("s_waitcnt lgkmcnt(%2)" : "+v"(qf[nt][0]), "+v"(qf[nt][1]) : "n"(12 - 2 * nt) : "memory");
        const u32x4 v = {qf[nt][0][0], qf[nt][0][1], qf[nt][1][0], qf[nt][1][1]};
        asm volatile("global_store_dwordx4 %0, %1, %2 offset:%3 sc1\n\ts_nop 1" ::"v"((unsigned)lane * 16u), "v"(v), "s"(d), "n"(nt * 1024 - 3072) : "memory");
      }
      if (st == 8) lds_wr32(lds0 + (unsigned)(LW_OFF + LW_Q * 4), 1u);
    }
  }
  DEVI void stage_x(unsigned so) {
    asm volatile(
        "s_mov_b32 m0, %6\n\ts_mov_b64 exec, %7\n\tglobal_load_lds_dwordx4 %0, %5\n\ts_mov_b64 exec, -1\n\t"
        "global_load_lds_dwordx4 %1, %5 offset:1024\n\tglobal_load_lds_dwordx4 %2, %5 offset:2048\n\tglobal_load_lds_dwordx4 %3, %5 offset:3072\n\t"
        "s_add_u32 m0, m0, 0x1000\n\ts_mov_b32 exec_hi, 0\n\tglobal_load_lds_dwordx4 %4, %5\n\ts_mov_b32 exec_hi, -1" ::"v"(xoff[0]),
        "v"(xoff[1]), "v"(xoff[2]), "v"(xoff[3]), "v"(xoff[4]), "s"(xp), "s"(m0x + so), "s"(xact0)
        : "memory", "scc");
    xp += 128;
  }
  DEVI void stage_w(unsigned so) {
    asm volatile(
        "s_mov_b32 m0, %6\n\ts_mov_b64 exec, %7\n\tglobal_load_lds_dwordx4 %0, %5\n\ts_mov_b64 exec, %8\n\t"
        "global_load_lds_dwordx4 %1, %5 offset:1024\n\ts_mov_b64 exec, %9\n\tglobal_load_lds_dwordx4 %2, %5 offset:2048\n\t"
        "s_mov_b64 exec, %10\n\tglobal_load_lds_dwordx4 %3, %5 offset:3072\n\ts_add_u32 m0, m0, 0x1000\n\ts_mov_b64 exec, %11\n\t"
        "global_load_lds_dwordx4 %4, %5\n\ts_mov_b64 exec, -1" ::"v"(woff[0]),
        "v"(woff[1]), "v"(woff[2]), "v"(woff[3]), "v"(woff[4]), "s"(wp), "s"(m0x + so + (unsigned)K1R::XB), "s"(wact[0]), "s"(wact[1]),
        "s"(wact[2]), "s"(wact[3]), "s"(wact[4])
        : "memory", "scc");
    wp += 64 * NQKV * 2;
  }
  DEVI void stage(unsigned so) {
    stage_x(so);
    stage_w(so);
  }
  template <class F>
  DEVI void front(const u16* x, const u16* wqkv, const u16* wo, char* ws, int bid, unsigned lds0, int wl, int lane, int rt, int hp,
                  int& pj, F jobs, const char*& pg MF_ARG) {
    xp = uni_ptr((const char*)x - 3072);
    wp = uni_ptr((const char*)wqkv + ((hp * 72) & ~15) - 3072);
    m0x = lds0 + (unsigned)(wl * 288 * 16);
    xact0 = wl ? ~0ull : ~0xffull;
    const int row0 = wl * 36 + (lane >> 3);
#pragma unroll
    for (int i = 0; i < 5; ++i) {
      const int row = row0 + i * 8;
      int gr = rt * 128 - 16 + row;
      if (gr < 0) gr = 0;
      xoff[i] = (unsigned)(gr * (D_ * 2) + (3 - (i & 3)) * 1024) + (unsigned)(((lane ^ (row >> 1)) & 7) * 16);
    }
    stage_x(0u);
    u32x2 tg;
    asm volatile("global_load_dwordx2 %0, %1, %2 sc1" : "=v"(tg) : "v"(0u), "s"(uni_ptr(ws + OFF_TAG)) : "memory");
    asm volatile("" : "+v"(lane));
#pragma unroll
    for (int i = 0; i < 5; ++i) {
      const unsigned c = (unsigned)(i * 64 + (lane & 63));
      const unsigned wr = (c * 3641u) >> 16, wc = c - wr * 18u, seg = (wc * 13u) >> 6;
      wact[i] = __ballot(wc < 15u) & (i < 4 ? ~0ull : 0xffffffffull);
      woff[i] = (unsigned)(wl * 16 + swap23((int)wr)) * (unsigned)(NQKV * 2) + seg * (unsigned)(D_ * 2 - 80) + wc * 16u + (unsigned)((3 - (i & 3)) * 1024);
    }
    stage_w(0u);
    stage((unsigned)SB);
    jobs();
    MF(10);
    asm volatile("s_waitcnt vmcnt(%1)" : "+v"(tg) : "n"(VPS) : "memory");
    tag = (((unsigned long long)(unsigned)__builtin_amdgcn_readfirstlane(tg[1]) << 32) | (unsigned)__builtin_amdgcn_readfirstlane(tg[0])) +
          0x9e3779b97f4a7c15ull;
    MF(1);
    __builtin_amdgcn_s_barrier();
    MF(15);
    if (wl == 0 && lane == 0)
      asm volatile("global_store_dwordx2 %0, %1, off sc1\n\ts_nop 1" ::"v"(ws + OFF_HELLO + bid * 8), "v"(tag) : "memory");
    stage((unsigned)(2 * SB));
    qb = wl == 3 ? rt : ((wl == 2 && rt < 2) ? 16 + rt : 99);
    q_hook(0, ws, lds0, hp);
    const int nb = pj;
    const bool po = pj >= 0 && wl < 2;
    u32x4 v[8];
    unsigned da = 0u;
    if (po) {
      const int l54 = lane < 54 ? lane : 53, gl = l54 / 9, c = l54 - gl * 9, kl = wl * 6 + gl, t = nb / 6, kg = (nb - t * 6) * 12 + kl;
      da = lds0 + (unsigned)(K1R_PO + (kl >> 2) * 4608 + (16 * (kl & 1) + ((kl >> 1) & 1)) * 144 + c * 16);
      const unsigned sa[2] = {(unsigned)(okmap(kg * 8) * D_ + t * 72 + c * 8) * 2u, (unsigned)(okmap(kg * 8 + 4) * D_ + t * 72 + c * 8) * 2u};
      const char* ps = uni_ptr((const char*)wo);
#pragma unroll
      for (int j = 0; j < 8; ++j)
        asm volatile("global_load_dwordx4 %0, %1, %2 offset:%3" : "=v"(v[j]) : "v"(sa[j / 4]), "s"(ps), "n"((j % 4) * D_ * 2) : "memory");
    } else {
#pragma unroll
      for (int j = 0; j < 8; ++j) asm volatile("" : "=v"(v[j]));
    }
    unsigned slot = (unsigned)(3 * SB), pd = 0u;
    const unsigned gk = (unsigned)(bid >> 3) & 15u;
    unsigned gp = gk * (unsigned)SZ_X;
    gp = gk > 5u ? (unsigned)(OFF_Z2 - GAP) + (gk - 5u) * (unsigned)SZ_Z : gp;
    gp = gk > 9u ? (unsigned)(OFF_PO - GAP) : gp;
    gp = gk > 10u ? (unsigned)(OFF_PI - GAP) : gp;
    gp = gk > 11u ? (unsigned)(OFF_PW - GAP) : gp;
    gp = gk > 12u ? (unsigned)(OFF_CSP - GAP) : gp;
#pragma unroll 1
    for (int S = 0; S < RAW_S0; ++S) {
      if (wl == 2 && S == 1) asm volatile("s_load_dword %0, %1, 0\n\ts_load_dword %0, %2, 0" : "+s"(pd) : "s"(ws + gp), "s"(pg));
      asm volatile("" : "+s"(pd));
      wait_vm<VPS>();
      __builtin_amdgcn_s_barrier();
      if (S == 0) MF(16);
      if (S == 1) MF(17);
      if (S == 2) MF(18);
      if (S == 3) MF(19);
      stage(slot);
      slot = (slot == (unsigned)(3 * SB)) ? 0u : slot + (unsigned)SB;
      q_hook(S + 1, ws, lds0, hp);
      if (po && S == 1) {
        float cs[8];
        asm volatile("s_nop 0 ; LOOPWAIT" : "+v"(v[0]), "+v"(v[1]), "+v"(v[2]), "+v"(v[3]), "+v"(v[4]), "+v"(v[5]), "+v"(v[6]), "+v"(v[7]));
        tr8_store<288, false>(v, da, cs, std::make_integer_sequence<int, 8>{});
        asm volatile("s_waitcnt lgkmcnt(0)" ::: "memory");
      }
      if (po && S == 2) lds_out<6912>(lds0 + (unsigned)(K1R_PO + wl * 6912), ws + OFF_PO + (size_t)nb * 13824 + wl * 6912, lane);
    }
    asm volatile("s_waitcnt lgkmcnt(0)" : "+s"(pd));
  }
};
template <bool JOB>
DEVI void k1_raw_loader(RawLd& ld, const u16* __restrict__ w_in, const u16* __restrict__ w_out, char* __restrict__ ws,
                        unsigned long long tag, int bid, unsigned lds0, int wl, int lane, int rt, int hp, int ct, int job, int pj MF_ARG) {
  constexpr int VPS = RawLd::VPS, SB = RawLd::SB, E = JOB ? 16 : 0;
  const bool isi = job < 144;
  const int kb = isi ? job >> 3 : job - 144;
  const int pitch = isi ? F_ * 2 : D_ * 2;
  const int l36 = lane < 36 ? lane : 35;
  unsigned sa = (unsigned)l36 * 16u, da;
  if (isi) da = lds0 + (unsigned)((l36 >= 18 ? 9216 - 288 : 0) + wl * 8 * 288 + l36 * 16);
  else da = lds0 + (unsigned)((l36 / 9) * (4608 - 144) + (16 * (wl & 1) + (wl >> 1)) * 144 + l36 * 16);
  asm volatile("" : "+v"(sa), "+v"(da));
  u32x4 v[2][8];
  const char* ps = uni_ptr((const char*)(isi ? w_in + (job & 7) * 576 : w_out) + (size_t)(kb * 32 + wl * 8) * pitch);
  auto extra = [&](auto J_) {
    constexpr int j = J_;
    const char* q = ps + (j / 2) * pitch;
    u32x4 t;
    asm volatile("global_load_dwordx4 %0, %1, %2 offset:%3" : "=v"(t) : "v"(sa), "s"(q), "n"((j % 2) * 576) : "memory");
    v[j % 2][j / 2] = t;
  };
  auto step = [&](auto S_) {
    constexpr int S = S_;
    wait_vm<RawX<E>::n(S - 2) + (S <= 6 ? VPS : 0) + RawX<E>::n(S - 1)>();
    __builtin_amdgcn_s_barrier();
    MF(16 + S);
    if constexpr (S == RAW_S0) {
      static_assert(RAW_S0 >= 4, "wo blocks");
      if (pj >= 0 && wl == 0 && lane == 0)
        asm volatile("global_store_dwordx2 %0, %1, off sc1\n\ts_nop 1" ::"v"(ws + OFF_POT + (pj / 6) * 128 + (pj % 6) * 8), "v"(tag) : "memory");
    }
    if constexpr (S <= 5) ld.stage((unsigned)(((S + 3) % 4) * SB));
    ld.q_hook(S + 1, ws, lds0, hp);
    static_for<RawX<E>::first(S), RawX<E>::first(S + 1)>(extra);
  };
  const char* hp_ = ws + OFF_HELLO + lane * 32;
  u32x4 a, b;
  if (wl == 1) asm volatile("global_load_dwordx4 %0, %2, off sc1\n\tglobal_load_dwordx4 %1, %2, off offset:16 sc1" : "=&v"(a), "=&v"(b) : "v"(hp_) : "memory");
  else asm volatile("" : "=v"(a), "=v"(b));
  static_for<RAW_S0, 8>(step);
  MF(2);
  if (wl == 1) {
    asm volatile("" : "+v"(a), "+v"(b));
    const unsigned t0 = (unsigned)tag, t1 = (unsigned)(tag >> 32);
#pragma unroll 1
    for (unsigned spin = 0; spin < SPIN_MEM; ++spin) {
      const unsigned bad = ((a[0] ^ t0) | (a[2] ^ t0) | (b[0] ^ t0) | (b[2] ^ t0)) | ((a[1] ^ t1) | (a[3] ^ t1) | (b[1] ^ t1) | (b[3] ^ t1));
      if (!__any(bad != 0u)) break;
      __builtin_amdgcn_s_sleep(4);
      asm volatile("global_load_dwordx4 %0, %2, off sc1\n\tglobal_load_dwordx4 %1, %2, off offset:16 sc1\n\ts_waitcnt vmcnt(0)"
                   : "=&v"(a), "=&v"(b)
                   : "v"(hp_)
                   : "memory");
    }
    lds_wr32(lds0 + (unsigned)(LW_OFF + LW_HELLO * 4), 1u);
    if (bid == 0 && lane == 0) asm volatile("global_store_dwordx2 %0, %1, off sc1\n\ts_nop 1" ::"v"(ws + OFF_TAG), "v"(tag) : "memory");
  }
  MF(11);
  if (rt < 2) lds_spin(lds0 + (unsigned)(LW_OFF + LW_Q * 4), 1u);
  lds_spin(lds0 + (unsigned)K1R_CNT, 8u);
  if constexpr (JOB) {
#pragma unroll
    for (int b = 0; b < 2; ++b)
      asm volatile("s_waitcnt vmcnt(0)"
                   : "+v"(v[b][0]), "+v"(v[b][1]), "+v"(v[b][2]), "+v"(v[b][3]), "+v"(v[b][4]), "+v"(v[b][5]), "+v"(v[b][6]), "+v"(v[b][7])
                   :
                   : "memory");
  }
  MF(12);
  const char* tp = ws + OFF_POT + ct * 128 + (lane < 6 ? lane : 5) * 8;
  u32x2 f;
  asm volatile("global_load_dwordx2 %0, %1, off sc1" : "=v"(f) : "v"(tp) : "memory");
  if constexpr (JOB) {
    static_assert(SB == 4 * 9216, "staging");
#pragma unroll
    for (int b = 0; b < 2; ++b) {
      float cs[8] = {0.f, 0.f, 0.f, 0.f, 0.f, 0.f, 0.f, 0.f};
      tr8_store<288, true>(v[b], da + (unsigned)(b * 18432), cs, std::make_integer_sequence<int, 8>{});
      const f32x4 c0 = {cs[0], cs[1], cs[2], cs[3]}, c1 = {cs[4], cs[5], cs[6], cs[7]};
      const unsigned ca = lds0 + (unsigned)(3 * SB + wl * 2304) + sa * 2u;
      asm volatile("ds_write_b128 %0, %1 offset:%2" ::"v"(ca), "v"(c0), "n"(b * 1152) : "memory");
      asm volatile("ds_write_b128 %0, %1 offset:%2" ::"v"(ca), "v"(c1), "n"(b * 1152 + 16) : "memory");
    }
    lds_inc(lds0 + (unsigned)(LW_OFF + LW_ST * 4));
    lds_spin(lds0 + (unsigned)(LW_OFF + LW_ST * 4), 4u);
    if (isi) {
      const int j = (job & 7) * 4 + wl, ti = j < 16 ? 2 * j + 1 : 2 * (j - 16);
      lds_out<9216>(lds0 + (unsigned)(wl * 9216), ws + OFF_PI + (size_t)(ti * 18 + kb) * 9216, lane);
      f32x4 c[4];
      const unsigned ca = lds0 + (unsigned)(3 * SB + wl * 576) + sa;
#pragma unroll
      for (int i = 0; i < 4; ++i) asm volatile("ds_read_b128 %0, %1 offset:%2" : "=v"(c[i]) : "v"(ca), "n"(i * 2304) : "memory");
      asm volatile("s_waitcnt lgkmcnt(0)" : "+v"(c[0]), "+v"(c[1]), "+v"(c[2]), "+v"(c[3]));
      const f32x4 t = (c[0] + c[1]) + (c[2] + c[3]);
      if (lane < 36) pk_st16(ws + OFF_CSP + ((size_t)kb * F_ + ti * 144) * 4 + sa, __builtin_bit_cast(u32x4, t));
    } else {
      lds_out<4608>(lds0 + (unsigned)(wl * 9216), ws + OFF_PW + (size_t)(wl * 144 + kb) * 4608, lane);
      lds_out<4608>(lds0 + (unsigned)(wl * 9216 + 4608), ws + OFF_PW + (size_t)(wl * 144 + 72 + kb) * 4608, lane);
    }
  }
  MF(8);
  announce(lds0 + (unsigned)(LW_OFF + LW_PK * 4), 4u, ws, CN_PK, isi ? 8 + (job & 3) * 4 + lane : 24 + lane, JOB && lane < (isi ? 4 : 8));
  MF(9);
  if (wl == 0) {
    asm volatile("" : "+v"(f));
#pragma unroll 1
    for (unsigned spin = 0; spin < SPIN_MEM; ++spin) {
      if (!__any(f[0] != (unsigned)tag || f[1] != (unsigned)(tag >> 32))) break;
      __builtin_amdgcn_s_sleep(2);
      asm volatile("global_load_dwordx2 %0, %1, off sc1\n\ts_waitcnt vmcnt(0)" : "=&v"(f) : "v"(tp) : "memory");
    }
  }
}

template <int MT, int NT>
DEVI void k1_gemm(char* smem, const char* ws, int rt, int hp, int mt0, int nt0,
                  bool late, int lane MK_ARG) {
  const unsigned lds0 = (unsigned)(size_t)smem;
  const int g = lane >> 4;
  f32x4 cs[NT];
  unsigned ta[NT];
  unsigned cad[NT];
#pragma unroll
  for (int nt = 0; nt < NT; ++nt) {
    int c = (nt0 + nt) * 16 + g * 4;
    if (c > 104) c = 104;
    const int part = c / 36, ci = c - part * 36;
    cad[nt] = lds0 + (unsigned)(K1_CS_OFF + (part * 40 + (hp & 1) * 4 + ci) * 4);
    asm volatile("" : "+v"(cad[nt]));
    const int to = (nt0 + nt) * 16 + g * 4 > 104 ? 120 : part * 80 + ci * 2 + (ci >= 20 ? 8 : 0);
    ta[nt] = lds0 + (unsigned)(K1_T_OFF + (mt0 * 16 + (lane & 15)) * K1_TP + to);
    asm volatile("" : "+v"(ta[nt]));
  }
  unsigned sa = lds0 + (unsigned)(K1_ST_OFF + (mt0 * 16 + (lane & 15)) * 8);
  asm volatile("" : "+v"(sa));
  f32x4 acc[MT][NT];
#pragma unroll
  for (int mt = 0; mt < MT; ++mt)
#pragma unroll
    for (int nt = 0; nt < NT; ++nt) acc[mt][nt] = f32x4{0.f, 0.f, 0.f, 0.f};
  f32x2 st[MT];
  if (late) bar();
  if constexpr (NT == 7) cnt_wait(ws, CN_X2, rt * 16 + hp, rt ? 24u : 16u);
  bar();
  MK(1);
  {
    GemmMath<K1C, MT, NT, (NT == 7 ? 5 : 3), (NT == 3 ? 6 : NT), true, 2, false, K1_WS> gm(acc);
    gm.setup(lds0, mt0, nt0, lane, 0u);
    bar();
#pragma unroll
    for (int nt = 0; nt < NT; ++nt) asm volatile("ds_read_b128 %0, %1" : "=v"(cs[nt]) : "v"(cad[nt]));
    gm.first();
    gm.template mid<8>();
#pragma unroll
    for (int mt = 0; mt < MT; ++mt) asm volatile("ds_read_b64 %0, %1 offset:%2" : "=v"(st[mt]) : "v"(sa), "n"(mt * 128));
    asm volatile("s_waitcnt vmcnt(0)" ::: "memory");
    gm.last();
  }
#pragma unroll
  for (int nt = 0; nt < NT; ++nt) asm volatile("" : "+v"(cs[nt]));
#pragma unroll
  for (int mt = 0; mt < MT; ++mt) asm volatile("" : "+v"(st[mt]));
  MK(2);
#pragma unroll
  for (int mt = 0; mt < MT; ++mt) {
    f32x2 M = {st[mt][0], st[mt][0]}, R = {st[mt][1], st[mt][1]};
    asm volatile("" : "+v"(M), "+v"(R));
#pragma unroll
    for (int nt = 0; nt < NT; ++nt) {
      const f32x2 v0 = R * __builtin_elementwise_fma(M, f32x2{cs[nt][0], cs[nt][1]}, f32x2{acc[mt][nt][0], acc[mt][nt][1]});
      const f32x2 v1 = R * __builtin_elementwise_fma(M, f32x2{cs[nt][2], cs[nt][3]}, f32x2{acc[mt][nt][2], acc[mt][nt][3]});
      acc[mt][nt][0] = __uint_as_float(pack2v(v0));
      acc[mt][nt][1] = __uint_as_float(pack2v(v1));
      const f32x2 pk = {acc[mt][nt][0], acc[mt][nt][1]};
      asm volatile("ds_write_b64 %0, %1 offset:%2" ::"v"(ta[nt]), "v"(pk), "n"(mt * 16 * K1_TP) : "memory");
    }
  }
}

DEVI float vmax(float a, float b) {
  float r;
  asm("v_max_f32 %0, %1, %2" : "=v"(r) : "v"(a), "v"(b));
  return r;
}
struct Attn {
  unsigned ka, kb, va, vb, m0, m1, m2, pm[2], oa, pa;
  f32x4 mk;
  unsigned long long sel[4];
  const char* ob;
  DEVI void setup(unsigned T, char* o, int r0, int hp, int wave, int lane) {
    const int g = lane >> 4, r = lane & 15, blk = wave >> 1, hd = wave & 1;
    const unsigned co = (unsigned)(g < 2 ? hd * 48 + g * 16 : 32);
    ka = T + (unsigned)((blk * 32 + 2 * r) * K1_TP) + co;
    kb = T + (unsigned)((blk * 32 + 2 * (r & 7)) * K1_TP) + co;
    const int vr = 4 * g + ((lane >> 2) & 3);
    va = T + (unsigned)((blk * 32 + 2 * vr) * K1_TP + (lane & 3) * 8);
    vb = T + (unsigned)((blk * 32 + 2 * (vr & 7)) * K1_TP + (lane & 3) * 8);
    m0 = (g < 2 || (g == 2 && hd == 0)) ? ~0u : 0u;
    m1 = (g < 2 || (g == 2 && hd == 1)) ? ~0u : 0u;
    m2 = g < 2 ? ~0u : 0u;
    pm[0] = pm[1] = 0u;
    const float ci = __uint_as_float(vconst(0xff800000u));
    const unsigned ch = vconst(0xffffu);
#pragma unroll
    for (int e = 0; e < 4; ++e) {
      const int w1 = r + 8 - 4 * g - e;
      mk[e] = (w1 & 15) <= 7 ? 0.f : ci;
      sel[e] = __ballot(w1 >= 8);
      if (w1 >= 8) pm[e >> 1] |= ch << (16 * (e & 1));
    }
    oa = (unsigned)((8 + hp * 4 + hd * 2 + (g >> 1)) * 1024 + ((blk & 1) * 32 + 2 * r + (g & 1)) * 16);
    pa = ((oa & 1023u) >> 2) + (unsigned)((hp * 2 + hd) * 256);
    ob = uni_ptr(o + (size_t)((r0 >> 6) + (wave >> 2)) * (72 * 1024));
    asm volatile("" : "+v"(ka), "+v"(kb), "+v"(va), "+v"(vb), "+v"(m0), "+v"(m1), "+v"(m2), "+v"(pm[0]), "+v"(pm[1]), "+v"(oa));
    asm volatile("" : "+v"(mk), "+v"(pa), "+s"(ob), "+s"(sel[0]), "+s"(sel[1]), "+s"(sel[2]), "+s"(sel[3]));
  }
  DEVI void run(int r0, int hp, int wave, int lane) {
    const int hd = wave & 1;
    u32x4 q[2], k0[2], k1[2];
    u32x2 v[2][2][2];
#pragma unroll
    for (int p = 0; p < 2; ++p) {
      asm volatile("ds_read_b128 %0, %1 offset:%2" : "=v"(q[p]) : "v"(ka), "n"((16 + p) * K1_TP) : "memory");
      asm volatile("ds_read_b128 %0, %1 offset:%2" : "=v"(k0[p]) : "v"(ka), "n"(p * K1_TP + 80) : "memory");
      asm volatile("ds_read_b128 %0, %1 offset:%2" : "=v"(k1[p]) : "v"(kb), "n"((32 + p) * K1_TP + 80) : "memory");
    }
    const unsigned vh = (unsigned)hd * 48u;
#pragma unroll
    for (int p = 0; p < 2; ++p) {
      asm volatile("ds_read_b64_tr_b16 %0, %1 offset:%2" : "=v"(v[p][0][0]) : "v"(va + vh), "n"(p * K1_TP + 160) : "memory");
      asm volatile("ds_read_b64_tr_b16 %0, %1 offset:%2" : "=v"(v[p][0][1]) : "v"(vb + vh), "n"((32 + p) * K1_TP + 160) : "memory");
      asm volatile("ds_read_b64_tr_b16 %0, %1 offset:%2" : "=v"(v[p][1][0]) : "v"(va), "n"(p * K1_TP + 192) : "memory");
      asm volatile("ds_read_b64_tr_b16 %0, %1 offset:%2" : "=v"(v[p][1][1]) : "v"(vb), "n"((32 + p) * K1_TP + 192) : "memory");
    }
    const u32x4 one4 = {vconst(0x3f803f80u), vconst(0x3f803f80u), vconst(0x3f803f80u), vconst(0x3f803f80u)};
    const s16x8 ones = __builtin_bit_cast(s16x8, one4);
    const float c = 0.23570226039551584f * 1.4426950408889634f;
    const f32x4 z = {0.f, 0.f, 0.f, 0.f};
    f32x4 sc[2][2];
#pragma unroll
    for (int p = 0; p < 2; ++p) {
      asm volatile("s_waitcnt lgkmcnt(%3)" : "+v"(q[p]), "+v"(k0[p]), "+v"(k1[p]) : "n"(11 - 3 * p));
      const s16x8 qf = __builtin_bit_cast(s16x8, q[p] & u32x4{m0, m1, m2, m2});
      sc[p][0] = __builtin_amdgcn_mfma_f32_16x16x32_bf16(__builtin_bit_cast(s16x8, k0[p]), qf, mk, 0, 0, 0);
      sc[p][1] = __builtin_amdgcn_mfma_f32_16x16x32_bf16(__builtin_bit_cast(s16x8, k1[p]), qf, mk, 0, 0, 0);
      __builtin_amdgcn_sched_barrier(0);
    }
    if (__builtin_expect(r0 == 0 && wave < 2, 0)) {
#pragma unroll
      for (int p = 0; p < 2; ++p)
#pragma unroll
        for (int e = 0; e < 4; ++e)
          if (lane < 32) sc[p][0][e] = mk[e] - 4.242640687e9f;
    }
    float lg[2][4], mx[2];
#pragma unroll
    for (int p = 0; p < 2; ++p) {
#pragma unroll
      for (int e = 0; e < 4; ++e) lg[p][e] = __builtin_amdgcn_inverse_ballot_w64(sel[e]) ? sc[p][1][e] : sc[p][0][e];
      asm("v_max3_f32 %0, %1, %2, %3\n\tv_max_f32 %0, %0, %4" : "=&v"(mx[p]) : "v"(lg[p][0]), "v"(lg[p][1]), "v"(lg[p][2]), "v"(lg[p][3]));
    }
    {
      const auto a = __builtin_amdgcn_permlane32_swap(__float_as_uint(mx[0]), __float_as_uint(mx[1]), false, false);
      const unsigned b = __float_as_uint(vmax(__uint_as_float(a[0]), __uint_as_float(a[1])));
      const auto d = __builtin_amdgcn_permlane16_swap(b, b, false, false);
      const unsigned f = __float_as_uint(vmax(__uint_as_float(d[0]), __uint_as_float(d[1])) * -c);
      const auto h = __builtin_amdgcn_permlane32_swap(f, f, false, false);
      mx[0] = __uint_as_float(h[0]);
      mx[1] = __uint_as_float(h[1]);
    }
#pragma unroll
    for (int p = 0; p < 2; ++p)
#pragma unroll
      for (int e = 0; e < 4; ++e) lg[p][e] = __builtin_amdgcn_exp2f(__builtin_fmaf(lg[p][e], c, mx[p]));
    asm volatile("s_waitcnt lgkmcnt(0)"
                 : "+v"(v[0][0][0]), "+v"(v[0][0][1]), "+v"(v[0][1][0]), "+v"(v[0][1][1]), "+v"(v[1][0][0]), "+v"(v[1][0][1]),
                   "+v"(v[1][1][0]), "+v"(v[1][1][1]));
    __builtin_amdgcn_sched_barrier(0);
    f32x4 su[2], o0[2], o1[2];
    s16x8 pp[2];
#pragma unroll
    for (int p = 0; p < 2; ++p) {
      const unsigned p0 = pack2(lg[p][0], lg[p][1]), p1 = pack2(lg[p][2], lg[p][3]);
      pp[p] = __builtin_bit_cast(s16x8, u32x4{p0 & ~pm[0], p1 & ~pm[1], p0 & pm[0], p1 & pm[1]});
    }
    __builtin_amdgcn_sched_barrier(0);
#pragma unroll
    for (int p = 0; p < 2; ++p) {
      const s16x8 pf = pp[p];
      const u32x4 v0 = {v[p][0][0][0], v[p][0][0][1], v[p][0][1][0], v[p][0][1][1]};
      const u32x4 v1 = {v[p][1][0][0], v[p][1][0][1], v[p][1][1][0], v[p][1][1][1]};
      su[p] = __builtin_amdgcn_mfma_f32_16x16x32_bf16(ones, pf, z, 0, 0, 0);
      o0[p] = __builtin_amdgcn_mfma_f32_16x16x32_bf16(__builtin_bit_cast(s16x8, v0), pf, z, 0, 0, 0);
      o1[p] = __builtin_amdgcn_mfma_f32_16x16x32_bf16(__builtin_bit_cast(s16x8, v1), pf, z, 0, 0, 0);
    }
    __builtin_amdgcn_sched_barrier(0);
    u32x2 y[2], yl;
    unsigned hm = (unsigned)hd - 1u;
    asm volatile("" : "+s"(hm));
#pragma unroll
    for (int p = 0; p < 2; ++p) {
      const float inv = __builtin_amdgcn_rcpf(su[p][0]);
      y[p] = u32x2{pack2(o0[p][0] * inv, o0[p][1] * inv), pack2(o0[p][2] * inv, o0[p][3] * inv)};
      const unsigned la = pack2(o1[p][0] * inv, o1[p][1] * inv), lb = pack2(o1[p][2] * inv, o1[p][3] * inv);
      yl[p] = (la & hm) | (lb & ~hm);
    }
    const auto s0 = __builtin_amdgcn_permlane16_swap(y[0][0], y[1][0], false, false);
    const auto s1 = __builtin_amdgcn_permlane16_swap(y[0][1], y[1][1], false, false);
    const u32x4 w = {s0[0], s1[0], s0[1], s1[1]};
    asm volatile("global_store_dwordx4 %0, %1, %4 sc1\n\ts_mov_b64 exec, 0xffff\n\tglobal_store_dwordx2 %2, %3, %4 sc1\n\ts_mov_b64 exec, -1\n\ts_nop 0" ::"v"(oa),
                 "v"(w), "v"(pa), "v"(yl), "s"(ob)
                 : "memory");
  }
};

#define NL_MAIN 661
DEVI void math_start(unsigned lds0, char* ws, int bid, int wave, int lane) {
  static_assert(2 * SZ_ZP == (size_t)256 * 2048 && NCNT <= 16 && OFF_CNT + NCNT * 32768 < 0xffffffffu, "mark split");
  const unsigned m1 = vconst(~0u), l = (unsigned)lane;
  const unsigned hi = wave < 2 ? 128u : 32768u;
  unsigned b0 = (unsigned)OFF_ZP + (unsigned)(bid * 2 + wave) * 1024u, b1 = (unsigned)OFF_CNT + (unsigned)((wave & 1) * 2048 + bid) * 128u;
  asm volatile("" : "+s"(b0), "+s"(b1));
  const unsigned base = wave < 2 ? b0 : b1;
  const unsigned long long act = wave < 3 ? ~0ull : (wave == 3 ? (1ull << ((NCNT - 8) * 8)) - 1ull : 0ull);
  asm volatile("s_mov_b64 exec, %3\n\tglobal_store_dwordx4 %0, %1, %2 sc1\n\ts_mov_b64 exec, -1\n\ts_nop 0" ::"v"(base + (l >> 3) * hi + (l & 7u) * 16u),
               "v"(u32x4{m1, m1, m1, m1}), "s"(ws), "s"(act)
               : "memory");
  unsigned a4 = (l < 32u ? (l >> 3) * (unsigned)K1R::SB : (unsigned)LW_OFF) + (l & 7u) * 16u, a0 = (unsigned)(wave * K1R::SB + K1R::XB + 240) + l * 288u;
  asm volatile("" : "+v"(a4), "+v"(a0));
  const unsigned a = wave < 4 ? a0 : a4;
  const unsigned v = (wave < 4 || l < 32u) ? 0x3f803f80u : 0u;
  asm volatile("s_mov_b64 exec, %2\n\tds_write_b128 %0, %1\n\ts_mov_b64 exec, -1\n\ts_waitcnt lgkmcnt(0)" ::"v"(lds0 + a), "v"(u32x4{v, v, v, v}),
               "s"(wave < 4 ? ~0ull : (wave == 4 ? 0xffffffffffull : 0ull))
               : "memory");
}

template <int NST, int NS, int NSW, int WOFF, int XW, int X0, int S0, unsigned BAR>
struct ResX {
  static constexpr int WS = 4 * 4608, R = 10, V1 = 5 * NS, NV = V1 + 10 * (NST - NS), NG = (NSW + 2) / 3;
  s16x8 xf[XW][2];
  s16x8 wf[R];
  unsigned wrd[2][NG], xo, ea, fa, fv, tag;
  const char* xp[3];
  f32x4 (&acc)[2][5];
  DEVI ResX(f32x4 (&a)[2][5]) : acc(a) {}
  static constexpr int st(int V) { return V < V1 ? V / 5 : NS + (V - V1) / 10; }
  static constexpr int hf(int V) { return V < V1 ? 0 : ((V - V1) / 5) & 1; }
  static constexpr bool first(int V) { return V < V1 ? V % 5 == 0 : (V - V1) % 10 == 0; }
  static constexpr int wreq(int V) {
    int n = V + 5;
    if (V >= V1 - 4) n += V < V1 ? V - (V1 - 5) : 4;
    return n < NV ? n : NV;
  }
  static constexpr int XU = V1 + 1, XF = V1 + 11, XR = V1 + 15, XC = V1 + 21;
  static constexpr int oth(int V) { return NS && ((V >= XU && V <= XF && (V - XU) % 2 == 0) || V == XR || (V > XC && V <= XC + 5)) ? 1 : 0; }
  static constexpr int newer(int V) {
    const int F = V + 1 < NV ? V + 1 : V;
    int v = V - 1, n = 0;
    for (; v >= 0 && wreq(v - 1) > F; --v) n += wreq(v) - wreq(v - 1) + oth(v);
    return n + wreq(v) - 1 - F + (v >= 0 ? oth(v) : 0);
  }
  static constexpr int xreq(int T) {
    int n = X0 + 2 * (T + 1);
    if (n > T + XW) n = T + XW;
    if (NS && T <= 11 && n > 18) n = 18;
    if (T < 0) n = X0;
    return n < NST ? n : NST;
  }
  static_assert(NS == 0 || (xreq(NS - 2) == NST && XC + 5 < NV), "the last steps only compute and swap");
  template <int T, int M>
  DEVI void xld() {
    asm volatile("global_load_dwordx4 %0, %1, %2 sc1" : "=v"(xf[T % XW][M]) : "v"(xo), "s"(xp[M == 0 ? 0 : (T < NS ? 1 : 2)]));
    if constexpr (M == 1 && T + 1 < NST) asm volatile("v_add_u32 %0, 0x2000, %0" : "+v"(xo));
  }
  template <int V>
  DEVI void rd() {
    constexpr int T = st(V), sl = (T / 2 + S0) % NSW;
    asm volatile("ds_read_b128 %0, %1 offset:%2" : "=v"(wf[V % R]) : "v"(wrd[hf(V)][sl / 3]), "n"((sl % 3) * WS + (T & 1) * 9216 + (V % 5) * 32));
  }
  template <int V, int M, int H>
  DEVI void mf() {
    acc[M][V % 5] = __builtin_amdgcn_mfma_f32_16x16x32_bf16(wf[V % R], xf[st(V) % XW][H], acc[M][V % 5], 0, 0, 0);
    __builtin_amdgcn_sched_barrier(0);
  }
  template <int V>
  DEVI void unit() {
    constexpr int T = st(V), n = V % 5, nx = xreq(T) - xreq(T - 1), nw = wreq(V) - wreq(V - 1);
    constexpr int vm = 2 * (xreq(T - 1) - 1 - T);
    static_assert(newer(V) <= 15, "lgkmcnt range");
    if constexpr (first(V) && V % 2 == 0) asm volatile("s_waitcnt vmcnt(%0) lgkmcnt(%1)" ::"n"(vm), "n"(newer(V)));
    else if constexpr (first(V)) asm volatile("s_waitcnt vmcnt(%0)" ::"n"(vm));
    else if constexpr (V % 2 == 0) asm volatile("s_waitcnt lgkmcnt(%0)" ::"n"(newer(V)));
    __builtin_amdgcn_sched_barrier(0);
    if constexpr (V < V1) mf<V, V & 1, V & 1>();
    else mf<V, 0, hf(V)>();
    if constexpr (nw > 0) rd<wreq(V - 1)>();
    __builtin_amdgcn_sched_barrier(0);
    if constexpr (V < V1) {
      mf<V, 1 - (V & 1), 1 - (V & 1)>();
      if constexpr (nw > 1) rd<wreq(V - 1) + 1>();
      if constexpr (nx == 2 && n < 4) xld<xreq(T - 1) + n / 2, n % 2>();
      if constexpr (nx == 1 && n == (T & 1)) xld<xreq(T - 1), 0>();
      if constexpr (nx == 1 && n == (T & 1) + 2) xld<xreq(T - 1), 1>();
      if constexpr (n == 4 && (T & 1) && ((BAR >> (T / 2)) & 1u)) bar();
    } else if constexpr (NS == 0) {
      constexpr int k = (V % 10) / 2;
      if constexpr (V % 2 == 0 && k < 2 * nx) xld<xreq(T - 1) + k / 2, k % 2>();
      if constexpr (V % 10 == 8 && nx > 0 && xreq(T) == NST)
        asm volatile("s_mov_b64 exec, 1\n\tds_add_u32 %0, %1\n\ts_mov_b64 exec, -1" ::"v"(fa), "v"(fv));
    } else {
      if constexpr (oth(V) && V < XF) asm volatile("ds_write_b128 %0, %1 offset:%2" ::"v"(ea), "v"(acc[1][(V - XU) / 2]), "n"((V - XU) * 512));
      if constexpr (V == XF) asm volatile("ds_write_b32 %0, %1" ::"v"(fa), "v"(fv));
      if constexpr (V == XF + 2) asm volatile("v_xor_b32 %0, 4, %0" : "+v"(fa));
      if constexpr (V == XR) asm volatile("ds_read_b32 %0, %1" : "+v"(fv) : "v"(fa));
      if constexpr (V == XR + 2) asm volatile("v_xor_b32 %0, 0x2000, %0" : "+v"(ea));
      if constexpr (V == XC) {
        unsigned c = 1u << 18;
        asm volatile(
            "s_waitcnt lgkmcnt(%4)\n\tv_cmp_ne_u32 vcc, %3, %0\n\ts_cbranch_vccnz .Lw%=\n\t.subsection 1\n.Lw%=:\n\tds_read_b32 %0, %2\n\t"
            "s_waitcnt lgkmcnt(0)\n\tv_cmp_ne_u32 vcc, %3, %0\n\t" SAFE_OUT(".Lr%=") "s_cbranch_vccnz .Lw%=\n\ts_branch .Lr%=\n\t.subsection 0\n.Lr%=:"
            : "+v"(fv), NOP_(c)
            : "v"(fa), "s"(tag), "n"(wreq(XC) - wreq(XR))
            : "vcc", "scc");
      }
      if constexpr (oth(V) && V > XC) asm volatile("ds_read_b128 %0, %1 offset:%2" : "=v"(acc[1][V - XC - 1]) : "v"(ea), "n"((V - XC - 1) * 1024));
    }
    __builtin_amdgcn_sched_barrier(0);
  }
  template <int... V>
  DEVI void units(std::integer_sequence<int, V...>) {
    (unit<V>(), ...);
  }
  DEVI void run() {
    units(std::make_integer_sequence<int, NV>{});
    if constexpr (NS > 0) {
      asm volatile("s_waitcnt lgkmcnt(0)" : "+v"(acc[1][0]), "+v"(acc[1][1]), "+v"(acc[1][2]), "+v"(acc[1][3]), "+v"(acc[1][4]));
#pragma unroll
      for (int i = 0; i < 5; ++i) acc[0][i] += acc[1][i];
    }
  }
  template <int... V>
  DEVI void pro(std::integer_sequence<int, V...>) {
    (rd<V>(), ...);
  }
  DEVI void prow() { pro(std::make_integer_sequence<int, wreq(-1)>{}); }
  template <int T0, int... T>
  DEVI void prox(std::integer_sequence<int, T...>) {
    ((xld<T0 + T, 0>(), xld<T0 + T, 1>()), ...);
  }
  DEVI void setup(unsigned lds0, const char* xbase, int wave, int lane, unsigned red, unsigned nth) {
    const int g = lane >> 4, r = lane & 15, kq = wave & 1;
    xp[0] = xbase + wave * 256;
    xp[1] = xbase + (wave ^ 1) * 256;
    xp[2] = xp[0] + (kq ? -4096 : 4096);
    xo = (unsigned)((4 * kq + g) * 1024 + r * 16);
#pragma unroll
    for (int i = 0; i < 2 * NG; ++i) {
      wrd[i / NG][i % NG] = lds0 + (unsigned)(WOFF + (i % NG) * 3 * WS + (kq ^ (i / NG)) * 4608 + (2 * (r & 7) + 16 * (g & 1) + (g >> 1)) * 144 + (r >> 3) * 16);
      if (i < NG || NS == 0 || i % NG == ((NS / 2 + S0) % NSW) / 3) asm volatile("" : "+v"(wrd[i / NG][i % NG]));
    }
    asm volatile("" : "+v"(xo));
    if constexpr (NS > 0) {
      ea = lds0 + red + (unsigned)((wave ^ 1) * 8192 + lane * 16);
      fa = lds0 + (unsigned)(LW_OFF + (LW_PAIR + wave) * 4);
      fv = tag = nth;
      asm volatile("" : "+v"(ea), "+v"(fa), "+v"(fv));
    }
  }
  template <int N>
  DEVI void init(u32x2 (&xv)[5]) {
    asm volatile("s_waitcnt vmcnt(%5)" : "+v"(xv[0]), "+v"(xv[1]), "+v"(xv[2]), "+v"(xv[3]), "+v"(xv[4]) : "n"(N));
#pragma unroll
    for (int i = 0; i < 5; ++i) {
      acc[0][i] = f32x4{bf_lo(xv[i][0]), bf_hi(xv[i][0]), bf_lo(xv[i][1]), bf_hi(xv[i][1])};
      asm volatile("" : "+v"(acc[0][i]));
      acc[1][i] = f32x4{0.f, 0.f, 0.f, 0.f};
    }
  }
};

constexpr int P5_XW = 8, P5_X0 = 5, P2_X0 = 4;
constexpr unsigned P5_BAR = 0x2aaau;
constexpr int P5_W = 15 * 1024;
constexpr int P2_W = K1_ST_OFF - 18 * 4608;
constexpr int P5_RED = 0;
static_assert(P5_RED % 16384 == 0 && P5_RED + 3 * 8192 + 5120 <= P5_W + 18432, "exchange area");
static_assert(P2_W >= K1_T_OFF + 144 * K1_TP + 64 && P2_W % 16 == 0 && P5_W + 8 * 18432 <= LW_OFF, "LDS placement");

struct ResIn {
  const char* xp;
  unsigned o, o2, st;
  DEVI void setup(const char* src, bool rm, int rb, int ct, int wave, int lane) {
    const int g = lane >> 4, g2 = g & 1, rl = wave * 16 + (lane & 15);
    xp = uni_ptr(rm ? src + ((size_t)rb * 64 * D_ + ct * 72) * 2 : src + (size_t)(rb * 72 + ct * 9) * 1024);
    o = rm ? (unsigned)(rl * (D_ * 2) + g * 8) : (unsigned)((g >> 1) * 1024 + rl * 16 + g2 * 8);
    o2 = rm ? (unsigned)(rl * (D_ * 2) + g2 * 8 + 128) : (unsigned)(8192 + rl * 16 + g2 * 8);
    st = rm ? 32u : 2048u;
    asm volatile("" : "+v"(o), "+v"(o2));
    asm volatile("" : "+s"(xp));
  }
  DEVI void load(u32x2 (&xv)[5]) {
#pragma unroll
    for (int i = 0; i < 4; ++i) asm volatile("global_load_dwordx2 %0, %1, %2 sc1" : "=v"(xv[i]) : "v"(o), "s"(xp + i * st));
    asm volatile("global_load_dwordx2 %0, %1, %2 sc1" : "=v"(xv[4]) : "v"(o2), "s"(xp));
  }
};

DEVI void p2_gemm(f32x4 (&acc)[2][5], u32x2 (&xv)[5], unsigned lds0, const char* ob, int layer, int wave, int lane MK_ARG) {
  ResX<9, 0, 5, P2_W, 9, P2_X0, 0, 0u> gm(acc);
  gm.setup(lds0, ob, wave, lane, 0u, 0u);
  unsigned a = (gm.xo & ~1023u) | ((gm.xo & 1023u) >> 2);
  gm.xo += 8192u;
  gm.fa = lds0 + (unsigned)(LW_OFF + (LW_T + layer) * 4);
  gm.fv = vconst(1u);
  asm volatile("" : "+v"(a), "+v"(gm.xo), "+v"(gm.fa));
  const char* pp[2] = {gm.xp[0] - wave * 192, gm.xp[2] - wave * 192};
  place();
  bar();
  MK(6);
  unsigned e[2][4];
#pragma unroll
  for (int d = 0; d < 8; ++d)
    asm volatile("global_load_dword %0, %1, %2 offset:%3 sc1" : "=v"(e[d / 4][d % 4]) : "v"(a), "s"(pp[d / 4]), "n"((d % 4) * 256));
  gm.template prox<1>(std::make_integer_sequence<int, P2_X0 - 1>{});
  gm.prow();
  gm.template init<6 + 2 * P2_X0>(xv);
  asm volatile("s_waitcnt vmcnt(%8)"
               : "+v"(e[0][0]), "+v"(e[0][1]), "+v"(e[0][2]), "+v"(e[0][3]), "+v"(e[1][0]), "+v"(e[1][1]), "+v"(e[1][2]), "+v"(e[1][3])
               : "n"(2 * P2_X0 - 2));
  gm.xf[0][0] = __builtin_bit_cast(s16x8, u32x4{e[0][0], e[0][1], e[0][2], e[0][3]});
  gm.xf[0][1] = __builtin_bit_cast(s16x8, u32x4{e[1][0], e[1][1], e[1][2], e[1][3]});
  __builtin_amdgcn_sched_barrier(0);
  gm.run();
}

struct ResOut {
  const char *xp[3], *sp;
  unsigned xo, so;
  DEVI void setup(char* xim, char* xs, u16* out, bool fin, int rb, int ct, int wave, int lane) {
    const int g = lane >> 4, rl = wave * 16 + (lane & 15);
    xp[0] = uni_ptr(fin ? (const char*)(out + (size_t)rb * 64 * D_ + ct * 72) : xim + (size_t)(rb * 72 + ct * 9) * 1024);
    xp[1] = xp[0] + (fin ? 64 : 4096);
    xp[2] = xp[0] + (fin ? 128 : 8192);
    sp = uni_ptr(xs + (size_t)(rb * 8 + ct) * 512);
    xo = (unsigned)(((g & 1) * 2 + (g >> 1)) * (fin ? 16 : 1024) + rl * (fin ? D_ * 2 : 16));
    so = (unsigned)(rl * 8);
    asm volatile("" : "+v"(xo), "+v"(so), "+s"(xp[0]), "+s"(xp[1]), "+s"(xp[2]), "+s"(sp));
  }
  DEVI void run(const f32x4 (&a)[5], bool fin) {
    u32x2 y[5];
    u32x4 v[3];
#pragma unroll
    for (int nt = 0; nt < 5; ++nt) y[nt] = u32x2{pack2(a[nt][0], a[nt][1]), pack2(a[nt][2], a[nt][3])};
#pragma unroll
    for (int i = 0; i < 3; ++i) {
      const auto s0 = __builtin_amdgcn_permlane16_swap(y[2 * i][0], y[i < 2 ? 2 * i + 1 : 4][0], false, false);
      const auto s1 = __builtin_amdgcn_permlane16_swap(y[2 * i][1], y[i < 2 ? 2 * i + 1 : 4][1], false, false);
      v[i] = u32x4{s0[0], s1[0], s0[1], s1[1]};
    }
    if (fin) {
      asm volatile("global_store_dwordx4 %0, %1, %4 nt\n\tglobal_store_dwordx4 %0, %2, %5 nt\n\ts_mov_b64 exec, 0xffff\n\tglobal_store_dwordx4 %0, %3, %6 nt\n\t"
                   "s_mov_b64 exec, -1\n\ts_nop 0" ::"v"(xo), "v"(v[0]), "v"(v[1]), "v"(v[2]), "s"(xp[0]), "s"(xp[1]), "s"(xp[2])
                   : "memory");
      return;
    }
    asm volatile("global_store_dwordx4 %0, %1, %4 sc1\n\tglobal_store_dwordx4 %0, %2, %5 sc1\n\ts_mov_b64 exec, 0xffff\n\t"
                 "global_store_dwordx4 %0, %3, %6 sc1\n\ts_mov_b64 exec, -1\n\ts_nop 0" ::"v"(xo), "v"(v[0]), "v"(v[1]), "v"(v[2]),
                 "s"(xp[0]), "s"(xp[1]), "s"(xp[2])
                 : "memory");
    const u32x4 one4 = {vconst(0x3f803f80u), vconst(0x3f803f80u), vconst(0x3f803f80u), vconst(0x3f803f80u)};
    const bool lo = __builtin_amdgcn_inverse_ballot_w64(0xffffull);
    v[2] = u32x4{lo ? v[2][0] : 0u, lo ? v[2][1] : 0u, lo ? v[2][2] : 0u, lo ? v[2][3] : 0u};
    f32x4 s = {0.f, 0.f, 0.f, 0.f}, q = s;
#pragma unroll
    for (int i = 0; i < 3; ++i) {
      const s16x8 u = __builtin_bit_cast(s16x8, v[i]);
      q = __builtin_amdgcn_mfma_f32_16x16x32_bf16(u, u, q, 0, 0, 0);
      s = __builtin_amdgcn_mfma_f32_16x16x32_bf16(__builtin_bit_cast(s16x8, one4), u, s, 0, 0, 0);
    }
    float q0 = q[0], q1 = q[1], q2 = q[2], q3 = q[3];
    const bool b0 = __builtin_amdgcn_inverse_ballot_w64(0xaaaaaaaaaaaaaaaaull), b1 = __builtin_amdgcn_inverse_ballot_w64(0xccccccccccccccccull);
    q0 = b0 ? q1 : q0;
    q2 = b0 ? q3 : q2;
    asm("" : "+v"(q0), "+v"(q2));
    const f32x2 sq = {s[0], b1 ? q2 : q0};
    asm volatile("s_mov_b64 exec, %3\n\tglobal_store_dwordx2 %0, %1, %2 sc1\n\ts_mov_b64 exec, -1" ::"v"(so), "v"(sq), "s"(sp), "s"(0xf0000f0000f0000full) : "memory");
  }
};

using K3C = GemmCfg<128, 144>;
constexpr int K3_Z1_OFF = K3C::LDS_BYTES;
constexpr int K3_C2_OFF = K3_Z1_OFF + K3C::XB;
constexpr int K3_ZC_OFF = K3_C2_OFF + 2048;
constexpr int K3_DM_OFF = K3_ZC_OFF + 1024;
constexpr int K3_ST_OFF = K3_Z1_OFF + 4 * 5120;
constexpr int K3_C1_OFF = K3_ST_OFF + 2048;
constexpr int K3_BS_OFF = K3_C1_OFF + 576;
static_assert(K3_BS_OFF + 512 <= LW_OFF, "K3 LDS");
static_assert(P5_W + 19 * 1024 == K3C::SB && K3C::SB == 34 * 1024, "w_out ring");

DEVI void red4(float (&t)[4], float& s, float& q) {
#pragma unroll
  for (int j = 0; j < 2; ++j) {
    const auto p = __builtin_amdgcn_permlane16_swap(__float_as_uint(t[j]), __float_as_uint(t[2 + j]), false, false);
    const unsigned v = __float_as_uint(__uint_as_float(p[0]) + __uint_as_float(p[1]));
    const auto w = __builtin_amdgcn_permlane32_swap(v, v, false, false);
    t[j] = __uint_as_float(w[0]) + __uint_as_float(w[1]);
    asm volatile("" : "+v"(t[j]));
  }
  s = t[0];
  q = t[1];
}
DEVI void red8(const f32x4& a, const f32x4& b, float& s, float& q) {
  float t[4];
#pragma unroll
  for (int j = 0; j < 4; ++j) {
    t[j] = a[j] + b[j];
    asm volatile("" : "+v"(t[j]));
  }
  red4(t, s, q);
}
DEVI void red16(const f32x4& a, const f32x4& b, const f32x4& c, const f32x4& d, float& s, float& q) {
  float t[4];
#pragma unroll
  for (int j = 0; j < 4; ++j) {
    float u = a[j] + b[j], v = c[j] + d[j];
    asm volatile("" : "+v"(u), "+v"(v));
    t[j] = u + v;
    asm volatile("" : "+v"(t[j]));
  }
  red4(t, s, q);
}

constexpr int b_k(int s) { return s == 0 ? 0 : (s < 3 ? 6 + s : (s == 3 ? 1 : (s < 6 ? 1 + s : (s == 6 ? 4 : s - 5)))); }
constexpr int b_xp(int s) { return s == 0 ? 0 : (b_k(s) >= 4 ? (b_k(s) % 5) / 2 : b_xp(s - 1)); }
template <bool B>
struct UMath {
  static constexpr int D = 4, R = 6, NV = 162, SB = K3C::SB, P = K3C::P;
  s16x8 wf[R], xf[2][2];
  s16x8 (&xc)[4][2][2];
  f32x4 (&acc)[2][9];
  f32x4 rc[4][2];
  f32x2 cs[9];
  unsigned ra, ca;
  template <int I>
  DEVI void cread() {
    if constexpr (I < 8) asm volatile("ds_read_b128 %0, %1 offset:%2" : "=v"(rc[I / 2][I % 2]) : "v"(ra), "n"((I / 2) * 512 + (I % 2) * 16));
    else if constexpr (I < 17) asm volatile("ds_read_b64 %0, %1 offset:%2" : "=v"(cs[I - 8]) : "v"(ca), "n"((I - 8) * 128));
  }
  unsigned xa, wa;
  unsigned fa, fw, fv;
  DEVI UMath(f32x4 (&a)[2][9], s16x8 (&x)[4][2][2]) : xc(x), acc(a) {}
  static constexpr int wslot(int s) { return B ? (s + 1) % 4 : s % 4; }
  static constexpr int xslot(int s) { return B ? b_k(s) % 5 : s % 5; }
  static constexpr int xpair(int s) { return B ? b_xp(s) : xslot(s) / 2; }
  static constexpr bool res(int q) { return !B || b_k(q / 2) >= 4; }
  static constexpr bool chk(int s) { return s >= 3 && s < 7; }
  static constexpr bool xop(int v) { return v + D < NV && (v + D) % 9 < 2 && res((v + D) / 9); }
  static constexpr int ops1(int v) { return (xop(v) || (B && ((v % 18 == 9 && chk(v / 18)) || (v % 18 == 0 && v > 0)))) ? 1 : 0; }
  static constexpr int newer(int V) {
    int v0 = (V % 9 == 8 ? V : V + 1) - D, n = 0;
    if (v0 >= 0 && !(V % 9 == 0 && res(V / 9))) n += ops1(v0);
    for (int v = v0 + 1; v < V; ++v)
      if (v >= 0) n += (v + D < NV ? 1 : (B ? 0 : 4)) + ops1(v);
    return n;
  }
  template <int V>
  DEVI void wread() {
    asm volatile("ds_read_b128 %0, %1 offset:%2" : "=v"(wf[V % R]) : "v"(wa), "n"((wslot(V / 18) & 1) * SB + ((V % 18) / 9) * 32 * P + (V % 9) * 32));
  }
  template <int Q, int M>
  DEVI void xread() {
    asm volatile("ds_read_b128 %0, %1 offset:%2" : "=v"(xf[Q & 1][M]) : "v"(xa), "n"((xslot(Q / 2) & 1) * SB + (Q & 1) * 8192 + M * (B ? 256 : 64)));
  }
  template <int V, int M_>
  DEVI void mf() {
    constexpr int M = ((V % 9) & 1) ? 1 - M_ : M_, q = V / 9;
    if constexpr (!B) acc[M][V % 9] = __builtin_amdgcn_mfma_f32_16x16x32_bf16(xf[q & 1][M], wf[V % R], acc[M][V % 9], 0, 0, 0);
    else if constexpr (res(q)) acc[M][V % 9] = __builtin_amdgcn_mfma_f32_16x16x32_bf16(wf[V % R], xf[q & 1][M], acc[M][V % 9], 0, 0, 0);
    else acc[M][V % 9] = __builtin_amdgcn_mfma_f32_16x16x32_bf16(wf[V % R], xc[b_k(q / 2)][q & 1][M], acc[M][V % 9], 0, 0, 0);
    __builtin_amdgcn_sched_barrier(0);
  }
  template <int V>
  DEVI void unit() {
    constexpr int U = V % 18, s = V / 18;
    static_assert(newer(V) <= 15, "lgkmcnt range");
    if constexpr ((V % 9) % 2 == 0 && V > 0) asm volatile("s_waitcnt lgkmcnt(%0)" ::"n"(newer(V)));
    __builtin_amdgcn_sched_barrier(0);
    mf<V, 0>();
    if constexpr (V + D < NV) wread<V + D>();
    if constexpr (!B && V + D >= NV) {
      cread<(V + D - NV) * 4>();
      cread<(V + D - NV) * 4 + 1>();
    }
    if constexpr (s < 8 && U == 13 && (wslot(s + 1) >> 1) != (wslot(s) >> 1))
      asm volatile("v_add_u32 %0, %1, %0" : "+v"(wa) : "n"(((wslot(s + 1) >> 1) - (wslot(s) >> 1)) * 2 * SB));
    __builtin_amdgcn_sched_barrier(0);
    mf<V, 1>();
    if constexpr (xop(V)) xread<(V + D) / 9, (V + D) % 9>();
    if constexpr (!B && V + D >= NV) {
      cread<(V + D - NV) * 4 + 2>();
      cread<(V + D - NV) * 4 + 3>();
      if constexpr (V == NV - 1) cread<16>();
    }
    if constexpr (s < 8) {
      if constexpr (U == 11 && xpair(s + 1) != xpair(s)) asm volatile("v_add_u32 %0, %1, %0" : "+v"(xa) : "n"((xpair(s + 1) - xpair(s)) * 2 * SB));
      if constexpr (!B && U == 13) __builtin_amdgcn_s_barrier();
      if constexpr (B && U == 9 && chk(s)) asm volatile("ds_read_u8 %0, %1 offset:%2" : "+v"(fv) : "v"(fa), "n"(s == 6 ? 0 : 8));
      unsigned n = 1u << 18;
      if constexpr (B && U == 13 && chk(s)) {
        asm volatile(
            "v_cmp_gt_u32 vcc, %3, %0\n\ts_cbranch_vccnz .Lw%=\n\t.subsection 1\n.Lw%=:\n\tds_read_u8 %0, %2 offset:%4\n\ts_waitcnt lgkmcnt(0)\n\t"
            "v_cmp_gt_u32 vcc, %3, %0\n\t" SAFE_OUT(".Lr%=") "s_cbranch_vccnz .Lw%=\n\ts_branch .Lr%=\n\t.subsection 0\n.Lr%=:"
            : "+v"(fv), NOP_(n)
            : "v"(fa), "n"(s == 6 ? 4 : s + 2), "n"(s == 6 ? 0 : 8)
            : "vcc", "scc");
      }
    }
    if constexpr (B && U == 15) asm volatile("v_mov_b32 %0, %1" : "+v"(fv) : "n"(s + 1));
    if constexpr (B && ((U == 0 && s > 0) || V == NV - 1)) asm volatile("ds_write_b8 %0, %1" ::"v"(fw), "v"(fv));
    __builtin_amdgcn_sched_barrier(0);
  }
  template <int... V>
  DEVI void units(std::integer_sequence<int, V...>) {
    (unit<V>(), ...);
  }
  DEVI void first() {
    if constexpr (!B) {
      xread<0, 0>();
      xread<0, 1>();
    }
    wread<0>();
    wread<1>();
    wread<2>();
    wread<3>();
    asm volatile("s_waitcnt lgkmcnt(0)");
  }
  DEVI void run() {
    units(std::make_integer_sequence<int, NV>{});
#pragma unroll
    for (int m = 0; m < 2; ++m)
#pragma unroll
      for (int n = 0; n < 9; ++n) asm volatile("" : "+v"(acc[m][n]));
  }
};

struct CMath {
  static constexpr int D = 4, R = 6, SB = K3C::SB;
  s16x8 wf[R], xf[2][2];
  f32x4 (&acc)[2][9];
  unsigned wa[2], xa[2][2];
  DEVI CMath(f32x4 (&a)[2][9]) : acc(a) {}
  static constexpr int xop(int u, bool last) { return (u + D) % 9 < 2 && !(last && u + D >= 18) ? 1 : 0; }
  static constexpr int newer(int U, bool last) {
    int v0 = (U % 9 == 8 ? U : U + 1) - D, n = 0;
    if (U % 9 != 0) n += xop((v0 + 18) % 18, last && v0 >= 0);
    for (int v = v0 + 1; v < U; ++v) n += (last && v + D >= 18 ? 0 : 1) + xop((v + 18) % 18, last && v >= 0);
    return n;
  }
  template <int SL, int U>
  DEVI void wread() {
    asm volatile("ds_read_b128 %0, %1 offset:%2" : "=v"(wf[U % R]) : "v"(wa[SL / 2]), "n"((SL & 1) * SB + U * 1024));
  }
  template <int SL, int H, int M>
  DEVI void xread() {
    asm volatile("ds_read_b128 %0, %1 offset:%2" : "=v"(xf[H][M]) : "v"(xa[SL / 2][H]), "n"((SL & 1) * SB + M * 2048));
  }
  template <int U, int M_>
  DEVI void mf() {
    constexpr int M = ((U % 9) & 1) ? 1 - M_ : M_;
    acc[M][U % 9] = __builtin_amdgcn_mfma_f32_16x16x32_bf16(wf[U % R], xf[U / 9][M], acc[M][U % 9], 0, 0, 0);
    __builtin_amdgcn_sched_barrier(0);
  }
  template <int SL, bool BAR, bool LAST, int U>
  DEVI void unit() {
    constexpr int PU = (U + D) % 18, PS = U + D < 18 ? SL : (SL + 1) % 4;
    if constexpr ((U % 9) % 2 == 0) asm volatile("s_waitcnt lgkmcnt(%0)" ::"n"(newer(U, LAST)));
    __builtin_amdgcn_sched_barrier(0);
    mf<U, 0>();
    if constexpr (!(LAST && U + D >= 18)) wread<PS, PU>();
    __builtin_amdgcn_sched_barrier(0);
    mf<U, 1>();
    if constexpr (xop(U, LAST)) xread<PS, PU / 9, PU % 9>();
    if constexpr (BAR && U == 13) __builtin_amdgcn_s_barrier();
    __builtin_amdgcn_sched_barrier(0);
  }
  template <int SL, bool BAR, bool LAST, int... U>
  DEVI void units(std::integer_sequence<int, U...>) {
    (unit<SL, BAR, LAST, U>(), ...);
  }
  template <int SL, bool BAR = true, bool LAST = false>
  DEVI void stage() {
    units<SL, BAR, LAST>(std::make_integer_sequence<int, 18>{});
  }
  DEVI void setup(unsigned lds0, int wm, int lane) {
    const int g = lane >> 4, r = lane & 15;
#pragma unroll
    for (int i = 0; i < 2; ++i) {
      wa[i] = lds0 + (unsigned)(i * 2 * SB + K3C::XB + lane * 16);
#pragma unroll
      for (int h = 0; h < 2; ++h) xa[i][h] = lds0 + (unsigned)(i * 2 * SB + (wm * 32 + r) * 128 + (((4 * h + g) ^ ((r >> 1) & 7)) * 16));
    }
    asm volatile("" : "+v"(wa[0]), "+v"(wa[1]), "+v"(xa[0][0]), "+v"(xa[0][1]), "+v"(xa[1][0]), "+v"(xa[1][1]));
  }
  DEVI void first() {
    xread<2, 0, 0>();
    xread<2, 0, 1>();
    wread<2, 0>();
    wread<2, 1>();
    wread<2, 2>();
    wread<2, 3>();
    asm volatile("s_waitcnt lgkmcnt(0)");
  }
};

constexpr int ZQ = 4;
constexpr int NR1 = 3;
DEVI void p3_g0(char* smem, char* __restrict__ ws, int layer, int mi, int ni, int wm, int lane MK_ARG) {
  constexpr int SB = K3C::SB;
  const unsigned lds0 = (unsigned)(size_t)smem;
  const int g = lane >> 4, r = lane & 15;
  const unsigned lw = lds0 + (unsigned)(LW_OFF + layer * 4), me = 1u << (8 * wm);
  f32x4 ac[2][9];
#pragma unroll
  for (int mt = 0; mt < 2; ++mt)
#pragma unroll
    for (int j = 0; j < 9; ++j) ac[mt][j] = f32x4{0.f, 0.f, 0.f, 0.f};
  unsigned ra = lds0 + (unsigned)(K3_ST_OFF + (wm * 32 + g * 8) * 4), ca = lds0 + (unsigned)(K3_C2_OFF + r * 8);
  asm volatile("" : "+v"(ra), "+v"(ca));
  s16x8 xc[4][2][2];
  UMath<false> gm(ac, xc);
  gm.ra = ra;
  gm.ca = ca;
  gm.xa = lds0 + (unsigned)(g * 2048 + (wm * 32 + 8 * (r >> 2) + (r & 3)) * 16);
  gm.wa = lds0 + (unsigned)(K3C::XB + (g * 8 + (r & 7)) * K3C::P + (r >> 3) * 16);
  unsigned adw = lw + LW_AD * 4, mev = vconst(me), po = (unsigned)(ni * 1024 + lane * 4);
  const char* zp = uni_ptr(ws + OFF_ZP + (size_t)layer * SZ_ZP + (size_t)mi * 16384 + wm * 256);
  asm volatile("" : "+v"(gm.xa), "+v"(gm.wa), "+v"(adw), "+v"(po));
  asm volatile("" : "+s"(zp));
  place();
  bar();
  gm.first();
  gm.run();
  asm volatile("s_mov_b64 exec, 1\n\tds_add_u32 %0, %1\n\ts_mov_b64 exec, -1" ::"v"(adw), "v"(mev) : "memory");
  MK(11);
  f32x2 z[9][4];
  f32x2 sm[4], sq[4];
  {
    f32x4 (&rc)[4][2] = gm.rc;
    f32x2 (&cs)[9] = gm.cs;
    asm volatile("s_waitcnt lgkmcnt(1)"
                 : "+v"(rc[0][0]), "+v"(rc[0][1]), "+v"(rc[1][0]), "+v"(rc[1][1]), "+v"(rc[2][0]), "+v"(rc[2][1]), "+v"(rc[3][0]), "+v"(rc[3][1]));
#pragma unroll
    for (int j = 0; j < 9; ++j) asm volatile("" : "+v"(cs[j]));
    auto RC = [&](int k, int i) { return f32x2{rc[k][i / 2][(i % 2) * 2], rc[k][i / 2][(i % 2) * 2 + 1]}; };
#pragma unroll
    for (int j = 0; j < 9; ++j) {
      f32x2 u[4], w[4];
#pragma unroll
      for (int i = 0; i < 4; ++i) u[i] = __builtin_elementwise_fma(RC(0, i), cs[j], f32x2{ac[i / 2][j][(i % 2) * 2], ac[i / 2][j][(i % 2) * 2 + 1]});
#pragma unroll
      for (int i = 0; i < 4; ++i) w[i] = u[i] * u[i];
#pragma unroll
      for (int i = 0; i < 4; ++i) w[i] = __builtin_elementwise_fma(w[i], RC(3, i), RC(2, i));
#pragma unroll
      for (int i = 0; i < 4; ++i) w[i] = u[i] * w[i];
      __builtin_amdgcn_sched_barrier(0);
#pragma unroll
      for (int i = 0; i < 4; ++i) w[i] = f32x2{__builtin_amdgcn_exp2f(w[i][0]), __builtin_amdgcn_exp2f(w[i][1])};
      __builtin_amdgcn_sched_barrier(0);
#pragma unroll
      for (int i = 0; i < 4; ++i) w[i] = __builtin_elementwise_fma(w[i], RC(1, i), RC(1, i));
      __builtin_amdgcn_sched_barrier(0);
#pragma unroll
      for (int i = 0; i < 4; ++i) w[i] = f32x2{__builtin_amdgcn_rcpf(w[i][0]), __builtin_amdgcn_rcpf(w[i][1])};
      __builtin_amdgcn_sched_barrier(0);
#pragma unroll
      for (int i = 0; i < 4; ++i) z[j][i] = u[i] * w[i];
#pragma unroll
      for (int i = 0; i < 4; ++i) {
        if (j == 1) sm[i] = z[0][i] + z[1][i];
        if (j > 1) sm[i] += z[j][i];
      }
#pragma unroll
      for (int i = 0; i < 4; ++i) sq[i] = j ? __builtin_elementwise_fma(z[j][i], z[j][i], sq[i]) : z[j][i] * z[j][i];
      asm volatile("" : "+v"(z[j][0]), "+v"(z[j][1]), "+v"(z[j][2]), "+v"(z[j][3]));
      __builtin_amdgcn_sched_barrier(0);
    }
  }
  f32x4 e;
  {
    float v[4][4], d[2][4], f[4];
#pragma unroll
    for (int i = 0; i < 4; ++i) {
      v[i][0] = sm[i][0];
      v[i][1] = sq[i][0];
      v[i][2] = sm[i][1];
      v[i][3] = sq[i][1];
    }
    asm volatile("s_nop 1" : "+v"(v[3][1]), "+v"(v[3][3]), "+v"(v[2][1]), "+v"(v[2][3]));
#pragma unroll
    for (int i = 0; i < 2; ++i)
#pragma unroll
      for (int c = 0; c < 4; ++c)
        asm volatile("v_add_f32_dpp %0, %1, %1 row_ror:8 row_mask:0xf bank_mask:0x3\n\tv_add_f32_dpp %0, %2, %2 row_ror:8 row_mask:0xf bank_mask:0xc"
                     : "=&v"(d[i][c])
                     : "v"(v[i][c]), "v"(v[i + 2][c]));
#pragma unroll
    for (int c = 0; c < 4; ++c)
      asm volatile("v_add_f32_dpp %0, %1, %1 row_shl:4 row_mask:0xf bank_mask:0x5\n\tv_add_f32_dpp %0, %2, %2 row_shr:4 row_mask:0xf bank_mask:0xa"
                   : "=&v"(f[c])
                   : "v"(d[0][c]), "v"(d[1][c]));
    asm volatile(
        "v_add_f32_dpp %0, %0, %0 quad_perm:[1,0,3,2] row_mask:0xf bank_mask:0xf\n\t"
        "v_add_f32_dpp %1, %1, %1 quad_perm:[1,0,3,2] row_mask:0xf bank_mask:0xf\n\t"
        "v_add_f32_dpp %2, %2, %2 quad_perm:[1,0,3,2] row_mask:0xf bank_mask:0xf\n\t"
        "v_add_f32_dpp %3, %3, %3 quad_perm:[1,0,3,2] row_mask:0xf bank_mask:0xf\n\t"
        "v_add_f32_dpp %0, %0, %0 quad_perm:[2,3,0,1] row_mask:0xf bank_mask:0xf\n\t"
        "v_add_f32_dpp %1, %1, %1 quad_perm:[2,3,0,1] row_mask:0xf bank_mask:0xf\n\t"
        "v_add_f32_dpp %2, %2, %2 quad_perm:[2,3,0,1] row_mask:0xf bank_mask:0xf\n\t"
        "v_add_f32_dpp %3, %3, %3 quad_perm:[2,3,0,1] row_mask:0xf bank_mask:0xf"
        : "+v"(f[0]), "+v"(f[1]), "+v"(f[2]), "+v"(f[3]));
    e = f32x4{f[0], __uint_as_float(__float_as_uint(f[1]) & 0x7fffffffu), f[2], __uint_as_float(__float_as_uint(f[3]) & 0x7fffffffu)};
  }
  asm volatile("s_mov_b64 exec, %3\n\tglobal_store_dwordx4 %0, %1, %2 sc1\n\ts_mov_b64 exec, -1\n\ts_nop 0" ::"v"(po), "v"(e), "s"(zp),
               "s"(0x1111111111111111ull)
               : "memory");
  asm volatile("s_mov_b64 exec, 1\n\tds_add_u32 %0, %1\n\ts_mov_b64 exec, -1" ::"v"(adw), "v"(mev) : "memory");
  __builtin_amdgcn_s_setprio(2);
  MK(12);
  const char* dst = uni_ptr(ws + OFF_Z2 + (size_t)layer * SZ_Z + (size_t)ni * S_ * 288 + (size_t)(mi * 4 + wm) * 9216 + 4096);
  const unsigned lane16 = (unsigned)lane * 16u;
  u32x4 wb[9];
  {
    static_assert(b_k(7) == 2 && b_k(8) == 3, "order");
    const char* wp = uni_ptr(ws + OFF_PI + (size_t)ni * (18 * K3C::WB) + 11 * K3C::WB + wm * 9216 + 4096);
#pragma unroll
    for (int j = 0; j < 8; ++j) asm volatile("global_load_dwordx4 %0, %1, %2 offset:%3 sc1" : "=v"(wb[j]) : "v"(lane16), "s"(wp), "n"(j * 1024 - 4096) : "memory");
    asm volatile("global_load_dwordx4 %0, %1, %2 sc1" : "=v"(wb[8]) : "v"(lane16 + 4096u), "s"(wp) : "memory");
  }
  u32x4 y[9];
  {
    const unsigned src = (unsigned)(g * 4096 + r * 16);
    unsigned zw = lds0 + (unsigned)(K3_ZC_OFF + (wm * 32 + 2 * r + (g & 1)) * 4), zr = lds0 + (unsigned)(K3_ZC_OFF + (wm * 32 + g * 8) * 4);
    asm volatile("" : "+v"(zw), "+v"(zr));
    f32x4 a, b, c, d;
    __builtin_amdgcn_s_sleep(6);
#pragma unroll 1
    for (unsigned spin = 0; spin < SPIN_MEM; ++spin) {
      asm volatile(
          "global_load_dwordx4 %0, %4, %5 sc1\n\t"
          "global_load_dwordx4 %1, %4, %5 offset:1024 sc1\n\t"
          "global_load_dwordx4 %2, %4, %5 offset:2048 sc1\n\t"
          "global_load_dwordx4 %3, %4, %5 offset:3072 sc1\n\t"
          "s_waitcnt vmcnt(0)"
          : "=&v"(a), "=&v"(b), "=&v"(c), "=&v"(d)
          : "v"(src), "s"(zp)
          : "memory");
      const unsigned bad = (__float_as_uint(a[1]) | __float_as_uint(a[3]) | __float_as_uint(b[1]) | __float_as_uint(b[3]) |
                            __float_as_uint(c[1]) | __float_as_uint(c[3]) | __float_as_uint(d[1]) | __float_as_uint(d[3])) >> 31;
      if (!__any(bad)) break;
    }
    MK(13);
    float s, q, zmb, zi;
    red16(a, b, c, d, s, q);
    ln_finish(s, q, 1.0f / (float)FH_, zmb, zi);
    f32x4 R[2], M[2];
    asm volatile(
        "ds_write2st64_b32 %4, %5, %6 offset1:2\n\t"
        "ds_read_b128 %0, %7\n\tds_read_b128 %2, %7 offset:512\n\tds_read_b128 %1, %7 offset:16\n\tds_read_b128 %3, %7 offset:528\n\t"
        "s_waitcnt lgkmcnt(0)"
        : "=&v"(R[0]), "=&v"(R[1]), "=&v"(M[0]), "=&v"(M[1])
        : "v"(zw), "v"(zi), "v"(-zmb * zi), "v"(zr)
        : "memory");
    MK(28);
    auto st = [&](int j) {
      if (j < 8) asm volatile("global_store_dwordx4 %0, %1, %2 offset:%3 sc1\n\ts_nop 1" ::"v"(lane16), "v"(y[j]), "s"(dst), "n"(j * 1024 - 4096) : "memory");
      else asm volatile("global_store_dwordx4 %0, %1, %2 sc1\n\ts_nop 1" ::"v"(lane16 + 4096u), "v"(y[j]), "s"(dst) : "memory");
    };
#pragma unroll
    for (int j = 0; j < 9; ++j) {
      {
        float t[8];
#pragma unroll
        for (int k = 0; k < 8; ++k) {
          t[k] = __builtin_fmaf(z[j][k / 2][k % 2], R[k / 4][k % 4], M[k / 4][k % 4]);
          asm volatile("" : "+v"(t[k]));
        }
        y[j] = u32x4{pack2(t[0], t[1]), pack2(t[2], t[3]), pack2(t[4], t[5]), pack2(t[6], t[7])};
      }
      st(j);
      __builtin_amdgcn_sched_barrier(0);
    }
    MK(29);
  }
  lds_inc(lw + LW_AD * 4, me);
  MK(24);
  __builtin_amdgcn_s_setprio(2);
  lds_spin4<2>(lw + LW_S * 4, 4u + (unsigned)(wm >> 1));
  {
    const unsigned fa = lds0 + (unsigned)((wm >> 1) * SB + K3C::XB + (wm & 1) * 9216) + lane16;
#pragma unroll
    for (int j = 0; j < 9; ++j) {
      asm volatile("ds_write_b128 %0, %1 offset:%2" ::"v"(fa), "v"(wb[j]), "n"(j * 1024) : "memory");
    }
    lds_inc(lw + LW_AD * 4, me);
  }
  announce(lw + LW_Z * 4, 4u, ws, ((mi >= ZQ) ? CN_Z2 : CN_Z) + layer, lane * 16 + ni, lane < 16);
  lds_inc(lw + LW_AD * 4, me);
  MK(27);
  f32x4 acc[2][9];
  {
    float bs[2];
    asm volatile("ds_read2_b32 %0, %1 offset1:16\n\ts_waitcnt lgkmcnt(0)"
                 : "=v"(*reinterpret_cast<f32x2*>(bs))
                 : "v"(lds0 + (unsigned)(K3_BS_OFF + (wm * 32 + r) * 4))
                 : "memory");
    lds_spin4<2>(lw + LW_S * 4, 6u + (unsigned)(wm >> 1));
    const unsigned fa = lds0 + (unsigned)((2 + (wm >> 1)) * SB + K3C::XB + (wm & 1) * 9216) + lane16;
#pragma unroll
    for (int j = 0; j < 9; ++j) {
      asm volatile("ds_write_b128 %0, %1 offset:%2" ::"v"(fa), "v"(y[j]), "n"(j * 1024) : "memory");
    }
    lds_inc(lw + LW_AD * 4, me);
#pragma unroll
    for (int mt = 0; mt < 2; ++mt) {
      f32x4 b4 = {bs[mt], bs[mt], bs[mt], bs[mt]};
      asm volatile("" : "+v"(b4));
#pragma unroll
      for (int j = 0; j < 9; ++j) acc[mt][j] = b4;
    }
  }
  u32x4 z1[9];
  const char* hp = uni_ptr(ws + OFF_H + (size_t)layer * SZ_Z + (size_t)((mi * 2 + (wm >> 1)) * 288 + ni * 18) * 1024);
  unsigned ho = (unsigned)((lane >> 5) * 1024 + ((wm & 1) * 32 + (lane & 31)) * 16);
  unsigned zb = lds0 + (unsigned)(wm * 4096) + lane16, zf = lw + (unsigned)(LW_S * 4 + wm);
  asm volatile("" : "+v"(ho), "+v"(zb), "+v"(zf));
  asm volatile("" : "+s"(hp));
  {
    CMath gm(acc);
    gm.setup(lds0, wm, lane);
    unsigned za = lds0 + (unsigned)(K3_Z1_OFF + wm * 5120) + lane16;
    asm volatile("" : "+v"(za));
    MK(15);
    lds_spin4<2>(lw + LW_L * 4, 8u);
    lds_spin4(lw + LW_AD * 4, 6u);
    MK(17);
    gm.first();
    __builtin_amdgcn_s_setprio(0);
    gm.stage<2, false>();
    gm.stage<3>();
    place();
#pragma unroll 1
    for (int t = 0; t < 7; ++t) {
      gm.stage<0>();
#pragma unroll
      for (int j = 0; j < NR1; ++j) asm volatile("ds_read_b128 %0, %1 offset:%2" : "=v"(z1[j]) : "v"(za), "n"(j * 1024));
      gm.stage<1>();
#pragma unroll
      for (int j = 0; j < NR1; ++j) asm volatile("" : "+v"(z1[j]));
      gm.stage<2>();
      gm.stage<3>();
    }
    gm.stage<0>();
    gm.stage<1, false, true>();
#pragma unroll
    for (int j = 0; j < 18; ++j) asm volatile("" : "+v"(acc[j / 9][j % 9]));
  }
  MK(16);
  if (NR1 < 5) lds_inc(lw + LW_AD * 4, me);
  unsigned zv;
  u32x4 zl[4];
#pragma unroll
  for (int j = 0; j < 9; ++j) {
    const bool sec = j >= NR1 + 3;
    if (j == NR1 || j == NR1 + 4) {
      unsigned n = 1u << 18;
      asm volatile(
          "s_waitcnt lgkmcnt(0)\n\tv_cmp_gt_u32 vcc, %8, %0\n\ts_cbranch_vccnz .Lw%=\n\t.subsection 1\n.Lw%=:\n\tds_read_u8 %0, %6\n\tds_read_b128 %2, %7 offset:%9\n\t"
          "ds_read_b128 %3, %7 offset:%9+1024\n\tds_read_b128 %4, %7 offset:%9+2048\n\tds_read_b128 %5, %7 offset:%9+3072\n\ts_waitcnt lgkmcnt(0)\n\t"
          "v_cmp_gt_u32 vcc, %8, %0\n\t" SAFE_OUT(".Lr%=") "s_cbranch_vccnz .Lw%=\n\ts_branch .Lr%=\n\t.subsection 0\n.Lr%=:"
          : "+v"(zv), NOP_(n), "+v"(zl[0]), "+v"(zl[1]), "+v"(zl[2]), "+v"(zl[3])
          : "v"(zf), "v"(zb), "n"(sec ? 11 : 10), "n"(sec ? SB : 0)
          : "memory", "vcc", "scc");
#pragma unroll
      for (int k = 0; k < 4; ++k)
        if (j + k < 9) z1[j + k] = zl[k];
    }
    if (j == NR1 - 1 || (j == NR1 + 3 && j < 8))
      asm volatile("ds_read_u8 %0, %5\n\tds_read_b128 %1, %6 offset:%7\n\tds_read_b128 %2, %6 offset:%7+1024\n\tds_read_b128 %3, %6 offset:%7+2048\n\t"
                   "ds_read_b128 %4, %6 offset:%7+3072"
                   : "=&v"(zv), "=&v"(zl[0]), "=&v"(zl[1]), "=&v"(zl[2]), "=&v"(zl[3])
                   : "v"(zf), "v"(zb), "n"(sec ? SB : 0)
                   : "memory");
    asm volatile("" : "+v"(z1[j]));
    u32x2 h[2];
#pragma unroll
    for (int mt = 0; mt < 2; ++mt) {
      const f32x4 a = acc[mt][j];
      h[mt] = u32x2{pack2v(f32x2{bf_lo(z1[j][2 * mt]), bf_hi(z1[j][2 * mt])} * f32x2{a[0], a[1]}),
                    pack2v(f32x2{bf_lo(z1[j][2 * mt + 1]), bf_hi(z1[j][2 * mt + 1])} * f32x2{a[2], a[3]})};
    }
    const auto s0 = __builtin_amdgcn_permlane16_swap(h[0][0], h[1][0], false, false);
    const auto s1 = __builtin_amdgcn_permlane16_swap(h[0][1], h[1][1], false, false);
    const u32x4 v = {s0[0], s1[0], s0[1], s1[1]};
    asm volatile("global_store_dwordx4 %0, %1, %2 offset:%3 sc1\n\ts_nop 1" ::"v"(ho), "v"(v), "s"(hp + (j / 2) * 4096), "n"((j % 2) * 2048) : "memory");
    __builtin_amdgcn_sched_barrier(0);
  }
  MK(18);
}

DEVI void k3_const(char* smem, char* ws, const u16* b_sp, int layer, int bid, int mi, int ni, int wm) {
  const int lane = lane_id(), l = lane < 54 ? lane : 53, p = l / 18;
  const char* kp = uni_ptr(ws + OFF_CSK + bid * 2048 + wm * 512);
  const unsigned q16 = (unsigned)(l - p * 18) * 16u;
  unsigned bs;
  f32x4 t;
  lds_spin((unsigned)(size_t)smem + (unsigned)(LW_OFF + (layer ? LW_T + 1 : LW_HELLO) * 4), layer ? 12u : 2u);
  asm volatile("global_load_ushort %0, %1, %2" : "=v"(bs) : "v"((unsigned)(lane & 31) * 2u), "s"(uni_ptr((const char*)(b_sp + mi * 128 + wm * 32))) : "memory");
  if (layer) {
    asm volatile("global_load_dwordx4 %0, %1, %2 sc1\n\ts_waitcnt vmcnt(0)" : "=&v"(t) : "v"(q16), "s"(kp) : "memory");
  } else {
    const char* cp = uni_ptr(ws + OFF_CSP + ni * 1152 + wm * 288);
    f32x4 a[6];
#pragma unroll
    for (int i = 0; i < 6; ++i)
      asm volatile("global_load_dwordx4 %0, %1, %2 sc1" : "=v"(a[i]) : "v"(q16 + (unsigned)(p * (6 * F_ * 4))), "s"(cp + i * (F_ * 4)) : "memory");
    asm volatile("s_waitcnt vmcnt(0)" : "+v"(a[0]), "+v"(a[1]), "+v"(a[2]), "+v"(a[3]), "+v"(a[4]), "+v"(a[5]));
    t = ((a[0] + a[1]) + (a[2] + a[3])) + (a[4] + a[5]);
#pragma unroll
    for (int j = 0; j < 4; ++j)
      t[j] += __int_as_float(__builtin_amdgcn_ds_bpermute((lane + 18) * 4, __float_as_int(t[j]))) +
              __int_as_float(__builtin_amdgcn_ds_bpermute((lane + 36) * 4, __float_as_int(t[j])));
  }
  asm volatile("" : "+v"(bs));
  const unsigned lds0 = (unsigned)(size_t)smem;
  f32x4 t0 = wm < 2 ? f32x4{t[0], t[0], t[1], t[1]} : t, t1 = wm < 2 ? f32x4{t[2], t[2], t[3], t[3]} : t;
  unsigned a0 = lds0 + (unsigned)(wm < 2 ? K3_C2_OFF + wm * 576 + lane * 32 : K3_C1_OFF + (wm - 2) * 288 + lane * 16), a1 = a0 + (wm < 2 ? 16u : 0u);
  unsigned ab = lds0 + (unsigned)(K3_BS_OFF + (wm * 32 + (lane & 31)) * 4);
  bs <<= 16;
  asm volatile("" : "+v"(t0), "+v"(t1), "+v"(a0), "+v"(a1), "+v"(ab), "+v"(bs));
  bar();
  asm volatile("ds_write_b32 %4, %5\n\ts_mov_b64 exec, 0x3ffff\n\tds_write_b128 %0, %2\n\tds_write_b128 %1, %3\n\ts_mov_b64 exec, -1" ::"v"(a0), "v"(a1),
               "v"(t0), "v"(t1), "v"(ab), "v"(bs)
               : "memory");
  if (layer == 0 && lane < 24) asm volatile("global_store_dwordx4 %0, %1, %2\n\ts_nop 1" ::"v"((unsigned)lane * 16u), "v"(t), "s"(kp) : "memory");
}

DEVI void p3_g1(char* smem, char* __restrict__ ws, int layer, int mi, int ni, int wm, int lane MK_ARG) {
  const unsigned lds0 = (unsigned)(size_t)smem;
  const int g = lane >> 4, r = lane & 15;
  const unsigned lw = lds0 + (unsigned)(LW_OFF + layer * 4);
  const unsigned xrd = lds0 + (unsigned)(g * 2048 + (wm * 32 + r) * 16);
  s16x8 xc[4][2][2];
  {
    const char* xs = uni_ptr(ws + OFF_XS + (size_t)layer * 2 * SZ_XS + (size_t)(mi * 2 + (wm >> 1)) * 4096 + (wm & 1) * 256);
    f32x4 a, b;
    auto pick = [&](int t) {
      bar();
      asm volatile(
          "ds_read_b128 %0, %4\n\tds_read_b128 %1, %4 offset:256\n\tds_read_b128 %2, %4 offset:8192\n\tds_read_b128 %3, %4 offset:8448\n\t"
          "s_waitcnt lgkmcnt(0)"
          : "=&v"(xc[t][0][0]), "=&v"(xc[t][0][1]), "=&v"(xc[t][1][0]), "=&v"(xc[t][1][1])
          : "v"(xrd + (unsigned)(t * K3C::SB))
          : "memory");
    };
    __builtin_amdgcn_s_setprio(2);
    pick(0);
    pick(1);
    asm volatile("global_load_dwordx4 %0, %2, %3 sc1\n\tglobal_load_dwordx4 %1, %2, %3 offset:512 sc1"
                 : "=&v"(a), "=&v"(b)
                 : "v"((unsigned)(g * 1024 + r * 16)), "s"(xs)
                 : "memory");
    pick(2);
    pick(3);
    bar();
    asm volatile("s_waitcnt vmcnt(0)" : "+v"(a), "+v"(b) : : "memory");
    float s, q, mb, rinv;
    red8(a, b, s, q);
    bar();
    asm volatile("" : "+v"(s), "+v"(q));
    ln_finish(s, q, 1.0f / (float)D_, mb, rinv);
    asm volatile("" : "+v"(mb), "+v"(rinv));
    bar();
    asm volatile("" : "+v"(mb), "+v"(rinv));
    const float k0 = -2.0f * 0.7978845608028654f * 1.4426950408889634f, k1 = k0 * 0.044715f;
    float sd = __builtin_amdgcn_rcpf(rinv), c0 = k0 * rinv, c1 = k1 * rinv * rinv * rinv;
    asm volatile("" : "+v"(sd));
    asm volatile("" : "+v"(c0));
    asm volatile("" : "+v"(c1));
    const float e0 = lane < 32 ? -mb : c0, e1 = lane < 32 ? sd : c1;
    asm volatile("ds_write2st64_b32 %0, %1, %2 offset1:2" ::"v"(
                     lds0 + (unsigned)(K3_ST_OFF + (g >> 1) * 1024 + (wm * 32 + 2 * r + (g & 1)) * 4)),
                 "v"(e0), "v"(e1)
                 : "memory");
    bar();
    asm volatile("s_waitcnt lgkmcnt(0)" ::: "memory");
    bar();
    __builtin_amdgcn_s_setprio(0);
  }
  f32x4 ac[2][9];
#pragma unroll
  for (int mt = 0; mt < 2; ++mt)
#pragma unroll
    for (int j = 0; j < 9; ++j) ac[mt][j] = f32x4{0.f, 0.f, 0.f, 0.f};
  {
    UMath<true> gm(ac, xc);
    gm.xa = xrd;
    gm.wa = lds0 + (unsigned)(K3C::XB + (g * 8 + (r & 7)) * K3C::P + (r >> 3) * 16);
    gm.fa = lw + (unsigned)(LW_AD * 4 + (lane & 3));
    gm.fw = lw + (unsigned)(LW_S * 4 + wm);
    gm.fv = 0u;
    asm volatile("" : "+v"(gm.fa), "+v"(gm.fw), "+v"(gm.fv), "+v"(gm.xa), "+v"(gm.wa));
    __builtin_amdgcn_s_sleep(40);
    lds_spin4<4>(lw + LW_L * 4, 4u);
    gm.first();
    place();
    lds_spin4(lw + LW_AD * 4, 2u);
    __builtin_amdgcn_s_setprio(1);
    gm.run();
    __builtin_amdgcn_s_setprio(0);
  }
  MK(14);
  f32x2 rc[4];
  f32x4 cs[9];
  const int l2 = lane_id();
  {
    const unsigned ra = lds0 + (unsigned)(K3_ST_OFF + (wm * 32 + (l2 & 15)) * 4), ca = lds0 + (unsigned)(K3_C1_OFF + (l2 & 48));
#pragma unroll
    for (int k = 0; k < 4; ++k) asm volatile("ds_read2_b32 %0, %1 offset1:16" : "=v"(rc[k]) : "v"(ra + (unsigned)(k * 512)) : "memory");
#pragma unroll
    for (int j = 0; j < 9; ++j) asm volatile("ds_read_b128 %0, %1 offset:%2" : "=v"(cs[j]) : "v"(ca), "n"(j * 64) : "memory");
    asm volatile("s_waitcnt lgkmcnt(0)" : "+v"(rc[0]), "+v"(rc[1]), "+v"(rc[2]), "+v"(rc[3]));
#pragma unroll
    for (int j = 0; j < 9; ++j) asm volatile("" : "+v"(cs[j]));
  }
  const unsigned za = lds0 + (unsigned)(K3_Z1_OFF + wm * 5120 + l2 * 16);
  u32x4 z1[9];
  unsigned zf;
#pragma unroll
  for (int t = 0; t < 18; ++t) {
    const int j = t / 2, mt = t % 2;
    float u[4], w[4];
#define Q4(A, B, C, D) A "\n\t" B "\n\t" C "\n\t" D "\n\t"
    asm volatile(Q4("v_fma_f32 %0, %8, %12, %16", "v_fma_f32 %1, %8, %13, %17", "v_fma_f32 %2, %8, %14, %18", "v_fma_f32 %3, %8, %15, %19")
                 Q4("v_mul_f32 %4, %0, %0", "v_mul_f32 %5, %1, %1", "v_mul_f32 %6, %2, %2", "v_mul_f32 %7, %3, %3")
                 Q4("v_fma_f32 %4, %4, %11, %10", "v_fma_f32 %5, %5, %11, %10", "v_fma_f32 %6, %6, %11, %10", "v_fma_f32 %7, %7, %11, %10")
                 Q4("v_mul_f32 %4, %4, %0", "v_mul_f32 %5, %5, %1", "v_mul_f32 %6, %6, %2", "v_mul_f32 %7, %7, %3")
                 Q4("v_exp_f32 %4, %4", "v_exp_f32 %5, %5", "v_exp_f32 %6, %6", "v_exp_f32 %7, %7")
                 Q4("v_fma_f32 %4, %4, %9, %9", "v_fma_f32 %5, %5, %9, %9", "v_fma_f32 %6, %6, %9, %9", "v_fma_f32 %7, %7, %9, %9")
                 Q4("v_rcp_f32 %4, %4", "v_rcp_f32 %5, %5", "v_rcp_f32 %6, %6", "v_rcp_f32 %7, %7")
                 Q4("v_mul_f32 %4, %4, %0", "v_mul_f32 %5, %5, %1", "v_mul_f32 %6, %6, %2", "v_mul_f32 %7, %7, %3")
                 : "=&v"(u[0]), "=&v"(u[1]), "=&v"(u[2]), "=&v"(u[3]), "=&v"(w[0]), "=&v"(w[1]), "=&v"(w[2]), "=&v"(w[3])
                 : "v"(rc[0][mt]), "v"(rc[1][mt]), "v"(rc[2][mt]), "v"(rc[3][mt]), "v"(cs[j][0]), "v"(cs[j][1]), "v"(cs[j][2]), "v"(cs[j][3]),
                   "v"(ac[mt][j][0]), "v"(ac[mt][j][1]), "v"(ac[mt][j][2]), "v"(ac[mt][j][3]));
    z1[j][2 * mt] = pack2(w[0], w[1]);
    z1[j][2 * mt + 1] = pack2(w[2], w[3]);
    if (mt && j < NR1) asm volatile("ds_write_b128 %0, %1 offset:%2" ::"v"(za), "v"(z1[j]), "n"(j * 1024) : "memory");
    if (t == 9) asm volatile("s_waitcnt lgkmcnt(0)" ::: "memory");
    __builtin_amdgcn_sched_barrier(0);
    if (t == 2 * ZQ - 7)
      asm volatile("global_load_dword %0, %1, %2 sc1"
                   : "=v"(zf)
                   : "v"(vconst(0u)), "s"(uni_ptr(ws + OFF_CNT + (size_t)((CN_Z2 + layer) * 256 + mi * 16 + ni) * 128))
                   : "memory");
    if (t == 2 * ZQ - 4) {
      asm volatile("s_waitcnt vmcnt(0)" : "+v"(zf) : : "memory");
      if (wm == 1 && __builtin_amdgcn_readfirstlane(zf) != 15u - ZQ) cnt_wait(ws, CN_Z2 + layer, mi * 16 + ni, 16u - ZQ);
    }
    bar();
    __builtin_amdgcn_sched_barrier(0);
  }
  MK(15);
#pragma unroll 1
  for (int t = 20; t < 32; ++t) bar();
  __builtin_amdgcn_s_sleep(9);
#pragma unroll
  for (int k = NR1; k < 9; ++k) {
    if (k == NR1 + 4) lds_spin4(lw + LW_AD * 4, 7u);
    asm volatile("ds_write_b128 %0, %1 offset:%2" ::"v"(lds0 + (unsigned)(wm * 4096 + l2 * 16)), "v"(z1[k]),
                 "n"(((k - NR1) / 4) * K3C::SB + ((k - NR1) % 4) * 1024)
                 : "memory");
    if (k == NR1 + 3 || k == 8) asm volatile("ds_write_b8 %0, %1" ::"v"(lw + (unsigned)(LW_S * 4 + wm)), "v"(vconst(k == NR1 + 3 ? 10u : 11u)) : "memory");
  }
}

constexpr int W5A = 22;
constexpr int NPF = 30;
struct Loader {
  u32x4 pf[NPF];
  unsigned lds0, lane16;
  int wl, lane;

  template <int R0, int N, int P0, int PE>
  DEVI void pf_load(const char* src) {
    const unsigned o = lane16 + (unsigned)(wl * 1024);
#pragma unroll
    for (int t = 0; t < N; ++t) {
      unsigned a = o + (unsigned)((P0 + 4 * t) * 1024);
      if (P0 + 4 * t + 3 >= PE && P0 + 4 * t + wl >= PE) a = lane16;
      asm volatile("global_load_dwordx4 %0, %1, %2 sc1" : "=v"(pf[R0 + t]) : "v"(a), "s"(src) : "memory");
    }
  }
  template <int R0, int N>
  DEVI void pf_pin() {
#pragma unroll
    for (int t = 0; t < N; ++t) asm volatile("" : "+v"(pf[R0 + t]));
  }
  template <int R0, int N>
  DEVI void pf_undef() {
#pragma unroll
    for (int t = 0; t < N; ++t) asm volatile("" : "=v"(pf[R0 + t]));
  }
  template <int PB, int A, int N>
  DEVI void pf_put(unsigned dst) {
    constexpr int T0 = (A - PB) / 4, T1 = (A + N - 1 - PB) / 4;
    const unsigned a = lds0 + dst + lane16 + (unsigned)((wl + PB + 4 * T0 - A + 4) * 1024) - 4096u;
#pragma unroll
    for (int t = T0; t <= T1; ++t) {
      const int p0 = PB + 4 * t;
      if ((p0 >= A || p0 + wl >= A) && (p0 + 3 < A + N || p0 + wl < A + N))
        asm volatile("ds_write_b128 %0, %1 offset:%2" ::"v"(a), "v"(pf[t]), "n"((t - T0) * 4096) : "memory");
    }
  }
  template <int N>
  DEVI void dma_run(const char* src, unsigned dst) {
    constexpr int B = N / 4, R = N % 4;
    const unsigned f = (unsigned)(wl * B + (wl < R ? wl : R)) * 1024u, o = lane16 + f, m = lds0 + dst + f;
    dma_seq<B>(src, o, m);
    if (wl < R) dma16_sc1(src + B * 1024, o, m + (unsigned)(B * 1024));
  }
  template <int V>
  DEVI void wait_w() {
    asm volatile("s_cmp_lt_u32 %0, 2\n\ts_cbranch_scc1 1f\n\ts_waitcnt vmcnt(%1)\n1:\n\ts_waitcnt vmcnt(%2)" ::"s"(wl), "n"(V - 1), "n"(V) : "memory", "scc");
  }
  template <int V>
  DEVI void wbar() {
    asm volatile("s_waitcnt vmcnt(%0) lgkmcnt(0)" ::"n"(V) : "memory");
    bar();
  }

  DEVI void p1(char* smem, char* ws, int bid, int rt, int hp MK_ARG) {
    constexpr int SB = K1C::SB;
    static_assert(K1C::XB == 18 * 1024 && K1C::WB == 14 * 1024, "K1 shape");
    const char* wp = uni_ptr(ws + OFF_PQ + (size_t)hp * (9 * K1C::WB) + 4 * K1C::WB);
    static_assert(3 * SB + K1C::XB >= P5_W + 5 * 18432, "free while the GEMM before ends");
    const char* wq = wp - 4 * K1C::WB;
    dma_run<14>(wq + 3 * K1C::WB, (unsigned)(3 * SB + K1C::XB));
    dma_run<28>(wp, (unsigned)K1_WX);
    if (wl == 3) dma16_sc1(uni_ptr(ws + OFF_CSQ + bid * 512), lane16 & 511u, lds0 + (unsigned)K1_CS_OFF);
    __builtin_amdgcn_s_setprio(0);
    wait_vm<0>();
    const bool lo = wl < 2;
    const int x0 = lo ? wl * 5 : wl * 4 + 2, w0 = lo ? wl * 3 : wl * 4 - 2;
    unsigned xo[5];
#pragma unroll
    for (int i = 0; i < 5; ++i) {
      const int c = (x0 + i) * 64 + lane;
      const int ch = c / 144;
      int gr = rt * 128 - 16 + (c - ch * 144);
      if (gr < 0) gr = 0;
      xo[i] = (unsigned)(((gr >> 6) * 72 + ch + 3 - (i & 3)) * 1024 + (gr & 63) * 16);
      asm volatile("" : "+v"(xo[i]));
    }
    const char* xp = uni_ptr(ws + OFF_X1 + SZ_X - 3072);
    wp += 2 * K1C::WB + w0 * 1024;
    auto dma_x = [&](unsigned so) {
      const unsigned m = lds0 + so + (unsigned)(x0 * 1024);
      dma4<4, true>(xp, xo[0], xo[1], xo[2], xo[3], m);
      if (lo) dma16_sc1(xp, xo[4], m + 4096u);
      xp += 8192;
    };
    auto dma_w = [&](unsigned so) {
      const unsigned m = lds0 + so + (unsigned)(K1C::XB + w0 * 1024);
      dma_seq<3>(wp, lane16, m);
      if (!lo) dma16_sc1(wp + 3072, lane16, m + 3072u);
      wp += K1C::WB;
    };
    const char* xs = uni_ptr(ws + OFF_XS + SZ_XS);
    const int g = lane >> 4, p2 = 64 + (lane & 7);
    int ra = rt * 128 - 16 + wl * 32 + 2 * (lane & 15);
    if (ra < 0) ra = 0;
    unsigned sa = (unsigned)(((ra >> 6) * 8 + g * 2) * 512 + (ra & 63) * 8);
    unsigned sb = (unsigned)(((rt * 2 + 1) * 8 + (lane >> 3)) * 512 + (2 * p2 - 80) * 8);
    unsigned sw = lds0 + (unsigned)(K1_ST_OFF + (2 * (lane < 32 ? wl * 16 + (lane & 15) : p2) + (g & 1)) * 8);
    asm volatile("" : "+v"(sa), "+v"(sb), "+v"(sw));
    MK(22);
    __builtin_amdgcn_s_setprio(3);
    bar();
    dma_run<14>(wq, (unsigned)K1C::XB);
    dma_run<14>(wq + K1C::WB, (unsigned)(SB + K1C::XB));
    dma_run<14>(wq + 2 * K1C::WB, (unsigned)(2 * SB + K1C::XB));
    MK(1);
    bar();
    dma_x(0u);
    dma_x((unsigned)SB);
    wbar<4>();
    MK(29);
    dma_x((unsigned)(2 * SB));
    dma_x((unsigned)(3 * SB));
    wbar<8>();
    MX(1, 3);
    dma_x(0u);
    wbar<8>();
    MX(1, 4);
    dma_x((unsigned)SB);
    f32x4 a, b, e;
    asm volatile("global_load_dwordx4 %0, %3, %5 sc1\n\tglobal_load_dwordx4 %1, %3, %5 offset:512 sc1\n\tglobal_load_dwordx4 %2, %4, %5 sc1"
                 : "=&v"(a), "=&v"(b), "=&v"(e)
                 : "v"(sa), "v"(sb), "s"(xs)
                 : "memory");
    wbar<11>();
    MX(1, 5);
    dma_w((unsigned)SB);
    dma_x((unsigned)(2 * SB));
    wbar<15>();
    MX(1, 6);
    dma_w((unsigned)(2 * SB));
    dma_x((unsigned)(3 * SB));
    wbar<19>();
    MX(1, 7);
    dma_w((unsigned)(3 * SB));
    dma_x(0u);
    wbar<16>();
    MX(1, 8);
    asm volatile("" : "+v"(a), "+v"(b), "+v"(e));
    float s, q;
    {
      float t[4] = {e[0], e[1], e[2], e[3]}, s2, q2;
      asm volatile(
          "v_add_f32_dpp %0, %0, %0 row_ror:8 row_mask:0xf bank_mask:0xf\n\tv_add_f32_dpp %1, %1, %1 row_ror:8 row_mask:0xf bank_mask:0xf\n\t"
          "v_add_f32_dpp %2, %2, %2 row_ror:8 row_mask:0xf bank_mask:0xf\n\tv_add_f32_dpp %3, %3, %3 row_ror:8 row_mask:0xf bank_mask:0xf\n\ts_nop 1"
          : "+v"(t[0]), "+v"(t[1]), "+v"(t[2]), "+v"(t[3]));
      red4(t, s2, q2);
      red8(a, b, s, q);
      s = lane < 32 ? s : s2;
      q = lane < 32 ? q : q2;
      asm volatile("" : "+v"(s), "+v"(q));
    }
    wbar<8>();
    MX(1, 9);
    {
      asm volatile("" : "+v"(s), "+v"(q));
      float mb, rinv;
      ln_finish(s, q, 1.0f / (float)D_, mb, rinv);
      asm volatile("ds_write_b64 %0, %1" ::"v"(sw), "v"(f32x2{-mb, rinv}) : "memory");
    }
    wbar<0>();
    MK(30);
  }

  template <int I>
  DEVI void p5_step(const char* wp) {
    constexpr int T = 2 * I + 1;
    wbar<(T == 1 ? 63 : (T <= 11 ? 3 : 0))>();
    if constexpr (T <= 9) {
      const unsigned wo = (unsigned)(12288 + (wl & 1) * 3072), so = (unsigned)(P5_W + ((T + 2 + (wl >> 1)) & 7) * 18432);
      dma_seq<3>(wp + (T + 7) * 18432, lane16 + wo + (unsigned)((wl >> 1) * 18432), lds0 + so + wo);
      pf_put<0, 12 * (T - 1), 12>((unsigned)(P5_W + ((T + 2) % 8) * 18432));
      pf_put<0, 12 * T, 12>((unsigned)(P5_W + ((T + 3) % 8) * 18432));
    }
  }
  template <int... S>
  DEVI void p5_steps(const char* wp, std::integer_sequence<int, S...>) {
    (p5_step<S>(wp), ...);
  }

  template <int T0, int T1>
  DEVI void w5_load(const char* w5p) {
    const unsigned o = lane16 + (unsigned)(wl * 1024);
#pragma unroll
    for (int t = T0; t < T1; ++t)
      asm volatile("global_load_dwordx4 %0, %1, %2 sc1" : "=v"(pf[t]) : "v"(o + (unsigned)((t / 3 + 8) * 18432 + (t % 3) * 4096)), "s"(w5p) : "memory");
  }
  DEVI void p35(char* smem, char* ws, const u16* w_sp, int layer, int bid, int mi, int ni, int rb, int ct, int hp MK_ARG) {
    using C = K3C;
    constexpr int NS = C::NS, SB = C::SB, NDX = 4, NDW = 5, VPS = NDX + NDW;
    const char* wip = uni_ptr(ws + OFF_PI + (size_t)ni * (18 * C::WB));
    if (layer) {
      pf_put<-48, 36, 18>((unsigned)(2 * SB + C::XB));
      pf_put<-48, 54, 18>((unsigned)(3 * SB + C::XB));
    } else {
      dma_run<18>(wip + 2 * C::WB, (unsigned)(2 * SB + C::XB));
      dma_run<18>(wip + 3 * C::WB, (unsigned)(3 * SB + C::XB));
    }
    dma_run<18>(wip + C::WB, (unsigned)(SB + C::XB));
    const char* w5p = uni_ptr(ws + OFF_PW + (size_t)ct * (36 * 9216));
    const int w0 = wl < 2 ? wl * 5120 : wl * 4096 + 2048;
    const unsigned m0x = lds0 + (unsigned)(wl * 4096), m0w = lds0 + (unsigned)(C::XB + w0);
    const unsigned wo = lane16 + (unsigned)w0;
    const unsigned lw = lds0 + (unsigned)(LW_OFF + layer * 4), me = 1u << (8 * wl);
    const unsigned wo4 = wo + 4096u;
    auto dma_w = [&](const char* wp, unsigned so) {
      dma_seq<4>(wp, wo, m0w + so);
      if (wl < 2) dma16_sc1(wp, wo4, m0w + so + 4096u);
    };
    {
      const char* xp = uni_ptr(ws + OFF_X1 + (size_t)layer * 2 * SZ_X + (size_t)(mi * 2 * 72 + wl * 2) * 1024 - 1024);
      const unsigned xo0 = lane16 + 1024u, xo1 = lane16 + 72u * 1024u, xo3 = lane16 + 71u * 1024u;
      auto dma_x = [&](int k, unsigned so) { dma4<4, true>(xp + k * 8192, xo0, xo1, lane16, xo3, m0x + so); };
      MK(10);
      bar();
      dma_x(0, 0u);
      dma_x(1, (unsigned)SB);
      wait_vm<4>(); MX(3, 2); wbar<4>(); MX(2, 2);
      MK(25);
      {
        dma_x(2, (unsigned)(2 * SB));
        dma_x(3, (unsigned)(3 * SB));
        dma_x(4, (unsigned)(4 * SB));
        wbar<12>(); MX(2, 3);
        dma_x(5, 0u);
        wait_vm<12>();
      }
      bar(); MX(2, 4);
      dma_w(wip + 4 * C::WB, 0u);
      dma_x(6, (unsigned)SB);
      wait_w<8 + VPS>();
#pragma unroll 1
      for (int t = 1; t < 3; ++t) {
        bar(); MX(2, 4 + t);
        dma_w(wip + (t + 4) * C::WB, (unsigned)(t * SB));
        dma_x(t + 6, (unsigned)((t + 1) * SB));
        wait_w<NDX + VPS>();
      }
      bar(); MX(2, 7);
      dma_w(wip + 7 * C::WB, (unsigned)(3 * SB));
      wait_w<VPS>();
      bar(); MX(2, 8);
      dma_w(wip + 8 * C::WB, 0u);
      wait_w<NDW>();
      bar(); MX(2, 9);
      const char* wp = wip + 9 * C::WB;
      dma_w(wp + b_k(0) * C::WB, (unsigned)SB);
      if (layer == 0) {
        const char* sp = uni_ptr((const char*)w_sp + (size_t)(bid * 4 + wl) * 8192);
        const unsigned so = (unsigned)((lane >> 5) * 4096 + (lane & 31) * 16);
#pragma unroll
        for (int i = 0; i < WSP_V; ++i) dma16(sp + i * 512, so, lds0 + (unsigned)K3_DM_OFF);
        dma16(sp + (bid >> 4) * 256, so & ~0x100u, lds0 + (unsigned)K3_DM_OFF);
      }
      w5_load<0, 4>(w5p);
      wait_w<NDW + 4>();
      MX(3, 10); bar(); MX(2, 10);
      dma_w(wp + b_k(1) * C::WB, (unsigned)(2 * SB));
      w5_load<4, 8>(w5p);
      MK(26);
      lds_spin4(lw + LW_AD * 4, 1u);
      dma_w(wp + b_k(2) * C::WB, (unsigned)(3 * SB));
      dma_w(wp + b_k(3) * C::WB, 0u);
      w5_load<8, W5A>(w5p);
      wait_vm<W5A - 8>();
      lds_inc(lw + LW_L * 4, 4u * me);
      w5_load<W5A, 30>(w5p);
      MK(11);
    }
    {
      unsigned xoff[NDX];
#pragma unroll
      for (int i = 0; i < NDX; ++i) {
        const int c = wl * 256 + i * 64 + lane;
        const int row = c >> 3, cc = c & 7;
        xoff[i] = (unsigned)((mi * 128 + row) * S_ * 2 - i * 1024) + (unsigned)((cc ^ ((row >> 1) & 7)) * 16);
      }
      auto dma_x = [&](const char* xp, unsigned so) { dma4<4, false>(xp, xoff[0], xoff[1], xoff[2], xoff[3], m0x + so); };
      const char* xp = uni_ptr((const char*)w_sp);
      const char* wp = uni_ptr(ws + OFF_Z2 + (size_t)layer * SZ_Z + (size_t)ni * S_ * 288);
      static_for<4, 7>([&](auto T_) {
        constexpr int t = T_;
        lds_spin4<2>(lw + LW_S * 4, (unsigned)(t - 3));
        dma_w(wip + (9 + b_k(t)) * C::WB, (unsigned)(((t + 1) % 4) * SB));
        if constexpr (t > 4) {
          dma_x(xp + mi * 256 + (t - 5) * 128, (unsigned)((t - 3) * SB));
          wait_w<NDW + (t - 4) * NDX>();
          lds_inc(lw + LW_L * 4, me);
        }
      });
      wait_vm<NDX>();
      lds_inc(lw + LW_L * 4, me);
      wait_vm<0>();
      lds_inc(lw + LW_L * 4, me);
      pf_pin<0, 30>();
      MK(13);
      lds_spin4<2>(lw + LW_AD * 4, 5u);
      MK(24);
      int left = 2 * mi;
      auto next = [&]() {
        const int d = --left == 0 ? 3 : 1;
        xp += d * 128;
        wp += d * C::WB;
      };
      if (mi == 0) {
        xp += 256;
        wp += 2 * C::WB;
      }
      if (wl == 0) {
        cnt_wait(ws, CN_Z + layer, mi * 16 + ni, (unsigned)ZQ);
        lds_wr32(lw + LW_GO * 4, 1u);
      }
      lds_spin4<2>(lw + LW_AD * 4, 6u);
      dma_x(xp, 0u);
      dma_x(xp + 128, (unsigned)SB);
      spin_ge<2>(lw + LW_GO * 4, 1u);
      lds_spin4<2>(lw + LW_S * 4, 8u);
      dma_w(wp, 0u);
      lds_spin4<2>(lw + LW_S * 4, 9u);
      dma_w(wp + C::WB, (unsigned)SB);
      next();
      next();
      unsigned slot = (unsigned)(2 * SB);
      auto cissue = [&]() {
        dma_x(xp, slot);
        dma_w(wp, slot);
        next();
        slot = (slot == (unsigned)((NS - 1) * SB)) ? 0u : slot + (unsigned)SB;
      };
      auto cstep = [&]() {
        bar();
        cissue();
      };
      wait_w<NDW>();
      MK(15);
      int j = 2;
      if (layer == 0) {
        const char* sp = uni_ptr((const char*)w_sp + (size_t)(bid * 4 + wl) * 8192 + WSP_V * 512);
        unsigned d = 0u;
#pragma unroll 1
        for (; j < 2 + 32 - 4 * WSP_V; ++j) {
          cstep();
          asm volatile("s_load_dword %0, %1, 0\n\ts_load_dword %0, %1, 64\n\ts_load_dword %0, %1, 0x1000\n\ts_load_dword %0, %1, 0x1040"
                       : "+s"(d)
                       : "s"(sp));
          sp += 128;
          wait_w<VPS>();
        }
#pragma unroll 1
        for (int e = j + 4; j < e; ++j) {
          cstep();
          asm volatile("" : "+s"(d));
          wait_w<VPS>();
        }
        asm volatile("s_waitcnt lgkmcnt(0)" : "+s"(d));
      }
      place();
#pragma unroll 1
      for (; j <= 29; ++j) {
        cstep();
        wait_w<VPS>();
      }
      bar();
    }
    dma_run<33>(w5p, (unsigned)(P5_W + 54 * 1024));
    dma_run<23>(w5p + 67 * 1024, (unsigned)(P5_W + 121 * 1024));
    if (wl == 3) dma16_sc1(w5p + 143 * 1024, lane16, lds0 + (unsigned)(P5_W + 53 * 1024));
    wait_vm<14>();
    bar();
    dma_run<34>(w5p + 33 * 1024, (unsigned)(P5_W + 87 * 1024));
    wait_vm<0>();
    bar();
    if (wl) {
      const unsigned f = (unsigned)(wl - 1) * 18432u;
      dma_seq<17>(w5p + 90 * 1024, lane16 + f, lds0 + (unsigned)P5_W + f);
      if (wl < 3) dma16_sc1(w5p + 107 * 1024, lane16 + f, lds0 + (unsigned)(P5_W + 17 * 1024) + f);
    } else {
      cnt_wait(ws, CN_H + layer, rb * 8 + ct, 8u);
    }
    MK(20);
    bar();
    p5_steps(w5p, std::make_integer_sequence<int, 7>{});
    if (layer == 0) {
      if (bid < 24 && wl == 0 && lane < 8) pk_st16(ws + OFF_HELLO + bid * 128 + lane16, u32x4{0u, 0u, 0u, 0u});
    }
  }
};

__global__ __launch_bounds__(768, 3) void k_main(const u16* __restrict__ x0, const u16* __restrict__ wqkv, const u16* __restrict__ wo,
                                                 const u16* __restrict__ w_in, const u16* __restrict__ w_sp,
                                                 const u16* __restrict__ b_sp, const u16* __restrict__ w_out,
                                                 u16* __restrict__ out, char* ws) {
  extern __shared__ __attribute__((aligned(16))) char smem[];
  const char* code0 = code_here();
  (void)code0;
  const unsigned lds0 = (unsigned)(size_t)smem;
  int wave = __builtin_amdgcn_readfirstlane((int)threadIdx.x >> 6);
  int bid = blockIdx.x;
#define FRESH() asm volatile("" : "+s"(bid), "+s"(ws), "+s"(wave))
#define XCD (bid & 7)
#define IDX (bid >> 3)
#define MI ((XCD >> 2) * 8 + (IDX >> 2))
#define NI ((XCD & 1) * 8 + (XCD & 2) * 2 + (IDX & 3))
#define RB (XCD * 4 + (IDX >> 3))
#define CT (IDX & 7)
#define RT ((XCD >> 1) * 4 + (IDX >> 3))
#define HP ((XCD & 1) * 8 + (IDX & 7))
  MK(0);
  if (wave < 8) {
    constexpr int PER = (2 * NL_MAIN + 31) / 32;
    static_assert(PER <= 64 && 31 * PER < 2 * NL_MAIN, "one load");
    unsigned o, last, st, t;
    const char* p;
    asm volatile(
        "s_lshr_b32 %3, %5, 3\n\t"
        "s_cmp_eq_u32 %3, 31\n\ts_cselect_b32 %1, 63, 0\n\ts_cselect_b32 %2, %7, %6\n\t"
        "s_mul_i32 %0, %3, %8\n\t"
        "s_cmp_eq_u32 %4, 2\n\ts_cselect_b32 %0, %0, 0\n\ts_cselect_b32 %1, %2, %1\n\t"
        "s_mul_i32 %3, %3, 0xf300\n\ts_add_u32 %3, %3, 0x200\n\t"
        "s_cmp_eq_u32 %4, 3\n\ts_cselect_b32 %0, %3, %0\n\ts_cselect_b32 %1, 53, %1\n\ts_cselect_b32 %2, 0x480, 64\n\t"
        "s_mul_i32 %3, %5, 0x1e60\n\t"
        "s_cmp_eq_u32 %4, 5\n\ts_cselect_b32 %0, %3, %0\n\t"
        "s_add_u32 %3, %3, 0x1000\n\t"
        "s_cmp_eq_u32 %4, 4\n\ts_cselect_b32 %0, %3, %0\n\ts_cselect_b32 %1, 57, %1\n\t"
        "s_cmp_ge_u32 %4, 5\n\ts_cselect_b32 %1, 63, %1\n\t"
        "s_mul_i32 %3, %5, 0x2400\n\t"
        "s_cmp_eq_u32 %4, 7\n\ts_cselect_b32 %0, %3, %0\n\t"
        "s_add_u32 %3, %3, 0x1000\n\t"
        "s_cmp_eq_u32 %4, 6\n\ts_cselect_b32 %0, %3, %0\n\t"
        "s_add_u32 %3, %3, 0x1000\n\t"
        "s_cmp_eq_u32 %4, 1\n\ts_cselect_b32 %0, %3, %0\n\ts_cselect_b32 %1, 15, %1"
        : "=&s"(o), "=&s"(last), "=&s"(st), "=&s"(t)
        : "s"(wave), "s"(bid), "n"(PER - 1), "n"(2 * NL_MAIN - 1 - 31 * PER), "n"(PER * 64)
        : "scc");
    const unsigned l = (unsigned)lane_id();
    unsigned a = o + (l < last ? l : last) * st;
    asm volatile("" : "+v"(a));
    asm volatile("s_cmp_lg_u32 %1, 2\n\ts_cbranch_scc1 1f\n\ts_mov_b32 m0, %3\n\ts_nop 0\n\tglobal_load_lds_dword %0, %2\n\tv_mov_b32 %0, 0\n1:"
                          : "+v"(a)
                          : "s"(wave), "s"(code0), "s"(lds0 + (unsigned)K1R_DUMMY)
                          : "memory", "scc");
    asm volatile("s_cmp_eq_u32 %1, 2\n\ts_cselect_b64 %0, %4, %5\n\ts_bitcmp1_b32 0x38, %1\n\ts_cselect_b64 %0, %3, %0\n\t"
                 "s_bitcmp1_b32 0xc2, %1\n\ts_cselect_b64 %0, %2, %0"
                 : "=&s"(p)
                 : "s"(wave), "s"(x0), "s"(wqkv), "s"(wo), "s"(b_sp)
                 : "scc");
    touch64(p, a, lds0 + (unsigned)K1R_DUMMY);
    MF(12);
    math_start(lds0, ws, bid, wave, (int)l);
    MF(13);
  }

  if (__builtin_expect(wave >= 8, 1)) {
    __builtin_amdgcn_s_setprio(3);
    {
      const int lane = lane_id();
      RawLd rl;
      int job, pj;
      const char* pg;
      rl.front(x0, wqkv, wo, ws, bid, lds0, wave - 8, lane, RT, HP, pj, [&]() {
        int b = bid;
        asm volatile("" : "+s"(b));
        const int xcd = b & 7, c16 = b >> 4, hp = (b & 1) * 8 + ((b >> 3) & 7);
        const bool cross = (((hp * 72) & ~15) & 127) > 48;
        const int cj = (int)((0x65743210u >> (4 * xcd)) & 7u) * 16 + c16;
        job = cross ? (cj < 88 ? 128 + cj : 999) : xcd * 16 + c16;
        pj = cross ? cj - 88 : ((xcd & 5) == 0 && (c16 & 3) == (c16 >> 2) ? 40 + xcd * 2 + (c16 >> 2) : -1);
        const int k = b >> 3;
        pg = k < 11 ? (const char*)w_in + k * 0x80000 : k < 17 ? (const char*)w_out + (k - 11) * 0x80000 : k < 22 ? (const char*)out + (k - 17) * 0x80000
             : (const char*)w_sp + (k < 30 ? (k - 22) * 0x100000 : 0x7ffff0);
      }, pg MF_PASS);
      if (job < 216) k1_raw_loader<true>(rl, w_in, w_out, ws, rl.tag, bid, lds0, wave - 8, lane, RT, HP, CT, job, pj MF_PASS);
      else k1_raw_loader<false>(rl, w_in, w_out, ws, rl.tag, bid, lds0, wave - 8, lane, RT, HP, CT, job, pj MF_PASS);
    }
    Loader ld;
    ld.lds0 = lds0;
#pragma unroll 1
    for (int layer = 0; layer < 2; ++layer) {
      FRESH();
      ld.wl = wave - 8;
      ld.lane = lane_id();
      ld.lane16 = (unsigned)ld.lane * 16u;
      const char* wip = uni_ptr(ws + OFF_PI + (size_t)NI * (18 * K3C::WB));
      if (layer) {
        ld.p1(smem, ws, bid, RT, HP MK_PASS);
        ld.pf_load<0, 21, 0, 81>(uni_ptr(ws + OFF_PO + (size_t)CT * (18 * 4608)));
        ld.pf_load<21, 9, 36, 72>(wip);
        wait_vm<9>();
        ld.pf_pin<0, 21>();
        lds_spin(lds0 + (unsigned)(LW_OFF + (LW_T + layer) * 4), 8u);
        __builtin_amdgcn_s_sleep(4);
        ld.pf_put<0, 0, 40>((unsigned)P2_W);
        wait_vm<0>();
        ld.pf_pin<21, 9>();
        bar();
        ld.pf_put<0, 40, 41>((unsigned)(P2_W + 40 * 1024));
      } else {
        ld.pf_undef<21, 9>();
        MK(3);
        MK(4);
        bar();
        ld.dma_run<81>(uni_ptr(ws + OFF_PO + (size_t)CT * (18 * 4608)), (unsigned)P2_W);
      }
      if (layer == 0 && ld.wl == 0) cnt_wait(ws, CN_O + layer, RB * 8 + CT, 16u);
      MK(6);
      asm volatile("s_waitcnt vmcnt(0) lgkmcnt(0)" ::: "memory");
      bar();
      lds_spin(lds0 + (unsigned)(LW_OFF + (LW_T + layer) * 4), 12u);
      if (layer == 0) {
        if (ld.wl == 0) {
          cnt_wait(ws, CN_PK, 8 + NI, 36u);
          lds_wr32(lds0 + (unsigned)(LW_OFF + LW_HELLO * 4), 2u);
        } else {
          lds_spin(lds0 + (unsigned)(LW_OFF + LW_HELLO * 4), 2u);
        }
        MK(7);
      }
      static_assert(2 * K3C::SB <= P2_W, "free during the first residual GEMM");
      ld.dma_run<18>(wip, (unsigned)K3C::XB);
      MX(6, 24);
      if (layer == 0 && ld.wl == 0) cnt_wait(ws, CN_PK, 24 + CT, 72u);
      wait_vm<0>();
      ld.pf_pin<21, 9>();
      MX(6, 25);
      bar();
      FRESH();
      ld.wl = wave - 8;
      ld.p35(smem, ws, w_sp, layer, bid, MI, NI, RB, CT, HP MK_PASS);
    }
  } else {
#pragma unroll 1
    for (int layer = 0; layer < 2; ++layer) {
      FRESH();
      {
        const int lane = lane_id(), r0 = RT * 128;
        float* csq = reinterpret_cast<float*>(ws + OFF_CSQ + bid * 512);
        Attn at;
        at.setup(lds0 + (unsigned)(layer ? K1_T_OFF : K1R_T_OFF), ws + OFF_O + (size_t)layer * SZ_X, r0, HP, wave, lane);
        if (layer) {
          if ((wave & 2) == 0) k1_gemm<2, 4>(smem, ws, RT, HP, (wave & 1) * 2 + (wave & 4), 0, wave >= 4, lane MK_PASS);
          else if (wave < 4) k1_gemm<3, 3>(smem, ws, RT, HP, (wave & 1) * 3, 4, false, lane MK_PASS);
          else if (wave == 6) k1_gemm<2, 3>(smem, ws, RT, HP, 6, 4, true, lane MK_PASS);
          else k1_gemm<1, 7>(smem, ws, RT, HP, 8, 0, true, lane MK_PASS);
        } else {
          if (wave == 0) k1_gemm_raw<3, false>(smem, HP, 0, lane MF_PASS);
          else if (wave < 4) k1_gemm_raw<2, false>(smem, HP, 1 + 2 * wave, lane MF_PASS);
          else if (wave < 7) k1_gemm_raw<2, true>(smem, HP, 2 * (wave - 4), lane MF_PASS);
          else k1_gemm_raw<3, true>(smem, HP, 6, lane MF_PASS);
        }
        msync(lds0 + (unsigned)(LW_OFF + (LW_T + layer) * 4));
        MK(3);
        at.run(r0, HP, wave, lane);
        if (layer == 0) {
          if (wave == 0 && lane < 32)
            asm volatile("global_store_dwordx4 %0, %1, %2 sc1\n\ts_nop 1" ::"v"((unsigned)lane * 16u),
                         "v"(*reinterpret_cast<const u32x4*>(smem + K1R_CS_OFF + lane * 16)), "s"(uni_ptr((const char*)csq))
                         : "memory");
          lds_spin(lds0 + (unsigned)(LW_OFF + LW_HELLO * 4), 1u);
        }
        MK(4);
        announce(lds0 + (unsigned)(LW_OFF + (LW_O + layer * 22 + (wave >> 2)) * 4), 4u, ws, CN_O + layer, (RT * 2 + (wave >> 2)) * 8 + lane,
                 lane < 8);
        MK(5);
        bar();
      }
      FRESH();
      {
        if (wave < 4) {
          const int lane = lane_id();
          u32x2 xv[5];
          ResIn ri;
          ri.setup(layer ? ws + OFF_X1 + SZ_X : (const char*)x0, layer == 0, RB, CT, wave, lane);
          ri.load(xv);
          ResOut ro;
          ro.setup(ws + OFF_X1 + (size_t)layer * 2 * SZ_X, ws + OFF_XS + (size_t)layer * 2 * SZ_XS, out, false, RB, CT, wave, lane);
          f32x4 acc[2][5];
          p2_gemm(acc, xv, lds0, uni_ptr(ws + OFF_O + (size_t)layer * SZ_X + (size_t)RB * (72 * 1024)), layer, wave, lane MK_PASS);
          MK(7);
          ro.run(acc[0], false);
          MK(8);
          announce(lds0 + (unsigned)(LW_OFF + (LW_X1 + layer) * 4), 4u, ws, CN_X1 + layer, (RB >> 1) * 16 + lane, lane < 16);
          MK(9);
          bar();
          FRESH();
          if (wave == 3) cnt_wait(ws, CN_X1 + layer, MI * 16 + NI, 16u);
          bar();
          MK(10);
          p3_g0(smem, ws, layer, MI, NI, wave, lane_id() MK_PASS);
        } else {
          if (layer && wave == 5) cnt_wait(ws, CN_O + layer, RB * 8 + CT, 16u);
          bar();
          k3_const(smem, ws, b_sp, layer, bid, MI, NI, wave & 3);
          MX(6, 26);
          bar();
          MK(10);
          FRESH();
          p3_g1(smem, ws, layer, MI, NI, wave & 3, lane_id() MK_PASS);
        }
      }
      FRESH();
      {
        const int lane = lane_id();
        if (wave < 4) {
          u32x2 xv[5];
          ResIn ri;
          ri.setup(ws + OFF_X1 + (size_t)layer * 2 * SZ_X, false, RB, CT, wave, lane);
          f32x4 acc[2][5];
          ResOut ro;
          ro.setup(ws + OFF_X1 + SZ_X, ws + OFF_XS + SZ_XS, out, layer != 0, RB, CT, wave, lane);
          ResX<36, 33, 8, P5_W, P5_XW, P5_X0, 3, P5_BAR> gm(acc);
          gm.setup(lds0, uni_ptr(ws + OFF_H + (size_t)layer * SZ_Z + (size_t)RB * (288 * 1024)), wave, lane, (unsigned)P5_RED, layer + 1u);
          announce(lds0 + (unsigned)(LW_OFF + (LW_H + layer * 2 + (wave >> 1)) * 4), 2u, ws, ((NI >= 8) ? CN_H2 : CN_H) + layer,
                   (MI * 2 + (wave >> 1)) * 8 + lane, lane < 8);
          MK(19);
          bar();
          ri.load(xv);
          gm.prow();
          place();
          bar();
          MK(20);
          gm.prox<0>(std::make_integer_sequence<int, P5_X0>{});
          gm.init<2 * P5_X0>(xv);
          __builtin_amdgcn_sched_barrier(0);
          gm.run();
          MK(21);
          ro.run(acc[0], layer != 0);
          MK(22);
          if (layer == 0) {
            announce(lds0 + (unsigned)(LW_OFF + LW_X2 * 4), 4u, ws, CN_X2, (RB >> 1) * 16 + lane,
                     lane < 16 || (lane < 32 && (RB & 1) && RB < 31));
            MK(23);
          }
        } else {
          bar();
          bar();
#pragma unroll
          for (int i = 0; i < 7; ++i) {
            if (i == 1 && wave == 5) {
              unsigned f;
              const char* cp = uni_ptr(ws + OFF_CNT + (size_t)((CN_H2 + layer) * 256 + RB * 8 + CT) * 128);
              asm volatile("global_load_dword %0, %1, %2 sc1" : "=v"(f) : "v"(vconst(0u)), "s"(cp) : "memory");
              bar();
              asm volatile("s_waitcnt vmcnt(0)" : "+v"(f) : : "memory");
              if (__builtin_amdgcn_readfirstlane(f) != 7u) cnt_wait(ws, CN_H2 + layer, RB * 8 + CT, 8u);
              continue;
            }
            asm volatile("s_waitcnt lgkmcnt(0)" ::: "memory");
            bar();
          }
          MK(22);
        }
        if (layer == 0 && wave < 4) bar();
      }
    }
  }
}

}

extern "C" void kernel_launch(void* const* d_in, const int* in_sizes, int n_in, void* d_out, int out_size,
                              void* d_ws, size_t ws_size, hipStream_t stream) {
  const u16* x0 = (const u16*)d_in[0];
  const u16* wqkv = (const u16*)d_in[1];
  const u16* wo = (const u16*)d_in[2];
  const u16* w_in = (const u16*)d_in[4];
  const u16* w_sp = (const u16*)d_in[5];
  const u16* b_sp = (const u16*)d_in[6];
  const u16* w_out = (const u16*)d_in[7];
  char* ws = (char*)d_ws;
  (void)hipFuncSetAttribute((const void*)k_main, hipFuncAttributeMaxDynamicSharedMemorySize, LDS_ALL);
  k_main<<<256, 768, LDS_ALL, stream>>>(x0, wqkv, wo, w_in, w_sp, b_sp, w_out, (u16*)d_out, ws);
}
```

```cpp
#include <hip/hip_runtime.h>
#include <hip/hip_bf16.h>
#include <stdint.h>

#define DEVI __device__ __forceinline__

typedef unsigned short u16;
typedef __attribute__((ext_vector_type(8))) short s16x8;
typedef __attribute__((ext_vector_type(4))) short s16x4;
typedef __attribute__((ext_vector_type(4))) float f32x4;
typedef __attribute__((ext_vector_type(2))) float f32x2;
typedef __attribute__((ext_vector_type(2))) __bf16 bf16x2;
typedef __attribute__((ext_vector_type(2))) unsigned u32x2;
typedef __attribute__((ext_vector_type(4))) unsigned u32x4;

#define MK(I) ((void)0)
#define MX(S, I) ((void)0)
#define MF(I) ((void)0)
#define MF_ARG
#define MF_PASS
#define MK_ARG
#define MK_PASS

#define SPIN_MEM (1u << 22)
#define SAFE_OUT(L)
#define SAFE_AND
#define NOP_(n) "=s"(n)

namespace {

constexpr int S_ = 2048, D_ = 576, F_ = 4608, FH_ = 2304;
constexpr int NQKV = 3 * D_;

constexpr size_t GAP = 256;
constexpr size_t SZ_X = (size_t)S_ * D_ * 2 + GAP;
constexpr size_t SZ_Z = (size_t)S_ * FH_ * 2 + GAP;
constexpr size_t OFF_X1 = GAP;
constexpr size_t OFF_O = OFF_X1 + 3 * SZ_X;
constexpr size_t OFF_Z2 = OFF_O + 2 * SZ_X;
constexpr size_t OFF_H = OFF_Z2 + 2 * SZ_Z;
constexpr size_t SZ_XS = (size_t)S_ * 8 * 8;
constexpr size_t OFF_XS = OFF_H + 2 * SZ_Z;
constexpr size_t OFF_CSQ = OFF_XS + 3 * SZ_XS;
constexpr size_t OFF_ZP = OFF_CSQ + 256 * 512;
constexpr size_t SZ_ZP = (size_t)16 * 16 * 1024;
constexpr size_t OFF_PQ = OFF_ZP + 2 * SZ_ZP;
constexpr size_t OFF_PO = OFF_PQ + (size_t)16 * 9 * 64 * 112 * 2 + GAP;
constexpr size_t OFF_PI = OFF_PO + (size_t)D_ * D_ * 2 + GAP;
constexpr size_t OFF_PW = OFF_PI + (size_t)32 * 9 * 64 * 144 * 2 + GAP;
constexpr size_t OFF_CSP = OFF_PW + (size_t)FH_ * D_ * 2 + GAP;
constexpr size_t OFF_CNT = OFF_CSP + (size_t)18 * F_ * 4;
constexpr int CN_X1 = 0, CN_H = 2, CN_X2 = 4, CN_O = 5, CN_Z = 7, CN_PK = 9, CN_H2 = 10, CN_Z2 = 12, NCNT = 14;
constexpr size_t OFF_TAG = OFF_CNT + (size_t)NCNT * 256 * 128;
constexpr size_t OFF_HELLO = OFF_TAG + 128;
constexpr size_t OFF_POT = OFF_HELLO + 2048;
constexpr size_t OFF_CSK = OFF_POT + 1024;
constexpr size_t WS_END = OFF_CSK + 256 * 2048;
static_assert(WS_END <= (size_t)64 << 20, "workspace");

DEVI float bf_lo(unsigned u) { return __uint_as_float(u << 16); }
DEVI float bf_hi(unsigned u) { return __uint_as_float(u & 0xffff0000u); }
DEVI unsigned pack2(float a, float b) {
  f32x2 v = {a, b};
  bf16x2 r = __builtin_convertvector(v, bf16x2);
  return __builtin_bit_cast(unsigned, r);
}
DEVI float rbf(float a) {
  return bf_lo(pack2(a, 0.f));
}

DEVI float sum_g(float x) {
  const unsigned u = __float_as_uint(x);
  const auto p = __builtin_amdgcn_permlane32_swap(u, u, false, false);
  const unsigned v = __float_as_uint(__uint_as_float(p[0]) + __uint_as_float(p[1]));
  const auto q = __builtin_amdgcn_permlane16_swap(v, v, false, false);
  return __uint_as_float(q[0]) + __uint_as_float(q[1]);
}

DEVI unsigned vconst(unsigned c) {
  unsigned r;
  asm volatile("v_mov_b32 %0, %1" : "=v"(r) : "s"(c));
  return r;
}
DEVI void place() {
  __builtin_amdgcn_sched_barrier(0);
  asm volatile(".p2align 6");
  __builtin_amdgcn_sched_barrier(0);
}
DEVI const char* uni_ptr(const char* p) {
  unsigned long long v = (unsigned long long)p;
  unsigned lo = __builtin_amdgcn_readfirstlane((unsigned)v), hi = __builtin_amdgcn_readfirstlane((unsigned)(v >> 32));
  asm("s_nop 4" : "+s"(lo), "+s"(hi));
  return (const char*)(((unsigned long long)hi << 32) | lo);
}

DEVI void dma16(const char* sbase, unsigned voff, unsigned m0val) {
  asm volatile("s_mov_b32 m0, %2\n\ts_nop 0\n\tglobal_load_lds_dwordx4 %0, %1"
               :
               : "v"(voff), "s"(sbase), "s"(m0val)
               : "memory");
}

DEVI void ln_finish(float s, float q, float invn, float& mb, float& rinv) {
  float mean = s * invn;
  asm("" : "+v"(mean));
  float var = fmaxf(__builtin_fmaf(-mean, mean, q * invn), 0.f);
  mb = rbf(mean);
  float v = rbf(var);
  float t = rbf(v + 1e-5f);
  float sd = rbf(__builtin_amdgcn_sqrtf(t));
  rinv = __builtin_amdgcn_rcpf(sd);
}

DEVI unsigned pack2v(f32x2 v) { return pack2(v[0], v[1]); }

template <int BM_, int BN_>
struct GemmCfg {
  static constexpr int BM = BM_, BN = BN_, NS = 4;
  static constexpr int P = BN * 2;
  static constexpr int XB = BM * 128, WB = 64 * P, SB = XB + WB;
  static constexpr int LDS_BYTES = NS * SB;
};

DEVI int swap23(int r) { return (r & ~12) | ((r & 4) << 1) | ((r & 8) >> 1); }

DEVI void lds_wr32(unsigned addr, unsigned v) { asm volatile("ds_write_b32 %0, %1" ::"v"(addr), "v"(v) : "memory"); }

template <class C, int MT, int NT, int D, int R, bool XCM, int WT, bool SQ = false, unsigned long long WS = 0>
struct GemmMath {
  static constexpr int WO = WT ? 1 : 2;
  static constexpr int NS = C::NS, SB = C::SB, P = C::P, NU = 2 * NT;
  static constexpr int XPU = MT <= 2 ? MT : MT - 2;
  static constexpr int XU = (MT + XPU - 1) / XPU;
  static_assert(NU % R == 0 && R > D && D <= NT && XU <= D, "shape");
  s16x4 wlo[R], whi[R];
  s16x8 wf[R];
  s16x8 xf[2][MT];
  unsigned xrd[2], wrd, xa[2], wa, so;
  f32x4 (&acc)[MT][NT];
  f32x4 accq[SQ ? MT : 1];
  unsigned long long wtab = WS;
  DEVI GemmMath(f32x4 (&a)[MT][NT]) : acc(a) {}

  template <int PU>
  DEVI void wread_lo() {
    constexpr int off = (PU / NT) * 32 * P + (PU % NT) * (WT == 2 ? 1024 : 32);
    if constexpr (WT) asm volatile("ds_read_b128 %0, %1 offset:%2" : "=v"(wf[PU % R]) : "v"(wa), "n"(off));
    else asm volatile("ds_read_b64_tr_b16 %0, %1 offset:%2" : "=v"(wlo[PU % R]) : "v"(wa), "n"(off));
  }
  template <int PU>
  DEVI void wread_hi() {
    if constexpr (!WT)
      asm volatile("ds_read_b64_tr_b16 %0, %1 offset:%2" : "=v"(whi[PU % R]) : "v"(wa), "n"((PU / NT) * 32 * P + (PU % NT) * 32 + 8 * P));
  }
  template <int H, int M>
  DEVI void xread1() {
    asm volatile("ds_read_b128 %0, %1 offset:%2" : "=v"(xf[H][M]) : "v"(xa[H]), "n"(M * (XCM ? 256 : 2048)));
  }
  static constexpr int xops(int v) {
    const int k = (v + D) % NT;
    if (k >= XU) return 0;
    const int n = MT - k * XPU;
    return n < XPU ? n : XPU;
  }
  static constexpr int ops(int v, bool last) { return (last && v + D >= NU) ? 0 : WO + xops(v); }
  static constexpr int newer(int U, bool last) {
    int n = 0;
    int v0 = U - D;
    if (U % NT == 0) v0 += XU - 1;
    else n += v0 >= 0 ? xops(v0) : xops(v0 + NU);
    for (int v = v0 + 1; v < U; ++v) n += v >= 0 ? ops(v, last) : ops(v + NU, false);
    return n;
  }
  template <int U, int M_>
  DEVI void mf() {
    constexpr int M = ((U % NT) & 1) ? MT - 1 - M_ : M_;
    s16x8 w;
    if constexpr (WT) w = wf[U % R];
    else w = __builtin_shufflevector(wlo[U % R], whi[U % R], 0, 1, 2, 3, 4, 5, 6, 7);
    acc[M][U % NT] = __builtin_amdgcn_mfma_f32_16x16x32_bf16(w, xf[U / NT][M], acc[M][U % NT], 0, 0, 0);
    __builtin_amdgcn_sched_barrier(0);
  }
  DEVI void pin() {
#pragma unroll
    for (int m = 0; m < MT; ++m)
#pragma unroll
      for (int n = 0; n < NT; ++n) asm volatile("" : "+v"(acc[m][n]));
  }
  template <int U, int I>
  DEVI void dsop() {
    constexpr int PU = (U + D) % NU;
    if constexpr (I == 0) wread_lo<PU>();
    else if constexpr (I == 1) wread_hi<PU>();
    else if constexpr (I - 2 < xops(U)) {
      constexpr int k = (U + D) % NT;
      xread1<((U + D - k) % NU) / NT, k * XPU + I - 2>();
    }
  }
  template <int U, bool LAST, bool BAR, int M>
  DEVI void slot() {
    constexpr bool PF = !(LAST && U + D >= NU);
    mf<U, M>();
    if constexpr (M == 0 && PF && U + D == NU) {
      if constexpr (BAR) __builtin_amdgcn_s_barrier();
      so = (so == (unsigned)((NS - 1) * SB)) ? 0u : so + (unsigned)SB;
      wa = wrd + (WS ? ((unsigned)wtab & 63u) << 11 : so);
      wtab >>= 6;
      xa[0] = xrd[0] + so;
      xa[1] = xrd[1] + so;
      __builtin_amdgcn_sched_barrier(0);
    }
    if constexpr (PF) {
      dsop<U, M>();
      if constexpr (M == MT - 1) {
        if constexpr (MT <= 1) dsop<U, 1>();
        if constexpr (MT <= 2) {
          dsop<U, 2>();
          dsop<U, 3>();
        }
      }
      __builtin_amdgcn_sched_barrier(0);
    }
  }
  template <int U, bool LAST, bool BAR, int... M>
  DEVI void unit(std::integer_sequence<int, M...>) {
    static_assert(newer(U, LAST) <= 15, "lgkmcnt range");
    asm volatile("s_waitcnt lgkmcnt(%0)" ::"n"(newer(U, LAST)));
    __builtin_amdgcn_sched_barrier(0);
    (slot<U, LAST, BAR, M>(), ...);
    if constexpr (SQ && (U % NT) < MT) {
      constexpr int m = U % NT;
      accq[m] = __builtin_amdgcn_mfma_f32_16x16x32_bf16(xf[U / NT][m], xf[U / NT][m], accq[m], 0, 0, 0);
      __builtin_amdgcn_sched_barrier(0);
    }
    __builtin_amdgcn_sched_barrier(0);
  }
  template <bool LAST, bool BAR, int... U>
  DEVI void stage(std::integer_sequence<int, U...>) {
    (unit<U, LAST, BAR>(std::make_integer_sequence<int, MT>{}), ...);
  }
  template <int... U>
  DEVI void prow(std::integer_sequence<int, U...>) {
    ((wread_lo<U>(), wread_hi<U>()), ...);
  }
  template <int... M>
  DEVI void prox(std::integer_sequence<int, M...>) {
    (xread1<0, M>(), ...);
  }
  DEVI void setup(unsigned lds0, int mt0, int nt0, int lane, unsigned so0) {
    const int g = lane >> 4, r = lane & 15, q = (lane >> 2) & 3, p = lane & 3;
#pragma unroll
    for (int h = 0; h < 2; ++h)
      xrd[h] = XCM ? lds0 + (unsigned)((4 * h + g) * (C::BM * 16) + (mt0 * 16 + r) * 16)
                   : lds0 + (unsigned)((mt0 * 16 + r) * 128 + (((4 * h + g) ^ ((r >> 1) & 7)) * 16));
    wrd = WT == 2   ? lds0 + (unsigned)(C::XB + nt0 * 1024 + lane * 16)
          : WT == 1 ? lds0 + (unsigned)(C::XB + (g * 8 + (r & 7)) * P + (r >> 3) * 16 + nt0 * 32)
                    : lds0 + (unsigned)(C::XB + (16 * (g >> 1) + 4 * (g & 1) + q) * P + 8 * p + nt0 * 32);
    so = so0;
    xa[0] = xrd[0] + so;
    xa[1] = xrd[1] + so;
    wa = wrd + so;
  }
  DEVI void first() {
    prox(std::make_integer_sequence<int, MT>{});
    prow(std::make_integer_sequence<int, D>{});
    asm volatile("s_waitcnt lgkmcnt(0)");
  }
  template <int N, bool BAR = true>
  DEVI void mid() {
    if constexpr (N > 1) place();
#pragma unroll 1
    for (int s = 0; s < N; ++s) stage<false, BAR>(std::make_integer_sequence<int, NU>{});
  }
  DEVI void last() {
    stage<true, true>(std::make_integer_sequence<int, NU>{});
    pin();
  }
  template <int KS>
  DEVI void run(unsigned lds0, int mt0, int nt0, int lane, unsigned so0) {
    setup(lds0, mt0, nt0, lane, so0);
    __builtin_amdgcn_s_barrier();
    first();
    mid<KS - 1>();
    last();
  }
};

DEVI const char* code_here() {
  unsigned long long pc;
  asm volatile("s_getpc_b64 %0" : "=s"(pc));
  return (const char*)pc;
}
DEVI int okmap(int p) {
  const int ch = p >> 3, e = p & 7, c = (ch - 8) & 3;
  return ch < 8 ? (2 * ch + (e >> 2)) * 36 + 16 + (e & 3) : ((ch - 8) >> 2) * 36 + c * 8 + (c >> 1) * 4 + e;
}

DEVI void pk_st16(void* p, u32x4 v) {
  asm volatile("global_store_dwordx4 %0, %1, off sc1\n\ts_nop 1" ::"v"(p), "v"(v) : "memory");
}

using K1C = GemmCfg<144, 112>;
constexpr int K1_TP = 240;
constexpr int K1_T_OFF = K1C::SB;
constexpr int K1_ST_OFF = 163840 - 128 - 1152;
constexpr int K1_CS_OFF = 4 * K1C::SB;
constexpr int K1_WX = 4 * K1C::SB + 2048;
constexpr unsigned long long K1_WS = 16ull | 32ull << 6 | 48ull << 12 | 56ull << 18 | 63ull << 24 | 16ull << 30 | 32ull << 36 | 48ull << 42;
static_assert(K1C::SB == 16 * 2048 && K1_WX - K1C::XB == 56 * 2048 && K1C::WB == 7 * 2048 && K1_WX + 2 * K1C::WB <= K1_ST_OFF, "K1 w places");
static_assert(K1_T_OFF + 144 * K1_TP + 64 <= 2 * K1C::SB + K1C::XB, "K1 LDS alias");
using K1R = GemmCfg<144, 144>;
constexpr int K1R_T_OFF = K1R::SB;
constexpr int K1R_CS_OFF = K1R_T_OFF + 144 * K1_TP;
constexpr int K1R_ST_OFF = K1R_CS_OFF + 512;
constexpr int K1R_DUMMY = K1R::LDS_BYTES;
constexpr int K1R_PO = K1R_DUMMY + 1024;
constexpr int K1R_CNT = 163840 - 128 + 28 * 4;
#define SPIN_ASM(TEST)                                                                                                   \
  if constexpr (SL <= 1)                                                                                                 \
    asm volatile(".La%=:\n\tds_read_b32 %0, %3\n\ts_waitcnt lgkmcnt(0)\n\tv_readfirstlane_b32 %2, %0\n\t" TEST SAFE_AND \
                 "s_cbranch_scc1 .La%="                                                                                  \
                 : "=&v"(v), NOP_(c), "=&s"(t)                                                                           \
                 : "v"(addr), "s"(k)                                                                                     \
                 : "memory", "scc");                                                                                     \
  else                                                                                                                   \
    asm volatile("s_branch .Lb%=\n.La%=:\n\ts_sleep %5\n.Lb%=:\n\tds_read_b32 %0, %3\n\ts_waitcnt lgkmcnt(0)\n\t"     \
                 "v_readfirstlane_b32 %2, %0\n\t" TEST SAFE_AND "s_cbranch_scc1 .La%="                                  \
                 : "=&v"(v), NOP_(c), "=&s"(t)                                                                           \
                 : "v"(addr), "s"(k), "n"(SL)                                                                            \
                 : "memory", "scc")
template <int SL>
DEVI void spin_ge(unsigned addr, unsigned k) {
  unsigned v, c = 1u << 18, t;
  SPIN_ASM("s_cmp_lt_u32 %2, %4\n\t");
}
template <int SL>
DEVI void spin_b4(unsigned addr, unsigned n) {
  unsigned v, c = 1u << 18, t;
  const unsigned k = (0x80u - n) * 0x01010101u;
  SPIN_ASM("s_add_u32 %2, %2, %4\n\ts_andn2_b32 %2, 0x80808080, %2\n\t");
}
DEVI void msync(unsigned addr) {
  asm volatile("s_waitcnt lgkmcnt(0)" ::: "memory");
  asm volatile("s_mov_b64 exec, 1\n\tds_add_u32 %0, %1\n\ts_mov_b64 exec, -1" ::"v"(addr), "v"(vconst(1u)) : "memory");
  spin_ge<0>(addr, 8u);
}
static_assert(K1R_PO + 13824 + 256 <= 163840 - 128 && K1R_ST_OFF + 144 * 8 <= 3 * K1R::SB, "K1R LDS");

DEVI void touch64(const char* sbase, unsigned voff, unsigned m0val) {
  asm volatile("s_mov_b32 m0, %2\n\ts_nop 0\n\tglobal_load_lds_dword %0, %1" : : "v"(voff), "s"(sbase), "s"(m0val) : "memory");
}

template <int MT, bool G1>
DEVI void k1_gemm_raw(char* smem, int hp, int mt0, int lane MF_ARG) {
  constexpr int NT = 4, nt0 = G1 ? 4 : 0;
  const unsigned lds0 = (unsigned)(size_t)smem;
  const int g = lane >> 4, r = lane & 15;
  unsigned ta[NT];
#pragma unroll
  for (int nt = 0; nt < NT; ++nt) {
    const int j = (nt0 + nt) * 16 + g * 4, seg = j / 40, cj = j - seg * 40 - (hp & 1) * 4;
    const int to = (seg < 3 && cj >= 0 && cj < 36) ? seg * 80 + cj * 2 + (cj >= 20 ? 8 : 0) : (seg < 3 ? 40 : 120);
    ta[nt] = lds0 + (unsigned)(K1R_T_OFF + (mt0 * 16 + r) * K1_TP + to);
    asm volatile("" : "+v"(ta[nt]));
  }
  unsigned sa = lds0 + (unsigned)(K1R_ST_OFF + (mt0 * 16 + r) * 8), ca = lds0 + (unsigned)(K1R_CS_OFF + (nt0 * 16 + g * 4) * 4);
  asm volatile("" : "+v"(sa), "+v"(ca));
  f32x4 acc[MT][NT];
#pragma unroll
  for (int mt = 0; mt < MT; ++mt)
#pragma unroll
    for (int nt = 0; nt < NT; ++nt) acc[mt][nt] = f32x4{0.f, 0.f, 0.f, 0.f};
  {
    GemmMath<K1R, MT, 4, 3, 4, false, 0, G1> gm(acc);
    if constexpr (G1) {
#pragma unroll
      for (int mt = 0; mt < MT; ++mt) gm.accq[mt] = f32x4{0.f, 0.f, 0.f, 0.f};
    }
    MF(14);
    if constexpr (!G1) {
      asm volatile("s_waitcnt vmcnt(0)" ::: "memory");
    }
    MF(15);
    gm.template run<9>(lds0, mt0, nt0, lane, 0u);
    MF(2);
    if constexpr (G1) {
#pragma unroll
      for (int mt = 0; mt < MT; ++mt) {
        const f32x4 q = gm.accq[mt];
        const float d = (r & 2) ? ((r & 1) ? q[3] : q[2]) : ((r & 1) ? q[1] : q[0]);
        const float qq = sum_g(g == (r >> 2) ? d : 0.f);
        float mb, rinv;
        ln_finish(acc[mt][3][0], qq, 1.0f / (float)D_, mb, rinv);
        const f32x2 st = {-mb, rinv};
        if (g == 2) asm volatile("ds_write_b64 %0, %1 offset:%2" ::"v"(sa), "v"(st), "n"(mt * 128) : "memory");
      }
    }
  }
  if (mt0 == 0 && r == 0) {
#pragma unroll
    for (int nt = 0; nt < NT; ++nt) *reinterpret_cast<f32x4*>(smem + K1R_CS_OFF + ((nt0 + nt) * 16 + g * 4) * 4) = acc[0][nt];
  }
  msync(lds0 + (unsigned)K1R_CNT);
  MF(3);
  f32x2 st[MT];
  f32x4 cs[NT];
#pragma unroll
  for (int nt = 0; nt < NT; ++nt) asm volatile("ds_read_b128 %0, %1 offset:%2" : "=v"(cs[nt]) : "v"(ca), "n"(nt * 64) : "memory");
#pragma unroll
  for (int mt = 0; mt < MT; ++mt) asm volatile("ds_read_b64 %0, %1 offset:%2" : "=v"(st[mt]) : "v"(sa), "n"(mt * 128) : "memory");
#pragma unroll
  for (int i = 0; i < MT; ++i) asm volatile("s_waitcnt lgkmcnt(0)" : "+v"(st[i]));
#pragma unroll
  for (int i = 0; i < NT; ++i) asm volatile("" : "+v"(cs[i]));
#pragma unroll
  for (int mt = 0; mt < MT; ++mt) {
    f32x2 M = {st[mt][0], st[mt][0]}, R = {st[mt][1], st[mt][1]};
    asm volatile("" : "+v"(M), "+v"(R));
#pragma unroll
    for (int nt = 0; nt < NT; ++nt) {
      const f32x2 v0 = R * __builtin_elementwise_fma(M, f32x2{cs[nt][0], cs[nt][1]}, f32x2{acc[mt][nt][0], acc[mt][nt][1]});
      const f32x2 v1 = R * __builtin_elementwise_fma(M, f32x2{cs[nt][2], cs[nt][3]}, f32x2{acc[mt][nt][2], acc[mt][nt][3]});
      acc[mt][nt][0] = __uint_as_float(pack2v(v0));
      acc[mt][nt][1] = __uint_as_float(pack2v(v1));
      const f32x2 pk = {acc[mt][nt][0], acc[mt][nt][1]};
      asm volatile("ds_write_b64 %0, %1 offset:%2" ::"v"(ta[nt]), "v"(pk), "n"(mt * 16 * K1_TP) : "memory");
    }
  }
}

template <int N>
DEVI void wait_vm() {
  asm volatile("s_waitcnt vmcnt(%0)" ::"n"(N) : "memory");
}

constexpr int LDS_ALL = 163840;
constexpr int LW_OFF = LDS_ALL - 128;
constexpr int LW_GO = 0, LW_T = 2, LW_O = 4  , LW_X1 = 6, LW_H = 8, LW_X2 = 12, LW_PK = 13, LW_Z = 14, LW_PAIR = 16, LW_AD = 20,
              LW_L = 22, LW_S = 24, LW_ST = 29, LW_HELLO = 30, LW_Q = 31;
DEVI void announce(unsigned addr, unsigned n, char* ws, int kind, int who, bool act) {
  const char* cb = uni_ptr(ws + OFF_CNT + (size_t)kind * 256 * 128);
  const unsigned long long mask = __ballot(act);
  unsigned t, u;
  asm volatile(
      "s_waitcnt vmcnt(0)\n\t"
      "s_mov_b64 exec, 1\n\t"
      "ds_add_rtn_u32 %0, %2, %3\n\t"
      "s_waitcnt lgkmcnt(0)\n\t"
      "v_readfirstlane_b32 %1, %0\n\t"
      "s_cmp_eq_u32 %1, %4\n\t"
      "s_cselect_b64 exec, %5, 0\n\t"
      "global_atomic_add %6, %3, %7 sc1\n\t"
      "s_mov_b64 exec, -1\n\t"
      "s_nop 0"
      : "=&v"(t), "=&s"(u)
      : "v"(addr), "v"(vconst(1u)), "s"(n - 1u), "s"(mask), "v"((unsigned)(who & 255) * 128u), "s"(cb)
      : "memory", "scc");
}
DEVI void cnt_wait(const char* ws, int kind, int who, unsigned n) {
  const char* cp = uni_ptr(ws + OFF_CNT + (size_t)(kind * 256 + who) * 128);
  unsigned v, c = SPIN_MEM, t;
  asm volatile(
      ".La%=:\n\tglobal_load_dword %0, %3, %4 sc1\n\ts_sub_u32 %1, %1, 1\n\ts_waitcnt vmcnt(0)\n\tv_readfirstlane_b32 %2, %0\n\ts_cmp_lg_u32 %2, %5\n\t"
      "s_cselect_b32 %2, %1, 0\n\ts_cmp_lg_u32 %2, 0\n\ts_cbranch_scc1 .La%="
      : "=&v"(v), "+s"(c), "=&s"(t)
      : "v"(vconst(0u)), "s"(cp), "s"(n - 1u)
      : "memory", "scc");
}
DEVI void dma16_sc1(const char* sbase, unsigned voff, unsigned m0val) {
  asm volatile("s_mov_b32 m0, %2\n\ts_nop 0\n\tglobal_load_lds_dwordx4 %0, %1 sc1"
               :
               : "v"(voff), "s"(sbase), "s"(m0val)
               : "memory");
}
template <int N, bool SC1>
DEVI void dma4(const char* sbase, unsigned v0, unsigned v1, unsigned v2, unsigned v3, unsigned m0val) {
#define DMA4(S)                                                                                                               \
  asm volatile("s_mov_b32 m0, %5\n\ts_nop 0\n\tglobal_load_lds_dwordx4 %0, %4" S "\n\t.if %6 > 1\n\tglobal_load_lds_dwordx4 %1, %4 offset:1024" S \
               "\n\t.endif\n\t.if %6 > 2\n\tglobal_load_lds_dwordx4 %2, %4 offset:2048" S                                    \
               "\n\t.endif\n\t.if %6 > 3\n\tglobal_load_lds_dwordx4 %3, %4 offset:3072" S "\n\t.endif"                      \
               :                                                                                                              \
               : "v"(v0), "v"(v1), "v"(v2), "v"(v3), "s"(sbase), "s"(m0val), "n"(N)                                           \
               : "memory")
  if constexpr (SC1) DMA4(" sc1");
  else DMA4("");
}
template <int N>
DEVI void dma_seq(const char* src, unsigned voff, unsigned m0val) {
  if constexpr (N > 0) {
    dma4<(N < 4 ? N : 4), true>(src, voff, voff, voff, voff, m0val);
    dma_seq<N - 4>(src + 4096, voff, m0val + 4096u);
  }
}
DEVI void bar() { __builtin_amdgcn_s_barrier(); }
DEVI void lds_inc(unsigned addr, unsigned by = 1u) {
  asm volatile("s_mov_b64 exec, 1\n\tds_add_u32 %0, %1\n\ts_mov_b64 exec, -1" ::"v"(addr), "v"(vconst(by)) : "memory");
}
DEVI void lds_spin(unsigned addr, unsigned n) {
  spin_ge<1>(addr, n);
}
template <int SL = 1>
DEVI void lds_spin4(unsigned addr, unsigned n) {
  spin_b4<SL>(addr, n);
}
DEVI int lane_id() {
  int l;
  asm volatile("v_mbcnt_lo_u32_b32 %0, -1, 0\n\tv_mbcnt_hi_u32_b32 %0, -1, %0" : "=v"(l));
  __builtin_assume((unsigned)l < 64u);
  return l;
}

template <int I>
DEVI u32x4 tr8(const u32x4 (&v)[8]) {
  constexpr unsigned sel = (I & 1) ? 0x07060302u : 0x05040100u;
  return u32x4{__builtin_amdgcn_perm(v[1][I / 2], v[0][I / 2], sel), __builtin_amdgcn_perm(v[3][I / 2], v[2][I / 2], sel),
               __builtin_amdgcn_perm(v[5][I / 2], v[4][I / 2], sel), __builtin_amdgcn_perm(v[7][I / 2], v[6][I / 2], sel)};
}
template <int PITCH, bool SUM, int... I>
DEVI void tr8_store(const u32x4 (&v)[8], unsigned dst, float (&cs)[8], std::integer_sequence<int, I...>) {
  auto one = [&](auto I_) {
    const u32x4 o = tr8<I_>(v);
    const unsigned d_ = dst;
    asm volatile("ds_write_b128 %0, %1 offset:%2" ::"v"(d_), "v"(o), "n"(I_ * PITCH) : "memory");
    if constexpr (SUM) {
#pragma unroll
      for (int d = 0; d < 4; ++d) {
        const unsigned od = o[d];
        cs[I_] = __builtin_amdgcn_fdot2_f32_bf16(__builtin_bit_cast(bf16x2, od), __builtin_bit_cast(bf16x2, 0x3f803f80u), cs[I_], false);
      }
    }
  };
  (one(std::integral_constant<int, I>{}), ...);
}
template <int NB>
DEVI void lds_out(unsigned src, char* dst, int lane) {
  constexpr int N = (NB + 1023) / 1024;
  u32x4 t[N];
  const unsigned a = src + (unsigned)lane * 16u;
#pragma unroll
  for (int i = 0; i < N; ++i) asm volatile("ds_read_b128 %0, %1 offset:%2" : "=v"(t[i]) : "v"(a), "n"(i * 1024) : "memory");
  const char* d = uni_ptr(dst);
#pragma unroll
  for (int i = 0; i < N; ++i) {
    asm volatile("s_waitcnt lgkmcnt(%1)" : "+v"(t[i]) : "n"(N - 1 - i) : "memory");
    if (i < N - 1 || lane < (NB - (N - 1) * 1024) / 16)
      asm volatile("global_store_dwordx4 %0, %1, %2 sc1\n\ts_nop 1" ::"v"((unsigned)lane * 16u), "v"(t[i]), "s"(d + i * 1024) : "memory");
  }
}
template <int A, int B, class F>
DEVI void static_for(F f) {
  if constexpr (A < B) {
    f(std::integral_constant<int, A>{});
    static_for<A + 1, B>(f);
  }
}
constexpr int WSP_V = 3;
constexpr int RAW_S1 = 6;
constexpr int RAW_S0 = 4;
template <int E>
struct RawX {
  static constexpr int first(int s) {
    return s <= RAW_S0 ? 0 : (s > RAW_S1 ? E : (E * (s - RAW_S0) + RAW_S1 - RAW_S0) / (RAW_S1 + 1 - RAW_S0));
  }
  static constexpr int n(int s) { return first(s + 1) - first(s); }
};
struct RawLd {
  static constexpr int VPS = 10, SB = K1R::SB;
  unsigned xoff[5], woff[5], m0x;
  unsigned long long xact0, wact[5], tag;
  const char *xp, *wp;
  int qb;
  DEVI void q_hook(int st, char* ws, unsigned lds0, int hp) {
    if ((qb >> 1) == st) {
      const int lane = lane_id(), g = lane >> 4;
      const unsigned a = lds0 + (unsigned)((st & 3) * SB + K1R::XB + ((qb & 1) * 32 + 16 * (g >> 1) + 4 * (g & 1) + ((lane >> 2) & 3)) * 288 +
                                           (lane & 3) * 8 + (hp & 1) * 8);
      const unsigned a2 = a + ((lane & 3) >= 1 ? 8u : 0u), a4 = a + ((lane & 3) >= 2 ? 8u : 0u);
      u32x2 qf[7][2];
#pragma unroll
      for (int nt = 0; nt < 7; ++nt)
        asm volatile("ds_read_b64_tr_b16 %0, %2 offset:%3\n\tds_read_b64_tr_b16 %1, %2 offset:%4"
                     : "=&v"(qf[nt][0]), "=&v"(qf[nt][1])
                     : "v"(nt == 2 ? a2 : (nt == 4 ? a4 : a)), "n"(nt * 32 + (nt > 4 ? 16 : (nt > 2 ? 8 : 0))),
                       "n"(2304 + nt * 32 + (nt > 4 ? 16 : (nt > 2 ? 8 : 0)))
                     : "memory");
      const char* d = uni_ptr(ws + OFF_PQ + (size_t)(hp * 18 + qb) * 7168 + 3072);
#pragma unroll
      for (int nt = 0; nt < 7; ++nt) {
        asm volatile("s_waitcnt lgkmcnt(%2)" : "+v"(qf[nt][0]), "+v"(qf[nt][1]) : "n"(12 - 2 * nt) : "memory");
        const u32x4 v = {qf[nt][0][0], qf[nt][0][1], qf[nt][1][0], qf[nt][1][1]};
        asm volatile("global_store_dwordx4 %0, %1, %2 offset:%3 sc1\n\ts_nop 1" ::"v"((unsigned)lane * 16u), "v"(v), "s"(d), "n"(nt * 1024 - 3072) : "memory");
      }
      if (st == 8) lds_wr32(lds0 + (unsigned)(LW_OFF + LW_Q * 4), 1u);
    }
  }
  DEVI void stage_x(unsigned so) {
    asm volatile(
        "s_mov_b32 m0, %6\n\ts_mov_b64 exec, %7\n\tglobal_load_lds_dwordx4 %0, %5\n\ts_mov_b64 exec, -1\n\t"
        "global_load_lds_dwordx4 %1, %5 offset:1024\n\tglobal_load_lds_dwordx4 %2, %5 offset:2048\n\tglobal_load_lds_dwordx4 %3, %5 offset:3072\n\t"
        "s_add_u32 m0, m0, 0x1000\n\ts_mov_b32 exec_hi, 0\n\tglobal_load_lds_dwordx4 %4, %5\n\ts_mov_b32 exec_hi, -1" ::"v"(xoff[0]),
        "v"(xoff[1]), "v"(xoff[2]), "v"(xoff[3]), "v"(xoff[4]), "s"(xp), "s"(m0x + so), "s"(xact0)
        : "memory", "scc");
    xp += 128;
  }
  DEVI void stage_w(unsigned so) {
    asm volatile(
        "s_mov_b32 m0, %6\n\ts_mov_b64 exec, %7\n\tglobal_load_lds_dwordx4 %0, %5\n\ts_mov_b64 exec, %8\n\t"
        "global_load_lds_dwordx4 %1, %5 offset:1024\n\ts_mov_b64 exec, %9\n\tglobal_load_lds_dwordx4 %2, %5 offset:2048\n\t"
        "s_mov_b64 exec, %10\n\tglobal_load_lds_dwordx4 %3, %5 offset:3072\n\ts_add_u32 m0, m0, 0x1000\n\ts_mov_b64 exec, %11\n\t"
        "global_load_lds_dwordx4 %4, %5\n\ts_mov_b64 exec, -1" ::"v"(woff[0]),
        "v"(woff[1]), "v"(woff[2]), "v"(woff[3]), "v"(woff[4]), "s"(wp), "s"(m0x + so + (unsigned)K1R::XB), "s"(wact[0]), "s"(wact[1]),
        "s"(wact[2]), "s"(wact[3]), "s"(wact[4])
        : "memory", "scc");
    wp += 64 * NQKV * 2;
  }
  DEVI void stage(unsigned so) {
    stage_x(so);
    stage_w(so);
  }
  template <class F>
  DEVI void front(const u16* x, const u16* wqkv, const u16* wo, char* ws, int bid, unsigned lds0, int wl, int lane, int rt, int hp,
                  int& pj, F jobs, const char*& pg MF_ARG) {
    xp = uni_ptr((const char*)x - 3072);
    wp = uni_ptr((const char*)wqkv + ((hp * 72) & ~15) - 3072);
    m0x = lds0 + (unsigned)(wl * 288 * 16);
    xact0 = wl ? ~0ull : ~0xffull;
    const int row0 = wl * 36 + (lane >> 3);
#pragma unroll
    for (int i = 0; i < 5; ++i) {
      const int row = row0 + i * 8;
      int gr = rt * 128 - 16 + row;
      if (gr < 0) gr = 0;
      xoff[i] = (unsigned)(gr * (D_ * 2) + (3 - (i & 3)) * 1024) + (unsigned)(((lane ^ (row >> 1)) & 7) * 16);
    }
    stage_x(0u);
    u32x2 tg;
    asm volatile("global_load_dwordx2 %0, %1, %2 sc1" : "=v"(tg) : "v"(0u), "s"(uni_ptr(ws + OFF_TAG)) : "memory");
    asm volatile("" : "+v"(lane));
#pragma unroll
    for (int i = 0; i < 5; ++i) {
      const unsigned c = (unsigned)(i * 64 + (lane & 63));
      const unsigned wr = (c * 3641u) >> 16, wc = c - wr * 18u, seg = (wc * 13u) >> 6;
      wact[i] = __ballot(wc < 15u) & (i < 4 ? ~0ull : 0xffffffffull);
      woff[i] = (unsigned)(wl * 16 + swap23((int)wr)) * (unsigned)(NQKV * 2) + seg * (unsigned)(D_ * 2 - 80) + wc * 16u + (unsigned)((3 - (i & 3)) * 1024);
    }
    stage_w(0u);
    stage((unsigned)SB);
    jobs();
    MF(10);
    asm volatile("s_waitcnt vmcnt(%1)" : "+v"(tg) : "n"(VPS) : "memory");
    tag = (((unsigned long long)(unsigned)__builtin_amdgcn_readfirstlane(tg[1]) << 32) | (unsigned)__builtin_amdgcn_readfirstlane(tg[0])) +
          0x9e3779b97f4a7c15ull;
    MF(1);
    __builtin_amdgcn_s_barrier();
    MF(15);
    if (wl == 0 && lane == 0)
      asm volatile("global_store_dwordx2 %0, %1, off sc1\n\ts_nop 1" ::"v"(ws + OFF_HELLO + bid * 8), "v"(tag) : "memory");
    stage((unsigned)(2 * SB));
    qb = wl == 3 ? rt : ((wl == 2 && rt < 2) ? 16 + rt : 99);
    q_hook(0, ws, lds0, hp);
    const int nb = pj;
    const bool po = pj >= 0 && wl < 2;
    u32x4 v[8];
    unsigned da = 0u;
    if (po) {
      const int l54 = lane < 54 ? lane : 53, gl = l54 / 9, c = l54 - gl * 9, kl = wl * 6 + gl, t = nb / 6, kg = (nb - t * 6) * 12 + kl;
      da = lds0 + (unsigned)(K1R_PO + (kl >> 2) * 4608 + (16 * (kl & 1) + ((kl >> 1) & 1)) * 144 + c * 16);
      const unsigned sa[2] = {(unsigned)(okmap(kg * 8) * D_ + t * 72 + c * 8) * 2u, (unsigned)(okmap(kg * 8 + 4) * D_ + t * 72 + c * 8) * 2u};
      const char* ps = uni_ptr((const char*)wo);
#pragma unroll
      for (int j = 0; j < 8; ++j)
        asm volatile("global_load_dwordx4 %0, %1, %2 offset:%3" : "=v"(v[j]) : "v"(sa[j / 4]), "s"(ps), "n"((j % 4) * D_ * 2) : "memory");
    } else {
#pragma unroll
      for (int j = 0; j < 8; ++j) asm volatile("" : "=v"(v[j]));
    }
    unsigned slot = (unsigned)(3 * SB), pd = 0u;
    const unsigned gk = (unsigned)(bid >> 3) & 15u;
    unsigned gp = gk * (unsigned)SZ_X;
    gp = gk > 5u ? (unsigned)(OFF_Z2 - GAP) + (gk - 5u) * (unsigned)SZ_Z : gp;
    gp = gk > 9u ? (unsigned)(OFF_PO - GAP) : gp;
    gp = gk > 10u ? (unsigned)(OFF_PI - GAP) : gp;
    gp = gk > 11u ? (unsigned)(OFF_PW - GAP) : gp;
    gp = gk > 12u ? (unsigned)(OFF_CSP - GAP) : gp;
#pragma unroll 1
    for (int S = 0; S < RAW_S0; ++S) {
      if (wl == 2 && S == 1) asm volatile("s_load_dword %0, %1, 0\n\ts_load_dword %0, %2, 0" : "+s"(pd) : "s"(ws + gp), "s"(pg));
      asm volatile("" : "+s"(pd));
      wait_vm<VPS>();
      __builtin_amdgcn_s_barrier();
      if (S == 0) MF(16);
      if (S == 1) MF(17);
      if (S == 2) MF(18);
      if (S == 3) MF(19);
      stage(slot);
      slot = (slot == (unsigned)(3 * SB)) ? 0u : slot + (unsigned)SB;
      q_hook(S + 1, ws, lds0, hp);
      if (po && S == 1) {
        float cs[8];
        asm volatile("s_nop 0 ; LOOPWAIT" : "+v"(v[0]), "+v"(v[1]), "+v"(v[2]), "+v"(v[3]), "+v"(v[4]), "+v"(v[5]), "+v"(v[6]), "+v"(v[7]));
        tr8_store<288, false>(v, da, cs, std::make_integer_sequence<int, 8>{});
        asm volatile("s_waitcnt lgkmcnt(0)" ::: "memory");
      }
      if (po && S == 2) lds_out<6912>(lds0 + (unsigned)(K1R_PO + wl * 6912), ws + OFF_PO + (size_t)nb * 13824 + wl * 6912, lane);
    }
    asm volatile("s_waitcnt lgkmcnt(0)" : "+s"(pd));
  }
};
template <bool JOB>
DEVI void k1_raw_loader(RawLd& ld, const u16* __restrict__ w_in, const u16* __restrict__ w_out, char* __restrict__ ws,
                        unsigned long long tag, int bid, unsigned lds0, int wl, int lane, int rt, int hp, int ct, int job, int pj MF_ARG) {
  constexpr int VPS = RawLd::VPS, SB = RawLd::SB, E = JOB ? 16 : 0;
  const bool isi = job < 144;
  const int kb = isi ? job >> 3 : job - 144;
  const int pitch = isi ? F_ * 2 : D_ * 2;
  const int l36 = lane < 36 ? lane : 35;
  unsigned sa = (unsigned)l36 * 16u, da;
  if (isi) da = lds0 + (unsigned)((l36 >= 18 ? 9216 - 288 : 0) + wl * 8 * 288 + l36 * 16);
  else da = lds0 + (unsigned)((l36 / 9) * (4608 - 144) + (16 * (wl & 1) + (wl >> 1)) * 144 + l36 * 16);
  asm volatile("" : "+v"(sa), "+v"(da));
  u32x4 v[2][8];
  const char* ps = uni_ptr((const char*)(isi ? w_in + (job & 7) * 576 : w_out) + (size_t)(kb * 32 + wl * 8) * pitch);
  auto extra = [&](auto J_) {
    constexpr int j = J_;
    const char* q = ps + (j / 2) * pitch;
    u32x4 t;
    asm volatile("global_load_dwordx4 %0, %1, %2 offset:%3" : "=v"(t) : "v"(sa), "s"(q), "n"((j % 2) * 576) : "memory");
    v[j % 2][j / 2] = t;
  };
  auto step = [&](auto S_) {
    constexpr int S = S_;
    wait_vm<RawX<E>::n(S - 2) + (S <= 6 ? VPS : 0) + RawX<E>::n(S - 1)>();
    __builtin_amdgcn_s_barrier();
    MF(16 + S);
    if constexpr (S == RAW_S0) {
      static_assert(RAW_S0 >= 4, "wo blocks");
      if (pj >= 0 && wl == 0 && lane == 0)
        asm volatile("global_store_dwordx2 %0, %1, off sc1\n\ts_nop 1" ::"v"(ws + OFF_POT + (pj / 6) * 128 + (pj % 6) * 8), "v"(tag) : "memory");
    }
    if constexpr (S <= 5) ld.stage((unsigned)(((S + 3) % 4) * SB));
    ld.q_hook(S + 1, ws, lds0, hp);
    static_for<RawX<E>::first(S), RawX<E>::first(S + 1)>(extra);
  };
  const char* hp_ = ws + OFF_HELLO + lane * 32;
  u32x4 a, b;
  if (wl == 1) asm volatile("global_load_dwordx4 %0, %2, off sc1\n\tglobal_load_dwordx4 %1, %2, off offset:16 sc1" : "=&v"(a), "=&v"(b) : "v"(hp_) : "memory");
  else asm volatile("" : "=v"(a), "=v"(b));
  static_for<RAW_S0, 8>(step);
  MF(2);
  if (wl == 1) {
    asm volatile("" : "+v"(a), "+v"(b));
    const unsigned t0 = (unsigned)tag, t1 = (unsigned)(tag >> 32);
#pragma unroll 1
    for (unsigned spin = 0; spin < SPIN_MEM; ++spin) {
      const unsigned bad = ((a[0] ^ t0) | (a[2] ^ t0) | (b[0] ^ t0) | (b[2] ^ t0)) | ((a[1] ^ t1) | (a[3] ^ t1) | (b[1] ^ t1) | (b[3] ^ t1));
      if (!__any(bad != 0u)) break;
      __builtin_amdgcn_s_sleep(4);
      asm volatile("global_load_dwordx4 %0, %2, off sc1\n\tglobal_load_dwordx4 %1, %2, off offset:16 sc1\n\ts_waitcnt vmcnt(0)"
                   : "=&v"(a), "=&v"(b)
                   : "v"(hp_)
                   : "memory");
    }
    lds_wr32(lds0 + (unsigned)(LW_OFF + LW_HELLO * 4), 1u);
    if (bid == 0 && lane == 0) asm volatile("global_store_dwordx2 %0, %1, off sc1\n\ts_nop 1" ::"v"(ws + OFF_TAG), "v"(tag) : "memory");
  }
  MF(11);
  if (rt < 2) lds_spin(lds0 + (unsigned)(LW_OFF + LW_Q * 4), 1u);
  lds_spin(lds0 + (unsigned)K1R_CNT, 8u);
  if constexpr (JOB) {
#pragma unroll
    for (int b = 0; b < 2; ++b)
      asm volatile("s_waitcnt vmcnt(0)"
                   : "+v"(v[b][0]), "+v"(v[b][1]), "+v"(v[b][2]), "+v"(v[b][3]), "+v"(v[b][4]), "+v"(v[b][5]), "+v"(v[b][6]), "+v"(v[b][7])
                   :
                   : "memory");
  }
  MF(12);
  const char* tp = ws + OFF_POT + ct * 128 + (lane < 6 ? lane : 5) * 8;
  u32x2 f;
  asm volatile("global_load_dwordx2 %0, %1, off sc1" : "=v"(f) : "v"(tp) : "memory");
  if constexpr (JOB) {
    static_assert(SB == 4 * 9216, "staging");
#pragma unroll
    for (int b = 0; b < 2; ++b) {
      float cs[8] = {0.f, 0.f, 0.f, 0.f, 0.f, 0.f, 0.f, 0.f};
      tr8_store<288, true>(v[b], da + (unsigned)(b * 18432), cs, std::make_integer_sequence<int, 8>{});
      const f32x4 c0 = {cs[0], cs[1], cs[2], cs[3]}, c1 = {cs[4], cs[5], cs[6], cs[7]};
      const unsigned ca = lds0 + (unsigned)(3 * SB + wl * 2304) + sa * 2u;
      asm volatile("ds_write_b128 %0, %1 offset:%2" ::"v"(ca), "v"(c0), "n"(b * 1152) : "memory");
      asm volatile("ds_write_b128 %0, %1 offset:%2" ::"v"(ca), "v"(c1), "n"(b * 1152 + 16) : "memory");
    }
    lds_inc(lds0 + (unsigned)(LW_OFF + LW_ST * 4));
    lds_spin(lds0 + (unsigned)(LW_OFF + LW_ST * 4), 4u);
    if (isi) {
      const int j = (job & 7) * 4 + wl, ti = j < 16 ? 2 * j + 1 : 2 * (j - 16);
      lds_out<9216>(lds0 + (unsigned)(wl * 9216), ws + OFF_PI + (size_t)(ti * 18 + kb) * 9216, lane);
      f32x4 c[4];
      const unsigned ca = lds0 + (unsigned)(3 * SB + wl * 576) + sa;
#pragma unroll
      for (int i = 0; i < 4; ++i) asm volatile("ds_read_b128 %0, %1 offset:%2" : "=v"(c[i]) : "v"(ca), "n"(i * 2304) : "memory");
      asm volatile("s_waitcnt lgkmcnt(0)" : "+v"(c[0]), "+v"(c[1]), "+v"(c[2]), "+v"(c[3]));
      const f32x4 t = (c[0] + c[1]) + (c[2] + c[3]);
      if (lane < 36) pk_st16(ws + OFF_CSP + ((size_t)kb * F_ + ti * 144) * 4 + sa, __builtin_bit_cast(u32x4, t));
    } else {
      lds_out<4608>(lds0 + (unsigned)(wl * 9216), ws + OFF_PW + (size_t)(wl * 144 + kb) * 4608, lane);
      lds_out<4608>(lds0 + (unsigned)(wl * 9216 + 4608), ws + OFF_PW + (size_t)(wl * 144 + 72 + kb) * 4608, lane);
    }
  }
  MF(8);
  announce(lds0 + (unsigned)(LW_OFF + LW_PK * 4), 4u, ws, CN_PK, isi ? 8 + (job & 3) * 4 + lane : 24 + lane, JOB && lane < (isi ? 4 : 8));
  MF(9);
  if (wl == 0) {
    asm volatile("" : "+v"(f));
#pragma unroll 1
    for (unsigned spin = 0; spin < SPIN_MEM; ++spin) {
      if (!__any(f[0] != (unsigned)tag || f[1] != (unsigned)(tag >> 32))) break;
      __builtin_amdgcn_s_sleep(2);
      asm volatile("global_load_dwordx2 %0, %1, off sc1\n\ts_waitcnt vmcnt(0)" : "=&v"(f) : "v"(tp) : "memory");
    }
  }
}

template <int MT, int NT>
DEVI void k1_gemm(char* smem, const char* ws, int rt, int hp, int mt0, int nt0,
                  bool late, int lane MK_ARG) {
  const unsigned lds0 = (unsigned)(size_t)smem;
  const int g = lane >> 4;
  f32x4 cs[NT];
  unsigned ta[NT];
  unsigned cad[NT];
#pragma unroll
  for (int nt = 0; nt < NT; ++nt) {
    int c = (nt0 + nt) * 16 + g * 4;
    if (c > 104) c = 104;
    const int part = c / 36, ci = c - part * 36;
    cad[nt] = lds0 + (unsigned)(K1_CS_OFF + (part * 40 + (hp & 1) * 4 + ci) * 4);
    asm volatile("" : "+v"(cad[nt]));
    const int to = (nt0 + nt) * 16 + g * 4 > 104 ? 120 : part * 80 + ci * 2 + (ci >= 20 ? 8 : 0);
    ta[nt] = lds0 + (unsigned)(K1_T_OFF + (mt0 * 16 + (lane & 15)) * K1_TP + to);
    asm volatile("" : "+v"(ta[nt]));
  }
  unsigned sa = lds0 + (unsigned)(K1_ST_OFF + (mt0 * 16 + (lane & 15)) * 8);
  asm volatile("" : "+v"(sa));
  f32x4 acc[MT][NT];
#pragma unroll
  for (int mt = 0; mt < MT; ++mt)
#pragma unroll
    for (int nt = 0; nt < NT; ++nt) acc[mt][nt] = f32x4{0.f, 0.f, 0.f, 0.f};
  f32x2 st[MT];
  if (late) bar();
  if constexpr (NT == 7) cnt_wait(ws, CN_X2, rt * 16 + hp, rt ? 24u : 16u);
  bar();
  MK(1);
  {
    GemmMath<K1C, MT, NT, (NT == 7 ? 5 : 3), (NT == 3 ? 6 : NT), true, 2, false, K1_WS> gm(acc);
    gm.setup(lds0, mt0, nt0, lane, 0u);
    bar();
#pragma unroll
    for (int nt = 0; nt < NT; ++nt) asm volatile("ds_read_b128 %0, %1" : "=v"(cs[nt]) : "v"(cad[nt]));
    gm.first();
    gm.template mid<8>();
#pragma unroll
    for (int mt = 0; mt < MT; ++mt) asm volatile("ds_read_b64 %0, %1 offset:%2" : "=v"(st[mt]) : "v"(sa), "n"(mt * 128));
    asm volatile("s_waitcnt vmcnt(0)" ::: "memory");
    gm.last();
  }
#pragma unroll
  for (int nt = 0; nt < NT; ++nt) asm volatile("" : "+v"(cs[nt]));
#pragma unroll
  for (int mt = 0; mt < MT; ++mt) asm volatile("" : "+v"(st[mt]));
  MK(2);
#pragma unroll
  for (int mt = 0; mt < MT; ++mt) {
    f32x2 M = {st[mt][0], st[mt][0]}, R = {st[mt][1], st[mt][1]};
    asm volatile("" : "+v"(M), "+v"(R));
#pragma unroll
    for (int nt = 0; nt < NT; ++nt) {
      const f32x2 v0 = R * __builtin_elementwise_fma(M, f32x2{cs[nt][0], cs[nt][1]}, f32x2{acc[mt][nt][0], acc[mt][nt][1]});
      const f32x2 v1 = R * __builtin_elementwise_fma(M, f32x2{cs[nt][2], cs[nt][3]}, f32x2{acc[mt][nt][2], acc[mt][nt][3]});
      acc[mt][nt][0] = __uint_as_float(pack2v(v0));
      acc[mt][nt][1] = __uint_as_float(pack2v(v1));
      const f32x2 pk = {acc[mt][nt][0], acc[mt][nt][1]};
      asm volatile("ds_write_b64 %0, %1 offset:%2" ::"v"(ta[nt]), "v"(pk), "n"(mt * 16 * K1_TP) : "memory");
    }
  }
}

DEVI float vmax(float a, float b) {
  float r;
  asm("v_max_f32 %0, %1, %2" : "=v"(r) : "v"(a), "v"(b));
  return r;
}
struct Attn {
  unsigned ka, kb, va, vb, m0, m1, m2, pm[2], oa, pa;
  f32x4 mk;
  unsigned long long sel[4];
  const char* ob;
  DEVI void setup(unsigned T, char* o, int r0, int hp, int wave, int lane) {
    const int g = lane >> 4, r = lane & 15, blk = wave >> 1, hd = wave & 1;
    const unsigned co = (unsigned)(g < 2 ? hd * 48 + g * 16 : 32);
    ka = T + (unsigned)((blk * 32 + 2 * r) * K1_TP) + co;
    kb = T + (unsigned)((blk * 32 + 2 * (r & 7)) * K1_TP) + co;
    const int vr = 4 * g + ((lane >> 2) & 3);
    va = T + (unsigned)((blk * 32 + 2 * vr) * K1_TP + (lane & 3) * 8);
    vb = T + (unsigned)((blk * 32 + 2 * (vr & 7)) * K1_TP + (lane & 3) * 8);
    m0 = (g < 2 || (g == 2 && hd == 0)) ? ~0u : 0u;
    m1 = (g < 2 || (g == 2 && hd == 1)) ? ~0u : 0u;
    m2 = g < 2 ? ~0u : 0u;
    pm[0] = pm[1] = 0u;
    const float ci = __uint_as_float(vconst(0xff800000u));
    const unsigned ch = vconst(0xffffu);
#pragma unroll
    for (int e = 0; e < 4; ++e) {
      const int w1 = r + 8 - 4 * g - e;
      mk[e] = (w1 & 15) <= 7 ? 0.f : ci;
      sel[e] = __ballot(w1 >= 8);
      if (w1 >= 8) pm[e >> 1] |= ch << (16 * (e & 1));
    }
    oa = (unsigned)((8 + hp * 4 + hd * 2 + (g >> 1)) * 1024 + ((blk & 1) * 32 + 2 * r + (g & 1)) * 16);
    pa = ((oa & 1023u) >> 2) + (unsigned)((hp * 2 + hd) * 256);
    ob = uni_ptr(o + (size_t)((r0 >> 6) + (wave >> 2)) * (72 * 1024));
    asm volatile("" : "+v"(ka), "+v"(kb), "+v"(va), "+v"(vb), "+v"(m0), "+v"(m1), "+v"(m2), "+v"(pm[0]), "+v"(pm[1]), "+v"(oa));
    asm volatile("" : "+v"(mk), "+v"(pa), "+s"(ob), "+s"(sel[0]), "+s"(sel[1]), "+s"(sel[2]), "+s"(sel[3]));
  }
  DEVI void run(int r0, int hp, int wave, int lane) {
    const int hd = wave & 1;
    u32x4 q[2], k0[2], k1[2];
    u32x2 v[2][2][2];
#pragma unroll
    for (int p = 0; p < 2; ++p) {
      asm volatile("ds_read_b128 %0, %1 offset:%2" : "=v"(q[p]) : "v"(ka), "n"((16 + p) * K1_TP) : "memory");
      asm volatile("ds_read_b128 %0, %1 offset:%2" : "=v"(k0[p]) : "v"(ka), "n"(p * K1_TP + 80) : "memory");
      asm volatile("ds_read_b128 %0, %1 offset:%2" : "=v"(k1[p]) : "v"(kb), "n"((32 + p) * K1_TP + 80) : "memory");
    }
    const unsigned vh = (unsigned)hd * 48u;
#pragma unroll
    for (int p = 0; p < 2; ++p) {
      asm volatile("ds_read_b64_tr_b16 %0, %1 offset:%2" : "=v"(v[p][0][0]) : "v"(va + vh), "n"(p * K1_TP + 160) : "memory");
      asm volatile("ds_read_b64_tr_b16 %0, %1 offset:%2" : "=v"(v[p][0][1]) : "v"(vb + vh), "n"((32 + p) * K1_TP + 160) : "memory");
      asm volatile("ds_read_b64_tr_b16 %0, %1 offset:%2" : "=v"(v[p][1][0]) : "v"(va), "n"(p * K1_TP + 192) : "memory");
      asm volatile("ds_read_b64_tr_b16 %0, %1 offset:%2" : "=v"(v[p][1][1]) : "v"(vb), "n"((32 + p) * K1_TP + 192) : "memory");
    }
    const u32x4 one4 = {vconst(0x3f803f80u), vconst(0x3f803f80u), vconst(0x3f803f80u), vconst(0x3f803f80u)};
    const s16x8 ones = __builtin_bit_cast(s16x8, one4);
    const float c = 0.23570226039551584f * 1.4426950408889634f;
    const f32x4 z = {0.f, 0.f, 0.f, 0.f};
    f32x4 sc[2][2];
#pragma unroll
    for (int p = 0; p < 2; ++p) {
      asm volatile("s_waitcnt lgkmcnt(%3)" : "+v"(q[p]), "+v"(k0[p]), "+v"(k1[p]) : "n"(11 - 3 * p));
      const s16x8 qf = __builtin_bit_cast(s16x8, q[p] & u32x4{m0, m1, m2, m2});
      sc[p][0] = __builtin_amdgcn_mfma_f32_16x16x32_bf16(__builtin_bit_cast(s16x8, k0[p]), qf, mk, 0, 0, 0);
      sc[p][1] = __builtin_amdgcn_mfma_f32_16x16x32_bf16(__builtin_bit_cast(s16x8, k1[p]), qf, mk, 0, 0, 0);
      __builtin_amdgcn_sched_barrier(0);
    }
    if (__builtin_expect(r0 == 0 && wave < 2, 0)) {
#pragma unroll
      for (int p = 0; p < 2; ++p)
#pragma unroll
        for (int e = 0; e < 4; ++e)
          if (lane < 32) sc[p][0][e] = mk[e] - 4.242640687e9f;
    }
    float lg[2][4], mx[2];
#pragma unroll
    for (int p = 0; p < 2; ++p) {
#pragma unroll
      for (int e = 0; e < 4; ++e) lg[p][e] = __builtin_amdgcn_inverse_ballot_w64(sel[e]) ? sc[p][1][e] : sc[p][0][e];
      asm("v_max3_f32 %0, %1, %2, %3\n\tv_max_f32 %0, %0, %4" : "=&v"(mx[p]) : "v"(lg[p][0]), "v"(lg[p][1]), "v"(lg[p][2]), "v"(lg[p][3]));
    }
    {
      const auto a = __builtin_amdgcn_permlane32_swap(__float_as_uint(mx[0]), __float_as_uint(mx[1]), false, false);
      const unsigned b = __float_as_uint(vmax(__uint_as_float(a[0]), __uint_as_float(a[1])));
      const auto d = __builtin_amdgcn_permlane16_swap(b, b, false, false);
      const unsigned f = __float_as_uint(vmax(__uint_as_float(d[0]), __uint_as_float(d[1])) * -c);
      const auto h = __builtin_amdgcn_permlane32_swap(f, f, false, false);
      mx[0] = __uint_as_float(h[0]);
      mx[1] = __uint_as_float(h[1]);
    }
#pragma unroll
    for (int p = 0; p < 2; ++p)
#pragma unroll
      for (int e = 0; e < 4; ++e) lg[p][e] = __builtin_amdgcn_exp2f(__builtin_fmaf(lg[p][e], c, mx[p]));
    asm volatile("s_waitcnt lgkmcnt(0)"
                 : "+v"(v[0][0][0]), "+v"(v[0][0][1]), "+v"(v[0][1][0]), "+v"(v[0][1][1]), "+v"(v[1][0][0]), "+v"(v[1][0][1]),
                   "+v"(v[1][1][0]), "+v"(v[1][1][1]));
    __builtin_amdgcn_sched_barrier(0);
    f32x4 su[2], o0[2], o1[2];
    s16x8 pp[2];
#pragma unroll
    for (int p = 0; p < 2; ++p) {
      const unsigned p0 = pack2(lg[p][0], lg[p][1]), p1 = pack2(lg[p][2], lg[p][3]);
      pp[p] = __builtin_bit_cast(s16x8, u32x4{p0 & ~pm[0], p1 & ~pm[1], p0 & pm[0], p1 & pm[1]});
    }
    __builtin_amdgcn_sched_barrier(0);
#pragma unroll
    for (int p = 0; p < 2; ++p) {
      const s16x8 pf = pp[p];
      const u32x4 v0 = {v[p][0][0][0], v[p][0][0][1], v[p][0][1][0], v[p][0][1][1]};
      const u32x4 v1 = {v[p][1][0][0], v[p][1][0][1], v[p][1][1][0], v[p][1][1][1]};
      su[p] = __builtin_amdgcn_mfma_f32_16x16x32_bf16(ones, pf, z, 0, 0, 0);
      o0[p] = __builtin_amdgcn_mfma_f32_16x16x32_bf16(__builtin_bit_cast(s16x8, v0), pf, z, 0, 0, 0);
      o1[p] = __builtin_amdgcn_mfma_f32_16x16x32_bf16(__builtin_bit_cast(s16x8, v1), pf, z, 0, 0, 0);
    }
    __builtin_amdgcn_sched_barrier(0);
    u32x2 y[2], yl;
    unsigned hm = (unsigned)hd - 1u;
    asm volatile("" : "+s"(hm));
#pragma unroll
    for (int p = 0; p < 2; ++p) {
      const float inv = __builtin_amdgcn_rcpf(su[p][0]);
      y[p] = u32x2{pack2(o0[p][0] * inv, o0[p][1] * inv), pack2(o0[p][2] * inv, o0[p][3] * inv)};
      const unsigned la = pack2(o1[p][0] * inv, o1[p][1] * inv), lb = pack2(o1[p][2] * inv, o1[p][3] * inv);
      yl[p] = (la & hm) | (lb & ~hm);
    }
    const auto s0 = __builtin_amdgcn_permlane16_swap(y[0][0], y[1][0], false, false);
    const auto s1 = __builtin_amdgcn_permlane16_swap(y[0][1], y[1][1], false, false);
    const u32x4 w = {s0[0], s1[0], s0[1], s1[1]};
    asm volatile("global_store_dwordx4 %0, %1, %4 sc1\n\ts_mov_b64 exec, 0xffff\n\tglobal_store_dwordx2 %2, %3, %4 sc1\n\ts_mov_b64 exec, -1\n\ts_nop 0" ::"v"(oa),
                 "v"(w), "v"(pa), "v"(yl), "s"(ob)
                 : "memory");
  }
};

#define NL_MAIN 662
DEVI void math_start(unsigned lds0, char* ws, int bid, int wave, int lane) {
  static_assert(2 * SZ_ZP == (size_t)256 * 2048 && NCNT <= 16 && OFF_CNT + NCNT * 32768 < 0xffffffffu, "mark split");
  const unsigned m1 = vconst(~0u), l = (unsigned)lane;
  const unsigned hi = wave < 2 ? 128u : 32768u;
  unsigned b0 = (unsigned)OFF_ZP + (unsigned)(bid * 2 + wave) * 1024u, b1 = (unsigned)OFF_CNT + (unsigned)((wave & 1) * 2048 + bid) * 128u;
  asm volatile("" : "+s"(b0), "+s"(b1));
  const unsigned base = wave < 2 ? b0 : b1;
  const unsigned long long act = wave < 3 ? ~0ull : (wave == 3 ? (1ull << ((NCNT - 8) * 8)) - 1ull : 0ull);
  asm volatile("s_mov_b64 exec, %3\n\tglobal_store_dwordx4 %0, %1, %2 sc1\n\ts_mov_b64 exec, -1\n\ts_nop 0" ::"v"(base + (l >> 3) * hi + (l & 7u) * 16u),
               "v"(u32x4{m1, m1, m1, m1}), "s"(ws), "s"(act)
               : "memory");
  unsigned a4 = (l < 32u ? (l >> 3) * (unsigned)K1R::SB : (unsigned)LW_OFF) + (l & 7u) * 16u, a0 = (unsigned)(wave * K1R::SB + K1R::XB + 240) + l * 288u;
  asm volatile("" : "+v"(a4), "+v"(a0));
  const unsigned a = wave < 4 ? a0 : a4;
  const unsigned v = (wave < 4 || l < 32u) ? 0x3f803f80u : 0u;
  asm volatile("s_mov_b64 exec, %2\n\tds_write_b128 %0, %1\n\ts_mov_b64 exec, -1\n\ts_waitcnt lgkmcnt(0)" ::"v"(lds0 + a), "v"(u32x4{v, v, v, v}),
               "s"(wave < 4 ? ~0ull : (wave == 4 ? 0xffffffffffull : 0ull))
               : "memory");
}

template <int NST, int NS, int NSW, int WOFF, int XW, int X0, int S0, unsigned BAR>
struct ResX {
  static constexpr int WS = 4 * 4608, R = 10, V1 = 5 * NS, NV = V1 + 10 * (NST - NS), NG = (NSW + 2) / 3;
  s16x8 xf[XW][2];
  s16x8 wf[R];
  unsigned wrd[2][NG], xo, ea, fa, fv, tag;
  const char* xp[3];
  f32x4 (&acc)[2][5];
  DEVI ResX(f32x4 (&a)[2][5]) : acc(a) {}
  static constexpr int st(int V) { return V < V1 ? V / 5 : NS + (V - V1) / 10; }
  static constexpr int hf(int V) { return V < V1 ? 0 : ((V - V1) / 5) & 1; }
  static constexpr bool first(int V) { return V < V1 ? V % 5 == 0 : (V - V1) % 10 == 0; }
  static constexpr int wreq(int V) {
    int n = V + 5;
    if (V >= V1 - 4) n += V < V1 ? V - (V1 - 5) : 4;
    return n < NV ? n : NV;
  }
  static constexpr int XU = V1 + 1, XF = V1 + 11, XR = V1 + 15, XC = V1 + 21;
  static constexpr int oth(int V) { return NS && ((V >= XU && V <= XF && (V - XU) % 2 == 0) || V == XR || (V > XC && V <= XC + 5)) ? 1 : 0; }
  static constexpr int newer(int V) {
    const int F = V + 1 < NV ? V + 1 : V;
    int v = V - 1, n = 0;
    for (; v >= 0 && wreq(v - 1) > F; --v) n += wreq(v) - wreq(v - 1) + oth(v);
    return n + wreq(v) - 1 - F + (v >= 0 ? oth(v) : 0);
  }
  static constexpr int xreq(int T) {
    int n = X0 + 2 * (T + 1);
    if (n > T + XW) n = T + XW;
    if (NS && T <= 11 && n > 18) n = 18;
    if (T < 0) n = X0;
    return n < NST ? n : NST;
  }
  static_assert(NS == 0 || (xreq(NS - 2) == NST && XC + 5 < NV), "the last steps only compute and swap");
  template <int T, int M>
  DEVI void xld() {
    asm volatile("global_load_dwordx4 %0, %1, %2 sc1" : "=v"(xf[T % XW][M]) : "v"(xo), "s"(xp[M == 0 ? 0 : (T < NS ? 1 : 2)]));
    if constexpr (M == 1 && T + 1 < NST) asm volatile("v_add_u32 %0, 0x2000, %0" : "+v"(xo));
  }
  template <int V>
  DEVI void rd() {
    constexpr int T = st(V), sl = (T / 2 + S0) % NSW;
    asm volatile("ds_read_b128 %0, %1 offset:%2" : "=v"(wf[V % R]) : "v"(wrd[hf(V)][sl / 3]), "n"((sl % 3) * WS + (T & 1) * 9216 + (V % 5) * 32));
  }
  template <int V, int M, int H>
  DEVI void mf() {
    acc[M][V % 5] = __builtin_amdgcn_mfma_f32_16x16x32_bf16(wf[V % R], xf[st(V) % XW][H], acc[M][V % 5], 0, 0, 0);
    __builtin_amdgcn_sched_barrier(0);
  }
  template <int V>
  DEVI void unit() {
    constexpr int T = st(V), n = V % 5, nx = xreq(T) - xreq(T - 1), nw = wreq(V) - wreq(V - 1);
    constexpr int vm = 2 * (xreq(T - 1) - 1 - T);
    static_assert(newer(V) <= 15, "lgkmcnt range");
    if constexpr (first(V) && V % 2 == 0) asm volatile("s_waitcnt vmcnt(%0) lgkmcnt(%1)" ::"n"(vm), "n"(newer(V)));
    else if constexpr (first(V)) asm volatile("s_waitcnt vmcnt(%0)" ::"n"(vm));
    else if constexpr (V % 2 == 0) asm volatile("s_waitcnt lgkmcnt(%0)" ::"n"(newer(V)));
    __builtin_amdgcn_sched_barrier(0);
    if constexpr (V < V1) mf<V, V & 1, V & 1>();
    else mf<V, 0, hf(V)>();
    if constexpr (nw > 0) rd<wreq(V - 1)>();
    __builtin_amdgcn_sched_barrier(0);
    if constexpr (V < V1) {
      mf<V, 1 - (V & 1), 1 - (V & 1)>();
      if constexpr (nw > 1) rd<wreq(V - 1) + 1>();
      if constexpr (nx == 2 && n < 4) xld<xreq(T - 1) + n / 2, n % 2>();
      if constexpr (nx == 1 && n == (T & 1)) xld<xreq(T - 1), 0>();
      if constexpr (nx == 1 && n == (T & 1) + 2) xld<xreq(T - 1), 1>();
      if constexpr (n == 4 && (T & 1) && ((BAR >> (T / 2)) & 1u)) bar();
    } else if constexpr (NS == 0) {
      constexpr int k = (V % 10) / 2;
      if constexpr (V % 2 == 0 && k < 2 * nx) xld<xreq(T - 1) + k / 2, k % 2>();
      if constexpr (V % 10 == 8 && nx > 0 && xreq(T) == NST)
        asm volatile("s_mov_b64 exec, 1\n\tds_add_u32 %0, %1\n\ts_mov_b64 exec, -1" ::"v"(fa), "v"(fv));
    } else {
      if constexpr (oth(V) && V < XF) asm volatile("ds_write_b128 %0, %1 offset:%2" ::"v"(ea), "v"(acc[1][(V - XU) / 2]), "n"((V - XU) * 512));
      if constexpr (V == XF) asm volatile("ds_write_b32 %0, %1" ::"v"(fa), "v"(fv));
      if constexpr (V == XF + 2) asm volatile("v_xor_b32 %0, 4, %0" : "+v"(fa));
      if constexpr (V == XR) asm volatile("ds_read_b32 %0, %1" : "+v"(fv) : "v"(fa));
      if constexpr (V == XR + 2) asm volatile("v_xor_b32 %0, 0x2000, %0" : "+v"(ea));
      if constexpr (V == XC) {
        unsigned c = 1u << 18;
        asm volatile(
            "s_waitcnt lgkmcnt(%4)\n\tv_cmp_ne_u32 vcc, %3, %0\n\ts_cbranch_vccnz .Lw%=\n\t.subsection 1\n.Lw%=:\n\tds_read_b32 %0, %2\n\t"
            "s_waitcnt lgkmcnt(0)\n\tv_cmp_ne_u32 vcc, %3, %0\n\t" SAFE_OUT(".Lr%=") "s_cbranch_vccnz .Lw%=\n\ts_branch .Lr%=\n\t.subsection 0\n.Lr%=:"
            : "+v"(fv), NOP_(c)
            : "v"(fa), "s"(tag), "n"(wreq(XC) - wreq(XR))
            : "vcc", "scc");
      }
      if constexpr (oth(V) && V > XC) asm volatile("ds_read_b128 %0, %1 offset:%2" : "=v"(acc[1][V - XC - 1]) : "v"(ea), "n"((V - XC - 1) * 1024));
    }
    __builtin_amdgcn_sched_barrier(0);
  }
  template <int... V>
  DEVI void units(std::integer_sequence<int, V...>) {
    (unit<V>(), ...);
  }
  DEVI void run() {
    units(std::make_integer_sequence<int, NV>{});
    if constexpr (NS > 0) {
      asm volatile("s_waitcnt lgkmcnt(0)" : "+v"(acc[1][0]), "+v"(acc[1][1]), "+v"(acc[1][2]), "+v"(acc[1][3]), "+v"(acc[1][4]));
#pragma unroll
      for (int i = 0; i < 5; ++i) acc[0][i] += acc[1][i];
    }
  }
  template <int... V>
  DEVI void pro(std::integer_sequence<int, V...>) {
    (rd<V>(), ...);
  }
  DEVI void prow() { pro(std::make_integer_sequence<int, wreq(-1)>{}); }
  template <int T0, int... T>
  DEVI void prox(std::integer_sequence<int, T...>) {
    ((xld<T0 + T, 0>(), xld<T0 + T, 1>()), ...);
  }
  DEVI void setup(unsigned lds0, const char* xbase, int wave, int lane, unsigned red, unsigned nth) {
    const int g = lane >> 4, r = lane & 15, kq = wave & 1;
    xp[0] = xbase + wave * 256;
    xp[1] = xbase + (wave ^ 1) * 256;
    xp[2] = xp[0] + (kq ? -4096 : 4096);
    xo = (unsigned)((4 * kq + g) * 1024 + r * 16);
#pragma unroll
    for (int i = 0; i < 2 * NG; ++i) {
      wrd[i / NG][i % NG] = lds0 + (unsigned)(WOFF + (i % NG) * 3 * WS + (kq ^ (i / NG)) * 4608 + (2 * (r & 7) + 16 * (g & 1) + (g >> 1)) * 144 + (r >> 3) * 16);
      if (i < NG || NS == 0 || i % NG == ((NS / 2 + S0) % NSW) / 3) asm volatile("" : "+v"(wrd[i / NG][i % NG]));
    }
    asm volatile("" : "+v"(xo));
    if constexpr (NS > 0) {
      ea = lds0 + red + (unsigned)((wave ^ 1) * 8192 + lane * 16);
      fa = lds0 + (unsigned)(LW_OFF + (LW_PAIR + wave) * 4);
      fv = tag = nth;
      asm volatile("" : "+v"(ea), "+v"(fa), "+v"(fv));
    }
  }
  template <int N>
  DEVI void init(u32x2 (&xv)[5]) {
    asm volatile("s_waitcnt vmcnt(%5)" : "+v"(xv[0]), "+v"(xv[1]), "+v"(xv[2]), "+v"(xv[3]), "+v"(xv[4]) : "n"(N));
#pragma unroll
    for (int i = 0; i < 5; ++i) {
      acc[0][i] = f32x4{bf_lo(xv[i][0]), bf_hi(xv[i][0]), bf_lo(xv[i][1]), bf_hi(xv[i][1])};
      asm volatile("" : "+v"(acc[0][i]));
      acc[1][i] = f32x4{0.f, 0.f, 0.f, 0.f};
    }
  }
};

constexpr int P5_XW = 8, P5_X0 = 5, P2_X0 = 4;
constexpr unsigned P5_BAR = 0x2aaau;
constexpr int P5_W = 15 * 1024;
constexpr int P2_W = K1_ST_OFF - 18 * 4608;
constexpr int P5_RED = 0;
static_assert(P5_RED % 16384 == 0 && P5_RED + 3 * 8192 + 5120 <= P5_W + 18432, "exchange area");
static_assert(P2_W >= K1_T_OFF + 144 * K1_TP + 64 && P2_W % 16 == 0 && P5_W + 8 * 18432 <= LW_OFF, "LDS placement");

struct ResIn {
  const char* xp;
  unsigned o, o2, st;
  DEVI void setup(const char* src, bool rm, int rb, int ct, int wave, int lane) {
    const int g = lane >> 4, g2 = g & 1, rl = wave * 16 + (lane & 15);
    xp = uni_ptr(rm ? src + ((size_t)rb * 64 * D_ + ct * 72) * 2 : src + (size_t)(rb * 72 + ct * 9) * 1024);
    o = rm ? (unsigned)(rl * (D_ * 2) + g * 8) : (unsigned)((g >> 1) * 1024 + rl * 16 + g2 * 8);
    o2 = rm ? (unsigned)(rl * (D_ * 2) + g2 * 8 + 128) : (unsigned)(8192 + rl * 16 + g2 * 8);
    st = rm ? 32u : 2048u;
    asm volatile("" : "+v"(o), "+v"(o2));
    asm volatile("" : "+s"(xp));
  }
  DEVI void load(u32x2 (&xv)[5]) {
#pragma unroll
    for (int i = 0; i < 4; ++i) asm volatile("global_load_dwordx2 %0, %1, %2 sc1" : "=v"(xv[i]) : "v"(o), "s"(xp + i * st));
    asm volatile("global_load_dwordx2 %0, %1, %2 sc1" : "=v"(xv[4]) : "v"(o2), "s"(xp));
  }
};

DEVI void p2_gemm(f32x4 (&acc)[2][5], u32x2 (&xv)[5], unsigned lds0, const char* ob, int layer, int wave, int lane MK_ARG) {
  ResX<9, 0, 5, P2_W, 9, P2_X0, 0, 0u> gm(acc);
  gm.setup(lds0, ob, wave, lane, 0u, 0u);
  unsigned a = (gm.xo & ~1023u) | ((gm.xo & 1023u) >> 2);
  gm.xo += 8192u;
  gm.fa = lds0 + (unsigned)(LW_OFF + (LW_T + layer) * 4);
  gm.fv = vconst(1u);
  asm volatile("" : "+v"(a), "+v"(gm.xo), "+v"(gm.fa));
  const char* pp[2] = {gm.xp[0] - wave * 192, gm.xp[2] - wave * 192};
  place();
  bar();
  MK(6);
  unsigned e[2][4];
#pragma unroll
  for (int d = 0; d < 8; ++d)
    asm volatile("global_load_dword %0, %1, %2 offset:%3 sc1" : "=v"(e[d / 4][d % 4]) : "v"(a), "s"(pp[d / 4]), "n"((d % 4) * 256));
  gm.template prox<1>(std::make_integer_sequence<int, P2_X0 - 1>{});
  gm.prow();
  gm.template init<6 + 2 * P2_X0>(xv);
  asm volatile("s_waitcnt vmcnt(%8)"
               : "+v"(e[0][0]), "+v"(e[0][1]), "+v"(e[0][2]), "+v"(e[0][3]), "+v"(e[1][0]), "+v"(e[1][1]), "+v"(e[1][2]), "+v"(e[1][3])
               : "n"(2 * P2_X0 - 2));
  gm.xf[0][0] = __builtin_bit_cast(s16x8, u32x4{e[0][0], e[0][1], e[0][2], e[0][3]});
  gm.xf[0][1] = __builtin_bit_cast(s16x8, u32x4{e[1][0], e[1][1], e[1][2], e[1][3]});
  __builtin_amdgcn_sched_barrier(0);
  gm.run();
}

struct ResOut {
  const char *xp[3], *sp;
  unsigned xo, so;
  DEVI void setup(char* xim, char* xs, u16* out, bool fin, int rb, int ct, int wave, int lane) {
    const int g = lane >> 4, rl = wave * 16 + (lane & 15);
    xp[0] = uni_ptr(fin ? (const char*)(out + (size_t)rb * 64 * D_ + ct * 72) : xim + (size_t)(rb * 72 + ct * 9) * 1024);
    xp[1] = xp[0] + (fin ? 64 : 4096);
    xp[2] = xp[0] + (fin ? 128 : 8192);
    sp = uni_ptr(xs + (size_t)(rb * 8 + ct) * 512);
    xo = (unsigned)(((g & 1) * 2 + (g >> 1)) * (fin ? 16 : 1024) + rl * (fin ? D_ * 2 : 16));
    so = (unsigned)(rl * 8);
    asm volatile("" : "+v"(xo), "+v"(so), "+s"(xp[0]), "+s"(xp[1]), "+s"(xp[2]), "+s"(sp));
  }
  DEVI void run(const f32x4 (&a)[5], bool fin) {
    u32x2 y[5];
    u32x4 v[3];
#pragma unroll
    for (int nt = 0; nt < 5; ++nt) y[nt] = u32x2{pack2(a[nt][0], a[nt][1]), pack2(a[nt][2], a[nt][3])};
#pragma unroll
    for (int i = 0; i < 3; ++i) {
      const auto s0 = __builtin_amdgcn_permlane16_swap(y[2 * i][0], y[i < 2 ? 2 * i + 1 : 4][0], false, false);
      const auto s1 = __builtin_amdgcn_permlane16_swap(y[2 * i][1], y[i < 2 ? 2 * i + 1 : 4][1], false, false);
      v[i] = u32x4{s0[0], s1[0], s0[1], s1[1]};
    }
    if (fin) {
      asm volatile("global_store_dwordx4 %0, %1, %4 nt\n\tglobal_store_dwordx4 %0, %2, %5 nt\n\ts_mov_b64 exec, 0xffff\n\tglobal_store_dwordx4 %0, %3, %6 nt\n\t"
                   "s_mov_b64 exec, -1\n\ts_nop 0" ::"v"(xo), "v"(v[0]), "v"(v[1]), "v"(v[2]), "s"(xp[0]), "s"(xp[1]), "s"(xp[2])
                   : "memory");
      return;
    }
    asm volatile("global_store_dwordx4 %0, %1, %4 sc1\n\tglobal_store_dwordx4 %0, %2, %5 sc1\n\ts_mov_b64 exec, 0xffff\n\t"
                 "global_store_dwordx4 %0, %3, %6 sc1\n\ts_mov_b64 exec, -1\n\ts_nop 0" ::"v"(xo), "v"(v[0]), "v"(v[1]), "v"(v[2]),
                 "s"(xp[0]), "s"(xp[1]), "s"(xp[2])
                 : "memory");
    const u32x4 one4 = {vconst(0x3f803f80u), vconst(0x3f803f80u), vconst(0x3f803f80u), vconst(0x3f803f80u)};
    const bool lo = __builtin_amdgcn_inverse_ballot_w64(0xffffull);
    v[2] = u32x4{lo ? v[2][0] : 0u, lo ? v[2][1] : 0u, lo ? v[2][2] : 0u, lo ? v[2][3] : 0u};
    f32x4 s = {0.f, 0.f, 0.f, 0.f}, q = s;
#pragma unroll
    for (int i = 0; i < 3; ++i) {
      const s16x8 u = __builtin_bit_cast(s16x8, v[i]);
      q = __builtin_amdgcn_mfma_f32_16x16x32_bf16(u, u, q, 0, 0, 0);
      s = __builtin_amdgcn_mfma_f32_16x16x32_bf16(__builtin_bit_cast(s16x8, one4), u, s, 0, 0, 0);
    }
    float q0 = q[0], q1 = q[1], q2 = q[2], q3 = q[3];
    const bool b0 = __builtin_amdgcn_inverse_ballot_w64(0xaaaaaaaaaaaaaaaaull), b1 = __builtin_amdgcn_inverse_ballot_w64(0xccccccccccccccccull);
    q0 = b0 ? q1 : q0;
    q2 = b0 ? q3 : q2;
    asm("" : "+v"(q0), "+v"(q2));
    const f32x2 sq = {s[0], b1 ? q2 : q0};
    asm volatile("s_mov_b64 exec, %3\n\tglobal_store_dwordx2 %0, %1, %2 sc1\n\ts_mov_b64 exec, -1" ::"v"(so), "v"(sq), "s"(sp), "s"(0xf0000f0000f0000full) : "memory");
  }
};

using K3C = GemmCfg<128, 144>;
constexpr int K3_Z1_OFF = K3C::LDS_BYTES;
constexpr int K3_C2_OFF = K3_Z1_OFF + K3C::XB;
constexpr int K3_ZC_OFF = K3_C2_OFF + 2048;
constexpr int K3_DM_OFF = K3_ZC_OFF + 1024;
constexpr int K3_ST_OFF = K3_Z1_OFF + 4 * 5120;
constexpr int K3_C1_OFF = K3_ST_OFF + 2048;
constexpr int K3_BS_OFF = K3_C1_OFF + 576;
static_assert(K3_BS_OFF + 512 <= LW_OFF, "K3 LDS");
static_assert(P5_W + 19 * 1024 == K3C::SB && K3C::SB == 34 * 1024, "w_out ring");

DEVI void red4(float (&t)[4], float& s, float& q) {
#pragma unroll
  for (int j = 0; j < 2; ++j) {
    const auto p = __builtin_amdgcn_permlane16_swap(__float_as_uint(t[j]), __float_as_uint(t[2 + j]), false, false);
    const unsigned v = __float_as_uint(__uint_as_float(p[0]) + __uint_as_float(p[1]));
    const auto w = __builtin_amdgcn_permlane32_swap(v, v, false, false);
    t[j] = __uint_as_float(w[0]) + __uint_as_float(w[1]);
    asm volatile("" : "+v"(t[j]));
  }
  s = t[0];
  q = t[1];
}
DEVI void red8(const f32x4& a, const f32x4& b, float& s, float& q) {
  float t[4];
#pragma unroll
  for (int j = 0; j < 4; ++j) {
    t[j] = a[j] + b[j];
    asm volatile("" : "+v"(t[j]));
  }
  red4(t, s, q);
}
DEVI void red16(const f32x4& a, const f32x4& b, const f32x4& c, const f32x4& d, float& s, float& q) {
  float t[4];
#pragma unroll
  for (int j = 0; j < 4; ++j) {
    float u = a[j] + b[j], v = c[j] + d[j];
    asm volatile("" : "+v"(u), "+v"(v));
    t[j] = u + v;
    asm volatile("" : "+v"(t[j]));
  }
  red4(t, s, q);
}

constexpr int b_k(int s) { return s == 0 ? 0 : (s < 3 ? 6 + s : (s == 3 ? 1 : (s < 6 ? 1 + s : (s == 6 ? 4 : s - 5)))); }
constexpr int b_xp(int s) { return s == 0 ? 0 : (b_k(s) >= 4 ? (b_k(s) % 5) / 2 : b_xp(s - 1)); }
template <bool B>
struct UMath {
  static constexpr int D = 4, R = 6, NV = 162, SB = K3C::SB, P = K3C::P;
  s16x8 wf[R], xf[2][2];
  s16x8 (&xc)[4][2][2];
  f32x4 (&acc)[2][9];
  f32x4 rc[4][2];
  f32x2 cs[9];
  unsigned ra, ca;
  template <int I>
  DEVI void cread() {
    if constexpr (I < 8) asm volatile("ds_read_b128 %0, %1 offset:%2" : "=v"(rc[I / 2][I % 2]) : "v"(ra), "n"((I / 2) * 512 + (I % 2) * 16));
    else if constexpr (I < 17) asm volatile("ds_read_b64 %0, %1 offset:%2" : "=v"(cs[I - 8]) : "v"(ca), "n"((I - 8) * 128));
  }
  unsigned xa, wa;
  unsigned fa, fw, fv;
  DEVI UMath(f32x4 (&a)[2][9], s16x8 (&x)[4][2][2]) : xc(x), acc(a) {}
  static constexpr int wslot(int s) { return B ? (s + 1) % 4 : s % 4; }
  static constexpr int xslot(int s) { return B ? b_k(s) % 5 : s % 5; }
  static constexpr int xpair(int s) { return B ? b_xp(s) : xslot(s) / 2; }
  static constexpr bool res(int q) { return !B || b_k(q / 2) >= 4; }
  static constexpr bool chk(int s) { return s >= 3 && s < 7; }
  static constexpr bool xop(int v) { return v + D < NV && (v + D) % 9 < 2 && res((v + D) / 9); }
  static constexpr int ops1(int v) { return (xop(v) || (B && ((v % 18 == 9 && chk(v / 18)) || (v % 18 == 0 && v > 0)))) ? 1 : 0; }
  static constexpr int newer(int V) {
    int v0 = (V % 9 == 8 ? V : V + 1) - D, n = 0;
    if (v0 >= 0 && !(V % 9 == 0 && res(V / 9))) n += ops1(v0);
    for (int v = v0 + 1; v < V; ++v)
      if (v >= 0) n += (v + D < NV ? 1 : (B ? 0 : 4)) + ops1(v);
    return n;
  }
  template <int V>
  DEVI void wread() {
    asm volatile("ds_read_b128 %0, %1 offset:%2" : "=v"(wf[V % R]) : "v"(wa), "n"((wslot(V / 18) & 1) * SB + ((V % 18) / 9) * 32 * P + (V % 9) * 32));
  }
  template <int Q, int M>
  DEVI void xread() {
    asm volatile("ds_read_b128 %0, %1 offset:%2" : "=v"(xf[Q & 1][M]) : "v"(xa), "n"((xslot(Q / 2) & 1) * SB + (Q & 1) * 8192 + M * (B ? 256 : 64)));
  }
  template <int V, int M_>
  DEVI void mf() {
    constexpr int M = ((V % 9) & 1) ? 1 - M_ : M_, q = V / 9;
    if constexpr (!B) acc[M][V % 9] = __builtin_amdgcn_mfma_f32_16x16x32_bf16(xf[q & 1][M], wf[V % R], acc[M][V % 9], 0, 0, 0);
    else if constexpr (res(q)) acc[M][V % 9] = __builtin_amdgcn_mfma_f32_16x16x32_bf16(wf[V % R], xf[q & 1][M], acc[M][V % 9], 0, 0, 0);
    else acc[M][V % 9] = __builtin_amdgcn_mfma_f32_16x16x32_bf16(wf[V % R], xc[b_k(q / 2)][q & 1][M], acc[M][V % 9], 0, 0, 0);
    __builtin_amdgcn_sched_barrier(0);
  }
  template <int V>
  DEVI void unit() {
    constexpr int U = V % 18, s = V / 18;
    static_assert(newer(V) <= 15, "lgkmcnt range");
    if constexpr ((V % 9) % 2 == 0 && V > 0) asm volatile("s_waitcnt lgkmcnt(%0)" ::"n"(newer(V)));
    __builtin_amdgcn_sched_barrier(0);
    mf<V, 0>();
    if constexpr (V + D < NV) wread<V + D>();
    if constexpr (!B && V + D >= NV) {
      cread<(V + D - NV) * 4>();
      cread<(V + D - NV) * 4 + 1>();
    }
    if constexpr (s < 8 && U == 13 && (wslot(s + 1) >> 1) != (wslot(s) >> 1))
      asm volatile("v_add_u32 %0, %1, %0" : "+v"(wa) : "n"(((wslot(s + 1) >> 1) - (wslot(s) >> 1)) * 2 * SB));
    __builtin_amdgcn_sched_barrier(0);
    mf<V, 1>();
    if constexpr (xop(V)) xread<(V + D) / 9, (V + D) % 9>();
    if constexpr (!B && V + D >= NV) {
      cread<(V + D - NV) * 4 + 2>();
      cread<(V + D - NV) * 4 + 3>();
      if constexpr (V == NV - 1) cread<16>();
    }
    if constexpr (s < 8) {
      if constexpr (U == 11 && xpair(s + 1) != xpair(s)) asm volatile("v_add_u32 %0, %1, %0" : "+v"(xa) : "n"((xpair(s + 1) - xpair(s)) * 2 * SB));
      if constexpr (!B && U == 13) __builtin_amdgcn_s_barrier();
      if constexpr (B && U == 9 && chk(s)) asm volatile("ds_read_u8 %0, %1 offset:%2" : "+v"(fv) : "v"(fa), "n"(s == 6 ? 0 : 8));
      unsigned n = 1u << 18;
      if constexpr (B && U == 13 && chk(s)) {
        asm volatile(
            "v_cmp_gt_u32 vcc, %3, %0\n\ts_cbranch_vccnz .Lw%=\n\t.subsection 1\n.Lw%=:\n\tds_read_u8 %0, %2 offset:%4\n\ts_waitcnt lgkmcnt(0)\n\t"
            "v_cmp_gt_u32 vcc, %3, %0\n\t" SAFE_OUT(".Lr%=") "s_cbranch_vccnz .Lw%=\n\ts_branch .Lr%=\n\t.subsection 0\n.Lr%=:"
            : "+v"(fv), NOP_(n)
            : "v"(fa), "n"(s == 6 ? 4 : s + 2), "n"(s == 6 ? 0 : 8)
            : "vcc", "scc");
      }
    }
    if constexpr (B && U == 15) asm volatile("v_mov_b32 %0, %1" : "+v"(fv) : "n"(s + 1));
    if constexpr (B && ((U == 0 && s > 0) || V == NV - 1)) asm volatile("ds_write_b8 %0, %1" ::"v"(fw), "v"(fv));
    __builtin_amdgcn_sched_barrier(0);
  }
  template <int... V>
  DEVI void units(std::integer_sequence<int, V...>) {
    (unit<V>(), ...);
  }
  DEVI void first() {
    if constexpr (!B) {
      xread<0, 0>();
      xread<0, 1>();
    }
    wread<0>();
    wread<1>();
    wread<2>();
    wread<3>();
    asm volatile("s_waitcnt lgkmcnt(0)");
  }
  DEVI void run() {
    units(std::make_integer_sequence<int, NV>{});
#pragma unroll
    for (int m = 0; m < 2; ++m)
#pragma unroll
      for (int n = 0; n < 9; ++n) asm volatile("" : "+v"(acc[m][n]));
  }
};

struct CMath {
  static constexpr int D = 4, R = 6, SB = K3C::SB;
  s16x8 wf[R], xf[2][2];
  f32x4 (&acc)[2][9];
  unsigned wa[2], xa[2][2];
  DEVI CMath(f32x4 (&a)[2][9]) : acc(a) {}
  static constexpr int xop(int u, bool last) { return (u + D) % 9 < 2 && !(last && u + D >= 18) ? 1 : 0; }
  static constexpr int newer(int U, bool last) {
    int v0 = (U % 9 == 8 ? U : U + 1) - D, n = 0;
    if (U % 9 != 0) n += xop((v0 + 18) % 18, last && v0 >= 0);
    for (int v = v0 + 1; v < U; ++v) n += (last && v + D >= 18 ? 0 : 1) + xop((v + 18) % 18, last && v >= 0);
    return n;
  }
  template <int SL, int U>
  DEVI void wread() {
    asm volatile("ds_read_b128 %0, %1 offset:%2" : "=v"(wf[U % R]) : "v"(wa[SL / 2]), "n"((SL & 1) * SB + U * 1024));
  }
  template <int SL, int H, int M>
  DEVI void xread() {
    asm volatile("ds_read_b128 %0, %1 offset:%2" : "=v"(xf[H][M]) : "v"(xa[SL / 2][H]), "n"((SL & 1) * SB + M * 2048));
  }
  template <int U, int M_>
  DEVI void mf() {
    constexpr int M = ((U % 9) & 1) ? 1 - M_ : M_;
    acc[M][U % 9] = __builtin_amdgcn_mfma_f32_16x16x32_bf16(wf[U % R], xf[U / 9][M], acc[M][U % 9], 0, 0, 0);
    __builtin_amdgcn_sched_barrier(0);
  }
  template <int SL, bool BAR, bool LAST, int U>
  DEVI void unit() {
    constexpr int PU = (U + D) % 18, PS = U + D < 18 ? SL : (SL + 1) % 4;
    if constexpr ((U % 9) % 2 == 0) asm volatile("s_waitcnt lgkmcnt(%0)" ::"n"(newer(U, LAST)));
    __builtin_amdgcn_sched_barrier(0);
    mf<U, 0>();
    if constexpr (!(LAST && U + D >= 18)) wread<PS, PU>();
    __builtin_amdgcn_sched_barrier(0);
    mf<U, 1>();
    if constexpr (xop(U, LAST)) xread<PS, PU / 9, PU % 9>();
    if constexpr (BAR && U == 13) __builtin_amdgcn_s_barrier();
    __builtin_amdgcn_sched_barrier(0);
  }
  template <int SL, bool BAR, bool LAST, int... U>
  DEVI void units(std::integer_sequence<int, U...>) {
    (unit<SL, BAR, LAST, U>(), ...);
  }
  template <int SL, bool BAR = true, bool LAST = false>
  DEVI void stage() {
    units<SL, BAR, LAST>(std::make_integer_sequence<int, 18>{});
  }
  DEVI void setup(unsigned lds0, int wm, int lane) {
    const int g = lane >> 4, r = lane & 15;
#pragma unroll
    for (int i = 0; i < 2; ++i) {
      wa[i] = lds0 + (unsigned)(i * 2 * SB + K3C::XB + lane * 16);
#pragma unroll
      for (int h = 0; h < 2; ++h) xa[i][h] = lds0 + (unsigned)(i * 2 * SB + (wm * 32 + r) * 128 + (((4 * h + g) ^ ((r >> 1) & 7)) * 16));
    }
    asm volatile("" : "+v"(wa[0]), "+v"(wa[1]), "+v"(xa[0][0]), "+v"(xa[0][1]), "+v"(xa[1][0]), "+v"(xa[1][1]));
  }
  DEVI void first() {
    xread<2, 0, 0>();
    xread<2, 0, 1>();
    wread<2, 0>();
    wread<2, 1>();
    wread<2, 2>();
    wread<2, 3>();
    asm volatile("s_waitcnt lgkmcnt(0)");
  }
};

constexpr int ZQ = 4;
constexpr int NR1 = 3;
DEVI void p3_g0(char* smem, char* __restrict__ ws, int layer, int mi, int ni, int wm, int lane MK_ARG) {
  constexpr int SB = K3C::SB;
  const unsigned lds0 = (unsigned)(size_t)smem;
  const int g = lane >> 4, r = lane & 15;
  const unsigned lw = lds0 + (unsigned)(LW_OFF + layer * 4), me = 1u << (8 * wm);
  f32x4 ac[2][9];
#pragma unroll
  for (int mt = 0; mt < 2; ++mt)
#pragma unroll
    for (int j = 0; j < 9; ++j) ac[mt][j] = f32x4{0.f, 0.f, 0.f, 0.f};
  unsigned ra = lds0 + (unsigned)(K3_ST_OFF + (wm * 32 + g * 8) * 4), ca = lds0 + (unsigned)(K3_C2_OFF + r * 8);
  asm volatile("" : "+v"(ra), "+v"(ca));
  s16x8 xc[4][2][2];
  UMath<false> gm(ac, xc);
  gm.ra = ra;
  gm.ca = ca;
  gm.xa = lds0 + (unsigned)(g * 2048 + (wm * 32 + 8 * (r >> 2) + (r & 3)) * 16);
  gm.wa = lds0 + (unsigned)(K3C::XB + (g * 8 + (r & 7)) * K3C::P + (r >> 3) * 16);
  unsigned adw = lw + LW_AD * 4, mev = vconst(me), po = (unsigned)(ni * 1024 + lane * 4);
  const char* zp = uni_ptr(ws + OFF_ZP + (size_t)layer * SZ_ZP + (size_t)mi * 16384 + wm * 256);
  asm volatile("" : "+v"(gm.xa), "+v"(gm.wa), "+v"(adw), "+v"(po));
  asm volatile("" : "+s"(zp));
  place();
  bar();
  gm.first();
  gm.run();
  asm volatile("s_mov_b64 exec, 1\n\tds_add_u32 %0, %1\n\ts_mov_b64 exec, -1" ::"v"(adw), "v"(mev) : "memory");
  MK(11);
  f32x2 z[9][4];
  f32x2 sm[4], sq[4];
  {
    f32x4 (&rc)[4][2] = gm.rc;
    f32x2 (&cs)[9] = gm.cs;
    asm volatile("s_waitcnt lgkmcnt(1)"
                 : "+v"(rc[0][0]), "+v"(rc[0][1]), "+v"(rc[1][0]), "+v"(rc[1][1]), "+v"(rc[2][0]), "+v"(rc[2][1]), "+v"(rc[3][0]), "+v"(rc[3][1]));
#pragma unroll
    for (int j = 0; j < 9; ++j) asm volatile("" : "+v"(cs[j]));
    auto RC = [&](int k, int i) { return f32x2{rc[k][i / 2][(i % 2) * 2], rc[k][i / 2][(i % 2) * 2 + 1]}; };
#pragma unroll
    for (int j = 0; j < 9; ++j) {
      f32x2 u[4], w[4];
#pragma unroll
      for (int i = 0; i < 4; ++i) u[i] = __builtin_elementwise_fma(RC(0, i), cs[j], f32x2{ac[i / 2][j][(i % 2) * 2], ac[i / 2][j][(i % 2) * 2 + 1]});
#pragma unroll
      for (int i = 0; i < 4; ++i) w[i] = u[i] * u[i];
#pragma unroll
      for (int i = 0; i < 4; ++i) w[i] = __builtin_elementwise_fma(w[i], RC(3, i), RC(2, i));
#pragma unroll
      for (int i = 0; i < 4; ++i) w[i] = u[i] * w[i];
      __builtin_amdgcn_sched_barrier(0);
#pragma unroll
      for (int i = 0; i < 4; ++i) w[i] = f32x2{__builtin_amdgcn_exp2f(w[i][0]), __builtin_amdgcn_exp2f(w[i][1])};
      __builtin_amdgcn_sched_barrier(0);
#pragma unroll
      for (int i = 0; i < 4; ++i) w[i] = __builtin_elementwise_fma(w[i], RC(1, i), RC(1, i));
      __builtin_amdgcn_sched_barrier(0);
#pragma unroll
      for (int i = 0; i < 4; ++i) w[i] = f32x2{__builtin_amdgcn_rcpf(w[i][0]), __builtin_amdgcn_rcpf(w[i][1])};
      __builtin_amdgcn_sched_barrier(0);
#pragma unroll
      for (int i = 0; i < 4; ++i) z[j][i] = u[i] * w[i];
#pragma unroll
      for (int i = 0; i < 4; ++i) {
        if (j == 1) sm[i] = z[0][i] + z[1][i];
        if (j > 1) sm[i] += z[j][i];
      }
#pragma unroll
      for (int i = 0; i < 4; ++i) sq[i] = j ? __builtin_elementwise_fma(z[j][i], z[j][i], sq[i]) : z[j][i] * z[j][i];
      asm volatile("" : "+v"(z[j][0]), "+v"(z[j][1]), "+v"(z[j][2]), "+v"(z[j][3]));
      __builtin_amdgcn_sched_barrier(0);
    }
  }
  f32x4 e;
  {
    float v[4][4], d[2][4], f[4];
#pragma unroll
    for (int i = 0; i < 4; ++i) {
      v[i][0] = sm[i][0];
      v[i][1] = sq[i][0];
      v[i][2] = sm[i][1];
      v[i][3] = sq[i][1];
    }
    asm volatile("s_nop 1" : "+v"(v[3][1]), "+v"(v[3][3]), "+v"(v[2][1]), "+v"(v[2][3]));
#pragma unroll
    for (int i = 0; i < 2; ++i)
#pragma unroll
      for (int c = 0; c < 4; ++c)
        asm volatile("v_add_f32_dpp %0, %1, %1 row_ror:8 row_mask:0xf bank_mask:0x3\n\tv_add_f32_dpp %0, %2, %2 row_ror:8 row_mask:0xf bank_mask:0xc"
                     : "=&v"(d[i][c])
                     : "v"(v[i][c]), "v"(v[i + 2][c]));
#pragma unroll
    for (int c = 0; c < 4; ++c)
      asm volatile("v_add_f32_dpp %0, %1, %1 row_shl:4 row_mask:0xf bank_mask:0x5\n\tv_add_f32_dpp %0, %2, %2 row_shr:4 row_mask:0xf bank_mask:0xa"
                   : "=&v"(f[c])
                   : "v"(d[0][c]), "v"(d[1][c]));
    asm volatile(
        "v_add_f32_dpp %0, %0, %0 quad_perm:[1,0,3,2] row_mask:0xf bank_mask:0xf\n\t"
        "v_add_f32_dpp %1, %1, %1 quad_perm:[1,0,3,2] row_mask:0xf bank_mask:0xf\n\t"
        "v_add_f32_dpp %2, %2, %2 quad_perm:[1,0,3,2] row_mask:0xf bank_mask:0xf\n\t"
        "v_add_f32_dpp %3, %3, %3 quad_perm:[1,0,3,2] row_mask:0xf bank_mask:0xf\n\t"
        "v_add_f32_dpp %0, %0, %0 quad_perm:[2,3,0,1] row_mask:0xf bank_mask:0xf\n\t"
        "v_add_f32_dpp %1, %1, %1 quad_perm:[2,3,0,1] row_mask:0xf bank_mask:0xf\n\t"
        "v_add_f32_dpp %2, %2, %2 quad_perm:[2,3,0,1] row_mask:0xf bank_mask:0xf\n\t"
        "v_add_f32_dpp %3, %3, %3 quad_perm:[2,3,0,1] row_mask:0xf bank_mask:0xf"
        : "+v"(f[0]), "+v"(f[1]), "+v"(f[2]), "+v"(f[3]));
    e = f32x4{f[0], __uint_as_float(__float_as_uint(f[1]) & 0x7fffffffu), f[2], __uint_as_float(__float_as_uint(f[3]) & 0x7fffffffu)};
  }
  asm volatile("s_mov_b64 exec, %3\n\tglobal_store_dwordx4 %0, %1, %2 sc1\n\ts_mov_b64 exec, -1\n\ts_nop 0" ::"v"(po), "v"(e), "s"(zp),
               "s"(0x1111111111111111ull)
               : "memory");
  asm volatile("s_mov_b64 exec, 1\n\tds_add_u32 %0, %1\n\ts_mov_b64 exec, -1" ::"v"(adw), "v"(mev) : "memory");
  __builtin_amdgcn_s_setprio(2);
  MK(12);
  const char* dst = uni_ptr(ws + OFF_Z2 + (size_t)layer * SZ_Z + (size_t)ni * S_ * 288 + (size_t)(mi * 4 + wm) * 9216 + 4096);
  const unsigned lane16 = (unsigned)lane * 16u;
  u32x4 wb[9];
  {
    static_assert(b_k(7) == 2 && b_k(8) == 3, "order");
    const char* wp = uni_ptr(ws + OFF_PI + (size_t)ni * (18 * K3C::WB) + 11 * K3C::WB + wm * 9216 + 4096);
#pragma unroll
    for (int j = 0; j < 8; ++j) asm volatile("global_load_dwordx4 %0, %1, %2 offset:%3 sc1" : "=v"(wb[j]) : "v"(lane16), "s"(wp), "n"(j * 1024 - 4096) : "memory");
    asm volatile("global_load_dwordx4 %0, %1, %2 sc1" : "=v"(wb[8]) : "v"(lane16 + 4096u), "s"(wp) : "memory");
  }
  u32x4 y[9];
  {
    const unsigned src = (unsigned)(g * 4096 + r * 16);
    unsigned zw = lds0 + (unsigned)(K3_ZC_OFF + (wm * 32 + 2 * r + (g & 1)) * 4), zr = lds0 + (unsigned)(K3_ZC_OFF + (wm * 32 + g * 8) * 4);
    asm volatile("" : "+v"(zw), "+v"(zr));
    f32x4 a, b, c, d;
    __builtin_amdgcn_s_sleep(6);
#pragma unroll 1
    for (unsigned spin = 0; spin < SPIN_MEM; ++spin) {
      asm volatile(
          "global_load_dwordx4 %0, %4, %5 sc1\n\t"
          "global_load_dwordx4 %1, %4, %5 offset:1024 sc1\n\t"
          "global_load_dwordx4 %2, %4, %5 offset:2048 sc1\n\t"
          "global_load_dwordx4 %3, %4, %5 offset:3072 sc1\n\t"
          "s_waitcnt vmcnt(0)"
          : "=&v"(a), "=&v"(b), "=&v"(c), "=&v"(d)
          : "v"(src), "s"(zp)
          : "memory");
      const unsigned bad = (__float_as_uint(a[1]) | __float_as_uint(a[3]) | __float_as_uint(b[1]) | __float_as_uint(b[3]) |
                            __float_as_uint(c[1]) | __float_as_uint(c[3]) | __float_as_uint(d[1]) | __float_as_uint(d[3])) >> 31;
      if (!__any(bad)) break;
    }
    MK(13);
    float s, q, zmb, zi;
    red16(a, b, c, d, s, q);
    ln_finish(s, q, 1.0f / (float)FH_, zmb, zi);
    f32x4 R[2], M[2];
    asm volatile(
        "ds_write2st64_b32 %4, %5, %6 offset1:2\n\t"
        "ds_read_b128 %0, %7\n\tds_read_b128 %2, %7 offset:512\n\tds_read_b128 %1, %7 offset:16\n\tds_read_b128 %3, %7 offset:528\n\t"
        "s_waitcnt lgkmcnt(0)"
        : "=&v"(R[0]), "=&v"(R[1]), "=&v"(M[0]), "=&v"(M[1])
        : "v"(zw), "v"(zi), "v"(-zmb * zi), "v"(zr)
        : "memory");
    MK(28);
    auto st = [&](int j) {
      if (j < 8) asm volatile("global_store_dwordx4 %0, %1, %2 offset:%3 sc1\n\ts_nop 1" ::"v"(lane16), "v"(y[j]), "s"(dst), "n"(j * 1024 - 4096) : "memory");
      else asm volatile("global_store_dwordx4 %0, %1, %2 sc1\n\ts_nop 1" ::"v"(lane16 + 4096u), "v"(y[j]), "s"(dst) : "memory");
    };
#pragma unroll
    for (int j = 0; j < 9; ++j) {
      {
        float t[8];
#pragma unroll
        for (int k = 0; k < 8; ++k) {
          t[k] = __builtin_fmaf(z[j][k / 2][k % 2], R[k / 4][k % 4], M[k / 4][k % 4]);
          asm volatile("" : "+v"(t[k]));
        }
        y[j] = u32x4{pack2(t[0], t[1]), pack2(t[2], t[3]), pack2(t[4], t[5]), pack2(t[6], t[7])};
      }
      st(j);
      __builtin_amdgcn_sched_barrier(0);
    }
    MK(29);
  }
  lds_inc(lw + LW_AD * 4, me);
  MK(24);
  __builtin_amdgcn_s_setprio(2);
  lds_spin4<2>(lw + LW_S * 4, 4u + (unsigned)(wm >> 1));
  {
    const unsigned fa = lds0 + (unsigned)((wm >> 1) * SB + K3C::XB + (wm & 1) * 9216) + lane16;
#pragma unroll
    for (int j = 0; j < 9; ++j) {
      asm volatile("ds_write_b128 %0, %1 offset:%2" ::"v"(fa), "v"(wb[j]), "n"(j * 1024) : "memory");
    }
    lds_inc(lw + LW_AD * 4, me);
  }
  announce(lw + LW_Z * 4, 4u, ws, ((mi >= ZQ) ? CN_Z2 : CN_Z) + layer, lane * 16 + ni, lane < 16);
  lds_inc(lw + LW_AD * 4, me);
  MK(27);
  f32x4 acc[2][9];
  {
    float bs[2];
    asm volatile("ds_read2_b32 %0, %1 offset1:16\n\ts_waitcnt lgkmcnt(0)"
                 : "=v"(*reinterpret_cast<f32x2*>(bs))
                 : "v"(lds0 + (unsigned)(K3_BS_OFF + (wm * 32 + r) * 4))
                 : "memory");
    lds_spin4<2>(lw + LW_S * 4, 6u + (unsigned)(wm >> 1));
    const unsigned fa = lds0 + (unsigned)((2 + (wm >> 1)) * SB + K3C::XB + (wm & 1) * 9216) + lane16;
#pragma unroll
    for (int j = 0; j < 9; ++j) {
      asm volatile("ds_write_b128 %0, %1 offset:%2" ::"v"(fa), "v"(y[j]), "n"(j * 1024) : "memory");
    }
    lds_inc(lw + LW_AD * 4, me);
#pragma unroll
    for (int mt = 0; mt < 2; ++mt) {
      f32x4 b4 = {bs[mt], bs[mt], bs[mt], bs[mt]};
      asm volatile("" : "+v"(b4));
#pragma unroll
      for (int j = 0; j < 9; ++j) acc[mt][j] = b4;
    }
  }
  u32x4 z1[9];
  const char* hp = uni_ptr(ws + OFF_H + (size_t)layer * SZ_Z + (size_t)((mi * 2 + (wm >> 1)) * 288 + ni * 18) * 1024);
  unsigned ho = (unsigned)((lane >> 5) * 1024 + ((wm & 1) * 32 + (lane & 31)) * 16);
  unsigned zb = lds0 + (unsigned)(wm * 4096) + lane16, zf = lw + (unsigned)(LW_S * 4 + wm);
  asm volatile("" : "+v"(ho), "+v"(zb), "+v"(zf));
  asm volatile("" : "+s"(hp));
  {
    CMath gm(acc);
    gm.setup(lds0, wm, lane);
    unsigned za = lds0 + (unsigned)(K3_Z1_OFF + wm * 5120) + lane16;
    asm volatile("" : "+v"(za));
    MK(15);
    lds_spin4<2>(lw + LW_L * 4, 8u);
    lds_spin4(lw + LW_AD * 4, 6u);
    MK(17);
    gm.first();
    __builtin_amdgcn_s_setprio(0);
    gm.stage<2, false>();
    gm.stage<3>();
    place();
#pragma unroll 1
    for (int t = 0; t < 7; ++t) {
      gm.stage<0>();
#pragma unroll
      for (int j = 0; j < NR1; ++j) asm volatile("ds_read_b128 %0, %1 offset:%2" : "=v"(z1[j]) : "v"(za), "n"(j * 1024));
      gm.stage<1>();
#pragma unroll
      for (int j = 0; j < NR1; ++j) asm volatile("" : "+v"(z1[j]));
      gm.stage<2>();
      gm.stage<3>();
    }
    gm.stage<0>();
    gm.stage<1, false, true>();
#pragma unroll
    for (int j = 0; j < 18; ++j) asm volatile("" : "+v"(acc[j / 9][j % 9]));
  }
  MK(16);
  if (NR1 < 5) lds_inc(lw + LW_AD * 4, me);
  unsigned zv;
  u32x4 zl[4];
#pragma unroll
  for (int j = 0; j < 9; ++j) {
    const bool sec = j >= NR1 + 3;
    if (j == NR1 || j == NR1 + 4) {
      unsigned n = 1u << 18;
      asm volatile(
          "s_waitcnt lgkmcnt(0)\n\tv_cmp_gt_u32 vcc, %8, %0\n\ts_cbranch_vccnz .Lw%=\n\t.subsection 1\n.Lw%=:\n\tds_read_u8 %0, %6\n\tds_read_b128 %2, %7 offset:%9\n\t"
          "ds_read_b128 %3, %7 offset:%9+1024\n\tds_read_b128 %4, %7 offset:%9+2048\n\tds_read_b128 %5, %7 offset:%9+3072\n\ts_waitcnt lgkmcnt(0)\n\t"
          "v_cmp_gt_u32 vcc, %8, %0\n\t" SAFE_OUT(".Lr%=") "s_cbranch_vccnz .Lw%=\n\ts_branch .Lr%=\n\t.subsection 0\n.Lr%=:"
          : "+v"(zv), NOP_(n), "+v"(zl[0]), "+v"(zl[1]), "+v"(zl[2]), "+v"(zl[3])
          : "v"(zf), "v"(zb), "n"(sec ? 11 : 10), "n"(sec ? SB : 0)
          : "memory", "vcc", "scc");
#pragma unroll
      for (int k = 0; k < 4; ++k)
        if (j + k < 9) z1[j + k] = zl[k];
    }
    if (j == NR1 - 1 || (j == NR1 + 3 && j < 8))
      asm volatile("ds_read_u8 %0, %5\n\tds_read_b128 %1, %6 offset:%7\n\tds_read_b128 %2, %6 offset:%7+1024\n\tds_read_b128 %3, %6 offset:%7+2048\n\t"
                   "ds_read_b128 %4, %6 offset:%7+3072"
                   : "=&v"(zv), "=&v"(zl[0]), "=&v"(zl[1]), "=&v"(zl[2]), "=&v"(zl[3])
                   : "v"(zf), "v"(zb), "n"(sec ? SB : 0)
                   : "memory");
    asm volatile("" : "+v"(z1[j]));
    u32x2 h[2];
#pragma unroll
    for (int mt = 0; mt < 2; ++mt) {
      const f32x4 a = acc[mt][j];
      h[mt] = u32x2{pack2v(f32x2{bf_lo(z1[j][2 * mt]), bf_hi(z1[j][2 * mt])} * f32x2{a[0], a[1]}),
                    pack2v(f32x2{bf_lo(z1[j][2 * mt + 1]), bf_hi(z1[j][2 * mt + 1])} * f32x2{a[2], a[3]})};
    }
    const auto s0 = __builtin_amdgcn_permlane16_swap(h[0][0], h[1][0], false, false);
    const auto s1 = __builtin_amdgcn_permlane16_swap(h[0][1], h[1][1], false, false);
    const u32x4 v = {s0[0], s1[0], s0[1], s1[1]};
    asm volatile("global_store_dwordx4 %0, %1, %2 offset:%3 sc1\n\ts_nop 1" ::"v"(ho), "v"(v), "s"(hp + (j / 2) * 4096), "n"((j % 2) * 2048) : "memory");
    __builtin_amdgcn_sched_barrier(0);
  }
  MK(18);
}

DEVI void k3_const(char* smem, char* ws, const u16* b_sp, int layer, int bid, int mi, int ni, int wm) {
  const int lane = lane_id(), l = lane < 54 ? lane : 53, p = l / 18;
  const char* kp = uni_ptr(ws + OFF_CSK + bid * 2048 + wm * 512);
  const unsigned q16 = (unsigned)(l - p * 18) * 16u;
  unsigned bs;
  f32x4 t;
  lds_spin((unsigned)(size_t)smem + (unsigned)(LW_OFF + (LW_T + layer) * 4), 12u);
  lds_spin((unsigned)(size_t)smem + (unsigned)(LW_OFF + LW_HELLO * 4), 2u);
  asm volatile("global_load_ushort %0, %1, %2" : "=v"(bs) : "v"((unsigned)(lane & 31) * 2u), "s"(uni_ptr((const char*)(b_sp + mi * 128 + wm * 32))) : "memory");
  if (layer) {
    asm volatile("global_load_dwordx4 %0, %1, %2 sc1\n\ts_waitcnt vmcnt(0)" : "=&v"(t) : "v"(q16), "s"(kp) : "memory");
  } else {
    const char* cp = uni_ptr(ws + OFF_CSP + ni * 1152 + wm * 288);
    f32x4 a[6];
#pragma unroll
    for (int i = 0; i < 6; ++i)
      asm volatile("global_load_dwordx4 %0, %1, %2 sc1" : "=v"(a[i]) : "v"(q16 + (unsigned)(p * (6 * F_ * 4))), "s"(cp + i * (F_ * 4)) : "memory");
    asm volatile("s_waitcnt vmcnt(0)" : "+v"(a[0]), "+v"(a[1]), "+v"(a[2]), "+v"(a[3]), "+v"(a[4]), "+v"(a[5]));
    t = ((a[0] + a[1]) + (a[2] + a[3])) + (a[4] + a[5]);
#pragma unroll
    for (int j = 0; j < 4; ++j)
      t[j] += __int_as_float(__builtin_amdgcn_ds_bpermute((lane + 18) * 4, __float_as_int(t[j]))) +
              __int_as_float(__builtin_amdgcn_ds_bpermute((lane + 36) * 4, __float_as_int(t[j])));
  }
  asm volatile("" : "+v"(bs));
  const unsigned lds0 = (unsigned)(size_t)smem;
  f32x4 t0 = wm < 2 ? f32x4{t[0], t[0], t[1], t[1]} : t, t1 = wm < 2 ? f32x4{t[2], t[2], t[3], t[3]} : t;
  unsigned a0 = lds0 + (unsigned)(wm < 2 ? K3_C2_OFF + wm * 576 + lane * 32 : K3_C1_OFF + (wm - 2) * 288 + lane * 16), a1 = a0 + (wm < 2 ? 16u : 0u);
  unsigned ab = lds0 + (unsigned)(K3_BS_OFF + (wm * 32 + (lane & 31)) * 4);
  bs <<= 16;
  asm volatile("" : "+v"(t0), "+v"(t1), "+v"(a0), "+v"(a1), "+v"(ab), "+v"(bs));
  bar();
  asm volatile("ds_write_b32 %4, %5\n\ts_mov_b64 exec, 0x3ffff\n\tds_write_b128 %0, %2\n\tds_write_b128 %1, %3\n\ts_mov_b64 exec, -1" ::"v"(a0), "v"(a1),
               "v"(t0), "v"(t1), "v"(ab), "v"(bs)
               : "memory");
  if (layer == 0 && lane < 24) asm volatile("global_store_dwordx4 %0, %1, %2\n\ts_nop 1" ::"v"((unsigned)lane * 16u), "v"(t), "s"(kp) : "memory");
}

DEVI void p3_g1(char* smem, char* __restrict__ ws, int layer, int mi, int ni, int wm, int lane MK_ARG) {
  const unsigned lds0 = (unsigned)(size_t)smem;
  const int g = lane >> 4, r = lane & 15;
  const unsigned lw = lds0 + (unsigned)(LW_OFF + layer * 4);
  const unsigned xrd = lds0 + (unsigned)(g * 2048 + (wm * 32 + r) * 16);
  s16x8 xc[4][2][2];
  {
    const char* xs = uni_ptr(ws + OFF_XS + (size_t)layer * 2 * SZ_XS + (size_t)(mi * 2 + (wm >> 1)) * 4096 + (wm & 1) * 256);
    f32x4 a, b;
    auto pick = [&](int t) {
      bar();
      asm volatile(
          "ds_read_b128 %0, %4\n\tds_read_b128 %1, %4 offset:256\n\tds_read_b128 %2, %4 offset:8192\n\tds_read_b128 %3, %4 offset:8448\n\t"
          "s_waitcnt lgkmcnt(0)"
          : "=&v"(xc[t][0][0]), "=&v"(xc[t][0][1]), "=&v"(xc[t][1][0]), "=&v"(xc[t][1][1])
          : "v"(xrd + (unsigned)(t * K3C::SB))
          : "memory");
    };
    __builtin_amdgcn_s_setprio(2);
    pick(0);
    pick(1);
    asm volatile("global_load_dwordx4 %0, %2, %3 sc1\n\tglobal_load_dwordx4 %1, %2, %3 offset:512 sc1"
                 : "=&v"(a), "=&v"(b)
                 : "v"((unsigned)(g * 1024 + r * 16)), "s"(xs)
                 : "memory");
    pick(2);
    pick(3);
    bar();
    asm volatile("s_waitcnt vmcnt(0)" : "+v"(a), "+v"(b) : : "memory");
    float s, q, mb, rinv;
    red8(a, b, s, q);
    bar();
    asm volatile("" : "+v"(s), "+v"(q));
    ln_finish(s, q, 1.0f / (float)D_, mb, rinv);
    asm volatile("" : "+v"(mb), "+v"(rinv));
    bar();
    asm volatile("" : "+v"(mb), "+v"(rinv));
    const float k0 = -2.0f * 0.7978845608028654f * 1.4426950408889634f, k1 = k0 * 0.044715f;
    float sd = __builtin_amdgcn_rcpf(rinv), c0 = k0 * rinv, c1 = k1 * rinv * rinv * rinv;
    asm volatile("" : "+v"(sd));
    asm volatile("" : "+v"(c0));
    asm volatile("" : "+v"(c1));
    const float e0 = lane < 32 ? -mb : c0, e1 = lane < 32 ? sd : c1;
    asm volatile("ds_write2st64_b32 %0, %1, %2 offset1:2" ::"v"(
                     lds0 + (unsigned)(K3_ST_OFF + (g >> 1) * 1024 + (wm * 32 + 2 * r + (g & 1)) * 4)),
                 "v"(e0), "v"(e1)
                 : "memory");
    bar();
    asm volatile("s_waitcnt lgkmcnt(0)" ::: "memory");
    bar();
    __builtin_amdgcn_s_setprio(0);
  }
  f32x4 ac[2][9];
#pragma unroll
  for (int mt = 0; mt < 2; ++mt)
#pragma unroll
    for (int j = 0; j < 9; ++j) ac[mt][j] = f32x4{0.f, 0.f, 0.f, 0.f};
  {
    UMath<true> gm(ac, xc);
    gm.xa = xrd;
    gm.wa = lds0 + (unsigned)(K3C::XB + (g * 8 + (r & 7)) * K3C::P + (r >> 3) * 16);
    gm.fa = lw + (unsigned)(LW_AD * 4 + (lane & 3));
    gm.fw = lw + (unsigned)(LW_S * 4 + wm);
    gm.fv = 0u;
    asm volatile("" : "+v"(gm.fa), "+v"(gm.fw), "+v"(gm.fv), "+v"(gm.xa), "+v"(gm.wa));
    __builtin_amdgcn_s_sleep(40);
    lds_spin4<4>(lw + LW_L * 4, 4u);
    gm.first();
    place();
    lds_spin4(lw + LW_AD * 4, 2u);
    __builtin_amdgcn_s_setprio(1);
    gm.run();
    __builtin_amdgcn_s_setprio(0);
  }
  MK(14);
  f32x2 rc[4];
  f32x4 cs[9];
  const int l2 = lane_id();
  {
    const unsigned ra = lds0 + (unsigned)(K3_ST_OFF + (wm * 32 + (l2 & 15)) * 4), ca = lds0 + (unsigned)(K3_C1_OFF + (l2 & 48));
#pragma unroll
    for (int k = 0; k < 4; ++k) asm volatile("ds_read2_b32 %0, %1 offset1:16" : "=v"(rc[k]) : "v"(ra + (unsigned)(k * 512)) : "memory");
#pragma unroll
    for (int j = 0; j < 9; ++j) asm volatile("ds_read_b128 %0, %1 offset:%2" : "=v"(cs[j]) : "v"(ca), "n"(j * 64) : "memory");
    asm volatile("s_waitcnt lgkmcnt(0)" : "+v"(rc[0]), "+v"(rc[1]), "+v"(rc[2]), "+v"(rc[3]));
#pragma unroll
    for (int j = 0; j < 9; ++j) asm volatile("" : "+v"(cs[j]));
  }
  const unsigned za = lds0 + (unsigned)(K3_Z1_OFF + wm * 5120 + l2 * 16);
  u32x4 z1[9];
  unsigned zf;
#pragma unroll
  for (int t = 0; t < 18; ++t) {
    const int j = t / 2, mt = t % 2;
    float u[4], w[4];
#define Q4(A, B, C, D) A "\n\t" B "\n\t" C "\n\t" D "\n\t"
    asm volatile(Q4("v_fma_f32 %0, %8, %12, %16", "v_fma_f32 %1, %8, %13, %17", "v_fma_f32 %2, %8, %14, %18", "v_fma_f32 %3, %8, %15, %19")
                 Q4("v_mul_f32 %4, %0, %0", "v_mul_f32 %5, %1, %1", "v_mul_f32 %6, %2, %2", "v_mul_f32 %7, %3, %3")
                 Q4("v_fma_f32 %4, %4, %11, %10", "v_fma_f32 %5, %5, %11, %10", "v_fma_f32 %6, %6, %11, %10", "v_fma_f32 %7, %7, %11, %10")
                 Q4("v_mul_f32 %4, %4, %0", "v_mul_f32 %5, %5, %1", "v_mul_f32 %6, %6, %2", "v_mul_f32 %7, %7, %3")
                 Q4("v_exp_f32 %4, %4", "v_exp_f32 %5, %5", "v_exp_f32 %6, %6", "v_exp_f32 %7, %7")
                 Q4("v_fma_f32 %4, %4, %9, %9", "v_fma_f32 %5, %5, %9, %9", "v_fma_f32 %6, %6, %9, %9", "v_fma_f32 %7, %7, %9, %9")
                 Q4("v_rcp_f32 %4, %4", "v_rcp_f32 %5, %5", "v_rcp_f32 %6, %6", "v_rcp_f32 %7, %7")
                 Q4("v_mul_f32 %4, %4, %0", "v_mul_f32 %5, %5, %1", "v_mul_f32 %6, %6, %2", "v_mul_f32 %7, %7, %3")
                 : "=&v"(u[0]), "=&v"(u[1]), "=&v"(u[2]), "=&v"(u[3]), "=&v"(w[0]), "=&v"(w[1]), "=&v"(w[2]), "=&v"(w[3])
                 : "v"(rc[0][mt]), "v"(rc[1][mt]), "v"(rc[2][mt]), "v"(rc[3][mt]), "v"(cs[j][0]), "v"(cs[j][1]), "v"(cs[j][2]), "v"(cs[j][3]),
                   "v"(ac[mt][j][0]), "v"(ac[mt][j][1]), "v"(ac[mt][j][2]), "v"(ac[mt][j][3]));
    z1[j][2 * mt] = pack2(w[0], w[1]);
    z1[j][2 * mt + 1] = pack2(w[2], w[3]);
    if (mt && j < NR1) asm volatile("ds_write_b128 %0, %1 offset:%2" ::"v"(za), "v"(z1[j]), "n"(j * 1024) : "memory");
    if (t == 9) asm volatile("s_waitcnt lgkmcnt(0)" ::: "memory");
    __builtin_amdgcn_sched_barrier(0);
    if (t == 2 * ZQ - 7)
      asm volatile("global_load_dword %0, %1, %2 sc1"
                   : "=v"(zf)
                   : "v"(vconst(0u)), "s"(uni_ptr(ws + OFF_CNT + (size_t)((CN_Z2 + layer) * 256 + mi * 16 + ni) * 128))
                   : "memory");
    if (t == 2 * ZQ - 4) {
      asm volatile("s_waitcnt vmcnt(0)" : "+v"(zf) : : "memory");
      if (wm == 1 && __builtin_amdgcn_readfirstlane(zf) != 15u - ZQ) cnt_wait(ws, CN_Z2 + layer, mi * 16 + ni, 16u - ZQ);
    }
    bar();
    __builtin_amdgcn_sched_barrier(0);
  }
  MK(15);
#pragma unroll 1
  for (int t = 20; t < 32; ++t) bar();
  __builtin_amdgcn_s_sleep(9);
#pragma unroll
  for (int k = NR1; k < 9; ++k) {
    if (k == NR1 + 4) lds_spin4(lw + LW_AD * 4, 7u);
    asm volatile("ds_write_b128 %0, %1 offset:%2" ::"v"(lds0 + (unsigned)(wm * 4096 + l2 * 16)), "v"(z1[k]),
                 "n"(((k - NR1) / 4) * K3C::SB + ((k - NR1) % 4) * 1024)
                 : "memory");
    if (k == NR1 + 3 || k == 8) asm volatile("ds_write_b8 %0, %1" ::"v"(lw + (unsigned)(LW_S * 4 + wm)), "v"(vconst(k == NR1 + 3 ? 10u : 11u)) : "memory");
  }
}

constexpr int W5A = 22;
constexpr int NPF = 30;
struct Loader {
  u32x4 pf[NPF];
  unsigned lds0, lane16;
  int wl, lane;

  template <int R0, int N, int P0, int PE>
  DEVI void pf_load(const char* src) {
    const unsigned o = lane16 + (unsigned)(wl * 1024);
#pragma unroll
    for (int t = 0; t < N; ++t) {
      unsigned a = o + (unsigned)((P0 + 4 * t) * 1024);
      if (P0 + 4 * t + 3 >= PE && P0 + 4 * t + wl >= PE) a = lane16;
      asm volatile("global_load_dwordx4 %0, %1, %2 sc1" : "=v"(pf[R0 + t]) : "v"(a), "s"(src) : "memory");
    }
  }
  template <int R0, int N>
  DEVI void pf_pin() {
#pragma unroll
    for (int t = 0; t < N; ++t) asm volatile("" : "+v"(pf[R0 + t]));
  }
  template <int R0, int N>
  DEVI void pf_undef() {
#pragma unroll
    for (int t = 0; t < N; ++t) asm volatile("" : "=v"(pf[R0 + t]));
  }
  template <int PB, int A, int N>
  DEVI void pf_put(unsigned dst) {
    constexpr int T0 = (A - PB) / 4, T1 = (A + N - 1 - PB) / 4;
    const unsigned a = lds0 + dst + lane16 + (unsigned)((wl + PB + 4 * T0 - A + 4) * 1024) - 4096u;
#pragma unroll
    for (int t = T0; t <= T1; ++t) {
      const int p0 = PB + 4 * t;
      if ((p0 >= A || p0 + wl >= A) && (p0 + 3 < A + N || p0 + wl < A + N))
        asm volatile("ds_write_b128 %0, %1 offset:%2" ::"v"(a), "v"(pf[t]), "n"((t - T0) * 4096) : "memory");
    }
  }
  template <int N>
  DEVI void dma_run(const char* src, unsigned dst) {
    constexpr int B = N / 4, R = N % 4;
    const unsigned f = (unsigned)(wl * B + (wl < R ? wl : R)) * 1024u, o = lane16 + f, m = lds0 + dst + f;
    dma_seq<B>(src, o, m);
    if (wl < R) dma16_sc1(src + B * 1024, o, m + (unsigned)(B * 1024));
  }
  template <int V>
  DEVI void wait_w() {
    asm volatile("s_cmp_lt_u32 %0, 2\n\ts_cbranch_scc1 1f\n\ts_waitcnt vmcnt(%1)\n1:\n\ts_waitcnt vmcnt(%2)" ::"s"(wl), "n"(V - 1), "n"(V) : "memory", "scc");
  }
  template <int V>
  DEVI void wbar() {
    asm volatile("s_waitcnt vmcnt(%0) lgkmcnt(0)" ::"n"(V) : "memory");
    bar();
  }

  DEVI void p1(char* smem, char* ws, int bid, int rt, int hp MK_ARG) {
    constexpr int SB = K1C::SB;
    static_assert(K1C::XB == 18 * 1024 && K1C::WB == 14 * 1024, "K1 shape");
    const char* wp = uni_ptr(ws + OFF_PQ + (size_t)hp * (9 * K1C::WB) + 4 * K1C::WB);
    static_assert(3 * SB + K1C::XB >= P5_W + 5 * 18432, "free while the GEMM before ends");
    const char* wq = wp - 4 * K1C::WB;
    dma_run<14>(wq + 3 * K1C::WB, (unsigned)(3 * SB + K1C::XB));
    dma_run<28>(wp, (unsigned)K1_WX);
    if (wl == 3) dma16_sc1(uni_ptr(ws + OFF_CSQ + bid * 512), lane16 & 511u, lds0 + (unsigned)K1_CS_OFF);
    __builtin_amdgcn_s_setprio(0);
    wait_vm<0>();
    const bool lo = wl < 2;
    const int x0 = lo ? wl * 5 : wl * 4 + 2, w0 = lo ? wl * 3 : wl * 4 - 2;
    unsigned xo[5];
#pragma unroll
    for (int i = 0; i < 5; ++i) {
      const int c = (x0 + i) * 64 + lane;
      const int ch = c / 144;
      int gr = rt * 128 - 16 + (c - ch * 144);
      if (gr < 0) gr = 0;
      xo[i] = (unsigned)(((gr >> 6) * 72 + ch + 3 - (i & 3)) * 1024 + (gr & 63) * 16);
      asm volatile("" : "+v"(xo[i]));
    }
    const char* xp = uni_ptr(ws + OFF_X1 + SZ_X - 3072);
    wp += 2 * K1C::WB + w0 * 1024;
    auto dma_x = [&](unsigned so) {
      const unsigned m = lds0 + so + (unsigned)(x0 * 1024);
      dma4<4, true>(xp, xo[0], xo[1], xo[2], xo[3], m);
      if (lo) dma16_sc1(xp, xo[4], m + 4096u);
      xp += 8192;
    };
    auto dma_w = [&](unsigned so) {
      const unsigned m = lds0 + so + (unsigned)(K1C::XB + w0 * 1024);
      dma_seq<3>(wp, lane16, m);
      if (!lo) dma16_sc1(wp + 3072, lane16, m + 3072u);
      wp += K1C::WB;
    };
    const char* xs = uni_ptr(ws + OFF_XS + SZ_XS);
    const int g = lane >> 4, p2 = 64 + (lane & 7);
    int ra = rt * 128 - 16 + wl * 32 + 2 * (lane & 15);
    if (ra < 0) ra = 0;
    unsigned sa = (unsigned)(((ra >> 6) * 8 + g * 2) * 512 + (ra & 63) * 8);
    unsigned sb = (unsigned)(((rt * 2 + 1) * 8 + (lane >> 3)) * 512 + (2 * p2 - 80) * 8);
    unsigned sw = lds0 + (unsigned)(K1_ST_OFF + (2 * (lane < 32 ? wl * 16 + (lane & 15) : p2) + (g & 1)) * 8);
    asm volatile("" : "+v"(sa), "+v"(sb), "+v"(sw));
    MK(22);
    __builtin_amdgcn_s_setprio(3);
    bar();
    dma_run<14>(wq, (unsigned)K1C::XB);
    dma_run<14>(wq + K1C::WB, (unsigned)(SB + K1C::XB));
    dma_run<14>(wq + 2 * K1C::WB, (unsigned)(2 * SB + K1C::XB));
    MK(1);
    bar();
    dma_x(0u);
    dma_x((unsigned)SB);
    wbar<4>();
    MK(29);
    dma_x((unsigned)(2 * SB));
    dma_x((unsigned)(3 * SB));
    wbar<8>();
    MX(1, 3);
    dma_x(0u);
    wbar<8>();
    MX(1, 4);
    dma_x((unsigned)SB);
    f32x4 a, b, e;
    asm volatile("global_load_dwordx4 %0, %3, %5 sc1\n\tglobal_load_dwordx4 %1, %3, %5 offset:512 sc1\n\tglobal_load_dwordx4 %2, %4, %5 sc1"
                 : "=&v"(a), "=&v"(b), "=&v"(e)
                 : "v"(sa), "v"(sb), "s"(xs)
                 : "memory");
    wbar<11>();
    MX(1, 5);
    dma_w((unsigned)SB);
    dma_x((unsigned)(2 * SB));
    wbar<15>();
    MX(1, 6);
    dma_w((unsigned)(2 * SB));
    dma_x((unsigned)(3 * SB));
    wbar<19>();
    MX(1, 7);
    dma_w((unsigned)(3 * SB));
    dma_x(0u);
    wbar<16>();
    MX(1, 8);
    asm volatile("" : "+v"(a), "+v"(b), "+v"(e));
    float s, q;
    {
      float t[4] = {e[0], e[1], e[2], e[3]}, s2, q2;
      asm volatile(
          "v_add_f32_dpp %0, %0, %0 row_ror:8 row_mask:0xf bank_mask:0xf\n\tv_add_f32_dpp %1, %1, %1 row_ror:8 row_mask:0xf bank_mask:0xf\n\t"
          "v_add_f32_dpp %2, %2, %2 row_ror:8 row_mask:0xf bank_mask:0xf\n\tv_add_f32_dpp %3, %3, %3 row_ror:8 row_mask:0xf bank_mask:0xf\n\ts_nop 1"
          : "+v"(t[0]), "+v"(t[1]), "+v"(t[2]), "+v"(t[3]));
      red4(t, s2, q2);
      red8(a, b, s, q);
      s = lane < 32 ? s : s2;
      q = lane < 32 ? q : q2;
      asm volatile("" : "+v"(s), "+v"(q));
    }
    wbar<8>();
    MX(1, 9);
    {
      asm volatile("" : "+v"(s), "+v"(q));
      float mb, rinv;
      ln_finish(s, q, 1.0f / (float)D_, mb, rinv);
      asm volatile("ds_write_b64 %0, %1" ::"v"(sw), "v"(f32x2{-mb, rinv}) : "memory");
    }
    wbar<0>();
    MK(30);
  }

  template <int I>
  DEVI void p5_step(const char* wp) {
    constexpr int T = 2 * I + 1;
    wbar<(T == 1 ? 63 : (T <= 11 ? 3 : 0))>();
    if constexpr (T <= 9) {
      const unsigned wo = (unsigned)(12288 + (wl & 1) * 3072), so = (unsigned)(P5_W + ((T + 2 + (wl >> 1)) & 7) * 18432);
      dma_seq<3>(wp + (T + 7) * 18432, lane16 + wo + (unsigned)((wl >> 1) * 18432), lds0 + so + wo);
      pf_put<0, 12 * (T - 1), 12>((unsigned)(P5_W + ((T + 2) % 8) * 18432));
      pf_put<0, 12 * T, 12>((unsigned)(P5_W + ((T + 3) % 8) * 18432));
    }
  }
  template <int... S>
  DEVI void p5_steps(const char* wp, std::integer_sequence<int, S...>) {
    (p5_step<S>(wp), ...);
  }

  template <int T0, int T1>
  DEVI void w5_load(const char* w5p) {
    const unsigned o = lane16 + (unsigned)(wl * 1024);
#pragma unroll
    for (int t = T0; t < T1; ++t)
      asm volatile("global_load_dwordx4 %0, %1, %2 sc1" : "=v"(pf[t]) : "v"(o + (unsigned)((t / 3 + 8) * 18432 + (t % 3) * 4096)), "s"(w5p) : "memory");
  }
  DEVI void p35(char* smem, char* ws, const u16* w_sp, int layer, int bid, int mi, int ni, int rb, int ct, int hp MK_ARG) {
    using C = K3C;
    constexpr int NS = C::NS, SB = C::SB, NDX = 4, NDW = 5, VPS = NDX + NDW;
    const char* wip = uni_ptr(ws + OFF_PI + (size_t)ni * (18 * C::WB));
    if (layer) {
      pf_put<-48, 36, 18>((unsigned)(2 * SB + C::XB));
      pf_put<-48, 54, 18>((unsigned)(3 * SB + C::XB));
    } else {
      dma_run<18>(wip + 2 * C::WB, (unsigned)(2 * SB + C::XB));
      dma_run<18>(wip + 3 * C::WB, (unsigned)(3 * SB + C::XB));
    }
    dma_run<18>(wip + C::WB, (unsigned)(SB + C::XB));
    const char* w5p = uni_ptr(ws + OFF_PW + (size_t)ct * (36 * 9216));
    const int w0 = wl < 2 ? wl * 5120 : wl * 4096 + 2048;
    const unsigned m0x = lds0 + (unsigned)(wl * 4096), m0w = lds0 + (unsigned)(C::XB + w0);
    const unsigned wo = lane16 + (unsigned)w0;
    const unsigned lw = lds0 + (unsigned)(LW_OFF + layer * 4), me = 1u << (8 * wl);
    const unsigned wo4 = wo + 4096u;
    auto dma_w = [&](const char* wp, unsigned so) {
      dma_seq<4>(wp, wo, m0w + so);
      if (wl < 2) dma16_sc1(wp, wo4, m0w + so + 4096u);
    };
    {
      const char* xp = uni_ptr(ws + OFF_X1 + (size_t)layer * 2 * SZ_X + (size_t)(mi * 2 * 72 + wl * 2) * 1024 - 1024);
      const unsigned xo0 = lane16 + 1024u, xo1 = lane16 + 72u * 1024u, xo3 = lane16 + 71u * 1024u;
      auto dma_x = [&](int k, unsigned so) { dma4<4, true>(xp + k * 8192, xo0, xo1, lane16, xo3, m0x + so); };
      MK(10);
      bar();
      dma_x(0, 0u);
      dma_x(1, (unsigned)SB);
      wait_vm<4>(); MX(3, 2); wbar<4>(); MX(2, 2);
      MK(25);
      {
        dma_x(2, (unsigned)(2 * SB));
        dma_x(3, (unsigned)(3 * SB));
        dma_x(4, (unsigned)(4 * SB));
        wbar<12>(); MX(2, 3);
        dma_x(5, 0u);
        wait_vm<12>();
      }
      bar(); MX(2, 4);
      dma_w(wip + 4 * C::WB, 0u);
      dma_x(6, (unsigned)SB);
      wait_w<8 + VPS>();
#pragma unroll 1
      for (int t = 1; t < 3; ++t) {
        bar(); MX(2, 4 + t);
        dma_w(wip + (t + 4) * C::WB, (unsigned)(t * SB));
        dma_x(t + 6, (unsigned)((t + 1) * SB));
        wait_w<NDX + VPS>();
      }
      bar(); MX(2, 7);
      dma_w(wip + 7 * C::WB, (unsigned)(3 * SB));
      wait_w<VPS>();
      bar(); MX(2, 8);
      dma_w(wip + 8 * C::WB, 0u);
      wait_w<NDW>();
      bar(); MX(2, 9);
      const char* wp = wip + 9 * C::WB;
      dma_w(wp + b_k(0) * C::WB, (unsigned)SB);
      if (layer == 0) {
        const char* sp = uni_ptr((const char*)w_sp + (size_t)(bid * 4 + wl) * 8192);
        const unsigned so = (unsigned)((lane >> 5) * 4096 + (lane & 31) * 16);
#pragma unroll
        for (int i = 0; i < WSP_V; ++i) dma16(sp + i * 512, so, lds0 + (unsigned)K3_DM_OFF);
        dma16(sp + (bid >> 4) * 256, so & ~0x100u, lds0 + (unsigned)K3_DM_OFF);
      }
      w5_load<0, 4>(w5p);
      wait_w<NDW + 4>();
      MX(3, 10); bar(); MX(2, 10);
      dma_w(wp + b_k(1) * C::WB, (unsigned)(2 * SB));
      w5_load<4, 8>(w5p);
      MK(26);
      lds_spin4(lw + LW_AD * 4, 1u);
      dma_w(wp + b_k(2) * C::WB, (unsigned)(3 * SB));
      dma_w(wp + b_k(3) * C::WB, 0u);
      w5_load<8, W5A>(w5p);
      wait_vm<W5A - 8>();
      lds_inc(lw + LW_L * 4, 4u * me);
      w5_load<W5A, 30>(w5p);
      MK(11);
    }
    {
      unsigned xoff[NDX];
#pragma unroll
      for (int i = 0; i < NDX; ++i) {
        const int c = wl * 256 + i * 64 + lane;
        const int row = c >> 3, cc = c & 7;
        xoff[i] = (unsigned)((mi * 128 + row) * S_ * 2 - i * 1024) + (unsigned)((cc ^ ((row >> 1) & 7)) * 16);
      }
      auto dma_x = [&](const char* xp, unsigned so) { dma4<4, false>(xp, xoff[0], xoff[1], xoff[2], xoff[3], m0x + so); };
      const char* xp = uni_ptr((const char*)w_sp);
      const char* wp = uni_ptr(ws + OFF_Z2 + (size_t)layer * SZ_Z + (size_t)ni * S_ * 288);
      static_for<4, 7>([&](auto T_) {
        constexpr int t = T_;
        lds_spin4<2>(lw + LW_S * 4, (unsigned)(t - 3));
        dma_w(wip + (9 + b_k(t)) * C::WB, (unsigned)(((t + 1) % 4) * SB));
        if constexpr (t > 4) {
          dma_x(xp + mi * 256 + (t - 5) * 128, (unsigned)((t - 3) * SB));
          wait_w<NDW + (t - 4) * NDX>();
          lds_inc(lw + LW_L * 4, me);
        }
      });
      wait_vm<NDX>();
      lds_inc(lw + LW_L * 4, me);
      wait_vm<0>();
      lds_inc(lw + LW_L * 4, me);
      pf_pin<0, 30>();
      MK(13);
      lds_spin4<2>(lw + LW_AD * 4, 5u);
      MK(24);
      int left = 2 * mi;
      auto next = [&]() {
        const int d = --left == 0 ? 3 : 1;
        xp += d * 128;
        wp += d * C::WB;
      };
      if (mi == 0) {
        xp += 256;
        wp += 2 * C::WB;
      }
      if (wl == 0) {
        cnt_wait(ws, CN_Z + layer, mi * 16 + ni, (unsigned)ZQ);
        lds_wr32(lw + LW_GO * 4, 1u);
      }
      lds_spin4<2>(lw + LW_AD * 4, 6u);
      dma_x(xp, 0u);
      dma_x(xp + 128, (unsigned)SB);
      spin_ge<2>(lw + LW_GO * 4, 1u);
      lds_spin4<2>(lw + LW_S * 4, 8u);
      dma_w(wp, 0u);
      lds_spin4<2>(lw + LW_S * 4, 9u);
      dma_w(wp + C::WB, (unsigned)SB);
      next();
      next();
      unsigned slot = (unsigned)(2 * SB);
      auto cissue = [&]() {
        dma_x(xp, slot);
        dma_w(wp, slot);
        next();
        slot = (slot == (unsigned)((NS - 1) * SB)) ? 0u : slot + (unsigned)SB;
      };
      auto cstep = [&]() {
        bar();
        cissue();
      };
      wait_w<NDW>();
      MK(15);
      int j = 2;
      if (layer == 0) {
        const char* sp = uni_ptr((const char*)w_sp + (size_t)(bid * 4 + wl) * 8192 + WSP_V * 512);
        unsigned d = 0u;
#pragma unroll 1
        for (; j < 2 + 32 - 4 * WSP_V; ++j) {
          cstep();
          asm volatile("s_load_dword %0, %1, 0\n\ts_load_dword %0, %1, 64\n\ts_load_dword %0, %1, 0x1000\n\ts_load_dword %0, %1, 0x1040"
                       : "+s"(d)
                       : "s"(sp));
          sp += 128;
          wait_w<VPS>();
        }
#pragma unroll 1
        for (int e = j + 4; j < e; ++j) {
          cstep();
          asm volatile("" : "+s"(d));
          wait_w<VPS>();
        }
        asm volatile("s_waitcnt lgkmcnt(0)" : "+s"(d));
      }
      place();
#pragma unroll 1
      for (; j <= 29; ++j) {
        cstep();
        wait_w<VPS>();
      }
      bar();
    }
    dma_run<33>(w5p, (unsigned)(P5_W + 54 * 1024));
    dma_run<23>(w5p + 67 * 1024, (unsigned)(P5_W + 121 * 1024));
    if (wl == 3) dma16_sc1(w5p + 143 * 1024, lane16, lds0 + (unsigned)(P5_W + 53 * 1024));
    wait_vm<14>();
    bar();
    dma_run<34>(w5p + 33 * 1024, (unsigned)(P5_W + 87 * 1024));
    wait_vm<0>();
    bar();
    if (wl) {
      const unsigned f = (unsigned)(wl - 1) * 18432u;
      dma_seq<17>(w5p + 90 * 1024, lane16 + f, lds0 + (unsigned)P5_W + f);
      if (wl < 3) dma16_sc1(w5p + 107 * 1024, lane16 + f, lds0 + (unsigned)(P5_W + 17 * 1024) + f);
    } else {
      cnt_wait(ws, CN_H + layer, rb * 8 + ct, 8u);
    }
    MK(20);
    bar();
    p5_steps(w5p, std::make_integer_sequence<int, 7>{});
    if (layer == 0) {
      if (bid < 24 && wl == 0 && lane < 8) pk_st16(ws + OFF_HELLO + bid * 128 + lane16, u32x4{0u, 0u, 0u, 0u});
    }
  }
};

__global__ __launch_bounds__(768, 3) void k_main(const u16* __restrict__ x0, const u16* __restrict__ wqkv, const u16* __restrict__ wo,
                                                 const u16* __restrict__ w_in, const u16* __restrict__ w_sp,
                                                 const u16* __restrict__ b_sp, const u16* __restrict__ w_out,
                                                 u16* __restrict__ out, char* ws) {
  extern __shared__ __attribute__((aligned(16))) char smem[];
  const char* code0 = code_here();
  (void)code0;
  const unsigned lds0 = (unsigned)(size_t)smem;
  int wave = __builtin_amdgcn_readfirstlane((int)threadIdx.x >> 6);
  int bid = blockIdx.x;
#define FRESH() asm volatile("" : "+s"(bid), "+s"(ws), "+s"(wave))
#define XCD (bid & 7)
#define IDX (bid >> 3)
#define MI ((XCD >> 2) * 8 + (IDX >> 2))
#define NI ((XCD & 1) * 8 + (XCD & 2) * 2 + (IDX & 3))
#define RB (XCD * 4 + (IDX >> 3))
#define CT (IDX & 7)
#define RT ((XCD >> 1) * 4 + (IDX >> 3))
#define HP ((XCD & 1) * 8 + (IDX & 7))
  MK(0);
  if (wave < 8) {
    constexpr int PER = (2 * NL_MAIN + 31) / 32;
    static_assert(PER <= 64 && 31 * PER < 2 * NL_MAIN, "one load");
    unsigned o, last, st, t;
    const char* p;
    asm volatile(
        "s_lshr_b32 %3, %5, 3\n\t"
        "s_cmp_eq_u32 %3, 31\n\ts_cselect_b32 %1, 63, 0\n\ts_cselect_b32 %2, %7, %6\n\t"
        "s_mul_i32 %0, %3, %8\n\t"
        "s_cmp_eq_u32 %4, 2\n\ts_cselect_b32 %0, %0, 0\n\ts_cselect_b32 %1, %2, %1\n\t"
        "s_mul_i32 %3, %3, 0xf300\n\ts_add_u32 %3, %3, 0x200\n\t"
        "s_cmp_eq_u32 %4, 3\n\ts_cselect_b32 %0, %3, %0\n\ts_cselect_b32 %1, 53, %1\n\ts_cselect_b32 %2, 0x480, 64\n\t"
        "s_mul_i32 %3, %5, 0x1e60\n\t"
        "s_cmp_eq_u32 %4, 5\n\ts_cselect_b32 %0, %3, %0\n\t"
        "s_add_u32 %3, %3, 0x1000\n\t"
        "s_cmp_eq_u32 %4, 4\n\ts_cselect_b32 %0, %3, %0\n\ts_cselect_b32 %1, 57, %1\n\t"
        "s_cmp_ge_u32 %4, 5\n\ts_cselect_b32 %1, 63, %1\n\t"
        "s_mul_i32 %3, %5, 0x2400\n\t"
        "s_cmp_eq_u32 %4, 7\n\ts_cselect_b32 %0, %3, %0\n\t"
        "s_add_u32 %3, %3, 0x1000\n\t"
        "s_cmp_eq_u32 %4, 6\n\ts_cselect_b32 %0, %3, %0\n\t"
        "s_add_u32 %3, %3, 0x1000\n\t"
        "s_cmp_eq_u32 %4, 1\n\ts_cselect_b32 %0, %3, %0\n\ts_cselect_b32 %1, 15, %1"
        : "=&s"(o), "=&s"(last), "=&s"(st), "=&s"(t)
        : "s"(wave), "s"(bid), "n"(PER - 1), "n"(2 * NL_MAIN - 1 - 31 * PER), "n"(PER * 64)
        : "scc");
    const unsigned l = (unsigned)lane_id();
    unsigned a = o + (l < last ? l : last) * st;
    asm volatile("" : "+v"(a));
    asm volatile("s_cmp_lg_u32 %1, 2\n\ts_cbranch_scc1 1f\n\ts_mov_b32 m0, %3\n\ts_nop 0\n\tglobal_load_lds_dword %0, %2\n\tv_mov_b32 %0, 0\n1:"
                          : "+v"(a)
                          : "s"(wave), "s"(code0), "s"(lds0 + (unsigned)K1R_DUMMY)
                          : "memory", "scc");
    asm volatile("s_cmp_eq_u32 %1, 2\n\ts_cselect_b64 %0, %4, %5\n\ts_bitcmp1_b32 0x38, %1\n\ts_cselect_b64 %0, %3, %0\n\t"
                 "s_bitcmp1_b32 0xc2, %1\n\ts_cselect_b64 %0, %2, %0"
                 : "=&s"(p)
                 : "s"(wave), "s"(x0), "s"(wqkv), "s"(wo), "s"(b_sp)
                 : "scc");
    touch64(p, a, lds0 + (unsigned)K1R_DUMMY);
    MF(12);
    math_start(lds0, ws, bid, wave, (int)l);
    MF(13);
  }

  if (__builtin_expect(wave >= 8, 1)) {
    __builtin_amdgcn_s_setprio(3);
    {
      const int lane = lane_id();
      RawLd rl;
      int job, pj;
      const char* pg;
      rl.front(x0, wqkv, wo, ws, bid, lds0, wave - 8, lane, RT, HP, pj, [&]() {
        int b = bid;
        asm volatile("" : "+s"(b));
        const int xcd = b & 7, c16 = b >> 4, hp = (b & 1) * 8 + ((b >> 3) & 7);
        const bool cross = (((hp * 72) & ~15) & 127) > 48;
        const int cj = (int)((0x65743210u >> (4 * xcd)) & 7u) * 16 + c16;
        job = cross ? (cj < 88 ? 128 + cj : 999) : xcd * 16 + c16;
        pj = cross ? cj - 88 : ((xcd & 5) == 0 && (c16 & 3) == (c16 >> 2) ? 40 + xcd * 2 + (c16 >> 2) : -1);
        const int k = b >> 3;
        pg = k < 11 ? (const char*)w_in + k * 0x80000 : k < 17 ? (const char*)w_out + (k - 11) * 0x80000 : k < 22 ? (const char*)out + (k - 17) * 0x80000
             : (const char*)w_sp + (k < 30 ? (k - 22) * 0x100000 : 0x7ffff0);
      }, pg MF_PASS);
      if (job < 216) k1_raw_loader<true>(rl, w_in, w_out, ws, rl.tag, bid, lds0, wave - 8, lane, RT, HP, CT, job, pj MF_PASS);
      else k1_raw_loader<false>(rl, w_in, w_out, ws, rl.tag, bid, lds0, wave - 8, lane, RT, HP, CT, job, pj MF_PASS);
    }
    Loader ld;
    ld.lds0 = lds0;
#pragma unroll 1
    for (int layer = 0; layer < 2; ++layer) {
      FRESH();
      ld.wl = wave - 8;
      ld.lane = lane_id();
      ld.lane16 = (unsigned)ld.lane * 16u;
      const char* wip = uni_ptr(ws + OFF_PI + (size_t)NI * (18 * K3C::WB));
      if (layer) {
        ld.p1(smem, ws, bid, RT, HP MK_PASS);
        ld.pf_load<0, 21, 0, 81>(uni_ptr(ws + OFF_PO + (size_t)CT * (18 * 4608)));
        ld.pf_load<21, 9, 36, 72>(wip);
        wait_vm<9>();
        ld.pf_pin<0, 21>();
        lds_spin(lds0 + (unsigned)(LW_OFF + (LW_T + layer) * 4), 8u);
        __builtin_amdgcn_s_sleep(4);
        ld.pf_put<0, 0, 40>((unsigned)P2_W);
        wait_vm<0>();
        ld.pf_pin<21, 9>();
        bar();
        ld.pf_put<0, 40, 41>((unsigned)(P2_W + 40 * 1024));
      } else {
        ld.pf_undef<21, 9>();
        MK(3);
        MK(4);
        bar();
        ld.dma_run<81>(uni_ptr(ws + OFF_PO + (size_t)CT * (18 * 4608)), (unsigned)P2_W);
      }
      if (layer == 0 && ld.wl == 0) cnt_wait(ws, CN_O + layer, RB * 8 + CT, 16u);
      bool pk = false;
      if (layer == 0 && ld.wl == 1) {
        unsigned f;
        asm volatile("global_load_dword %0, %1, %2 sc1\n\ts_waitcnt vmcnt(0)"
                     : "=&v"(f)
                     : "v"(vconst(0u)), "s"(uni_ptr(ws + OFF_CNT + (size_t)(CN_PK * 256 + 8 + NI) * 128))
                     : "memory");
        pk = __builtin_amdgcn_readfirstlane(f) == 35u;
        if (pk) lds_wr32(lds0 + (unsigned)(LW_OFF + LW_HELLO * 4), 2u);
      }
      MK(6);
      asm volatile("s_waitcnt vmcnt(0) lgkmcnt(0)" ::: "memory");
      bar();
      lds_spin(lds0 + (unsigned)(LW_OFF + (LW_T + layer) * 4), 12u);
      if (layer == 0) {
        if (ld.wl == 1) {
          if (!pk) cnt_wait(ws, CN_PK, 8 + NI, 36u);
          lds_wr32(lds0 + (unsigned)(LW_OFF + LW_HELLO * 4), 2u);
        } else {
          lds_spin(lds0 + (unsigned)(LW_OFF + LW_HELLO * 4), 2u);
        }
        MK(7);
      }
      static_assert(2 * K3C::SB <= P2_W, "free during the first residual GEMM");
      ld.dma_run<18>(wip, (unsigned)K3C::XB);
      MX(6, 24);
      if (layer == 0 && ld.wl == 0) cnt_wait(ws, CN_PK, 24 + CT, 72u);
      wait_vm<0>();
      ld.pf_pin<21, 9>();
      MX(6, 25);
      bar();
      FRESH();
      ld.wl = wave - 8;
      ld.p35(smem, ws, w_sp, layer, bid, MI, NI, RB, CT, HP MK_PASS);
    }
  } else {
#pragma unroll 1
    for (int layer = 0; layer < 2; ++layer) {
      FRESH();
      {
        const int lane = lane_id(), r0 = RT * 128;
        float* csq = reinterpret_cast<float*>(ws + OFF_CSQ + bid * 512);
        Attn at;
        at.setup(lds0 + (unsigned)(layer ? K1_T_OFF : K1R_T_OFF), ws + OFF_O + (size_t)layer * SZ_X, r0, HP, wave, lane);
        if (layer) {
          if ((wave & 2) == 0) k1_gemm<2, 4>(smem, ws, RT, HP, (wave & 1) * 2 + (wave & 4), 0, wave >= 4, lane MK_PASS);
          else if (wave < 4) k1_gemm<3, 3>(smem, ws, RT, HP, (wave & 1) * 3, 4, false, lane MK_PASS);
          else if (wave == 6) k1_gemm<2, 3>(smem, ws, RT, HP, 6, 4, true, lane MK_PASS);
          else k1_gemm<1, 7>(smem, ws, RT, HP, 8, 0, true, lane MK_PASS);
        } else {
          if (wave == 0) k1_gemm_raw<3, false>(smem, HP, 0, lane MF_PASS);
          else if (wave < 4) k1_gemm_raw<2, false>(smem, HP, 1 + 2 * wave, lane MF_PASS);
          else if (wave < 7) k1_gemm_raw<2, true>(smem, HP, 2 * (wave - 4), lane MF_PASS);
          else k1_gemm_raw<3, true>(smem, HP, 6, lane MF_PASS);
        }
        msync(lds0 + (unsigned)(LW_OFF + (LW_T + layer) * 4));
        MK(3);
        at.run(r0, HP, wave, lane);
        if (layer == 0) {
          if (wave == 0 && lane < 32)
            asm volatile("global_store_dwordx4 %0, %1, %2 sc1\n\ts_nop 1" ::"v"((unsigned)lane * 16u),
                         "v"(*reinterpret_cast<const u32x4*>(smem + K1R_CS_OFF + lane * 16)), "s"(uni_ptr((const char*)csq))
                         : "memory");
          lds_spin(lds0 + (unsigned)(LW_OFF + LW_HELLO * 4), 1u);
        }
        MK(4);
        announce(lds0 + (unsigned)(LW_OFF + (LW_O + layer * 22 + (wave >> 2)) * 4), 4u, ws, CN_O + layer, (RT * 2 + (wave >> 2)) * 8 + lane,
                 lane < 8);
        MK(5);
        bar();
      }
      FRESH();
      {
        if (wave < 4) {
          const int lane = lane_id();
          u32x2 xv[5];
          ResIn ri;
          ri.setup(layer ? ws + OFF_X1 + SZ_X : (const char*)x0, layer == 0, RB, CT, wave, lane);
          ri.load(xv);
          ResOut ro;
          ro.setup(ws + OFF_X1 + (size_t)layer * 2 * SZ_X, ws + OFF_XS + (size_t)layer * 2 * SZ_XS, out, false, RB, CT, wave, lane);
          f32x4 acc[2][5];
          p2_gemm(acc, xv, lds0, uni_ptr(ws + OFF_O + (size_t)layer * SZ_X + (size_t)RB * (72 * 1024)), layer, wave, lane MK_PASS);
          MK(7);
          ro.run(acc[0], false);
          MK(8);
          announce(lds0 + (unsigned)(LW_OFF + (LW_X1 + layer) * 4), 4u, ws, CN_X1 + layer, (RB >> 1) * 16 + lane, lane < 16);
          MK(9);
          bar();
          FRESH();
          if (wave == 3) cnt_wait(ws, CN_X1 + layer, MI * 16 + NI, 16u);
          bar();
          MK(10);
          p3_g0(smem, ws, layer, MI, NI, wave, lane_id() MK_PASS);
        } else {
          if (layer && wave == 5) cnt_wait(ws, CN_O + layer, RB * 8 + CT, 16u);
          bar();
          k3_const(smem, ws, b_sp, layer, bid, MI, NI, wave & 3);
          MX(6, 26);
          bar();
          MK(10);
          FRESH();
          p3_g1(smem, ws, layer, MI, NI, wave & 3, lane_id() MK_PASS);
        }
      }
      FRESH();
      {
        const int lane = lane_id();
        if (wave < 4) {
          u32x2 xv[5];
          ResIn ri;
          ri.setup(ws + OFF_X1 + (size_t)layer * 2 * SZ_X, false, RB, CT, wave, lane);
          f32x4 acc[2][5];
          ResOut ro;
          ro.setup(ws + OFF_X1 + SZ_X, ws + OFF_XS + SZ_XS, out, layer != 0, RB, CT, wave, lane);
          ResX<36, 33, 8, P5_W, P5_XW, P5_X0, 3, P5_BAR> gm(acc);
          gm.setup(lds0, uni_ptr(ws + OFF_H + (size_t)layer * SZ_Z + (size_t)RB * (288 * 1024)), wave, lane, (unsigned)P5_RED, layer + 1u);
          announce(lds0 + (unsigned)(LW_OFF + (LW_H + layer * 2 + (wave >> 1)) * 4), 2u, ws, ((NI >= 8) ? CN_H2 : CN_H) + layer,
                   (MI * 2 + (wave >> 1)) * 8 + lane, lane < 8);
          MK(19);
          bar();
          ri.load(xv);
          gm.prow();
          place();
          bar();
          MK(20);
          gm.prox<0>(std::make_integer_sequence<int, P5_X0>{});
          gm.init<2 * P5_X0>(xv);
          __builtin_amdgcn_sched_barrier(0);
          gm.run();
          MK(21);
          ro.run(acc[0], layer != 0);
          MK(22);
          if (layer == 0) {
            announce(lds0 + (unsigned)(LW_OFF + LW_X2 * 4), 4u, ws, CN_X2, (RB >> 1) * 16 + lane,
                     lane < 16 || (lane < 32 && (RB & 1) && RB < 31));
            MK(23);
          }
        } else {
          bar();
          bar();
#pragma unroll
          for (int i = 0; i < 7; ++i) {
            if (i == 1 && wave == 5) {
              unsigned f;
              const char* cp = uni_ptr(ws + OFF_CNT + (size_t)((CN_H2 + layer) * 256 + RB * 8 + CT) * 128);
              asm volatile("global_load_dword %0, %1, %2 sc1" : "=v"(f) : "v"(vconst(0u)), "s"(cp) : "memory");
              bar();
              asm volatile("s_waitcnt vmcnt(0)" : "+v"(f) : : "memory");
              if (__builtin_amdgcn_readfirstlane(f) != 7u) cnt_wait(ws, CN_H2 + layer, RB * 8 + CT, 8u);
              continue;
            }
            asm volatile("s_waitcnt lgkmcnt(0)" ::: "memory");
            bar();
          }
          MK(22);
        }
        if (layer == 0 && wave < 4) bar();
      }
    }
  }
}

}

extern "C" void kernel_launch(void* const* d_in, const int* in_sizes, int n_in, void* d_out, int out_size,
                              void* d_ws, size_t ws_size, hipStream_t stream) {
  const u16* x0 = (const u16*)d_in[0];
  const u16* wqkv = (const u16*)d_in[1];
  const u16* wo = (const u16*)d_in[2];
  const u16* w_in = (const u16*)d_in[4];
  const u16* w_sp = (const u16*)d_in[5];
  const u16* b_sp = (const u16*)d_in[6];
  const u16* w_out = (const u16*)d_in[7];
  char* ws = (char*)d_ws;
  (void)hipFuncSetAttribute((const void*)k_main, hipFuncAttributeMaxDynamicSharedMemorySize, LDS_ALL);
  k_main<<<256, 768, LDS_ALL, stream>>>(x0, wqkv, wo, w_in, w_sp, b_sp, w_out, (u16*)d_out, ws);
}
```

```cpp
#include <hip/hip_runtime.h>
#include <hip/hip_bf16.h>
#include <stdint.h>

#define DEVI __device__ __forceinline__

typedef unsigned short u16;
typedef __attribute__((ext_vector_type(8))) short s16x8;
typedef __attribute__((ext_vector_type(4))) short s16x4;
typedef __attribute__((ext_vector_type(4))) float f32x4;
typedef __attribute__((ext_vector_type(2))) float f32x2;
typedef __attribute__((ext_vector_type(2))) __bf16 bf16x2;
typedef __attribute__((ext_vector_type(2))) unsigned u32x2;
typedef __attribute__((ext_vector_type(4))) unsigned u32x4;

#define MK(I) ((void)0)
#define MX(S, I) ((void)0)
#define MF(I) ((void)0)
#define MF_ARG
#define MF_PASS
#define MK_ARG
#define MK_PASS

#define SPIN_MEM (1u << 22)
#define SAFE_OUT(L)
#define SAFE_AND
#define NOP_(n) "=s"(n)

namespace {

constexpr int S_ = 2048, D_ = 576, F_ = 4608, FH_ = 2304;
constexpr int NQKV = 3 * D_;

constexpr size_t GAP = 256;
constexpr size_t SZ_X = (size_t)S_ * D_ * 2 + GAP;
constexpr size_t SZ_Z = (size_t)S_ * FH_ * 2 + GAP;
constexpr size_t OFF_X1 = GAP;
constexpr size_t OFF_O = OFF_X1 + 3 * SZ_X;
constexpr size_t OFF_Z2 = OFF_O + 2 * SZ_X;
constexpr size_t OFF_H = OFF_Z2 + 2 * SZ_Z;
constexpr size_t SZ_XS = (size_t)S_ * 8 * 8;
constexpr size_t OFF_XS = OFF_H + 2 * SZ_Z;
constexpr size_t OFF_CSQ = OFF_XS + 3 * SZ_XS;
constexpr size_t OFF_ZP = OFF_CSQ + 256 * 512;
constexpr size_t SZ_ZP = (size_t)16 * 16 * 1024;
constexpr size_t OFF_PQ = OFF_ZP + 2 * SZ_ZP;
constexpr size_t OFF_PO = OFF_PQ + (size_t)16 * 9 * 64 * 112 * 2 + GAP;
constexpr size_t OFF_PI = OFF_PO + (size_t)D_ * D_ * 2 + GAP;
constexpr size_t OFF_PW = OFF_PI + (size_t)32 * 9 * 64 * 144 * 2 + GAP;
constexpr size_t OFF_CSP = OFF_PW + (size_t)FH_ * D_ * 2 + GAP;
constexpr size_t OFF_CNT = OFF_CSP + (size_t)18 * F_ * 4;
constexpr int CN_X1 = 0, CN_H = 2, CN_X2 = 4, CN_O = 5, CN_Z = 7, CN_PK = 9, CN_H2 = 10, CN_Z2 = 12, NCNT = 14;
constexpr size_t OFF_TAG = OFF_CNT + (size_t)NCNT * 256 * 128;
constexpr size_t OFF_HELLO = OFF_TAG + 128;
constexpr size_t OFF_POT = OFF_HELLO + 2048;
constexpr size_t OFF_CSK = OFF_POT + 1024;
constexpr size_t WS_END = OFF_CSK + 256 * 2048;
static_assert(WS_END <= (size_t)64 << 20, "workspace");

DEVI float bf_lo(unsigned u) { return __uint_as_float(u << 16); }
DEVI float bf_hi(unsigned u) { return __uint_as_float(u & 0xffff0000u); }
DEVI unsigned pack2(float a, float b) {
  f32x2 v = {a, b};
  bf16x2 r = __builtin_convertvector(v, bf16x2);
  return __builtin_bit_cast(unsigned, r);
}
DEVI float rbf(float a) {
  return bf_lo(pack2(a, 0.f));
}

DEVI float sum_g(float x) {
  const unsigned u = __float_as_uint(x);
  const auto p = __builtin_amdgcn_permlane32_swap(u, u, false, false);
  const unsigned v = __float_as_uint(__uint_as_float(p[0]) + __uint_as_float(p[1]));
  const auto q = __builtin_amdgcn_permlane16_swap(v, v, false, false);
  return __uint_as_float(q[0]) + __uint_as_float(q[1]);
}

DEVI unsigned vconst(unsigned c) {
  unsigned r;
  asm volatile("v_mov_b32 %0, %1" : "=v"(r) : "s"(c));
  return r;
}
DEVI void place() {
  __builtin_amdgcn_sched_barrier(0);
  asm volatile(".p2align 6");
  __builtin_amdgcn_sched_barrier(0);
}
DEVI const char* uni_ptr(const char* p) {
  unsigned long long v = (unsigned long long)p;
  unsigned lo = __builtin_amdgcn_readfirstlane((unsigned)v), hi = __builtin_amdgcn_readfirstlane((unsigned)(v >> 32));
  asm("s_nop 4" : "+s"(lo), "+s"(hi));
  return (const char*)(((unsigned long long)hi << 32) | lo);
}

DEVI void dma16(const char* sbase, unsigned voff, unsigned m0val) {
  asm volatile("s_mov_b32 m0, %2\n\ts_nop 0\n\tglobal_load_lds_dwordx4 %0, %1"
               :
               : "v"(voff), "s"(sbase), "s"(m0val)
               : "memory");
}

DEVI void ln_finish(float s, float q, float invn, float& mb, float& rinv) {
  float mean = s * invn;
  asm("" : "+v"(mean));
  float var = fmaxf(__builtin_fmaf(-mean, mean, q * invn), 0.f);
  mb = rbf(mean);
  float v = rbf(var);
  float t = rbf(v + 1e-5f);
  float sd = rbf(__builtin_amdgcn_sqrtf(t));
  rinv = __builtin_amdgcn_rcpf(sd);
}

DEVI unsigned pack2v(f32x2 v) { return pack2(v[0], v[1]); }

template <int BM_, int BN_>
struct GemmCfg {
  static constexpr int BM = BM_, BN = BN_, NS = 4;
  static constexpr int P = BN * 2;
  static constexpr int XB = BM * 128, WB = 64 * P, SB = XB + WB;
  static constexpr int LDS_BYTES = NS * SB;
};

DEVI int swap23(int r) { return (r & ~12) | ((r & 4) << 1) | ((r & 8) >> 1); }

DEVI void lds_wr32(unsigned addr, unsigned v) { asm volatile("ds_write_b32 %0, %1" ::"v"(addr), "v"(v) : "memory"); }

template <class C, int MT, int NT, int D, int R, bool XCM, int WT, bool SQ = false, unsigned long long WS = 0>
struct GemmMath {
  static constexpr int WO = WT ? 1 : 2;
  static constexpr int NS = C::NS, SB = C::SB, P = C::P, NU = 2 * NT;
  static constexpr int XPU = MT <= 2 ? MT : MT - 2;
  static constexpr int XU = (MT + XPU - 1) / XPU;
  static_assert(NU % R == 0 && R > D && D <= NT && XU <= D, "shape");
  s16x4 wlo[R], whi[R];
  s16x8 wf[R];
  s16x8 xf[2][MT];
  unsigned xrd[2], wrd, xa[2], wa, so;
  f32x4 (&acc)[MT][NT];
  f32x4 accq[SQ ? MT : 1];
  unsigned long long wtab = WS;
  DEVI GemmMath(f32x4 (&a)[MT][NT]) : acc(a) {}

  template <int PU>
  DEVI void wread_lo() {
    constexpr int off = (PU / NT) * 32 * P + (PU % NT) * (WT == 2 ? 1024 : 32);
    if constexpr (WT) asm volatile("ds_read_b128 %0, %1 offset:%2" : "=v"(wf[PU % R]) : "v"(wa), "n"(off));
    else asm volatile("ds_read_b64_tr_b16 %0, %1 offset:%2" : "=v"(wlo[PU % R]) : "v"(wa), "n"(off));
  }
  template <int PU>
  DEVI void wread_hi() {
    if constexpr (!WT)
      asm volatile("ds_read_b64_tr_b16 %0, %1 offset:%2" : "=v"(whi[PU % R]) : "v"(wa), "n"((PU / NT) * 32 * P + (PU % NT) * 32 + 8 * P));
  }
  template <int H, int M>
  DEVI void xread1() {
    asm volatile("ds_read_b128 %0, %1 offset:%2" : "=v"(xf[H][M]) : "v"(xa[H]), "n"(M * (XCM ? 256 : 2048)));
  }
  static constexpr int xops(int v) {
    const int k = (v + D) % NT;
    if (k >= XU) return 0;
    const int n = MT - k * XPU;
    return n < XPU ? n : XPU;
  }
  static constexpr int ops(int v, bool last) { return (last && v + D >= NU) ? 0 : WO + xops(v); }
  static constexpr int newer(int U, bool last) {
    int n = 0;
    int v0 = U - D;
    if (U % NT == 0) v0 += XU - 1;
    else n += v0 >= 0 ? xops(v0) : xops(v0 + NU);
    for (int v = v0 + 1; v < U; ++v) n += v >= 0 ? ops(v, last) : ops(v + NU, false);
    return n;
  }
  template <int U, int M_>
  DEVI void mf() {
    constexpr int M = ((U % NT) & 1) ? MT - 1 - M_ : M_;
    s16x8 w;
    if constexpr (WT) w = wf[U % R];
    else w = __builtin_shufflevector(wlo[U % R], whi[U % R], 0, 1, 2, 3, 4, 5, 6, 7);
    acc[M][U % NT] = __builtin_amdgcn_mfma_f32_16x16x32_bf16(w, xf[U / NT][M], acc[M][U % NT], 0, 0, 0);
    __builtin_amdgcn_sched_barrier(0);
  }
  DEVI void pin() {
#pragma unroll
    for (int m = 0; m < MT; ++m)
#pragma unroll
      for (int n = 0; n < NT; ++n) asm volatile("" : "+v"(acc[m][n]));
  }
  template <int U, int I>
  DEVI void dsop() {
    constexpr int PU = (U + D) % NU;
    if constexpr (I == 0) wread_lo<PU>();
    else if constexpr (I == 1) wread_hi<PU>();
    else if constexpr (I - 2 < xops(U)) {
      constexpr int k = (U + D) % NT;
      xread1<((U + D - k) % NU) / NT, k * XPU + I - 2>();
    }
  }
  template <int U, bool LAST, bool BAR, int M>
  DEVI void slot() {
    constexpr bool PF = !(LAST && U + D >= NU);
    mf<U, M>();
    if constexpr (M == 0 && PF && U + D == NU) {
      if constexpr (BAR) __builtin_amdgcn_s_barrier();
      so = (so == (unsigned)((NS - 1) * SB)) ? 0u : so + (unsigned)SB;
      wa = wrd + (WS ? ((unsigned)wtab & 63u) << 11 : so);
      wtab >>= 6;
      xa[0] = xrd[0] + so;
      xa[1] = xrd[1] + so;
      __builtin_amdgcn_sched_barrier(0);
    }
    if constexpr (PF) {
      dsop<U, M>();
      if constexpr (M == MT - 1) {
        if constexpr (MT <= 1) dsop<U, 1>();
        if constexpr (MT <= 2) {
          dsop<U, 2>();
          dsop<U, 3>();
        }
      }
      __builtin_amdgcn_sched_barrier(0);
    }
  }
  template <int U, bool LAST, bool BAR, int... M>
  DEVI void unit(std::integer_sequence<int, M...>) {
    static_assert(newer(U, LAST) <= 15, "lgkmcnt range");
    asm volatile("s_waitcnt lgkmcnt(%0)" ::"n"(newer(U, LAST)));
    __builtin_amdgcn_sched_barrier(0);
    (slot<U, LAST, BAR, M>(), ...);
    if constexpr (SQ && (U % NT) < MT) {
      constexpr int m = U % NT;
      accq[m] = __builtin_amdgcn_mfma_f32_16x16x32_bf16(xf[U / NT][m], xf[U / NT][m], accq[m], 0, 0, 0);
      __builtin_amdgcn_sched_barrier(0);
    }
    __builtin_amdgcn_sched_barrier(0);
  }
  template <bool LAST, bool BAR, int... U>
  DEVI void stage(std::integer_sequence<int, U...>) {
    (unit<U, LAST, BAR>(std::make_integer_sequence<int, MT>{}), ...);
  }
  template <int... U>
  DEVI void prow(std::integer_sequence<int, U...>) {
    ((wread_lo<U>(), wread_hi<U>()), ...);
  }
  template <int... M>
  DEVI void prox(std::integer_sequence<int, M...>) {
    (xread1<0, M>(), ...);
  }
  DEVI void setup(unsigned lds0, int mt0, int nt0, int lane, unsigned so0) {
    const int g = lane >> 4, r = lane & 15, q = (lane >> 2) & 3, p = lane & 3;
#pragma unroll
    for (int h = 0; h < 2; ++h)
      xrd[h] = XCM ? lds0 + (unsigned)((4 * h + g) * (C::BM * 16) + (mt0 * 16 + r) * 16)
                   : lds0 + (unsigned)((mt0 * 16 + r) * 128 + (((4 * h + g) ^ ((r >> 1) & 7)) * 16));
    wrd = WT == 2   ? lds0 + (unsigned)(C::XB + nt0 * 1024 + lane * 16)
          : WT == 1 ? lds0 + (unsigned)(C::XB + (g * 8 + (r & 7)) * P + (r >> 3) * 16 + nt0 * 32)
                    : lds0 + (unsigned)(C::XB + (16 * (g >> 1) + 4 * (g & 1) + q) * P + 8 * p + nt0 * 32);
    so = so0;
    xa[0] = xrd[0] + so;
    xa[1] = xrd[1] + so;
    wa = wrd + so;
  }
  DEVI void first() {
    prox(std::make_integer_sequence<int, MT>{});
    prow(std::make_integer_sequence<int, D>{});
    asm volatile("s_waitcnt lgkmcnt(0)");
  }
  template <int N, bool BAR = true>
  DEVI void mid() {
    if constexpr (N > 1) place();
#pragma unroll 1
    for (int s = 0; s < N; ++s) stage<false, BAR>(std::make_integer_sequence<int, NU>{});
  }
  DEVI void last() {
    stage<true, true>(std::make_integer_sequence<int, NU>{});
    pin();
  }
  template <int KS>
  DEVI void run(unsigned lds0, int mt0, int nt0, int lane, unsigned so0) {
    setup(lds0, mt0, nt0, lane, so0);
    __builtin_amdgcn_s_barrier();
    first();
    mid<KS - 1>();
    last();
  }
};

DEVI const char* code_here() {
  unsigned long long pc;
  asm volatile("s_getpc_b64 %0" : "=s"(pc));
  return (const char*)pc;
}
DEVI int okmap(int p) {
  const int ch = p >> 3, e = p & 7, c = (ch - 8) & 3;
  return ch < 8 ? (2 * ch + (e >> 2)) * 36 + 16 + (e & 3) : ((ch - 8) >> 2) * 36 + c * 8 + (c >> 1) * 4 + e;
}

DEVI void pk_st16(void* p, u32x4 v) {
  asm volatile("global_store_dwordx4 %0, %1, off sc1\n\ts_nop 1" ::"v"(p), "v"(v) : "memory");
}

using K1C = GemmCfg<144, 112>;
constexpr int K1_TP = 240;
constexpr int K1_T_OFF = K1C::SB;
constexpr int K1_ST_OFF = 163840 - 128 - 1152;
constexpr int K1_CS_OFF = 4 * K1C::SB;
constexpr int K1_WX = 4 * K1C::SB + 2048;
constexpr unsigned long long K1_WS = 16ull | 32ull << 6 | 48ull << 12 | 56ull << 18 | 63ull << 24 | 16ull << 30 | 32ull << 36 | 48ull << 42;
static_assert(K1C::SB == 16 * 2048 && K1_WX - K1C::XB == 56 * 2048 && K1C::WB == 7 * 2048 && K1_WX + 2 * K1C::WB <= K1_ST_OFF, "K1 w places");
static_assert(K1_T_OFF + 144 * K1_TP + 64 <= 2 * K1C::SB + K1C::XB, "K1 LDS alias");
using K1R = GemmCfg<144, 144>;
constexpr int K1R_T_OFF = K1R::SB;
constexpr int K1R_CS_OFF = K1R_T_OFF + 144 * K1_TP;
constexpr int K1R_ST_OFF = K1R_CS_OFF + 512;
constexpr int K1R_DUMMY = K1R::LDS_BYTES;
constexpr int K1R_PO = K1R_DUMMY + 1024;
constexpr int K1R_CNT = 163840 - 128 + 28 * 4;
#define SPIN_ASM(TEST)                                                                                                   \
  if constexpr (SL <= 1)                                                                                                 \
    asm volatile(".La%=:\n\tds_read_b32 %0, %3\n\ts_waitcnt lgkmcnt(0)\n\tv_readfirstlane_b32 %2, %0\n\t" TEST SAFE_AND \
                 "s_cbranch_scc1 .La%="                                                                                  \
                 : "=&v"(v), NOP_(c), "=&s"(t)                                                                           \
                 : "v"(addr), "s"(k)                                                                                     \
                 : "memory", "scc");                                                                                     \
  else                                                                                                                   \
    asm volatile("s_branch .Lb%=\n.La%=:\n\ts_sleep %5\n.Lb%=:\n\tds_read_b32 %0, %3\n\ts_waitcnt lgkmcnt(0)\n\t"     \
                 "v_readfirstlane_b32 %2, %0\n\t" TEST SAFE_AND "s_cbranch_scc1 .La%="                                  \
                 : "=&v"(v), NOP_(c), "=&s"(t)                                                                           \
                 : "v"(addr), "s"(k), "n"(SL)                                                                            \
                 : "memory", "scc")
template <int SL>
DEVI void spin_ge(unsigned addr, unsigned k) {
  unsigned v, c = 1u << 18, t;
  SPIN_ASM("s_cmp_lt_u32 %2, %4\n\t");
}
template <int SL>
DEVI void spin_b4(unsigned addr, unsigned n) {
  unsigned v, c = 1u << 18, t;
  const unsigned k = (0x80u - n) * 0x01010101u;
  SPIN_ASM("s_add_u32 %2, %2, %4\n\ts_andn2_b32 %2, 0x80808080, %2\n\t");
}
DEVI void msync(unsigned addr) {
  asm volatile("s_waitcnt lgkmcnt(0)" ::: "memory");
  asm volatile("s_mov_b64 exec, 1\n\tds_add_u32 %0, %1\n\ts_mov_b64 exec, -1" ::"v"(addr), "v"(vconst(1u)) : "memory");
  spin_ge<0>(addr, 8u);
}
static_assert(K1R_PO + 13824 + 256 <= 163840 - 128 && K1R_ST_OFF + 144 * 8 <= 3 * K1R::SB, "K1R LDS");

DEVI void touch64(const char* sbase, unsigned voff, unsigned m0val) {
  asm volatile("s_mov_b32 m0, %2\n\ts_nop 0\n\tglobal_load_lds_dword %0, %1" : : "v"(voff), "s"(sbase), "s"(m0val) : "memory");
}

template <int MT, bool G1>
DEVI void k1_gemm_raw(char* smem, int hp, int mt0, int lane MF_ARG) {
  constexpr int NT = 4, nt0 = G1 ? 4 : 0;
  const unsigned lds0 = (unsigned)(size_t)smem;
  const int g = lane >> 4, r = lane & 15;
  unsigned ta[NT];
#pragma unroll
  for (int nt = 0; nt < NT; ++nt) {
    const int j = (nt0 + nt) * 16 + g * 4, seg = j / 40, cj = j - seg * 40 - (hp & 1) * 4;
    const int to = (seg < 3 && cj >= 0 && cj < 36) ? seg * 80 + cj * 2 + (cj >= 20 ? 8 : 0) : (seg < 3 ? 40 : 120);
    ta[nt] = lds0 + (unsigned)(K1R_T_OFF + (mt0 * 16 + r) * K1_TP + to);
    asm volatile("" : "+v"(ta[nt]));
  }
  unsigned sa = lds0 + (unsigned)(K1R_ST_OFF + (mt0 * 16 + r) * 8), ca = lds0 + (unsigned)(K1R_CS_OFF + (nt0 * 16 + g * 4) * 4);
  asm volatile("" : "+v"(sa), "+v"(ca));
  f32x4 acc[MT][NT];
#pragma unroll
  for (int mt = 0; mt < MT; ++mt)
#pragma unroll
    for (int nt = 0; nt < NT; ++nt) acc[mt][nt] = f32x4{0.f, 0.f, 0.f, 0.f};
  {
    GemmMath<K1R, MT, 4, 3, 4, false, 0, G1> gm(acc);
    if constexpr (G1) {
#pragma unroll
      for (int mt = 0; mt < MT; ++mt) gm.accq[mt] = f32x4{0.f, 0.f, 0.f, 0.f};
    }
    MF(14);
    if constexpr (!G1) {
      asm volatile("s_waitcnt vmcnt(0)" ::: "memory");
    }
    MF(15);
    gm.template run<9>(lds0, mt0, nt0, lane, 0u);
    MF(2);
    if constexpr (G1) {
#pragma unroll
      for (int mt = 0; mt < MT; ++mt) {
        const f32x4 q = gm.accq[mt];
        const float d = (r & 2) ? ((r & 1) ? q[3] : q[2]) : ((r & 1) ? q[1] : q[0]);
        const float qq = sum_g(g == (r >> 2) ? d : 0.f);
        float mb, rinv;
        ln_finish(acc[mt][3][0], qq, 1.0f / (float)D_, mb, rinv);
        const f32x2 st = {-mb, rinv};
        if (g == 2) asm volatile("ds_write_b64 %0, %1 offset:%2" ::"v"(sa), "v"(st), "n"(mt * 128) : "memory");
      }
    }
  }
  if (mt0 == 0 && r == 0) {
#pragma unroll
    for (int nt = 0; nt < NT; ++nt) *reinterpret_cast<f32x4*>(smem + K1R_CS_OFF + ((nt0 + nt) * 16 + g * 4) * 4) = acc[0][nt];
  }
  msync(lds0 + (unsigned)K1R_CNT);
  MF(3);
  f32x2 st[MT];
  f32x4 cs[NT];
#pragma unroll
  for (int nt = 0; nt < NT; ++nt) asm volatile("ds_read_b128 %0, %1 offset:%2" : "=v"(cs[nt]) : "v"(ca), "n"(nt * 64) : "memory");
#pragma unroll
  for (int mt = 0; mt < MT; ++mt) asm volatile("ds_read_b64 %0, %1 offset:%2" : "=v"(st[mt]) : "v"(sa), "n"(mt * 128) : "memory");
#pragma unroll
  for (int i = 0; i < MT; ++i) asm volatile("s_waitcnt lgkmcnt(0)" : "+v"(st[i]));
#pragma unroll
  for (int i = 0; i < NT; ++i) asm volatile("" : "+v"(cs[i]));
#pragma unroll
  for (int mt = 0; mt < MT; ++mt) {
    f32x2 M = {st[mt][0], st[mt][0]}, R = {st[mt][1], st[mt][1]};
    asm volatile("" : "+v"(M), "+v"(R));
#pragma unroll
    for (int nt = 0; nt < NT; ++nt) {
      const f32x2 v0 = R * __builtin_elementwise_fma(M, f32x2{cs[nt][0], cs[nt][1]}, f32x2{acc[mt][nt][0], acc[mt][nt][1]});
      const f32x2 v1 = R * __builtin_elementwise_fma(M, f32x2{cs[nt][2], cs[nt][3]}, f32x2{acc[mt][nt][2], acc[mt][nt][3]});
      acc[mt][nt][0] = __uint_as_float(pack2v(v0));
      acc[mt][nt][1] = __uint_as_float(pack2v(v1));
      const f32x2 pk = {acc[mt][nt][0], acc[mt][nt][1]};
      asm volatile("ds_write_b64 %0, %1 offset:%2" ::"v"(ta[nt]), "v"(pk), "n"(mt * 16 * K1_TP) : "memory");
    }
  }
}

template <int N>
DEVI void wait_vm() {
  asm volatile("s_waitcnt vmcnt(%0)" ::"n"(N) : "memory");
}

constexpr int LDS_ALL = 163840;
constexpr int LW_OFF = LDS_ALL - 128;
constexpr int LW_GO = 0, LW_T = 2, LW_O = 4  , LW_X1 = 6, LW_H = 8, LW_X2 = 12, LW_PK = 13, LW_Z = 14, LW_PAIR = 16, LW_AD = 20,
              LW_L = 22, LW_S = 24, LW_ST = 29, LW_HELLO = 30, LW_Q = 31;
DEVI void announce(unsigned addr, unsigned n, char* ws, int kind, int who, bool act) {
  const char* cb = uni_ptr(ws + OFF_CNT + (size_t)kind * 256 * 128);
  const unsigned long long mask = __ballot(act);
  unsigned t, u;
  asm volatile(
      "s_waitcnt vmcnt(0)\n\t"
      "s_mov_b64 exec, 1\n\t"
      "ds_add_rtn_u32 %0, %2, %3\n\t"
      "s_waitcnt lgkmcnt(0)\n\t"
      "v_readfirstlane_b32 %1, %0\n\t"
      "s_cmp_eq_u32 %1, %4\n\t"
      "s_cselect_b64 exec, %5, 0\n\t"
      "global_atomic_add %6, %3, %7 sc1\n\t"
      "s_mov_b64 exec, -1\n\t"
      "s_nop 0"
      : "=&v"(t), "=&s"(u)
      : "v"(addr), "v"(vconst(1u)), "s"(n - 1u), "s"(mask), "v"((unsigned)(who & 255) * 128u), "s"(cb)
      : "memory", "scc");
}
DEVI void cnt_wait(const char* ws, int kind, int who, unsigned n) {
  const char* cp = uni_ptr(ws + OFF_CNT + (size_t)(kind * 256 + who) * 128);
  unsigned v, c = SPIN_MEM, t;
  asm volatile(
      ".La%=:\n\tglobal_load_dword %0, %3, %4 sc1\n\ts_sub_u32 %1, %1, 1\n\ts_waitcnt vmcnt(0)\n\tv_readfirstlane_b32 %2, %0\n\ts_cmp_lg_u32 %2, %5\n\t"
      "s_cselect_b32 %2, %1, 0\n\ts_cmp_lg_u32 %2, 0\n\ts_cbranch_scc1 .La%="
      : "=&v"(v), "+s"(c), "=&s"(t)
      : "v"(vconst(0u)), "s"(cp), "s"(n - 1u)
      : "memory", "scc");
}
DEVI void dma16_sc1(const char* sbase, unsigned voff, unsigned m0val) {
  asm volatile("s_mov_b32 m0, %2\n\ts_nop 0\n\tglobal_load_lds_dwordx4 %0, %1 sc1"
               :
               : "v"(voff), "s"(sbase), "s"(m0val)
               : "memory");
}
template <int N, bool SC1>
DEVI void dma4(const char* sbase, unsigned v0, unsigned v1, unsigned v2, unsigned v3, unsigned m0val) {
#define DMA4(S)                                                                                                               \
  asm volatile("s_mov_b32 m0, %5\n\ts_nop 0\n\tglobal_load_lds_dwordx4 %0, %4" S "\n\t.if %6 > 1\n\tglobal_load_lds_dwordx4 %1, %4 offset:1024" S \
               "\n\t.endif\n\t.if %6 > 2\n\tglobal_load_lds_dwordx4 %2, %4 offset:2048" S                                    \
               "\n\t.endif\n\t.if %6 > 3\n\tglobal_load_lds_dwordx4 %3, %4 offset:3072" S "\n\t.endif"                      \
               :                                                                                                              \
               : "v"(v0), "v"(v1), "v"(v2), "v"(v3), "s"(sbase), "s"(m0val), "n"(N)                                           \
               : "memory")
  if constexpr (SC1) DMA4(" sc1");
  else DMA4("");
}
template <int N>
DEVI void dma_seq(const char* src, unsigned voff, unsigned m0val) {
  if constexpr (N > 0) {
    dma4<(N < 4 ? N : 4), true>(src, voff, voff, voff, voff, m0val);
    dma_seq<N - 4>(src + 4096, voff, m0val + 4096u);
  }
}
DEVI void bar() { __builtin_amdgcn_s_barrier(); }
DEVI void lds_inc(unsigned addr, unsigned by = 1u) {
  asm volatile("s_mov_b64 exec, 1\n\tds_add_u32 %0, %1\n\ts_mov_b64 exec, -1" ::"v"(addr), "v"(vconst(by)) : "memory");
}
DEVI void lds_spin(unsigned addr, unsigned n) {
  spin_ge<1>(addr, n);
}
template <int SL = 1>
DEVI void lds_spin4(unsigned addr, unsigned n) {
  spin_b4<SL>(addr, n);
}
DEVI int lane_id() {
  int l;
  asm volatile("v_mbcnt_lo_u32_b32 %0, -1, 0\n\tv_mbcnt_hi_u32_b32 %0, -1, %0" : "=v"(l));
  __builtin_assume((unsigned)l < 64u);
  return l;
}

template <int I>
DEVI u32x4 tr8(const u32x4 (&v)[8]) {
  constexpr unsigned sel = (I & 1) ? 0x07060302u : 0x05040100u;
  return u32x4{__builtin_amdgcn_perm(v[1][I / 2], v[0][I / 2], sel), __builtin_amdgcn_perm(v[3][I / 2], v[2][I / 2], sel),
               __builtin_amdgcn_perm(v[5][I / 2], v[4][I / 2], sel), __builtin_amdgcn_perm(v[7][I / 2], v[6][I / 2], sel)};
}
template <int PITCH, bool SUM, int... I>
DEVI void tr8_store(const u32x4 (&v)[8], unsigned dst, float (&cs)[8], std::integer_sequence<int, I...>) {
  auto one = [&](auto I_) {
    const u32x4 o = tr8<I_>(v);
    const unsigned d_ = dst;
    asm volatile("ds_write_b128 %0, %1 offset:%2" ::"v"(d_), "v"(o), "n"(I_ * PITCH) : "memory");
    if constexpr (SUM) {
#pragma unroll
      for (int d = 0; d < 4; ++d) {
        const unsigned od = o[d];
        cs[I_] = __builtin_amdgcn_fdot2_f32_bf16(__builtin_bit_cast(bf16x2, od), __builtin_bit_cast(bf16x2, 0x3f803f80u), cs[I_], false);
      }
    }
  };
  (one(std::integral_constant<int, I>{}), ...);
}
template <int NB>
DEVI void lds_out(unsigned src, char* dst, int lane) {
  constexpr int N = (NB + 1023) / 1024;
  u32x4 t[N];
  const unsigned a = src + (unsigned)lane * 16u;
#pragma unroll
  for (int i = 0; i < N; ++i) asm volatile("ds_read_b128 %0, %1 offset:%2" : "=v"(t[i]) : "v"(a), "n"(i * 1024) : "memory");
  const char* d = uni_ptr(dst);
#pragma unroll
  for (int i = 0; i < N; ++i) {
    asm volatile("s_waitcnt lgkmcnt(%1)" : "+v"(t[i]) : "n"(N - 1 - i) : "memory");
    if (i < N - 1 || lane < (NB - (N - 1) * 1024) / 16)
      asm volatile("global_store_dwordx4 %0, %1, %2 sc1\n\ts_nop 1" ::"v"((unsigned)lane * 16u), "v"(t[i]), "s"(d + i * 1024) : "memory");
  }
}
template <int A, int B, class F>
DEVI void static_for(F f) {
  if constexpr (A < B) {
    f(std::integral_constant<int, A>{});
    static_for<A + 1, B>(f);
  }
}
constexpr int WSP_V = 3;
constexpr int RAW_S1 = 6;
constexpr int RAW_S0 = 4;
template <int E>
struct RawX {
  static constexpr int first(int s) {
    return s <= RAW_S0 ? 0 : (s > RAW_S1 ? E : (E * (s - RAW_S0) + RAW_S1 - RAW_S0) / (RAW_S1 + 1 - RAW_S0));
  }
  static constexpr int n(int s) { return first(s + 1) - first(s); }
};
struct RawLd {
  static constexpr int VPS = 10, SB = K1R::SB;
  unsigned xoff[5], woff[5], m0x;
  unsigned long long xact0, wact[5], tag;
  const char *xp, *wp;
  int qb;
  DEVI void q_hook(int st, char* ws, unsigned lds0, int hp) {
    if ((qb >> 1) == st) {
      const int lane = lane_id(), g = lane >> 4;
      const unsigned a = lds0 + (unsigned)((st & 3) * SB + K1R::XB + ((qb & 1) * 32 + 16 * (g >> 1) + 4 * (g & 1) + ((lane >> 2) & 3)) * 288 +
                                           (lane & 3) * 8 + (hp & 1) * 8);
      const unsigned a2 = a + ((lane & 3) >= 1 ? 8u : 0u), a4 = a + ((lane & 3) >= 2 ? 8u : 0u);
      u32x2 qf[7][2];
#pragma unroll
      for (int nt = 0; nt < 7; ++nt)
        asm volatile("ds_read_b64_tr_b16 %0, %2 offset:%3\n\tds_read_b64_tr_b16 %1, %2 offset:%4"
                     : "=&v"(qf[nt][0]), "=&v"(qf[nt][1])
                     : "v"(nt == 2 ? a2 : (nt == 4 ? a4 : a)), "n"(nt * 32 + (nt > 4 ? 16 : (nt > 2 ? 8 : 0))),
                       "n"(2304 + nt * 32 + (nt > 4 ? 16 : (nt > 2 ? 8 : 0)))
                     : "memory");
      const char* d = uni_ptr(ws + OFF_PQ + (size_t)(hp * 18 + qb) * 7168 + 3072);
#pragma unroll
      for (int nt = 0; nt < 7; ++nt) {
        asm volatile("s_waitcnt lgkmcnt(%2)" : "+v"(qf[nt][0]), "+v"(qf[nt][1]) : "n"(12 - 2 * nt) : "memory");
        const u32x4 v = {qf[nt][0][0], qf[nt][0][1], qf[nt][1][0], qf[nt][1][1]};
        asm volatile("global_store_dwordx4 %0, %1, %2 offset:%3 sc1\n\ts_nop 1" ::"v"((unsigned)lane * 16u), "v"(v), "s"(d), "n"(nt * 1024 - 3072) : "memory");
      }
      if (st == 8) lds_wr32(lds0 + (unsigned)(LW_OFF + LW_Q * 4), 1u);
    }
  }
  DEVI void stage_x(unsigned so) {
    asm volatile(
        "s_mov_b32 m0, %6\n\ts_mov_b64 exec, %7\n\tglobal_load_lds_dwordx4 %0, %5\n\ts_mov_b64 exec, -1\n\t"
        "global_load_lds_dwordx4 %1, %5 offset:1024\n\tglobal_load_lds_dwordx4 %2, %5 offset:2048\n\tglobal_load_lds_dwordx4 %3, %5 offset:3072\n\t"
        "s_add_u32 m0, m0, 0x1000\n\ts_mov_b32 exec_hi, 0\n\tglobal_load_lds_dwordx4 %4, %5\n\ts_mov_b32 exec_hi, -1" ::"v"(xoff[0]),
        "v"(xoff[1]), "v"(xoff[2]), "v"(xoff[3]), "v"(xoff[4]), "s"(xp), "s"(m0x + so), "s"(xact0)
        : "memory", "scc");
    xp += 128;
  }
  DEVI void stage_w(unsigned so) {
    asm volatile(
        "s_mov_b32 m0, %6\n\ts_mov_b64 exec, %7\n\tglobal_load_lds_dwordx4 %0, %5\n\ts_mov_b64 exec, %8\n\t"
        "global_load_lds_dwordx4 %1, %5 offset:1024\n\ts_mov_b64 exec, %9\n\tglobal_load_lds_dwordx4 %2, %5 offset:2048\n\t"
        "s_mov_b64 exec, %10\n\tglobal_load_lds_dwordx4 %3, %5 offset:3072\n\ts_add_u32 m0, m0, 0x1000\n\ts_mov_b64 exec, %11\n\t"
        "global_load_lds_dwordx4 %4, %5\n\ts_mov_b64 exec, -1" ::"v"(woff[0]),
        "v"(woff[1]), "v"(woff[2]), "v"(woff[3]), "v"(woff[4]), "s"(wp), "s"(m0x + so + (unsigned)K1R::XB), "s"(wact[0]), "s"(wact[1]),
        "s"(wact[2]), "s"(wact[3]), "s"(wact[4])
        : "memory", "scc");
    wp += 64 * NQKV * 2;
  }
  DEVI void stage(unsigned so) {
    stage_x(so);
    stage_w(so);
  }
  template <class F>
  DEVI void front(const u16* x, const u16* wqkv, const u16* wo, char* ws, int bid, unsigned lds0, int wl, int lane, int rt, int hp,
                  int& pj, F jobs, const char*& pg MF_ARG) {
    xp = uni_ptr((const char*)x - 3072);
    wp = uni_ptr((const char*)wqkv + ((hp * 72) & ~15) - 3072);
    m0x = lds0 + (unsigned)(wl * 288 * 16);
    xact0 = wl ? ~0ull : ~0xffull;
    const int row0 = wl * 36 + (lane >> 3);
#pragma unroll
    for (int i = 0; i < 5; ++i) {
      const int row = row0 + i * 8;
      int gr = rt * 128 - 16 + row;
      if (gr < 0) gr = 0;
      xoff[i] = (unsigned)(gr * (D_ * 2) + (3 - (i & 3)) * 1024) + (unsigned)(((lane ^ (row >> 1)) & 7) * 16);
    }
    stage_x(0u);
    u32x2 tg;
    asm volatile("global_load_dwordx2 %0, %1, %2 sc1" : "=v"(tg) : "v"(0u), "s"(uni_ptr(ws + OFF_TAG)) : "memory");
    asm volatile("" : "+v"(lane));
#pragma unroll
    for (int i = 0; i < 5; ++i) {
      const unsigned c = (unsigned)(i * 64 + (lane & 63));
      const unsigned wr = (c * 3641u) >> 16, wc = c - wr * 18u, seg = (wc * 13u) >> 6;
      wact[i] = __ballot(wc < 15u) & (i < 4 ? ~0ull : 0xffffffffull);
      woff[i] = (unsigned)(wl * 16 + swap23((int)wr)) * (unsigned)(NQKV * 2) + seg * (unsigned)(D_ * 2 - 80) + wc * 16u + (unsigned)((3 - (i & 3)) * 1024);
    }
    stage_w(0u);
    stage((unsigned)SB);
    jobs();
    MF(10);
    asm volatile("s_waitcnt vmcnt(%1)" : "+v"(tg) : "n"(VPS) : "memory");
    tag = (((unsigned long long)(unsigned)__builtin_amdgcn_readfirstlane(tg[1]) << 32) | (unsigned)__builtin_amdgcn_readfirstlane(tg[0])) +
          0x9e3779b97f4a7c15ull;
    MF(1);
    __builtin_amdgcn_s_barrier();
    MF(15);
    if (wl == 0 && lane == 0)
      asm volatile("global_store_dwordx2 %0, %1, off sc1\n\ts_nop 1" ::"v"(ws + OFF_HELLO + bid * 8), "v"(tag) : "memory");
    stage((unsigned)(2 * SB));
    qb = wl == 3 ? rt : ((wl == 2 && rt < 2) ? 16 + rt : 99);
    q_hook(0, ws, lds0, hp);
    const int nb = pj;
    const bool po = pj >= 0 && wl < 2;
    u32x4 v[8];
    unsigned da = 0u;
    if (po) {
      const int l54 = lane < 54 ? lane : 53, gl = l54 / 9, c = l54 - gl * 9, kl = wl * 6 + gl, t = nb / 6, kg = (nb - t * 6) * 12 + kl;
      da = lds0 + (unsigned)(K1R_PO + (kl >> 2) * 4608 + (16 * (kl & 1) + ((kl >> 1) & 1)) * 144 + c * 16);
      const unsigned sa[2] = {(unsigned)(okmap(kg * 8) * D_ + t * 72 + c * 8) * 2u, (unsigned)(okmap(kg * 8 + 4) * D_ + t * 72 + c * 8) * 2u};
      const char* ps = uni_ptr((const char*)wo);
#pragma unroll
      for (int j = 0; j < 8; ++j)
        asm volatile("global_load_dwordx4 %0, %1, %2 offset:%3" : "=v"(v[j]) : "v"(sa[j / 4]), "s"(ps), "n"((j % 4) * D_ * 2) : "memory");
    } else {
#pragma unroll
      for (int j = 0; j < 8; ++j) asm volatile("" : "=v"(v[j]));
    }
    unsigned slot = (unsigned)(3 * SB), pd = 0u;
    const unsigned gk = (unsigned)(bid >> 3) & 15u;
    unsigned gp = gk * (unsigned)SZ_X;
    gp = gk > 5u ? (unsigned)(OFF_Z2 - GAP) + (gk - 5u) * (unsigned)SZ_Z : gp;
    gp = gk > 9u ? (unsigned)(OFF_PO - GAP) : gp;
    gp = gk > 10u ? (unsigned)(OFF_PI - GAP) : gp;
    gp = gk > 11u ? (unsigned)(OFF_PW - GAP) : gp;
    gp = gk > 12u ? (unsigned)(OFF_CSP - GAP) : gp;
#pragma unroll 1
    for (int S = 0; S < RAW_S0; ++S) {
      if (wl == 2 && S == 1) asm volatile("s_load_dword %0, %1, 0\n\ts_load_dword %0, %2, 0" : "+s"(pd) : "s"(ws + gp), "s"(pg));
      asm volatile("" : "+s"(pd));
      wait_vm<VPS>();
      __builtin_amdgcn_s_barrier();
      if (S == 0) MF(16);
      if (S == 1) MF(17);
      if (S == 2) MF(18);
      if (S == 3) MF(19);
      stage(slot);
      slot = (slot == (unsigned)(3 * SB)) ? 0u : slot + (unsigned)SB;
      q_hook(S + 1, ws, lds0, hp);
      if (po && S == 1) {
        float cs[8];
        asm volatile("s_nop 0 ; LOOPWAIT" : "+v"(v[0]), "+v"(v[1]), "+v"(v[2]), "+v"(v[3]), "+v"(v[4]), "+v"(v[5]), "+v"(v[6]), "+v"(v[7]));
        tr8_store<288, false>(v, da, cs, std::make_integer_sequence<int, 8>{});
        asm volatile("s_waitcnt lgkmcnt(0)" ::: "memory");
      }
      if (po && S == 2) lds_out<6912>(lds0 + (unsigned)(K1R_PO + wl * 6912), ws + OFF_PO + (size_t)nb * 13824 + wl * 6912, lane);
    }
    asm volatile("s_waitcnt lgkmcnt(0)" : "+s"(pd));
  }
};
template <bool JOB>
DEVI void k1_raw_loader(RawLd& ld, const u16* __restrict__ w_in, const u16* __restrict__ w_out, char* __restrict__ ws,
                        unsigned long long tag, int bid, unsigned lds0, int wl, int lane, int rt, int hp, int ct, int job, int pj MF_ARG) {
  constexpr int VPS = RawLd::VPS, SB = RawLd::SB, E = JOB ? 16 : 0;
  const bool isi = job < 144;
  const int kb = isi ? job >> 3 : job - 144;
  const int pitch = isi ? F_ * 2 : D_ * 2;
  const int l36 = lane < 36 ? lane : 35;
  unsigned sa = (unsigned)l36 * 16u, da;
  if (isi) da = lds0 + (unsigned)((l36 >= 18 ? 9216 - 288 : 0) + wl * 8 * 288 + l36 * 16);
  else da = lds0 + (unsigned)((l36 / 9) * (4608 - 144) + (16 * (wl & 1) + (wl >> 1)) * 144 + l36 * 16);
  asm volatile("" : "+v"(sa), "+v"(da));
  u32x4 v[2][8];
  const char* ps = uni_ptr((const char*)(isi ? w_in + (job & 7) * 576 : w_out) + (size_t)(kb * 32 + wl * 8) * pitch);
  auto extra = [&](auto J_) {
    constexpr int j = J_;
    const char* q = ps + (j / 2) * pitch;
    u32x4 t;
    asm volatile("global_load_dwordx4 %0, %1, %2 offset:%3" : "=v"(t) : "v"(sa), "s"(q), "n"((j % 2) * 576) : "memory");
    v[j % 2][j / 2] = t;
  };
  auto step = [&](auto S_) {
    constexpr int S = S_;
    wait_vm<RawX<E>::n(S - 2) + (S <= 6 ? VPS : 0) + RawX<E>::n(S - 1)>();
    __builtin_amdgcn_s_barrier();
    MF(16 + S);
    if constexpr (S == RAW_S0) {
      static_assert(RAW_S0 >= 4, "wo blocks");
      if (pj >= 0 && wl == 0 && lane == 0)
        asm volatile("global_store_dwordx2 %0, %1, off sc1\n\ts_nop 1" ::"v"(ws + OFF_POT + (pj / 6) * 128 + (pj % 6) * 8), "v"(tag) : "memory");
    }
    if constexpr (S <= 5) ld.stage((unsigned)(((S + 3) % 4) * SB));
    ld.q_hook(S + 1, ws, lds0, hp);
    static_for<RawX<E>::first(S), RawX<E>::first(S + 1)>(extra);
  };
  const char* hp_ = ws + OFF_HELLO + lane * 32;
  u32x4 a, b;
  if (wl == 1) asm volatile("global_load_dwordx4 %0, %2, off sc1\n\tglobal_load_dwordx4 %1, %2, off offset:16 sc1" : "=&v"(a), "=&v"(b) : "v"(hp_) : "memory");
  else asm volatile("" : "=v"(a), "=v"(b));
  static_for<RAW_S0, 8>(step);
  MF(2);
  if (wl == 1) {
    asm volatile("" : "+v"(a), "+v"(b));
    const unsigned t0 = (unsigned)tag, t1 = (unsigned)(tag >> 32);
#pragma unroll 1
    for (unsigned spin = 0; spin < SPIN_MEM; ++spin) {
      const unsigned bad = ((a[0] ^ t0) | (a[2] ^ t0) | (b[0] ^ t0) | (b[2] ^ t0)) | ((a[1] ^ t1) | (a[3] ^ t1) | (b[1] ^ t1) | (b[3] ^ t1));
      if (!__any(bad != 0u)) break;
      __builtin_amdgcn_s_sleep(4);
      asm volatile("global_load_dwordx4 %0, %2, off sc1\n\tglobal_load_dwordx4 %1, %2, off offset:16 sc1\n\ts_waitcnt vmcnt(0)"
                   : "=&v"(a), "=&v"(b)
                   : "v"(hp_)
                   : "memory");
    }
    lds_wr32(lds0 + (unsigned)(LW_OFF + LW_HELLO * 4), 1u);
    if (bid == 0 && lane == 0) asm volatile("global_store_dwordx2 %0, %1, off sc1\n\ts_nop 1" ::"v"(ws + OFF_TAG), "v"(tag) : "memory");
  }
  MF(11);
  if (rt < 2) lds_spin(lds0 + (unsigned)(LW_OFF + LW_Q * 4), 1u);
  lds_spin(lds0 + (unsigned)K1R_CNT, 8u);
  if constexpr (JOB) {
#pragma unroll
    for (int b = 0; b < 2; ++b)
      asm volatile("s_waitcnt vmcnt(0)"
                   : "+v"(v[b][0]), "+v"(v[b][1]), "+v"(v[b][2]), "+v"(v[b][3]), "+v"(v[b][4]), "+v"(v[b][5]), "+v"(v[b][6]), "+v"(v[b][7])
                   :
                   : "memory");
  }
  MF(12);
  const char* tp = ws + OFF_POT + ct * 128 + (lane < 6 ? lane : 5) * 8;
  u32x2 f;
  asm volatile("global_load_dwordx2 %0, %1, off sc1" : "=v"(f) : "v"(tp) : "memory");
  if constexpr (JOB) {
    static_assert(SB == 4 * 9216, "staging");
#pragma unroll
    for (int b = 0; b < 2; ++b) {
      float cs[8] = {0.f, 0.f, 0.f, 0.f, 0.f, 0.f, 0.f, 0.f};
      tr8_store<288, true>(v[b], da + (unsigned)(b * 18432), cs, std::make_integer_sequence<int, 8>{});
      const f32x4 c0 = {cs[0], cs[1], cs[2], cs[3]}, c1 = {cs[4], cs[5], cs[6], cs[7]};
      const unsigned ca = lds0 + (unsigned)(3 * SB + wl * 2304) + sa * 2u;
      asm volatile("ds_write_b128 %0, %1 offset:%2" ::"v"(ca), "v"(c0), "n"(b * 1152) : "memory");
      asm volatile("ds_write_b128 %0, %1 offset:%2" ::"v"(ca), "v"(c1), "n"(b * 1152 + 16) : "memory");
    }
    lds_inc(lds0 + (unsigned)(LW_OFF + LW_ST * 4));
    lds_spin(lds0 + (unsigned)(LW_OFF + LW_ST * 4), 4u);
    if (isi) {
      const int j = (job & 7) * 4 + wl, ti = j < 16 ? 2 * j + 1 : 2 * (j - 16);
      lds_out<9216>(lds0 + (unsigned)(wl * 9216), ws + OFF_PI + (size_t)(ti * 18 + kb) * 9216, lane);
      f32x4 c[4];
      const unsigned ca = lds0 + (unsigned)(3 * SB + wl * 576) + sa;
#pragma unroll
      for (int i = 0; i < 4; ++i) asm volatile("ds_read_b128 %0, %1 offset:%2" : "=v"(c[i]) : "v"(ca), "n"(i * 2304) : "memory");
      asm volatile("s_waitcnt lgkmcnt(0)" : "+v"(c[0]), "+v"(c[1]), "+v"(c[2]), "+v"(c[3]));
      const f32x4 t = (c[0] + c[1]) + (c[2] + c[3]);
      if (lane < 36) pk_st16(ws + OFF_CSP + ((size_t)kb * F_ + ti * 144) * 4 + sa, __builtin_bit_cast(u32x4, t));
    } else {
      lds_out<4608>(lds0 + (unsigned)(wl * 9216), ws + OFF_PW + (size_t)(wl * 144 + kb) * 4608, lane);
      lds_out<4608>(lds0 + (unsigned)(wl * 9216 + 4608), ws + OFF_PW + (size_t)(wl * 144 + 72 + kb) * 4608, lane);
    }
  }
  MF(8);
  announce(lds0 + (unsigned)(LW_OFF + LW_PK * 4), 4u, ws, CN_PK, isi ? 8 + (job & 3) * 4 + lane : 24 + lane, JOB && lane < (isi ? 4 : 8));
  MF(9);
  if (wl == 0) {
    asm volatile("" : "+v"(f));
#pragma unroll 1
    for (unsigned spin = 0; spin < SPIN_MEM; ++spin) {
      if (!__any(f[0] != (unsigned)tag || f[1] != (unsigned)(tag >> 32))) break;
      __builtin_amdgcn_s_sleep(2);
      asm volatile("global_load_dwordx2 %0, %1, off sc1\n\ts_waitcnt vmcnt(0)" : "=&v"(f) : "v"(tp) : "memory");
    }
  }
}

template <int MT, int NT>
DEVI void k1_gemm(char* smem, const char* ws, int rt, int hp, int mt0, int nt0,
                  bool late, int lane MK_ARG) {
  const unsigned lds0 = (unsigned)(size_t)smem;
  const int g = lane >> 4;
  f32x4 cs[NT];
  unsigned ta[NT];
  unsigned cad[NT];
#pragma unroll
  for (int nt = 0; nt < NT; ++nt) {
    int c = (nt0 + nt) * 16 + g * 4;
    if (c > 104) c = 104;
    const int part = c / 36, ci = c - part * 36;
    cad[nt] = lds0 + (unsigned)(K1_CS_OFF + (part * 40 + (hp & 1) * 4 + ci) * 4);
    asm volatile("" : "+v"(cad[nt]));
    const int to = (nt0 + nt) * 16 + g * 4 > 104 ? 120 : part * 80 + ci * 2 + (ci >= 20 ? 8 : 0);
    ta[nt] = lds0 + (unsigned)(K1_T_OFF + (mt0 * 16 + (lane & 15)) * K1_TP + to);
    asm volatile("" : "+v"(ta[nt]));
  }
  unsigned sa = lds0 + (unsigned)(K1_ST_OFF + (mt0 * 16 + (lane & 15)) * 8);
  asm volatile("" : "+v"(sa));
  f32x4 acc[MT][NT];
#pragma unroll
  for (int mt = 0; mt < MT; ++mt)
#pragma unroll
    for (int nt = 0; nt < NT; ++nt) acc[mt][nt] = f32x4{0.f, 0.f, 0.f, 0.f};
  f32x2 st[MT];
  if (late) bar();
  if constexpr (NT == 7) cnt_wait(ws, CN_X2, rt * 16 + hp, rt ? 24u : 16u);
  bar();
  MK(1);
  {
    GemmMath<K1C, MT, NT, (NT == 7 ? 5 : 3), (NT == 3 ? 6 : NT), true, 2, false, K1_WS> gm(acc);
    gm.setup(lds0, mt0, nt0, lane, 0u);
    bar();
#pragma unroll
    for (int nt = 0; nt < NT; ++nt) asm volatile("ds_read_b128 %0, %1" : "=v"(cs[nt]) : "v"(cad[nt]));
    gm.first();
    gm.template mid<8>();
#pragma unroll
    for (int mt = 0; mt < MT; ++mt) asm volatile("ds_read_b64 %0, %1 offset:%2" : "=v"(st[mt]) : "v"(sa), "n"(mt * 128));
    asm volatile("s_waitcnt vmcnt(0)" ::: "memory");
    gm.last();
  }
#pragma unroll
  for (int nt = 0; nt < NT; ++nt) asm volatile("" : "+v"(cs[nt]));
#pragma unroll
  for (int mt = 0; mt < MT; ++mt) asm volatile("" : "+v"(st[mt]));
  MK(2);
#pragma unroll
  for (int mt = 0; mt < MT; ++mt) {
    f32x2 M = {st[mt][0], st[mt][0]}, R = {st[mt][1], st[mt][1]};
    asm volatile("" : "+v"(M), "+v"(R));
#pragma unroll
    for (int nt = 0; nt < NT; ++nt) {
      const f32x2 v0 = R * __builtin_elementwise_fma(M, f32x2{cs[nt][0], cs[nt][1]}, f32x2{acc[mt][nt][0], acc[mt][nt][1]});
      const f32x2 v1 = R * __builtin_elementwise_fma(M, f32x2{cs[nt][2], cs[nt][3]}, f32x2{acc[mt][nt][2], acc[mt][nt][3]});
      acc[mt][nt][0] = __uint_as_float(pack2v(v0));
      acc[mt][nt][1] = __uint_as_float(pack2v(v1));
      const f32x2 pk = {acc[mt][nt][0], acc[mt][nt][1]};
      asm volatile("ds_write_b64 %0, %1 offset:%2" ::"v"(ta[nt]), "v"(pk), "n"(mt * 16 * K1_TP) : "memory");
    }
  }
}

DEVI float vmax(float a, float b) {
  float r;
  asm("v_max_f32 %0, %1, %2" : "=v"(r) : "v"(a), "v"(b));
  return r;
}
struct Attn {
  unsigned ka, kb, va, vb, m0, m1, m2, pm[2], oa, pa;
  f32x4 mk;
  unsigned long long sel[4];
  const char* ob;
  DEVI void setup(unsigned T, char* o, int r0, int hp, int wave, int lane) {
    const int g = lane >> 4, r = lane & 15, blk = wave >> 1, hd = wave & 1;
    const unsigned co = (unsigned)(g < 2 ? hd * 48 + g * 16 : 32);
    ka = T + (unsigned)((blk * 32 + 2 * r) * K1_TP) + co;
    kb = T + (unsigned)((blk * 32 + 2 * (r & 7)) * K1_TP) + co;
    const int vr = 4 * g + ((lane >> 2) & 3);
    va = T + (unsigned)((blk * 32 + 2 * vr) * K1_TP + (lane & 3) * 8);
    vb = T + (unsigned)((blk * 32 + 2 * (vr & 7)) * K1_TP + (lane & 3) * 8);
    m0 = (g < 2 || (g == 2 && hd == 0)) ? ~0u : 0u;
    m1 = (g < 2 || (g == 2 && hd == 1)) ? ~0u : 0u;
    m2 = g < 2 ? ~0u : 0u;
    pm[0] = pm[1] = 0u;
    const float ci = __uint_as_float(vconst(0xff800000u));
    const unsigned ch = vconst(0xffffu);
#pragma unroll
    for (int e = 0; e < 4; ++e) {
      const int w1 = r + 8 - 4 * g - e;
      mk[e] = (w1 & 15) <= 7 ? 0.f : ci;
      sel[e] = __ballot(w1 >= 8);
      if (w1 >= 8) pm[e >> 1] |= ch << (16 * (e & 1));
    }
    oa = (unsigned)((8 + hp * 4 + hd * 2 + (g >> 1)) * 1024 + ((blk & 1) * 32 + 2 * r + (g & 1)) * 16);
    pa = ((oa & 1023u) >> 2) + (unsigned)((hp * 2 + hd) * 256);
    ob = uni_ptr(o + (size_t)((r0 >> 6) + (wave >> 2)) * (72 * 1024));
    asm volatile("" : "+v"(ka), "+v"(kb), "+v"(va), "+v"(vb), "+v"(m0), "+v"(m1), "+v"(m2), "+v"(pm[0]), "+v"(pm[1]), "+v"(oa));
    asm volatile("" : "+v"(mk), "+v"(pa), "+s"(ob), "+s"(sel[0]), "+s"(sel[1]), "+s"(sel[2]), "+s"(sel[3]));
  }
  DEVI void run(int r0, int hp, int wave, int lane) {
    const int hd = wave & 1;
    u32x4 q[2], k0[2], k1[2];
    u32x2 v[2][2][2];
#pragma unroll
    for (int p = 0; p < 2; ++p) {
      asm volatile("ds_read_b128 %0, %1 offset:%2" : "=v"(q[p]) : "v"(ka), "n"((16 + p) * K1_TP) : "memory");
      asm volatile("ds_read_b128 %0, %1 offset:%2" : "=v"(k0[p]) : "v"(ka), "n"(p * K1_TP + 80) : "memory");
      asm volatile("ds_read_b128 %0, %1 offset:%2" : "=v"(k1[p]) : "v"(kb), "n"((32 + p) * K1_TP + 80) : "memory");
    }
    const unsigned vh = (unsigned)hd * 48u;
#pragma unroll
    for (int p = 0; p < 2; ++p) {
      asm volatile("ds_read_b64_tr_b16 %0, %1 offset:%2" : "=v"(v[p][0][0]) : "v"(va + vh), "n"(p * K1_TP + 160) : "memory");
      asm volatile("ds_read_b64_tr_b16 %0, %1 offset:%2" : "=v"(v[p][0][1]) : "v"(vb + vh), "n"((32 + p) * K1_TP + 160) : "memory");
      asm volatile("ds_read_b64_tr_b16 %0, %1 offset:%2" : "=v"(v[p][1][0]) : "v"(va), "n"(p * K1_TP + 192) : "memory");
      asm volatile("ds_read_b64_tr_b16 %0, %1 offset:%2" : "=v"(v[p][1][1]) : "v"(vb), "n"((32 + p) * K1_TP + 192) : "memory");
    }
    const u32x4 one4 = {vconst(0x3f803f80u), vconst(0x3f803f80u), vconst(0x3f803f80u), vconst(0x3f803f80u)};
    const s16x8 ones = __builtin_bit_cast(s16x8, one4);
    const float c = 0.23570226039551584f * 1.4426950408889634f;
    const f32x4 z = {0.f, 0.f, 0.f, 0.f};
    f32x4 sc[2][2];
#pragma unroll
    for (int p = 0; p < 2; ++p) {
      asm volatile("s_waitcnt lgkmcnt(%3)" : "+v"(q[p]), "+v"(k0[p]), "+v"(k1[p]) : "n"(11 - 3 * p));
      const s16x8 qf = __builtin_bit_cast(s16x8, q[p] & u32x4{m0, m1, m2, m2});
      sc[p][0] = __builtin_amdgcn_mfma_f32_16x16x32_bf16(__builtin_bit_cast(s16x8, k0[p]), qf, mk, 0, 0, 0);
      sc[p][1] = __builtin_amdgcn_mfma_f32_16x16x32_bf16(__builtin_bit_cast(s16x8, k1[p]), qf, mk, 0, 0, 0);
      __builtin_amdgcn_sched_barrier(0);
    }
    if (__builtin_expect(r0 == 0 && wave < 2, 0)) {
#pragma unroll
      for (int p = 0; p < 2; ++p)
#pragma unroll
        for (int e = 0; e < 4; ++e)
          if (lane < 32) sc[p][0][e] = mk[e] - 4.242640687e9f;
    }
    float lg[2][4], mx[2];
#pragma unroll
    for (int p = 0; p < 2; ++p) {
#pragma unroll
      for (int e = 0; e < 4; ++e) lg[p][e] = __builtin_amdgcn_inverse_ballot_w64(sel[e]) ? sc[p][1][e] : sc[p][0][e];
      asm("v_max3_f32 %0, %1, %2, %3\n\tv_max_f32 %0, %0, %4" : "=&v"(mx[p]) : "v"(lg[p][0]), "v"(lg[p][1]), "v"(lg[p][2]), "v"(lg[p][3]));
    }
    {
      const auto a = __builtin_amdgcn_permlane32_swap(__float_as_uint(mx[0]), __float_as_uint(mx[1]), false, false);
      const unsigned b = __float_as_uint(vmax(__uint_as_float(a[0]), __uint_as_float(a[1])));
      const auto d = __builtin_amdgcn_permlane16_swap(b, b, false, false);
      const unsigned f = __float_as_uint(vmax(__uint_as_float(d[0]), __uint_as_float(d[1])) * -c);
      const auto h = __builtin_amdgcn_permlane32_swap(f, f, false, false);
      mx[0] = __uint_as_float(h[0]);
      mx[1] = __uint_as_float(h[1]);
    }
#pragma unroll
    for (int p = 0; p < 2; ++p)
#pragma unroll
      for (int e = 0; e < 4; ++e) lg[p][e] = __builtin_amdgcn_exp2f(__builtin_fmaf(lg[p][e], c, mx[p]));
    asm volatile("s_waitcnt lgkmcnt(0)"
                 : "+v"(v[0][0][0]), "+v"(v[0][0][1]), "+v"(v[0][1][0]), "+v"(v[0][1][1]), "+v"(v[1][0][0]), "+v"(v[1][0][1]),
                   "+v"(v[1][1][0]), "+v"(v[1][1][1]));
    __builtin_amdgcn_sched_barrier(0);
    f32x4 su[2], o0[2], o1[2];
    s16x8 pp[2];
#pragma unroll
    for (int p = 0; p < 2; ++p) {
      const unsigned p0 = pack2(lg[p][0], lg[p][1]), p1 = pack2(lg[p][2], lg[p][3]);
      pp[p] = __builtin_bit_cast(s16x8, u32x4{p0 & ~pm[0], p1 & ~pm[1], p0 & pm[0], p1 & pm[1]});
    }
    __builtin_amdgcn_sched_barrier(0);
#pragma unroll
    for (int p = 0; p < 2; ++p) {
      const s16x8 pf = pp[p];
      const u32x4 v0 = {v[p][0][0][0], v[p][0][0][1], v[p][0][1][0], v[p][0][1][1]};
      const u32x4 v1 = {v[p][1][0][0], v[p][1][0][1], v[p][1][1][0], v[p][1][1][1]};
      su[p] = __builtin_amdgcn_mfma_f32_16x16x32_bf16(ones, pf, z, 0, 0, 0);
      o0[p] = __builtin_amdgcn_mfma_f32_16x16x32_bf16(__builtin_bit_cast(s16x8, v0), pf, z, 0, 0, 0);
      o1[p] = __builtin_amdgcn_mfma_f32_16x16x32_bf16(__builtin_bit_cast(s16x8, v1), pf, z, 0, 0, 0);
    }
    __builtin_amdgcn_sched_barrier(0);
    u32x2 y[2], yl;
    unsigned hm = (unsigned)hd - 1u;
    asm volatile("" : "+s"(hm));
#pragma unroll
    for (int p = 0; p < 2; ++p) {
      const float inv = __builtin_amdgcn_rcpf(su[p][0]);
      y[p] = u32x2{pack2(o0[p][0] * inv, o0[p][1] * inv), pack2(o0[p][2] * inv, o0[p][3] * inv)};
      const unsigned la = pack2(o1[p][0] * inv, o1[p][1] * inv), lb = pack2(o1[p][2] * inv, o1[p][3] * inv);
      yl[p] = (la & hm) | (lb & ~hm);
    }
    const auto s0 = __builtin_amdgcn_permlane16_swap(y[0][0], y[1][0], false, false);
    const auto s1 = __builtin_amdgcn_permlane16_swap(y[0][1], y[1][1], false, false);
    const u32x4 w = {s0[0], s1[0], s0[1], s1[1]};
    asm volatile("global_store_dwordx4 %0, %1, %4 sc1\n\ts_mov_b64 exec, 0xffff\n\tglobal_store_dwordx2 %2, %3, %4 sc1\n\ts_mov_b64 exec, -1\n\ts_nop 0" ::"v"(oa),
                 "v"(w), "v"(pa), "v"(yl), "s"(ob)
                 : "memory");
  }
};

#define NL_MAIN 663
DEVI void math_start(unsigned lds0, char* ws, int bid, int wave, int lane) {
  static_assert(2 * SZ_ZP == (size_t)256 * 2048 && NCNT <= 16 && OFF_CNT + NCNT * 32768 < 0xffffffffu, "mark split");
  const unsigned m1 = vconst(~0u), l = (unsigned)lane;
  const unsigned hi = wave < 2 ? 128u : 32768u;
  unsigned b0 = (unsigned)OFF_ZP + (unsigned)(bid * 2 + wave) * 1024u, b1 = (unsigned)OFF_CNT + (unsigned)((wave & 1) * 2048 + bid) * 128u;
  asm volatile("" : "+s"(b0), "+s"(b1));
  const unsigned base = wave < 2 ? b0 : b1;
  const unsigned long long act = wave < 3 ? ~0ull : (wave == 3 ? (1ull << ((NCNT - 8) * 8)) - 1ull : 0ull);
  asm volatile("s_mov_b64 exec, %3\n\tglobal_store_dwordx4 %0, %1, %2 sc1\n\ts_mov_b64 exec, -1\n\ts_nop 0" ::"v"(base + (l >> 3) * hi + (l & 7u) * 16u),
               "v"(u32x4{m1, m1, m1, m1}), "s"(ws), "s"(act)
               : "memory");
  unsigned a4 = (l < 32u ? (l >> 3) * (unsigned)K1R::SB : (unsigned)LW_OFF) + (l & 7u) * 16u, a0 = (unsigned)(wave * K1R::SB + K1R::XB + 240) + l * 288u;
  asm volatile("" : "+v"(a4), "+v"(a0));
  const unsigned a = wave < 4 ? a0 : a4;
  const unsigned v = (wave < 4 || l < 32u) ? 0x3f803f80u : 0u;
  asm volatile("s_mov_b64 exec, %2\n\tds_write_b128 %0, %1\n\ts_mov_b64 exec, -1\n\ts_waitcnt lgkmcnt(0)" ::"v"(lds0 + a), "v"(u32x4{v, v, v, v}),
               "s"(wave < 4 ? ~0ull : (wave == 4 ? 0xffffffffffull : 0ull))
               : "memory");
}

template <int NST, int NS, int NSW, int WOFF, int XW, int X0, int S0, unsigned BAR>
struct ResX {
  static constexpr int WS = 4 * 4608, R = 10, V1 = 5 * NS, NV = V1 + 10 * (NST - NS), NG = (NSW + 2) / 3;
  s16x8 xf[XW][2];
  s16x8 wf[R];
  unsigned wrd[2][NG], xo, ea, fa, fv, tag;
  const char* xp[3];
  f32x4 (&acc)[2][5];
  DEVI ResX(f32x4 (&a)[2][5]) : acc(a) {}
  static constexpr int st(int V) { return V < V1 ? V / 5 : NS + (V - V1) / 10; }
  static constexpr int hf(int V) { return V < V1 ? 0 : ((V - V1) / 5) & 1; }
  static constexpr bool first(int V) { return V < V1 ? V % 5 == 0 : (V - V1) % 10 == 0; }
  static constexpr int wreq(int V) {
    int n = V + 5;
    if (V >= V1 - 4) n += V < V1 ? V - (V1 - 5) : 4;
    return n < NV ? n : NV;
  }
  static constexpr int XU = V1 + 1, XF = V1 + 11, XR = V1 + 15, XC = V1 + 21;
  static constexpr int oth(int V) { return NS && ((V >= XU && V <= XF && (V - XU) % 2 == 0) || V == XR || (V > XC && V <= XC + 5)) ? 1 : 0; }
  static constexpr int newer(int V) {
    const int F = V + 1 < NV ? V + 1 : V;
    int v = V - 1, n = 0;
    for (; v >= 0 && wreq(v - 1) > F; --v) n += wreq(v) - wreq(v - 1) + oth(v);
    return n + wreq(v) - 1 - F + (v >= 0 ? oth(v) : 0);
  }
  static constexpr int xreq(int T) {
    int n = X0 + 2 * (T + 1);
    if (n > T + XW) n = T + XW;
    if (NS && T <= 11 && n > 18) n = 18;
    if (T < 0) n = X0;
    return n < NST ? n : NST;
  }
  static_assert(NS == 0 || (xreq(NS - 2) == NST && XC + 5 < NV), "the last steps only compute and swap");
  template <int T, int M>
  DEVI void xld() {
    asm volatile("global_load_dwordx4 %0, %1, %2 sc1" : "=v"(xf[T % XW][M]) : "v"(xo), "s"(xp[M == 0 ? 0 : (T < NS ? 1 : 2)]));
    if constexpr (M == 1 && T + 1 < NST) asm volatile("v_add_u32 %0, 0x2000, %0" : "+v"(xo));
  }
  template <int V>
  DEVI void rd() {
    constexpr int T = st(V), sl = (T / 2 + S0) % NSW;
    asm volatile("ds_read_b128 %0, %1 offset:%2" : "=v"(wf[V % R]) : "v"(wrd[hf(V)][sl / 3]), "n"((sl % 3) * WS + (T & 1) * 9216 + (V % 5) * 32));
  }
  template <int V, int M, int H>
  DEVI void mf() {
    acc[M][V % 5] = __builtin_amdgcn_mfma_f32_16x16x32_bf16(wf[V % R], xf[st(V) % XW][H], acc[M][V % 5], 0, 0, 0);
    __builtin_amdgcn_sched_barrier(0);
  }
  template <int V>
  DEVI void unit() {
    constexpr int T = st(V), n = V % 5, nx = xreq(T) - xreq(T - 1), nw = wreq(V) - wreq(V - 1);
    constexpr int vm = 2 * (xreq(T - 1) - 1 - T);
    static_assert(newer(V) <= 15, "lgkmcnt range");
    if constexpr (first(V) && V % 2 == 0) asm volatile("s_waitcnt vmcnt(%0) lgkmcnt(%1)" ::"n"(vm), "n"(newer(V)));
    else if constexpr (first(V)) asm volatile("s_waitcnt vmcnt(%0)" ::"n"(vm));
    else if constexpr (V % 2 == 0) asm volatile("s_waitcnt lgkmcnt(%0)" ::"n"(newer(V)));
    __builtin_amdgcn_sched_barrier(0);
    if constexpr (V < V1) mf<V, V & 1, V & 1>();
    else mf<V, 0, hf(V)>();
    if constexpr (nw > 0) rd<wreq(V - 1)>();
    __builtin_amdgcn_sched_barrier(0);
    if constexpr (V < V1) {
      mf<V, 1 - (V & 1), 1 - (V & 1)>();
      if constexpr (nw > 1) rd<wreq(V - 1) + 1>();
      if constexpr (nx == 2 && n < 4) xld<xreq(T - 1) + n / 2, n % 2>();
      if constexpr (nx == 1 && n == (T & 1)) xld<xreq(T - 1), 0>();
      if constexpr (nx == 1 && n == (T & 1) + 2) xld<xreq(T - 1), 1>();
      if constexpr (n == 4 && (T & 1) && ((BAR >> (T / 2)) & 1u)) bar();
    } else if constexpr (NS == 0) {
      constexpr int k = (V % 10) / 2;
      if constexpr (V % 2 == 0 && k < 2 * nx) xld<xreq(T - 1) + k / 2, k % 2>();
      if constexpr (V % 10 == 8 && nx > 0 && xreq(T) == NST)
        asm volatile("s_mov_b64 exec, 1\n\tds_add_u32 %0, %1\n\ts_mov_b64 exec, -1" ::"v"(fa), "v"(fv));
    } else {
      if constexpr (oth(V) && V < XF) asm volatile("ds_write_b128 %0, %1 offset:%2" ::"v"(ea), "v"(acc[1][(V - XU) / 2]), "n"((V - XU) * 512));
      if constexpr (V == XF) asm volatile("ds_write_b32 %0, %1" ::"v"(fa), "v"(fv));
      if constexpr (V == XF + 2) asm volatile("v_xor_b32 %0, 4, %0" : "+v"(fa));
      if constexpr (V == XR) asm volatile("ds_read_b32 %0, %1" : "+v"(fv) : "v"(fa));
      if constexpr (V == XR + 2) asm volatile("v_xor_b32 %0, 0x2000, %0" : "+v"(ea));
      if constexpr (V == XC) {
        unsigned c = 1u << 18;
        asm volatile(
            "s_waitcnt lgkmcnt(%4)\n\tv_cmp_ne_u32 vcc, %3, %0\n\ts_cbranch_vccnz .Lw%=\n\t.subsection 1\n.Lw%=:\n\tds_read_b32 %0, %2\n\t"
            "s_waitcnt lgkmcnt(0)\n\tv_cmp_ne_u32 vcc, %3, %0\n\t" SAFE_OUT(".Lr%=") "s_cbranch_vccnz .Lw%=\n\ts_branch .Lr%=\n\t.subsection 0\n.Lr%=:"
            : "+v"(fv), NOP_(c)
            : "v"(fa), "s"(tag), "n"(wreq(XC) - wreq(XR))
            : "vcc", "scc");
      }
      if constexpr (oth(V) && V > XC) asm volatile("ds_read_b128 %0, %1 offset:%2" : "=v"(acc[1][V - XC - 1]) : "v"(ea), "n"((V - XC - 1) * 1024));
    }
    __builtin_amdgcn_sched_barrier(0);
  }
  template <int... V>
  DEVI void units(std::integer_sequence<int, V...>) {
    (unit<V>(), ...);
  }
  DEVI void run() {
    units(std::make_integer_sequence<int, NV>{});
    if constexpr (NS > 0) {
      asm volatile("s_waitcnt lgkmcnt(0)" : "+v"(acc[1][0]), "+v"(acc[1][1]), "+v"(acc[1][2]), "+v"(acc[1][3]), "+v"(acc[1][4]));
#pragma unroll
      for (int i = 0; i < 5; ++i) acc[0][i] += acc[1][i];
    }
  }
  template <int... V>
  DEVI void pro(std::integer_sequence<int, V...>) {
    (rd<V>(), ...);
  }
  DEVI void prow() { pro(std::make_integer_sequence<int, wreq(-1)>{}); }
  template <int T0, int... T>
  DEVI void prox(std::integer_sequence<int, T...>) {
    ((xld<T0 + T, 0>(), xld<T0 + T, 1>()), ...);
  }
  DEVI void setup(unsigned lds0, const char* xbase, int wave, int lane, unsigned red, unsigned nth) {
    const int g = lane >> 4, r = lane & 15, kq = wave & 1;
    xp[0] = xbase + wave * 256;
    xp[1] = xbase + (wave ^ 1) * 256;
    xp[2] = xp[0] + (kq ? -4096 : 4096);
    xo = (unsigned)((4 * kq + g) * 1024 + r * 16);
#pragma unroll
    for (int i = 0; i < 2 * NG; ++i) {
      wrd[i / NG][i % NG] = lds0 + (unsigned)(WOFF + (i % NG) * 3 * WS + (kq ^ (i / NG)) * 4608 + (2 * (r & 7) + 16 * (g & 1) + (g >> 1)) * 144 + (r >> 3) * 16);
      if (i < NG || NS == 0 || i % NG == ((NS / 2 + S0) % NSW) / 3) asm volatile("" : "+v"(wrd[i / NG][i % NG]));
    }
    asm volatile("" : "+v"(xo));
    if constexpr (NS > 0) {
      ea = lds0 + red + (unsigned)((wave ^ 1) * 8192 + lane * 16);
      fa = lds0 + (unsigned)(LW_OFF + (LW_PAIR + wave) * 4);
      fv = tag = nth;
      asm volatile("" : "+v"(ea), "+v"(fa), "+v"(fv));
    }
  }
  template <int N>
  DEVI void init(u32x2 (&xv)[5]) {
    asm volatile("s_waitcnt vmcnt(%5)" : "+v"(xv[0]), "+v"(xv[1]), "+v"(xv[2]), "+v"(xv[3]), "+v"(xv[4]) : "n"(N));
#pragma unroll
    for (int i = 0; i < 5; ++i) {
      acc[0][i] = f32x4{bf_lo(xv[i][0]), bf_hi(xv[i][0]), bf_lo(xv[i][1]), bf_hi(xv[i][1])};
      asm volatile("" : "+v"(acc[0][i]));
      acc[1][i] = f32x4{0.f, 0.f, 0.f, 0.f};
    }
  }
};

constexpr int P5_XW = 8, P5_X0 = 5, P2_X0 = 4;
constexpr unsigned P5_BAR = 0x2aaau;
constexpr int P5_W = 15 * 1024;
constexpr int P2_W = K1_ST_OFF - 18 * 4608;
constexpr int P5_RED = 0;
static_assert(P5_RED % 16384 == 0 && P5_RED + 3 * 8192 + 5120 <= P5_W + 18432, "exchange area");
static_assert(P2_W >= K1_T_OFF + 144 * K1_TP + 64 && P2_W % 16 == 0 && P5_W + 8 * 18432 <= LW_OFF, "LDS placement");

struct ResIn {
  const char* xp;
  unsigned o, o2, st;
  DEVI void setup(const char* src, bool rm, int rb, int ct, int wave, int lane) {
    const int g = lane >> 4, g2 = g & 1, rl = wave * 16 + (lane & 15);
    xp = uni_ptr(rm ? src + ((size_t)rb * 64 * D_ + ct * 72) * 2 : src + (size_t)(rb * 72 + ct * 9) * 1024);
    o = rm ? (unsigned)(rl * (D_ * 2) + g * 8) : (unsigned)((g >> 1) * 1024 + rl * 16 + g2 * 8);
    o2 = rm ? (unsigned)(rl * (D_ * 2) + g2 * 8 + 128) : (unsigned)(8192 + rl * 16 + g2 * 8);
    st = rm ? 32u : 2048u;
    asm volatile("" : "+v"(o), "+v"(o2));
    asm volatile("" : "+s"(xp));
  }
  DEVI void load(u32x2 (&xv)[5]) {
#pragma unroll
    for (int i = 0; i < 4; ++i) asm volatile("global_load_dwordx2 %0, %1, %2 sc1" : "=v"(xv[i]) : "v"(o), "s"(xp + i * st));
    asm volatile("global_load_dwordx2 %0, %1, %2 sc1" : "=v"(xv[4]) : "v"(o2), "s"(xp));
  }
};

DEVI void p2_gemm(f32x4 (&acc)[2][5], u32x2 (&xv)[5], unsigned lds0, const char* ob, int layer, int wave, int lane MK_ARG) {
  ResX<9, 0, 5, P2_W, 9, P2_X0, 0, 0u> gm(acc);
  gm.setup(lds0, ob, wave, lane, 0u, 0u);
  unsigned a = (gm.xo & ~1023u) | ((gm.xo & 1023u) >> 2);
  gm.xo += 8192u;
  gm.fa = lds0 + (unsigned)(LW_OFF + (LW_T + layer) * 4);
  gm.fv = vconst(1u);
  asm volatile("" : "+v"(a), "+v"(gm.xo), "+v"(gm.fa));
  const char* pp[2] = {gm.xp[0] - wave * 192, gm.xp[2] - wave * 192};
  place();
  bar();
  MK(6);
  unsigned e[2][4];
#pragma unroll
  for (int d = 0; d < 8; ++d)
    asm volatile("global_load_dword %0, %1, %2 offset:%3 sc1" : "=v"(e[d / 4][d % 4]) : "v"(a), "s"(pp[d / 4]), "n"((d % 4) * 256));
  gm.template prox<1>(std::make_integer_sequence<int, P2_X0 - 1>{});
  gm.prow();
  gm.template init<6 + 2 * P2_X0>(xv);
  asm volatile("s_waitcnt vmcnt(%8)"
               : "+v"(e[0][0]), "+v"(e[0][1]), "+v"(e[0][2]), "+v"(e[0][3]), "+v"(e[1][0]), "+v"(e[1][1]), "+v"(e[1][2]), "+v"(e[1][3])
               : "n"(2 * P2_X0 - 2));
  gm.xf[0][0] = __builtin_bit_cast(s16x8, u32x4{e[0][0], e[0][1], e[0][2], e[0][3]});
  gm.xf[0][1] = __builtin_bit_cast(s16x8, u32x4{e[1][0], e[1][1], e[1][2], e[1][3]});
  __builtin_amdgcn_sched_barrier(0);
  gm.run();
}

struct ResOut {
  const char *xp[3], *sp;
  unsigned xo, so;
  DEVI void setup(char* xim, char* xs, u16* out, bool fin, int rb, int ct, int wave, int lane) {
    const int g = lane >> 4, rl = wave * 16 + (lane & 15);
    xp[0] = uni_ptr(fin ? (const char*)(out + (size_t)rb * 64 * D_ + ct * 72) : xim + (size_t)(rb * 72 + ct * 9) * 1024);
    xp[1] = xp[0] + (fin ? 64 : 4096);
    xp[2] = xp[0] + (fin ? 128 : 8192);
    sp = uni_ptr(xs + (size_t)(rb * 8 + ct) * 512);
    xo = (unsigned)(((g & 1) * 2 + (g >> 1)) * (fin ? 16 : 1024) + rl * (fin ? D_ * 2 : 16));
    so = (unsigned)(rl * 8);
    asm volatile("" : "+v"(xo), "+v"(so), "+s"(xp[0]), "+s"(xp[1]), "+s"(xp[2]), "+s"(sp));
  }
  DEVI void run(const f32x4 (&a)[5], bool fin) {
    u32x2 y[5];
    u32x4 v[3];
#pragma unroll
    for (int nt = 0; nt < 5; ++nt) y[nt] = u32x2{pack2(a[nt][0], a[nt][1]), pack2(a[nt][2], a[nt][3])};
#pragma unroll
    for (int i = 0; i < 3; ++i) {
      const auto s0 = __builtin_amdgcn_permlane16_swap(y[2 * i][0], y[i < 2 ? 2 * i + 1 : 4][0], false, false);
      const auto s1 = __builtin_amdgcn_permlane16_swap(y[2 * i][1], y[i < 2 ? 2 * i + 1 : 4][1], false, false);
      v[i] = u32x4{s0[0], s1[0], s0[1], s1[1]};
    }
    if (fin) {
      asm volatile("global_store_dwordx4 %0, %1, %4 nt\n\tglobal_store_dwordx4 %0, %2, %5 nt\n\ts_mov_b64 exec, 0xffff\n\tglobal_store_dwordx4 %0, %3, %6 nt\n\t"
                   "s_mov_b64 exec, -1\n\ts_nop 0" ::"v"(xo), "v"(v[0]), "v"(v[1]), "v"(v[2]), "s"(xp[0]), "s"(xp[1]), "s"(xp[2])
                   : "memory");
      return;
    }
    asm volatile("global_store_dwordx4 %0, %1, %4 sc1\n\tglobal_store_dwordx4 %0, %2, %5 sc1\n\ts_mov_b64 exec, 0xffff\n\t"
                 "global_store_dwordx4 %0, %3, %6 sc1\n\ts_mov_b64 exec, -1\n\ts_nop 0" ::"v"(xo), "v"(v[0]), "v"(v[1]), "v"(v[2]),
                 "s"(xp[0]), "s"(xp[1]), "s"(xp[2])
                 : "memory");
    const u32x4 one4 = {vconst(0x3f803f80u), vconst(0x3f803f80u), vconst(0x3f803f80u), vconst(0x3f803f80u)};
    const bool lo = __builtin_amdgcn_inverse_ballot_w64(0xffffull);
    v[2] = u32x4{lo ? v[2][0] : 0u, lo ? v[2][1] : 0u, lo ? v[2][2] : 0u, lo ? v[2][3] : 0u};
    f32x4 s = {0.f, 0.f, 0.f, 0.f}, q = s;
#pragma unroll
    for (int i = 0; i < 3; ++i) {
      const s16x8 u = __builtin_bit_cast(s16x8, v[i]);
      q = __builtin_amdgcn_mfma_f32_16x16x32_bf16(u, u, q, 0, 0, 0);
      s = __builtin_amdgcn_mfma_f32_16x16x32_bf16(__builtin_bit_cast(s16x8, one4), u, s, 0, 0, 0);
    }
    float q0 = q[0], q1 = q[1], q2 = q[2], q3 = q[3];
    const bool b0 = __builtin_amdgcn_inverse_ballot_w64(0xaaaaaaaaaaaaaaaaull), b1 = __builtin_amdgcn_inverse_ballot_w64(0xccccccccccccccccull);
    q0 = b0 ? q1 : q0;
    q2 = b0 ? q3 : q2;
    asm("" : "+v"(q0), "+v"(q2));
    const f32x2 sq = {s[0], b1 ? q2 : q0};
    asm volatile("s_mov_b64 exec, %3\n\tglobal_store_dwordx2 %0, %1, %2 sc1\n\ts_mov_b64 exec, -1" ::"v"(so), "v"(sq), "s"(sp), "s"(0xf0000f0000f0000full) : "memory");
  }
};

using K3C = GemmCfg<128, 144>;
constexpr int K3_Z1_OFF = K3C::LDS_BYTES;
constexpr int K3_C2_OFF = K3_Z1_OFF + K3C::XB;
constexpr int K3_ZC_OFF = K3_C2_OFF + 2048;
constexpr int K3_DM_OFF = K3_ZC_OFF + 1024;
constexpr int K3_ST_OFF = K3_Z1_OFF + 4 * 5120;
constexpr int K3_C1_OFF = K3_ST_OFF + 2048;
constexpr int K3_BS_OFF = K3_C1_OFF + 576;
static_assert(K3_BS_OFF + 512 <= LW_OFF, "K3 LDS");
static_assert(P5_W + 19 * 1024 == K3C::SB && K3C::SB == 34 * 1024, "w_out ring");

DEVI void red4(float (&t)[4], float& s, float& q) {
#pragma unroll
  for (int j = 0; j < 2; ++j) {
    const auto p = __builtin_amdgcn_permlane16_swap(__float_as_uint(t[j]), __float_as_uint(t[2 + j]), false, false);
    const unsigned v = __float_as_uint(__uint_as_float(p[0]) + __uint_as_float(p[1]));
    const auto w = __builtin_amdgcn_permlane32_swap(v, v, false, false);
    t[j] = __uint_as_float(w[0]) + __uint_as_float(w[1]);
    asm volatile("" : "+v"(t[j]));
  }
  s = t[0];
  q = t[1];
}
DEVI void red8(const f32x4& a, const f32x4& b, float& s, float& q) {
  float t[4];
#pragma unroll
  for (int j = 0; j < 4; ++j) {
    t[j] = a[j] + b[j];
    asm volatile("" : "+v"(t[j]));
  }
  red4(t, s, q);
}
DEVI void red16(const f32x4& a, const f32x4& b, const f32x4& c, const f32x4& d, float& s, float& q) {
  float t[4];
#pragma unroll
  for (int j = 0; j < 4; ++j) {
    float u = a[j] + b[j], v = c[j] + d[j];
    asm volatile("" : "+v"(u), "+v"(v));
    t[j] = u + v;
    asm volatile("" : "+v"(t[j]));
  }
  red4(t, s, q);
}

constexpr int b_k(int s) { return s == 0 ? 0 : (s < 3 ? 6 + s : (s == 3 ? 1 : (s < 6 ? 1 + s : (s == 6 ? 4 : s - 5)))); }
constexpr int b_xp(int s) { return s == 0 ? 0 : (b_k(s) >= 4 ? (b_k(s) % 5) / 2 : b_xp(s - 1)); }
template <bool B>
struct UMath {
  static constexpr int D = 4, R = 6, NV = 162, SB = K3C::SB, P = K3C::P;
  s16x8 wf[R], xf[2][2];
  s16x8 (&xc)[4][2][2];
  f32x4 (&acc)[2][9];
  f32x4 rc[4][2];
  f32x2 cs[9];
  unsigned ra, ca;
  template <int I>
  DEVI void cread() {
    if constexpr (I < 8) asm volatile("ds_read_b128 %0, %1 offset:%2" : "=v"(rc[I / 2][I % 2]) : "v"(ra), "n"((I / 2) * 512 + (I % 2) * 16));
    else if constexpr (I < 17) asm volatile("ds_read_b64 %0, %1 offset:%2" : "=v"(cs[I - 8]) : "v"(ca), "n"((I - 8) * 128));
  }
  unsigned xa, wa;
  unsigned fa, fw, fv;
  DEVI UMath(f32x4 (&a)[2][9], s16x8 (&x)[4][2][2]) : xc(x), acc(a) {}
  static constexpr int wslot(int s) { return B ? (s + 1) % 4 : s % 4; }
  static constexpr int xslot(int s) { return B ? b_k(s) % 5 : s % 5; }
  static constexpr int xpair(int s) { return B ? b_xp(s) : xslot(s) / 2; }
  static constexpr bool res(int q) { return !B || b_k(q / 2) >= 4; }
  static constexpr bool chk(int s) { return s >= 3 && s < 7; }
  static constexpr bool xop(int v) { return v + D < NV && (v + D) % 9 < 2 && res((v + D) / 9); }
  static constexpr int ops1(int v) { return (xop(v) || (B && ((v % 18 == 9 && chk(v / 18)) || (v % 18 == 0 && v > 0)))) ? 1 : 0; }
  static constexpr int newer(int V) {
    int v0 = (V % 9 == 8 ? V : V + 1) - D, n = 0;
    if (v0 >= 0 && !(V % 9 == 0 && res(V / 9))) n += ops1(v0);
    for (int v = v0 + 1; v < V; ++v)
      if (v >= 0) n += (v + D < NV ? 1 : (B ? 0 : 4)) + ops1(v);
    return n;
  }
  template <int V>
  DEVI void wread() {
    asm volatile("ds_read_b128 %0, %1 offset:%2" : "=v"(wf[V % R]) : "v"(wa), "n"((wslot(V / 18) & 1) * SB + ((V % 18) / 9) * 32 * P + (V % 9) * 32));
  }
  template <int Q, int M>
  DEVI void xread() {
    asm volatile("ds_read_b128 %0, %1 offset:%2" : "=v"(xf[Q & 1][M]) : "v"(xa), "n"((xslot(Q / 2) & 1) * SB + (Q & 1) * 8192 + M * (B ? 256 : 64)));
  }
  template <int V, int M_>
  DEVI void mf() {
    constexpr int M = ((V % 9) & 1) ? 1 - M_ : M_, q = V / 9;
    if constexpr (!B) acc[M][V % 9] = __builtin_amdgcn_mfma_f32_16x16x32_bf16(xf[q & 1][M], wf[V % R], acc[M][V % 9], 0, 0, 0);
    else if constexpr (res(q)) acc[M][V % 9] = __builtin_amdgcn_mfma_f32_16x16x32_bf16(wf[V % R], xf[q & 1][M], acc[M][V % 9], 0, 0, 0);
    else acc[M][V % 9] = __builtin_amdgcn_mfma_f32_16x16x32_bf16(wf[V % R], xc[b_k(q / 2)][q & 1][M], acc[M][V % 9], 0, 0, 0);
    __builtin_amdgcn_sched_barrier(0);
  }
  template <int V>
  DEVI void unit() {
    constexpr int U = V % 18, s = V / 18;
    static_assert(newer(V) <= 15, "lgkmcnt range");
    if constexpr ((V % 9) % 2 == 0 && V > 0) asm volatile("s_waitcnt lgkmcnt(%0)" ::"n"(newer(V)));
    __builtin_amdgcn_sched_barrier(0);
    mf<V, 0>();
    if constexpr (V + D < NV) wread<V + D>();
    if constexpr (!B && V + D >= NV) {
      cread<(V + D - NV) * 4>();
      cread<(V + D - NV) * 4 + 1>();
    }
    if constexpr (s < 8 && U == 13 && (wslot(s + 1) >> 1) != (wslot(s) >> 1))
      asm volatile("v_add_u32 %0, %1, %0" : "+v"(wa) : "n"(((wslot(s + 1) >> 1) - (wslot(s) >> 1)) * 2 * SB));
    __builtin_amdgcn_sched_barrier(0);
    mf<V, 1>();
    if constexpr (xop(V)) xread<(V + D) / 9, (V + D) % 9>();
    if constexpr (!B && V + D >= NV) {
      cread<(V + D - NV) * 4 + 2>();
      cread<(V + D - NV) * 4 + 3>();
      if constexpr (V == NV - 1) cread<16>();
    }
    if constexpr (s < 8) {
      if constexpr (U == 11 && xpair(s + 1) != xpair(s)) asm volatile("v_add_u32 %0, %1, %0" : "+v"(xa) : "n"((xpair(s + 1) - xpair(s)) * 2 * SB));
      if constexpr (!B && U == 13) __builtin_amdgcn_s_barrier();
      if constexpr (B && U == 9 && chk(s)) asm volatile("ds_read_u8 %0, %1 offset:%2" : "+v"(fv) : "v"(fa), "n"(s == 6 ? 0 : 8));
      unsigned n = 1u << 18;
      if constexpr (B && U == 13 && chk(s)) {
        asm volatile(
            "v_cmp_gt_u32 vcc, %3, %0\n\ts_cbranch_vccnz .Lw%=\n\t.subsection 1\n.Lw%=:\n\tds_read_u8 %0, %2 offset:%4\n\ts_waitcnt lgkmcnt(0)\n\t"
            "v_cmp_gt_u32 vcc, %3, %0\n\t" SAFE_OUT(".Lr%=") "s_cbranch_vccnz .Lw%=\n\ts_branch .Lr%=\n\t.subsection 0\n.Lr%=:"
            : "+v"(fv), NOP_(n)
            : "v"(fa), "n"(s == 6 ? 4 : s + 2), "n"(s == 6 ? 0 : 8)
            : "vcc", "scc");
      }
    }
    if constexpr (B && U == 15) asm volatile("v_mov_b32 %0, %1" : "+v"(fv) : "n"(s + 1));
    if constexpr (B && ((U == 0 && s > 0) || V == NV - 1)) asm volatile("ds_write_b8 %0, %1" ::"v"(fw), "v"(fv));
    __builtin_amdgcn_sched_barrier(0);
  }
  template <int... V>
  DEVI void units(std::integer_sequence<int, V...>) {
    (unit<V>(), ...);
  }
  DEVI void first() {
    if constexpr (!B) {
      xread<0, 0>();
      xread<0, 1>();
    }
    wread<0>();
    wread<1>();
    wread<2>();
    wread<3>();
    asm volatile("s_waitcnt lgkmcnt(0)");
  }
  DEVI void run() {
    units(std::make_integer_sequence<int, NV>{});
#pragma unroll
    for (int m = 0; m < 2; ++m)
#pragma unroll
      for (int n = 0; n < 9; ++n) asm volatile("" : "+v"(acc[m][n]));
  }
};

struct CMath {
  static constexpr int SB = K3C::SB;
  s16x8 wf[5], xf[2][4];
  f32x4 (&acc)[18];
  unsigned wa[2][2], xa[2][2][2];
  DEVI CMath(f32x4 (&a)[18]) : acc(a) {}
  template <int SL, int H, int U>
  DEVI void wread() {
    asm volatile("ds_read_b128 %0, %1 offset:%2" : "=v"(wf[U]) : "v"(wa[SL / 2][U / 4]), "n"((SL & 1) * SB + H * 9216 + (U & 3) * 1024));
  }
  template <int SL, int H, int M>
  DEVI void xread() {
    asm volatile("ds_read_b128 %0, %1 offset:%2" : "=v"(xf[H][M]) : "v"(xa[SL / 2][H][M / 2]), "n"((SL & 1) * SB + (M & 1) * 2048));
  }
  template <int SL, bool BAR, bool LAST, int H, int I>
  DEVI void one() {
    constexpr int u = I / 4, m = (u & 1) ? 3 - I % 4 : I % 4, NS = H ? (SL + 1) % 4 : SL;
    constexpr bool NX = !(LAST && H);
    if constexpr (I == 0) asm volatile("s_waitcnt lgkmcnt(3)");
    if constexpr (I == 2) asm volatile("s_waitcnt lgkmcnt(2)");
    if constexpr (I == 4 || I == 8) asm volatile("s_waitcnt lgkmcnt(1)");
    if constexpr (I == 12) asm volatile("s_waitcnt lgkmcnt(%0)" ::"n"(NX ? 4 : 1));
    if constexpr (I == 16) asm volatile("s_waitcnt lgkmcnt(%0)" ::"n"(NX ? 6 : 0));
    __builtin_amdgcn_sched_barrier(0);
    acc[u * 4 + m] = __builtin_amdgcn_mfma_f32_16x16x32_bf16(wf[u], xf[H][m], acc[u * 4 + m], 0, 0, 0);
    __builtin_amdgcn_sched_barrier(0);
    if constexpr (I == 0 || I == 4 || I == 8) wread<SL, H, u + 2>();
    if constexpr (BAR && H == 1 && I == 8) __builtin_amdgcn_s_barrier();
    if constexpr (NX && (I == 10 || I == 14)) wread<NS, 1 - H, (I - 10) / 4>();
    if constexpr (NX && I == 9) xread<NS, 1 - H, 0>();
    if constexpr (NX && I >= 11 && I <= 13) xread<NS, 1 - H, I - 10>();
    __builtin_amdgcn_sched_barrier(0);
  }
  template <int SL, bool BAR, bool LAST, int... I>
  DEVI void all(std::integer_sequence<int, I...>) {
    (one<SL, BAR, LAST, I / 18, I % 18>(), ...);
  }
  template <int SL, bool BAR = true, bool LAST = false>
  DEVI void stage() {
    all<SL, BAR, LAST>(std::make_integer_sequence<int, 36>{});
  }
  DEVI void setup(unsigned lds0, int wm, int lane) {
    const int g = lane >> 4, r = lane & 15;
#pragma unroll
    for (int i = 0; i < 2; ++i) {
      wa[i][0] = lds0 + (unsigned)(i * 2 * SB + K3C::XB + (wm & 1) * 5120 + lane * 16);
      wa[i][1] = lds0 + (unsigned)(i * 2 * SB + K3C::XB + 4096 + lane * 16);
#pragma unroll
      for (int h = 0; h < 2; ++h)
#pragma unroll
        for (int b = 0; b < 2; ++b)
          xa[i][h][b] = lds0 + (unsigned)(i * 2 * SB + ((wm ^ b) * 32 + r) * 128 + (((4 * h + g) ^ ((r >> 1) & 7)) * 16));
      asm volatile("" : "+v"(wa[i][0]), "+v"(wa[i][1]), "+v"(xa[i][0][0]), "+v"(xa[i][0][1]), "+v"(xa[i][1][0]), "+v"(xa[i][1][1]));
    }
  }
  DEVI void first() {
    xread<2, 0, 0>();
    xread<2, 0, 1>();
    xread<2, 0, 2>();
    xread<2, 0, 3>();
    wread<2, 0, 0>();
    wread<2, 0, 1>();
    asm volatile("s_waitcnt lgkmcnt(0)");
  }
};

constexpr int ZQ = 4;
constexpr int NR1 = 3;
constexpr int z_acc(int j) { return j < 2 ? 2 * j : (j == 2 ? 16 : 2 * (j - 1)); }
constexpr int z_nt(int k) { return k < 2 ? 5 * k : (k == 2 ? 4 : (k - 1) / 2 + 5 * ((k - 1) % 2)); }
static_assert(NR1 == 3, "rounds");
DEVI void p3_g0(char* smem, char* __restrict__ ws, int layer, int mi, int ni, int wm, int lane MK_ARG) {
  constexpr int SB = K3C::SB;
  const unsigned lds0 = (unsigned)(size_t)smem;
  const int g = lane >> 4, r = lane & 15;
  const unsigned lw = lds0 + (unsigned)(LW_OFF + layer * 4), me = 1u << (8 * wm);
  f32x4 ac[2][9];
#pragma unroll
  for (int mt = 0; mt < 2; ++mt)
#pragma unroll
    for (int j = 0; j < 9; ++j) ac[mt][j] = f32x4{0.f, 0.f, 0.f, 0.f};
  unsigned ra = lds0 + (unsigned)(K3_ST_OFF + (wm * 32 + g * 8) * 4), ca = lds0 + (unsigned)(K3_C2_OFF + r * 8);
  asm volatile("" : "+v"(ra), "+v"(ca));
  s16x8 xc[4][2][2];
  UMath<false> gm(ac, xc);
  gm.ra = ra;
  gm.ca = ca;
  gm.xa = lds0 + (unsigned)(g * 2048 + (wm * 32 + 8 * (r >> 2) + (r & 3)) * 16);
  gm.wa = lds0 + (unsigned)(K3C::XB + (g * 8 + (r & 7)) * K3C::P + (r >> 3) * 16);
  unsigned adw = lw + LW_AD * 4, mev = vconst(me), po = (unsigned)(ni * 1024 + lane * 4);
  const char* zp = uni_ptr(ws + OFF_ZP + (size_t)layer * SZ_ZP + (size_t)mi * 16384 + wm * 256);
  asm volatile("" : "+v"(gm.xa), "+v"(gm.wa), "+v"(adw), "+v"(po));
  asm volatile("" : "+s"(zp));
  place();
  bar();
  gm.first();
  gm.run();
  asm volatile("s_mov_b64 exec, 1\n\tds_add_u32 %0, %1\n\ts_mov_b64 exec, -1" ::"v"(adw), "v"(mev) : "memory");
  MK(11);
  f32x2 z[9][4];
  f32x2 sm[4], sq[4];
  {
    f32x4 (&rc)[4][2] = gm.rc;
    f32x2 (&cs)[9] = gm.cs;
    asm volatile("s_waitcnt lgkmcnt(1)"
                 : "+v"(rc[0][0]), "+v"(rc[0][1]), "+v"(rc[1][0]), "+v"(rc[1][1]), "+v"(rc[2][0]), "+v"(rc[2][1]), "+v"(rc[3][0]), "+v"(rc[3][1]));
#pragma unroll
    for (int j = 0; j < 9; ++j) asm volatile("" : "+v"(cs[j]));
    auto RC = [&](int k, int i) { return f32x2{rc[k][i / 2][(i % 2) * 2], rc[k][i / 2][(i % 2) * 2 + 1]}; };
#pragma unroll
    for (int j = 0; j < 9; ++j) {
      f32x2 u[4], w[4];
#pragma unroll
      for (int i = 0; i < 4; ++i) u[i] = __builtin_elementwise_fma(RC(0, i), cs[j], f32x2{ac[i / 2][j][(i % 2) * 2], ac[i / 2][j][(i % 2) * 2 + 1]});
#pragma unroll
      for (int i = 0; i < 4; ++i) w[i] = u[i] * u[i];
#pragma unroll
      for (int i = 0; i < 4; ++i) w[i] = __builtin_elementwise_fma(w[i], RC(3, i), RC(2, i));
#pragma unroll
      for (int i = 0; i < 4; ++i) w[i] = u[i] * w[i];
      __builtin_amdgcn_sched_barrier(0);
#pragma unroll
      for (int i = 0; i < 4; ++i) w[i] = f32x2{__builtin_amdgcn_exp2f(w[i][0]), __builtin_amdgcn_exp2f(w[i][1])};
      __builtin_amdgcn_sched_barrier(0);
#pragma unroll
      for (int i = 0; i < 4; ++i) w[i] = __builtin_elementwise_fma(w[i], RC(1, i), RC(1, i));
      __builtin_amdgcn_sched_barrier(0);
#pragma unroll
      for (int i = 0; i < 4; ++i) w[i] = f32x2{__builtin_amdgcn_rcpf(w[i][0]), __builtin_amdgcn_rcpf(w[i][1])};
      __builtin_amdgcn_sched_barrier(0);
#pragma unroll
      for (int i = 0; i < 4; ++i) z[j][i] = u[i] * w[i];
#pragma unroll
      for (int i = 0; i < 4; ++i) {
        if (j == 1) sm[i] = z[0][i] + z[1][i];
        if (j > 1) sm[i] += z[j][i];
      }
#pragma unroll
      for (int i = 0; i < 4; ++i) sq[i] = j ? __builtin_elementwise_fma(z[j][i], z[j][i], sq[i]) : z[j][i] * z[j][i];
      asm volatile("" : "+v"(z[j][0]), "+v"(z[j][1]), "+v"(z[j][2]), "+v"(z[j][3]));
      __builtin_amdgcn_sched_barrier(0);
    }
  }
  f32x4 e;
  {
    float v[4][4], d[2][4], f[4];
#pragma unroll
    for (int i = 0; i < 4; ++i) {
      v[i][0] = sm[i][0];
      v[i][1] = sq[i][0];
      v[i][2] = sm[i][1];
      v[i][3] = sq[i][1];
    }
    asm volatile("s_nop 1" : "+v"(v[3][1]), "+v"(v[3][3]), "+v"(v[2][1]), "+v"(v[2][3]));
#pragma unroll
    for (int i = 0; i < 2; ++i)
#pragma unroll
      for (int c = 0; c < 4; ++c)
        asm volatile("v_add_f32_dpp %0, %1, %1 row_ror:8 row_mask:0xf bank_mask:0x3\n\tv_add_f32_dpp %0, %2, %2 row_ror:8 row_mask:0xf bank_mask:0xc"
                     : "=&v"(d[i][c])
                     : "v"(v[i][c]), "v"(v[i + 2][c]));
#pragma unroll
    for (int c = 0; c < 4; ++c)
      asm volatile("v_add_f32_dpp %0, %1, %1 row_shl:4 row_mask:0xf bank_mask:0x5\n\tv_add_f32_dpp %0, %2, %2 row_shr:4 row_mask:0xf bank_mask:0xa"
                   : "=&v"(f[c])
                   : "v"(d[0][c]), "v"(d[1][c]));
    asm volatile(
        "v_add_f32_dpp %0, %0, %0 quad_perm:[1,0,3,2] row_mask:0xf bank_mask:0xf\n\t"
        "v_add_f32_dpp %1, %1, %1 quad_perm:[1,0,3,2] row_mask:0xf bank_mask:0xf\n\t"
        "v_add_f32_dpp %2, %2, %2 quad_perm:[1,0,3,2] row_mask:0xf bank_mask:0xf\n\t"
        "v_add_f32_dpp %3, %3, %3 quad_perm:[1,0,3,2] row_mask:0xf bank_mask:0xf\n\t"
        "v_add_f32_dpp %0, %0, %0 quad_perm:[2,3,0,1] row_mask:0xf bank_mask:0xf\n\t"
        "v_add_f32_dpp %1, %1, %1 quad_perm:[2,3,0,1] row_mask:0xf bank_mask:0xf\n\t"
        "v_add_f32_dpp %2, %2, %2 quad_perm:[2,3,0,1] row_mask:0xf bank_mask:0xf\n\t"
        "v_add_f32_dpp %3, %3, %3 quad_perm:[2,3,0,1] row_mask:0xf bank_mask:0xf"
        : "+v"(f[0]), "+v"(f[1]), "+v"(f[2]), "+v"(f[3]));
    e = f32x4{f[0], __uint_as_float(__float_as_uint(f[1]) & 0x7fffffffu), f[2], __uint_as_float(__float_as_uint(f[3]) & 0x7fffffffu)};
  }
  asm volatile("s_mov_b64 exec, %3\n\tglobal_store_dwordx4 %0, %1, %2 sc1\n\ts_mov_b64 exec, -1\n\ts_nop 0" ::"v"(po), "v"(e), "s"(zp),
               "s"(0x1111111111111111ull)
               : "memory");
  asm volatile("s_mov_b64 exec, 1\n\tds_add_u32 %0, %1\n\ts_mov_b64 exec, -1" ::"v"(adw), "v"(mev) : "memory");
  __builtin_amdgcn_s_setprio(2);
  MK(12);
  const char* dst = uni_ptr(ws + OFF_Z2 + (size_t)layer * SZ_Z + (size_t)ni * S_ * 288 + (size_t)(mi * 4 + wm) * 9216 + 4096);
  const unsigned lane16 = (unsigned)lane * 16u;
  u32x4 wb[9];
  {
    static_assert(b_k(7) == 2 && b_k(8) == 3, "order");
    const char* wp = uni_ptr(ws + OFF_PI + (size_t)ni * (18 * K3C::WB) + 11 * K3C::WB + wm * 9216 + 4096);
#pragma unroll
    for (int j = 0; j < 8; ++j) asm volatile("global_load_dwordx4 %0, %1, %2 offset:%3 sc1" : "=v"(wb[j]) : "v"(lane16), "s"(wp), "n"(j * 1024 - 4096) : "memory");
    asm volatile("global_load_dwordx4 %0, %1, %2 sc1" : "=v"(wb[8]) : "v"(lane16 + 4096u), "s"(wp) : "memory");
  }
  u32x4 y[9];
  {
    const unsigned src = (unsigned)(g * 4096 + r * 16);
    unsigned zw = lds0 + (unsigned)(K3_ZC_OFF + (wm * 32 + 2 * r + (g & 1)) * 4), zr = lds0 + (unsigned)(K3_ZC_OFF + (wm * 32 + g * 8) * 4);
    asm volatile("" : "+v"(zw), "+v"(zr));
    f32x4 a, b, c, d;
    __builtin_amdgcn_s_sleep(6);
#pragma unroll 1
    for (unsigned spin = 0; spin < SPIN_MEM; ++spin) {
      asm volatile(
          "global_load_dwordx4 %0, %4, %5 sc1\n\t"
          "global_load_dwordx4 %1, %4, %5 offset:1024 sc1\n\t"
          "global_load_dwordx4 %2, %4, %5 offset:2048 sc1\n\t"
          "global_load_dwordx4 %3, %4, %5 offset:3072 sc1\n\t"
          "s_waitcnt vmcnt(0)"
          : "=&v"(a), "=&v"(b), "=&v"(c), "=&v"(d)
          : "v"(src), "s"(zp)
          : "memory");
      const unsigned bad = (__float_as_uint(a[1]) | __float_as_uint(a[3]) | __float_as_uint(b[1]) | __float_as_uint(b[3]) |
                            __float_as_uint(c[1]) | __float_as_uint(c[3]) | __float_as_uint(d[1]) | __float_as_uint(d[3])) >> 31;
      if (!__any(bad)) break;
    }
    MK(13);
    float s, q, zmb, zi;
    red16(a, b, c, d, s, q);
    ln_finish(s, q, 1.0f / (float)FH_, zmb, zi);
    f32x4 R[2], M[2];
    asm volatile(
        "ds_write2st64_b32 %4, %5, %6 offset1:2\n\t"
        "ds_read_b128 %0, %7\n\tds_read_b128 %2, %7 offset:512\n\tds_read_b128 %1, %7 offset:16\n\tds_read_b128 %3, %7 offset:528\n\t"
        "s_waitcnt lgkmcnt(0)"
        : "=&v"(R[0]), "=&v"(R[1]), "=&v"(M[0]), "=&v"(M[1])
        : "v"(zw), "v"(zi), "v"(-zmb * zi), "v"(zr)
        : "memory");
    MK(28);
    auto st = [&](int j) {
      if (j < 8) asm volatile("global_store_dwordx4 %0, %1, %2 offset:%3 sc1\n\ts_nop 1" ::"v"(lane16), "v"(y[j]), "s"(dst), "n"(j * 1024 - 4096) : "memory");
      else asm volatile("global_store_dwordx4 %0, %1, %2 sc1\n\ts_nop 1" ::"v"(lane16 + 4096u), "v"(y[j]), "s"(dst) : "memory");
    };
#pragma unroll
    for (int j = 0; j < 9; ++j) {
      {
        float t[8];
#pragma unroll
        for (int k = 0; k < 8; ++k) {
          t[k] = __builtin_fmaf(z[j][k / 2][k % 2], R[k / 4][k % 4], M[k / 4][k % 4]);
          asm volatile("" : "+v"(t[k]));
        }
        y[j] = u32x4{pack2(t[0], t[1]), pack2(t[2], t[3]), pack2(t[4], t[5]), pack2(t[6], t[7])};
      }
      st(j);
      __builtin_amdgcn_sched_barrier(0);
    }
    MK(29);
  }
  lds_inc(lw + LW_AD * 4, me);
  MK(24);
  __builtin_amdgcn_s_setprio(2);
  lds_spin4<2>(lw + LW_S * 4, 4u + (unsigned)(wm >> 1));
  {
    const unsigned fa = lds0 + (unsigned)((wm >> 1) * SB + K3C::XB + (wm & 1) * 9216) + lane16;
#pragma unroll
    for (int j = 0; j < 9; ++j) {
      asm volatile("ds_write_b128 %0, %1 offset:%2" ::"v"(fa), "v"(wb[j]), "n"(j * 1024) : "memory");
    }
    lds_inc(lw + LW_AD * 4, me);
  }
  announce(lw + LW_Z * 4, 4u, ws, ((mi >= ZQ) ? CN_Z2 : CN_Z) + layer, lane * 16 + ni, lane < 16);
  lds_inc(lw + LW_AD * 4, me);
  MK(27);
  f32x4 acc[18];
  {
    f32x2 bs[2];
    asm volatile("ds_read2_b32 %0, %2 offset1:16\n\tds_read2_b32 %1, %3 offset1:16\n\ts_waitcnt lgkmcnt(0)"
                 : "=&v"(bs[0]), "=&v"(bs[1])
                 : "v"(lds0 + (unsigned)(K3_BS_OFF + (wm * 32 + r) * 4)), "v"(lds0 + (unsigned)(K3_BS_OFF + ((wm ^ 1) * 32 + r) * 4))
                 : "memory");
    lds_spin4<2>(lw + LW_S * 4, 6u + (unsigned)(wm >> 1));
    const unsigned fa = lds0 + (unsigned)((2 + (wm >> 1)) * SB + K3C::XB + (wm & 1) * 9216) + lane16;
#pragma unroll
    for (int j = 0; j < 9; ++j) {
      asm volatile("ds_write_b128 %0, %1 offset:%2" ::"v"(fa), "v"(y[j]), "n"(j * 1024) : "memory");
    }
    lds_inc(lw + LW_AD * 4, me);
#pragma unroll
    for (int m = 0; m < 4; ++m) {
      const float b = bs[m / 2][m % 2];
      f32x4 b4 = {b, b, b, b};
      asm volatile("" : "+v"(b4));
#pragma unroll
      for (int j = m; j < 18; j += 4) acc[j] = b4;
    }
  }
  u32x4 z1[9];
  const char* hp = uni_ptr(ws + OFF_H + (size_t)layer * SZ_Z + (size_t)((mi * 2 + (wm >> 1)) * 288 + ni * 18) * 1024);
  unsigned ho = (unsigned)((wm & 1) * 10240 + (lane >> 5) * 1024 + ((wm & 1) * 32 + (lane & 31)) * 16);
  unsigned zb = lds0 + (unsigned)(wm * 4096) + lane16, zf = lw + (unsigned)(LW_S * 4 + (wm & 2) + (lane & 1));
  asm volatile("" : "+v"(ho), "+v"(zb), "+v"(zf));
  asm volatile("" : "+s"(hp));
  {
    CMath gm(acc);
    gm.setup(lds0, wm, lane);
    unsigned za = lds0 + (unsigned)(K3_Z1_OFF + wm * 5120) + lane16;
    asm volatile("" : "+v"(za));
    MK(15);
    lds_spin4<2>(lw + LW_L * 4, 8u);
    lds_spin4(lw + LW_AD * 4, 6u);
    MK(17);
    gm.first();
    __builtin_amdgcn_s_setprio(0);
    gm.stage<2, false>();
    gm.stage<3>();
    place();
#pragma unroll 1
    for (int t = 0; t < 7; ++t) {
      gm.stage<0>();
#pragma unroll
      for (int j = 0; j < NR1; ++j) asm volatile("ds_read_b128 %0, %1 offset:%2" : "=v"(z1[j]) : "v"(za), "n"(j * 1024));
      gm.stage<1>();
#pragma unroll
      for (int j = 0; j < NR1; ++j) asm volatile("" : "+v"(z1[j]));
      gm.stage<2>();
      gm.stage<3>();
    }
    gm.stage<0>();
    gm.stage<1, false, true>();
#pragma unroll
    for (int j = 0; j < 18; ++j) asm volatile("" : "+v"(acc[j]));
  }
  MK(16);
  if (NR1 < 5) lds_inc(lw + LW_AD * 4, me);
  const unsigned hx = ho ^ 512u, hm = (ho & 2047u) + 8192u;
  unsigned zv;
  u32x4 zl[4];
#pragma unroll
  for (int j = 0; j < 9; ++j) {
    const bool sec = j >= NR1 + 3;
    if (j == NR1 || j == NR1 + 4) {
      unsigned n = 1u << 18;
      asm volatile(
          "s_waitcnt lgkmcnt(0)\n\tv_cmp_gt_u32 vcc, %8, %0\n\ts_cbranch_vccnz .Lw%=\n\t.subsection 1\n.Lw%=:\n\tds_read_u8 %0, %6\n\tds_read_b128 %2, %7 offset:%9\n\t"
          "ds_read_b128 %3, %7 offset:%9+1024\n\tds_read_b128 %4, %7 offset:%9+2048\n\tds_read_b128 %5, %7 offset:%9+3072\n\ts_waitcnt lgkmcnt(0)\n\t"
          "v_cmp_gt_u32 vcc, %8, %0\n\t" SAFE_OUT(".Lr%=") "s_cbranch_vccnz .Lw%=\n\ts_branch .Lr%=\n\t.subsection 0\n.Lr%=:"
          : "+v"(zv), NOP_(n), "+v"(zl[0]), "+v"(zl[1]), "+v"(zl[2]), "+v"(zl[3])
          : "v"(zf), "v"(zb), "n"(sec ? 11 : 10), "n"(sec ? SB : 0)
          : "memory", "vcc", "scc");
#pragma unroll
      for (int k = 0; k < 4; ++k)
        if (j + k < 9) z1[j + k] = zl[k];
    }
    if (j == NR1 - 1 || (j == NR1 + 3 && j < 8))
      asm volatile("ds_read_u8 %0, %5\n\tds_read_b128 %1, %6 offset:%7\n\tds_read_b128 %2, %6 offset:%7+1024\n\tds_read_b128 %3, %6 offset:%7+2048\n\t"
                   "ds_read_b128 %4, %6 offset:%7+3072"
                   : "=&v"(zv), "=&v"(zl[0]), "=&v"(zl[1]), "=&v"(zl[2]), "=&v"(zl[3])
                   : "v"(zf), "v"(zb), "n"(sec ? SB : 0)
                   : "memory");
    asm volatile("" : "+v"(z1[j]));
    u32x2 h[2];
#pragma unroll
    for (int mt = 0; mt < 2; ++mt) {
      const f32x4 a = acc[z_acc(j) + mt];
      h[mt] = u32x2{pack2v(f32x2{bf_lo(z1[j][2 * mt]), bf_hi(z1[j][2 * mt])} * f32x2{a[0], a[1]}),
                    pack2v(f32x2{bf_lo(z1[j][2 * mt + 1]), bf_hi(z1[j][2 * mt + 1])} * f32x2{a[2], a[3]})};
    }
    const auto s0 = __builtin_amdgcn_permlane16_swap(h[0][0], h[1][0], false, false);
    const auto s1 = __builtin_amdgcn_permlane16_swap(h[0][1], h[1][1], false, false);
    const u32x4 v = {s0[0], s1[0], s0[1], s1[1]};
    const int u = z_acc(j) / 4;
    asm volatile("global_store_dwordx4 %0, %1, %2 offset:%3 sc1\n\ts_nop 1" ::"v"(u == 4 ? hm : ((z_acc(j) & 2) ? hx : ho)), "v"(v),
                 "s"(hp + ((u & 3) / 2) * 4096), "n"((u % 2) * 2048)
                 : "memory");
    __builtin_amdgcn_sched_barrier(0);
  }
  MK(18);
}

DEVI void k3_const(char* smem, char* ws, const u16* b_sp, int layer, int bid, int mi, int ni, int wm) {
  const int lane = lane_id(), l = lane < 54 ? lane : 53, p = l / 18;
  const char* kp = uni_ptr(ws + OFF_CSK + bid * 2048 + wm * 512);
  const unsigned q16 = (unsigned)(l - p * 18) * 16u;
  unsigned bs;
  f32x4 t;
  lds_spin((unsigned)(size_t)smem + (unsigned)(LW_OFF + (LW_T + layer) * 4), 12u);
  lds_spin((unsigned)(size_t)smem + (unsigned)(LW_OFF + LW_HELLO * 4), 2u);
  asm volatile("global_load_ushort %0, %1, %2" : "=v"(bs) : "v"((unsigned)(lane & 31) * 2u), "s"(uni_ptr((const char*)(b_sp + mi * 128 + wm * 32))) : "memory");
  if (layer) {
    asm volatile("global_load_dwordx4 %0, %1, %2 sc1\n\ts_waitcnt vmcnt(0)" : "=&v"(t) : "v"(q16), "s"(kp) : "memory");
  } else {
    const char* cp = uni_ptr(ws + OFF_CSP + ni * 1152 + wm * 288);
    f32x4 a[6];
#pragma unroll
    for (int i = 0; i < 6; ++i)
      asm volatile("global_load_dwordx4 %0, %1, %2 sc1" : "=v"(a[i]) : "v"(q16 + (unsigned)(p * (6 * F_ * 4))), "s"(cp + i * (F_ * 4)) : "memory");
    asm volatile("s_waitcnt vmcnt(0)" : "+v"(a[0]), "+v"(a[1]), "+v"(a[2]), "+v"(a[3]), "+v"(a[4]), "+v"(a[5]));
    t = ((a[0] + a[1]) + (a[2] + a[3])) + (a[4] + a[5]);
#pragma unroll
    for (int j = 0; j < 4; ++j)
      t[j] += __int_as_float(__builtin_amdgcn_ds_bpermute((lane + 18) * 4, __float_as_int(t[j]))) +
              __int_as_float(__builtin_amdgcn_ds_bpermute((lane + 36) * 4, __float_as_int(t[j])));
  }
  asm volatile("" : "+v"(bs));
  const unsigned lds0 = (unsigned)(size_t)smem;
  f32x4 t0 = wm < 2 ? f32x4{t[0], t[0], t[1], t[1]} : t, t1 = wm < 2 ? f32x4{t[2], t[2], t[3], t[3]} : t;
  unsigned a0 = lds0 + (unsigned)(wm < 2 ? K3_C2_OFF + wm * 576 + lane * 32 : K3_C1_OFF + (wm - 2) * 288 + lane * 16), a1 = a0 + (wm < 2 ? 16u : 0u);
  unsigned ab = lds0 + (unsigned)(K3_BS_OFF + (wm * 32 + (lane & 31)) * 4);
  bs <<= 16;
  asm volatile("" : "+v"(t0), "+v"(t1), "+v"(a0), "+v"(a1), "+v"(ab), "+v"(bs));
  bar();
  asm volatile("ds_write_b32 %4, %5\n\ts_mov_b64 exec, 0x3ffff\n\tds_write_b128 %0, %2\n\tds_write_b128 %1, %3\n\ts_mov_b64 exec, -1" ::"v"(a0), "v"(a1),
               "v"(t0), "v"(t1), "v"(ab), "v"(bs)
               : "memory");
  if (layer == 0 && lane < 24) asm volatile("global_store_dwordx4 %0, %1, %2\n\ts_nop 1" ::"v"((unsigned)lane * 16u), "v"(t), "s"(kp) : "memory");
}

DEVI void p3_g1(char* smem, char* __restrict__ ws, int layer, int mi, int ni, int wm, int lane MK_ARG) {
  const unsigned lds0 = (unsigned)(size_t)smem;
  const int g = lane >> 4, r = lane & 15;
  const unsigned lw = lds0 + (unsigned)(LW_OFF + layer * 4);
  const unsigned xrd = lds0 + (unsigned)(g * 2048 + (wm * 32 + r) * 16);
  s16x8 xc[4][2][2];
  {
    const char* xs = uni_ptr(ws + OFF_XS + (size_t)layer * 2 * SZ_XS + (size_t)(mi * 2 + (wm >> 1)) * 4096 + (wm & 1) * 256);
    f32x4 a, b;
    auto pick = [&](int t) {
      bar();
      asm volatile(
          "ds_read_b128 %0, %4\n\tds_read_b128 %1, %4 offset:256\n\tds_read_b128 %2, %4 offset:8192\n\tds_read_b128 %3, %4 offset:8448\n\t"
          "s_waitcnt lgkmcnt(0)"
          : "=&v"(xc[t][0][0]), "=&v"(xc[t][0][1]), "=&v"(xc[t][1][0]), "=&v"(xc[t][1][1])
          : "v"(xrd + (unsigned)(t * K3C::SB))
          : "memory");
    };
    __builtin_amdgcn_s_setprio(2);
    pick(0);
    pick(1);
    asm volatile("global_load_dwordx4 %0, %2, %3 sc1\n\tglobal_load_dwordx4 %1, %2, %3 offset:512 sc1"
                 : "=&v"(a), "=&v"(b)
                 : "v"((unsigned)(g * 1024 + r * 16)), "s"(xs)
                 : "memory");
    pick(2);
    pick(3);
    bar();
    asm volatile("s_waitcnt vmcnt(0)" : "+v"(a), "+v"(b) : : "memory");
    float s, q, mb, rinv;
    red8(a, b, s, q);
    bar();
    asm volatile("" : "+v"(s), "+v"(q));
    ln_finish(s, q, 1.0f / (float)D_, mb, rinv);
    asm volatile("" : "+v"(mb), "+v"(rinv));
    bar();
    asm volatile("" : "+v"(mb), "+v"(rinv));
    const float k0 = -2.0f * 0.7978845608028654f * 1.4426950408889634f, k1 = k0 * 0.044715f;
    float sd = __builtin_amdgcn_rcpf(rinv), c0 = k0 * rinv, c1 = k1 * rinv * rinv * rinv;
    asm volatile("" : "+v"(sd));
    asm volatile("" : "+v"(c0));
    asm volatile("" : "+v"(c1));
    const float e0 = lane < 32 ? -mb : c0, e1 = lane < 32 ? sd : c1;
    asm volatile("ds_write2st64_b32 %0, %1, %2 offset1:2" ::"v"(
                     lds0 + (unsigned)(K3_ST_OFF + (g >> 1) * 1024 + (wm * 32 + 2 * r + (g & 1)) * 4)),
                 "v"(e0), "v"(e1)
                 : "memory");
    bar();
    asm volatile("s_waitcnt lgkmcnt(0)" ::: "memory");
    bar();
    __builtin_amdgcn_s_setprio(0);
  }
  f32x4 ac[2][9];
#pragma unroll
  for (int mt = 0; mt < 2; ++mt)
#pragma unroll
    for (int j = 0; j < 9; ++j) ac[mt][j] = f32x4{0.f, 0.f, 0.f, 0.f};
  {
    UMath<true> gm(ac, xc);
    gm.xa = xrd;
    gm.wa = lds0 + (unsigned)(K3C::XB + (g * 8 + (r & 7)) * K3C::P + (r >> 3) * 16);
    gm.fa = lw + (unsigned)(LW_AD * 4 + (lane & 3));
    gm.fw = lw + (unsigned)(LW_S * 4 + wm);
    gm.fv = 0u;
    asm volatile("" : "+v"(gm.fa), "+v"(gm.fw), "+v"(gm.fv), "+v"(gm.xa), "+v"(gm.wa));
    __builtin_amdgcn_s_sleep(40);
    lds_spin4<4>(lw + LW_L * 4, 4u);
    gm.first();
    place();
    lds_spin4(lw + LW_AD * 4, 2u);
    __builtin_amdgcn_s_setprio(1);
    gm.run();
    __builtin_amdgcn_s_setprio(0);
  }
  MK(14);
  f32x2 rc[4];
  f32x4 cs[9];
  const int l2 = lane_id();
  {
    const unsigned ra = lds0 + (unsigned)(K3_ST_OFF + (wm * 32 + (l2 & 15)) * 4), ca = lds0 + (unsigned)(K3_C1_OFF + (l2 & 48));
#pragma unroll
    for (int k = 0; k < 4; ++k) asm volatile("ds_read2_b32 %0, %1 offset1:16" : "=v"(rc[k]) : "v"(ra + (unsigned)(k * 512)) : "memory");
#pragma unroll
    for (int j = 0; j < 9; ++j) asm volatile("ds_read_b128 %0, %1 offset:%2" : "=v"(cs[j]) : "v"(ca), "n"(j * 64) : "memory");
    asm volatile("s_waitcnt lgkmcnt(0)" : "+v"(rc[0]), "+v"(rc[1]), "+v"(rc[2]), "+v"(rc[3]));
#pragma unroll
    for (int j = 0; j < 9; ++j) asm volatile("" : "+v"(cs[j]));
  }
  const unsigned za[3] = {lds0 + (unsigned)(K3_Z1_OFF + (wm & 2) * 5120 + (wm & 1) * 1024 + l2 * 16),
                          lds0 + (unsigned)(K3_Z1_OFF + (wm | 1) * 5120 + (~wm & 1) * 1024 + l2 * 16),
                          lds0 + (unsigned)(K3_Z1_OFF + wm * 5120 + 2048 + l2 * 16)};
  u32x4 z1[9];
  unsigned zf;
#pragma unroll
  for (int t = 0; t < 18; ++t) {
    const int j = z_nt(t / 2), mt = t % 2;
    float u[4], w[4];
#define Q4(A, B, C, D) A "\n\t" B "\n\t" C "\n\t" D "\n\t"
    asm volatile(Q4("v_fma_f32 %0, %8, %12, %16", "v_fma_f32 %1, %8, %13, %17", "v_fma_f32 %2, %8, %14, %18", "v_fma_f32 %3, %8, %15, %19")
                 Q4("v_mul_f32 %4, %0, %0", "v_mul_f32 %5, %1, %1", "v_mul_f32 %6, %2, %2", "v_mul_f32 %7, %3, %3")
                 Q4("v_fma_f32 %4, %4, %11, %10", "v_fma_f32 %5, %5, %11, %10", "v_fma_f32 %6, %6, %11, %10", "v_fma_f32 %7, %7, %11, %10")
                 Q4("v_mul_f32 %4, %4, %0", "v_mul_f32 %5, %5, %1", "v_mul_f32 %6, %6, %2", "v_mul_f32 %7, %7, %3")
                 Q4("v_exp_f32 %4, %4", "v_exp_f32 %5, %5", "v_exp_f32 %6, %6", "v_exp_f32 %7, %7")
                 Q4("v_fma_f32 %4, %4, %9, %9", "v_fma_f32 %5, %5, %9, %9", "v_fma_f32 %6, %6, %9, %9", "v_fma_f32 %7, %7, %9, %9")
                 Q4("v_rcp_f32 %4, %4", "v_rcp_f32 %5, %5", "v_rcp_f32 %6, %6", "v_rcp_f32 %7, %7")
                 Q4("v_mul_f32 %4, %4, %0", "v_mul_f32 %5, %5, %1", "v_mul_f32 %6, %6, %2", "v_mul_f32 %7, %7, %3")
                 : "=&v"(u[0]), "=&v"(u[1]), "=&v"(u[2]), "=&v"(u[3]), "=&v"(w[0]), "=&v"(w[1]), "=&v"(w[2]), "=&v"(w[3])
                 : "v"(rc[0][mt]), "v"(rc[1][mt]), "v"(rc[2][mt]), "v"(rc[3][mt]), "v"(cs[j][0]), "v"(cs[j][1]), "v"(cs[j][2]), "v"(cs[j][3]),
                   "v"(ac[mt][j][0]), "v"(ac[mt][j][1]), "v"(ac[mt][j][2]), "v"(ac[mt][j][3]));
    z1[j][2 * mt] = pack2(w[0], w[1]);
    z1[j][2 * mt + 1] = pack2(w[2], w[3]);
    if (mt && t / 2 < NR1) asm volatile("ds_write_b128 %0, %1" ::"v"(za[t / 2]), "v"(z1[j]) : "memory");
    if (t == 9) asm volatile("s_waitcnt lgkmcnt(0)" ::: "memory");
    __builtin_amdgcn_sched_barrier(0);
    if (t == 2 * ZQ - 7)
      asm volatile("global_load_dword %0, %1, %2 sc1"
                   : "=v"(zf)
                   : "v"(vconst(0u)), "s"(uni_ptr(ws + OFF_CNT + (size_t)((CN_Z2 + layer) * 256 + mi * 16 + ni) * 128))
                   : "memory");
    if (t == 2 * ZQ - 4) {
      asm volatile("s_waitcnt vmcnt(0)" : "+v"(zf) : : "memory");
      if (wm == 1 && __builtin_amdgcn_readfirstlane(zf) != 15u - ZQ) cnt_wait(ws, CN_Z2 + layer, mi * 16 + ni, 16u - ZQ);
    }
    bar();
    __builtin_amdgcn_sched_barrier(0);
  }
  MK(15);
  unsigned zb[2] = {lds0 + (unsigned)((wm & 2) * 4096 + (wm & 1) * 1024 + l2 * 16), lds0 + (unsigned)((wm | 1) * 4096 + (~wm & 1) * 1024 + l2 * 16)};
  asm volatile("" : "+v"(zb[0]), "+v"(zb[1]));
#pragma unroll
  for (int k = NR1; k < 9; ++k) asm volatile("" : "+v"(z1[z_nt(k)]));
#pragma unroll 1
  for (int t = 20; t < 32; ++t) bar();
  __builtin_amdgcn_s_sleep(9);
#pragma unroll
  for (int k = NR1; k < 9; ++k) {
    if (k == NR1 + 4) lds_spin4(lw + LW_AD * 4, 7u);
    asm volatile("ds_write_b128 %0, %1 offset:%2" ::"v"(zb[(k - NR1) % 2]), "v"(z1[z_nt(k)]),
                 "n"(((k - NR1) / 4) * K3C::SB + (((k - NR1) % 4) / 2) * 2048)
                 : "memory");
    if (k == NR1 + 3 || k == 8) asm volatile("ds_write_b8 %0, %1" ::"v"(lw + (unsigned)(LW_S * 4 + wm)), "v"(vconst(k == NR1 + 3 ? 10u : 11u)) : "memory");
  }
}

constexpr int W5A = 22;
constexpr int NPF = 30;
struct Loader {
  u32x4 pf[NPF];
  unsigned lds0, lane16;
  int wl, lane;

  template <int R0, int N, int P0, int PE>
  DEVI void pf_load(const char* src) {
    const unsigned o = lane16 + (unsigned)(wl * 1024);
#pragma unroll
    for (int t = 0; t < N; ++t) {
      unsigned a = o + (unsigned)((P0 + 4 * t) * 1024);
      if (P0 + 4 * t + 3 >= PE && P0 + 4 * t + wl >= PE) a = lane16;
      asm volatile("global_load_dwordx4 %0, %1, %2 sc1" : "=v"(pf[R0 + t]) : "v"(a), "s"(src) : "memory");
    }
  }
  template <int R0, int N>
  DEVI void pf_pin() {
#pragma unroll
    for (int t = 0; t < N; ++t) asm volatile("" : "+v"(pf[R0 + t]));
  }
  template <int R0, int N>
  DEVI void pf_undef() {
#pragma unroll
    for (int t = 0; t < N; ++t) asm volatile("" : "=v"(pf[R0 + t]));
  }
  template <int PB, int A, int N>
  DEVI void pf_put(unsigned dst) {
    constexpr int T0 = (A - PB) / 4, T1 = (A + N - 1 - PB) / 4;
    const unsigned a = lds0 + dst + lane16 + (unsigned)((wl + PB + 4 * T0 - A + 4) * 1024) - 4096u;
#pragma unroll
    for (int t = T0; t <= T1; ++t) {
      const int p0 = PB + 4 * t;
      if ((p0 >= A || p0 + wl >= A) && (p0 + 3 < A + N || p0 + wl < A + N))
        asm volatile("ds_write_b128 %0, %1 offset:%2" ::"v"(a), "v"(pf[t]), "n"((t - T0) * 4096) : "memory");
    }
  }
  template <int N>
  DEVI void dma_run(const char* src, unsigned dst) {
    constexpr int B = N / 4, R = N % 4;
    const unsigned f = (unsigned)(wl * B + (wl < R ? wl : R)) * 1024u, o = lane16 + f, m = lds0 + dst + f;
    dma_seq<B>(src, o, m);
    if (wl < R) dma16_sc1(src + B * 1024, o, m + (unsigned)(B * 1024));
  }
  template <int V>
  DEVI void wait_w() {
    asm volatile("s_cmp_lt_u32 %0, 2\n\ts_cbranch_scc1 1f\n\ts_waitcnt vmcnt(%1)\n1:\n\ts_waitcnt vmcnt(%2)" ::"s"(wl), "n"(V - 1), "n"(V) : "memory", "scc");
  }
  template <int V>
  DEVI void wbar() {
    asm volatile("s_waitcnt vmcnt(%0) lgkmcnt(0)" ::"n"(V) : "memory");
    bar();
  }

  DEVI void p1(char* smem, char* ws, int bid, int rt, int hp MK_ARG) {
    constexpr int SB = K1C::SB;
    static_assert(K1C::XB == 18 * 1024 && K1C::WB == 14 * 1024, "K1 shape");
    const char* wp = uni_ptr(ws + OFF_PQ + (size_t)hp * (9 * K1C::WB) + 4 * K1C::WB);
    static_assert(3 * SB + K1C::XB >= P5_W + 5 * 18432, "free while the GEMM before ends");
    const char* wq = wp - 4 * K1C::WB;
    dma_run<14>(wq + 3 * K1C::WB, (unsigned)(3 * SB + K1C::XB));
    dma_run<28>(wp, (unsigned)K1_WX);
    if (wl == 3) dma16_sc1(uni_ptr(ws + OFF_CSQ + bid * 512), lane16 & 511u, lds0 + (unsigned)K1_CS_OFF);
    __builtin_amdgcn_s_setprio(0);
    wait_vm<0>();
    const bool lo = wl < 2;
    const int x0 = lo ? wl * 5 : wl * 4 + 2, w0 = lo ? wl * 3 : wl * 4 - 2;
    unsigned xo[5];
#pragma unroll
    for (int i = 0; i < 5; ++i) {
      const int c = (x0 + i) * 64 + lane;
      const int ch = c / 144;
      int gr = rt * 128 - 16 + (c - ch * 144);
      if (gr < 0) gr = 0;
      xo[i] = (unsigned)(((gr >> 6) * 72 + ch + 3 - (i & 3)) * 1024 + (gr & 63) * 16);
      asm volatile("" : "+v"(xo[i]));
    }
    const char* xp = uni_ptr(ws + OFF_X1 + SZ_X - 3072);
    wp += 2 * K1C::WB + w0 * 1024;
    auto dma_x = [&](unsigned so) {
      const unsigned m = lds0 + so + (unsigned)(x0 * 1024);
      dma4<4, true>(xp, xo[0], xo[1], xo[2], xo[3], m);
      if (lo) dma16_sc1(xp, xo[4], m + 4096u);
      xp += 8192;
    };
    auto dma_w = [&](unsigned so) {
      const unsigned m = lds0 + so + (unsigned)(K1C::XB + w0 * 1024);
      dma_seq<3>(wp, lane16, m);
      if (!lo) dma16_sc1(wp + 3072, lane16, m + 3072u);
      wp += K1C::WB;
    };
    const char* xs = uni_ptr(ws + OFF_XS + SZ_XS);
    const int g = lane >> 4, p2 = 64 + (lane & 7);
    int ra = rt * 128 - 16 + wl * 32 + 2 * (lane & 15);
    if (ra < 0) ra = 0;
    unsigned sa = (unsigned)(((ra >> 6) * 8 + g * 2) * 512 + (ra & 63) * 8);
    unsigned sb = (unsigned)(((rt * 2 + 1) * 8 + (lane >> 3)) * 512 + (2 * p2 - 80) * 8);
    unsigned sw = lds0 + (unsigned)(K1_ST_OFF + (2 * (lane < 32 ? wl * 16 + (lane & 15) : p2) + (g & 1)) * 8);
    asm volatile("" : "+v"(sa), "+v"(sb), "+v"(sw));
    MK(22);
    __builtin_amdgcn_s_setprio(3);
    bar();
    dma_run<14>(wq, (unsigned)K1C::XB);
    dma_run<14>(wq + K1C::WB, (unsigned)(SB + K1C::XB));
    dma_run<14>(wq + 2 * K1C::WB, (unsigned)(2 * SB + K1C::XB));
    MK(1);
    bar();
    dma_x(0u);
    dma_x((unsigned)SB);
    wbar<4>();
    MK(29);
    dma_x((unsigned)(2 * SB));
    dma_x((unsigned)(3 * SB));
    wbar<8>();
    MX(1, 3);
    dma_x(0u);
    wbar<8>();
    MX(1, 4);
    dma_x((unsigned)SB);
    f32x4 a, b, e;
    asm volatile("global_load_dwordx4 %0, %3, %5 sc1\n\tglobal_load_dwordx4 %1, %3, %5 offset:512 sc1\n\tglobal_load_dwordx4 %2, %4, %5 sc1"
                 : "=&v"(a), "=&v"(b), "=&v"(e)
                 : "v"(sa), "v"(sb), "s"(xs)
                 : "memory");
    wbar<11>();
    MX(1, 5);
    dma_w((unsigned)SB);
    dma_x((unsigned)(2 * SB));
    wbar<15>();
    MX(1, 6);
    dma_w((unsigned)(2 * SB));
    dma_x((unsigned)(3 * SB));
    wbar<19>();
    MX(1, 7);
    dma_w((unsigned)(3 * SB));
    dma_x(0u);
    wbar<16>();
    MX(1, 8);
    asm volatile("" : "+v"(a), "+v"(b), "+v"(e));
    float s, q;
    {
      float t[4] = {e[0], e[1], e[2], e[3]}, s2, q2;
      asm volatile(
          "v_add_f32_dpp %0, %0, %0 row_ror:8 row_mask:0xf bank_mask:0xf\n\tv_add_f32_dpp %1, %1, %1 row_ror:8 row_mask:0xf bank_mask:0xf\n\t"
          "v_add_f32_dpp %2, %2, %2 row_ror:8 row_mask:0xf bank_mask:0xf\n\tv_add_f32_dpp %3, %3, %3 row_ror:8 row_mask:0xf bank_mask:0xf\n\ts_nop 1"
          : "+v"(t[0]), "+v"(t[1]), "+v"(t[2]), "+v"(t[3]));
      red4(t, s2, q2);
      red8(a, b, s, q);
      s = lane < 32 ? s : s2;
      q = lane < 32 ? q : q2;
      asm volatile("" : "+v"(s), "+v"(q));
    }
    wbar<8>();
    MX(1, 9);
    {
      asm volatile("" : "+v"(s), "+v"(q));
      float mb, rinv;
      ln_finish(s, q, 1.0f / (float)D_, mb, rinv);
      asm volatile("ds_write_b64 %0, %1" ::"v"(sw), "v"(f32x2{-mb, rinv}) : "memory");
    }
    wbar<0>();
    MK(30);
  }

  template <int I>
  DEVI void p5_step(const char* wp) {
    constexpr int T = 2 * I + 1;
    wbar<(T == 1 ? 63 : (T <= 11 ? 3 : 0))>();
    if constexpr (T <= 9) {
      const unsigned wo = (unsigned)(12288 + (wl & 1) * 3072), so = (unsigned)(P5_W + ((T + 2 + (wl >> 1)) & 7) * 18432);
      dma_seq<3>(wp + (T + 7) * 18432, lane16 + wo + (unsigned)((wl >> 1) * 18432), lds0 + so + wo);
      pf_put<0, 12 * (T - 1), 12>((unsigned)(P5_W + ((T + 2) % 8) * 18432));
      pf_put<0, 12 * T, 12>((unsigned)(P5_W + ((T + 3) % 8) * 18432));
    }
  }
  template <int... S>
  DEVI void p5_steps(const char* wp, std::integer_sequence<int, S...>) {
    (p5_step<S>(wp), ...);
  }

  template <int T0, int T1>
  DEVI void w5_load(const char* w5p) {
    const unsigned o = lane16 + (unsigned)(wl * 1024);
#pragma unroll
    for (int t = T0; t < T1; ++t)
      asm volatile("global_load_dwordx4 %0, %1, %2 sc1" : "=v"(pf[t]) : "v"(o + (unsigned)((t / 3 + 8) * 18432 + (t % 3) * 4096)), "s"(w5p) : "memory");
  }
  DEVI void p35(char* smem, char* ws, const u16* w_sp, int layer, int bid, int mi, int ni, int rb, int ct, int hp MK_ARG) {
    using C = K3C;
    constexpr int NS = C::NS, SB = C::SB, NDX = 4, NDW = 5, VPS = NDX + NDW;
    const char* wip = uni_ptr(ws + OFF_PI + (size_t)ni * (18 * C::WB));
    if (layer) {
      pf_put<-48, 36, 18>((unsigned)(2 * SB + C::XB));
      pf_put<-48, 54, 18>((unsigned)(3 * SB + C::XB));
    } else {
      dma_run<18>(wip + 2 * C::WB, (unsigned)(2 * SB + C::XB));
      dma_run<18>(wip + 3 * C::WB, (unsigned)(3 * SB + C::XB));
    }
    dma_run<18>(wip + C::WB, (unsigned)(SB + C::XB));
    const char* w5p = uni_ptr(ws + OFF_PW + (size_t)ct * (36 * 9216));
    const int w0 = wl < 2 ? wl * 5120 : wl * 4096 + 2048;
    const unsigned m0x = lds0 + (unsigned)(wl * 4096), m0w = lds0 + (unsigned)(C::XB + w0);
    const unsigned wo = lane16 + (unsigned)w0;
    const unsigned lw = lds0 + (unsigned)(LW_OFF + layer * 4), me = 1u << (8 * wl);
    const unsigned wo4 = wo + 4096u;
    auto dma_w = [&](const char* wp, unsigned so) {
      dma_seq<4>(wp, wo, m0w + so);
      if (wl < 2) dma16_sc1(wp, wo4, m0w + so + 4096u);
    };
    {
      const char* xp = uni_ptr(ws + OFF_X1 + (size_t)layer * 2 * SZ_X + (size_t)(mi * 2 * 72 + wl * 2) * 1024 - 1024);
      const unsigned xo0 = lane16 + 1024u, xo1 = lane16 + 72u * 1024u, xo3 = lane16 + 71u * 1024u;
      auto dma_x = [&](int k, unsigned so) { dma4<4, true>(xp + k * 8192, xo0, xo1, lane16, xo3, m0x + so); };
      MK(10);
      bar();
      dma_x(0, 0u);
      dma_x(1, (unsigned)SB);
      wait_vm<4>(); MX(3, 2); wbar<4>(); MX(2, 2);
      MK(25);
      {
        dma_x(2, (unsigned)(2 * SB));
        dma_x(3, (unsigned)(3 * SB));
        dma_x(4, (unsigned)(4 * SB));
        wbar<12>(); MX(2, 3);
        dma_x(5, 0u);
        wait_vm<12>();
      }
      bar(); MX(2, 4);
      dma_w(wip + 4 * C::WB, 0u);
      dma_x(6, (unsigned)SB);
      wait_w<8 + VPS>();
#pragma unroll 1
      for (int t = 1; t < 3; ++t) {
        bar(); MX(2, 4 + t);
        dma_w(wip + (t + 4) * C::WB, (unsigned)(t * SB));
        dma_x(t + 6, (unsigned)((t + 1) * SB));
        wait_w<NDX + VPS>();
      }
      bar(); MX(2, 7);
      dma_w(wip + 7 * C::WB, (unsigned)(3 * SB));
      wait_w<VPS>();
      bar(); MX(2, 8);
      dma_w(wip + 8 * C::WB, 0u);
      wait_w<NDW>();
      bar(); MX(2, 9);
      const char* wp = wip + 9 * C::WB;
      dma_w(wp + b_k(0) * C::WB, (unsigned)SB);
      if (layer == 0) {
        const char* sp = uni_ptr((const char*)w_sp + (size_t)(bid * 4 + wl) * 8192);
        const unsigned so = (unsigned)((lane >> 5) * 4096 + (lane & 31) * 16);
#pragma unroll
        for (int i = 0; i < WSP_V; ++i) dma16(sp + i * 512, so, lds0 + (unsigned)K3_DM_OFF);
        dma16(sp + (bid >> 4) * 256, so & ~0x100u, lds0 + (unsigned)K3_DM_OFF);
      }
      w5_load<0, 4>(w5p);
      wait_w<NDW + 4>();
      MX(3, 10); bar(); MX(2, 10);
      dma_w(wp + b_k(1) * C::WB, (unsigned)(2 * SB));
      w5_load<4, 8>(w5p);
      MK(26);
      lds_spin4(lw + LW_AD * 4, 1u);
      dma_w(wp + b_k(2) * C::WB, (unsigned)(3 * SB));
      dma_w(wp + b_k(3) * C::WB, 0u);
      w5_load<8, W5A>(w5p);
      wait_vm<W5A - 8>();
      lds_inc(lw + LW_L * 4, 4u * me);
      w5_load<W5A, 30>(w5p);
      MK(11);
    }
    {
      unsigned xoff[NDX];
#pragma unroll
      for (int i = 0; i < NDX; ++i) {
        const int c = wl * 256 + i * 64 + lane;
        const int row = c >> 3, cc = c & 7;
        xoff[i] = (unsigned)((mi * 128 + row) * S_ * 2 - i * 1024) + (unsigned)((cc ^ ((row >> 1) & 7)) * 16);
      }
      auto dma_x = [&](const char* xp, unsigned so) { dma4<4, false>(xp, xoff[0], xoff[1], xoff[2], xoff[3], m0x + so); };
      const char* xp = uni_ptr((const char*)w_sp);
      const char* wp = uni_ptr(ws + OFF_Z2 + (size_t)layer * SZ_Z + (size_t)ni * S_ * 288);
      static_for<4, 7>([&](auto T_) {
        constexpr int t = T_;
        lds_spin4<2>(lw + LW_S * 4, (unsigned)(t - 3));
        dma_w(wip + (9 + b_k(t)) * C::WB, (unsigned)(((t + 1) % 4) * SB));
        if constexpr (t > 4) {
          dma_x(xp + mi * 256 + (t - 5) * 128, (unsigned)((t - 3) * SB));
          wait_w<NDW + (t - 4) * NDX>();
          lds_inc(lw + LW_L * 4, me);
        }
      });
      wait_vm<NDX>();
      lds_inc(lw + LW_L * 4, me);
      wait_vm<0>();
      lds_inc(lw + LW_L * 4, me);
      pf_pin<0, 30>();
      MK(13);
      lds_spin4<2>(lw + LW_AD * 4, 5u);
      MK(24);
      int left = 2 * mi;
      auto next = [&]() {
        const int d = --left == 0 ? 3 : 1;
        xp += d * 128;
        wp += d * C::WB;
      };
      if (mi == 0) {
        xp += 256;
        wp += 2 * C::WB;
      }
      if (wl == 0) {
        cnt_wait(ws, CN_Z + layer, mi * 16 + ni, (unsigned)ZQ);
        lds_wr32(lw + LW_GO * 4, 1u);
      }
      lds_spin4<2>(lw + LW_AD * 4, 6u);
      dma_x(xp, 0u);
      dma_x(xp + 128, (unsigned)SB);
      spin_ge<2>(lw + LW_GO * 4, 1u);
      lds_spin4<2>(lw + LW_S * 4, 8u);
      dma_w(wp, 0u);
      lds_spin4<2>(lw + LW_S * 4, 9u);
      dma_w(wp + C::WB, (unsigned)SB);
      next();
      next();
      unsigned slot = (unsigned)(2 * SB);
      auto cissue = [&]() {
        dma_x(xp, slot);
        dma_w(wp, slot);
        next();
        slot = (slot == (unsigned)((NS - 1) * SB)) ? 0u : slot + (unsigned)SB;
      };
      auto cstep = [&]() {
        bar();
        cissue();
      };
      wait_w<NDW>();
      MK(15);
      int j = 2;
      if (layer == 0) {
        const char* sp = uni_ptr((const char*)w_sp + (size_t)(bid * 4 + wl) * 8192 + WSP_V * 512);
        unsigned d = 0u;
#pragma unroll 1
        for (; j < 2 + 32 - 4 * WSP_V; ++j) {
          cstep();
          asm volatile("s_load_dword %0, %1, 0\n\ts_load_dword %0, %1, 64\n\ts_load_dword %0, %1, 0x1000\n\ts_load_dword %0, %1, 0x1040"
                       : "+s"(d)
                       : "s"(sp));
          sp += 128;
          wait_w<VPS>();
        }
#pragma unroll 1
        for (int e = j + 4; j < e; ++j) {
          cstep();
          asm volatile("" : "+s"(d));
          wait_w<VPS>();
        }
        asm volatile("s_waitcnt lgkmcnt(0)" : "+s"(d));
      }
      place();
#pragma unroll 1
      for (; j <= 29; ++j) {
        cstep();
        wait_w<VPS>();
      }
      bar();
    }
    dma_run<33>(w5p, (unsigned)(P5_W + 54 * 1024));
    dma_run<23>(w5p + 67 * 1024, (unsigned)(P5_W + 121 * 1024));
    if (wl == 3) dma16_sc1(w5p + 143 * 1024, lane16, lds0 + (unsigned)(P5_W + 53 * 1024));
    wait_vm<14>();
    bar();
    dma_run<34>(w5p + 33 * 1024, (unsigned)(P5_W + 87 * 1024));
    wait_vm<0>();
    bar();
    if (wl) {
      const unsigned f = (unsigned)(wl - 1) * 18432u;
      dma_seq<17>(w5p + 90 * 1024, lane16 + f, lds0 + (unsigned)P5_W + f);
      if (wl < 3) dma16_sc1(w5p + 107 * 1024, lane16 + f, lds0 + (unsigned)(P5_W + 17 * 1024) + f);
    } else {
      cnt_wait(ws, CN_H + layer, rb * 8 + ct, 8u);
    }
    MK(20);
    bar();
    p5_steps(w5p, std::make_integer_sequence<int, 7>{});
    if (layer == 0) {
      if (bid < 24 && wl == 0 && lane < 8) pk_st16(ws + OFF_HELLO + bid * 128 + lane16, u32x4{0u, 0u, 0u, 0u});
    }
  }
};

__global__ __launch_bounds__(768, 3) void k_main(const u16* __restrict__ x0, const u16* __restrict__ wqkv, const u16* __restrict__ wo,
                                                 const u16* __restrict__ w_in, const u16* __restrict__ w_sp,
                                                 const u16* __restrict__ b_sp, const u16* __restrict__ w_out,
                                                 u16* __restrict__ out, char* ws) {
  extern __shared__ __attribute__((aligned(16))) char smem[];
  const char* code0 = code_here();
  (void)code0;
  const unsigned lds0 = (unsigned)(size_t)smem;
  int wave = __builtin_amdgcn_readfirstlane((int)threadIdx.x >> 6);
  int bid = blockIdx.x;
#define FRESH() asm volatile("" : "+s"(bid), "+s"(ws), "+s"(wave))
#define XCD (bid & 7)
#define IDX (bid >> 3)
#define MI ((XCD >> 2) * 8 + (IDX >> 2))
#define NI ((XCD & 1) * 8 + (XCD & 2) * 2 + (IDX & 3))
#define RB (XCD * 4 + (IDX >> 3))
#define CT (IDX & 7)
#define RT ((XCD >> 1) * 4 + (IDX >> 3))
#define HP ((XCD & 1) * 8 + (IDX & 7))
  MK(0);
  if (wave < 8) {
    constexpr int PER = (2 * NL_MAIN + 31) / 32;
    static_assert(PER <= 64 && 31 * PER < 2 * NL_MAIN, "one load");
    unsigned o, last, st, t;
    const char* p;
    asm volatile(
        "s_lshr_b32 %3, %5, 3\n\t"
        "s_cmp_eq_u32 %3, 31\n\ts_cselect_b32 %1, 63, 0\n\ts_cselect_b32 %2, %7, %6\n\t"
        "s_mul_i32 %0, %3, %8\n\t"
        "s_cmp_eq_u32 %4, 2\n\ts_cselect_b32 %0, %0, 0\n\ts_cselect_b32 %1, %2, %1\n\t"
        "s_mul_i32 %3, %3, 0xf300\n\ts_add_u32 %3, %3, 0x200\n\t"
        "s_cmp_eq_u32 %4, 3\n\ts_cselect_b32 %0, %3, %0\n\ts_cselect_b32 %1, 53, %1\n\ts_cselect_b32 %2, 0x480, 64\n\t"
        "s_mul_i32 %3, %5, 0x1e60\n\t"
        "s_cmp_eq_u32 %4, 5\n\ts_cselect_b32 %0, %3, %0\n\t"
        "s_add_u32 %3, %3, 0x1000\n\t"
        "s_cmp_eq_u32 %4, 4\n\ts_cselect_b32 %0, %3, %0\n\ts_cselect_b32 %1, 57, %1\n\t"
        "s_cmp_ge_u32 %4, 5\n\ts_cselect_b32 %1, 63, %1\n\t"
        "s_mul_i32 %3, %5, 0x2400\n\t"
        "s_cmp_eq_u32 %4, 7\n\ts_cselect_b32 %0, %3, %0\n\t"
        "s_add_u32 %3, %3, 0x1000\n\t"
        "s_cmp_eq_u32 %4, 6\n\ts_cselect_b32 %0, %3, %0\n\t"
        "s_add_u32 %3, %3, 0x1000\n\t"
        "s_cmp_eq_u32 %4, 1\n\ts_cselect_b32 %0, %3, %0\n\ts_cselect_b32 %1, 15, %1"
        : "=&s"(o), "=&s"(last), "=&s"(st), "=&s"(t)
        : "s"(wave), "s"(bid), "n"(PER - 1), "n"(2 * NL_MAIN - 1 - 31 * PER), "n"(PER * 64)
        : "scc");
    const unsigned l = (unsigned)lane_id();
    unsigned a = o + (l < last ? l : last) * st;
    asm volatile("" : "+v"(a));
    asm volatile("s_cmp_lg_u32 %1, 2\n\ts_cbranch_scc1 1f\n\ts_mov_b32 m0, %3\n\ts_nop 0\n\tglobal_load_lds_dword %0, %2\n\tv_mov_b32 %0, 0\n1:"
                          : "+v"(a)
                          : "s"(wave), "s"(code0), "s"(lds0 + (unsigned)K1R_DUMMY)
                          : "memory", "scc");
    asm volatile("s_cmp_eq_u32 %1, 2\n\ts_cselect_b64 %0, %4, %5\n\ts_bitcmp1_b32 0x38, %1\n\ts_cselect_b64 %0, %3, %0\n\t"
                 "s_bitcmp1_b32 0xc2, %1\n\ts_cselect_b64 %0, %2, %0"
                 : "=&s"(p)
                 : "s"(wave), "s"(x0), "s"(wqkv), "s"(wo), "s"(b_sp)
                 : "scc");
    touch64(p, a, lds0 + (unsigned)K1R_DUMMY);
    MF(12);
    math_start(lds0, ws, bid, wave, (int)l);
    MF(13);
  }

  if (__builtin_expect(wave >= 8, 1)) {
    __builtin_amdgcn_s_setprio(3);
    {
      const int lane = lane_id();
      RawLd rl;
      int job, pj;
      const char* pg;
      rl.front(x0, wqkv, wo, ws, bid, lds0, wave - 8, lane, RT, HP, pj, [&]() {
        int b = bid;
        asm volatile("" : "+s"(b));
        const int xcd = b & 7, c16 = b >> 4, hp = (b & 1) * 8 + ((b >> 3) & 7);
        const bool cross = (((hp * 72) & ~15) & 127) > 48;
        const int cj = (int)((0x65743210u >> (4 * xcd)) & 7u) * 16 + c16;
        job = cross ? (cj < 88 ? 128 + cj : 999) : xcd * 16 + c16;
        pj = cross ? cj - 88 : ((xcd & 5) == 0 && (c16 & 3) == (c16 >> 2) ? 40 + xcd * 2 + (c16 >> 2) : -1);
        const int k = b >> 3;
        pg = k < 11 ? (const char*)w_in + k * 0x80000 : k < 17 ? (const char*)w_out + (k - 11) * 0x80000 : k < 22 ? (const char*)out + (k - 17) * 0x80000
             : (const char*)w_sp + (k < 30 ? (k - 22) * 0x100000 : 0x7ffff0);
      }, pg MF_PASS);
      if (job < 216) k1_raw_loader<true>(rl, w_in, w_out, ws, rl.tag, bid, lds0, wave - 8, lane, RT, HP, CT, job, pj MF_PASS);
      else k1_raw_loader<false>(rl, w_in, w_out, ws, rl.tag, bid, lds0, wave - 8, lane, RT, HP, CT, job, pj MF_PASS);
    }
    Loader ld;
    ld.lds0 = lds0;
#pragma unroll 1
    for (int layer = 0; layer < 2; ++layer) {
      FRESH();
      ld.wl = wave - 8;
      ld.lane = lane_id();
      ld.lane16 = (unsigned)ld.lane * 16u;
      const char* wip = uni_ptr(ws + OFF_PI + (size_t)NI * (18 * K3C::WB));
      if (layer) {
        ld.p1(smem, ws, bid, RT, HP MK_PASS);
        ld.pf_load<0, 21, 0, 81>(uni_ptr(ws + OFF_PO + (size_t)CT * (18 * 4608)));
        ld.pf_load<21, 9, 36, 72>(wip);
        wait_vm<9>();
        ld.pf_pin<0, 21>();
        lds_spin(lds0 + (unsigned)(LW_OFF + (LW_T + layer) * 4), 8u);
        __builtin_amdgcn_s_sleep(4);
        ld.pf_put<0, 0, 40>((unsigned)P2_W);
        wait_vm<0>();
        ld.pf_pin<21, 9>();
        bar();
        ld.pf_put<0, 40, 41>((unsigned)(P2_W + 40 * 1024));
      } else {
        ld.pf_undef<21, 9>();
        MK(3);
        MK(4);
        bar();
        ld.dma_run<81>(uni_ptr(ws + OFF_PO + (size_t)CT * (18 * 4608)), (unsigned)P2_W);
      }
      if (layer == 0 && ld.wl == 0) cnt_wait(ws, CN_O + layer, RB * 8 + CT, 16u);
      bool pk = false;
      if (layer == 0 && ld.wl == 1) {
        unsigned f;
        asm volatile("global_load_dword %0, %1, %2 sc1\n\ts_waitcnt vmcnt(0)"
                     : "=&v"(f)
                     : "v"(vconst(0u)), "s"(uni_ptr(ws + OFF_CNT + (size_t)(CN_PK * 256 + 8 + NI) * 128))
                     : "memory");
        pk = __builtin_amdgcn_readfirstlane(f) == 35u;
        if (pk) lds_wr32(lds0 + (unsigned)(LW_OFF + LW_HELLO * 4), 2u);
      }
      MK(6);
      asm volatile("s_waitcnt vmcnt(0) lgkmcnt(0)" ::: "memory");
      bar();
      lds_spin(lds0 + (unsigned)(LW_OFF + (LW_T + layer) * 4), 12u);
      if (layer == 0) {
        if (ld.wl == 1) {
          if (!pk) cnt_wait(ws, CN_PK, 8 + NI, 36u);
          lds_wr32(lds0 + (unsigned)(LW_OFF + LW_HELLO * 4), 2u);
        } else {
          lds_spin(lds0 + (unsigned)(LW_OFF + LW_HELLO * 4), 2u);
        }
        MK(7);
      }
      static_assert(2 * K3C::SB <= P2_W, "free during the first residual GEMM");
      ld.dma_run<18>(wip, (unsigned)K3C::XB);
      MX(6, 24);
      if (layer == 0 && ld.wl == 0) cnt_wait(ws, CN_PK, 24 + CT, 72u);
      wait_vm<0>();
      ld.pf_pin<21, 9>();
      MX(6, 25);
      bar();
      FRESH();
      ld.wl = wave - 8;
      ld.p35(smem, ws, w_sp, layer, bid, MI, NI, RB, CT, HP MK_PASS);
    }
  } else {
#pragma unroll 1
    for (int layer = 0; layer < 2; ++layer) {
      FRESH();
      {
        const int lane = lane_id(), r0 = RT * 128;
        float* csq = reinterpret_cast<float*>(ws + OFF_CSQ + bid * 512);
        Attn at;
        at.setup(lds0 + (unsigned)(layer ? K1_T_OFF : K1R_T_OFF), ws + OFF_O + (size_t)layer * SZ_X, r0, HP, wave, lane);
        if (layer) {
          if ((wave & 2) == 0) k1_gemm<2, 4>(smem, ws, RT, HP, (wave & 1) * 2 + (wave & 4), 0, wave >= 4, lane MK_PASS);
          else if (wave < 4) k1_gemm<3, 3>(smem, ws, RT, HP, (wave & 1) * 3, 4, false, lane MK_PASS);
          else if (wave == 6) k1_gemm<2, 3>(smem, ws, RT, HP, 6, 4, true, lane MK_PASS);
          else k1_gemm<1, 7>(smem, ws, RT, HP, 8, 0, true, lane MK_PASS);
        } else {
          if (wave == 0) k1_gemm_raw<3, false>(smem, HP, 0, lane MF_PASS);
          else if (wave < 4) k1_gemm_raw<2, false>(smem, HP, 1 + 2 * wave, lane MF_PASS);
          else if (wave < 7) k1_gemm_raw<2, true>(smem, HP, 2 * (wave - 4), lane MF_PASS);
          else k1_gemm_raw<3, true>(smem, HP, 6, lane MF_PASS);
        }
        msync(lds0 + (unsigned)(LW_OFF + (LW_T + layer) * 4));
        MK(3);
        at.run(r0, HP, wave, lane);
        if (layer == 0) {
          if (wave == 0 && lane < 32)
            asm volatile("global_store_dwordx4 %0, %1, %2 sc1\n\ts_nop 1" ::"v"((unsigned)lane * 16u),
                         "v"(*reinterpret_cast<const u32x4*>(smem + K1R_CS_OFF + lane * 16)), "s"(uni_ptr((const char*)csq))
                         : "memory");
          lds_spin(lds0 + (unsigned)(LW_OFF + LW_HELLO * 4), 1u);
        }
        MK(4);
        announce(lds0 + (unsigned)(LW_OFF + (LW_O + layer * 22 + (wave >> 2)) * 4), 4u, ws, CN_O + layer, (RT * 2 + (wave >> 2)) * 8 + lane,
                 lane < 8);
        MK(5);
        bar();
      }
      FRESH();
      {
        if (wave < 4) {
          const int lane = lane_id();
          u32x2 xv[5];
          ResIn ri;
          ri.setup(layer ? ws + OFF_X1 + SZ_X : (const char*)x0, layer == 0, RB, CT, wave, lane);
          ri.load(xv);
          ResOut ro;
          ro.setup(ws + OFF_X1 + (size_t)layer * 2 * SZ_X, ws + OFF_XS + (size_t)layer * 2 * SZ_XS, out, false, RB, CT, wave, lane);
          f32x4 acc[2][5];
          p2_gemm(acc, xv, lds0, uni_ptr(ws + OFF_O + (size_t)layer * SZ_X + (size_t)RB * (72 * 1024)), layer, wave, lane MK_PASS);
          MK(7);
          ro.run(acc[0], false);
          MK(8);
          announce(lds0 + (unsigned)(LW_OFF + (LW_X1 + layer) * 4), 4u, ws, CN_X1 + layer, (RB >> 1) * 16 + lane, lane < 16);
          MK(9);
          bar();
          FRESH();
          if (wave == 3) cnt_wait(ws, CN_X1 + layer, MI * 16 + NI, 16u);
          bar();
          MK(10);
          p3_g0(smem, ws, layer, MI, NI, wave, lane_id() MK_PASS);
        } else {
          if (layer && wave == 5) cnt_wait(ws, CN_O + layer, RB * 8 + CT, 16u);
          bar();
          k3_const(smem, ws, b_sp, layer, bid, MI, NI, wave & 3);
          MX(6, 26);
          bar();
          MK(10);
          FRESH();
          p3_g1(smem, ws, layer, MI, NI, wave & 3, lane_id() MK_PASS);
        }
      }
      FRESH();
      {
        const int lane = lane_id();
        if (wave < 4) {
          u32x2 xv[5];
          ResIn ri;
          ri.setup(ws + OFF_X1 + (size_t)layer * 2 * SZ_X, false, RB, CT, wave, lane);
          f32x4 acc[2][5];
          ResOut ro;
          ro.setup(ws + OFF_X1 + SZ_X, ws + OFF_XS + SZ_XS, out, layer != 0, RB, CT, wave, lane);
          ResX<36, 33, 8, P5_W, P5_XW, P5_X0, 3, P5_BAR> gm(acc);
          gm.setup(lds0, uni_ptr(ws + OFF_H + (size_t)layer * SZ_Z + (size_t)RB * (288 * 1024)), wave, lane, (unsigned)P5_RED, layer + 1u);
          announce(lds0 + (unsigned)(LW_OFF + (LW_H + layer * 2 + (wave >> 1)) * 4), 2u, ws, ((NI >= 8) ? CN_H2 : CN_H) + layer,
                   (MI * 2 + (wave >> 1)) * 8 + lane, lane < 8);
          MK(19);
          bar();
          ri.load(xv);
          gm.prow();
          place();
          bar();
          MK(20);
          gm.prox<0>(std::make_integer_sequence<int, P5_X0>{});
          gm.init<2 * P5_X0>(xv);
          __builtin_amdgcn_sched_barrier(0);
          gm.run();
          MK(21);
          ro.run(acc[0], layer != 0);
          MK(22);
          if (layer == 0) {
            announce(lds0 + (unsigned)(LW_OFF + LW_X2 * 4), 4u, ws, CN_X2, (RB >> 1) * 16 + lane,
                     lane < 16 || (lane < 32 && (RB & 1) && RB < 31));
            MK(23);
          }
        } else {
          bar();
          bar();
#pragma unroll
          for (int i = 0; i < 7; ++i) {
            if (i == 1 && wave == 5) {
              unsigned f;
              const char* cp = uni_ptr(ws + OFF_CNT + (size_t)((CN_H2 + layer) * 256 + RB * 8 + CT) * 128);
              asm volatile("global_load_dword %0, %1, %2 sc1" : "=v"(f) : "v"(vconst(0u)), "s"(cp) : "memory");
              bar();
              asm volatile("s_waitcnt vmcnt(0)" : "+v"(f) : : "memory");
              if (__builtin_amdgcn_readfirstlane(f) != 7u) cnt_wait(ws, CN_H2 + layer, RB * 8 + CT, 8u);
              continue;
            }
            asm volatile("s_waitcnt lgkmcnt(0)" ::: "memory");
            bar();
          }
          MK(22);
        }
        if (layer == 0 && wave < 4) bar();
      }
    }
  }
}

}

extern "C" void kernel_launch(void* const* d_in, const int* in_sizes, int n_in, void* d_out, int out_size,
                              void* d_ws, size_t ws_size, hipStream_t stream) {
  const u16* x0 = (const u16*)d_in[0];
  const u16* wqkv = (const u16*)d_in[1];
  const u16* wo = (const u16*)d_in[2];
  const u16* w_in = (const u16*)d_in[4];
  const u16* w_sp = (const u16*)d_in[5];
  const u16* b_sp = (const u16*)d_in[6];
  const u16* w_out = (const u16*)d_in[7];
  char* ws = (char*)d_ws;
  (void)hipFuncSetAttribute((const void*)k_main, hipFuncAttributeMaxDynamicSharedMemorySize, LDS_ALL);
  k_main<<<256, 768, LDS_ALL, stream>>>(x0, wqkv, wo, w_in, w_sp, b_sp, w_out, (u16*)d_out, ws);
}
```
